```python
import torch

def _softmax(x, axis=-1):
    return torch.softmax(x, dim=axis)

def _gelu(x):
    return torch.nn.functional.gelu(x, approximate='tanh')

def _silu(x):
    return torch.nn.functional.silu(x)

def _sigmoid(x):
    return torch.sigmoid(x)

def _layernorm(x, axis=-1, eps=1e-05):
    m = x.mean(dim=axis, keepdim=True)
    v = x.var(dim=axis, keepdim=True, unbiased=False)
    return (x - m) / torch.sqrt(v + eps)

def _scatter_add(tgt, idx, src, axis=0):
    return tgt.clone().index_add_(axis, idx, src)

def _segment_sum(src, seg, n):
    out = torch.zeros((n,) + src.shape[1:], dtype=src.dtype)
    return out.index_add_(0, seg, src)

def _pad(x, pw):
    flat = tuple((v for pair in reversed(pw) for v in pair))
    return torch.nn.functional.pad(x, flat)
kernel_name = 'standalone__neighborhood_dinat__vision'

def setup_inputs():

    _w = lambda a: (lambda t: t.to(torch.bfloat16) if t.is_floating_point() else t)(torch.as_tensor(a))
    ks, dil, nh = (7, 2, 4)
    H = W = max(ks * dil, 224 // dil * dil)
    C = max(nh, 192 // nh * nh)
    return {'x': _w((lambda *s: torch.randn(*s, dtype=torch.bfloat16) * 0.2)(1, H, W, C)), 'wqkv': _w((lambda *s: torch.randn(*s, dtype=torch.bfloat16) * 0.2)(C, 3 * C)), 'wo': _w((lambda *s: torch.randn(*s, dtype=torch.bfloat16) * 0.4)(C, C)), 'rpb': _w((lambda *s: torch.randn(*s, dtype=torch.bfloat16) * 0.4)(nh, 2 * ks - 1, 2 * ks - 1))}

def reference(x, wqkv, wo, rpb):
    B, H, W, C = x.shape
    ks, dil, nh = (7, 2, 4)
    hd = C // nh
    scale = hd ** (-0.5)
    qkv = (x @ wqkv).reshape(B, H, W, 3, nh, hd)
    q = qkv[:, :, :, 0].clone()
    k = qkv[:, :, :, 1].clone()
    v = qkv[:, :, :, 2].clone()
    ih = torch.arange(H)
    iw = torch.arange(W)
    m = torch.arange(ks)
    sh = torch.clip(ih // dil - ks // 2, 0, H // dil - ks)
    sw = torch.clip(iw // dil - ks // 2, 0, W // dil - ks)
    kh = (sh[:, None] + m[None, :]) * dil + (ih % dil)[:, None]
    kw = (sw[:, None] + m[None, :]) * dil + (iw % dil)[:, None]
    rh = sh[:, None] + m[None, :] - (ih // dil)[:, None] + (ks - 1)
    rw = sw[:, None] + m[None, :] - (iw // dil)[:, None] + (ks - 1)
    ih4 = kh[:, :, None, None].clone()
    iw4 = kw[None, None, :, :].clone()
    kg = (lambda x, *d: x.permute(*d))(k[:, ih4, iw4, :, :], 0, 1, 3, 5, 2, 4, 6).reshape(B, H, W, nh, ks * ks, hd)
    vg = (lambda x, *d: x.permute(*d))(v[:, ih4, iw4, :, :], 0, 1, 3, 5, 2, 4, 6).reshape(B, H, W, nh, ks * ks, hd)
    bg = (lambda x, *d: x.permute(*d))(rpb[:, rh[:, :, None, None], rw[None, None, :, :]], 1, 3, 0, 2, 4).reshape(1, H, W, nh, ks * ks)
    logits = torch.einsum('bhwnd,bhwnkd->bhwnk', q, kg) * scale + bg
    attn = _softmax(logits, axis=-1)
    out = torch.einsum('bhwnk,bhwnkd->bhwnd', attn, vg).reshape(B, H, W, C)
    return out @ wo
```

```cpp
#include <hip/hip_runtime.h>
#include <hip/hip_bf16.h>
#include <stdint.h>

#define VCLOB "v0", "v1", "v2", "v3", "v4", "v5", "v6", "v7", "v8", "v9", "v10", "v11", "v12", "v13", "v14", "v15", "v16", "v17", "v18", "v19", "v20", "v21", "v22", "v23", "v24", "v25", "v26", "v27", "v28", "v29", "v30", "v32", "v33", "v34", "v35", "v36", "v37", "v38", "v39", "v40", "v41", "v42", "v43", "v44", "v45", "v46", "v47", "v48", "v49", "v50", "v51", "v52", "v53", "v54", "v55", "v56", "v57", "v58", "v59", "v60", "v61", "v62", "v63", "v64", "v65", "v66", "v67", "v68", "v69", "v70", "v71", "v72", "v73", "v74", "v75", "v76", "v77", "v78", "v79", "v80", "v81", "v82", "v83", "v84", "v85", "v86", "v87", "v88", "v89", "v90", "v91", "v92", "v93", "v94", "v95", "v96", "v97", "v98", "v99", "v100", "v101", "v102", "v103", "v104", "v105", "v106", "v107", "v108", "v109", "v110", "v111", "v112", "v113", "v114", "v115", "v116", "v117", "v118", "v119", "v120", "v121", "v122", "v123", "v124", "v125", "v126", "v127", "v128", "v129", "v130", "v131", "v132", "v133", "v134", "v135", "v136", "v137", "v138", "v139", "v140", "v141", "v142", "v143", "v144", "v145", "v146", "v147", "v148", "v149", "v150", "v151", "v152", "v153", "v154", "v155", "v156", "v157", "v158", "v159", "v160", "v161", "v162", "v163", "v164", "v165", "v166", "v167", "v168", "v169", "v170", "v171", "v172", "v173", "v174", "v175", "v176", "v177", "v178", "v179", "v180", "v181", "v182", "v183", "v184", "v185", "v186", "v187", "v188", "v189", "v190", "v191", "v192", "v193", "v194", "v195", "v196", "v197", "v198", "v199", "v200", "v201", "v202", "v203", "v204", "v205", "v206", "v207", "v208", "v209", "v210", "v211", "v212", "v213", "v214", "v215", "v216", "v217", "v218", "v219", "v220", "v221", "v222", "v223", "v224", "v225", "v226", "v227", "v228", "v229", "v230", "v231", "v232", "v233", "v234", "v235", "v236", "v237", "v238", "v239", "v240", "v241", "v242", "v243", "v244", "v245", "v246", "v247", "v248", "v249", "v250", "v251", "v252", "v253", "v254", "v255"
#define ACLOB "a0", "a1", "a2", "a3", "a4", "a5", "a6", "a7", "a8", "a9", "a10", "a11", "a12", "a13", "a14", "a15", "a16", "a17", "a18", "a19", "a20", "a21", "a22", "a23", "a24", "a25", "a26", "a27", "a28", "a29", "a30", "a31", "a32", "a33", "a34", "a35", "a36", "a37", "a38", "a39", "a40", "a41", "a42", "a43", "a44", "a45", "a46", "a47", "a48", "a49", "a50", "a51", "a52", "a53", "a54", "a55", "a56", "a57", "a58", "a59", "a60", "a61", "a62", "a63", "a64", "a65", "a66", "a67", "a68", "a69", "a70", "a71", "a72", "a73", "a74", "a75", "a76", "a77", "a78", "a79", "a80", "a81", "a82", "a83", "a84", "a85", "a86", "a87", "a88", "a89", "a90", "a91", "a92", "a93", "a94", "a95", "a96", "a97", "a98", "a99", "a100", "a101", "a102", "a103", "a104", "a105", "a106", "a107", "a108", "a109", "a110", "a111", "a112", "a113", "a114", "a115", "a116", "a117", "a118", "a119", "a120", "a121", "a122", "a123", "a124", "a125", "a126", "a127", "a128", "a129", "a130", "a131", "a132", "a133", "a134", "a135", "a136", "a137", "a138", "a139", "a140", "a141", "a142", "a143", "a144", "a145", "a146", "a147", "a148", "a149", "a150", "a151", "a152", "a153", "a154", "a155", "a156", "a157", "a158", "a159", "a160", "a161", "a162", "a163", "a164", "a165", "a166", "a167", "a168", "a169", "a170", "a171", "a172", "a173", "a174", "a175", "a176", "a177", "a178", "a179", "a180", "a181", "a182", "a183", "a184", "a185", "a186", "a187", "a188", "a189", "a190", "a191", "a192", "a193", "a194", "a195", "a196", "a197", "a198", "a199", "a200", "a201", "a202", "a203", "a204", "a205", "a206", "a207", "a208", "a209", "a210", "a211", "a212", "a213", "a214", "a215", "a216", "a217", "a218", "a219", "a220", "a221", "a222", "a223", "a224", "a225", "a226", "a227", "a228", "a229", "a230", "a231", "a232", "a233", "a234", "a235", "a236", "a237", "a238", "a239", "a240", "a241", "a242", "a243", "a244", "a245", "a246", "a247", "a248", "a249", "a250", "a251", "a252", "a253", "a254", "a255"
#define SCLOB "s3", "s4", "s5", "s6", "s7", "s8", "s9", "s10", "s11", "s12", "s13", "s14", "s15", "s16", "s17", "s18", "s19", "s20", "s21", "s22", "s23", "s24", "s25", "s26", "s27", "s28", "s29", "s30", "s31", "s32", "s33", "s34", "s35", "s36", "s37", "s38", "s39", "s40", "s41", "s42", "s43", "s44", "s45", "s46", "s47", "s48", "s49", "s50", "s51", "s52", "s53", "s54", "s55", "s56", "s57", "s58", "s59", "s60", "s61", "s62", "s63", "s64", "s65", "s66", "s67", "s68", "s69", "s70", "s71", "s72", "s73", "s74", "s75", "s76", "s77", "s78", "s79", "s80", "s81", "s82", "s83", "s84", "s85", "s86", "s87", "s88", "s89", "s90", "s91", "s92", "s93", "s94", "s95", "s96", "s97", "s98", "s99", "s100", "s101"
#define LDS_BYTES 163392

__global__ __launch_bounds__(256, 1) __attribute__((aligned(4096))) void dinat_hs(const uint16_t* __restrict__ x, const uint16_t* __restrict__ wqkv,
                                                   const uint16_t* __restrict__ wo, const uint16_t* __restrict__ rpb,
                                                   uint16_t* __restrict__ out, char* __restrict__ ws
                                                   ) {
  __shared__ __attribute__((aligned(16))) char smem[LDS_BYTES];
  unsigned tid = threadIdx.x, bid = blockIdx.x;
  auto ka = __builtin_amdgcn_kernarg_segment_ptr();
  asm volatile(
"s_load_dwordx2 s[8:9], s[0:1], 0\n"
"s_load_dwordx2 s[12:13], s[0:1], 8\n"
"s_load_dwordx2 s[16:17], s[0:1], 16\n"
"s_load_dwordx2 s[20:21], s[0:1], 24\n"
"s_load_dwordx2 s[24:25], s[0:1], 32\n"
"s_load_dwordx2 s[28:29], s[0:1], 40\n"
"s_getpc_b64 s[4:5]\n"
".Lpc%=:\n"
"s_bfe_u32 s53, s2, 0x20005\n"
"s_and_b32 s54, s2, 1\n"
"s_lshr_b32 s55, s2, 7\n"
"s_mul_i32 s59, s53, 27\n"
"v_lshrrev_b32 v113, 6, v31\n"
"s_max_u32 s59, s59, 1\n"
"s_mul_i32 s58, s55, 4816896\n"
"s_mul_i32 s71, s54, 14278656\n"
"s_nop 0\n"
"v_readfirstlane_b32 s51, v113\n"
"s_xnor_b32 s56, s54, s55\n"
"s_sub_u32 s59, s59, 1\n"
"s_add_u32 s58, s58, s71\n"
"v_and_b32 v112, 63, v31\n"
"s_and_b32 s56, s56, 1\n"
"s_mul_i32 s72, s59, 768\n"
"s_and_b32 s73, s2, 4\n"
"s_lshr_b32 s71, s2, 3\n"
"s_mul_i32 s57, s54, -344064\n"
"s_add_u32 s58, s58, s72\n"
"s_mul_i32 s73, s73, 21504\n"
"s_and_b32 s74, s2, 2\n"
"v_lshlrev_b32 v120, 4, v112\n"
"s_mul_i32 s85, s56, 3\n"
"s_lshl2_add_u32 s71, s71, s51\n"
"v_min_u32 v121, 2, v112\n"
"s_add_u32 s57, s57, 172032\n"
"s_add_u32 s58, s58, s73\n"
"s_mul_i32 s74, s74, 192\n"
"s_lshl_b32 s61, s51, 3\n"
"v_lshrrev_b32 v114, 3, v112\n"
"v_xor_b32 v115, v120, v112\n"
"s_sub_u32 s86, s85, 3\n"
"s_mul_i32 s72, s71, 384\n"
"v_lshlrev_b32 v121, 7, v121\n"
"s_bfe_u32 s52, s2, 0x20003\n"
"s_add_u32 s58, s58, s74\n"
"s_lshr_b32 s84, s2, 5\n"
"v_and_b32 v115, 0x70, v115\n"
"v_add_u32 v114, s61, v114\n"
"s_mul_i32 s86, s86, s57\n"
"v_add_u32 v121, s72, v121\n"
"s_add_u32 s84, s84, s52\n"
"v_xor_b32 v116, 64, v115\n"
"v_mul_u32_u24 v117, 0x300, v114\n"
"s_add_u32 s86, s86, s58\n"
"v_min_u32 v121, .Lend%=-.Lpc%=-4, v121\n"
"v_mul_u32_u24 v123, 0x2aab, v112\n"
"v_add_u32 v124, 64, v112\n"
"s_mul_i32 s84, s84, 2\n"
"v_add3_u32 v119, v117, s86, v116\n"
"s_mov_b32 m0, 163136\n"
"s_mul_i32 s73, s52, 338\n"
"global_load_lds_dword v121, s[4:5]\n"
"s_and_b32 s74, s51, 1\n"
"v_lshrrev_b32 v123, 16, v123\n"
"v_mul_u32_u24 v124, 0x2aab, v124\n"
"s_and_b32 s65, s51, 1\n"
"s_lshr_b32 s75, 0x9249200, s84\n"
"s_lshr_b32 s76, 0x12492480, s84\n"
"s_lshr_b32 s77, 0x4924900, s84\n"
"s_and_b32 s73, s73, -4\n"
"s_mul_i32 s74, s74, 88\n"
"v_lshlrev_b32 v126, 1, v123\n"
"v_lshrrev_b32 v124, 16, v124\n"
"s_lshr_b32 s66, s51, 1\n"
"s_mul_i32 s70, s52, 96\n"
"s_mul_i32 s86, s65, 384\n"
"s_mov_b32 s22, 1352\n"
"s_mov_b32 s23, 0x20000\n"
"s_add_u32 s73, s73, s74\n"
"v_lshlrev_b32 v122, 2, v112\n"
"s_and_b32 s60, s2, 7\n"
"s_lshr_b32 s84, s2, 5\n"
"v_and_b32 v126, 24, v126\n"
"v_lshlrev_b32 v127, 1, v124\n"
"s_add_u32 s86, s86, s70\n"
"s_mul_i32 s71, s66, 36864\n"
"s_movk_i32 s69, 1152\n"
"s_and_b32 s75, s75, 0x180\n"
"s_lshl3_add_u32 s60, s60, s84\n"
"v_and_or_b32 v126, v123, 3, v126\n"
"v_and_b32 v127, 24, v127\n"
"v_add_u32 v125, 128, v112\n"
"s_add_u32 s86, s86, s71\n"
"v_mad_u32_u24 v126, v126, s69, v120\n"
"v_mul_u32_u24 v27, 96, v123\n"
"v_and_or_b32 v127, v124, 3, v127\n"
"v_lshrrev_b32 v128, 2, v124\n"
"v_mul_u32_u24 v125, 0x2aab, v125\n"
"s_add_u32 s86, s86, 384\n"
"s_mul_i32 s65, s65, 3072\n"
"s_mul_i32 s72, s66, 6752\n"
"s_mul_i32 s84, s75, 576\n"
"s_mov_b32 s14, 221184\n"
"s_mov_b32 s15, 0x20000\n"
"v_sub_u32 v27, v126, v27\n"
"v_and_or_b32 v127, v128, 4, v127\n"
"v_lshrrev_b32 v125, 16, v125\n"
"s_add_u32 s72, s72, s65\n"
"s_add_u32 s84, s84, s86\n"
"v_mad_u32_u24 v127, v127, s69, v120\n"
"v_mul_u32_u24 v28, 96, v124\n"
"v_lshlrev_b32 v129, 1, v125\n"
"v_sub_u32 v28, v127, v28\n"
"v_and_b32 v129, 24, v129\n"
"v_add_u32 v28, 1024, v28\n"
"v_and_or_b32 v129, v125, 3, v129\n"
"v_mad_u32_u24 v129, v129, s69, v120\n"
"v_mul_u32_u24 v29, 96, v125\n"
"s_sub_u32 s64, 111, s59\n"
"v_sub_u32 v29, v129, v29\n"
"s_min_u32 s64, s64, 32\n"
"v_max_u32 v118, s85, v114\n"
"v_add_u32 v29, 6656, v29\n"
"s_mul_i32 s64, s64, 768\n"
"v_add_u32 v118, -3, v118\n"
"s_add_u32 s64, s64, s58\n"
"s_mul_i32 s50, s51, 6752\n"
"v_mul_i32_i24 v118, s57, v118\n"
"s_mov_b32 s10, 19267584\n"
"s_mov_b32 s11, 0x20000\n"
"s_add_u32 s50, s50, 27008\n"
"v_add3_u32 v118, v118, s64, v115\n"
"s_mul_i32 s34, s51, 12288\n"
"s_waitcnt lgkmcnt(0)\n"
"s_add_u32 m0, s74, 162768\n"
"s_lshl_b32 s74, s56, 3\n"
"buffer_load_dword v122, s[20:23], s73 offen lds\n"
"s_add_u32 m0, s72, 0\n"
"s_sub_u32 s74, s74, 2\n"
"buffer_load_dwordx4 v27, s[12:15], s84 offen lds\n"
"s_mul_i32 s74, s74, s57\n"
"s_add_u32 s74, s74, s58\n"
"s_add_u32 s34, s34, 81024\n"
"v_add3_u32 v130, v117, s74, v115\n"
"s_add_u32 s73, s74, s57\n"
"s_lshl2_add_u32 s85, s60, s52\n"
"v_add3_u32 v131, v117, s73, v116\n"
"s_and_b32 s76, s76, 0x180\n"
"s_add_u32 m0, s72, 1024\n"
"s_lshl2_add_u32 s39, s85, s51\n"
"s_mul_i32 s85, s76, 576\n"
"s_add_u32 s85, s85, s86\n"
"s_and_b32 s77, s77, 0x180\n"
"s_mul_i32 s71, s77, 576\n"
"buffer_load_dwordx4 v28, s[12:15], s84 offen lds\n"
"s_add_u32 s71, s71, s86\n"
"s_mul_i32 s74, s54, 172032\n"
"s_sub_u32 s36, s58, s74\n"
"s_add_u32 s32, s36, s75\n"
"v_add3_u32 v190, v117, s74, v115\n"
"s_sub_u32 s73, 172032, s74\n"
"v_add3_u32 v191, v117, s73, v116\n"
"s_add_u32 s0, s36, s76\n"
"s_add_u32 s1, s36, s77\n"
"s_add_u32 m0, s72, 2048\n"
"s_mul_i32 s35, s51, 6144\n"
"s_lshl_b32 s33, s57, 1\n"
"s_add_u32 s35, s35, 130176\n"
"v_and_b32 v134, 3, v112\n"
"v_lshrrev_b32 v137, 1, v112\n"
"buffer_load_dwordx4 v29, s[12:15], s84 offen lds\n"
"v_and_or_b32 v137, v137, 4, v134\n"
"v_bfe_u32 v136, v112, 2, 1\n"
"v_lshrrev_b32 v140, 1, v137\n"
"v_lshrrev_b32 v132, 4, v112\n"
"v_lshl_add_u32 v151, v136, 2, v140\n"
"v_xor_b32 v151, v151, v132\n"
"v_lshlrev_b32 v151, 4, v151\n"
"s_add_u32 m0, s50, 0\n"
"v_lshl_add_u32 v151, v137, 7, v151\n"
"v_mul_u32_u24 v25, 0xc00, v136\n"
"v_add_u32 v25, v25, v151\n"
"v_add_u32 v188, s34, v25\n"
"v_xad_u32 v189, v25, 64, s34\n"
"buffer_load_dwordx4 v118, s[8:11], s75 offen lds\n"
"v_xor_b32 v71, 64, v25\n"
"v_add_u32 v70, s35, v25\n"
"v_add_u32 v71, s35, v71\n"
"s_mul_i32 s3, s65, 12\n"
"s_add_u32 s70, s70, s3\n"
"s_mul_i32 s81, s77, 576\n"
"s_add_u32 s81, s81, s70\n"
"s_add_u32 m0, s50, 3072\n"
"s_sub_u32 s3, s76, s75\n"
"s_mul_i32 s3, s3, s66\n"
"s_add_u32 s3, s3, s75\n"
"s_mul_i32 s3, s3, 576\n"
"s_add_u32 s3, s3, s70\n"
"buffer_load_dwordx4 v119, s[8:11], s75 offen lds\n"
"v_lshrrev_b32 v150, 2, v112\n"
"v_mul_u32_u24 v150, 96, v150\n"
"v_lshl_add_u32 v23, v134, 3, v150\n"
"s_mov_b32 s42, 0x80000\n"
"v_add_u32 v72, 54016, v23\n"
"v_lshrrev_b32 v139, 3, v112\n"
"s_add_u32 s62, s59, s61\n"
"v_and_b32 v139, 4, v139\n"
"s_sub_u32 s63, 108, s62\n"
"s_add_u32 m0, s34, 6144\n"
"s_sub_u32 s62, 3, s62\n"
"v_add_u32 v146, 1, v139\n"
"v_min_u32 v62, 0xdc, v31\n"
"v_max_i32 v145, s62, v139\n"
"v_max_i32 v146, s62, v146\n"
"buffer_load_dwordx4 v130, s[8:11], s75 offen lds\n"
"v_add_u32 v147, 2, v139\n"
"v_add_u32 v148, 3, v139\n"
"v_mul_u32_u24 v63, 0xf1, v62\n"
"s_mul_i32 s86, s54, -52\n"
"v_mov_b32 v149, 0x7f\n"
"v_min_i32 v145, s63, v145\n"
"v_min_i32 v146, s63, v146\n"
"s_add_u32 m0, s34, 9216\n"
"v_max_i32 v147, s62, v147\n"
"v_max_i32 v148, s62, v148\n"
"v_lshrrev_b32 v63, 12, v63\n"
"s_sub_u32 s86, s86, 8\n"
"v_lshlrev_b32 v145, v145, v149\n"
"buffer_load_dwordx4 v131, s[8:11], s75 offen lds\n"
"v_lshlrev_b32 v146, v146, v149\n"
"v_min_i32 v147, s63, v147\n"
"v_min_i32 v148, s63, v148\n"
"v_lshlrev_b32 v30, 3, v62\n"
"v_mul_i32_i24 v63, s86, v63\n"
"v_lshrrev_b32 v145, v137, v145\n"
"v_lshrrev_b32 v146, v137, v146\n"
"s_add_u32 m0, s72, 13504\n"
"v_lshlrev_b32 v147, v147, v149\n"
"v_lshlrev_b32 v148, v148, v149\n"
"v_lshl_add_u32 v62, v62, 1, v63\n"
"s_mov_b32 s74, 0xffff\n"
"v_lshrrev_b32 v147, v137, v147\n"
"buffer_load_dwordx4 v27, s[12:15], s85 offen lds\n"
"v_lshrrev_b32 v148, v137, v148\n"
"v_bfe_i32 v64, v145, 0, 1\n"
"v_bfe_i32 v65, v146, 0, 1\n"
"v_bfi_b32 v0, s74, v64, v65\n"
"v_bfe_i32 v66, v147, 0, 1\n"
"v_bfe_i32 v67, v148, 0, 1\n"
"v_bfi_b32 v1, s74, v66, v67\n"
"s_add_u32 m0, s72, 14528\n"
"v_xor_b32 v69, 64, v25\n"
"v_add_u32 v30, 160896, v30\n"
"v_add_u32 v68, s50, v25\n"
"v_add_u32 v69, s50, v69\n"
"s_mov_b32 s30, 23068672\n"
"buffer_load_dwordx4 v28, s[12:15], s85 offen lds\n"
"s_mov_b32 s31, 0x20000\n"
"s_lshl_b32 s39, s39, 7\n"
"v_add_u32 v142, s61, v137\n"
"v_bfe_u32 v152, v142, 1, 1\n"
"v_mul_u32_u24 v153, 43, v142\n"
"s_movk_i32 s68, 6752\n"
"v_and_b32 v133, 48, v112\n"
"v_xor_b32 v152, v152, v132\n"
"s_add_u32 m0, s72, 15552\n"
"v_lshrrev_b32 v153, 9, v153\n"
"s_movk_i32 s67, 192\n"
"v_lshlrev_b32 v152, 4, v152\n"
"v_mul_u32_u24 v153, 80832, v153\n"
"v_mad_u32_u24 v21, v142, s68, v133\n"
"buffer_load_dwordx4 v29, s[12:15], s85 offen lds\n"
"v_mad_u32_u24 v141, v142, s67, v152\n"
"v_sub_u32 v21, v21, v153\n"
"v_and_b32 v154, 1, v112\n"
"s_mul_i32 s48, s56, 13504\n"
"v_add_u32 v21, 6144, v21\n"
"v_add_u32 v187, 74272, v141\n"
"v_add_u32 v76, 60768, v141\n"
"s_add_u32 m0, s50, 1024\n"
"v_mad_u32_u24 v154, v154, s68, v133\n"
"s_mov_b32 s6, 0xf0f0f0f0\n"
"s_mov_b32 s7, 0xf0f0f0f0\n"
"s_mov_b64 exec, s[6:7]\n"
"v_add_u32 v21, s48, v141\n"
"v_mov_b32 v187, v141\n"
"v_add_u32 v76, 60544, v154\n"
"s_mov_b64 exec, -1\n"
"buffer_load_dwordx4 v118, s[8:11], s76 offen lds\n"
"v_max_i32 v248, s62, v137\n"
"v_min_i32 v248, s63, v248\n"
"v_bfe_i32 v244, v145, 8, 1\n"
"v_bfe_i32 v245, v146, 8, 1\n"
"v_bfe_i32 v246, v147, 8, 1\n"
"v_bfe_i32 v247, v148, 8, 1\n"
"v_lshlrev_b32 v248, v248, -1\n"
"s_add_u32 m0, s50, 4096\n"
"v_bfi_b32 v2, s74, v244, v245\n"
"v_bfi_b32 v3, s74, v246, v247\n"
"v_bfe_u32 v138, v112, 4, 1\n"
"v_lshrrev_b32 v248, v139, v248\n"
"buffer_load_dwordx4 v119, s[8:11], s76 offen lds\n"
"v_sub_u32 v253, v136, v138\n"
"v_bfe_i32 v249, v248, 0, 1\n"
"v_bfe_i32 v250, v248, 1, 1\n"
"v_bfe_i32 v251, v248, 2, 1\n"
"v_bfe_i32 v252, v248, 3, 1\n"
"v_ashrrev_i32 v145, 31, v253\n"
"v_add_u32 v146, -1, v253\n"
"v_bfi_b32 v176, s74, v249, v250\n"
"v_bfi_b32 v177, s74, v251, v252\n"
"v_not_b32 v145, v145\n"
"v_ashrrev_i32 v146, 31, v146\n"
"v_or_b32 v254, s56, v138\n"
"v_and_b32 v12, v145, v0\n"
"v_and_b32 v16, v146, v0\n"
"v_and_b32 v13, v145, v1\n"
"v_and_b32 v17, v146, v1\n"
"v_and_b32 v14, v145, v2\n"
"v_and_b32 v18, v146, v2\n"
"v_and_b32 v15, v145, v3\n"
"v_and_b32 v19, v146, v3\n"
"v_sub_u32 v254, v136, v254\n"
"v_mov_b32 v160, 0x40de\n"
"v_mov_b32 v161, 0\n"
"v_lshlrev_b32 v255, 4, v134\n"
"v_mov_b64 v[156:157], 0\n"
"s_mov_b32 s6, 0x00f0000f\n"
"s_mov_b32 s7, 0xf0000f00\n"
"v_ashrrev_i32 v145, 31, v254\n"
"v_add_u32 v146, -1, v254\n"
"s_mov_b64 exec, s[6:7]\n"
"v_lshlrev_b64 v[156:157], v255, v[160:161]\n"
"s_mov_b64 exec, -1\n"
"v_max_u32 v144, 3, v142\n"
"v_add_u32 v144, -3, v144\n"
"v_mov_b32 v143, s48\n"
"v_bfe_u32 v56, v144, 1, 1\n"
"v_mad_u32_u24 v143, v136, s68, v143\n"
"v_xor_b32 v56, v56, v132\n"
"v_lshl_add_u32 v56, v56, 4, v143\n"
"v_mad_u32_u24 v178, v144, s67, v56\n"
"v_add_u32 v144, 5, v142\n"
"v_min_u32 v144, 32, v144\n"
"v_bfe_u32 v57, v144, 1, 1\n"
"v_xor_b32 v57, v57, v132\n"
"v_bfe_u32 v135, v112, 2, 2\n"
"v_lshl_add_u32 v57, v57, 4, v143\n"
"v_mad_u32_u24 v179, v144, s67, v57\n"
"v_cmp_eq_u32 s[44:45], 32, v144\n"
"v_add3_u32 v142, v135, v139, s61\n"
"v_max_u32 v144, 3, v142\n"
"s_lshl_b32 s84, s48, 2\n"
"v_add_u32 v144, -3, v144\n"
"v_mad_u32_u24 v186, v136, s68, v141\n"
"s_add_u32 s84, s84, 6752\n"
"v_mov_b32 v143, s48\n"
"v_bfe_u32 v58, v144, 1, 1\n"
"v_add_u32 v22, s84, v186\n"
"v_mad_u32_u24 v143, v138, s68, v143\n"
"v_xor_b32 v58, v58, v134\n"
"v_and_b32 v155, 1, v114\n"
"v_lshl_add_u32 v58, v58, 4, v143\n"
"v_add_u32 v155, 29, v155\n"
"v_mad_u32_u24 v182, v144, s67, v58\n"
"v_mul_i32_i24 v155, s57, v155\n"
"v_add_u32 v144, 5, v142\n"
"v_add3_u32 v77, v155, s64, v116\n"
"s_mov_b64 exec, 0\n"
"s_cmp_eq_u32 s51, 0\n"
"s_cbranch_scc1 .Lic0%=\n"
"s_cmp_eq_u32 s51, 1\n"
"s_cbranch_scc1 .Lic1%=\n"
"s_cmp_eq_u32 s51, 2\n"
"s_cbranch_scc1 .Lic2%=\n"
"s_branch .Lic3%=\n"
".Lib0%=:\n"
"s_mov_b64 exec, -1\n"
"v_min_u32 v144, 32, v144\n"
"v_bfe_u32 v59, v144, 1, 1\n"
"v_sub_u32 v60, v136, v138\n"
"s_mul_i32 s49, s56, 272\n"
"v_xor_b32 v59, v59, v134\n"
"v_mul_i32_i24 v60, 0x88, v60\n"
"v_sub_u32 v61, v137, v139\n"
"s_add_u32 m0, s34, 7168\n"
"v_lshrrev_b32 v56, 2, v112\n"
"buffer_load_dwordx4 v130, s[8:11], s76 offen lds\n"
"v_sub_u32 v57, 11, v112\n"
"v_lshl_add_u32 v59, v59, 4, v143\n"
"v_lshl_add_u32 v60, v61, 3, v60\n"
"s_add_u32 s85, s49, 161064\n"
"v_and_b32 v57, 0x80000000, v57\n"
"v_mad_u32_u24 v183, v144, s67, v59\n"
"v_add_u32 v20, s85, v60\n"
"s_lshl_b32 s84, s66, 10\n"
"s_and_b32 s85, s51, 1\n"
"v_add_u32 v192, 99456, v120\n"
"s_lshl_b32 s73, s51, 10\n"
"s_add_u32 m0, s34, 10240\n"
"s_lshl3_add_u32 s84, s85, s84\n"
"buffer_load_dwordx4 v131, s[8:11], s76 offen lds\n"
"v_add_u32 v202, s73, v192\n"
"s_lshl_b32 s85, s52, 3\n"
"s_mov_b32 s73, 0xc0000007\n"
"s_cmp_eq_u32 s53, 0\n"
"s_cselect_b32 s73, 0xe0000000, s73\n"
"s_cmp_eq_u32 s53, 3\n"
"s_cselect_b32 s73, 7, s73\n"
"v_add_u32 v142, s85, v137\n"
"s_add_u32 s84, s84, 4096\n"
"v_lshrrev_b32 v150, v142, s73\n"
"v_xor_b32 v151, s54, v136\n"
"v_add_u32 v203, s84, v192\n"
"v_lshlrev_b32 v150, 31, v150\n"
"s_add_u32 m0, s72, 60768\n"
"v_mul_u32_u24 v151, 172032, v151\n"
"buffer_load_dwordx4 v27, s[12:15], s71 offen lds\n"
"s_waitcnt vmcnt(12)\n"
"s_barrier\n"
"ds_read_b64_tr_b16 a[0:1], v23\n"
"ds_read_b64_tr_b16 a[2:3], v23 offset:1536\n"
"ds_read_b64_tr_b16 a[4:5], v23 offset:32\n"
"ds_read_b64_tr_b16 a[6:7], v23 offset:1568\n"
"ds_read_b64_tr_b16 a[8:9], v23 offset:64\n"
"ds_read_b64_tr_b16 a[10:11], v23 offset:1600\n"
"ds_read_b64_tr_b16 a[72:73], v23 offset:3072\n"
"s_add_u32 m0, s72, 61792\n"
"ds_read_b64_tr_b16 a[74:75], v23 offset:4608\n"
"buffer_load_dwordx4 v28, s[12:15], s71 offen lds\n"
"ds_read_b64_tr_b16 a[76:77], v23 offset:3104\n"
"v_mul_u32_u24 v142, 0x300, v142\n"
"ds_read_b64_tr_b16 a[78:79], v23 offset:4640\n"
"ds_read_b64_tr_b16 a[80:81], v23 offset:3136\n"
"ds_read_b64_tr_b16 a[82:83], v23 offset:4672\n"
"v_min_u32 v196, 1, v112\n"
"ds_read_b64_tr_b16 a[12:13], v23 offset:6752\n"
"ds_read_b64_tr_b16 a[14:15], v23 offset:8288\n"
"ds_read_b64_tr_b16 a[16:17], v23 offset:6784\n"
"s_add_u32 m0, s72, 62816\n"
"ds_read_b64_tr_b16 a[18:19], v23 offset:8320\n"
"buffer_load_dwordx4 v29, s[12:15], s71 offen lds\n"
"s_waitcnt vmcnt(12)\n"
"ds_read_b128 a[144:147], v68\n"
"s_mul_i32 s72, s54, 312\n"
"s_and_b32 s71, s52, 1\n"
"ds_read_b128 a[148:151], v69\n"
"s_lshl1_add_u32 s72, s71, s72\n"
"s_add_u32 s72, s72, 162760\n"
"v_add_u32 v24, s72, v62\n"
"ds_read_b64_tr_b16 a[20:21], v23 offset:6816\n"
"ds_read_b64_tr_b16 a[22:23], v23 offset:8352\n"
"ds_read_b64_tr_b16 a[84:85], v23 offset:9824\n"
"s_add_u32 m0, s50, 2048\n"
"ds_read_b64_tr_b16 a[86:87], v23 offset:11360\n"
"buffer_load_dwordx4 v118, s[8:11], s77 offen lds\n"
"ds_read_b64_tr_b16 a[88:89], v23 offset:9856\n"
"s_lshl2_add_u32 s71, s60, s51\n"
"ds_read_b64_tr_b16 a[90:91], v23 offset:11392\n"
"s_lshl2_add_u32 s40, s71, s52\n"
"ds_read_b64_tr_b16 a[92:93], v23 offset:9888\n"
"s_mul_i32 s37, s71, 86016\n"
"ds_read_b64_tr_b16 a[94:95], v23 offset:11424\n"
"s_and_b32 s71, s52, 1\n"
"ds_read_u16 v64, v24 offset:6\n"
"s_lshl2_add_u32 s71, s71, s61\n"
"ds_read_u16 v65, v24 offset:4\n"
"v_add_u32 v155, s71, v134\n"
"v_mul_u32_u24 v155, 0x300, v155\n"
"v_lshl_add_u32 v155, v56, 7, v155\n"
"v_or_b32 v200, v57, v155\n"
"v_add3_u32 v150, v150, v151, v142\n"
"v_lshlrev_b32 v196, 31, v196\n"
"v_lshlrev_b32 v152, 3, v134\n"
"v_lshlrev_b32 v193, 3, v112\n"
"v_add_u32 v180, 40512, v178\n"
"v_add_u32 v181, 40512, v179\n"
"v_not_b32 v145, v145\n"
"v_cmp_eq_u32 s[46:47], 32, v144\n"
"v_and_b32 v4, v145, v0\n"
"v_and_b32 v5, v145, v1\n"
"v_and_b32 v6, v145, v2\n"
"v_and_b32 v7, v145, v3\n"
"v_ashrrev_i32 v146, 31, v146\n"
"v_and_b32 v8, v146, v0\n"
"v_and_b32 v9, v146, v1\n"
"v_and_b32 v10, v146, v2\n"
"v_and_b32 v11, v146, v3\n"
"v_mov_b32 v26, 0xf149f149\n"
"v_mov_b64 v[160:161], v[156:157]\n"
"v_add_u32 v184, 40512, v182\n"
"v_add_u32 v185, 40512, v183\n"
"v_mov_b32 v172, 0x3f803f80\n"
"v_mov_b32 v173, v172\n"
"v_mov_b64 v[174:175], v[172:173]\n"
"v_mov_b64 v[164:165], v[156:157]\n"
"v_add_u32 v193, 99456, v193\n"
"v_mov_b64 v[168:169], v[156:157]\n"
"s_waitcnt vmcnt(11) lgkmcnt(11)\n"
"v_mfma_f32_16x16x32_bf16 v[204:207], a[0:3], a[144:147], 0\n"
"s_waitcnt lgkmcnt(0)\n"
"ds_read_b128 a[168:171], v188 offset:6144\n"
"ds_read_b128 a[172:175], v189 offset:6144\n"
"v_mfma_f32_16x16x32_bf16 v[208:211], a[4:7], a[144:147], 0\n"
"s_add_u32 m0, s50, 5120\n"
"ds_read_u16 v66, v24 offset:2\n"
"buffer_load_dwordx4 v119, s[8:11], s77 offen lds\n"
"v_mfma_f32_16x16x32_bf16 v[212:215], a[8:11], a[144:147], 0\n"
"s_lshl_b32 s84, s51, 6\n"
"ds_read_u16 v67, v24\n"
"v_lshl_or_b32 v64, v65, 16, v64\n"
"v_mfma_f32_16x16x32_bf16 v[216:219], a[72:75], a[144:147], 0\n"
"s_add_u32 m0, s34, 8192\n"
"v_add3_u32 v194, v150, s84, v133\n"
"v_lshl_add_u32 v150, v132, 3, v150\n"
"v_mfma_f32_16x16x32_bf16 v[220:223], a[76:79], a[144:147], 0\n"
"s_add_u32 s41, s39, 131072\n"
"v_lshl_add_u32 v153, v23, 2, s84\n"
"v_sub_u32 v153, v153, v152\n"
"v_mfma_f32_16x16x32_bf16 v[224:227], a[80:83], a[144:147], 0\n"
"s_lshl2_add_u32 s86, s60, s52\n"
"buffer_load_dwordx4 v130, s[8:11], s77 offen lds\n"
"v_sub_u32 v153, v153, v152\n"
"v_mfma_f32_16x16x32_bf16 v[204:207], a[12:15], a[148:151], v[204:207]\n"
"s_mul_i32 s72, s51, 1536\n"
"v_add_u32 v236, 130176, v153\n"
"v_sub_u32 v154, v236, v152\n"
"v_mfma_f32_16x16x32_bf16 v[208:211], a[16:19], a[148:151], v[208:211]\n"
"s_add_u32 m0, s34, 11264\n"
"s_mul_i32 s38, s86, 86016\n"
"v_mfma_f32_16x16x32_bf16 v[212:215], a[20:23], a[148:151], v[212:215]\n"
"s_waitcnt lgkmcnt(3)\n"
"s_add_u32 s38, s38, s72\n"
"buffer_load_dwordx4 v131, s[8:11], s77 offen lds\n"
"v_mfma_f32_16x16x32_bf16 v[216:219], a[84:87], a[148:151], v[216:219]\n"
"s_lshl_b32 s86, s51, 5\n"
"s_add_u32 s72, s86, 256\n"
"v_add_u32 v195, s72, v150\n"
"v_mfma_f32_16x16x32_bf16 v[220:223], a[88:91], a[148:151], v[220:223]\n"
"s_waitcnt lgkmcnt(0)\n"
"v_lshl_or_b32 v65, v67, 16, v66\n"
"v_mfma_f32_16x16x32_bf16 v[224:227], a[92:95], a[148:151], v[224:227]\n"
"s_add_u32 m0, s34, 0\n"
"ds_write_b64 v30, v[64:65]\n"
"v_mfma_f32_16x16x32_bf16 v[244:247], a[0:3], a[168:171], 0\n"
"s_sub_u32 s86, 256, s86\n"
"buffer_load_dwordx4 v190, s[8:11], s32 offen lds\n"
"v_add_u32 v235, s86, v154\n"
"v_mfma_f32_16x16x32_bf16 v[248:251], a[4:7], a[168:171], 0\n"
"s_waitcnt vmcnt(10)\n"
"s_mov_b32 s18, 73728\n"
"v_mfma_f32_16x16x32_bf16 v[252:255], a[8:11], a[168:171], 0\n"
"s_mul_i32 s79, s51, 3072\n"
"ds_read_b128 a[152:155], v68 offset:1024\n"
"ds_read_b128 a[156:159], v69 offset:1024\n"
"v_mfma_f32_16x16x32_bf16 v[104:107], a[72:75], a[168:171], 0\n"
"s_add_u32 m0, s34, 3072\n"
"s_mov_b32 s19, 0x20000\n"
"s_add_u32 s43, s79, 0\n"
"v_mfma_f32_16x16x32_bf16 v[108:111], a[76:79], a[168:171], 0\n"
"s_sub_u32 s16, s16, 99456\n"
"s_subb_u32 s17, s17, 0\n"
"buffer_load_dwordx4 v191, s[8:11], s32 offen lds\n"
"v_mfma_f32_16x16x32_bf16 v[228:231], a[80:83], a[168:171], 0\n"
"s_add_u32 s32, s32, s33\n"
"s_mov_b32 s18, 173184\n"
"v_mfma_f32_16x16x32_bf16 v[244:247], a[12:15], a[172:175], v[244:247]\n"
"s_mul_i32 s74, s52, 1536\n"
"s_add_u32 s37, s37, s74\n"
"v_mfma_f32_16x16x32_bf16 v[248:251], a[16:19], a[172:175], v[248:251]\n"
"s_add_u32 m0, s34, 1024\n"
"s_add_u32 s37, s37, 949120\n"
"v_mfma_f32_16x16x32_bf16 v[252:255], a[20:23], a[172:175], v[252:255]\n"
"s_lshl_b32 s40, s40, 7\n"
"buffer_load_dwordx4 v190, s[8:11], s0 offen lds\n"
"v_mfma_f32_16x16x32_bf16 v[104:107], a[84:87], a[172:175], v[104:107]\n"
"s_waitcnt vmcnt(10)\n"
"s_lshr_b32 s74, s52, 1\n"
"v_mfma_f32_16x16x32_bf16 v[108:111], a[88:91], a[172:175], v[108:111]\n"
"s_mul_i32 s85, s66, -81024\n"
"ds_read_b128 a[176:179], v188 offset:7168\n"
"ds_read_b128 a[180:183], v189 offset:7168\n"
"v_mfma_f32_16x16x32_bf16 v[228:231], a[92:95], a[172:175], v[228:231]\n"
"s_add_u32 m0, s34, 4096\n"
"s_mul_i32 s74, s74, 172032\n"
"s_mul_i32 s80, s66, 12288\n"
"s_add_u32 s74, s74, s36\n"
"s_mul_i32 s78, s33, 6\n"
"buffer_load_dwordx4 v191, s[8:11], s0 offen lds\n"
"s_add_u32 s0, s0, s33\n"
"s_add_u32 s85, s85, s50\n"
"s_mov_b32 s26, 19267584\n"
"s_mov_b32 s27, 0x20000\n"
"s_add_u32 s38, s38, 949120\n"
"s_add_u32 s80, s80, s79\n"
"s_add_u32 s78, s78, s74\n"
"s_add_u32 s6, s85, 40512\n"
"s_add_u32 m0, s34, 2048\n"
"s_nop 0\n"
"buffer_load_dwordx4 v190, s[8:11], s1 offen lds\n"
"s_add_u32 m0, s34, 5120\n"
"s_nop 0\n"
"buffer_load_dwordx4 v191, s[8:11], s1 offen lds\n"
"s_add_u32 s1, s1, s33\n"
"s_mov_b64 exec, 0\n"
"s_cmp_eq_u32 s51, 0\n"
"s_cbranch_scc1 .Lic4%=\n"
"s_cmp_eq_u32 s51, 1\n"
"s_cbranch_scc1 .Lic5%=\n"
"s_cmp_eq_u32 s51, 2\n"
"s_cbranch_scc1 .Lic6%=\n"
"s_branch .Lic7%=\n"
".Lib1%=:\n"
"s_mov_b64 exec, -1\n"
"s_add_u32 m0, s35, 0\n"
"s_barrier\n"
"buffer_load_dwordx4 v190, s[8:11], s32 offen lds\n"
"ds_read_b64_tr_b16 a[24:25], v23 offset:13504\n"
"ds_read_b64_tr_b16 a[26:27], v23 offset:15040\n"
"ds_read_b64_tr_b16 a[28:29], v23 offset:13536\n"
"ds_read_b64_tr_b16 a[30:31], v23 offset:15072\n"
"ds_read_b64_tr_b16 a[32:33], v23 offset:13568\n"
"ds_read_b64_tr_b16 a[34:35], v23 offset:15104\n"
"ds_read_b64_tr_b16 a[96:97], v23 offset:16576\n"
"s_waitcnt vmcnt(11)\n"
"ds_read_b64_tr_b16 a[98:99], v23 offset:18112\n"
"s_add_u32 m0, s35, 3072\n"
"ds_read_b64_tr_b16 a[100:101], v23 offset:16608\n"
"s_barrier\n"
"ds_read_b64_tr_b16 a[102:103], v23 offset:18144\n"
"buffer_load_dwordx4 v191, s[8:11], s32 offen lds\n"
"s_add_u32 s32, s32, s33\n"
"ds_read_b64_tr_b16 a[104:105], v23 offset:16640\n"
"ds_read_b64_tr_b16 a[106:107], v23 offset:18176\n"
"ds_read_b64_tr_b16 a[36:37], v23 offset:20256\n"
"ds_read_b64_tr_b16 a[38:39], v23 offset:21792\n"
"ds_read_b64_tr_b16 a[40:41], v23 offset:20288\n"
"ds_read_b64_tr_b16 a[42:43], v23 offset:21824\n"
"s_add_u32 m0, s35, 1024\n"
"ds_read_b64_tr_b16 a[44:45], v23 offset:20320\n"
"ds_read_b64_tr_b16 a[46:47], v23 offset:21856\n"
"ds_read_b64_tr_b16 a[108:109], v23 offset:23328\n"
"buffer_load_dwordx4 v190, s[8:11], s0 offen lds\n"
"ds_read_b64_tr_b16 a[110:111], v23 offset:24864\n"
"s_waitcnt vmcnt(11) lgkmcnt(15)\n"
"v_mfma_f32_16x16x32_bf16 v[204:207], a[24:27], a[152:155], v[204:207]\n"
"ds_read_b128 a[160:163], v68 offset:2048\n"
"v_add_u32 v68, 130176, v23\n"
"v_mfma_f32_16x16x32_bf16 v[208:211], a[28:31], a[152:155], v[208:211]\n"
"s_waitcnt lgkmcnt(15)\n"
"ds_read_b128 a[164:167], v69 offset:2048\n"
"v_mfma_f32_16x16x32_bf16 v[212:215], a[32:35], a[152:155], v[212:215]\n"
"s_waitcnt lgkmcnt(14)\n"
"ds_read_b64_tr_b16 a[112:113], v23 offset:23360\n"
"ds_read_b64_tr_b16 a[114:115], v23 offset:24896\n"
"v_mfma_f32_16x16x32_bf16 v[216:219], a[96:99], a[152:155], v[216:219]\n"
"s_waitcnt lgkmcnt(14)\n"
"ds_read_b64_tr_b16 a[116:117], v23 offset:23392\n"
"ds_read_b64_tr_b16 a[118:119], v23 offset:24928\n"
"v_mfma_f32_16x16x32_bf16 v[220:223], a[100:103], a[152:155], v[220:223]\n"
"s_waitcnt lgkmcnt(14)\n"
"ds_read_b64_tr_b16 a[48:49], v23 offset:60768\n"
"ds_read_b64_tr_b16 a[50:51], v23 offset:62304\n"
"v_mfma_f32_16x16x32_bf16 v[224:227], a[104:107], a[152:155], v[224:227]\n"
"s_waitcnt lgkmcnt(14)\n"
"ds_read_b64_tr_b16 a[52:53], v23 offset:60800\n"
"ds_read_b64_tr_b16 a[54:55], v23 offset:62336\n"
"v_mfma_f32_16x16x32_bf16 v[204:207], a[36:39], a[156:159], v[204:207]\n"
"s_waitcnt vmcnt(9) lgkmcnt(14)\n"
"ds_read_b128 a[184:187], v188 offset:8192\n"
"v_mfma_f32_16x16x32_bf16 v[208:211], a[40:43], a[156:159], v[208:211]\n"
"s_waitcnt lgkmcnt(13)\n"
"ds_read_b128 a[188:191], v189 offset:8192\n"
"v_mfma_f32_16x16x32_bf16 v[212:215], a[44:47], a[156:159], v[212:215]\n"
"s_waitcnt lgkmcnt(12)\n"
"ds_read_b64_tr_b16 a[56:57], v23 offset:60832\n"
"ds_read_b64_tr_b16 a[58:59], v23 offset:62368\n"
"v_mfma_f32_16x16x32_bf16 v[216:219], a[108:111], a[156:159], v[216:219]\n"
"s_add_u32 m0, s35, 4096\n"
"ds_read_b64_tr_b16 a[120:121], v72 offset:9824\n"
"buffer_load_dwordx4 v191, s[8:11], s0 offen lds\n"
"v_mfma_f32_16x16x32_bf16 v[244:247], a[24:27], a[176:179], v[244:247]\n"
"s_add_u32 s0, s0, s33\n"
"ds_read_b64_tr_b16 a[122:123], v72 offset:11360\n"
"ds_read_b64_tr_b16 a[124:125], v72 offset:9856\n"
"v_mfma_f32_16x16x32_bf16 v[248:251], a[28:31], a[176:179], v[248:251]\n"
"s_waitcnt lgkmcnt(13)\n"
"ds_read_b64_tr_b16 a[126:127], v72 offset:11392\n"
"ds_read_b64_tr_b16 a[128:129], v72 offset:9888\n"
"v_mfma_f32_16x16x32_bf16 v[220:223], a[112:115], a[156:159], v[220:223]\n"
"s_waitcnt lgkmcnt(13)\n"
"ds_read_b64_tr_b16 a[130:131], v72 offset:11424\n"
"ds_read_b64_tr_b16 a[60:61], v72 offset:13504\n"
"v_mfma_f32_16x16x32_bf16 v[224:227], a[116:119], a[156:159], v[224:227]\n"
"s_waitcnt lgkmcnt(13)\n"
"ds_read_b64_tr_b16 a[62:63], v72 offset:15040\n"
"ds_read_b64_tr_b16 a[64:65], v72 offset:13536\n"
"v_mfma_f32_16x16x32_bf16 v[204:207], a[48:51], a[160:163], v[204:207]\n"
"s_waitcnt lgkmcnt(13)\n"
"ds_read_b64_tr_b16 a[66:67], v72 offset:15072\n"
"ds_read_b64_tr_b16 a[68:69], v72 offset:13568\n"
"v_mfma_f32_16x16x32_bf16 v[208:211], a[52:55], a[160:163], v[208:211]\n"
"s_add_u32 m0, s35, 2048\n"
"ds_read_b64_tr_b16 a[70:71], v72 offset:15104\n"
"buffer_load_dwordx4 v190, s[8:11], s1 offen lds\n"
"v_mfma_f32_16x16x32_bf16 v[252:255], a[32:35], a[176:179], v[252:255]\n"
"ds_read_b64_tr_b16 a[132:133], v72 offset:16576\n"
"ds_read_b64_tr_b16 a[134:135], v72 offset:18112\n"
"v_mfma_f32_16x16x32_bf16 v[104:107], a[96:99], a[176:179], v[104:107]\n"
"s_waitcnt lgkmcnt(14)\n"
"ds_read_b64_tr_b16 a[136:137], v72 offset:16608\n"
"ds_read_b64_tr_b16 a[138:139], v72 offset:18144\n"
"v_mfma_f32_16x16x32_bf16 v[212:215], a[56:59], a[160:163], v[212:215]\n"
"s_waitcnt lgkmcnt(14)\n"
"ds_read_b64_tr_b16 a[140:141], v72 offset:16640\n"
"ds_read_b64_tr_b16 a[142:143], v72 offset:18176\n"
"v_mfma_f32_16x16x32_bf16 v[216:219], a[120:123], a[160:163], v[216:219]\n"
"s_waitcnt lgkmcnt(14)\n"
"s_add_u32 m0, s35, 5120\n"
"v_mfma_f32_16x16x32_bf16 v[220:223], a[124:127], a[160:163], v[220:223]\n"
"s_waitcnt lgkmcnt(12)\n"
"buffer_load_dwordx4 v191, s[8:11], s1 offen lds\n"
"v_mfma_f32_16x16x32_bf16 v[224:227], a[128:131], a[160:163], v[224:227]\n"
"s_waitcnt lgkmcnt(10)\n"
"s_add_u32 s1, s1, s33\n"
"v_mfma_f32_16x16x32_bf16 v[204:207], a[60:63], a[164:167], v[204:207]\n"
"s_waitcnt lgkmcnt(8)\n"
"v_mfma_f32_16x16x32_bf16 v[208:211], a[64:67], a[164:167], v[208:211]\n"
"s_waitcnt lgkmcnt(6)\n"
"v_mfma_f32_16x16x32_bf16 v[212:215], a[68:71], a[164:167], v[212:215]\n"
"s_waitcnt lgkmcnt(4)\n"
"v_mfma_f32_16x16x32_bf16 v[216:219], a[132:135], a[164:167], v[216:219]\n"
"s_waitcnt lgkmcnt(2)\n"
"s_nop 0\n"
"v_cvt_pk_bf16_f32 v204, v204, v205\n"
"v_mfma_f32_16x16x32_bf16 v[220:223], a[136:139], a[164:167], v[220:223]\n"
"s_waitcnt lgkmcnt(0)\n"
"v_cvt_pk_bf16_f32 v205, v206, v207\n"
"v_cvt_pk_bf16_f32 v206, v208, v209\n"
"v_mfma_f32_16x16x32_bf16 v[224:227], a[140:143], a[164:167], v[224:227]\n"
"s_barrier\n"
"v_mfma_f32_16x16x32_bf16 v[108:111], a[100:103], a[176:179], v[108:111]\n"
"v_cvt_pk_bf16_f32 v207, v210, v211\n"
"v_cvt_pk_bf16_f32 v212, v212, v213\n"
"v_mfma_f32_16x16x32_bf16 v[228:231], a[104:107], a[176:179], v[228:231]\n"
"ds_write_b128 v21, v[204:207]\n"
"v_mfma_f32_16x16x32_bf16 v[244:247], a[36:39], a[180:183], v[244:247]\n"
"v_cvt_pk_bf16_f32 v213, v214, v215\n"
"v_cvt_pk_bf16_f32 v216, v216, v217\n"
"v_mfma_f32_16x16x32_bf16 v[248:251], a[40:43], a[180:183], v[248:251]\n"
"v_cvt_pk_bf16_f32 v217, v218, v219\n"
"v_cvt_pk_bf16_f32 v218, v220, v221\n"
"v_mfma_f32_16x16x32_bf16 v[252:255], a[44:47], a[180:183], v[252:255]\n"
"v_cvt_pk_bf16_f32 v219, v222, v223\n"
"v_cvt_pk_bf16_f32 v214, v224, v225\n"
"v_mfma_f32_16x16x32_bf16 v[104:107], a[108:111], a[180:183], v[104:107]\n"
"ds_write_b128 v21, v[216:219] offset:128\n"
"v_mfma_f32_16x16x32_bf16 v[108:111], a[112:115], a[180:183], v[108:111]\n"
"s_add_u32 m0, s34, 6144\n"
"v_cvt_pk_bf16_f32 v215, v226, v227\n"
"v_mfma_f32_16x16x32_bf16 v[228:231], a[116:119], a[180:183], v[228:231]\n"
"buffer_load_dwordx4 v190, s[8:11], s32 offen lds\n"
"ds_write_b128 v21, v[212:215] offset:64\n"
"v_mfma_f32_16x16x32_bf16 v[244:247], a[48:51], a[184:187], v[244:247]\n"
"s_waitcnt vmcnt(11)\n"
"s_add_u32 m0, s34, 9216\n"
"v_mfma_f32_16x16x32_bf16 v[248:251], a[52:55], a[184:187], v[248:251]\n"
"buffer_load_dwordx4 v191, s[8:11], s32 offen lds\n"
"ds_read_b128 v[32:35], v188\n"
"v_mfma_f32_16x16x32_bf16 v[252:255], a[56:59], a[184:187], v[252:255]\n"
"s_add_u32 s32, s32, s33\n"
"ds_read_b128 v[36:39], v189\n"
"ds_read_b64 v[80:81], v20 offset:272\n"
"v_mfma_f32_16x16x32_bf16 v[104:107], a[120:123], a[184:187], v[104:107]\n"
"ds_read_b64 v[86:87], v20 offset:336\n"
"ds_read_b64 v[88:89], v20 offset:544\n"
"v_mfma_f32_16x16x32_bf16 v[108:111], a[124:127], a[184:187], v[108:111]\n"
"ds_read_b64 v[94:95], v20 offset:608\n"
"ds_read_b64 v[96:97], v20 offset:816\n"
"v_mfma_f32_16x16x32_bf16 v[228:231], a[128:131], a[184:187], v[228:231]\n"
"s_add_u32 m0, s34, 7168\n"
"ds_read_b64 v[102:103], v20 offset:880\n"
"buffer_load_dwordx4 v190, s[8:11], s0 offen lds\n"
"v_mfma_f32_16x16x32_bf16 v[244:247], a[60:63], a[188:191], v[244:247]\n"
"v_mfma_f32_16x16x32_bf16 v[248:251], a[64:67], a[188:191], v[248:251]\n"
"v_mfma_f32_16x16x32_bf16 v[252:255], a[68:71], a[188:191], v[252:255]\n"
"s_waitcnt vmcnt(11)\n"
"s_add_u32 m0, s34, 10240\n"
"v_mfma_f32_16x16x32_bf16 v[104:107], a[132:135], a[188:191], v[104:107]\n"
"s_waitcnt lgkmcnt(7)\n"
"buffer_load_dwordx4 v191, s[8:11], s0 offen lds\n"
"ds_read_b128 v[40:43], v188 offset:1024\n"
"v_mfma_f32_16x16x32_bf16 v[108:111], a[136:139], a[188:191], v[108:111]\n"
"s_waitcnt lgkmcnt(6)\n"
"ds_read_b128 v[44:47], v189 offset:1024\n"
"v_cvt_pk_bf16_f32 v244, v244, v245\n"
"v_mfma_f32_16x16x32_bf16 v[228:231], a[140:143], a[188:191], v[228:231]\n"
"s_waitcnt lgkmcnt(5)\n"
"v_cvt_pk_bf16_f32 v245, v246, v247\n"
"v_cvt_pk_bf16_f32 v246, v248, v249\n"
"v_mfma_f32_16x16x32_bf16 v[204:207], a[0:3], v[32:35], 0\n"
"s_waitcnt lgkmcnt(3)\n"
"v_cvt_pk_bf16_f32 v247, v250, v251\n"
"v_cvt_pk_bf16_f32 v252, v252, v253\n"
"v_mfma_f32_16x16x32_bf16 v[208:211], a[4:7], v[32:35], 0\n"
"s_waitcnt lgkmcnt(2)\n"
"ds_write_b128 v22, v[244:247]\n"
"v_mfma_f32_16x16x32_bf16 v[212:215], a[8:11], v[32:35], 0\n"
"s_add_u32 m0, s34, 8192\n"
"v_cvt_pk_bf16_f32 v253, v254, v255\n"
"v_cvt_pk_bf16_f32 v104, v104, v105\n"
"v_mfma_f32_16x16x32_bf16 v[216:219], a[72:75], v[32:35], 0\n"
"s_waitcnt vmcnt(10)\n"
"buffer_load_dwordx4 v190, s[8:11], s1 offen lds\n"
"v_cvt_pk_bf16_f32 v105, v106, v107\n"
"v_mfma_f32_16x16x32_bf16 v[220:223], a[76:79], v[32:35], 0\n"
"s_add_u32 s0, s0, s33\n"
"ds_read_b128 v[48:51], v188 offset:2048\n"
"ds_read_b128 v[52:55], v189 offset:2048\n"
"v_mfma_f32_16x16x32_bf16 v[224:227], a[80:83], v[32:35], 0\n"
"s_add_u32 m0, s34, 11264\n"
"v_cvt_pk_bf16_f32 v106, v108, v109\n"
"v_cvt_pk_bf16_f32 v107, v110, v111\n"
"v_mfma_f32_16x16x32_bf16 v[204:207], a[12:15], v[36:39], v[204:207]\n"
"s_waitcnt lgkmcnt(4)\n"
"buffer_load_dwordx4 v191, s[8:11], s1 offen lds\n"
"ds_write_b128 v22, v[104:107] offset:128\n"
"v_mfma_f32_16x16x32_bf16 v[208:211], a[16:19], v[36:39], v[208:211]\n"
"s_add_u32 s1, s1, s33\n"
"v_cvt_pk_bf16_f32 v254, v228, v229\n"
"v_cvt_pk_bf16_f32 v255, v230, v231\n"
"v_mfma_f32_16x16x32_bf16 v[212:215], a[20:23], v[36:39], v[212:215]\n"
"s_add_u32 m0, s65, 154752\n"
"v_bfi_b32 v80, v4, v80, v26\n"
"v_bfi_b32 v81, v5, v81, v26\n"
"v_mfma_f32_16x16x32_bf16 v[216:219], a[84:87], v[36:39], v[216:219]\n"
"buffer_load_dwordx4 v27, s[12:15], s81 offen lds\n"
"ds_write_b128 v22, v[252:255] offset:64\n"
"v_mfma_f32_16x16x32_bf16 v[220:223], a[88:91], v[36:39], v[220:223]\n"
"v_bfi_b32 v86, v6, v86, v26\n"
"v_bfi_b32 v87, v7, v87, v26\n"
"v_mfma_f32_16x16x32_bf16 v[224:227], a[92:95], v[36:39], v[224:227]\n"
"s_waitcnt vmcnt(11)\n"
"v_bfi_b32 v88, v0, v88, v26\n"
"v_bfi_b32 v89, v1, v89, v26\n"
"v_mfma_f32_16x16x32_bf16 v[204:207], a[24:27], v[40:43], v[204:207]\n"
"s_waitcnt lgkmcnt(5)\n"
"ds_read_b128 a[216:219], v70\n"
"ds_read_b128 a[220:223], v71\n"
"v_mfma_f32_16x16x32_bf16 v[208:211], a[28:31], v[40:43], v[208:211]\n"
"s_add_u32 m0, s65, 155776\n"
"v_bfi_b32 v94, v2, v94, v26\n"
"v_bfi_b32 v95, v3, v95, v26\n"
"v_mfma_f32_16x16x32_bf16 v[212:215], a[32:35], v[40:43], v[212:215]\n"
"buffer_load_dwordx4 v28, s[12:15], s81 offen lds\n"
"v_bfi_b32 v96, v0, v96, v26\n"
"v_mfma_f32_16x16x32_bf16 v[216:219], a[96:99], v[40:43], v[216:219]\n"
"v_bfi_b32 v97, v1, v97, v26\n"
"v_bfi_b32 v102, v2, v102, v26\n"
"v_mfma_f32_16x16x32_bf16 v[220:223], a[100:103], v[40:43], v[220:223]\n"
"s_add_u32 m0, s65, 156800\n"
"v_bfi_b32 v103, v3, v103, v26\n"
"v_mfma_f32_16x16x32_bf16 v[224:227], a[104:107], v[40:43], v[224:227]\n"
"buffer_load_dwordx4 v29, s[12:15], s81 offen lds\n"
"v_mfma_f32_16x16x32_bf16 v[204:207], a[36:39], v[44:47], v[204:207]\n"
"s_waitcnt lgkmcnt(5)\n"
"v_mfma_f32_16x16x32_bf16 v[208:211], a[40:43], v[44:47], v[208:211]\n"
"v_mfma_f32_16x16x32_bf16 v[212:215], a[44:47], v[44:47], v[212:215]\n"
"s_waitcnt lgkmcnt(1)\n"
"v_mfma_f32_16x16x32_bf16 v[244:247], a[0:3], a[216:219], 0\n"
"s_waitcnt lgkmcnt(0)\n"
"v_mfma_f32_16x16x32_bf16 v[252:255], a[8:11], a[216:219], 0\n"
"v_mfma_f32_16x16x32_bf16 v[104:107], a[72:75], a[216:219], 0\n"
"v_mfma_f32_16x16x32_bf16 v[248:251], a[4:7], a[216:219], 0\n"
"v_mfma_f32_16x16x32_bf16 v[108:111], a[76:79], a[216:219], 0\n"
"v_mfma_f32_16x16x32_bf16 v[228:231], a[80:83], a[216:219], 0\n"
"v_mfma_f32_16x16x32_bf16 v[216:219], a[108:111], v[44:47], v[216:219]\n"
"v_mfma_f32_16x16x32_bf16 v[244:247], a[12:15], a[220:223], v[244:247]\n"
"v_mfma_f32_16x16x32_bf16 v[252:255], a[20:23], a[220:223], v[252:255]\n"
"v_mfma_f32_16x16x32_bf16 v[104:107], a[84:87], a[220:223], v[104:107]\n"
"v_mfma_f32_16x16x32_bf16 v[248:251], a[16:19], a[220:223], v[248:251]\n"
"v_mfma_f32_16x16x32_bf16 v[108:111], a[88:91], a[220:223], v[108:111]\n"
"v_mfma_f32_16x16x32_bf16 v[228:231], a[92:95], a[220:223], v[228:231]\n"
"v_mfma_f32_16x16x32_bf16 v[220:223], a[112:115], v[44:47], v[220:223]\n"
"v_mfma_f32_16x16x32_bf16 v[224:227], a[116:119], v[44:47], v[224:227]\n"
"v_mfma_f32_16x16x32_bf16 v[204:207], a[48:51], v[48:51], v[204:207]\n"
"v_mfma_f32_16x16x32_bf16 v[208:211], a[52:55], v[48:51], v[208:211]\n"
"v_mfma_f32_16x16x32_bf16 v[212:215], a[56:59], v[48:51], v[212:215]\n"
"v_mfma_f32_16x16x32_bf16 v[216:219], a[120:123], v[48:51], v[216:219]\n"
"v_mfma_f32_16x16x32_bf16 v[220:223], a[124:127], v[48:51], v[220:223]\n"
"v_mfma_f32_16x16x32_bf16 v[224:227], a[128:131], v[48:51], v[224:227]\n"
"v_mfma_f32_16x16x32_bf16 v[204:207], a[60:63], v[52:55], v[204:207]\n"
"v_mfma_f32_16x16x32_bf16 v[212:215], a[68:71], v[52:55], v[212:215]\n"
"v_mfma_f32_16x16x32_bf16 v[216:219], a[132:135], v[52:55], v[216:219]\n"
"s_waitcnt vmcnt(11)\n"
"ds_read_b128 a[224:227], v70 offset:1024\n"
"v_mfma_f32_16x16x32_bf16 v[208:211], a[64:67], v[52:55], v[208:211]\n"
"ds_read_b128 a[228:231], v71 offset:1024\n"
"v_mfma_f32_16x16x32_bf16 v[220:223], a[136:139], v[52:55], v[220:223]\n"
"s_nop 0\n"
"v_cvt_pk_bf16_f32 v204, v204, v205\n"
"v_mfma_f32_16x16x32_bf16 v[224:227], a[140:143], v[52:55], v[224:227]\n"
"v_cvt_pk_bf16_f32 v212, v212, v213\n"
"v_cvt_pk_bf16_f32 v216, v216, v217\n"
"v_cvt_pk_bf16_f32 v205, v206, v207\n"
"v_cvt_pk_bf16_f32 v213, v214, v215\n"
"v_cvt_pk_bf16_f32 v217, v218, v219\n"
"v_cvt_pk_bf16_f32 v206, v208, v209\n"
"v_cvt_pk_bf16_f32 v207, v210, v211\n"
"v_cvt_pk_bf16_f32 v218, v220, v221\n"
"v_cvt_pk_bf16_f32 v219, v222, v223\n"
"v_cvt_pk_bf16_f32 v214, v224, v225\n"
"v_cvt_pk_bf16_f32 v215, v226, v227\n"
"ds_write_b128 v186, v[204:207] offset:20256\n"
"s_waitcnt vmcnt(9) lgkmcnt(2)\n"
"v_mfma_f32_16x16x32_bf16 v[244:247], a[24:27], a[224:227], v[244:247]\n"
"s_waitcnt lgkmcnt(1)\n"
"ds_read_b128 a[232:235], v70 offset:2048\n"
"v_mfma_f32_16x16x32_bf16 v[252:255], a[32:35], a[224:227], v[252:255]\n"
"ds_read_b128 a[236:239], v71 offset:2048\n"
"ds_write_b128 v186, v[212:215] offset:20320\n"
"v_mfma_f32_16x16x32_bf16 v[104:107], a[96:99], a[224:227], v[104:107]\n"
"v_mfma_f32_16x16x32_bf16 v[248:251], a[28:31], a[224:227], v[248:251]\n"
"ds_write_b128 v186, v[216:219] offset:20384\n"
"v_mfma_f32_16x16x32_bf16 v[108:111], a[100:103], a[224:227], v[108:111]\n"
"v_mfma_f32_16x16x32_bf16 v[228:231], a[104:107], a[224:227], v[228:231]\n"
"s_waitcnt vmcnt(7)\n"
"ds_read_b128 a[240:243], v188 offset:6144\n"
"v_mfma_f32_16x16x32_bf16 v[244:247], a[36:39], a[228:231], v[244:247]\n"
"s_waitcnt lgkmcnt(4)\n"
"ds_read_b128 a[244:247], v189 offset:6144\n"
"v_mfma_f32_16x16x32_bf16 v[252:255], a[44:47], a[228:231], v[252:255]\n"
"s_waitcnt lgkmcnt(4)\n"
"v_mfma_f32_16x16x32_bf16 v[104:107], a[108:111], a[228:231], v[104:107]\n"
"s_add_u32 m0, s35, 0\n"
"s_nop 0\n"
"buffer_load_dwordx4 v27, s[12:15], s3 offen lds\n"
"v_mfma_f32_16x16x32_bf16 v[248:251], a[40:43], a[228:231], v[248:251]\n"
"v_mfma_f32_16x16x32_bf16 v[108:111], a[112:115], a[228:231], v[108:111]\n"
"s_add_u32 m0, s35, 1024\n"
"v_mfma_f32_16x16x32_bf16 v[228:231], a[116:119], a[228:231], v[228:231]\n"
"buffer_load_dwordx4 v28, s[12:15], s3 offen lds\n"
"v_mfma_f32_16x16x32_bf16 v[244:247], a[48:51], a[232:235], v[244:247]\n"
"v_mfma_f32_16x16x32_bf16 v[252:255], a[56:59], a[232:235], v[252:255]\n"
"s_waitcnt lgkmcnt(1)\n"
"v_mfma_f32_16x16x32_bf16 v[120:123], a[0:3], a[240:243], 0\n"
"s_waitcnt vmcnt(7) lgkmcnt(0)\n"
"s_add_u32 m0, s35, 2048\n"
"v_mfma_f32_16x16x32_bf16 v[128:131], a[8:11], a[240:243], 0\n"
"ds_read_b128 a[248:251], v188 offset:7168\n"
"buffer_load_dwordx4 v29, s[12:15], s3 offen lds\n"
"v_mfma_f32_16x16x32_bf16 v[132:135], a[72:75], a[240:243], 0\n"
"ds_read_b128 a[252:255], v189 offset:7168\n"
"v_mfma_f32_16x16x32_bf16 v[124:127], a[4:7], a[240:243], 0\n"
"v_mfma_f32_16x16x32_bf16 v[136:139], a[76:79], a[240:243], 0\n"
"s_add_u32 m0, s34, 0\n"
"v_mfma_f32_16x16x32_bf16 v[140:143], a[80:83], a[240:243], 0\n"
"buffer_load_dwordx4 v190, s[8:11], s32 offen lds\n"
"v_mfma_f32_16x16x32_bf16 v[120:123], a[12:15], a[244:247], v[120:123]\n"
"v_mfma_f32_16x16x32_bf16 v[128:131], a[20:23], a[244:247], v[128:131]\n"
"v_mfma_f32_16x16x32_bf16 v[132:135], a[84:87], a[244:247], v[132:135]\n"
"s_waitcnt vmcnt(7)\n"
"s_add_u32 m0, s34, 3072\n"
"v_mfma_f32_16x16x32_bf16 v[124:127], a[16:19], a[244:247], v[124:127]\n"
"s_waitcnt lgkmcnt(1)\n"
"ds_read_b128 v[56:59], v188 offset:8192\n"
"buffer_load_dwordx4 v191, s[8:11], s32 offen lds\n"
"v_mfma_f32_16x16x32_bf16 v[136:139], a[88:91], a[244:247], v[136:139]\n"
"s_add_u32 s32, s32, s33\n"
"ds_read_b128 v[60:63], v189 offset:8192\n"
"v_mfma_f32_16x16x32_bf16 v[140:143], a[92:95], a[244:247], v[140:143]\n"
"v_mfma_f32_16x16x32_bf16 v[120:123], a[24:27], a[248:251], v[120:123]\n"
"s_waitcnt lgkmcnt(2)\n"
"s_add_u32 m0, s34, 1024\n"
"v_mfma_f32_16x16x32_bf16 v[128:131], a[32:35], a[248:251], v[128:131]\n"
"buffer_load_dwordx4 v190, s[8:11], s0 offen lds\n"
"v_mfma_f32_16x16x32_bf16 v[132:135], a[96:99], a[248:251], v[132:135]\n"
"v_mfma_f32_16x16x32_bf16 v[124:127], a[28:31], a[248:251], v[124:127]\n"
"v_mfma_f32_16x16x32_bf16 v[136:139], a[100:103], a[248:251], v[136:139]\n"
"s_add_u32 m0, s34, 4096\n"
"v_mfma_f32_16x16x32_bf16 v[140:143], a[104:107], a[248:251], v[140:143]\n"
"buffer_load_dwordx4 v191, s[8:11], s0 offen lds\n"
"v_mfma_f32_16x16x32_bf16 v[120:123], a[36:39], a[252:255], v[120:123]\n"
"s_waitcnt lgkmcnt(1)\n"
"s_add_u32 s0, s0, s33\n"
"v_mfma_f32_16x16x32_bf16 v[128:131], a[44:47], a[252:255], v[128:131]\n"
"v_mfma_f32_16x16x32_bf16 v[132:135], a[108:111], a[252:255], v[132:135]\n"
"s_add_u32 m0, s34, 2048\n"
"v_mfma_f32_16x16x32_bf16 v[124:127], a[40:43], a[252:255], v[124:127]\n"
"buffer_load_dwordx4 v190, s[8:11], s1 offen lds\n"
"v_mfma_f32_16x16x32_bf16 v[136:139], a[112:115], a[252:255], v[136:139]\n"
"v_mfma_f32_16x16x32_bf16 v[140:143], a[116:119], a[252:255], v[140:143]\n"
"v_mfma_f32_16x16x32_bf16 v[104:107], a[120:123], a[232:235], v[104:107]\n"
"s_add_u32 m0, s34, 5120\n"
"v_mfma_f32_16x16x32_bf16 v[120:123], a[48:51], v[56:59], v[120:123]\n"
"s_waitcnt lgkmcnt(0)\n"
"buffer_load_dwordx4 v191, s[8:11], s1 offen lds\n"
"v_mfma_f32_16x16x32_bf16 v[128:131], a[56:59], v[56:59], v[128:131]\n"
"s_add_u32 s1, s1, s33\n"
"v_mfma_f32_16x16x32_bf16 v[132:135], a[120:123], v[56:59], v[132:135]\n"
"v_mfma_f32_16x16x32_bf16 v[248:251], a[52:55], a[232:235], v[248:251]\n"
"s_add_u32 m0, s34, 6144\n"
"v_mfma_f32_16x16x32_bf16 v[108:111], a[124:127], a[232:235], v[108:111]\n"
"buffer_load_dwordx4 v190, s[8:11], s32 offen lds\n"
"v_mfma_f32_16x16x32_bf16 v[228:231], a[128:131], a[232:235], v[228:231]\n"
"v_mfma_f32_16x16x32_bf16 v[124:127], a[52:55], v[56:59], v[124:127]\n"
"v_mfma_f32_16x16x32_bf16 v[136:139], a[124:127], v[56:59], v[136:139]\n"
"v_mfma_f32_16x16x32_bf16 v[140:143], a[128:131], v[56:59], v[140:143]\n"
"v_mfma_f32_16x16x32_bf16 v[244:247], a[60:63], a[236:239], v[244:247]\n"
"v_mfma_f32_16x16x32_bf16 v[252:255], a[68:71], a[236:239], v[252:255]\n"
"v_mfma_f32_16x16x32_bf16 v[104:107], a[132:135], a[236:239], v[104:107]\n"
"v_mfma_f32_16x16x32_bf16 v[120:123], a[60:63], v[60:63], v[120:123]\n"
"s_add_u32 m0, s34, 9216\n"
"v_mfma_f32_16x16x32_bf16 v[128:131], a[68:71], v[60:63], v[128:131]\n"
"buffer_load_dwordx4 v191, s[8:11], s32 offen lds\n"
"s_add_u32 s32, s32, s33\n"
"v_mfma_f32_16x16x32_bf16 v[132:135], a[132:135], v[60:63], v[132:135]\n"
"v_cvt_pk_bf16_f32 v244, v244, v245\n"
"v_cvt_pk_bf16_f32 v252, v252, v253\n"
"v_mfma_f32_16x16x32_bf16 v[248:251], a[64:67], a[236:239], v[248:251]\n"
"v_cvt_pk_bf16_f32 v104, v104, v105\n"
"v_cvt_pk_bf16_f32 v120, v120, v121\n"
"v_mfma_f32_16x16x32_bf16 v[108:111], a[136:139], a[236:239], v[108:111]\n"
"v_cvt_pk_bf16_f32 v128, v128, v129\n"
"v_cvt_pk_bf16_f32 v245, v246, v247\n"
"v_mfma_f32_16x16x32_bf16 v[228:231], a[140:143], a[236:239], v[228:231]\n"
"s_add_u32 m0, s34, 7168\n"
"v_cvt_pk_bf16_f32 v132, v132, v133\n"
"v_cvt_pk_bf16_f32 v253, v254, v255\n"
"v_mfma_f32_16x16x32_bf16 v[124:127], a[64:67], v[60:63], v[124:127]\n"
"buffer_load_dwordx4 v190, s[8:11], s0 offen lds\n"
"v_cvt_pk_bf16_f32 v105, v106, v107\n"
"v_mfma_f32_16x16x32_bf16 v[136:139], a[136:139], v[60:63], v[136:139]\n"
"v_cvt_pk_bf16_f32 v121, v122, v123\n"
"v_cvt_pk_bf16_f32 v129, v130, v131\n"
"v_mfma_f32_16x16x32_bf16 v[140:143], a[140:143], v[60:63], v[140:143]\n"
"v_cvt_pk_bf16_f32 v133, v134, v135\n"
"s_add_u32 m0, s34, 10240\n"
"v_cvt_pk_bf16_f32 v246, v248, v249\n"
"v_cvt_pk_bf16_f32 v247, v250, v251\n"
"buffer_load_dwordx4 v191, s[8:11], s0 offen lds\n"
"s_add_u32 s0, s0, s33\n"
"v_cvt_pk_bf16_f32 v106, v108, v109\n"
"v_cvt_pk_bf16_f32 v107, v110, v111\n"
"v_cvt_pk_bf16_f32 v254, v228, v229\n"
"v_cvt_pk_bf16_f32 v255, v230, v231\n"
"v_cvt_pk_bf16_f32 v122, v124, v125\n"
"v_cvt_pk_bf16_f32 v123, v126, v127\n"
"v_cvt_pk_bf16_f32 v134, v136, v137\n"
"v_cvt_pk_bf16_f32 v135, v138, v139\n"
"v_cvt_pk_bf16_f32 v130, v140, v141\n"
"s_add_u32 m0, s34, 8192\n"
"v_cvt_pk_bf16_f32 v131, v142, v143\n"
"ds_write_b128 v186, v[244:247] offset:33760\n"
"buffer_load_dwordx4 v190, s[8:11], s1 offen lds\n"
"s_add_u32 m0, s34, 11264\n"
"ds_write_b128 v186, v[252:255] offset:33824\n"
"buffer_load_dwordx4 v191, s[8:11], s1 offen lds\n"
"s_add_u32 s1, s1, s33\n"
"s_waitcnt vmcnt(12)\n"
"s_barrier\n"
"ds_read_b64_tr_b16 a[144:145], v68\n"
"ds_read_b64_tr_b16 a[146:147], v68 offset:1536\n"
"ds_read_b64_tr_b16 a[156:157], v68 offset:6144\n"
"ds_read_b64_tr_b16 a[158:159], v68 offset:7680\n"
"ds_read_b64_tr_b16 a[168:169], v68 offset:12288\n"
"ds_read_b64_tr_b16 a[170:171], v68 offset:13824\n"
"ds_read_b64_tr_b16 a[148:149], v68 offset:32\n"
"ds_read_b64_tr_b16 a[150:151], v68 offset:1568\n"
"ds_read_b64_tr_b16 a[180:181], v68 offset:18432\n"
"ds_read_b64_tr_b16 a[182:183], v68 offset:19968\n"
"ds_read_b64_tr_b16 a[152:153], v68 offset:64\n"
"ds_read_b64_tr_b16 a[154:155], v68 offset:1600\n"
"ds_read_b64_tr_b16 a[160:161], v68 offset:6176\n"
"ds_read_b64_tr_b16 a[162:163], v68 offset:7712\n"
"ds_read_b64_tr_b16 a[192:193], v68 offset:24576\n"
"ds_read_b64_tr_b16 a[194:195], v68 offset:26112\n"
"ds_write_b128 v186, v[104:107] offset:33888\n"
"s_waitcnt lgkmcnt(15)\n"
"v_mfma_f32_16x16x32_bf16 v[204:207], a[144:147], v[32:35], 0\n"
"s_waitcnt lgkmcnt(13)\n"
"v_mfma_f32_16x16x32_bf16 v[204:207], a[156:159], v[36:39], v[204:207]\n"
"s_waitcnt lgkmcnt(11)\n"
"ds_write_b128 v186, v[120:123] offset:47264\n"
"v_mfma_f32_16x16x32_bf16 v[204:207], a[168:171], v[40:43], v[204:207]\n"
"s_waitcnt lgkmcnt(8)\n"
"buffer_load_dword v199, off, s[28:31], s39 sc1\n"
"v_mfma_f32_16x16x32_bf16 v[208:211], a[148:151], v[32:35], 0\n"
"s_waitcnt lgkmcnt(6)\n"
"ds_write_b128 v186, v[128:131] offset:47328\n"
"v_mfma_f32_16x16x32_bf16 v[204:207], a[180:183], v[44:47], v[204:207]\n"
"s_waitcnt lgkmcnt(3)\n"
"v_mfma_f32_16x16x32_bf16 v[212:215], a[152:155], v[32:35], 0\n"
"ds_write_b128 v186, v[132:135] offset:47392\n"
"v_mfma_f32_16x16x32_bf16 v[208:211], a[160:163], v[36:39], v[208:211]\n"
"v_mfma_f32_16x16x32_bf16 v[204:207], a[192:195], v[48:51], v[204:207]\n"
"ds_read_b64_tr_b16 a[164:165], v68 offset:6208\n"
"v_mfma_f32_16x16x32_bf16 v[220:223], a[148:151], a[216:219], 0\n"
"ds_read_b64_tr_b16 a[166:167], v68 offset:7744\n"
"ds_read_b64_tr_b16 a[172:173], v68 offset:12320\n"
"v_mfma_f32_16x16x32_bf16 v[224:227], a[152:155], a[216:219], 0\n"
"ds_read_b64_tr_b16 a[174:175], v68 offset:13856\n"
"ds_read_b64_tr_b16 a[204:205], v68 offset:27648\n"
"v_mfma_f32_16x16x32_bf16 v[216:219], a[144:147], a[216:219], 0\n"
"ds_read_b64_tr_b16 a[206:207], v68 offset:29184\n"
"ds_read_b64_tr_b16 a[176:177], v68 offset:12352\n"
"v_mfma_f32_16x16x32_bf16 v[228:231], a[144:147], a[240:243], 0\n"
"ds_read_b64_tr_b16 a[178:179], v68 offset:13888\n"
"ds_read_b64_tr_b16 a[184:185], v68 offset:18464\n"
"v_mfma_f32_16x16x32_bf16 v[220:223], a[160:163], a[220:223], v[220:223]\n"
"ds_read_b64_tr_b16 a[186:187], v68 offset:20000\n"
"ds_read_b64_tr_b16 a[188:189], v68 offset:18496\n"
"v_mfma_f32_16x16x32_bf16 v[216:219], a[156:159], a[220:223], v[216:219]\n"
"s_waitcnt lgkmcnt(11)\n"
"ds_read_b64_tr_b16 a[190:191], v68 offset:20032\n"
"ds_read_b64_tr_b16 a[196:197], v68 offset:24608\n"
"v_mfma_f32_16x16x32_bf16 v[228:231], a[156:159], a[244:247], v[228:231]\n"
"s_barrier\n"
"v_mfma_f32_16x16x32_bf16 v[216:219], a[168:171], a[224:227], v[216:219]\n"
"s_waitcnt lgkmcnt(11)\n"
"ds_read_b64_tr_b16 a[198:199], v68 offset:26144\n"
"ds_read_b64_tr_b16 a[200:201], v68 offset:24640\n"
"v_mfma_f32_16x16x32_bf16 v[212:215], a[164:167], v[36:39], v[212:215]\n"
"s_waitcnt lgkmcnt(11)\n"
"ds_read_b64_tr_b16 a[202:203], v68 offset:26176\n"
"ds_read_b64_tr_b16 a[208:209], v68 offset:27680\n"
"v_mfma_f32_16x16x32_bf16 v[208:211], a[172:175], v[40:43], v[208:211]\n"
"s_waitcnt lgkmcnt(11)\n"
"ds_read_b64_tr_b16 a[210:211], v68 offset:29216\n"
"ds_read_b64_tr_b16 a[212:213], v68 offset:27712\n"
"v_mfma_f32_16x16x32_bf16 v[204:207], a[204:207], v[52:55], v[204:207]\n"
"s_waitcnt lgkmcnt(11)\n"
"ds_read_b64_tr_b16 a[214:215], v68 offset:29248\n"
"ds_read_b128 v[112:115], v178\n"
"v_mfma_f32_16x16x32_bf16 v[212:215], a[176:179], v[40:43], v[212:215]\n"
"s_waitcnt lgkmcnt(11)\n"
"ds_read_b128 v[116:119], v179\n"
"ds_read_b128 v[120:123], v178 offset:13504\n"
"v_mfma_f32_16x16x32_bf16 v[208:211], a[184:187], v[44:47], v[208:211]\n"
"s_waitcnt lgkmcnt(11)\n"
"ds_read_b128 v[124:127], v179 offset:13504\n"
"v_cvt_pk_bf16_f32 v144, v204, v205\n"
"v_mfma_f32_16x16x32_bf16 v[212:215], a[188:191], v[44:47], v[212:215]\n"
"ds_read_b128 v[128:131], v178 offset:27008\n"
"v_cvt_pk_bf16_f32 v145, v206, v207\n"
"v_mfma_f32_16x16x32_bf16 v[204:207], a[148:151], a[240:243], 0\n"
"s_waitcnt lgkmcnt(11)\n"
"ds_read_b128 v[132:135], v179 offset:27008\n"
"v_mfma_f32_16x16x32_bf16 v[208:211], a[196:199], v[48:51], v[208:211]\n"
"s_waitcnt lgkmcnt(10)\n"
"ds_read_b128 v[136:139], v180\n"
"v_mfma_f32_16x16x32_bf16 v[212:215], a[200:203], v[48:51], v[212:215]\n"
"s_waitcnt lgkmcnt(9)\n"
"ds_read_b128 v[140:143], v181\n"
"v_mfma_f32_16x16x32_bf16 v[208:211], a[208:211], v[52:55], v[208:211]\n"
"s_waitcnt lgkmcnt(8)\n"
"ds_read_b64 v[104:105], v20 offset:1088\n"
"v_mfma_f32_16x16x32_bf16 v[224:227], a[164:167], a[220:223], v[224:227]\n"
"ds_read_b64 v[110:111], v20 offset:1152\n"
"ds_read_b64 v[82:83], v178 offset:64\n"
"v_mfma_f32_16x16x32_bf16 v[204:207], a[160:163], a[244:247], v[204:207]\n"
"ds_read_b64 v[84:85], v179 offset:64\n"
"v_subrev_u32 v20, s49, v20\n"
"v_mfma_f32_16x16x32_bf16 v[212:215], a[212:215], v[52:55], v[212:215]\n"
"ds_read_b64 v[90:91], v178 offset:13568\n"
"v_cvt_pk_bf16_f32 v146, v208, v209\n"
"v_mfma_f32_16x16x32_bf16 v[220:223], a[172:175], a[224:227], v[220:223]\n"
"ds_read_b64 v[92:93], v179 offset:13568\n"
"v_cvt_pk_bf16_f32 v147, v210, v211\n"
"v_mfma_f32_16x16x32_bf16 v[224:227], a[176:179], a[224:227], v[224:227]\n"
"s_waitcnt lgkmcnt(9)\n"
"ds_read_b64 v[98:99], v178 offset:27072\n"
"v_cvt_pk_bf16_f32 v158, v212, v213\n"
"v_mfma_f32_16x16x32_bf16 v[228:231], a[168:171], a[248:251], v[228:231]\n"
"s_waitcnt lgkmcnt(9)\n"
"ds_read_b64 v[100:101], v179 offset:27072\n"
"v_cvt_pk_bf16_f32 v159, v214, v215\n"
"v_mfma_f32_16x16x32_bf16 v[204:207], a[172:175], a[248:251], v[204:207]\n"
"s_waitcnt lgkmcnt(9)\n"
"ds_read_b64 v[106:107], v180 offset:64\n"
"v_subrev_u32 v178, s48, v178\n"
"v_mfma_f32_16x16x32_bf16 v[220:223], a[184:187], a[228:231], v[220:223]\n"
"s_waitcnt lgkmcnt(9)\n"
"ds_read_b64 v[108:109], v181 offset:64\n"
"v_subrev_u32 v179, s48, v179\n"
"v_mfma_f32_16x16x32_bf16 v[224:227], a[188:191], a[228:231], v[224:227]\n"
"s_waitcnt lgkmcnt(8)\n"
"ds_read_b64_tr_b16 v[244:245], v182 offset:128\n"
"v_bfi_b32 v104, v8, v104, v26\n"
"v_mfma_f32_16x16x32_bf16 v[216:219], a[180:183], a[228:231], v[216:219]\n"
"s_waitcnt vmcnt(11)\n"
"ds_read_b128 v[32:35], v188\n"
"v_bfi_b32 v105, v9, v105, v26\n"
"v_mfma_f32_16x16x32_bf16 v[228:231], a[180:183], a[252:255], v[228:231]\n"
"ds_read_b128 v[36:39], v189\n"
"v_bfi_b32 v110, v10, v110, v26\n"
"v_mfma_f32_16x16x32_bf16 v[204:207], a[184:187], a[252:255], v[204:207]\n"
"ds_read_b64_tr_b16 v[246:247], v183 offset:128\n"
"v_bfi_b32 v111, v11, v111, v26\n"
"v_mfma_f32_16x16x32_bf16 v[220:223], a[196:199], a[232:235], v[220:223]\n"
"ds_read_b64_tr_b16 v[248:249], v182 offset:136\n"
"v_subrev_u32 v180, s48, v180\n"
"v_mfma_f32_16x16x32_bf16 v[224:227], a[200:203], a[232:235], v[224:227]\n"
"ds_read_b64_tr_b16 v[250:251], v183 offset:136\n"
"v_subrev_u32 v181, s48, v181\n"
"v_mfma_f32_16x16x32_bf16 v[112:115], v[112:115], v[144:147], 0\n"
"s_waitcnt lgkmcnt(13)\n"
"ds_read_b64_tr_b16 v[252:253], v182 offset:72\n"
"v_mfma_f32_16x16x32_bf16 v[116:119], v[116:119], v[144:147], 0\n"
"s_waitcnt lgkmcnt(13)\n"
"ds_read_b64_tr_b16 v[254:255], v183 offset:72\n"
"v_mfma_f32_16x16x32_bf16 v[120:123], v[120:123], v[144:147], 0\n"
"s_waitcnt lgkmcnt(13)\n"
"v_mfma_f32_16x16x32_bf16 v[124:127], v[124:127], v[144:147], 0\n"
"s_waitcnt vmcnt(9) lgkmcnt(12)\n"
"ds_read_b128 v[40:43], v188 offset:1024\n"
"ds_read_b128 v[44:47], v189 offset:1024\n"
"v_mfma_f32_16x16x32_bf16 v[216:219], a[192:195], a[232:235], v[216:219]\n"
"s_waitcnt lgkmcnt(8)\n"
"v_mfma_f32_16x16x32_bf16 v[228:231], a[192:195], v[56:59], v[228:231]\n"
"s_waitcnt lgkmcnt(7)\n"
"v_mfma_f32_16x16x32_bf16 v[204:207], a[196:199], v[56:59], v[204:207]\n"
"s_add_u32 m0, s34, 0\n"
"s_nop 0\n"
"buffer_load_dwordx4 v190, s[8:11], s32 offen lds\n"
"v_mfma_f32_16x16x32_bf16 v[128:131], v[128:131], v[144:147], 0\n"
"v_mfma_f32_16x16x32_bf16 v[132:135], v[132:135], v[144:147], 0\n"
"s_add_u32 m0, s34, 3072\n"
"v_mfma_f32_16x16x32_bf16 v[136:139], v[136:139], v[144:147], 0\n"
".Lic0%=:\n"
"s_cbranch_execz .Lib0%=\n"
"buffer_load_dwordx4 v191, s[8:11], s32 offen lds\n"
"v_mfma_f32_16x16x32_bf16 v[140:143], v[140:143], v[144:147], 0\n"
"s_add_u32 s32, s32, s33\n"
"v_mfma_f32_16x16x32_bf16 v[220:223], a[208:211], a[236:239], v[220:223]\n"
"s_waitcnt vmcnt(9)\n"
"ds_read_b128 v[48:51], v188 offset:2048\n"
"ds_read_b128 v[52:55], v189 offset:2048\n"
"v_mfma_f32_16x16x32_bf16 v[224:227], a[212:215], a[236:239], v[224:227]\n"
"v_mfma_f32_16x16x32_bf16 v[112:115], v[80:83], v[156:159], v[112:115]\n"
"ds_read_b64 v[80:81], v20 offset:272\n"
"v_mfma_f32_16x16x32_bf16 v[116:119], v[84:87], v[158:161], v[116:119]\n"
"ds_read_b64 v[86:87], v20 offset:336\n"
"v_cvt_pk_bf16_f32 v150, v220, v221\n"
"v_mfma_f32_16x16x32_bf16 v[120:123], v[88:91], v[156:159], v[120:123]\n"
"ds_read_b64 v[88:89], v20 offset:544\n"
"v_cvt_pk_bf16_f32 v151, v222, v223\n"
"v_mfma_f32_16x16x32_bf16 v[124:127], v[92:95], v[158:161], v[124:127]\n"
"ds_read_b64 v[94:95], v20 offset:608\n"
"v_cvt_pk_bf16_f32 v162, v224, v225\n"
"v_mfma_f32_16x16x32_bf16 v[216:219], a[204:207], a[236:239], v[216:219]\n"
"v_cvt_pk_bf16_f32 v163, v226, v227\n"
"v_max_f32 v112, v112, v116\n"
"v_mfma_f32_16x16x32_bf16 v[228:231], a[204:207], v[60:63], v[228:231]\n"
"v_max_f32 v113, v113, v117\n"
"v_max_f32 v114, v114, v118\n"
"v_mfma_f32_16x16x32_bf16 v[204:207], a[208:211], v[60:63], v[204:207]\n"
"ds_read_b64_tr_b16 v[116:117], v182 offset:13632\n"
"v_max_f32 v115, v115, v119\n"
"v_mfma_f32_16x16x32_bf16 v[128:131], v[96:99], v[156:159], v[128:131]\n"
"s_waitcnt lgkmcnt(4)\n"
"ds_read_b64_tr_b16 v[118:119], v183 offset:13632\n"
"ds_read_b64_tr_b16 v[98:99], v182 offset:27144\n"
"v_mfma_f32_16x16x32_bf16 v[132:135], v[100:103], v[158:161], v[132:135]\n"
"s_waitcnt lgkmcnt(5)\n"
"ds_read_b64_tr_b16 v[100:101], v183 offset:27144\n"
"v_max_f32 v170, v112, v113\n"
"v_mfma_f32_16x16x32_bf16 v[136:139], v[104:107], v[156:159], v[136:139]\n"
"s_waitcnt lgkmcnt(5)\n"
"ds_read_b64_tr_b16 v[106:107], v182 offset:27080\n"
"v_max_f32 v171, v114, v115\n"
"v_mfma_f32_16x16x32_bf16 v[140:143], v[108:111], v[158:161], v[140:143]\n"
"s_waitcnt lgkmcnt(5)\n"
"ds_read_b64_tr_b16 v[108:109], v183 offset:27080\n"
"v_max_f32 v120, v120, v124\n"
"v_mfma_f32_16x16x32_bf16 v[212:215], a[0:3], v[32:35], 0\n"
"s_add_u32 m0, s34, 1024\n"
"ds_read_b64 v[96:97], v20 offset:816\n"
"buffer_load_dwordx4 v190, s[8:11], s0 offen lds\n"
"v_mfma_f32_16x16x32_bf16 v[220:223], a[8:11], v[32:35], 0\n"
"ds_read_b64 v[102:103], v20 offset:880\n"
"ds_read_b64 v[104:105], v20 offset:1088\n"
"v_mfma_f32_16x16x32_bf16 v[224:227], a[72:75], v[32:35], 0\n"
"s_add_u32 m0, s34, 4096\n"
"ds_read_b64 v[110:111], v20 offset:1152\n"
"v_max_f32 v121, v121, v125\n"
"v_mfma_f32_16x16x32_bf16 v[212:215], a[12:15], v[36:39], v[212:215]\n"
"ds_read_b64_tr_b16 v[124:125], v182 offset:13640\n"
"buffer_load_dwordx4 v191, s[8:11], s0 offen lds\n"
"v_mfma_f32_16x16x32_bf16 v[220:223], a[20:23], v[36:39], v[220:223]\n"
"s_add_u32 s0, s0, s33\n"
"v_max_f32 v122, v122, v126\n"
"v_max_f32 v123, v123, v127\n"
"v_mfma_f32_16x16x32_bf16 v[224:227], a[84:87], v[36:39], v[224:227]\n"
"ds_read_b64_tr_b16 v[126:127], v183 offset:13640\n"
"v_cvt_pk_bf16_f32 v148, v216, v217\n"
"v_mfma_f32_16x16x32_bf16 v[212:215], a[24:27], v[40:43], v[212:215]\n"
"s_add_u32 m0, s34, 2048\n"
"v_cvt_pk_bf16_f32 v149, v218, v219\n"
"v_cvt_pk_bf16_f32 v152, v228, v229\n"
"v_mfma_f32_16x16x32_bf16 v[216:219], a[4:7], v[32:35], 0\n"
"v_cvt_pk_bf16_f32 v153, v230, v231\n"
"v_cvt_pk_bf16_f32 v154, v204, v205\n"
"v_mfma_f32_16x16x32_bf16 v[228:231], a[76:79], v[32:35], 0\n"
"s_waitcnt lgkmcnt(5)\n"
"buffer_load_dwordx4 v190, s[8:11], s1 offen lds\n"
"v_cvt_pk_bf16_f32 v155, v206, v207\n"
"v_mfma_f32_16x16x32_bf16 v[204:207], a[80:83], v[32:35], 0\n"
"s_waitcnt lgkmcnt(3)\n"
"v_max3_f32 v170, v170, v120, v121\n"
"v_max3_f32 v171, v171, v122, v123\n"
"v_mfma_f32_16x16x32_bf16 v[216:219], a[16:19], v[36:39], v[216:219]\n"
"s_waitcnt lgkmcnt(2)\n"
"v_max_f32 v128, v128, v132\n"
"v_max_f32 v129, v129, v133\n"
"v_mfma_f32_16x16x32_bf16 v[228:231], a[88:91], v[36:39], v[228:231]\n"
"s_add_u32 m0, s34, 5120\n"
"ds_read_b64_tr_b16 v[132:133], v182 offset:13576\n"
"buffer_load_dwordx4 v191, s[8:11], s1 offen lds\n"
"v_mfma_f32_16x16x32_bf16 v[204:207], a[92:95], v[36:39], v[204:207]\n"
"s_add_u32 s1, s1, s33\n"
"v_max_f32 v130, v130, v134\n"
"v_max_f32 v131, v131, v135\n"
"v_mfma_f32_16x16x32_bf16 v[220:223], a[32:35], v[40:43], v[220:223]\n"
"ds_read_b64_tr_b16 v[134:135], v183 offset:13576\n"
"v_max3_f32 v170, v170, v128, v129\n"
"v_mfma_f32_16x16x32_bf16 v[224:227], a[96:99], v[40:43], v[224:227]\n"
"v_max3_f32 v171, v171, v130, v131\n"
"v_max_f32 v136, v136, v140\n"
"v_mfma_f32_16x16x32_bf16 v[216:219], a[28:31], v[40:43], v[216:219]\n"
"v_max_f32 v137, v137, v141\n"
"v_max_f32 v138, v138, v142\n"
"v_mfma_f32_16x16x32_bf16 v[228:231], a[100:103], v[40:43], v[228:231]\n"
"ds_read_b64_tr_b16 v[140:141], v182 offset:27136\n"
"v_max_f32 v139, v139, v143\n"
"v_mfma_f32_16x16x32_bf16 v[204:207], a[104:107], v[40:43], v[204:207]\n"
"ds_read_b64_tr_b16 v[142:143], v183 offset:27136\n"
"v_max3_f32 v170, v170, v136, v137\n"
"v_mfma_f32_16x16x32_bf16 v[212:215], a[36:39], v[44:47], v[212:215]\n"
"v_max3_f32 v171, v171, v138, v139\n"
"v_max_f32 v170, v170, v171\n"
"v_mfma_f32_16x16x32_bf16 v[220:223], a[44:47], v[44:47], v[220:223]\n"
"v_mov_b32 v171, v170\n"
"v_bfi_b32 v80, v4, v80, v26\n"
"v_mfma_f32_16x16x32_bf16 v[224:227], a[108:111], v[44:47], v[224:227]\n"
"v_permlane16_swap_b32 v170, v171\n"
"v_mfma_f32_16x16x32_bf16 v[216:219], a[40:43], v[44:47], v[216:219]\n"
"v_max_f32 v170, v170, v171\n"
"v_mov_b32 v171, v170\n"
"v_mfma_f32_16x16x32_bf16 v[228:231], a[112:115], v[44:47], v[228:231]\n"
"s_nop 0\n"
"v_permlane32_swap_b32 v170, v171\n"
"v_mfma_f32_16x16x32_bf16 v[204:207], a[116:119], v[44:47], v[204:207]\n"
"v_max_f32 v170, v170, v171\n"
"v_mul_f32 v170, 0xbe553b95, v170\n"
"v_mfma_f32_16x16x32_bf16 v[212:215], a[48:51], v[48:51], v[212:215]\n"
"v_fmamk_f32 v112, v112, 0x3e553b95, v170\n"
"v_fmamk_f32 v113, v113, 0x3e553b95, v170\n"
"v_mfma_f32_16x16x32_bf16 v[220:223], a[56:59], v[48:51], v[220:223]\n"
"v_fmamk_f32 v114, v114, 0x3e553b95, v170\n"
"v_fmamk_f32 v115, v115, 0x3e553b95, v170\n"
"v_mfma_f32_16x16x32_bf16 v[224:227], a[120:123], v[48:51], v[224:227]\n"
"v_exp_f32 v112, v112\n"
"v_mfma_f32_16x16x32_bf16 v[216:219], a[52:55], v[48:51], v[216:219]\n"
"v_exp_f32 v113, v113\n"
"v_mfma_f32_16x16x32_bf16 v[228:231], a[124:127], v[48:51], v[228:231]\n"
"v_exp_f32 v114, v114\n"
"v_mfma_f32_16x16x32_bf16 v[204:207], a[128:131], v[48:51], v[204:207]\n"
"s_waitcnt vmcnt(6)\n"
"v_exp_f32 v115, v115\n"
"v_mfma_f32_16x16x32_bf16 v[212:215], a[60:63], v[52:55], v[212:215]\n"
"v_and_b32 v199, 0xffffff00, v199\n"
"v_add_u32 v199, 0x100, v199\n"
"v_mfma_f32_16x16x32_bf16 v[220:223], a[68:71], v[52:55], v[220:223]\n"
"buffer_store_dword v199, v196, s[28:31], s41 offen sc1\n"
"v_mfma_f32_16x16x32_bf16 v[224:227], a[132:135], v[52:55], v[224:227]\n"
"s_add_u32 s41, s40, 131072\n"
"v_cvt_pk_bf16_f32 v82, v112, v113\n"
"v_cvt_pk_bf16_f32 v83, v114, v115\n"
"v_mfma_f32_16x16x32_bf16 v[216:219], a[64:67], v[52:55], v[216:219]\n"
"v_fmamk_f32 v120, v120, 0x3e553b95, v170\n"
"v_fmamk_f32 v121, v121, 0x3e553b95, v170\n"
"v_mfma_f32_16x16x32_bf16 v[228:231], a[136:139], v[52:55], v[228:231]\n"
"v_fmamk_f32 v122, v122, 0x3e553b95, v170\n"
"v_fmamk_f32 v123, v123, 0x3e553b95, v170\n"
"v_mfma_f32_16x16x32_bf16 v[204:207], a[140:143], v[52:55], v[204:207]\n"
"v_and_b32 v112, v82, v176\n"
"v_and_b32 v113, v83, v177\n"
"v_mfma_f32_16x16x32_bf16 v[208:211], a[152:155], a[240:243], 0\n"
"v_exp_f32 v120, v120\n"
"v_mfma_f32_16x16x32_bf16 v[208:211], a[164:167], a[244:247], v[208:211]\n"
"v_exp_f32 v121, v121\n"
"v_mfma_f32_16x16x32_bf16 v[208:211], a[176:179], a[248:251], v[208:211]\n"
"v_exp_f32 v122, v122\n"
"v_mfma_f32_16x16x32_bf16 v[208:211], a[188:191], a[252:255], v[208:211]\n"
"v_exp_f32 v123, v123\n"
"v_mfma_f32_16x16x32_bf16 v[208:211], a[200:203], v[56:59], v[208:211]\n"
"v_xor_b32 v114, v82, v112\n"
"v_xor_b32 v115, v83, v113\n"
"v_mfma_f32_16x16x32_bf16 v[208:211], a[212:215], v[60:63], v[208:211]\n"
"v_cvt_pk_bf16_f32 v84, v120, v121\n"
"v_cvt_pk_bf16_f32 v85, v122, v123\n"
"v_mfma_f32_16x16x32_bf16 v[244:247], v[244:247], v[112:115], 0\n"
"v_and_b32 v120, v84, v176\n"
"v_and_b32 v121, v85, v177\n"
"v_mfma_f32_16x16x32_bf16 v[248:251], v[248:251], v[112:115], 0\n"
"v_xor_b32 v122, v84, v120\n"
"v_xor_b32 v123, v85, v121\n"
"v_mfma_f32_16x16x32_bf16 v[252:255], v[252:255], v[112:115], 0\n"
"s_waitcnt lgkmcnt(2)\n"
"ds_read_b64_tr_b16 v[112:113], v184 offset:128\n"
"ds_read_b64_tr_b16 v[114:115], v185 offset:128\n"
"v_mfma_f32_16x16x32_bf16 v[244:247], v[116:119], v[120:123], v[244:247]\n"
"ds_read_b64_tr_b16 v[116:117], v184 offset:72\n"
"v_cvt_pk_bf16_f32 v212, v212, v213\n"
"v_mfma_f32_16x16x32_bf16 v[248:251], v[124:127], v[120:123], v[248:251]\n"
"ds_read_b64_tr_b16 v[118:119], v185 offset:72\n"
"v_cvt_pk_bf16_f32 v220, v220, v221\n"
"v_mfma_f32_16x16x32_bf16 v[252:255], v[132:135], v[120:123], v[252:255]\n"
"ds_read_b64_tr_b16 v[120:121], v184 offset:136\n"
"v_cvt_pk_bf16_f32 v224, v224, v225\n"
"v_mfma_f32_16x16x32_bf16 v[82:85], v[172:175], v[82:85], 0\n"
"v_cvt_pk_bf16_f32 v213, v214, v215\n"
"v_cvt_pk_bf16_f32 v221, v222, v223\n"
"v_cvt_pk_bf16_f32 v225, v226, v227\n"
"v_cvt_pk_bf16_f32 v214, v216, v217\n"
"v_cvt_pk_bf16_f32 v215, v218, v219\n"
"v_mfma_f32_16x16x32_bf16 v[216:219], a[148:151], v[32:35], 0\n"
"ds_read_b64_tr_b16 v[122:123], v185 offset:136\n"
"ds_write_b128 v186, v[212:215] offset:60768\n"
"v_mfma_f32_16x16x32_bf16 v[212:215], a[152:155], v[32:35], 0\n"
"v_cvt_pk_bf16_f32 v226, v228, v229\n"
"v_cvt_pk_bf16_f32 v227, v230, v231\n"
"v_mfma_f32_16x16x32_bf16 v[216:219], a[160:163], v[36:39], v[216:219]\n"
"v_cvt_pk_bf16_f32 v222, v204, v205\n"
"v_cvt_pk_bf16_f32 v223, v206, v207\n"
"v_mfma_f32_16x16x32_bf16 v[212:215], a[164:167], v[36:39], v[212:215]\n"
"ds_write_b128 v186, v[220:223] offset:60832\n"
"v_mfma_f32_16x16x32_bf16 v[216:219], a[172:175], v[40:43], v[216:219]\n"
"v_fmamk_f32 v128, v128, 0x3e553b95, v170\n"
"v_fmamk_f32 v129, v129, 0x3e553b95, v170\n"
"v_mfma_f32_16x16x32_bf16 v[212:215], a[176:179], v[40:43], v[212:215]\n"
"v_fmamk_f32 v130, v130, 0x3e553b95, v170\n"
"v_fmamk_f32 v131, v131, 0x3e553b95, v170\n"
"v_mfma_f32_16x16x32_bf16 v[216:219], a[184:187], v[44:47], v[216:219]\n"
"ds_write_b128 v186, v[224:227] offset:60896\n"
"v_mfma_f32_16x16x32_bf16 v[212:215], a[188:191], v[44:47], v[212:215]\n"
"v_fmamk_f32 v136, v136, 0x3e553b95, v170\n"
"v_fmamk_f32 v137, v137, 0x3e553b95, v170\n"
"v_mfma_f32_16x16x32_bf16 v[216:219], a[196:199], v[48:51], v[216:219]\n"
"v_fmamk_f32 v138, v138, 0x3e553b95, v170\n"
"v_fmamk_f32 v139, v139, 0x3e553b95, v170\n"
"v_mfma_f32_16x16x32_bf16 v[212:215], a[200:203], v[48:51], v[212:215]\n"
"ds_read_b128 v[124:127], v178 offset:13504\n"
"ds_read_b128 v[132:135], v179 offset:13504\n"
"v_mfma_f32_16x16x32_bf16 v[216:219], a[208:211], v[52:55], v[216:219]\n"
"v_exp_f32 v128, v128\n"
"v_mfma_f32_16x16x32_bf16 v[212:215], a[212:215], v[52:55], v[212:215]\n"
"v_exp_f32 v129, v129\n"
"v_exp_f32 v130, v130\n"
"v_exp_f32 v131, v131\n"
"v_exp_f32 v136, v136\n"
"v_exp_f32 v137, v137\n"
"v_exp_f32 v138, v138\n"
"v_exp_f32 v139, v139\n"
"v_cvt_pk_bf16_f32 v90, v128, v129\n"
"v_cvt_pk_bf16_f32 v91, v130, v131\n"
"v_cvt_pk_bf16_f32 v92, v136, v137\n"
"v_cvt_pk_bf16_f32 v93, v138, v139\n"
"v_and_b32 v128, v90, v176\n"
"v_and_b32 v129, v91, v177\n"
"v_and_b32 v136, v92, v176\n"
"v_and_b32 v137, v93, v177\n"
"v_xor_b32 v130, v90, v128\n"
"v_xor_b32 v131, v91, v129\n"
"v_xor_b32 v138, v92, v136\n"
"v_xor_b32 v139, v93, v137\n"
"v_mfma_f32_16x16x32_bf16 v[90:93], v[172:175], v[90:93], v[82:85]\n"
"s_waitcnt lgkmcnt(0)\n"
"v_bfi_b32 v81, v5, v81, v26\n"
"v_bfi_b32 v86, v6, v86, v26\n"
"v_mfma_f32_16x16x32_bf16 v[244:247], v[140:143], v[128:131], v[244:247]\n"
"s_barrier\n"
"v_mfma_f32_16x16x32_bf16 v[248:251], v[98:101], v[128:131], v[248:251]\n"
"s_add_u32 m0, s79, 130176\n"
"ds_read_b128 v[140:143], v179 offset:27008\n"
"buffer_load_dwordx4 v192, s[16:19], s43 offen lds\n"
"v_mfma_f32_16x16x32_bf16 v[252:255], v[106:109], v[128:131], v[252:255]\n"
"ds_read_b128 v[128:131], v178 offset:27008\n"
"ds_read_b64 v[82:83], v178 offset:13568\n"
"v_mfma_f32_16x16x32_bf16 v[244:247], v[112:115], v[136:139], v[244:247]\n"
"s_add_u32 m0, s79, 130176\n"
"ds_read_b128 v[112:115], v181\n"
"v_bfi_b32 v87, v7, v87, v26\n"
"v_mfma_f32_16x16x32_bf16 v[248:251], v[120:123], v[136:139], v[248:251]\n"
"ds_read_b128 v[120:123], v180 offset:13504\n"
"buffer_load_dwordx4 v192, s[16:19], s43 offen offset:1024 lds\n"
"v_mfma_f32_16x16x32_bf16 v[252:255], v[116:119], v[136:139], v[252:255]\n"
"ds_read_b128 v[136:139], v180\n"
"v_bfi_b32 v88, v0, v88, v26\n"
"v_mfma_f32_16x16x32_bf16 v[124:127], v[124:127], v[148:151], 0\n"
"ds_read_b128 v[116:119], v181 offset:13504\n"
"v_bfi_b32 v89, v1, v89, v26\n"
"v_mfma_f32_16x16x32_bf16 v[132:135], v[132:135], v[148:151], 0\n"
"s_add_u32 m0, s79, 130176\n"
"ds_read_b64 v[84:85], v179 offset:13568\n"
"v_rcp_f32 v171, v90\n"
"v_bfi_b32 v94, v2, v94, v26\n"
"v_bfi_b32 v95, v3, v95, v26\n"
"v_bfi_b32 v96, v0, v96, v26\n"
"v_bfi_b32 v97, v1, v97, v26\n"
"v_bfi_b32 v102, v2, v102, v26\n"
"v_bfi_b32 v103, v3, v103, v26\n"
"v_bfi_b32 v104, v8, v104, v26\n"
"v_bfi_b32 v105, v9, v105, v26\n"
"v_bfi_b32 v110, v10, v110, v26\n"
"v_bfi_b32 v111, v11, v111, v26\n"
"v_mul_f32 v244, v171, v244\n"
"v_mul_f32 v245, v171, v245\n"
"v_mul_f32 v246, v171, v246\n"
"v_mul_f32 v247, v171, v247\n"
"v_cvt_pk_bf16_f32 v244, v244, v245\n"
"v_cvt_pk_bf16_f32 v245, v246, v247\n"
"v_mul_f32 v248, v171, v248\n"
"v_mul_f32 v249, v171, v249\n"
"v_mul_f32 v250, v171, v250\n"
"v_mul_f32 v251, v171, v251\n"
"v_cvt_pk_bf16_f32 v246, v248, v249\n"
"v_cvt_pk_bf16_f32 v247, v250, v251\n"
"v_mul_f32 v252, v171, v252\n"
"v_mul_f32 v253, v171, v253\n"
"v_mul_f32 v254, v171, v254\n"
"v_mul_f32 v255, v171, v255\n"
"v_cvt_pk_bf16_f32 v252, v252, v253\n"
"v_subrev_u32 v182, s48, v182\n"
"v_subrev_u32 v183, s48, v183\n"
"v_cvt_pk_bf16_f32 v253, v254, v255\n"
"v_subrev_u32 v184, s48, v184\n"
"v_subrev_u32 v185, s48, v185\n"
"v_cvt_pk_bf16_f32 v166, v208, v209\n"
"v_cvt_pk_bf16_f32 v167, v210, v211\n"
"v_mfma_f32_16x16x32_bf16 v[208:211], a[144:147], v[32:35], 0\n"
"s_waitcnt lgkmcnt(0)\n"
"buffer_load_dwordx4 v192, s[16:19], s43 offen offset:2048 lds\n"
"ds_read_b128 v[32:35], v188 offset:6144\n"
"v_mfma_f32_16x16x32_bf16 v[128:131], v[128:131], v[148:151], 0\n"
"s_add_u32 s43, s79, 18432\n"
"ds_read_b64 v[90:91], v178 offset:27072\n"
"ds_read_b64 v[92:93], v179 offset:27072\n"
"v_mfma_f32_16x16x32_bf16 v[140:143], v[140:143], v[148:151], 0\n"
"ds_read_b64 v[98:99], v180 offset:64\n"
"v_cvt_pk_bf16_f32 v146, v216, v217\n"
"v_mfma_f32_16x16x32_bf16 v[136:139], v[136:139], v[148:151], 0\n"
"buffer_store_dwordx4 v[244:247], v192, s[28:31], s37 offen sc1\n"
"v_mfma_f32_16x16x32_bf16 v[112:115], v[112:115], v[148:151], 0\n"
"ds_read_b64 v[100:101], v181 offset:64\n"
"ds_read_b64 v[106:107], v180 offset:13568\n"
"v_mfma_f32_16x16x32_bf16 v[120:123], v[120:123], v[148:151], 0\n"
"ds_read_b64 v[108:109], v181 offset:13568\n"
"v_cvt_pk_bf16_f32 v147, v218, v219\n"
"v_mfma_f32_16x16x32_bf16 v[116:119], v[116:119], v[148:151], 0\n"
"ds_read_b64_tr_b16 v[248:249], v182 offset:13632\n"
"buffer_store_dwordx2 v[252:253], v193, s[28:31], s37 offen offset:1024 sc1\n"
"v_mfma_f32_16x16x32_bf16 v[124:127], v[80:83], v[160:163], v[124:127]\n"
"s_waitcnt lgkmcnt(7)\n"
"ds_read_b64_tr_b16 v[250:251], v183 offset:13632\n"
"ds_read_b64_tr_b16 v[244:245], v182 offset:13640\n"
"v_mfma_f32_16x16x32_bf16 v[132:135], v[84:87], v[162:165], v[132:135]\n"
"s_waitcnt lgkmcnt(8)\n"
"ds_read_b64_tr_b16 v[246:247], v183 offset:13640\n"
"v_sub_u32 v197, 0, v199\n"
"v_mfma_f32_16x16x32_bf16 v[128:131], v[88:91], v[160:163], v[128:131]\n"
"s_waitcnt lgkmcnt(8)\n"
"ds_read_b64_tr_b16 v[252:253], v182 offset:13576\n"
"v_cvt_pk_bf16_f32 v158, v212, v213\n"
"v_mfma_f32_16x16x32_bf16 v[140:143], v[92:95], v[162:165], v[140:143]\n"
"s_waitcnt lgkmcnt(8)\n"
"ds_read_b64_tr_b16 v[254:255], v183 offset:13576\n"
"v_max_f32 v124, v124, v132\n"
"v_mfma_f32_16x16x32_bf16 v[136:139], v[96:99], v[160:163], v[136:139]\n"
"s_waitcnt lgkmcnt(8)\n"
"ds_read_b64_tr_b16 v[98:99], v184 offset:136\n"
"v_max_f32 v125, v125, v133\n"
"v_mfma_f32_16x16x32_bf16 v[112:115], v[100:103], v[162:165], v[112:115]\n"
"s_waitcnt lgkmcnt(8)\n"
"ds_read_b64_tr_b16 v[132:133], v182 offset:27136\n"
"v_max_f32 v126, v126, v134\n"
"v_mfma_f32_16x16x32_bf16 v[120:123], v[104:107], v[160:163], v[120:123]\n"
"s_waitcnt lgkmcnt(8)\n"
"ds_read_b64_tr_b16 v[106:107], v184 offset:72\n"
"v_max_f32 v127, v127, v135\n"
"v_mfma_f32_16x16x32_bf16 v[116:119], v[108:111], v[162:165], v[116:119]\n"
"s_add_u32 s37, s37, 6144\n"
"ds_read_b64_tr_b16 v[134:135], v183 offset:27136\n"
"v_max_f32 v170, v124, v125\n"
"v_mfma_f32_16x16x32_bf16 v[208:211], a[156:159], v[36:39], v[208:211]\n"
"ds_read_b128 v[36:39], v189 offset:6144\n"
"v_max_f32 v171, v126, v127\n"
"v_mfma_f32_16x16x32_bf16 v[208:211], a[168:171], v[40:43], v[208:211]\n"
"ds_read_b128 v[40:43], v188 offset:7168\n"
"v_max_f32 v128, v128, v140\n"
"v_mfma_f32_16x16x32_bf16 v[208:211], a[180:183], v[44:47], v[208:211]\n"
"ds_read_b128 v[44:47], v189 offset:7168\n"
"v_max_f32 v129, v129, v141\n"
"v_mfma_f32_16x16x32_bf16 v[208:211], a[192:195], v[48:51], v[208:211]\n"
"ds_read_b128 v[48:51], v188 offset:8192\n"
"v_max_f32 v130, v130, v142\n"
"v_mfma_f32_16x16x32_bf16 v[208:211], a[204:207], v[52:55], v[208:211]\n"
"ds_read_b128 v[52:55], v189 offset:8192\n"
"v_max_f32 v131, v131, v143\n"
"v_mfma_f32_16x16x32_bf16 v[228:231], a[0:3], v[32:35], 0\n"
"ds_read_b64_tr_b16 v[140:141], v182 offset:27144\n"
"v_max3_f32 v170, v170, v128, v129\n"
"v_mfma_f32_16x16x32_bf16 v[220:223], a[8:11], v[32:35], 0\n"
"ds_read_b64_tr_b16 v[142:143], v183 offset:27144\n"
"v_max3_f32 v171, v171, v130, v131\n"
"v_mfma_f32_16x16x32_bf16 v[224:227], a[72:75], v[32:35], 0\n"
"ds_read_b64_tr_b16 v[108:109], v185 offset:72\n"
"v_max_f32 v136, v136, v112\n"
"v_mfma_f32_16x16x32_bf16 v[204:207], a[4:7], v[32:35], 0\n"
"ds_read_b64_tr_b16 v[100:101], v185 offset:136\n"
"v_max_f32 v137, v137, v113\n"
"v_mfma_f32_16x16x32_bf16 v[216:219], a[80:83], v[32:35], 0\n"
"s_waitcnt lgkmcnt(8)\n"
"ds_read_b64_tr_b16 v[112:113], v182 offset:27080\n"
"v_max_f32 v138, v138, v114\n"
"v_mfma_f32_16x16x32_bf16 v[228:231], a[12:15], v[36:39], v[228:231]\n"
"s_waitcnt lgkmcnt(8)\n"
"v_max_f32 v139, v139, v115\n"
"v_max3_f32 v170, v170, v136, v137\n"
"v_mfma_f32_16x16x32_bf16 v[220:223], a[20:23], v[36:39], v[220:223]\n"
"s_waitcnt lgkmcnt(7)\n"
"ds_read_b64_tr_b16 v[114:115], v183 offset:27080\n"
"v_max3_f32 v171, v171, v138, v139\n"
"v_mfma_f32_16x16x32_bf16 v[224:227], a[84:87], v[36:39], v[224:227]\n"
"s_add_u32 m0, s34, 6144\n"
"v_max_f32 v120, v120, v116\n"
"v_max_f32 v121, v121, v117\n"
"v_mfma_f32_16x16x32_bf16 v[204:207], a[16:19], v[36:39], v[204:207]\n"
"buffer_load_dwordx4 v190, s[8:11], s32 offen lds\n"
"ds_read_b64_tr_b16 v[116:117], v184 offset:128\n"
"v_mfma_f32_16x16x32_bf16 v[216:219], a[92:95], v[36:39], v[216:219]\n"
"v_max_f32 v122, v122, v118\n"
"v_max_f32 v123, v123, v119\n"
"v_mfma_f32_16x16x32_bf16 v[228:231], a[24:27], v[40:43], v[228:231]\n"
"s_waitcnt lgkmcnt(7)\n"
"ds_read_b64_tr_b16 v[118:119], v185 offset:128\n"
"v_max3_f32 v170, v170, v120, v121\n"
"v_mfma_f32_16x16x32_bf16 v[220:223], a[32:35], v[40:43], v[220:223]\n"
"s_add_u32 m0, s34, 9216\n"
"v_max3_f32 v171, v171, v122, v123\n"
"v_max_f32 v170, v170, v171\n"
"v_mfma_f32_16x16x32_bf16 v[224:227], a[96:99], v[40:43], v[224:227]\n"
"buffer_load_dwordx4 v191, s[8:11], s32 offen lds\n"
"v_mov_b32 v171, v170\n"
"v_mfma_f32_16x16x32_bf16 v[204:207], a[28:31], v[40:43], v[204:207]\n"
"s_add_u32 s32, s32, s33\n"
"v_permlane16_swap_b32 v170, v171\n"
"v_mfma_f32_16x16x32_bf16 v[216:219], a[104:107], v[40:43], v[216:219]\n"
"s_add_u32 m0, s34, 7168\n"
"v_max_f32 v170, v170, v171\n"
"v_mov_b32 v171, v170\n"
"v_mfma_f32_16x16x32_bf16 v[228:231], a[36:39], v[44:47], v[228:231]\n"
"buffer_load_dwordx4 v190, s[8:11], s0 offen lds\n"
"v_permlane32_swap_b32 v170, v171\n"
"v_mfma_f32_16x16x32_bf16 v[220:223], a[44:47], v[44:47], v[220:223]\n"
"v_max_f32 v170, v170, v171\n"
"v_mul_f32 v170, 0xbe553b95, v170\n"
"v_mfma_f32_16x16x32_bf16 v[224:227], a[108:111], v[44:47], v[224:227]\n"
"v_fmamk_f32 v124, v124, 0x3e553b95, v170\n"
"v_fmamk_f32 v125, v125, 0x3e553b95, v170\n"
"v_mfma_f32_16x16x32_bf16 v[204:207], a[40:43], v[44:47], v[204:207]\n"
"s_add_u32 m0, s34, 10240\n"
"v_fmamk_f32 v126, v126, 0x3e553b95, v170\n"
"v_fmamk_f32 v127, v127, 0x3e553b95, v170\n"
"v_mfma_f32_16x16x32_bf16 v[216:219], a[116:119], v[44:47], v[216:219]\n"
"buffer_load_dwordx4 v191, s[8:11], s0 offen lds\n"
"v_fmamk_f32 v128, v128, 0x3e553b95, v170\n"
"v_mfma_f32_16x16x32_bf16 v[228:231], a[48:51], v[48:51], v[228:231]\n"
"s_add_u32 s0, s0, s33\n"
"v_exp_f32 v124, v124\n"
"v_mfma_f32_16x16x32_bf16 v[220:223], a[56:59], v[48:51], v[220:223]\n"
"v_exp_f32 v125, v125\n"
"v_mfma_f32_16x16x32_bf16 v[224:227], a[120:123], v[48:51], v[224:227]\n"
"s_add_u32 m0, s34, 8192\n"
"v_exp_f32 v126, v126\n"
"v_mfma_f32_16x16x32_bf16 v[204:207], a[52:55], v[48:51], v[204:207]\n"
"buffer_load_dwordx4 v190, s[8:11], s1 offen lds\n"
"v_cvt_pk_bf16_f32 v82, v124, v125\n"
"v_mfma_f32_16x16x32_bf16 v[216:219], a[128:131], v[48:51], v[216:219]\n"
"v_exp_f32 v127, v127\n"
"v_mfma_f32_16x16x32_bf16 v[228:231], a[60:63], v[52:55], v[228:231]\n"
"v_cvt_pk_bf16_f32 v83, v126, v127\n"
"v_fmamk_f32 v129, v129, 0x3e553b95, v170\n"
"v_mfma_f32_16x16x32_bf16 v[220:223], a[68:71], v[52:55], v[220:223]\n"
"v_fmamk_f32 v130, v130, 0x3e553b95, v170\n"
"v_fmamk_f32 v131, v131, 0x3e553b95, v170\n"
"v_mfma_f32_16x16x32_bf16 v[224:227], a[132:135], v[52:55], v[224:227]\n"
"v_and_b32 v124, v82, v176\n"
"v_and_b32 v125, v83, v177\n"
"v_mfma_f32_16x16x32_bf16 v[204:207], a[64:67], v[52:55], v[204:207]\n"
"v_exp_f32 v128, v128\n"
"v_mfma_f32_16x16x32_bf16 v[216:219], a[140:143], v[52:55], v[216:219]\n"
"s_add_u32 m0, s34, 11264\n"
"v_exp_f32 v129, v129\n"
"buffer_load_dwordx4 v191, s[8:11], s1 offen lds\n"
"s_add_u32 s1, s1, s33\n"
"v_exp_f32 v130, v130\n"
"v_exp_f32 v131, v131\n"
"v_xor_b32 v126, v82, v124\n"
"v_xor_b32 v127, v83, v125\n"
"v_cvt_pk_bf16_f32 v84, v128, v129\n"
"v_cvt_pk_bf16_f32 v85, v130, v131\n"
"v_mfma_f32_16x16x32_bf16 v[248:251], v[248:251], v[124:127], 0\n"
"v_and_b32 v128, v84, v176\n"
"v_and_b32 v129, v85, v177\n"
"v_mfma_f32_16x16x32_bf16 v[244:247], v[244:247], v[124:127], 0\n"
"v_xor_b32 v130, v84, v128\n"
"v_xor_b32 v131, v85, v129\n"
"v_mfma_f32_16x16x32_bf16 v[252:255], v[252:255], v[124:127], 0\n"
"s_waitcnt lgkmcnt(2)\n"
"ds_read_b64_tr_b16 v[124:125], v184 offset:13632\n"
"ds_read_b64_tr_b16 v[126:127], v185 offset:13632\n"
"v_mfma_f32_16x16x32_bf16 v[248:251], v[132:135], v[128:131], v[248:251]\n"
"ds_read_b64_tr_b16 v[132:133], v184 offset:13576\n"
"v_fmamk_f32 v136, v136, 0x3e553b95, v170\n"
"v_mfma_f32_16x16x32_bf16 v[244:247], v[140:143], v[128:131], v[244:247]\n"
"ds_read_b64_tr_b16 v[134:135], v185 offset:13576\n"
"v_fmamk_f32 v137, v137, 0x3e553b95, v170\n"
"v_mfma_f32_16x16x32_bf16 v[252:255], v[112:115], v[128:131], v[252:255]\n"
"ds_read_b64_tr_b16 v[128:129], v184 offset:13640\n"
"v_fmamk_f32 v138, v138, 0x3e553b95, v170\n"
"v_mfma_f32_16x16x32_bf16 v[82:85], v[172:175], v[82:85], 0\n"
"ds_read_b64_tr_b16 v[130:131], v185 offset:13640\n"
"v_fmamk_f32 v139, v139, 0x3e553b95, v170\n"
"v_exp_f32 v136, v136\n"
"v_exp_f32 v137, v137\n"
"v_exp_f32 v138, v138\n"
"v_exp_f32 v139, v139\n"
"v_fmamk_f32 v120, v120, 0x3e553b95, v170\n"
"v_fmamk_f32 v121, v121, 0x3e553b95, v170\n"
"v_fmamk_f32 v122, v122, 0x3e553b95, v170\n"
"v_fmamk_f32 v123, v123, 0x3e553b95, v170\n"
"v_cvt_pk_bf16_f32 v90, v136, v137\n"
"v_cvt_pk_bf16_f32 v91, v138, v139\n"
"v_exp_f32 v120, v120\n"
"v_exp_f32 v121, v121\n"
"v_exp_f32 v122, v122\n"
"v_exp_f32 v123, v123\n"
"v_and_b32 v136, v90, v176\n"
"v_and_b32 v137, v91, v177\n"
"v_xor_b32 v138, v90, v136\n"
"v_xor_b32 v139, v91, v137\n"
"v_cvt_pk_bf16_f32 v92, v120, v121\n"
"v_cvt_pk_bf16_f32 v93, v122, v123\n"
"v_mfma_f32_16x16x32_bf16 v[252:255], v[106:109], v[136:139], v[252:255]\n"
"s_waitcnt lgkmcnt(6)\n"
"v_and_b32 v120, v92, v176\n"
"v_and_b32 v121, v93, v177\n"
"v_mfma_f32_16x16x32_bf16 v[248:251], v[116:119], v[136:139], v[248:251]\n"
"v_xor_b32 v122, v92, v120\n"
"v_xor_b32 v123, v93, v121\n"
"v_mfma_f32_16x16x32_bf16 v[90:93], v[172:175], v[90:93], v[82:85]\n"
"s_waitcnt lgkmcnt(2)\n"
"v_cvt_pk_bf16_f32 v144, v208, v209\n"
"v_cvt_pk_bf16_f32 v145, v210, v211\n"
"v_mfma_f32_16x16x32_bf16 v[244:247], v[98:101], v[136:139], v[244:247]\n"
"s_waitcnt lgkmcnt(0)\n"
"v_cvt_pk_bf16_f32 v228, v228, v229\n"
"v_cvt_pk_bf16_f32 v220, v220, v221\n"
"v_mfma_f32_16x16x32_bf16 v[248:251], v[124:127], v[120:123], v[248:251]\n"
"v_rcp_f32 v171, v90\n"
"v_mfma_f32_16x16x32_bf16 v[252:255], v[132:135], v[120:123], v[252:255]\n"
"v_cvt_pk_bf16_f32 v224, v224, v225\n"
"v_cvt_pk_bf16_f32 v229, v230, v231\n"
"v_mfma_f32_16x16x32_bf16 v[244:247], v[128:131], v[120:123], v[244:247]\n"
"v_cvt_pk_bf16_f32 v221, v222, v223\n"
"v_cvt_pk_bf16_f32 v225, v226, v227\n"
"v_mfma_f32_16x16x32_bf16 v[208:211], a[76:79], v[32:35], 0\n"
"v_mul_f32 v248, v171, v248\n"
"v_mul_f32 v249, v171, v249\n"
"v_mfma_f32_16x16x32_bf16 v[208:211], a[88:91], v[36:39], v[208:211]\n"
"v_mul_f32 v250, v171, v250\n"
"v_mul_f32 v251, v171, v251\n"
"v_mfma_f32_16x16x32_bf16 v[208:211], a[100:103], v[40:43], v[208:211]\n"
"v_cvt_pk_bf16_f32 v248, v248, v249\n"
"v_mul_f32 v252, v171, v252\n"
"v_mfma_f32_16x16x32_bf16 v[208:211], a[112:115], v[44:47], v[208:211]\n"
"v_mul_f32 v253, v171, v253\n"
"v_cvt_pk_bf16_f32 v249, v250, v251\n"
"v_mfma_f32_16x16x32_bf16 v[208:211], a[124:127], v[48:51], v[208:211]\n"
"v_mul_f32 v244, v171, v244\n"
"v_mul_f32 v245, v171, v245\n"
"v_mfma_f32_16x16x32_bf16 v[208:211], a[136:139], v[52:55], v[208:211]\n"
"v_mul_f32 v246, v171, v246\n"
"v_mul_f32 v247, v171, v247\n"
"v_mul_f32 v254, v171, v254\n"
"v_mul_f32 v255, v171, v255\n"
"v_cvt_pk_bf16_f32 v252, v252, v253\n"
"v_cvt_pk_bf16_f32 v250, v244, v245\n"
"v_cvt_pk_bf16_f32 v251, v246, v247\n"
"buffer_store_dwordx4 v[248:251], v192, s[28:31], s37 offen sc1\n"
"v_cvt_pk_bf16_f32 v253, v254, v255\n"
"v_cvt_pk_bf16_f32 v230, v204, v205\n"
"v_cvt_pk_bf16_f32 v231, v206, v207\n"
"v_mfma_f32_16x16x32_bf16 v[204:207], a[148:151], v[32:35], 0\n"
"ds_write_b128 v187, v[228:231]\n"
"v_mfma_f32_16x16x32_bf16 v[228:231], a[152:155], v[32:35], 0\n"
"buffer_store_dwordx2 v[252:253], v193, s[28:31], s37 offen offset:1024 sc1\n"
"v_mfma_f32_16x16x32_bf16 v[204:207], a[160:163], v[36:39], v[204:207]\n"
"s_add_u32 s37, s37, 6144\n"
"v_cvt_pk_bf16_f32 v226, v208, v209\n"
"v_cvt_pk_bf16_f32 v227, v210, v211\n"
"v_mfma_f32_16x16x32_bf16 v[228:231], a[164:167], v[36:39], v[228:231]\n"
"ds_write_b128 v187, v[224:227] offset:128\n"
"v_mfma_f32_16x16x32_bf16 v[204:207], a[172:175], v[40:43], v[204:207]\n"
"v_cvt_pk_bf16_f32 v222, v216, v217\n"
"v_cvt_pk_bf16_f32 v223, v218, v219\n"
"v_mfma_f32_16x16x32_bf16 v[228:231], a[176:179], v[40:43], v[228:231]\n"
"v_cvt_pk_bf16_f32 v159, v214, v215\n"
"v_mfma_f32_16x16x32_bf16 v[212:215], a[144:147], v[32:35], 0\n"
"s_waitcnt vmcnt(18)\n"
"ds_write_b128 v187, v[220:223] offset:64\n"
"v_mfma_f32_16x16x32_bf16 v[212:215], a[156:159], v[36:39], v[212:215]\n"
"v_mfma_f32_16x16x32_bf16 v[212:215], a[168:171], v[40:43], v[212:215]\n"
"s_waitcnt vmcnt(16)\n"
"ds_read_b128 v[32:35], v188\n"
"v_mfma_f32_16x16x32_bf16 v[212:215], a[180:183], v[44:47], v[212:215]\n"
"ds_read_b128 v[36:39], v189\n"
"ds_read_b128 v[40:43], v188 offset:1024\n"
"v_mfma_f32_16x16x32_bf16 v[204:207], a[184:187], v[44:47], v[204:207]\n"
"ds_read_b64 v[80:81], v20 offset:272\n"
"ds_read_b64 v[86:87], v20 offset:336\n"
"v_mfma_f32_16x16x32_bf16 v[228:231], a[188:191], v[44:47], v[228:231]\n"
".Lic1%=:\n"
"s_cbranch_execz .Lib0%=\n"
"ds_read_b128 v[44:47], v189 offset:1024\n"
"v_mfma_f32_16x16x32_bf16 v[212:215], a[192:195], v[48:51], v[212:215]\n"
"ds_read_b64 v[104:105], v20 offset:1088\n"
"ds_read_b64 v[110:111], v20 offset:1152\n"
"v_mfma_f32_16x16x32_bf16 v[204:207], a[196:199], v[48:51], v[204:207]\n"
"ds_read_b128 v[140:143], v178 offset:27008\n"
"v_mfma_f32_16x16x32_bf16 v[228:231], a[200:203], v[48:51], v[228:231]\n"
"s_waitcnt vmcnt(10) lgkmcnt(9)\n"
"ds_read_b128 v[48:51], v188 offset:2048\n"
"s_barrier\n"
"v_mfma_f32_16x16x32_bf16 v[212:215], a[204:207], v[52:55], v[212:215]\n"
"s_waitcnt lgkmcnt(9)\n"
"buffer_load_dword v201, off, s[28:31], s41 sc1\n"
"ds_read_b128 v[112:115], v179 offset:27008\n"
"v_mfma_f32_16x16x32_bf16 v[204:207], a[208:211], v[52:55], v[204:207]\n"
"s_waitcnt lgkmcnt(9)\n"
"ds_read_b64 v[82:83], v178 offset:27072\n"
"ds_read_b64 v[84:85], v179 offset:27072\n"
"v_mfma_f32_16x16x32_bf16 v[228:231], a[212:215], v[52:55], v[228:231]\n"
"s_add_u32 s41, s40, 131072\n"
"v_cvt_pk_bf16_f32 v148, v212, v213\n"
"v_cvt_pk_bf16_f32 v149, v214, v215\n"
"v_mfma_f32_16x16x32_bf16 v[208:211], a[0:3], v[32:35], 0\n"
"s_waitcnt lgkmcnt(8)\n"
"ds_read_b128 v[52:55], v189 offset:2048\n"
"v_cvt_pk_bf16_f32 v150, v204, v205\n"
"v_mfma_f32_16x16x32_bf16 v[216:219], a[4:7], v[32:35], 0\n"
"s_waitcnt lgkmcnt(8)\n"
"v_cvt_pk_bf16_f32 v151, v206, v207\n"
"v_cvt_pk_bf16_f32 v162, v228, v229\n"
"v_mfma_f32_16x16x32_bf16 v[220:223], a[8:11], v[32:35], 0\n"
"s_waitcnt lgkmcnt(6)\n"
"v_cvt_pk_bf16_f32 v163, v230, v231\n"
"v_bfi_b32 v80, v12, v80, v26\n"
"v_mfma_f32_16x16x32_bf16 v[208:211], a[12:15], v[36:39], v[208:211]\n"
"s_waitcnt lgkmcnt(5)\n"
"ds_read_b128 v[136:139], v180\n"
"ds_read_b128 v[116:119], v181\n"
"v_mfma_f32_16x16x32_bf16 v[216:219], a[16:19], v[36:39], v[216:219]\n"
"s_add_u32 m0, s79, 142464\n"
"ds_read_b64 v[90:91], v180 offset:64\n"
"buffer_load_dwordx4 v192, s[16:19], s43 offen lds\n"
"v_mfma_f32_16x16x32_bf16 v[220:223], a[20:23], v[36:39], v[220:223]\n"
"s_waitcnt lgkmcnt(6)\n"
"ds_read_b64 v[92:93], v181 offset:64\n"
"v_bfi_b32 v81, v13, v81, v26\n"
"v_mfma_f32_16x16x32_bf16 v[208:211], a[24:27], v[40:43], v[208:211]\n"
"s_add_u32 m0, s79, 142464\n"
"ds_read_b128 v[120:123], v180 offset:13504\n"
"v_bfi_b32 v86, v14, v86, v26\n"
"v_mfma_f32_16x16x32_bf16 v[216:219], a[28:31], v[40:43], v[216:219]\n"
"ds_read_b128 v[124:127], v181 offset:13504\n"
"buffer_load_dwordx4 v192, s[16:19], s43 offen offset:1024 lds\n"
"v_mfma_f32_16x16x32_bf16 v[220:223], a[32:35], v[40:43], v[220:223]\n"
"ds_read_b64 v[98:99], v180 offset:13568\n"
"v_bfi_b32 v87, v15, v87, v26\n"
"v_mfma_f32_16x16x32_bf16 v[208:211], a[36:39], v[44:47], v[208:211]\n"
"ds_read_b64 v[100:101], v181 offset:13568\n"
"v_bfi_b32 v104, v16, v104, v26\n"
"v_mfma_f32_16x16x32_bf16 v[216:219], a[40:43], v[44:47], v[216:219]\n"
"s_waitcnt lgkmcnt(8)\n"
"ds_read_b128 v[128:131], v180 offset:27008\n"
"v_bfi_b32 v105, v17, v105, v26\n"
"v_mfma_f32_16x16x32_bf16 v[220:223], a[44:47], v[44:47], v[220:223]\n"
"s_waitcnt lgkmcnt(8)\n"
"ds_read_b128 v[132:135], v181 offset:27008\n"
"v_bfi_b32 v110, v18, v110, v26\n"
"v_mfma_f32_16x16x32_bf16 v[208:211], a[48:51], v[48:51], v[208:211]\n"
"s_waitcnt lgkmcnt(8)\n"
"ds_read_b64 v[106:107], v180 offset:27072\n"
"v_bfi_b32 v111, v19, v111, v26\n"
"v_mfma_f32_16x16x32_bf16 v[216:219], a[52:55], v[48:51], v[216:219]\n"
"s_add_u32 m0, s79, 142464\n"
"ds_read_b64 v[108:109], v181 offset:27072\n"
"buffer_load_dwordx4 v192, s[16:19], s43 offen offset:2048 lds\n"
"v_mfma_f32_16x16x32_bf16 v[220:223], a[56:59], v[48:51], v[220:223]\n"
"s_add_u32 s43, s79, 36864\n"
"ds_read_b64_tr_b16 a[216:217], v236\n"
"v_mov_b64 v[68:69], -1\n"
"v_mfma_f32_16x16x32_bf16 v[208:211], a[60:63], v[52:55], v[208:211]\n"
"s_waitcnt lgkmcnt(8)\n"
"v_mov_b64 v[70:71], -1\n"
"s_mov_b64 exec, s[44:45]\n"
"v_add_u32 v179, 0xc0, v179\n"
"s_mov_b64 exec, -1\n"
"v_mfma_f32_16x16x32_bf16 v[216:219], a[64:67], v[52:55], v[216:219]\n"
"s_waitcnt lgkmcnt(7)\n"
"ds_read_b64_tr_b16 v[244:245], v182 offset:27136\n"
"ds_read_b64_tr_b16 v[246:247], v183 offset:27136\n"
"v_mfma_f32_16x16x32_bf16 v[220:223], a[68:71], v[52:55], v[220:223]\n"
"s_add_u32 m0, s34, 0\n"
"ds_read_b64_tr_b16 a[218:219], v236 offset:6144\n"
"buffer_load_dwordx4 v190, s[8:11], s32 offen lds\n"
"v_mfma_f32_16x16x32_bf16 v[140:143], v[140:143], v[152:155], 0\n"
"s_waitcnt lgkmcnt(7)\n"
"v_cvt_pk_bf16_f32 v208, v208, v209\n"
"v_cvt_pk_bf16_f32 v209, v210, v211\n"
"v_mfma_f32_16x16x32_bf16 v[112:115], v[112:115], v[152:155], 0\n"
"s_waitcnt lgkmcnt(6)\n"
"ds_read_b64_tr_b16 v[248:249], v182 offset:27144\n"
"v_cvt_pk_bf16_f32 v210, v216, v217\n"
"v_mfma_f32_16x16x32_bf16 v[140:143], v[80:83], v[164:167], v[140:143]\n"
"ds_read_b64_tr_b16 v[250:251], v183 offset:27144\n"
"v_cvt_pk_bf16_f32 v211, v218, v219\n"
"v_mfma_f32_16x16x32_bf16 v[224:227], a[72:75], v[32:35], 0\n"
"ds_write_b128 v186, v[208:211] offset:6752\n"
"v_mfma_f32_16x16x32_bf16 v[112:115], v[84:87], v[166:169], v[112:115]\n"
"v_cvt_pk_bf16_f32 v220, v220, v221\n"
"v_cvt_pk_bf16_f32 v221, v222, v223\n"
"v_mfma_f32_16x16x32_bf16 v[212:215], a[76:79], v[32:35], 0\n"
"ds_read_b64_tr_b16 v[252:253], v182 offset:27080\n"
"v_mfma_f32_16x16x32_bf16 v[204:207], a[80:83], v[32:35], 0\n"
"ds_read_b64_tr_b16 v[254:255], v183 offset:27080\n"
"ds_read_b64_tr_b16 a[220:221], v236 offset:8\n"
"v_mfma_f32_16x16x32_bf16 v[136:139], v[136:139], v[152:155], 0\n"
"v_max_f32 v140, v140, v112\n"
"v_max_f32 v141, v141, v113\n"
"v_mfma_f32_16x16x32_bf16 v[116:119], v[116:119], v[152:155], 0\n"
"v_max_f32 v142, v142, v114\n"
"v_max_f32 v143, v143, v115\n"
"v_mfma_f32_16x16x32_bf16 v[224:227], a[84:87], v[36:39], v[224:227]\n"
"ds_read_b64_tr_b16 a[222:223], v236 offset:6152\n"
"v_max_f32 v170, v140, v141\n"
"v_mfma_f32_16x16x32_bf16 v[136:139], v[88:91], v[164:167], v[136:139]\n"
"v_max_f32 v171, v142, v143\n"
"s_mov_b64 exec, s[46:47]\n"
"v_add_u32 v183, 0xc0, v183\n"
"s_mov_b64 exec, -1\n"
"v_mfma_f32_16x16x32_bf16 v[212:215], a[88:91], v[36:39], v[212:215]\n"
"ds_read_b64_tr_b16 v[112:113], v184 offset:128\n"
"ds_read_b64_tr_b16 v[114:115], v185 offset:128\n"
"v_mfma_f32_16x16x32_bf16 v[116:119], v[92:95], v[166:169], v[116:119]\n"
"ds_read_b64_tr_b16 a[224:225], v235\n"
"v_mfma_f32_16x16x32_bf16 v[204:207], a[92:95], v[36:39], v[204:207]\n"
"v_mfma_f32_16x16x32_bf16 v[120:123], v[120:123], v[152:155], 0\n"
"ds_read_b64_tr_b16 a[226:227], v235 offset:6144\n"
"v_mfma_f32_16x16x32_bf16 v[224:227], a[96:99], v[40:43], v[224:227]\n"
"s_add_u32 m0, s34, 3072\n"
"v_mfma_f32_16x16x32_bf16 v[124:127], v[124:127], v[152:155], 0\n"
"buffer_load_dwordx4 v191, s[8:11], s32 offen lds\n"
"v_max_f32 v136, v136, v116\n"
"v_mfma_f32_16x16x32_bf16 v[212:215], a[100:103], v[40:43], v[212:215]\n"
"s_add_u32 s32, s32, s33\n"
"v_max_f32 v137, v137, v117\n"
"v_max_f32 v138, v138, v118\n"
"v_mfma_f32_16x16x32_bf16 v[120:123], v[96:99], v[164:167], v[120:123]\n"
"ds_read_b64_tr_b16 v[116:117], v184 offset:136\n"
"ds_read_b64_tr_b16 v[98:99], v184 offset:13640\n"
"v_mfma_f32_16x16x32_bf16 v[124:127], v[100:103], v[166:169], v[124:127]\n"
"ds_read_b64_tr_b16 v[100:101], v185 offset:13640\n"
"v_max_f32 v139, v139, v119\n"
"v_mfma_f32_16x16x32_bf16 v[204:207], a[104:107], v[40:43], v[204:207]\n"
"ds_read_b64_tr_b16 v[118:119], v185 offset:136\n"
"v_max3_f32 v170, v170, v136, v137\n"
"v_mfma_f32_16x16x32_bf16 v[224:227], a[108:111], v[44:47], v[224:227]\n"
"v_max3_f32 v171, v171, v138, v139\n"
"v_mfma_f32_16x16x32_bf16 v[128:131], v[128:131], v[152:155], 0\n"
"s_waitcnt lgkmcnt(15)\n"
"v_max_f32 v120, v120, v124\n"
"v_max_f32 v121, v121, v125\n"
"v_mfma_f32_16x16x32_bf16 v[212:215], a[112:115], v[44:47], v[212:215]\n"
"ds_read_b64_tr_b16 v[124:125], v184 offset:72\n"
"v_max_f32 v122, v122, v126\n"
"v_mfma_f32_16x16x32_bf16 v[132:135], v[132:135], v[152:155], 0\n"
"v_max_f32 v123, v123, v127\n"
"v_max3_f32 v170, v170, v120, v121\n"
"v_mfma_f32_16x16x32_bf16 v[128:131], v[104:107], v[164:167], v[128:131]\n"
"ds_read_b64_tr_b16 v[126:127], v185 offset:72\n"
"ds_read_b64_tr_b16 v[106:107], v184 offset:13576\n"
"v_mfma_f32_16x16x32_bf16 v[204:207], a[116:119], v[44:47], v[204:207]\n"
"v_max3_f32 v171, v171, v122, v123\n"
"v_mfma_f32_16x16x32_bf16 v[132:135], v[108:111], v[166:169], v[132:135]\n"
"ds_read_b64_tr_b16 v[108:109], v185 offset:13576\n"
"v_mfma_f32_16x16x32_bf16 v[224:227], a[120:123], v[48:51], v[224:227]\n"
"v_mfma_f32_16x16x32_bf16 v[212:215], a[124:127], v[48:51], v[212:215]\n"
"v_mfma_f32_16x16x32_bf16 v[204:207], a[128:131], v[48:51], v[204:207]\n"
"v_mfma_f32_16x16x32_bf16 v[224:227], a[132:135], v[52:55], v[224:227]\n"
"v_mfma_f32_16x16x32_bf16 v[212:215], a[136:139], v[52:55], v[212:215]\n"
"v_mfma_f32_16x16x32_bf16 v[204:207], a[140:143], v[52:55], v[204:207]\n"
"s_add_u32 m0, s34, 1024\n"
"v_max_f32 v128, v128, v132\n"
"v_mfma_f32_16x16x32_bf16 v[228:231], a[144:147], v[32:35], 0\n"
"buffer_load_dwordx4 v190, s[8:11], s0 offen lds\n"
"v_max_f32 v129, v129, v133\n"
"v_mfma_f32_16x16x32_bf16 v[216:219], a[148:151], v[32:35], 0\n"
"ds_read_b64_tr_b16 v[132:133], v184 offset:13632\n"
"v_cvt_pk_bf16_f32 v224, v224, v225\n"
"v_mfma_f32_16x16x32_bf16 v[208:211], a[152:155], v[32:35], 0\n"
"s_waitcnt vmcnt(13)\n"
"ds_read_b128 v[32:35], v188 offset:6144\n"
"v_cvt_pk_bf16_f32 v225, v226, v227\n"
"v_mfma_f32_16x16x32_bf16 v[228:231], a[156:159], v[36:39], v[228:231]\n"
"v_cvt_pk_bf16_f32 v226, v212, v213\n"
"v_cvt_pk_bf16_f32 v227, v214, v215\n"
"v_mfma_f32_16x16x32_bf16 v[216:219], a[160:163], v[36:39], v[216:219]\n"
"s_add_u32 m0, s34, 4096\n"
"ds_write_b128 v186, v[224:227] offset:6880\n"
"v_mfma_f32_16x16x32_bf16 v[208:211], a[164:167], v[36:39], v[208:211]\n"
"buffer_load_dwordx4 v191, s[8:11], s0 offen lds\n"
"v_cvt_pk_bf16_f32 v222, v204, v205\n"
"v_mfma_f32_16x16x32_bf16 v[228:231], a[168:171], v[40:43], v[228:231]\n"
"s_add_u32 s0, s0, s33\n"
"v_max_f32 v130, v130, v134\n"
"v_cvt_pk_bf16_f32 v223, v206, v207\n"
"v_mfma_f32_16x16x32_bf16 v[216:219], a[172:175], v[40:43], v[216:219]\n"
"s_add_u32 m0, s34, 2048\n"
"ds_read_b128 v[36:39], v189 offset:6144\n"
"ds_write_b128 v186, v[220:223] offset:6816\n"
"v_mfma_f32_16x16x32_bf16 v[208:211], a[176:179], v[40:43], v[208:211]\n"
"s_waitcnt vmcnt(12) lgkmcnt(3)\n"
"buffer_load_dwordx4 v190, s[8:11], s1 offen lds\n"
"v_max_f32 v131, v131, v135\n"
"v_mfma_f32_16x16x32_bf16 v[228:231], a[180:183], v[44:47], v[228:231]\n"
"v_max3_f32 v170, v170, v128, v129\n"
"v_max3_f32 v171, v171, v130, v131\n"
"v_mfma_f32_16x16x32_bf16 v[216:219], a[184:187], v[44:47], v[216:219]\n"
"s_add_u32 m0, s34, 5120\n"
"ds_read_b128 v[40:43], v188 offset:7168\n"
"ds_read_b64_tr_b16 v[134:135], v185 offset:13632\n"
"v_mfma_f32_16x16x32_bf16 v[208:211], a[188:191], v[44:47], v[208:211]\n"
"buffer_load_dwordx4 v191, s[8:11], s1 offen lds\n"
"ds_read_b128 v[44:47], v189 offset:7168\n"
"v_mfma_f32_16x16x32_bf16 v[228:231], a[192:195], v[48:51], v[228:231]\n"
"s_add_u32 s1, s1, s33\n"
"v_max_f32 v170, v170, v171\n"
"v_mov_b32 v171, v170\n"
"v_mfma_f32_16x16x32_bf16 v[216:219], a[196:199], v[48:51], v[216:219]\n"
"s_nop 0\n"
"v_permlane16_swap_b32 v170, v171\n"
"v_mfma_f32_16x16x32_bf16 v[208:211], a[200:203], v[48:51], v[208:211]\n"
"s_waitcnt vmcnt(12)\n"
"ds_read_b128 v[48:51], v188 offset:8192\n"
"v_max_f32 v170, v170, v171\n"
"v_mfma_f32_16x16x32_bf16 v[228:231], a[204:207], v[52:55], v[228:231]\n"
"v_mov_b32 v171, v170\n"
"v_mfma_f32_16x16x32_bf16 v[216:219], a[208:211], v[52:55], v[216:219]\n"
"s_nop 0\n"
"v_permlane32_swap_b32 v170, v171\n"
"v_mfma_f32_16x16x32_bf16 v[208:211], a[212:215], v[52:55], v[208:211]\n"
"ds_read_b128 v[52:55], v189 offset:8192\n"
"v_max_f32 v170, v170, v171\n"
"v_mfma_f32_16x16x32_bf16 v[212:215], a[0:3], v[32:35], 0\n"
"s_waitcnt lgkmcnt(6)\n"
"v_mul_f32 v170, 0xbe553b95, v170\n"
"v_fmamk_f32 v140, v140, 0x3e553b95, v170\n"
"v_mfma_f32_16x16x32_bf16 v[204:207], a[4:7], v[32:35], 0\n"
"v_fmamk_f32 v141, v141, 0x3e553b95, v170\n"
"v_fmamk_f32 v142, v142, 0x3e553b95, v170\n"
"v_mfma_f32_16x16x32_bf16 v[220:223], a[8:11], v[32:35], 0\n"
"v_fmamk_f32 v143, v143, 0x3e553b95, v170\n"
"v_fmamk_f32 v136, v136, 0x3e553b95, v170\n"
"v_mfma_f32_16x16x32_bf16 v[212:215], a[12:15], v[36:39], v[212:215]\n"
"s_waitcnt lgkmcnt(4)\n"
"v_exp_f32 v140, v140\n"
"v_mfma_f32_16x16x32_bf16 v[204:207], a[16:19], v[36:39], v[204:207]\n"
"v_exp_f32 v141, v141\n"
"v_mfma_f32_16x16x32_bf16 v[220:223], a[20:23], v[36:39], v[220:223]\n"
"v_exp_f32 v142, v142\n"
"v_mfma_f32_16x16x32_bf16 v[212:215], a[24:27], v[40:43], v[212:215]\n"
"s_waitcnt lgkmcnt(2)\n"
"v_exp_f32 v143, v143\n"
"v_mfma_f32_16x16x32_bf16 v[204:207], a[28:31], v[40:43], v[204:207]\n"
"v_cvt_pk_bf16_f32 v82, v140, v141\n"
"v_cvt_pk_bf16_f32 v83, v142, v143\n"
"v_mfma_f32_16x16x32_bf16 v[220:223], a[32:35], v[40:43], v[220:223]\n"
"v_fmamk_f32 v137, v137, 0x3e553b95, v170\n"
"v_fmamk_f32 v138, v138, 0x3e553b95, v170\n"
"v_mfma_f32_16x16x32_bf16 v[212:215], a[36:39], v[44:47], v[212:215]\n"
"s_waitcnt lgkmcnt(1)\n"
"v_fmamk_f32 v139, v139, 0x3e553b95, v170\n"
"v_and_b32 v140, v82, v176\n"
"v_mfma_f32_16x16x32_bf16 v[204:207], a[40:43], v[44:47], v[204:207]\n"
"v_and_b32 v141, v83, v177\n"
"v_xor_b32 v142, v82, v140\n"
"v_mfma_f32_16x16x32_bf16 v[220:223], a[44:47], v[44:47], v[220:223]\n"
"v_exp_f32 v136, v136\n"
"v_mfma_f32_16x16x32_bf16 v[212:215], a[48:51], v[48:51], v[212:215]\n"
"s_waitcnt lgkmcnt(0)\n"
"v_exp_f32 v137, v137\n"
"v_mfma_f32_16x16x32_bf16 v[204:207], a[52:55], v[48:51], v[204:207]\n"
"v_exp_f32 v138, v138\n"
"v_mfma_f32_16x16x32_bf16 v[220:223], a[56:59], v[48:51], v[220:223]\n"
"v_exp_f32 v139, v139\n"
"v_mfma_f32_16x16x32_bf16 v[212:215], a[60:63], v[52:55], v[212:215]\n"
"v_xor_b32 v143, v83, v141\n"
"v_cvt_pk_bf16_f32 v84, v136, v137\n"
"v_mfma_f32_16x16x32_bf16 v[204:207], a[64:67], v[52:55], v[204:207]\n"
"v_cvt_pk_bf16_f32 v85, v138, v139\n"
"v_and_b32 v136, v84, v176\n"
"v_mfma_f32_16x16x32_bf16 v[244:247], v[244:247], v[140:143], 0\n"
"v_and_b32 v137, v85, v177\n"
"v_xor_b32 v138, v84, v136\n"
"v_mfma_f32_16x16x32_bf16 v[248:251], v[248:251], v[140:143], 0\n"
"s_add_u32 m0, s34, 6144\n"
"v_xor_b32 v139, v85, v137\n"
"v_fmamk_f32 v120, v120, 0x3e553b95, v170\n"
"v_mfma_f32_16x16x32_bf16 v[252:255], v[252:255], v[140:143], 0\n"
"buffer_load_dwordx4 v190, s[8:11], s32 offen lds\n"
"ds_read_b64_tr_b16 v[140:141], v184 offset:27136\n"
"v_mfma_f32_16x16x32_bf16 v[244:247], v[112:115], v[136:139], v[244:247]\n"
"ds_read_b64_tr_b16 v[142:143], v185 offset:27136\n"
"ds_read_b64_tr_b16 v[112:113], v184 offset:27080\n"
"v_mfma_f32_16x16x32_bf16 v[248:251], v[116:119], v[136:139], v[248:251]\n"
"ds_read_b64_tr_b16 v[114:115], v185 offset:27080\n"
"v_fmamk_f32 v121, v121, 0x3e553b95, v170\n"
"v_mfma_f32_16x16x32_bf16 v[252:255], v[124:127], v[136:139], v[252:255]\n"
"ds_read_b64_tr_b16 v[136:137], v184 offset:27144\n"
"v_fmamk_f32 v122, v122, 0x3e553b95, v170\n"
"v_mfma_f32_16x16x32_bf16 v[82:85], v[172:175], v[82:85], 0\n"
"ds_read_b64_tr_b16 v[138:139], v185 offset:27144\n"
"v_fmamk_f32 v123, v123, 0x3e553b95, v170\n"
"v_mfma_f32_16x16x32_bf16 v[220:223], a[68:71], v[52:55], v[220:223]\n"
"v_exp_f32 v120, v120\n"
"v_mfma_f32_16x16x32_bf16 v[224:227], a[72:75], v[32:35], 0\n"
"v_exp_f32 v121, v121\n"
"v_mfma_f32_16x16x32_bf16 v[224:227], a[84:87], v[36:39], v[224:227]\n"
"v_exp_f32 v122, v122\n"
"v_mfma_f32_16x16x32_bf16 v[224:227], a[96:99], v[40:43], v[224:227]\n"
"v_exp_f32 v123, v123\n"
"v_mfma_f32_16x16x32_bf16 v[224:227], a[108:111], v[44:47], v[224:227]\n"
"v_fmamk_f32 v128, v128, 0x3e553b95, v170\n"
"v_fmamk_f32 v129, v129, 0x3e553b95, v170\n"
"v_mfma_f32_16x16x32_bf16 v[224:227], a[120:123], v[48:51], v[224:227]\n"
"v_fmamk_f32 v130, v130, 0x3e553b95, v170\n"
"v_fmamk_f32 v131, v131, 0x3e553b95, v170\n"
"v_mfma_f32_16x16x32_bf16 v[224:227], a[132:135], v[52:55], v[224:227]\n"
"v_cvt_pk_bf16_f32 v90, v120, v121\n"
"v_cvt_pk_bf16_f32 v91, v122, v123\n"
"v_exp_f32 v128, v128\n"
"v_exp_f32 v129, v129\n"
"v_exp_f32 v130, v130\n"
"v_exp_f32 v131, v131\n"
"s_waitcnt vmcnt(7)\n"
"v_and_b32 v201, 0xffffff00, v201\n"
"v_or_b32 v198, 0, v201\n"
"v_add_u32 v198, 2, v198\n"
"buffer_store_dword v198, v196, s[28:31], s40 offen sc1\n"
"v_and_b32 v120, v90, v176\n"
"v_and_b32 v121, v91, v177\n"
"v_xor_b32 v122, v90, v120\n"
"v_xor_b32 v123, v91, v121\n"
"v_cvt_pk_bf16_f32 v92, v128, v129\n"
"v_cvt_pk_bf16_f32 v93, v130, v131\n"
"v_mfma_f32_16x16x32_bf16 v[244:247], v[132:135], v[120:123], v[244:247]\n"
"s_add_u32 m0, s34, 9216\n"
"v_and_b32 v128, v92, v176\n"
"v_and_b32 v129, v93, v177\n"
"v_mfma_f32_16x16x32_bf16 v[248:251], v[98:101], v[120:123], v[248:251]\n"
"s_barrier\n"
"v_mfma_f32_16x16x32_bf16 v[252:255], v[106:109], v[120:123], v[252:255]\n"
"buffer_load_dwordx4 v191, s[8:11], s32 offen lds\n"
"s_add_u32 s32, s32, s33\n"
"v_xor_b32 v130, v92, v128\n"
"v_xor_b32 v131, v93, v129\n"
"v_mfma_f32_16x16x32_bf16 v[90:93], v[172:175], v[90:93], v[82:85]\n"
"s_waitcnt lgkmcnt(0)\n"
"v_cvt_pk_bf16_f32 v152, v228, v229\n"
"v_cvt_pk_bf16_f32 v153, v230, v231\n"
"v_mfma_f32_16x16x32_bf16 v[244:247], v[140:143], v[128:131], v[244:247]\n"
"s_add_u32 m0, s79, 130176\n"
"v_cvt_pk_bf16_f32 v154, v216, v217\n"
"v_cvt_pk_bf16_f32 v155, v218, v219\n"
"v_mfma_f32_16x16x32_bf16 v[248:251], v[136:139], v[128:131], v[248:251]\n"
"buffer_load_dwordx4 v192, s[16:19], s43 offen lds\n"
"v_rcp_f32 v171, v90\n"
"v_mfma_f32_16x16x32_bf16 v[252:255], v[112:115], v[128:131], v[252:255]\n"
"v_cvt_pk_bf16_f32 v166, v208, v209\n"
"v_mul_f32 v244, v171, v244\n"
"v_mfma_f32_16x16x32_bf16 v[228:231], a[76:79], v[32:35], 0\n"
"s_add_u32 m0, s79, 130176\n"
"v_mul_f32 v245, v171, v245\n"
"v_mul_f32 v246, v171, v246\n"
"v_mfma_f32_16x16x32_bf16 v[216:219], a[80:83], v[32:35], 0\n"
"v_mul_f32 v247, v171, v247\n"
"v_cvt_pk_bf16_f32 v244, v244, v245\n"
"v_mfma_f32_16x16x32_bf16 v[228:231], a[88:91], v[36:39], v[228:231]\n"
"buffer_load_dwordx4 v192, s[16:19], s43 offen offset:1024 lds\n"
"v_cvt_pk_bf16_f32 v245, v246, v247\n"
"v_mfma_f32_16x16x32_bf16 v[216:219], a[92:95], v[36:39], v[216:219]\n"
"v_mul_f32 v248, v171, v248\n"
"v_mul_f32 v249, v171, v249\n"
"v_mfma_f32_16x16x32_bf16 v[228:231], a[100:103], v[40:43], v[228:231]\n"
"s_add_u32 m0, s79, 130176\n"
"v_mul_f32 v250, v171, v250\n"
"v_mul_f32 v251, v171, v251\n"
"v_mfma_f32_16x16x32_bf16 v[216:219], a[104:107], v[40:43], v[216:219]\n"
"v_cvt_pk_bf16_f32 v246, v248, v249\n"
"v_cvt_pk_bf16_f32 v247, v250, v251\n"
"v_mfma_f32_16x16x32_bf16 v[228:231], a[112:115], v[44:47], v[228:231]\n"
"buffer_load_dwordx4 v192, s[16:19], s43 offen offset:2048 lds\n"
"v_mul_f32 v252, v171, v252\n"
"v_mfma_f32_16x16x32_bf16 v[216:219], a[116:119], v[44:47], v[216:219]\n"
"s_add_u32 s43, s79, 55296\n"
"v_mul_f32 v253, v171, v253\n"
"v_mul_f32 v254, v171, v254\n"
"v_mfma_f32_16x16x32_bf16 v[228:231], a[124:127], v[48:51], v[228:231]\n"
"v_mul_f32 v255, v171, v255\n"
"v_cvt_pk_bf16_f32 v252, v252, v253\n"
"v_mfma_f32_16x16x32_bf16 v[216:219], a[128:131], v[48:51], v[216:219]\n"
"v_cvt_pk_bf16_f32 v253, v254, v255\n"
"v_cvt_pk_bf16_f32 v167, v210, v211\n"
"v_mfma_f32_16x16x32_bf16 v[228:231], a[136:139], v[52:55], v[228:231]\n"
"buffer_store_dwordx4 v[244:247], v192, s[28:31], s37 offen sc1\n"
"v_mfma_f32_16x16x32_bf16 v[216:219], a[140:143], v[52:55], v[216:219]\n"
"ds_read_b128 v[116:119], v180\n"
"ds_read_b128 v[124:127], v181\n"
"v_mfma_f32_16x16x32_bf16 v[208:211], a[144:147], v[32:35], 0\n"
"ds_read_b64 v[82:83], v180 offset:64\n"
"v_cvt_pk_bf16_f32 v212, v212, v213\n"
"v_mfma_f32_16x16x32_bf16 v[208:211], a[156:159], v[36:39], v[208:211]\n"
"buffer_store_dwordx2 v[252:253], v193, s[28:31], s37 offen offset:1024 sc1\n"
"v_mfma_f32_16x16x32_bf16 v[208:211], a[168:171], v[40:43], v[208:211]\n"
"s_add_u32 s37, s37, 6144\n"
"ds_read_b64 v[84:85], v181 offset:64\n"
"ds_read_b128 v[120:123], v180 offset:13504\n"
"v_mfma_f32_16x16x32_bf16 v[208:211], a[180:183], v[44:47], v[208:211]\n"
"ds_read_b128 v[132:135], v181 offset:13504\n"
"v_cvt_pk_bf16_f32 v213, v214, v215\n"
"v_mfma_f32_16x16x32_bf16 v[208:211], a[192:195], v[48:51], v[208:211]\n"
"ds_read_b64 v[90:91], v180 offset:13568\n"
"v_cvt_pk_bf16_f32 v214, v204, v205\n"
"v_mfma_f32_16x16x32_bf16 v[208:211], a[204:207], v[52:55], v[208:211]\n"
"ds_read_b64 v[92:93], v181 offset:13568\n"
"v_cvt_pk_bf16_f32 v215, v206, v207\n"
"v_mfma_f32_16x16x32_bf16 v[204:207], a[148:151], v[32:35], 0\n"
"s_waitcnt lgkmcnt(7)\n"
"ds_write_b128 v186, v[212:215] offset:20256\n"
"v_mfma_f32_16x16x32_bf16 v[116:119], v[116:119], v[144:147], 0\n"
"s_waitcnt lgkmcnt(7)\n"
"v_cvt_pk_bf16_f32 v220, v220, v221\n"
"v_cvt_pk_bf16_f32 v221, v222, v223\n"
"v_mfma_f32_16x16x32_bf16 v[124:127], v[124:127], v[144:147], 0\n"
"s_waitcnt lgkmcnt(6)\n"
"v_cvt_pk_bf16_f32 v224, v224, v225\n"
"v_cvt_pk_bf16_f32 v225, v226, v227\n"
"v_mfma_f32_16x16x32_bf16 v[116:119], v[80:83], v[156:159], v[116:119]\n"
"s_waitcnt lgkmcnt(5)\n"
"ds_read_b128 v[128:131], v180 offset:27008\n"
"ds_read_b128 v[140:143], v181 offset:27008\n"
"v_mfma_f32_16x16x32_bf16 v[124:127], v[84:87], v[158:161], v[124:127]\n"
"s_waitcnt lgkmcnt(6)\n"
"ds_read_b64 v[98:99], v180 offset:27072\n"
"v_cvt_pk_bf16_f32 v226, v228, v229\n"
"v_mfma_f32_16x16x32_bf16 v[120:123], v[120:123], v[144:147], 0\n"
"s_waitcnt lgkmcnt(6)\n"
"ds_read_b64 v[100:101], v181 offset:27072\n"
"v_cvt_pk_bf16_f32 v227, v230, v231\n"
"v_mfma_f32_16x16x32_bf16 v[132:135], v[132:135], v[144:147], 0\n"
"s_waitcnt lgkmcnt(6)\n"
"ds_read_b128 v[136:139], v178\n"
"v_max_f32 v116, v116, v124\n"
"v_mfma_f32_16x16x32_bf16 v[120:123], v[88:91], v[156:159], v[120:123]\n"
"s_waitcnt lgkmcnt(6)\n"
"ds_read_b128 v[112:115], v179\n"
"v_max_f32 v117, v117, v125\n"
"v_mfma_f32_16x16x32_bf16 v[132:135], v[92:95], v[158:161], v[132:135]\n"
"ds_read_b64 v[106:107], v178 offset:64\n"
"v_max_f32 v118, v118, v126\n"
"v_mfma_f32_16x16x32_bf16 v[212:215], a[152:155], v[32:35], 0\n"
"s_waitcnt vmcnt(12)\n"
"ds_read_b128 v[32:35], v188\n"
"v_max_f32 v119, v119, v127\n"
"v_mfma_f32_16x16x32_bf16 v[204:207], a[160:163], v[36:39], v[204:207]\n"
"ds_read_b64 v[108:109], v179 offset:64\n"
"v_max_f32 v170, v116, v117\n"
"v_mfma_f32_16x16x32_bf16 v[212:215], a[164:167], v[36:39], v[212:215]\n"
"s_waitcnt lgkmcnt(8)\n"
"ds_read_b128 v[36:39], v189\n"
"v_max_f32 v171, v118, v119\n"
"v_mfma_f32_16x16x32_bf16 v[128:131], v[128:131], v[144:147], 0\n"
"s_waitcnt lgkmcnt(8)\n"
"ds_read_b64_tr_b16 a[228:229], v236 offset:12288\n"
"v_max_f32 v120, v120, v132\n"
"v_mfma_f32_16x16x32_bf16 v[140:143], v[140:143], v[144:147], 0\n"
"s_waitcnt lgkmcnt(8)\n"
"v_max_f32 v121, v121, v133\n"
"v_max_f32 v122, v122, v134\n"
"v_mfma_f32_16x16x32_bf16 v[128:131], v[96:99], v[156:159], v[128:131]\n"
"s_waitcnt lgkmcnt(7)\n"
"ds_read_b64_tr_b16 v[248:249], v184 offset:128\n"
"v_max_f32 v123, v123, v135\n"
"v_mfma_f32_16x16x32_bf16 v[140:143], v[100:103], v[158:161], v[140:143]\n"
"s_waitcnt lgkmcnt(7)\n"
"ds_read_b64_tr_b16 v[250:251], v185 offset:128\n"
"v_max3_f32 v170, v170, v120, v121\n"
"v_mfma_f32_16x16x32_bf16 v[136:139], v[136:139], v[144:147], 0\n"
"s_waitcnt lgkmcnt(7)\n"
"ds_read_b64_tr_b16 v[244:245], v184 offset:136\n"
"v_max3_f32 v171, v171, v122, v123\n"
"v_mfma_f32_16x16x32_bf16 v[112:115], v[112:115], v[144:147], 0\n"
"s_waitcnt lgkmcnt(7)\n"
"ds_read_b64_tr_b16 v[246:247], v185 offset:136\n"
"v_max_f32 v128, v128, v140\n"
"v_mfma_f32_16x16x32_bf16 v[136:139], v[104:107], v[156:159], v[136:139]\n"
"s_waitcnt lgkmcnt(7)\n"
"ds_read_b64_tr_b16 v[252:253], v184 offset:72\n"
"v_max_f32 v129, v129, v141\n"
"v_mfma_f32_16x16x32_bf16 v[204:207], a[172:175], v[40:43], v[204:207]\n"
"s_waitcnt lgkmcnt(7)\n"
"ds_read_b64_tr_b16 v[254:255], v185 offset:72\n"
"v_max_f32 v130, v130, v142\n"
"v_mfma_f32_16x16x32_bf16 v[112:115], v[108:111], v[158:161], v[112:115]\n"
"s_waitcnt lgkmcnt(7)\n"
"ds_read_b64_tr_b16 a[230:231], v236 offset:18432\n"
"v_max_f32 v131, v131, v143\n"
"v_mfma_f32_16x16x32_bf16 v[212:215], a[176:179], v[40:43], v[212:215]\n"
"s_add_u32 m0, s34, 7168\n"
"v_max3_f32 v170, v170, v128, v129\n"
"v_max3_f32 v171, v171, v130, v131\n"
"v_mfma_f32_16x16x32_bf16 v[204:207], a[184:187], v[44:47], v[204:207]\n"
"s_waitcnt vmcnt(10)\n"
"buffer_load_dwordx4 v190, s[8:11], s0 offen lds\n"
"ds_read_b128 v[40:43], v188 offset:1024\n"
"v_mfma_f32_16x16x32_bf16 v[212:215], a[188:191], v[44:47], v[212:215]\n"
"ds_read_b128 v[44:47], v189 offset:1024\n"
"v_max_f32 v136, v136, v112\n"
"v_mfma_f32_16x16x32_bf16 v[204:207], a[196:199], v[48:51], v[204:207]\n"
"ds_read_b64_tr_b16 a[232:233], v236 offset:12296\n"
"v_max_f32 v137, v137, v113\n"
"v_mfma_f32_16x16x32_bf16 v[212:215], a[200:203], v[48:51], v[212:215]\n"
"v_max_f32 v138, v138, v114\n"
"v_max_f32 v139, v139, v115\n"
"v_mfma_f32_16x16x32_bf16 v[204:207], a[208:211], v[52:55], v[204:207]\n"
"ds_read_b64_tr_b16 v[124:125], v184 offset:13632\n"
"v_max3_f32 v170, v170, v136, v137\n"
"v_mfma_f32_16x16x32_bf16 v[212:215], a[212:215], v[52:55], v[212:215]\n"
"ds_read_b64_tr_b16 v[126:127], v185 offset:13632\n"
"v_max3_f32 v171, v171, v138, v139\n"
"v_mfma_f32_16x16x32_bf16 v[228:231], a[0:3], v[32:35], 0\n"
"s_add_u32 m0, s34, 10240\n"
"ds_read_b64_tr_b16 v[132:133], v184 offset:13640\n"
"buffer_load_dwordx4 v191, s[8:11], s0 offen lds\n"
"v_mfma_f32_16x16x32_bf16 v[228:231], a[12:15], v[36:39], v[228:231]\n"
"s_waitcnt vmcnt(10)\n"
"ds_read_b128 v[48:51], v188 offset:2048\n"
"v_cvt_pk_bf16_f32 v222, v216, v217\n"
"v_cvt_pk_bf16_f32 v223, v218, v219\n"
"v_mfma_f32_16x16x32_bf16 v[216:219], a[4:7], v[32:35], 0\n"
"s_waitcnt lgkmcnt(6)\n"
"ds_read_b128 v[52:55], v189 offset:2048\n"
"ds_write_b128 v186, v[220:223] offset:20320\n"
"v_mfma_f32_16x16x32_bf16 v[220:223], a[8:11], v[32:35], 0\n"
"s_add_u32 m0, s34, 8192\n"
"v_max_f32 v170, v170, v171\n"
"v_mov_b32 v171, v170\n"
"v_mfma_f32_16x16x32_bf16 v[216:219], a[16:19], v[36:39], v[216:219]\n"
"s_add_u32 s0, s0, s33\n"
"buffer_load_dwordx4 v190, s[8:11], s1 offen lds\n"
"ds_read_b64_tr_b16 v[134:135], v185 offset:13640\n"
"v_mfma_f32_16x16x32_bf16 v[220:223], a[20:23], v[36:39], v[220:223]\n"
"ds_read_b64_tr_b16 v[140:141], v184 offset:13576\n"
"ds_read_b64_tr_b16 v[142:143], v185 offset:13576\n"
"v_mfma_f32_16x16x32_bf16 v[228:231], a[24:27], v[40:43], v[228:231]\n"
"s_waitcnt lgkmcnt(10)\n"
"v_permlane16_swap_b32 v170, v171\n"
"v_mfma_f32_16x16x32_bf16 v[216:219], a[28:31], v[40:43], v[216:219]\n"
"s_add_u32 m0, s34, 11264\n"
"ds_read_b64_tr_b16 a[234:235], v236 offset:18440\n"
"buffer_load_dwordx4 v191, s[8:11], s1 offen lds\n"
"v_mfma_f32_16x16x32_bf16 v[220:223], a[32:35], v[40:43], v[220:223]\n"
"s_add_u32 s1, s1, s33\n"
"v_max_f32 v170, v170, v171\n"
"v_mov_b32 v171, v170\n"
"v_mfma_f32_16x16x32_bf16 v[228:231], a[36:39], v[44:47], v[228:231]\n"
"s_waitcnt lgkmcnt(6)\n"
"ds_read_b64_tr_b16 v[112:113], v184 offset:27136\n"
"ds_read_b64_tr_b16 v[114:115], v185 offset:27136\n"
"v_mfma_f32_16x16x32_bf16 v[216:219], a[40:43], v[44:47], v[216:219]\n"
"v_permlane32_swap_b32 v170, v171\n"
"v_mfma_f32_16x16x32_bf16 v[220:223], a[44:47], v[44:47], v[220:223]\n"
"s_waitcnt lgkmcnt(7)\n"
"ds_read_b64_tr_b16 v[98:99], v184 offset:27144\n"
"ds_read_b64_tr_b16 v[100:101], v185 offset:27144\n"
"v_mfma_f32_16x16x32_bf16 v[228:231], a[48:51], v[48:51], v[228:231]\n"
"s_add_u32 m0, s34, 0\n"
"v_max_f32 v170, v170, v171\n"
"v_mul_f32 v170, 0xbe553b95, v170\n"
"v_mfma_f32_16x16x32_bf16 v[216:219], a[52:55], v[48:51], v[216:219]\n"
"buffer_load_dwordx4 v190, s[8:11], s32 offen lds\n"
"ds_read_b64_tr_b16 v[106:107], v184 offset:27080\n"
"v_mfma_f32_16x16x32_bf16 v[220:223], a[56:59], v[48:51], v[220:223]\n"
"ds_write_b128 v186, v[224:227] offset:20384\n"
"v_mfma_f32_16x16x32_bf16 v[228:231], a[60:63], v[52:55], v[228:231]\n"
"v_fmamk_f32 v116, v116, 0x3e553b95, v170\n"
"v_fmamk_f32 v117, v117, 0x3e553b95, v170\n"
"v_mfma_f32_16x16x32_bf16 v[216:219], a[64:67], v[52:55], v[216:219]\n"
"v_fmamk_f32 v118, v118, 0x3e553b95, v170\n"
"v_fmamk_f32 v119, v119, 0x3e553b95, v170\n"
"v_mfma_f32_16x16x32_bf16 v[220:223], a[68:71], v[52:55], v[220:223]\n"
"ds_read_b64_tr_b16 v[108:109], v185 offset:27080\n"
"ds_read_b64_tr_b16 a[236:237], v235 offset:12288\n"
"v_mfma_f32_16x16x32_bf16 v[224:227], a[72:75], v[32:35], 0\n"
"v_exp_f32 v116, v116\n"
"v_mfma_f32_16x16x32_bf16 v[224:227], a[84:87], v[36:39], v[224:227]\n"
".Lic2%=:\n"
"s_cbranch_execz .Lib0%=\n"
"ds_read_b64_tr_b16 a[238:239], v235 offset:18432\n"
"v_fmamk_f32 v120, v120, 0x3e553b95, v170\n"
"v_mfma_f32_16x16x32_bf16 v[224:227], a[96:99], v[40:43], v[224:227]\n"
"v_exp_f32 v117, v117\n"
"v_mfma_f32_16x16x32_bf16 v[224:227], a[108:111], v[44:47], v[224:227]\n"
"v_exp_f32 v118, v118\n"
"v_mfma_f32_16x16x32_bf16 v[224:227], a[120:123], v[48:51], v[224:227]\n"
"v_exp_f32 v119, v119\n"
"v_mfma_f32_16x16x32_bf16 v[224:227], a[132:135], v[52:55], v[224:227]\n"
"v_cvt_pk_bf16_f32 v82, v116, v117\n"
"v_cvt_pk_bf16_f32 v83, v118, v119\n"
"v_fmamk_f32 v121, v121, 0x3e553b95, v170\n"
"v_fmamk_f32 v122, v122, 0x3e553b95, v170\n"
"v_fmamk_f32 v123, v123, 0x3e553b95, v170\n"
"v_and_b32 v116, v82, v176\n"
"v_and_b32 v117, v83, v177\n"
"v_exp_f32 v120, v120\n"
"v_exp_f32 v121, v121\n"
"v_exp_f32 v122, v122\n"
"v_exp_f32 v123, v123\n"
"v_xor_b32 v118, v82, v116\n"
"v_xor_b32 v119, v83, v117\n"
"v_cvt_pk_bf16_f32 v84, v120, v121\n"
"v_cvt_pk_bf16_f32 v85, v122, v123\n"
"v_mfma_f32_16x16x32_bf16 v[248:251], v[248:251], v[116:119], 0\n"
"v_and_b32 v120, v84, v176\n"
"v_and_b32 v121, v85, v177\n"
"v_mfma_f32_16x16x32_bf16 v[244:247], v[244:247], v[116:119], 0\n"
"v_xor_b32 v122, v84, v120\n"
"v_xor_b32 v123, v85, v121\n"
"v_mfma_f32_16x16x32_bf16 v[252:255], v[252:255], v[116:119], 0\n"
"s_waitcnt lgkmcnt(10)\n"
"ds_read_b64_tr_b16 v[116:117], v182 offset:128\n"
"ds_read_b64_tr_b16 v[118:119], v183 offset:128\n"
"v_mfma_f32_16x16x32_bf16 v[248:251], v[124:127], v[120:123], v[248:251]\n"
"s_add_u32 m0, s34, 3072\n"
"ds_read_b64_tr_b16 v[124:125], v182 offset:72\n"
"buffer_load_dwordx4 v191, s[8:11], s32 offen lds\n"
"v_mfma_f32_16x16x32_bf16 v[244:247], v[132:135], v[120:123], v[244:247]\n"
"s_add_u32 s32, s32, s33\n"
"ds_read_b64_tr_b16 v[126:127], v183 offset:72\n"
"v_fmamk_f32 v128, v128, 0x3e553b95, v170\n"
"v_mfma_f32_16x16x32_bf16 v[252:255], v[140:143], v[120:123], v[252:255]\n"
"ds_read_b64_tr_b16 v[120:121], v182 offset:136\n"
"v_fmamk_f32 v129, v129, 0x3e553b95, v170\n"
"v_mfma_f32_16x16x32_bf16 v[82:85], v[172:175], v[82:85], 0\n"
"ds_read_b64_tr_b16 v[122:123], v183 offset:136\n"
"v_fmamk_f32 v130, v130, 0x3e553b95, v170\n"
"v_fmamk_f32 v131, v131, 0x3e553b95, v170\n"
"v_exp_f32 v128, v128\n"
"v_exp_f32 v129, v129\n"
"v_exp_f32 v130, v130\n"
"v_exp_f32 v131, v131\n"
"v_fmamk_f32 v136, v136, 0x3e553b95, v170\n"
"v_fmamk_f32 v137, v137, 0x3e553b95, v170\n"
"v_fmamk_f32 v138, v138, 0x3e553b95, v170\n"
"v_fmamk_f32 v139, v139, 0x3e553b95, v170\n"
"v_cvt_pk_bf16_f32 v90, v128, v129\n"
"v_cvt_pk_bf16_f32 v91, v130, v131\n"
"v_exp_f32 v136, v136\n"
"v_exp_f32 v137, v137\n"
"v_exp_f32 v138, v138\n"
"v_exp_f32 v139, v139\n"
"v_and_b32 v128, v90, v176\n"
"v_and_b32 v129, v91, v177\n"
"v_xor_b32 v130, v90, v128\n"
"v_xor_b32 v131, v91, v129\n"
"v_cvt_pk_bf16_f32 v92, v136, v137\n"
"v_cvt_pk_bf16_f32 v93, v138, v139\n"
"v_and_b32 v136, v92, v176\n"
"v_and_b32 v137, v93, v177\n"
"v_xor_b32 v138, v92, v136\n"
"v_xor_b32 v139, v93, v137\n"
"v_mfma_f32_16x16x32_bf16 v[90:93], v[172:175], v[90:93], v[82:85]\n"
"s_waitcnt lgkmcnt(8)\n"
"v_cvt_pk_bf16_f32 v144, v208, v209\n"
"v_cvt_pk_bf16_f32 v145, v210, v211\n"
"v_mfma_f32_16x16x32_bf16 v[248:251], v[112:115], v[128:131], v[248:251]\n"
"s_waitcnt lgkmcnt(4)\n"
"v_cvt_pk_bf16_f32 v146, v204, v205\n"
"v_cvt_pk_bf16_f32 v147, v206, v207\n"
"v_mfma_f32_16x16x32_bf16 v[244:247], v[98:101], v[128:131], v[244:247]\n"
"s_waitcnt lgkmcnt(0)\n"
"v_rcp_f32 v171, v90\n"
"v_mfma_f32_16x16x32_bf16 v[252:255], v[106:109], v[128:131], v[252:255]\n"
"v_cvt_pk_bf16_f32 v158, v212, v213\n"
"v_cvt_pk_bf16_f32 v159, v214, v215\n"
"v_mfma_f32_16x16x32_bf16 v[248:251], v[116:119], v[136:139], v[248:251]\n"
"ds_read_b128 v[132:135], v180 offset:13504\n"
"ds_read_b128 v[140:143], v181 offset:13504\n"
"v_mfma_f32_16x16x32_bf16 v[244:247], v[120:123], v[136:139], v[244:247]\n"
"ds_read_b64 v[82:83], v180 offset:13568\n"
"v_cvt_pk_bf16_f32 v228, v228, v229\n"
"v_mfma_f32_16x16x32_bf16 v[252:255], v[124:127], v[136:139], v[252:255]\n"
"ds_read_b64 v[84:85], v181 offset:13568\n"
"v_cvt_pk_bf16_f32 v229, v230, v231\n"
"v_mfma_f32_16x16x32_bf16 v[208:211], a[76:79], v[32:35], 0\n"
"v_mul_f32 v248, v171, v248\n"
"v_mul_f32 v249, v171, v249\n"
"v_mfma_f32_16x16x32_bf16 v[204:207], a[80:83], v[32:35], 0\n"
"v_mul_f32 v250, v171, v250\n"
"v_mul_f32 v251, v171, v251\n"
"v_mfma_f32_16x16x32_bf16 v[208:211], a[88:91], v[36:39], v[208:211]\n"
"v_cvt_pk_bf16_f32 v248, v248, v249\n"
"v_cvt_pk_bf16_f32 v249, v250, v251\n"
"v_mfma_f32_16x16x32_bf16 v[204:207], a[92:95], v[36:39], v[204:207]\n"
"v_mul_f32 v244, v171, v244\n"
"v_mul_f32 v245, v171, v245\n"
"v_mfma_f32_16x16x32_bf16 v[208:211], a[100:103], v[40:43], v[208:211]\n"
"s_add_u32 m0, s34, 1024\n"
"v_mul_f32 v246, v171, v246\n"
"v_mul_f32 v247, v171, v247\n"
"v_mfma_f32_16x16x32_bf16 v[204:207], a[104:107], v[40:43], v[204:207]\n"
"s_waitcnt lgkmcnt(3)\n"
"buffer_load_dwordx4 v190, s[8:11], s0 offen lds\n"
"v_cvt_pk_bf16_f32 v250, v244, v245\n"
"v_mfma_f32_16x16x32_bf16 v[132:135], v[132:135], v[148:151], 0\n"
"s_waitcnt lgkmcnt(2)\n"
"v_cvt_pk_bf16_f32 v251, v246, v247\n"
"v_mul_f32 v252, v171, v252\n"
"v_mfma_f32_16x16x32_bf16 v[140:143], v[140:143], v[148:151], 0\n"
"s_waitcnt lgkmcnt(1)\n"
"v_mul_f32 v253, v171, v253\n"
"v_mul_f32 v254, v171, v254\n"
"v_mfma_f32_16x16x32_bf16 v[132:135], v[80:83], v[160:163], v[132:135]\n"
"s_waitcnt lgkmcnt(0)\n"
"buffer_store_dwordx4 v[248:251], v192, s[28:31], s37 offen sc1\n"
"v_mfma_f32_16x16x32_bf16 v[140:143], v[84:87], v[162:165], v[140:143]\n"
"v_mul_f32 v255, v171, v255\n"
"v_cvt_pk_bf16_f32 v252, v252, v253\n"
"v_mfma_f32_16x16x32_bf16 v[208:211], a[112:115], v[44:47], v[208:211]\n"
"s_add_u32 m0, s34, 4096\n"
"v_cvt_pk_bf16_f32 v253, v254, v255\n"
"v_cvt_pk_bf16_f32 v230, v216, v217\n"
"v_mfma_f32_16x16x32_bf16 v[204:207], a[116:119], v[44:47], v[204:207]\n"
"ds_read_b128 v[128:131], v180 offset:27008\n"
"buffer_load_dwordx4 v191, s[8:11], s0 offen lds\n"
"v_mfma_f32_16x16x32_bf16 v[208:211], a[124:127], v[48:51], v[208:211]\n"
"s_add_u32 s0, s0, s33\n"
"ds_read_b128 v[112:115], v181 offset:27008\n"
"ds_read_b64 v[90:91], v180 offset:27072\n"
"v_mfma_f32_16x16x32_bf16 v[204:207], a[128:131], v[48:51], v[204:207]\n"
"s_waitcnt vmcnt(11)\n"
"ds_read_b64 v[92:93], v181 offset:27072\n"
"v_cvt_pk_bf16_f32 v231, v218, v219\n"
"v_mfma_f32_16x16x32_bf16 v[208:211], a[136:139], v[52:55], v[208:211]\n"
"s_add_u32 m0, s34, 2048\n"
"ds_read_b128 v[136:139], v178\n"
"v_cvt_pk_bf16_f32 v220, v220, v221\n"
"v_mfma_f32_16x16x32_bf16 v[204:207], a[140:143], v[52:55], v[204:207]\n"
"s_barrier\n"
"v_mfma_f32_16x16x32_bf16 v[212:215], a[144:147], v[32:35], 0\n"
"buffer_load_dwordx4 v190, s[8:11], s1 offen lds\n"
"ds_write_b128 v186, v[228:231] offset:33760\n"
"v_mfma_f32_16x16x32_bf16 v[216:219], a[148:151], v[32:35], 0\n"
"s_add_u32 m0, s79, 142464\n"
"v_cvt_pk_bf16_f32 v221, v222, v223\n"
"v_max_f32 v132, v132, v140\n"
"v_mfma_f32_16x16x32_bf16 v[228:231], a[152:155], v[32:35], 0\n"
"s_waitcnt lgkmcnt(5)\n"
"v_max_f32 v133, v133, v141\n"
"v_max_f32 v134, v134, v142\n"
"v_mfma_f32_16x16x32_bf16 v[128:131], v[128:131], v[148:151], 0\n"
"s_waitcnt lgkmcnt(4)\n"
"buffer_load_dwordx4 v192, s[16:19], s43 offen lds\n"
"ds_read_b128 v[32:35], v188 offset:6144\n"
"v_mfma_f32_16x16x32_bf16 v[112:115], v[112:115], v[148:151], 0\n"
"s_waitcnt lgkmcnt(4)\n"
"ds_read_b128 v[116:119], v179\n"
"ds_read_b64 v[98:99], v178 offset:64\n"
"v_mfma_f32_16x16x32_bf16 v[128:131], v[88:91], v[160:163], v[128:131]\n"
"s_waitcnt lgkmcnt(5)\n"
"ds_read_b64 v[100:101], v179 offset:64\n"
"v_max_f32 v135, v135, v143\n"
"v_mfma_f32_16x16x32_bf16 v[112:115], v[92:95], v[162:165], v[112:115]\n"
"s_waitcnt lgkmcnt(5)\n"
"ds_read_b128 v[120:123], v178 offset:13504\n"
"buffer_store_dwordx2 v[252:253], v193, s[28:31], s37 offen offset:1024 sc1\n"
"v_mfma_f32_16x16x32_bf16 v[136:139], v[136:139], v[148:151], 0\n"
"ds_read_b128 v[124:127], v179 offset:13504\n"
"ds_read_b64 v[106:107], v178 offset:13568\n"
"v_mfma_f32_16x16x32_bf16 v[212:215], a[156:159], v[36:39], v[212:215]\n"
"v_max_f32 v170, v132, v133\n"
"v_max_f32 v171, v134, v135\n"
"v_mfma_f32_16x16x32_bf16 v[216:219], a[160:163], v[36:39], v[216:219]\n"
"ds_read_b64 v[108:109], v179 offset:13568\n"
"ds_read_b64_tr_b16 a[240:241], v236\n"
"v_mfma_f32_16x16x32_bf16 v[228:231], a[164:167], v[36:39], v[228:231]\n"
"v_max_f32 v128, v128, v112\n"
"v_max_f32 v129, v129, v113\n"
"v_mfma_f32_16x16x32_bf16 v[212:215], a[168:171], v[40:43], v[212:215]\n"
"s_waitcnt lgkmcnt(7)\n"
"v_max_f32 v130, v130, v114\n"
"v_max_f32 v131, v131, v115\n"
"v_mfma_f32_16x16x32_bf16 v[216:219], a[172:175], v[40:43], v[216:219]\n"
"s_waitcnt lgkmcnt(6)\n"
"ds_read_b128 v[36:39], v189 offset:6144\n"
"ds_read_b64_tr_b16 v[244:245], v184 offset:13632\n"
"v_mfma_f32_16x16x32_bf16 v[116:119], v[116:119], v[148:151], 0\n"
"s_waitcnt lgkmcnt(7)\n"
"v_max3_f32 v170, v170, v128, v129\n"
"v_max3_f32 v171, v171, v130, v131\n"
"v_mfma_f32_16x16x32_bf16 v[136:139], v[96:99], v[160:163], v[136:139]\n"
"s_waitcnt lgkmcnt(6)\n"
"ds_read_b64_tr_b16 v[246:247], v185 offset:13632\n"
"ds_read_b64_tr_b16 v[248:249], v184 offset:13640\n"
"v_mfma_f32_16x16x32_bf16 v[116:119], v[100:103], v[162:165], v[116:119]\n"
"s_waitcnt lgkmcnt(7)\n"
"v_cvt_pk_bf16_f32 v224, v224, v225\n"
"v_cvt_pk_bf16_f32 v225, v226, v227\n"
"v_mfma_f32_16x16x32_bf16 v[120:123], v[120:123], v[148:151], 0\n"
"s_waitcnt lgkmcnt(6)\n"
"ds_read_b64_tr_b16 v[250:251], v185 offset:13640\n"
"ds_read_b64_tr_b16 v[252:253], v184 offset:13576\n"
"v_mfma_f32_16x16x32_bf16 v[124:127], v[124:127], v[148:151], 0\n"
"s_waitcnt lgkmcnt(7)\n"
"v_max_f32 v136, v136, v116\n"
"v_max_f32 v137, v137, v117\n"
"v_mfma_f32_16x16x32_bf16 v[120:123], v[104:107], v[160:163], v[120:123]\n"
"s_add_u32 m0, s79, 142464\n"
"buffer_load_dword v199, off, s[28:31], s39 sc1\n"
"ds_read_b64_tr_b16 v[254:255], v185 offset:13576\n"
"v_mfma_f32_16x16x32_bf16 v[124:127], v[108:111], v[162:165], v[124:127]\n"
"ds_read_b64_tr_b16 a[242:243], v236 offset:6144\n"
"v_max_f32 v138, v138, v118\n"
"v_mfma_f32_16x16x32_bf16 v[228:231], a[176:179], v[40:43], v[228:231]\n"
"v_max_f32 v139, v139, v119\n"
"v_max3_f32 v170, v170, v136, v137\n"
"v_mfma_f32_16x16x32_bf16 v[212:215], a[180:183], v[44:47], v[212:215]\n"
"ds_read_b64_tr_b16 a[244:245], v236 offset:8\n"
"v_max3_f32 v171, v171, v138, v139\n"
"v_mfma_f32_16x16x32_bf16 v[216:219], a[184:187], v[44:47], v[216:219]\n"
"v_max_f32 v120, v120, v124\n"
"v_max_f32 v121, v121, v125\n"
"v_mfma_f32_16x16x32_bf16 v[228:231], a[188:191], v[44:47], v[228:231]\n"
"ds_read_b64_tr_b16 v[140:141], v184 offset:27136\n"
"ds_read_b64_tr_b16 v[142:143], v185 offset:27136\n"
"v_mfma_f32_16x16x32_bf16 v[212:215], a[192:195], v[48:51], v[212:215]\n"
"v_max_f32 v122, v122, v126\n"
"v_max_f32 v123, v123, v127\n"
"v_mfma_f32_16x16x32_bf16 v[216:219], a[196:199], v[48:51], v[216:219]\n"
"ds_read_b64_tr_b16 v[112:113], v184 offset:27144\n"
"ds_read_b64_tr_b16 v[114:115], v185 offset:27144\n"
"v_mfma_f32_16x16x32_bf16 v[228:231], a[200:203], v[48:51], v[228:231]\n"
"v_max3_f32 v170, v170, v120, v121\n"
"v_max3_f32 v171, v171, v122, v123\n"
"v_mfma_f32_16x16x32_bf16 v[212:215], a[204:207], v[52:55], v[212:215]\n"
"ds_read_b64_tr_b16 v[116:117], v184 offset:27080\n"
"ds_read_b64_tr_b16 v[118:119], v185 offset:27080\n"
"v_mfma_f32_16x16x32_bf16 v[216:219], a[208:211], v[52:55], v[216:219]\n"
"v_cvt_pk_bf16_f32 v226, v208, v209\n"
"v_cvt_pk_bf16_f32 v227, v210, v211\n"
"v_mfma_f32_16x16x32_bf16 v[228:231], a[212:215], v[52:55], v[228:231]\n"
"s_waitcnt vmcnt(11)\n"
"ds_read_b64_tr_b16 v[124:125], v182 offset:128\n"
"ds_read_b64_tr_b16 v[126:127], v183 offset:128\n"
"v_mfma_f32_16x16x32_bf16 v[208:211], a[0:3], v[32:35], 0\n"
"s_waitcnt lgkmcnt(15)\n"
"v_cvt_pk_bf16_f32 v222, v204, v205\n"
"v_cvt_pk_bf16_f32 v223, v206, v207\n"
"v_mfma_f32_16x16x32_bf16 v[204:207], a[4:7], v[32:35], 0\n"
"ds_read_b128 v[40:43], v188 offset:7168\n"
"ds_read_b128 v[44:47], v189 offset:7168\n"
"v_mfma_f32_16x16x32_bf16 v[208:211], a[12:15], v[36:39], v[208:211]\n"
"v_max_f32 v170, v170, v171\n"
"v_mov_b32 v171, v170\n"
"v_mfma_f32_16x16x32_bf16 v[204:207], a[16:19], v[36:39], v[204:207]\n"
"ds_write_b128 v186, v[220:223] offset:33824\n"
"v_mfma_f32_16x16x32_bf16 v[220:223], a[8:11], v[32:35], 0\n"
"v_permlane16_swap_b32 v170, v171\n"
"v_mfma_f32_16x16x32_bf16 v[220:223], a[20:23], v[36:39], v[220:223]\n"
"v_max_f32 v170, v170, v171\n"
"v_mov_b32 v171, v170\n"
"ds_read_b64_tr_b16 v[98:99], v182 offset:136\n"
"ds_read_b64_tr_b16 a[246:247], v236 offset:6152\n"
"v_permlane32_swap_b32 v170, v171\n"
"v_max_f32 v170, v170, v171\n"
"v_mul_f32 v170, 0xbe553b95, v170\n"
"v_fmamk_f32 v132, v132, 0x3e553b95, v170\n"
"v_fmamk_f32 v133, v133, 0x3e553b95, v170\n"
"v_fmamk_f32 v134, v134, 0x3e553b95, v170\n"
"v_fmamk_f32 v135, v135, 0x3e553b95, v170\n"
"v_fmamk_f32 v128, v128, 0x3e553b95, v170\n"
"v_fmamk_f32 v129, v129, 0x3e553b95, v170\n"
"v_exp_f32 v132, v132\n"
"v_exp_f32 v133, v133\n"
"v_exp_f32 v134, v134\n"
"v_exp_f32 v135, v135\n"
"v_cvt_pk_bf16_f32 v82, v132, v133\n"
"v_cvt_pk_bf16_f32 v83, v134, v135\n"
"v_fmamk_f32 v130, v130, 0x3e553b95, v170\n"
"v_fmamk_f32 v131, v131, 0x3e553b95, v170\n"
"v_and_b32 v132, v82, v176\n"
"v_and_b32 v133, v83, v177\n"
"v_exp_f32 v128, v128\n"
"v_exp_f32 v129, v129\n"
"v_exp_f32 v130, v130\n"
"v_exp_f32 v131, v131\n"
"v_xor_b32 v134, v82, v132\n"
"v_xor_b32 v135, v83, v133\n"
"v_cvt_pk_bf16_f32 v84, v128, v129\n"
"v_cvt_pk_bf16_f32 v85, v130, v131\n"
"v_and_b32 v128, v84, v176\n"
"v_and_b32 v129, v85, v177\n"
"v_xor_b32 v130, v84, v128\n"
"v_xor_b32 v131, v85, v129\n"
"v_mfma_f32_16x16x32_bf16 v[82:85], v[172:175], v[82:85], 0\n"
"s_waitcnt vmcnt(9) lgkmcnt(4)\n"
"buffer_load_dwordx4 v192, s[16:19], s43 offen offset:1024 lds\n"
"ds_read_b128 v[48:51], v188 offset:8192\n"
"v_mfma_f32_16x16x32_bf16 v[244:247], v[244:247], v[132:135], 0\n"
"ds_read_b128 v[52:55], v189 offset:8192\n"
"ds_read_b64_tr_b16 v[100:101], v183 offset:136\n"
"v_mfma_f32_16x16x32_bf16 v[248:251], v[248:251], v[132:135], 0\n"
"s_waitcnt lgkmcnt(6)\n"
"ds_read_b64_tr_b16 v[106:107], v182 offset:72\n"
"v_fmamk_f32 v136, v136, 0x3e553b95, v170\n"
"v_mfma_f32_16x16x32_bf16 v[252:255], v[252:255], v[132:135], 0\n"
"s_add_u32 m0, s79, 142464\n"
"ds_read_b64_tr_b16 v[108:109], v183 offset:72\n"
"buffer_load_dwordx4 v192, s[16:19], s43 offen offset:2048 lds\n"
"v_mfma_f32_16x16x32_bf16 v[244:247], v[140:143], v[128:131], v[244:247]\n"
"s_add_u32 s43, s80, 12288\n"
"ds_write_b128 v186, v[224:227] offset:33888\n"
"v_mfma_f32_16x16x32_bf16 v[248:251], v[112:115], v[128:131], v[248:251]\n"
"s_add_u32 m0, s34, 5120\n"
"v_fmamk_f32 v137, v137, 0x3e553b95, v170\n"
"v_fmamk_f32 v138, v138, 0x3e553b95, v170\n"
"v_mfma_f32_16x16x32_bf16 v[252:255], v[116:119], v[128:131], v[252:255]\n"
"v_fmamk_f32 v139, v139, 0x3e553b95, v170\n"
"v_fmamk_f32 v120, v120, 0x3e553b95, v170\n"
"v_mfma_f32_16x16x32_bf16 v[208:211], a[24:27], v[40:43], v[208:211]\n"
"buffer_load_dwordx4 v191, s[8:11], s1 offen lds\n"
"ds_read_b64_tr_b16 v[132:133], v182 offset:13632\n"
"v_mfma_f32_16x16x32_bf16 v[204:207], a[28:31], v[40:43], v[204:207]\n"
"s_add_u32 s1, s1, s33\n"
"ds_read_b64_tr_b16 v[134:135], v183 offset:13632\n"
"ds_read_b64_tr_b16 a[248:249], v235\n"
"v_mfma_f32_16x16x32_bf16 v[220:223], a[32:35], v[40:43], v[220:223]\n"
"s_waitcnt lgkmcnt(7)\n"
"v_exp_f32 v136, v136\n"
"v_mfma_f32_16x16x32_bf16 v[208:211], a[36:39], v[44:47], v[208:211]\n"
"s_add_u32 m0, s34, 6144\n"
"ds_read_b64_tr_b16 v[128:129], v182 offset:13640\n"
"buffer_load_dwordx4 v190, s[8:11], s32 offen lds\n"
"v_mfma_f32_16x16x32_bf16 v[204:207], a[40:43], v[44:47], v[204:207]\n"
"ds_read_b64_tr_b16 v[130:131], v183 offset:13640\n"
"v_fmamk_f32 v121, v121, 0x3e553b95, v170\n"
"v_mfma_f32_16x16x32_bf16 v[220:223], a[44:47], v[44:47], v[220:223]\n"
"ds_read_b64_tr_b16 v[140:141], v182 offset:13576\n"
"v_fmamk_f32 v122, v122, 0x3e553b95, v170\n"
"v_mfma_f32_16x16x32_bf16 v[208:211], a[48:51], v[48:51], v[208:211]\n"
"ds_read_b64_tr_b16 v[142:143], v183 offset:13576\n"
"v_fmamk_f32 v123, v123, 0x3e553b95, v170\n"
"v_mfma_f32_16x16x32_bf16 v[204:207], a[52:55], v[48:51], v[204:207]\n"
"ds_read_b64_tr_b16 a[250:251], v235 offset:6144\n"
"v_exp_f32 v137, v137\n"
"v_mfma_f32_16x16x32_bf16 v[220:223], a[56:59], v[48:51], v[220:223]\n"
"v_exp_f32 v138, v138\n"
"v_mfma_f32_16x16x32_bf16 v[208:211], a[60:63], v[52:55], v[208:211]\n"
"v_exp_f32 v139, v139\n"
"v_mfma_f32_16x16x32_bf16 v[204:207], a[64:67], v[52:55], v[204:207]\n"
"v_cvt_pk_bf16_f32 v90, v136, v137\n"
"v_cvt_pk_bf16_f32 v91, v138, v139\n"
"v_mfma_f32_16x16x32_bf16 v[220:223], a[68:71], v[52:55], v[220:223]\n"
"v_exp_f32 v120, v120\n"
"v_mfma_f32_16x16x32_bf16 v[224:227], a[72:75], v[32:35], 0\n"
"s_add_u32 m0, s34, 9216\n"
"v_exp_f32 v121, v121\n"
"v_mfma_f32_16x16x32_bf16 v[224:227], a[84:87], v[36:39], v[224:227]\n"
"buffer_load_dwordx4 v191, s[8:11], s32 offen lds\n"
"v_and_b32 v136, v90, v176\n"
"v_mfma_f32_16x16x32_bf16 v[224:227], a[96:99], v[40:43], v[224:227]\n"
"s_add_u32 s32, s32, s33\n"
"v_exp_f32 v122, v122\n"
"v_mfma_f32_16x16x32_bf16 v[224:227], a[108:111], v[44:47], v[224:227]\n"
"v_exp_f32 v123, v123\n"
"v_mfma_f32_16x16x32_bf16 v[224:227], a[120:123], v[48:51], v[224:227]\n"
"v_and_b32 v137, v91, v177\n"
"v_xor_b32 v138, v90, v136\n"
"v_mfma_f32_16x16x32_bf16 v[224:227], a[132:135], v[52:55], v[224:227]\n"
"v_xor_b32 v139, v91, v137\n"
"v_cvt_pk_bf16_f32 v92, v120, v121\n"
"v_cvt_pk_bf16_f32 v93, v122, v123\n"
"v_mfma_f32_16x16x32_bf16 v[244:247], v[124:127], v[136:139], v[244:247]\n"
"s_waitcnt lgkmcnt(9)\n"
"v_and_b32 v120, v92, v176\n"
"v_and_b32 v121, v93, v177\n"
"v_mfma_f32_16x16x32_bf16 v[248:251], v[98:101], v[136:139], v[248:251]\n"
"v_xor_b32 v122, v92, v120\n"
"v_xor_b32 v123, v93, v121\n"
"v_mfma_f32_16x16x32_bf16 v[90:93], v[172:175], v[90:93], v[82:85]\n"
"s_waitcnt lgkmcnt(3)\n"
"v_cvt_pk_bf16_f32 v148, v212, v213\n"
"v_cvt_pk_bf16_f32 v149, v214, v215\n"
"v_mfma_f32_16x16x32_bf16 v[252:255], v[106:109], v[136:139], v[252:255]\n"
"s_waitcnt lgkmcnt(1)\n"
"v_cvt_pk_bf16_f32 v150, v216, v217\n"
"v_cvt_pk_bf16_f32 v151, v218, v219\n"
"v_mfma_f32_16x16x32_bf16 v[244:247], v[132:135], v[120:123], v[244:247]\n"
"v_rcp_f32 v171, v90\n"
"v_mfma_f32_16x16x32_bf16 v[248:251], v[128:131], v[120:123], v[248:251]\n"
"v_cvt_pk_bf16_f32 v162, v228, v229\n"
"v_cvt_pk_bf16_f32 v163, v230, v231\n"
"v_mfma_f32_16x16x32_bf16 v[252:255], v[140:143], v[120:123], v[252:255]\n"
"ds_read_b128 v[112:115], v180 offset:27008\n"
"ds_read_b128 v[116:119], v181 offset:27008\n"
"v_mfma_f32_16x16x32_bf16 v[212:215], a[76:79], v[32:35], 0\n"
"v_mul_f32 v244, v171, v244\n"
"v_mul_f32 v245, v171, v245\n"
"v_mfma_f32_16x16x32_bf16 v[216:219], a[80:83], v[32:35], 0\n"
"v_mul_f32 v246, v171, v246\n"
"v_mul_f32 v247, v171, v247\n"
"v_mfma_f32_16x16x32_bf16 v[212:215], a[88:91], v[36:39], v[212:215]\n"
"v_cvt_pk_bf16_f32 v244, v244, v245\n"
"v_cvt_pk_bf16_f32 v245, v246, v247\n"
"v_mfma_f32_16x16x32_bf16 v[216:219], a[92:95], v[36:39], v[216:219]\n"
"v_mul_f32 v248, v171, v248\n"
"v_mul_f32 v249, v171, v249\n"
"v_mfma_f32_16x16x32_bf16 v[212:215], a[100:103], v[40:43], v[212:215]\n"
"v_mul_f32 v250, v171, v250\n"
"v_mul_f32 v251, v171, v251\n"
"v_mfma_f32_16x16x32_bf16 v[216:219], a[104:107], v[40:43], v[216:219]\n"
"v_cvt_pk_bf16_f32 v246, v248, v249\n"
"v_cvt_pk_bf16_f32 v247, v250, v251\n"
"v_mfma_f32_16x16x32_bf16 v[212:215], a[112:115], v[44:47], v[212:215]\n"
"v_mul_f32 v252, v171, v252\n"
"v_mul_f32 v253, v171, v253\n"
"v_mfma_f32_16x16x32_bf16 v[216:219], a[116:119], v[44:47], v[216:219]\n"
"s_waitcnt lgkmcnt(1)\n"
"v_mul_f32 v254, v171, v254\n"
"v_mul_f32 v255, v171, v255\n"
"v_mfma_f32_16x16x32_bf16 v[112:115], v[112:115], v[152:155], 0\n"
"s_waitcnt lgkmcnt(0)\n"
"v_cvt_pk_bf16_f32 v252, v252, v253\n"
"v_cvt_pk_bf16_f32 v253, v254, v255\n"
"v_mfma_f32_16x16x32_bf16 v[116:119], v[116:119], v[152:155], 0\n"
"ds_read_b64 v[82:83], v180 offset:27072\n"
"ds_read_b64 v[84:85], v181 offset:27072\n"
"v_mfma_f32_16x16x32_bf16 v[212:215], a[124:127], v[48:51], v[212:215]\n"
"ds_read_b128 v[136:139], v178\n"
"v_cvt_pk_bf16_f32 v208, v208, v209\n"
"v_mfma_f32_16x16x32_bf16 v[216:219], a[128:131], v[48:51], v[216:219]\n"
"ds_read_b128 v[124:127], v179\n"
"v_cvt_pk_bf16_f32 v209, v210, v211\n"
"v_mfma_f32_16x16x32_bf16 v[212:215], a[136:139], v[52:55], v[212:215]\n"
"ds_read_b64 v[90:91], v178 offset:64\n"
"v_cvt_pk_bf16_f32 v210, v204, v205\n"
"v_mfma_f32_16x16x32_bf16 v[216:219], a[140:143], v[52:55], v[216:219]\n"
"ds_read_b64 v[92:93], v179 offset:64\n"
"v_cvt_pk_bf16_f32 v211, v206, v207\n"
"v_mfma_f32_16x16x32_bf16 v[228:231], a[144:147], v[32:35], 0\n"
"ds_read_b128 v[120:123], v178 offset:13504\n"
"v_cvt_pk_bf16_f32 v220, v220, v221\n"
"v_mfma_f32_16x16x32_bf16 v[204:207], a[148:151], v[32:35], 0\n"
"ds_read_b128 v[132:135], v179 offset:13504\n"
"v_cvt_pk_bf16_f32 v221, v222, v223\n"
"v_mfma_f32_16x16x32_bf16 v[228:231], a[156:159], v[36:39], v[228:231]\n"
"ds_read_b64 v[98:99], v178 offset:13568\n"
"v_cvt_pk_bf16_f32 v224, v224, v225\n"
"v_mfma_f32_16x16x32_bf16 v[204:207], a[160:163], v[36:39], v[204:207]\n"
"s_waitcnt lgkmcnt(8)\n"
"ds_read_b64 v[100:101], v179 offset:13568\n"
"v_cvt_pk_bf16_f32 v225, v226, v227\n"
"v_mfma_f32_16x16x32_bf16 v[112:115], v[80:83], v[164:167], v[112:115]\n"
"s_waitcnt lgkmcnt(8)\n"
"ds_read_b64_tr_b16 v[248:249], v184 offset:27136\n"
"v_cvt_pk_bf16_f32 v226, v212, v213\n"
"v_mfma_f32_16x16x32_bf16 v[116:119], v[84:87], v[166:169], v[116:119]\n"
"s_waitcnt lgkmcnt(8)\n"
"ds_read_b64_tr_b16 v[250:251], v185 offset:27136\n"
"v_cvt_pk_bf16_f32 v227, v214, v215\n"
"v_mfma_f32_16x16x32_bf16 v[136:139], v[136:139], v[152:155], 0\n"
"s_waitcnt lgkmcnt(8)\n"
"ds_read_b64_tr_b16 v[254:255], v185 offset:27080\n"
"v_cvt_pk_bf16_f32 v222, v216, v217\n"
"v_mfma_f32_16x16x32_bf16 v[124:127], v[124:127], v[152:155], 0\n"
"s_waitcnt lgkmcnt(8)\n"
"v_max_f32 v112, v112, v116\n"
"v_max_f32 v113, v113, v117\n"
"v_mfma_f32_16x16x32_bf16 v[136:139], v[88:91], v[164:167], v[136:139]\n"
"s_waitcnt lgkmcnt(7)\n"
"ds_read_b64_tr_b16 v[116:117], v182 offset:128\n"
"v_max_f32 v114, v114, v118\n"
"v_mfma_f32_16x16x32_bf16 v[124:127], v[92:95], v[166:169], v[124:127]\n"
"s_waitcnt lgkmcnt(7)\n"
"v_max_f32 v115, v115, v119\n"
"v_max_f32 v170, v112, v113\n"
"v_mfma_f32_16x16x32_bf16 v[120:123], v[120:123], v[152:155], 0\n"
"s_waitcnt lgkmcnt(6)\n"
"ds_read_b64_tr_b16 v[118:119], v183 offset:128\n"
"v_max_f32 v171, v114, v115\n"
"v_mfma_f32_16x16x32_bf16 v[132:135], v[132:135], v[152:155], 0\n"
"s_waitcnt lgkmcnt(6)\n"
"v_max_f32 v136, v136, v124\n"
"v_max_f32 v137, v137, v125\n"
"v_mfma_f32_16x16x32_bf16 v[120:123], v[96:99], v[164:167], v[120:123]\n"
"s_waitcnt lgkmcnt(5)\n"
"ds_read_b64_tr_b16 v[124:125], v182 offset:136\n"
"ds_read_b64_tr_b16 v[98:99], v182 offset:13640\n"
"v_mfma_f32_16x16x32_bf16 v[132:135], v[100:103], v[166:169], v[132:135]\n"
"s_add_u32 m0, s34, 7168\n"
"ds_read_b64_tr_b16 v[100:101], v183 offset:13640\n"
"buffer_load_dwordx4 v190, s[8:11], s0 offen lds\n"
"v_mfma_f32_16x16x32_bf16 v[228:231], a[168:171], v[40:43], v[228:231]\n"
"v_max_f32 v138, v138, v126\n"
"v_max_f32 v139, v139, v127\n"
"v_mfma_f32_16x16x32_bf16 v[204:207], a[172:175], v[40:43], v[204:207]\n"
"s_add_u32 m0, s34, 10240\n"
"ds_read_b64_tr_b16 v[126:127], v183 offset:136\n"
"v_max3_f32 v170, v170, v136, v137\n"
"v_mfma_f32_16x16x32_bf16 v[228:231], a[180:183], v[44:47], v[228:231]\n"
"v_max3_f32 v171, v171, v138, v139\n"
"v_max_f32 v120, v120, v132\n"
"v_mfma_f32_16x16x32_bf16 v[204:207], a[184:187], v[44:47], v[204:207]\n"
"buffer_load_dwordx4 v191, s[8:11], s0 offen lds\n"
"v_max_f32 v121, v121, v133\n"
"v_mfma_f32_16x16x32_bf16 v[228:231], a[192:195], v[48:51], v[228:231]\n"
"s_add_u32 s0, s0, s33\n"
"ds_read_b64_tr_b16 v[132:133], v182 offset:72\n"
"v_max_f32 v122, v122, v134\n"
"v_mfma_f32_16x16x32_bf16 v[204:207], a[196:199], v[48:51], v[204:207]\n"
"s_add_u32 m0, s34, 8192\n"
"v_max_f32 v123, v123, v135\n"
"v_max3_f32 v170, v170, v120, v121\n"
"v_mfma_f32_16x16x32_bf16 v[228:231], a[204:207], v[52:55], v[228:231]\n"
"ds_read_b64_tr_b16 v[134:135], v183 offset:72\n"
"buffer_load_dwordx4 v190, s[8:11], s1 offen lds\n"
"v_mfma_f32_16x16x32_bf16 v[204:207], a[208:211], v[52:55], v[204:207]\n"
"v_max3_f32 v171, v171, v122, v123\n"
"v_cvt_pk_bf16_f32 v223, v218, v219\n"
"s_mov_b64 exec, s[44:45]\n"
"v_add_u32 v181, 0xc0, v181\n"
"s_mov_b64 exec, -1\n"
"s_add_u32 m0, s34, 11264\n"
"s_nop 0\n"
"buffer_load_dwordx4 v191, s[8:11], s1 offen lds\n"
"s_add_u32 s1, s1, s33\n"
"s_waitcnt vmcnt(7)\n"
"v_add3_u32 v199, v199, v197, -2\n"
"v_cmp_lt_u32 vcc, 64, v199\n"
"s_barrier\n"
"s_add_u32 m0, s79, 130176\n"
"ds_write_b128 v186, v[208:211] offset:47264\n"
"v_mfma_f32_16x16x32_bf16 v[208:211], a[152:155], v[32:35], 0\n"
"s_nop 0\n"
"s_cbranch_vccnz .Lp1%=\n"
".Lr1%=:\n"
"v_mfma_f32_16x16x32_bf16 v[208:211], a[164:167], v[36:39], v[208:211]\n"
"buffer_load_dwordx4 v192, s[16:19], s43 offen lds\n"
"ds_read_b128 v[32:35], v188\n"
"v_mfma_f32_16x16x32_bf16 v[208:211], a[176:179], v[40:43], v[208:211]\n"
"ds_read_b128 v[36:39], v189\n"
"ds_read_b128 v[40:43], v188 offset:1024\n"
"v_mfma_f32_16x16x32_bf16 v[208:211], a[188:191], v[44:47], v[208:211]\n"
"ds_read_b128 v[44:47], v189 offset:1024\n"
"v_mfma_f32_16x16x32_bf16 v[208:211], a[200:203], v[48:51], v[208:211]\n"
"ds_read_b128 v[128:131], v178 offset:27008\n"
"v_mfma_f32_16x16x32_bf16 v[208:211], a[212:215], v[52:55], v[208:211]\n"
"ds_read_b128 v[140:143], v179 offset:27008\n"
"ds_read_b64 v[106:107], v178 offset:27072\n"
"ds_read_b64 v[108:109], v179 offset:27072\n"
"ds_read_b64_tr_b16 a[252:253], v236 offset:12288\n"
"s_waitcnt lgkmcnt(8)\n"
"v_mfma_f32_16x16x32_bf16 v[212:215], a[0:3], v[32:35], 0\n"
"s_waitcnt lgkmcnt(7)\n"
"ds_read_b64_tr_b16 a[254:255], v236 offset:18432\n"
"v_mfma_f32_16x16x32_bf16 v[216:219], a[4:7], v[32:35], 0\n"
"v_mfma_f32_16x16x32_bf16 v[212:215], a[12:15], v[36:39], v[212:215]\n"
"s_waitcnt lgkmcnt(7)\n"
"ds_write_b128 v186, v[220:223] offset:47328\n"
"v_mfma_f32_16x16x32_bf16 v[220:223], a[8:11], v[32:35], 0\n"
"s_waitcnt lgkmcnt(5)\n"
"v_mfma_f32_16x16x32_bf16 v[128:131], v[128:131], v[152:155], 0\n"
"s_waitcnt lgkmcnt(4)\n"
"ds_write_b128 v186, v[224:227] offset:47392\n"
"v_mfma_f32_16x16x32_bf16 v[140:143], v[140:143], v[152:155], 0\n"
"s_waitcnt lgkmcnt(4)\n"
"v_cvt_pk_bf16_f32 v152, v228, v229\n"
"v_cvt_pk_bf16_f32 v153, v230, v231\n"
"v_mfma_f32_16x16x32_bf16 v[128:131], v[104:107], v[164:167], v[128:131]\n"
"v_cvt_pk_bf16_f32 v154, v204, v205\n"
"v_cvt_pk_bf16_f32 v155, v206, v207\n"
"v_mfma_f32_16x16x32_bf16 v[140:143], v[108:111], v[166:169], v[140:143]\n"
"ds_read_b64_tr_b16 v[106:107], v182 offset:13576\n"
"ds_read_b64_tr_b16 v[108:109], v183 offset:13576\n"
"v_mfma_f32_16x16x32_bf16 v[216:219], a[16:19], v[36:39], v[216:219]\n"
"ds_read_b64_tr_b16 v[0:1], v236 offset:12296\n"
"v_cvt_pk_bf16_f32 v166, v208, v209\n"
"v_mfma_f32_16x16x32_bf16 v[220:223], a[20:23], v[36:39], v[220:223]\n"
"s_waitcnt vmcnt(7)\n"
"buffer_load_dword v201, off, s[28:31], s41 sc1\n"
"v_max_f32 v128, v128, v140\n"
"v_mfma_f32_16x16x32_bf16 v[212:215], a[24:27], v[40:43], v[212:215]\n"
"s_add_u32 s41, s37, 49152\n"
"buffer_store_dwordx4 v[68:71], v192, s[28:31], s41 offen sc1\n"
"v_mfma_f32_16x16x32_bf16 v[216:219], a[28:31], v[40:43], v[216:219]\n"
"s_add_u32 s37, s37, 6144\n"
"ds_read_b128 v[48:51], v188 offset:2048\n"
"ds_read_b128 v[52:55], v189 offset:2048\n"
"v_mfma_f32_16x16x32_bf16 v[220:223], a[32:35], v[40:43], v[220:223]\n"
"v_max_f32 v129, v129, v141\n"
"v_max_f32 v130, v130, v142\n"
"v_mfma_f32_16x16x32_bf16 v[212:215], a[36:39], v[44:47], v[212:215]\n"
"buffer_store_dwordx4 v[244:247], v192, s[28:31], s37 offen sc1\n"
"v_mfma_f32_16x16x32_bf16 v[216:219], a[40:43], v[44:47], v[216:219]\n"
"s_nop 0\n"
"ds_read_b64_tr_b16 v[244:245], v184 offset:27144\n"
"ds_read_b64_tr_b16 v[246:247], v185 offset:27144\n"
"v_mfma_f32_16x16x32_bf16 v[220:223], a[44:47], v[44:47], v[220:223]\n"
".Lic3%=:\n"
"s_cbranch_execz .Lib0%=\n"
"ds_read_b64_tr_b16 v[140:141], v182 offset:13632\n"
"v_max_f32 v131, v131, v143\n"
"v_mfma_f32_16x16x32_bf16 v[224:227], a[72:75], v[32:35], 0\n"
"buffer_store_dwordx2 v[252:253], v193, s[28:31], s37 offen offset:1024 sc1\n"
"v_mfma_f32_16x16x32_bf16 v[228:231], a[76:79], v[32:35], 0\n"
"ds_read_b64_tr_b16 v[252:253], v184 offset:27080\n"
"ds_read_b64_tr_b16 v[142:143], v183 offset:13632\n"
"v_mfma_f32_16x16x32_bf16 v[204:207], a[80:83], v[32:35], 0\n"
"s_waitcnt lgkmcnt(6)\n"
"v_max3_f32 v170, v170, v128, v129\n"
"v_max3_f32 v171, v171, v130, v131\n"
"v_mfma_f32_16x16x32_bf16 v[212:215], a[48:51], v[48:51], v[212:215]\n"
"s_waitcnt lgkmcnt(5)\n"
"ds_read_b64_tr_b16 v[2:3], v236 offset:18440\n"
"v_max_f32 v170, v170, v171\n"
"v_mfma_f32_16x16x32_bf16 v[216:219], a[52:55], v[48:51], v[216:219]\n"
"s_add_u32 m0, s79, 130176\n"
"buffer_load_dwordx4 v[56:59], v192, s[28:31], s38 offen sc1\n"
"ds_read_b64_tr_b16 v[4:5], v235 offset:12288\n"
"v_mfma_f32_16x16x32_bf16 v[220:223], a[56:59], v[48:51], v[220:223]\n"
"v_mov_b32 v171, v170\n"
"v_cvt_pk_bf16_f32 v167, v210, v211\n"
"v_mfma_f32_16x16x32_bf16 v[212:215], a[60:63], v[52:55], v[212:215]\n"
"ds_read_b64_tr_b16 v[6:7], v235 offset:18432\n"
"v_permlane16_swap_b32 v170, v171\n"
"v_mfma_f32_16x16x32_bf16 v[216:219], a[64:67], v[52:55], v[216:219]\n"
"v_max_f32 v170, v170, v171\n"
"v_mov_b32 v171, v170\n"
"v_mfma_f32_16x16x32_bf16 v[220:223], a[68:71], v[52:55], v[220:223]\n"
"buffer_load_dwordx4 v192, s[16:19], s43 offen offset:1024 lds\n"
"v_permlane32_swap_b32 v170, v171\n"
"v_mfma_f32_16x16x32_bf16 v[224:227], a[84:87], v[36:39], v[224:227]\n"
"v_max_f32 v170, v170, v171\n"
"v_mul_f32 v170, 0xbe553b95, v170\n"
"v_mfma_f32_16x16x32_bf16 v[228:231], a[88:91], v[36:39], v[228:231]\n"
"s_add_u32 m0, s79, 130176\n"
"v_fmamk_f32 v112, v112, 0x3e553b95, v170\n"
"v_fmamk_f32 v113, v113, 0x3e553b95, v170\n"
"v_mfma_f32_16x16x32_bf16 v[204:207], a[92:95], v[36:39], v[204:207]\n"
"v_fmamk_f32 v114, v114, 0x3e553b95, v170\n"
"v_fmamk_f32 v115, v115, 0x3e553b95, v170\n"
"v_mfma_f32_16x16x32_bf16 v[224:227], a[96:99], v[40:43], v[224:227]\n"
"buffer_load_dwordx4 v192, s[16:19], s43 offen offset:2048 lds\n"
"v_fmamk_f32 v136, v136, 0x3e553b95, v170\n"
"v_mfma_f32_16x16x32_bf16 v[228:231], a[100:103], v[40:43], v[228:231]\n"
"s_add_u32 s43, s80, 49152\n"
"v_exp_f32 v112, v112\n"
"v_mfma_f32_16x16x32_bf16 v[204:207], a[104:107], v[40:43], v[204:207]\n"
"v_exp_f32 v113, v113\n"
"v_mfma_f32_16x16x32_bf16 v[224:227], a[108:111], v[44:47], v[224:227]\n"
"v_exp_f32 v114, v114\n"
"v_mfma_f32_16x16x32_bf16 v[228:231], a[112:115], v[44:47], v[228:231]\n"
"s_add_u32 m0, s34, 0\n"
"buffer_store_dwordx2 v[68:69], v193, s[28:31], s41 offen offset:1024 sc1\n"
"v_mfma_f32_16x16x32_bf16 v[204:207], a[116:119], v[44:47], v[204:207]\n"
"s_add_u32 s41, s37, 49152\n"
"v_exp_f32 v115, v115\n"
"v_mfma_f32_16x16x32_bf16 v[224:227], a[120:123], v[48:51], v[224:227]\n"
"s_add_u32 s37, s37, 6144\n"
"v_cvt_pk_bf16_f32 v82, v112, v113\n"
"v_cvt_pk_bf16_f32 v83, v114, v115\n"
"v_mfma_f32_16x16x32_bf16 v[228:231], a[124:127], v[48:51], v[228:231]\n"
"buffer_load_dwordx4 v190, s[8:11], s32 offen lds\n"
"v_and_b32 v112, v82, v176\n"
"v_mfma_f32_16x16x32_bf16 v[204:207], a[128:131], v[48:51], v[204:207]\n"
"v_and_b32 v113, v83, v177\n"
"v_xor_b32 v114, v82, v112\n"
"v_mfma_f32_16x16x32_bf16 v[224:227], a[132:135], v[52:55], v[224:227]\n"
"v_xor_b32 v115, v83, v113\n"
"v_fmamk_f32 v137, v137, 0x3e553b95, v170\n"
"v_mfma_f32_16x16x32_bf16 v[228:231], a[136:139], v[52:55], v[228:231]\n"
"s_waitcnt lgkmcnt(4)\n"
"v_fmamk_f32 v138, v138, 0x3e553b95, v170\n"
"v_fmamk_f32 v139, v139, 0x3e553b95, v170\n"
"v_mfma_f32_16x16x32_bf16 v[248:251], v[248:251], v[112:115], 0\n"
"buffer_store_dwordx4 v[68:71], v192, s[28:31], s41 offen sc1\n"
"v_mfma_f32_16x16x32_bf16 v[244:247], v[244:247], v[112:115], 0\n"
"v_exp_f32 v136, v136\n"
"v_mfma_f32_16x16x32_bf16 v[252:255], v[252:255], v[112:115], 0\n"
"ds_read_b64_tr_b16 v[112:113], v182 offset:27136\n"
"ds_read_b64_tr_b16 v[114:115], v183 offset:27136\n"
"v_mfma_f32_16x16x32_bf16 v[204:207], a[140:143], v[52:55], v[204:207]\n"
"v_exp_f32 v137, v137\n"
"v_mfma_f32_16x16x32_bf16 v[208:211], a[144:147], v[32:35], 0\n"
"buffer_store_dwordx2 v[68:69], v193, s[28:31], s41 offen offset:1024 sc1\n"
"v_mfma_f32_16x16x32_bf16 v[208:211], a[156:159], v[36:39], v[208:211]\n"
"s_add_u32 s41, s37, 49152\n"
"v_exp_f32 v138, v138\n"
"v_mfma_f32_16x16x32_bf16 v[208:211], a[168:171], v[40:43], v[208:211]\n"
"v_exp_f32 v139, v139\n"
"v_mfma_f32_16x16x32_bf16 v[208:211], a[180:183], v[44:47], v[208:211]\n"
"buffer_store_dwordx4 v[68:71], v192, s[28:31], s41 offen sc1\n"
"v_mfma_f32_16x16x32_bf16 v[208:211], a[192:195], v[48:51], v[208:211]\n"
"v_cvt_pk_bf16_f32 v84, v136, v137\n"
"v_cvt_pk_bf16_f32 v85, v138, v139\n"
"v_mfma_f32_16x16x32_bf16 v[208:211], a[204:207], v[52:55], v[208:211]\n"
"v_and_b32 v136, v84, v176\n"
"v_and_b32 v137, v85, v177\n"
"v_xor_b32 v138, v84, v136\n"
"v_xor_b32 v139, v85, v137\n"
"v_mfma_f32_16x16x32_bf16 v[82:85], v[172:175], v[82:85], 0\n"
"buffer_store_dwordx2 v[68:69], v193, s[28:31], s41 offen offset:1024 sc1\n"
"v_mfma_f32_16x16x32_bf16 v[248:251], v[116:119], v[136:139], v[248:251]\n"
"ds_read_b64_tr_b16 v[116:117], v182 offset:27080\n"
"ds_read_b64_tr_b16 v[118:119], v183 offset:27080\n"
"v_mfma_f32_16x16x32_bf16 v[244:247], v[124:127], v[136:139], v[244:247]\n"
"v_fmamk_f32 v120, v120, 0x3e553b95, v170\n"
"v_fmamk_f32 v121, v121, 0x3e553b95, v170\n"
"v_mfma_f32_16x16x32_bf16 v[252:255], v[132:135], v[136:139], v[252:255]\n"
"v_fmamk_f32 v122, v122, 0x3e553b95, v170\n"
"v_fmamk_f32 v123, v123, 0x3e553b95, v170\n"
"v_exp_f32 v120, v120\n"
"s_add_u32 m0, s34, 3072\n"
"v_exp_f32 v121, v121\n"
"buffer_load_dwordx4 v191, s[8:11], s32 offen lds\n"
"s_add_u32 s32, s32, s33\n"
"v_exp_f32 v122, v122\n"
"v_exp_f32 v123, v123\n"
"v_cvt_pk_bf16_f32 v90, v120, v121\n"
"v_cvt_pk_bf16_f32 v91, v122, v123\n"
"v_and_b32 v120, v90, v176\n"
"v_and_b32 v121, v91, v177\n"
"v_xor_b32 v122, v90, v120\n"
"v_xor_b32 v123, v91, v121\n"
"v_fmamk_f32 v128, v128, 0x3e553b95, v170\n"
"v_fmamk_f32 v129, v129, 0x3e553b95, v170\n"
"v_mfma_f32_16x16x32_bf16 v[244:247], v[98:101], v[120:123], v[244:247]\n"
"s_waitcnt lgkmcnt(7)\n"
"ds_read_b64_tr_b16 v[136:137], v182 offset:27144\n"
"ds_read_b64_tr_b16 v[138:139], v183 offset:27144\n"
"v_mfma_f32_16x16x32_bf16 v[248:251], v[140:143], v[120:123], v[248:251]\n"
"v_fmamk_f32 v130, v130, 0x3e553b95, v170\n"
"v_fmamk_f32 v131, v131, 0x3e553b95, v170\n"
"v_mfma_f32_16x16x32_bf16 v[252:255], v[106:109], v[120:123], v[252:255]\n"
"v_exp_f32 v128, v128\n"
"v_exp_f32 v129, v129\n"
"v_exp_f32 v130, v130\n"
"v_exp_f32 v131, v131\n"
"v_cvt_pk_bf16_f32 v92, v128, v129\n"
"v_cvt_pk_bf16_f32 v93, v130, v131\n"
"v_and_b32 v128, v92, v176\n"
"v_and_b32 v129, v93, v177\n"
"v_xor_b32 v130, v92, v128\n"
"v_xor_b32 v131, v93, v129\n"
"v_mfma_f32_16x16x32_bf16 v[90:93], v[172:175], v[90:93], v[82:85]\n"
"s_waitcnt lgkmcnt(2)\n"
"ds_read_b128 v[124:127], v178\n"
"ds_read_b128 v[132:135], v179\n"
"v_mfma_f32_16x16x32_bf16 v[248:251], v[112:115], v[128:131], v[248:251]\n"
"ds_read_b64 v[82:83], v178 offset:64\n"
"s_mov_b64 exec, s[46:47]\n"
"v_add_u32 v185, 0xc0, v185\n"
"s_mov_b64 exec, -1\n"
"v_mfma_f32_16x16x32_bf16 v[252:255], v[116:119], v[128:131], v[252:255]\n"
"s_waitcnt lgkmcnt(3)\n"
"ds_read_b64 v[84:85], v179 offset:64\n"
"ds_read_b128 v[120:123], v178 offset:13504\n"
"v_mfma_f32_16x16x32_bf16 v[244:247], v[136:139], v[128:131], v[244:247]\n"
"v_rcp_f32 v171, v90\n"
"v_cvt_pk_bf16_f32 v212, v212, v213\n"
"v_mul_f32 v248, v171, v248\n"
"v_mul_f32 v249, v171, v249\n"
"v_mul_f32 v250, v171, v250\n"
"s_add_u32 m0, s34, 1024\n"
"v_mul_f32 v251, v171, v251\n"
"v_cvt_pk_bf16_f32 v248, v248, v249\n"
"v_cvt_pk_bf16_f32 v249, v250, v251\n"
"v_mul_f32 v244, v171, v244\n"
"v_mul_f32 v245, v171, v245\n"
"v_mul_f32 v246, v171, v246\n"
"v_mul_f32 v247, v171, v247\n"
"v_cvt_pk_bf16_f32 v250, v244, v245\n"
"v_cvt_pk_bf16_f32 v251, v246, v247\n"
"v_mul_f32 v252, v171, v252\n"
"v_mul_f32 v253, v171, v253\n"
"v_mul_f32 v254, v171, v254\n"
"v_mul_f32 v255, v171, v255\n"
"v_cvt_pk_bf16_f32 v252, v252, v253\n"
"v_cvt_pk_bf16_f32 v253, v254, v255\n"
"v_cvt_pk_bf16_f32 v213, v214, v215\n"
"v_cvt_pk_bf16_f32 v214, v216, v217\n"
"v_cvt_pk_bf16_f32 v215, v218, v219\n"
"v_mfma_f32_16x16x32_bf16 v[216:219], a[148:151], v[32:35], 0\n"
"s_waitcnt lgkmcnt(3)\n"
"buffer_load_dwordx4 v190, s[8:11], s0 offen lds\n"
"ds_read_b128 v[140:143], v179 offset:13504\n"
"v_mfma_f32_16x16x32_bf16 v[124:127], v[124:127], v[144:147], 0\n"
"s_waitcnt lgkmcnt(1)\n"
"ds_read_b64 v[90:91], v178 offset:13568\n"
"ds_read_b64 v[92:93], v179 offset:13568\n"
"v_mfma_f32_16x16x32_bf16 v[132:135], v[132:135], v[144:147], 0\n"
"s_add_u32 m0, s34, 4096\n"
"ds_read_b128 v[128:131], v178 offset:27008\n"
"v_cvt_pk_bf16_f32 v220, v220, v221\n"
"v_mfma_f32_16x16x32_bf16 v[124:127], v[80:83], v[156:159], v[124:127]\n"
"buffer_load_dwordx4 v191, s[8:11], s0 offen lds\n"
"ds_read_b128 v[112:115], v179 offset:27008\n"
"v_mfma_f32_16x16x32_bf16 v[132:135], v[84:87], v[158:161], v[132:135]\n"
"s_add_u32 s0, s0, s33\n"
"ds_read_b64 v[98:99], v178 offset:27072\n"
"v_cvt_pk_bf16_f32 v221, v222, v223\n"
"v_mfma_f32_16x16x32_bf16 v[120:123], v[120:123], v[144:147], 0\n"
"ds_read_b64 v[100:101], v179 offset:27072\n"
"v_cvt_pk_bf16_f32 v224, v224, v225\n"
"v_mfma_f32_16x16x32_bf16 v[216:219], a[160:163], v[36:39], v[216:219]\n"
"s_add_u32 m0, s34, 2048\n"
"ds_read_b64_tr_b16 v[244:245], v182 offset:128\n"
"v_max_f32 v124, v124, v132\n"
"v_mfma_f32_16x16x32_bf16 v[216:219], a[172:175], v[40:43], v[216:219]\n"
"buffer_load_dwordx4 v190, s[8:11], s1 offen lds\n"
"ds_read_b64_tr_b16 v[246:247], v183 offset:128\n"
"v_mfma_f32_16x16x32_bf16 v[216:219], a[184:187], v[44:47], v[216:219]\n"
"s_waitcnt lgkmcnt(8)\n"
"ds_read_b64_tr_b16 v[254:255], v183 offset:72\n"
"v_max_f32 v125, v125, v133\n"
"v_mfma_f32_16x16x32_bf16 v[140:143], v[140:143], v[144:147], 0\n"
"s_waitcnt lgkmcnt(8)\n"
"ds_read_b64_tr_b16 v[132:133], v182 offset:13632\n"
"v_max_f32 v126, v126, v134\n"
"v_mfma_f32_16x16x32_bf16 v[120:123], v[88:91], v[156:159], v[120:123]\n"
"s_waitcnt lgkmcnt(8)\n"
"v_max_f32 v127, v127, v135\n"
"v_max_f32 v170, v124, v125\n"
"v_mfma_f32_16x16x32_bf16 v[140:143], v[92:95], v[158:161], v[140:143]\n"
"s_waitcnt lgkmcnt(7)\n"
"buffer_load_dwordx2 v[60:61], v193, s[28:31], s38 offen offset:1024 sc1\n"
"ds_read_b64_tr_b16 v[134:135], v183 offset:13632\n"
"v_mfma_f32_16x16x32_bf16 v[128:131], v[128:131], v[144:147], 0\n"
"s_waitcnt lgkmcnt(7)\n"
"v_max_f32 v171, v126, v127\n"
"v_cvt_pk_bf16_f32 v225, v226, v227\n"
"v_mfma_f32_16x16x32_bf16 v[112:115], v[112:115], v[144:147], 0\n"
"s_waitcnt lgkmcnt(6)\n"
"v_max_f32 v120, v120, v140\n"
"v_max_f32 v121, v121, v141\n"
"v_mfma_f32_16x16x32_bf16 v[128:131], v[96:99], v[156:159], v[128:131]\n"
"s_waitcnt lgkmcnt(5)\n"
"ds_read_b64_tr_b16 v[140:141], v182 offset:13640\n"
"ds_read_b64_tr_b16 v[98:99], v182 offset:27144\n"
"v_mfma_f32_16x16x32_bf16 v[112:115], v[100:103], v[158:161], v[112:115]\n"
"s_add_u32 s38, s38, 6144\n"
"buffer_store_dwordx4 v[248:251], v192, s[28:31], s37 offen sc1\n"
"v_mfma_f32_16x16x32_bf16 v[216:219], a[196:199], v[48:51], v[216:219]\n"
"s_nop 0\n"
"ds_read_b64_tr_b16 v[248:249], v182 offset:136\n"
"ds_read_b64_tr_b16 v[250:251], v183 offset:136\n"
"v_mfma_f32_16x16x32_bf16 v[216:219], a[208:211], v[52:55], v[216:219]\n"
"v_max_f32 v122, v122, v142\n"
"v_max_f32 v123, v123, v143\n"
"v_max3_f32 v170, v170, v120, v121\n"
"v_max3_f32 v171, v171, v122, v123\n"
"v_max_f32 v128, v128, v112\n"
"buffer_store_dwordx2 v[252:253], v193, s[28:31], s37 offen offset:1024 sc1\n"
"s_add_u32 s37, s37, 6144\n"
"ds_read_b64_tr_b16 v[252:253], v182 offset:72\n"
"v_max_f32 v129, v129, v113\n"
"v_max_f32 v130, v130, v114\n"
"v_max_f32 v131, v131, v115\n"
"v_max3_f32 v170, v170, v128, v129\n"
"v_max3_f32 v171, v171, v130, v131\n"
"v_cvt_pk_bf16_f32 v226, v228, v229\n"
"v_cvt_pk_bf16_f32 v227, v230, v231\n"
"v_cvt_pk_bf16_f32 v222, v204, v205\n"
"v_cvt_pk_bf16_f32 v223, v206, v207\n"
"ds_read_b64_tr_b16 v[142:143], v183 offset:13640\n"
"s_waitcnt vmcnt(19)\n"
"v_and_b32 v201, 0xffffff00, v201\n"
"v_or_b32 v198, 2, v201\n"
"v_add_u32 v198, 2, v198\n"
"buffer_store_dword v198, v196, s[28:31], s40 offen sc1\n"
"v_add_u32 v198, 2, v198\n"
"ds_read_b64_tr_b16 v[112:113], v182 offset:13576\n"
"ds_read_b64_tr_b16 v[114:115], v183 offset:13576\n"
"ds_read_b64_tr_b16 v[100:101], v183 offset:27144\n"
"s_add_u32 m0, s34, 5120\n"
"s_nop 0\n"
"buffer_load_dwordx4 v191, s[8:11], s1 offen lds\n"
"s_add_u32 s1, s1, s33\n"
"buffer_load_dwordx4 v[62:65], v192, s[28:31], s38 offen sc1\n"
"buffer_load_dwordx2 v[66:67], v193, s[28:31], s38 offen offset:1024 sc1\n"
"s_add_u32 s38, s38, 6144\n"
"s_waitcnt vmcnt(17)\n"
"s_barrier\n"
"s_add_u32 m0, s79, 142464\n"
"ds_write_b128 v186, v[212:215] offset:60768\n"
"v_mfma_f32_16x16x32_bf16 v[212:215], a[152:155], v[32:35], 0\n"
"buffer_load_dwordx4 v192, s[16:19], s43 offen lds\n"
"v_mfma_f32_16x16x32_bf16 v[212:215], a[164:167], v[36:39], v[212:215]\n"
"ds_read_b128 v[32:35], v188 offset:6144\n"
"v_mfma_f32_16x16x32_bf16 v[212:215], a[176:179], v[40:43], v[212:215]\n"
"s_add_u32 m0, s79, 142464\n"
"ds_read_b128 v[36:39], v189 offset:6144\n"
"ds_read_b128 v[40:43], v188 offset:7168\n"
"v_mfma_f32_16x16x32_bf16 v[212:215], a[188:191], v[44:47], v[212:215]\n"
"buffer_load_dwordx4 v192, s[16:19], s43 offen offset:1024 lds\n"
"ds_read_b128 v[44:47], v189 offset:7168\n"
"v_mfma_f32_16x16x32_bf16 v[212:215], a[200:203], v[48:51], v[212:215]\n"
"ds_read_b128 v[48:51], v188 offset:8192\n"
"v_mfma_f32_16x16x32_bf16 v[212:215], a[212:215], v[52:55], v[212:215]\n"
"ds_read_b128 v[52:55], v189 offset:8192\n"
"ds_write_b128 v202, v[56:59] offset:55296\n"
"ds_read_b128 v[136:139], v180\n"
"ds_read_b128 v[116:119], v181\n"
"s_waitcnt lgkmcnt(8)\n"
"v_mfma_f32_16x16x32_bf16 v[228:231], a[0:3], v[32:35], 0\n"
"s_waitcnt lgkmcnt(7)\n"
"ds_read_b64 v[106:107], v180 offset:64\n"
"v_mfma_f32_16x16x32_bf16 v[204:207], a[4:7], v[32:35], 0\n"
"ds_read_b64 v[108:109], v181 offset:64\n"
"v_mfma_f32_16x16x32_bf16 v[228:231], a[12:15], v[36:39], v[228:231]\n"
"s_waitcnt lgkmcnt(8)\n"
"ds_read_b64_tr_b16 v[8:9], v236\n"
"v_mfma_f32_16x16x32_bf16 v[204:207], a[16:19], v[36:39], v[204:207]\n"
"v_mfma_f32_16x16x32_bf16 v[228:231], a[24:27], v[40:43], v[228:231]\n"
"s_waitcnt lgkmcnt(8)\n"
"ds_read_b64_tr_b16 v[10:11], v236 offset:6144\n"
"v_mfma_f32_16x16x32_bf16 v[204:207], a[28:31], v[40:43], v[204:207]\n"
"v_mfma_f32_16x16x32_bf16 v[228:231], a[36:39], v[44:47], v[228:231]\n"
"s_waitcnt lgkmcnt(8)\n"
"ds_write_b128 v186, v[220:223] offset:60832\n"
"v_mfma_f32_16x16x32_bf16 v[220:223], a[8:11], v[32:35], 0\n"
"s_waitcnt lgkmcnt(5)\n"
"v_mfma_f32_16x16x32_bf16 v[136:139], v[136:139], v[144:147], 0\n"
"s_waitcnt lgkmcnt(4)\n"
"ds_write_b128 v186, v[224:227] offset:60896\n"
"v_mfma_f32_16x16x32_bf16 v[116:119], v[116:119], v[144:147], 0\n"
"s_waitcnt lgkmcnt(4)\n"
"v_cvt_pk_bf16_f32 v144, v208, v209\n"
"v_cvt_pk_bf16_f32 v145, v210, v211\n"
"v_mfma_f32_16x16x32_bf16 v[136:139], v[104:107], v[156:159], v[136:139]\n"
"v_cvt_pk_bf16_f32 v146, v216, v217\n"
"v_cvt_pk_bf16_f32 v147, v218, v219\n"
"v_mfma_f32_16x16x32_bf16 v[116:119], v[108:111], v[158:161], v[116:119]\n"
"ds_read_b64_tr_b16 v[106:107], v182 offset:27080\n"
"ds_read_b64_tr_b16 v[108:109], v183 offset:27080\n"
"v_mfma_f32_16x16x32_bf16 v[220:223], a[20:23], v[36:39], v[220:223]\n"
"ds_read_b64_tr_b16 v[12:13], v236 offset:8\n"
"v_cvt_pk_bf16_f32 v158, v212, v213\n"
"v_mfma_f32_16x16x32_bf16 v[220:223], a[32:35], v[40:43], v[220:223]\n"
"v_cvt_pk_bf16_f32 v159, v214, v215\n"
"s_add_u32 m0, s79, 142464\n"
"v_mfma_f32_16x16x32_bf16 v[204:207], a[40:43], v[44:47], v[204:207]\n"
"buffer_load_dwordx4 v192, s[16:19], s43 offen offset:2048 lds\n"
"v_max_f32 v136, v136, v116\n"
"v_mfma_f32_16x16x32_bf16 v[220:223], a[44:47], v[44:47], v[220:223]\n"
"v_max_f32 v137, v137, v117\n"
"v_max_f32 v138, v138, v118\n"
"v_mfma_f32_16x16x32_bf16 v[228:231], a[48:51], v[48:51], v[228:231]\n"
"ds_read_b64_tr_b16 v[116:117], v182 offset:27136\n"
"v_max_f32 v139, v139, v119\n"
"v_mfma_f32_16x16x32_bf16 v[204:207], a[52:55], v[48:51], v[204:207]\n"
"ds_read_b64_tr_b16 v[118:119], v183 offset:27136\n"
"v_max3_f32 v170, v170, v136, v137\n"
"v_mfma_f32_16x16x32_bf16 v[220:223], a[56:59], v[48:51], v[220:223]\n"
"v_max3_f32 v171, v171, v138, v139\n"
"v_max_f32 v170, v170, v171\n"
"v_mfma_f32_16x16x32_bf16 v[228:231], a[60:63], v[52:55], v[228:231]\n"
"ds_read_b64_tr_b16 v[14:15], v236 offset:6152\n"
"v_mov_b32 v171, v170\n"
"v_mfma_f32_16x16x32_bf16 v[204:207], a[64:67], v[52:55], v[204:207]\n"
"ds_read_b64_tr_b16 v[16:17], v235\n"
"v_permlane16_swap_b32 v170, v171\n"
"v_mfma_f32_16x16x32_bf16 v[220:223], a[68:71], v[52:55], v[220:223]\n"
"v_max_f32 v170, v170, v171\n"
"v_mov_b32 v171, v170\n"
"v_mfma_f32_16x16x32_bf16 v[224:227], a[72:75], v[32:35], 0\n"
"ds_read_b64_tr_b16 v[18:19], v235 offset:6144\n"
"v_permlane32_swap_b32 v170, v171\n"
"v_mfma_f32_16x16x32_bf16 v[208:211], a[76:79], v[32:35], 0\n"
"v_max_f32 v170, v170, v171\n"
"v_mul_f32 v170, 0xbe553b95, v170\n"
"v_mfma_f32_16x16x32_bf16 v[216:219], a[80:83], v[32:35], 0\n"
"v_fmamk_f32 v124, v124, 0x3e553b95, v170\n"
"v_fmamk_f32 v125, v125, 0x3e553b95, v170\n"
"v_mfma_f32_16x16x32_bf16 v[224:227], a[84:87], v[36:39], v[224:227]\n"
"v_fmamk_f32 v126, v126, 0x3e553b95, v170\n"
"v_fmamk_f32 v127, v127, 0x3e553b95, v170\n"
"v_mfma_f32_16x16x32_bf16 v[208:211], a[88:91], v[36:39], v[208:211]\n"
"v_exp_f32 v124, v124\n"
"v_mfma_f32_16x16x32_bf16 v[216:219], a[92:95], v[36:39], v[216:219]\n"
"v_exp_f32 v125, v125\n"
"v_mfma_f32_16x16x32_bf16 v[224:227], a[96:99], v[40:43], v[224:227]\n"
"s_waitcnt vmcnt(9)\n"
"v_exp_f32 v126, v126\n"
"v_mfma_f32_16x16x32_bf16 v[208:211], a[100:103], v[40:43], v[208:211]\n"
"ds_write_b64 v203, v[60:61] offset:55296\n"
"v_mfma_f32_16x16x32_bf16 v[216:219], a[104:107], v[40:43], v[216:219]\n"
"s_add_u32 m0, s34, 6144\n"
"v_exp_f32 v127, v127\n"
"v_mfma_f32_16x16x32_bf16 v[224:227], a[108:111], v[44:47], v[224:227]\n"
"buffer_load_dwordx4 v190, s[8:11], s32 offen lds\n"
"v_cvt_pk_bf16_f32 v82, v124, v125\n"
"v_mfma_f32_16x16x32_bf16 v[208:211], a[112:115], v[44:47], v[208:211]\n"
"v_cvt_pk_bf16_f32 v83, v126, v127\n"
"v_and_b32 v124, v82, v176\n"
"v_mfma_f32_16x16x32_bf16 v[216:219], a[116:119], v[44:47], v[216:219]\n"
"v_and_b32 v125, v83, v177\n"
"v_xor_b32 v126, v82, v124\n"
"v_mfma_f32_16x16x32_bf16 v[224:227], a[120:123], v[48:51], v[224:227]\n"
"s_add_u32 m0, s34, 9216\n"
"v_xor_b32 v127, v83, v125\n"
"v_fmamk_f32 v120, v120, 0x3e553b95, v170\n"
"v_mfma_f32_16x16x32_bf16 v[208:211], a[124:127], v[48:51], v[208:211]\n"
"buffer_load_dwordx4 v191, s[8:11], s32 offen lds\n"
"v_fmamk_f32 v121, v121, 0x3e553b95, v170\n"
"v_mfma_f32_16x16x32_bf16 v[244:247], v[244:247], v[124:127], 0\n"
"s_add_u32 s32, s32, s33\n"
"v_fmamk_f32 v122, v122, 0x3e553b95, v170\n"
"v_fmamk_f32 v123, v123, 0x3e553b95, v170\n"
"v_mfma_f32_16x16x32_bf16 v[248:251], v[248:251], v[124:127], 0\n"
"v_exp_f32 v120, v120\n"
"v_mfma_f32_16x16x32_bf16 v[252:255], v[252:255], v[124:127], 0\n"
"s_add_u32 m0, s34, 7168\n"
"ds_read_b64_tr_b16 v[124:125], v184 offset:128\n"
"buffer_load_dwordx4 v190, s[8:11], s0 offen lds\n"
"v_mfma_f32_16x16x32_bf16 v[216:219], a[128:131], v[48:51], v[216:219]\n"
"ds_read_b64_tr_b16 v[126:127], v185 offset:128\n"
"v_fmamk_f32 v128, v128, 0x3e553b95, v170\n"
"v_mfma_f32_16x16x32_bf16 v[224:227], a[132:135], v[52:55], v[224:227]\n"
"v_exp_f32 v121, v121\n"
"v_mfma_f32_16x16x32_bf16 v[208:211], a[136:139], v[52:55], v[208:211]\n"
"v_exp_f32 v122, v122\n"
"v_mfma_f32_16x16x32_bf16 v[216:219], a[140:143], v[52:55], v[216:219]\n"
"s_add_u32 m0, s34, 10240\n"
"v_exp_f32 v123, v123\n"
"v_mfma_f32_16x16x32_bf16 v[212:215], a[144:147], v[32:35], 0\n"
"buffer_load_dwordx4 v191, s[8:11], s0 offen lds\n"
"v_cvt_pk_bf16_f32 v84, v120, v121\n"
"v_mfma_f32_16x16x32_bf16 v[212:215], a[156:159], v[36:39], v[212:215]\n"
"s_add_u32 s0, s0, s33\n"
"v_cvt_pk_bf16_f32 v85, v122, v123\n"
"v_and_b32 v120, v84, v176\n"
"v_mfma_f32_16x16x32_bf16 v[212:215], a[168:171], v[40:43], v[212:215]\n"
"v_and_b32 v121, v85, v177\n"
"v_xor_b32 v122, v84, v120\n"
"v_mfma_f32_16x16x32_bf16 v[212:215], a[180:183], v[44:47], v[212:215]\n"
"v_xor_b32 v123, v85, v121\n"
"v_fmamk_f32 v129, v129, 0x3e553b95, v170\n"
"v_mfma_f32_16x16x32_bf16 v[82:85], v[172:175], v[82:85], 0\n"
"v_fmamk_f32 v130, v130, 0x3e553b95, v170\n"
"v_fmamk_f32 v131, v131, 0x3e553b95, v170\n"
"v_mfma_f32_16x16x32_bf16 v[244:247], v[132:135], v[120:123], v[244:247]\n"
"ds_read_b64_tr_b16 v[132:133], v184 offset:72\n"
"ds_read_b64_tr_b16 v[134:135], v185 offset:72\n"
"v_mfma_f32_16x16x32_bf16 v[248:251], v[140:143], v[120:123], v[248:251]\n"
"v_exp_f32 v128, v128\n"
"v_mfma_f32_16x16x32_bf16 v[252:255], v[112:115], v[120:123], v[252:255]\n"
"ds_read_b64_tr_b16 v[120:121], v184 offset:136\n"
"ds_read_b64_tr_b16 v[122:123], v185 offset:136\n"
"v_mfma_f32_16x16x32_bf16 v[212:215], a[192:195], v[48:51], v[212:215]\n"
"v_exp_f32 v129, v129\n"
"v_mfma_f32_16x16x32_bf16 v[212:215], a[204:207], v[52:55], v[212:215]\n"
"v_exp_f32 v130, v130\n"
"v_exp_f32 v131, v131\n"
"v_cvt_pk_bf16_f32 v90, v128, v129\n"
"v_cvt_pk_bf16_f32 v91, v130, v131\n"
"v_and_b32 v128, v90, v176\n"
"v_and_b32 v129, v91, v177\n"
"v_xor_b32 v130, v90, v128\n"
"v_xor_b32 v131, v91, v129\n"
"v_fmamk_f32 v136, v136, 0x3e553b95, v170\n"
"v_fmamk_f32 v137, v137, 0x3e553b95, v170\n"
"v_mfma_f32_16x16x32_bf16 v[248:251], v[98:101], v[128:131], v[248:251]\n"
"s_waitcnt lgkmcnt(10)\n"
"v_fmamk_f32 v138, v138, 0x3e553b95, v170\n"
"v_fmamk_f32 v139, v139, 0x3e553b95, v170\n"
"v_mfma_f32_16x16x32_bf16 v[244:247], v[116:119], v[128:131], v[244:247]\n"
"v_exp_f32 v136, v136\n"
"v_mfma_f32_16x16x32_bf16 v[252:255], v[106:109], v[128:131], v[252:255]\n"
"v_exp_f32 v137, v137\n"
"v_exp_f32 v138, v138\n"
"v_exp_f32 v139, v139\n"
"v_cvt_pk_bf16_f32 v92, v136, v137\n"
"v_cvt_pk_bf16_f32 v93, v138, v139\n"
"v_and_b32 v136, v92, v176\n"
"v_and_b32 v137, v93, v177\n"
"v_xor_b32 v138, v92, v136\n"
"v_xor_b32 v139, v93, v137\n"
"v_mfma_f32_16x16x32_bf16 v[90:93], v[172:175], v[90:93], v[82:85]\n"
"s_waitcnt lgkmcnt(0)\n"
"ds_read_b128 v[140:143], v178 offset:13504\n"
"ds_read_b128 v[112:115], v179 offset:13504\n"
"v_mfma_f32_16x16x32_bf16 v[244:247], v[124:127], v[136:139], v[244:247]\n"
"ds_read_b64 v[82:83], v178 offset:13568\n"
"v_cvt_pk_bf16_f32 v228, v228, v229\n"
"v_mfma_f32_16x16x32_bf16 v[248:251], v[120:123], v[136:139], v[248:251]\n"
"s_add_u32 m0, s34, 8192\n"
"ds_read_b64 v[84:85], v179 offset:13568\n"
"buffer_load_dwordx4 v190, s[8:11], s1 offen lds\n"
"v_mfma_f32_16x16x32_bf16 v[252:255], v[132:135], v[136:139], v[252:255]\n"
"v_rcp_f32 v171, v90\n"
"v_cvt_pk_bf16_f32 v229, v230, v231\n"
"v_mul_f32 v244, v171, v244\n"
"v_mul_f32 v245, v171, v245\n"
"v_mul_f32 v246, v171, v246\n"
"v_mul_f32 v247, v171, v247\n"
"v_cvt_pk_bf16_f32 v244, v244, v245\n"
"v_cvt_pk_bf16_f32 v245, v246, v247\n"
"s_waitcnt vmcnt(12)\n"
"v_mul_f32 v248, v171, v248\n"
"v_mul_f32 v249, v171, v249\n"
"v_mul_f32 v250, v171, v250\n"
"v_mul_f32 v251, v171, v251\n"
"v_cvt_pk_bf16_f32 v246, v248, v249\n"
"v_cvt_pk_bf16_f32 v247, v250, v251\n"
"v_mul_f32 v252, v171, v252\n"
"v_mul_f32 v253, v171, v253\n"
"v_mul_f32 v254, v171, v254\n"
"v_mul_f32 v255, v171, v255\n"
"v_cvt_pk_bf16_f32 v252, v252, v253\n"
"v_cvt_pk_bf16_f32 v253, v254, v255\n"
"v_cvt_pk_bf16_f32 v230, v204, v205\n"
"v_cvt_pk_bf16_f32 v231, v206, v207\n"
"v_mfma_f32_16x16x32_bf16 v[204:207], a[148:151], v[32:35], 0\n"
"s_waitcnt lgkmcnt(2)\n"
"buffer_store_dword v198, v196, s[28:31], s40 offen sc1\n"
"v_mfma_f32_16x16x32_bf16 v[140:143], v[140:143], v[148:151], 0\n"
"s_waitcnt lgkmcnt(1)\n"
"v_add_u32 v198, 2, v198\n"
"v_cvt_pk_bf16_f32 v220, v220, v221\n"
"v_mfma_f32_16x16x32_bf16 v[112:115], v[112:115], v[148:151], 0\n"
"s_waitcnt lgkmcnt(0)\n"
"buffer_store_dwordx4 v[244:247], v192, s[28:31], s37 offen sc1\n"
"v_mfma_f32_16x16x32_bf16 v[140:143], v[80:83], v[160:163], v[140:143]\n"
"ds_read_b128 v[128:131], v178 offset:27008\n"
"ds_read_b128 v[116:119], v179 offset:27008\n"
"v_mfma_f32_16x16x32_bf16 v[112:115], v[84:87], v[162:165], v[112:115]\n"
"ds_read_b64 v[90:91], v178 offset:27072\n"
"v_cvt_pk_bf16_f32 v221, v222, v223\n"
"v_mfma_f32_16x16x32_bf16 v[204:207], a[160:163], v[36:39], v[204:207]\n"
"buffer_store_dwordx2 v[252:253], v193, s[28:31], s37 offen offset:1024 sc1\n"
"v_mfma_f32_16x16x32_bf16 v[204:207], a[172:175], v[40:43], v[204:207]\n"
"s_add_u32 s37, s37, 6144\n"
"ds_read_b64 v[92:93], v179 offset:27072\n"
"ds_read_b128 v[136:139], v180\n"
"v_mfma_f32_16x16x32_bf16 v[204:207], a[184:187], v[44:47], v[204:207]\n"
"s_waitcnt vmcnt(8)\n"
"ds_read_b128 v[124:127], v181\n"
"v_max_f32 v140, v140, v112\n"
"v_mfma_f32_16x16x32_bf16 v[204:207], a[196:199], v[48:51], v[204:207]\n"
"s_add_u32 m0, s34, 11264\n"
"ds_read_b64 v[98:99], v180 offset:64\n"
"v_max_f32 v141, v141, v113\n"
"v_mfma_f32_16x16x32_bf16 v[204:207], a[208:211], v[52:55], v[204:207]\n"
"s_barrier\n"
"buffer_load_dwordx4 v191, s[8:11], s1 offen lds\n"
"ds_read_b128 v[68:71], v192 offset:55296\n"
"ds_read_b128 v[72:75], v192 offset:56320\n"
"v_max_f32 v142, v142, v114\n"
"v_max_f32 v143, v143, v115\n"
"v_max_f32 v170, v140, v141\n"
"v_max_f32 v171, v142, v143\n"
"v_cvt_pk_bf16_f32 v224, v224, v225\n"
"v_cvt_pk_bf16_f32 v225, v226, v227\n"
"v_cvt_pk_bf16_f32 v226, v208, v209\n"
"v_cvt_pk_bf16_f32 v227, v210, v211\n"
"v_cvt_pk_bf16_f32 v222, v216, v217\n"
"v_cvt_pk_bf16_f32 v223, v218, v219\n"
"s_waitcnt lgkmcnt(7)\n"
"v_mfma_f32_16x16x32_bf16 v[128:131], v[128:131], v[148:151], 0\n"
"s_waitcnt lgkmcnt(6)\n"
"ds_write_b128 v187, v[228:231]\n"
"v_mfma_f32_16x16x32_bf16 v[116:119], v[116:119], v[148:151], 0\n"
"s_waitcnt lgkmcnt(4)\n"
"s_add_u32 s1, s1, s33\n"
"v_mfma_f32_16x16x32_bf16 v[128:131], v[88:91], v[160:163], v[128:131]\n"
"ds_read_b64 v[100:101], v181 offset:64\n"
"v_mfma_f32_16x16x32_bf16 v[116:119], v[92:95], v[162:165], v[116:119]\n"
"s_waitcnt lgkmcnt(3)\n"
"ds_write_b128 v202, v[62:65] offset:30720\n"
"v_mfma_f32_16x16x32_bf16 v[136:139], v[136:139], v[148:151], 0\n"
"buffer_load_dword v199, off, s[28:31], s39 sc1\n"
"v_mfma_f32_16x16x32_bf16 v[124:127], v[124:127], v[148:151], 0\n"
"ds_read_b128 v[120:123], v180 offset:13504\n"
"v_mfma_f32_16x16x32_bf16 v[136:139], v[96:99], v[160:163], v[136:139]\n"
"ds_read_b128 v[132:135], v181 offset:13504\n"
"ds_read_b64 v[106:107], v180 offset:13568\n"
"v_mfma_f32_16x16x32_bf16 v[240:243], a[224:227], v[68:71], 0\n"
"s_waitcnt lgkmcnt(6)\n"
"ds_read_b64 v[108:109], v181 offset:13568\n"
"v_max_f32 v128, v128, v116\n"
"v_mfma_f32_16x16x32_bf16 v[228:231], a[152:155], v[32:35], 0\n"
"ds_read_b128 v[32:35], v188\n"
"v_max_f32 v129, v129, v117\n"
"v_mfma_f32_16x16x32_bf16 v[240:243], a[236:239], v[72:75], v[240:243]\n"
"s_waitcnt lgkmcnt(6)\n"
"ds_read_b64_tr_b16 v[20:21], v236 offset:12288\n"
"v_max_f32 v130, v130, v118\n"
"v_mfma_f32_16x16x32_bf16 v[124:127], v[100:103], v[162:165], v[124:127]\n"
"v_max_f32 v131, v131, v119\n"
"v_max3_f32 v170, v170, v128, v129\n"
"v_mfma_f32_16x16x32_bf16 v[228:231], a[164:167], v[36:39], v[228:231]\n"
"ds_read_b128 v[36:39], v189\n"
"v_max3_f32 v171, v171, v130, v131\n"
"v_mfma_f32_16x16x32_bf16 v[228:231], a[176:179], v[40:43], v[228:231]\n"
"ds_read_b128 v[40:43], v188 offset:1024\n"
"v_mfma_f32_16x16x32_bf16 v[228:231], a[188:191], v[44:47], v[228:231]\n"
"s_waitcnt lgkmcnt(7)\n"
"ds_read_b128 v[44:47], v189 offset:1024\n"
"v_max_f32 v136, v136, v124\n"
"v_mfma_f32_16x16x32_bf16 v[120:123], v[120:123], v[148:151], 0\n"
"s_waitcnt lgkmcnt(7)\n"
"ds_read_b64_tr_b16 v[248:249], v182 offset:13632\n"
"v_max_f32 v137, v137, v125\n"
"v_mfma_f32_16x16x32_bf16 v[132:135], v[132:135], v[148:151], 0\n"
"s_waitcnt lgkmcnt(7)\n"
"ds_read_b64_tr_b16 v[250:251], v183 offset:13632\n"
"v_max_f32 v138, v138, v126\n"
"v_mfma_f32_16x16x32_bf16 v[120:123], v[104:107], v[160:163], v[120:123]\n"
"s_waitcnt lgkmcnt(7)\n"
"ds_read_b64_tr_b16 v[22:23], v236 offset:18432\n"
"v_max_f32 v139, v139, v127\n"
"v_mfma_f32_16x16x32_bf16 v[132:135], v[108:111], v[162:165], v[132:135]\n"
"s_waitcnt lgkmcnt(7)\n"
"v_max3_f32 v170, v170, v136, v137\n"
".Lic4%=:\n"
"s_cbranch_execz .Lib1%=\n"
"v_max3_f32 v171, v171, v138, v139\n"
"v_mfma_f32_16x16x32_bf16 v[228:231], a[200:203], v[48:51], v[228:231]\n"
"ds_read_b128 v[48:51], v188 offset:2048\n"
"v_cvt_pk_bf16_f32 v148, v212, v213\n"
"v_mfma_f32_16x16x32_bf16 v[228:231], a[212:215], v[52:55], v[228:231]\n"
"ds_read_b128 v[52:55], v189 offset:2048\n"
"v_max_f32 v120, v120, v132\n"
"v_mfma_f32_16x16x32_bf16 v[208:211], a[0:3], v[32:35], 0\n"
"s_waitcnt lgkmcnt(7)\n"
"ds_read_b64_tr_b16 v[244:245], v182 offset:13640\n"
"v_max_f32 v121, v121, v133\n"
"v_mfma_f32_16x16x32_bf16 v[216:219], a[4:7], v[32:35], 0\n"
"s_add_u32 m0, s34, 0\n"
"ds_read_b64_tr_b16 v[246:247], v183 offset:13640\n"
"buffer_load_dwordx4 v190, s[8:11], s32 offen lds\n"
"v_mfma_f32_16x16x32_bf16 v[208:211], a[12:15], v[36:39], v[208:211]\n"
"s_waitcnt lgkmcnt(8)\n"
"ds_read_b64_tr_b16 v[252:253], v182 offset:13576\n"
"v_max_f32 v122, v122, v134\n"
"v_mfma_f32_16x16x32_bf16 v[216:219], a[16:19], v[36:39], v[216:219]\n"
"s_waitcnt lgkmcnt(8)\n"
"ds_read_b64_tr_b16 v[24:25], v236 offset:12296\n"
"v_max_f32 v123, v123, v135\n"
"v_mfma_f32_16x16x32_bf16 v[208:211], a[24:27], v[40:43], v[208:211]\n"
"s_add_u32 m0, s34, 3072\n"
"v_max3_f32 v170, v170, v120, v121\n"
"v_max3_f32 v171, v171, v122, v123\n"
"v_mfma_f32_16x16x32_bf16 v[216:219], a[28:31], v[40:43], v[216:219]\n"
"buffer_load_dwordx4 v191, s[8:11], s32 offen lds\n"
"ds_read_b64_tr_b16 v[254:255], v183 offset:13576\n"
"v_mfma_f32_16x16x32_bf16 v[208:211], a[36:39], v[44:47], v[208:211]\n"
"s_add_u32 s32, s32, s33\n"
"ds_read_b64_tr_b16 v[26:27], v236 offset:18440\n"
"v_max_f32 v170, v170, v171\n"
"v_mfma_f32_16x16x32_bf16 v[236:239], a[220:223], v[68:71], 0\n"
"s_waitcnt lgkmcnt(7)\n"
"v_mov_b32 v171, v170\n"
"v_cvt_pk_bf16_f32 v149, v214, v215\n"
"v_mfma_f32_16x16x32_bf16 v[236:239], a[232:235], v[72:75], v[236:239]\n"
"ds_read_b64_tr_b16 v[28:29], v235 offset:12288\n"
"v_permlane16_swap_b32 v170, v171\n"
"v_mfma_f32_16x16x32_bf16 v[216:219], a[40:43], v[44:47], v[216:219]\n"
"v_max_f32 v170, v170, v171\n"
"v_mov_b32 v171, v170\n"
"v_mfma_f32_16x16x32_bf16 v[208:211], a[48:51], v[48:51], v[208:211]\n"
"s_waitcnt lgkmcnt(7)\n"
"ds_read_b64_tr_b16 v[30:31], v235 offset:18432\n"
"v_permlane32_swap_b32 v170, v171\n"
"v_mfma_f32_16x16x32_bf16 v[232:235], a[216:219], v[68:71], 0\n"
"v_max_f32 v170, v170, v171\n"
"v_mul_f32 v170, 0xbe553b95, v170\n"
"v_mfma_f32_16x16x32_bf16 v[232:235], a[228:231], v[72:75], v[232:235]\n"
"s_add_u32 m0, s34, 1024\n"
"ds_read_b128 v[68:71], v192 offset:58368\n"
"buffer_load_dwordx4 v190, s[8:11], s0 offen lds\n"
"v_mfma_f32_16x16x32_bf16 v[216:219], a[52:55], v[48:51], v[216:219]\n"
"ds_read_b128 v[72:75], v192 offset:59392\n"
"ds_write_b64 v202, v[76:77] offset:36864\n"
"v_mfma_f32_16x16x32_bf16 v[208:211], a[60:63], v[52:55], v[208:211]\n"
"v_fmamk_f32 v140, v140, 0x3e553b95, v170\n"
"v_fmamk_f32 v141, v141, 0x3e553b95, v170\n"
"v_mfma_f32_16x16x32_bf16 v[216:219], a[64:67], v[52:55], v[216:219]\n"
"ds_read_b128 v[76:79], v192 offset:57344\n"
"ds_read_b64_tr_b16 v[112:113], v182 offset:27136\n"
"v_mfma_f32_16x16x32_bf16 v[212:215], a[76:79], v[32:35], 0\n"
"ds_read_b64_tr_b16 v[114:115], v183 offset:27136\n"
"v_fmamk_f32 v142, v142, 0x3e553b95, v170\n"
"v_mfma_f32_16x16x32_bf16 v[212:215], a[88:91], v[36:39], v[212:215]\n"
"ds_read_b64_tr_b16 v[116:117], v182 offset:27144\n"
"v_fmamk_f32 v143, v143, 0x3e553b95, v170\n"
"v_mfma_f32_16x16x32_bf16 v[212:215], a[100:103], v[40:43], v[212:215]\n"
"ds_read_b64_tr_b16 v[118:119], v183 offset:27144\n"
"v_fmamk_f32 v128, v128, 0x3e553b95, v170\n"
"v_mfma_f32_16x16x32_bf16 v[212:215], a[112:115], v[44:47], v[212:215]\n"
"ds_write_b64 v203, v[66:67] offset:30720\n"
"v_mfma_f32_16x16x32_bf16 v[212:215], a[124:127], v[48:51], v[212:215]\n"
"v_exp_f32 v140, v140\n"
"v_mfma_f32_16x16x32_bf16 v[212:215], a[136:139], v[52:55], v[212:215]\n"
"ds_read_b64_tr_b16 v[124:125], v182 offset:27080\n"
"ds_read_b64_tr_b16 v[126:127], v183 offset:27080\n"
"ds_read_b64_tr_b16 v[132:133], v184 offset:128\n"
"v_exp_f32 v141, v141\n"
"v_exp_f32 v142, v142\n"
"v_cvt_pk_bf16_f32 v82, v140, v141\n"
"v_exp_f32 v143, v143\n"
"v_and_b32 v140, v82, v176\n"
"v_cvt_pk_bf16_f32 v83, v142, v143\n"
"v_and_b32 v141, v83, v177\n"
"v_xor_b32 v142, v82, v140\n"
"v_xor_b32 v143, v83, v141\n"
"v_fmamk_f32 v129, v129, 0x3e553b95, v170\n"
"v_fmamk_f32 v130, v130, 0x3e553b95, v170\n"
"v_mfma_f32_16x16x32_bf16 v[248:251], v[248:251], v[140:143], 0\n"
"s_waitcnt lgkmcnt(8)\n"
"ds_write_b128 v187, v[220:223] offset:64\n"
"v_mfma_f32_16x16x32_bf16 v[232:235], a[240:243], v[76:79], v[232:235]\n"
"s_add_u32 m0, s34, 4096\n"
"v_fmamk_f32 v131, v131, 0x3e553b95, v170\n"
"v_fmamk_f32 v136, v136, 0x3e553b95, v170\n"
"v_mfma_f32_16x16x32_bf16 v[236:239], a[244:247], v[76:79], v[236:239]\n"
"buffer_load_dwordx4 v191, s[8:11], s0 offen lds\n"
"ds_read_b64_tr_b16 v[134:135], v185 offset:128\n"
"v_mfma_f32_16x16x32_bf16 v[240:243], a[248:251], v[76:79], v[240:243]\n"
"s_add_u32 s0, s0, s33\n"
"ds_read_b128 v[76:79], v192 offset:60416\n"
"ds_read_b64_tr_b16 v[98:99], v184 offset:136\n"
"v_mfma_f32_16x16x32_bf16 v[232:235], a[252:255], v[68:71], v[232:235]\n"
"ds_read_b64_tr_b16 v[100:101], v185 offset:136\n"
"v_fmamk_f32 v137, v137, 0x3e553b95, v170\n"
"v_mfma_f32_16x16x32_bf16 v[236:239], v[0:3], v[68:71], v[236:239]\n"
"ds_read_b64_tr_b16 v[106:107], v184 offset:72\n"
"v_fmamk_f32 v138, v138, 0x3e553b95, v170\n"
"v_mfma_f32_16x16x32_bf16 v[240:243], v[4:7], v[68:71], v[240:243]\n"
"ds_read_b64_tr_b16 v[108:109], v185 offset:72\n"
"v_fmamk_f32 v139, v139, 0x3e553b95, v170\n"
"v_mfma_f32_16x16x32_bf16 v[232:235], v[8:11], v[72:75], v[232:235]\n"
"ds_write_b128 v187, v[224:227] offset:128\n"
"v_mfma_f32_16x16x32_bf16 v[236:239], v[12:15], v[72:75], v[236:239]\n"
"v_exp_f32 v128, v128\n"
"v_mfma_f32_16x16x32_bf16 v[240:243], v[16:19], v[72:75], v[240:243]\n"
"v_exp_f32 v129, v129\n"
"v_mfma_f32_16x16x32_bf16 v[244:247], v[244:247], v[140:143], 0\n"
"s_waitcnt lgkmcnt(5)\n"
"v_exp_f32 v130, v130\n"
"v_mfma_f32_16x16x32_bf16 v[252:255], v[252:255], v[140:143], 0\n"
"v_exp_f32 v131, v131\n"
"v_mfma_f32_16x16x32_bf16 v[232:235], v[20:23], v[76:79], v[232:235]\n"
"s_add_u32 m0, s34, 2048\n"
"ds_read_b64_tr_b16 v[140:141], v184 offset:13632\n"
"buffer_load_dwordx4 v190, s[8:11], s1 offen lds\n"
"v_mfma_f32_16x16x32_bf16 v[236:239], v[24:27], v[76:79], v[236:239]\n"
"ds_read_b64_tr_b16 v[142:143], v185 offset:13632\n"
"v_cvt_pk_bf16_f32 v84, v128, v129\n"
"v_mfma_f32_16x16x32_bf16 v[240:243], v[28:31], v[76:79], v[240:243]\n"
"v_cvt_pk_bf16_f32 v85, v130, v131\n"
"v_and_b32 v128, v84, v176\n"
"v_mfma_f32_16x16x32_bf16 v[220:223], a[8:11], v[32:35], 0\n"
"v_and_b32 v129, v85, v177\n"
"v_xor_b32 v130, v84, v128\n"
"v_mfma_f32_16x16x32_bf16 v[220:223], a[20:23], v[36:39], v[220:223]\n"
"s_waitcnt lgkmcnt(2)\n"
"v_xor_b32 v131, v85, v129\n"
"v_fmamk_f32 v120, v120, 0x3e553b95, v170\n"
"v_mfma_f32_16x16x32_bf16 v[82:85], v[172:175], v[82:85], 0\n"
"s_add_u32 m0, s34, 5120\n"
"v_exp_f32 v136, v136\n"
"v_mfma_f32_16x16x32_bf16 v[248:251], v[112:115], v[128:131], v[248:251]\n"
"s_barrier\n"
"v_mfma_f32_16x16x32_bf16 v[244:247], v[116:119], v[128:131], v[244:247]\n"
"buffer_load_dwordx4 v191, s[8:11], s1 offen lds\n"
"ds_read_b128 v[68:71], v192 offset:30720\n"
"v_mfma_f32_16x16x32_bf16 v[252:255], v[124:127], v[128:131], v[252:255]\n"
"s_add_u32 s1, s1, s33\n"
"ds_read_b128 v[72:75], v192 offset:31744\n"
"ds_read_b128 v[76:79], v192 offset:32768\n"
"v_mfma_f32_16x16x32_bf16 v[220:223], a[32:35], v[40:43], v[220:223]\n"
"ds_read_b64_tr_b16 v[128:129], v184 offset:13640\n"
"v_fmamk_f32 v121, v121, 0x3e553b95, v170\n"
"v_mfma_f32_16x16x32_bf16 v[220:223], a[44:47], v[44:47], v[220:223]\n"
"ds_read_b64_tr_b16 v[130:131], v185 offset:13640\n"
"v_fmamk_f32 v122, v122, 0x3e553b95, v170\n"
"v_mfma_f32_16x16x32_bf16 v[220:223], a[56:59], v[48:51], v[220:223]\n"
"ds_read_b64_tr_b16 v[112:113], v184 offset:13576\n"
"v_fmamk_f32 v123, v123, 0x3e553b95, v170\n"
"v_mfma_f32_16x16x32_bf16 v[220:223], a[68:71], v[52:55], v[220:223]\n"
"ds_read_b64_tr_b16 v[114:115], v185 offset:13576\n"
"v_cvt_pk_bf16_f32 v150, v204, v205\n"
"v_mfma_f32_16x16x32_bf16 v[224:227], a[72:75], v[32:35], 0\n"
"v_exp_f32 v137, v137\n"
"v_mfma_f32_16x16x32_bf16 v[224:227], a[84:87], v[36:39], v[224:227]\n"
"v_exp_f32 v138, v138\n"
"v_mfma_f32_16x16x32_bf16 v[224:227], a[96:99], v[40:43], v[224:227]\n"
"v_exp_f32 v139, v139\n"
"v_mfma_f32_16x16x32_bf16 v[224:227], a[108:111], v[44:47], v[224:227]\n"
"v_cvt_pk_bf16_f32 v90, v136, v137\n"
"v_cvt_pk_bf16_f32 v91, v138, v139\n"
"v_mfma_f32_16x16x32_bf16 v[224:227], a[120:123], v[48:51], v[224:227]\n"
"v_and_b32 v136, v90, v176\n"
"v_and_b32 v137, v91, v177\n"
"v_mfma_f32_16x16x32_bf16 v[224:227], a[132:135], v[52:55], v[224:227]\n"
"v_xor_b32 v138, v90, v136\n"
"v_xor_b32 v139, v91, v137\n"
"v_exp_f32 v120, v120\n"
"s_nop 0\n"
"v_mfma_f32_16x16x32_bf16 v[248:251], v[132:135], v[136:139], v[248:251]\n"
"v_cvt_pk_bf16_f32 v151, v206, v207\n"
"v_cvt_pk_bf16_f32 v162, v228, v229\n"
"v_mfma_f32_16x16x32_bf16 v[244:247], v[98:101], v[136:139], v[244:247]\n"
"v_exp_f32 v121, v121\n"
"v_mfma_f32_16x16x32_bf16 v[252:255], v[106:109], v[136:139], v[252:255]\n"
"v_exp_f32 v122, v122\n"
"v_mfma_f32_16x16x32_bf16 v[204:207], a[80:83], v[32:35], 0\n"
"v_cvt_pk_bf16_f32 v163, v230, v231\n"
"v_cvt_pk_bf16_f32 v92, v120, v121\n"
"v_mfma_f32_16x16x32_bf16 v[204:207], a[92:95], v[36:39], v[204:207]\n"
"v_exp_f32 v123, v123\n"
"v_mfma_f32_16x16x32_bf16 v[204:207], a[104:107], v[40:43], v[204:207]\n"
"v_cvt_pk_bf16_f32 v93, v122, v123\n"
"v_and_b32 v120, v92, v176\n"
"v_mfma_f32_16x16x32_bf16 v[204:207], a[116:119], v[44:47], v[204:207]\n"
"v_and_b32 v121, v93, v177\n"
"v_xor_b32 v122, v92, v120\n"
"v_mfma_f32_16x16x32_bf16 v[204:207], a[128:131], v[48:51], v[204:207]\n"
"v_xor_b32 v123, v93, v121\n"
"v_cvt_pk_bf16_f32 v232, v232, v233\n"
"v_mfma_f32_16x16x32_bf16 v[90:93], v[172:175], v[90:93], v[82:85]\n"
"s_waitcnt lgkmcnt(0)\n"
"ds_read_b128 v[116:119], v178 offset:27008\n"
"ds_read_b128 v[124:127], v179 offset:27008\n"
"v_mfma_f32_16x16x32_bf16 v[248:251], v[140:143], v[120:123], v[248:251]\n"
"ds_read_b64 v[82:83], v178 offset:27072\n"
"v_cvt_pk_bf16_f32 v233, v234, v235\n"
"v_mfma_f32_16x16x32_bf16 v[244:247], v[128:131], v[120:123], v[244:247]\n"
"ds_read_b64 v[84:85], v179 offset:27072\n"
"v_cvt_pk_bf16_f32 v234, v236, v237\n"
"v_mfma_f32_16x16x32_bf16 v[252:255], v[112:115], v[120:123], v[252:255]\n"
"ds_read_b128 v[136:139], v180\n"
"v_cvt_pk_bf16_f32 v235, v238, v239\n"
"v_mfma_f32_16x16x32_bf16 v[236:239], a[220:223], v[68:71], 0\n"
"buffer_store_dwordx4 v[232:235], v194, s[24:27], s36 offen nt\n"
"s_nop 1\n"
"v_mfma_f32_16x16x32_bf16 v[232:235], a[216:219], v[68:71], 0\n"
"ds_read_b128 v[132:135], v181\n"
"ds_read_b64 v[92:93], v181 offset:64\n"
"v_mfma_f32_16x16x32_bf16 v[204:207], a[140:143], v[52:55], v[204:207]\n"
"v_rcp_f32 v171, v90\n"
"v_mfma_f32_16x16x32_bf16 v[232:235], a[228:231], v[72:75], v[232:235]\n"
"s_waitcnt lgkmcnt(6)\n"
"ds_read_b64 v[90:91], v180 offset:64\n"
"ds_read_b128 v[120:123], v180 offset:13504\n"
"v_mfma_f32_16x16x32_bf16 v[116:119], v[116:119], v[152:155], 0\n"
"s_waitcnt lgkmcnt(7)\n"
"ds_read_b128 v[140:143], v181 offset:13504\n"
"v_cvt_pk_bf16_f32 v240, v240, v241\n"
"v_mfma_f32_16x16x32_bf16 v[124:127], v[124:127], v[152:155], 0\n"
"s_waitcnt lgkmcnt(7)\n"
"ds_read_b64 v[98:99], v180 offset:13568\n"
"v_mul_f32 v248, v171, v248\n"
"v_mfma_f32_16x16x32_bf16 v[116:119], v[80:83], v[164:167], v[116:119]\n"
"s_waitcnt lgkmcnt(7)\n"
"ds_read_b64 v[100:101], v181 offset:13568\n"
"v_cvt_pk_bf16_f32 v241, v242, v243\n"
"v_mfma_f32_16x16x32_bf16 v[124:127], v[84:87], v[166:169], v[124:127]\n"
"s_waitcnt lgkmcnt(7)\n"
"v_mul_f32 v249, v171, v249\n"
"v_mul_f32 v250, v171, v250\n"
"v_mfma_f32_16x16x32_bf16 v[136:139], v[136:139], v[152:155], 0\n"
"v_mul_f32 v251, v171, v251\n"
"v_cvt_pk_bf16_f32 v248, v248, v249\n"
"v_mfma_f32_16x16x32_bf16 v[236:239], a[232:235], v[72:75], v[236:239]\n"
"s_waitcnt lgkmcnt(6)\n"
"ds_read_b128 v[128:131], v180 offset:27008\n"
"v_cvt_pk_bf16_f32 v249, v250, v251\n"
"v_mfma_f32_16x16x32_bf16 v[132:135], v[132:135], v[152:155], 0\n"
"s_waitcnt lgkmcnt(6)\n"
"ds_read_b128 v[112:115], v181 offset:27008\n"
"v_mul_f32 v244, v171, v244\n"
"v_mfma_f32_16x16x32_bf16 v[132:135], v[92:95], v[166:169], v[132:135]\n"
"s_waitcnt lgkmcnt(6)\n"
"v_mul_f32 v245, v171, v245\n"
"v_mul_f32 v246, v171, v246\n"
"v_mfma_f32_16x16x32_bf16 v[136:139], v[88:91], v[164:167], v[136:139]\n"
"s_waitcnt lgkmcnt(5)\n"
"ds_read_b64 v[106:107], v180 offset:27072\n"
"v_mul_f32 v247, v171, v247\n"
"v_mfma_f32_16x16x32_bf16 v[120:123], v[120:123], v[152:155], 0\n"
"s_waitcnt lgkmcnt(5)\n"
"ds_read_b64 v[108:109], v181 offset:27072\n"
"v_cvt_pk_bf16_f32 v250, v244, v245\n"
"v_mfma_f32_16x16x32_bf16 v[140:143], v[140:143], v[152:155], 0\n"
"s_waitcnt lgkmcnt(5)\n"
"v_cvt_pk_bf16_f32 v251, v246, v247\n"
"v_mul_f32 v252, v171, v252\n"
"v_mfma_f32_16x16x32_bf16 v[120:123], v[96:99], v[164:167], v[120:123]\n"
"s_waitcnt lgkmcnt(4)\n"
"buffer_store_dwordx4 v[248:251], v192, s[28:31], s37 offen sc1\n"
"v_mfma_f32_16x16x32_bf16 v[140:143], v[100:103], v[166:169], v[140:143]\n"
"s_waitcnt lgkmcnt(3)\n"
"v_mul_f32 v253, v171, v253\n"
"v_mul_f32 v254, v171, v254\n"
"v_mfma_f32_16x16x32_bf16 v[128:131], v[128:131], v[152:155], 0\n"
"s_waitcnt lgkmcnt(2)\n"
"ds_read_b32 v187, v202 offset:36864\n"
"v_mul_f32 v255, v171, v255\n"
"v_mfma_f32_16x16x32_bf16 v[112:115], v[112:115], v[152:155], 0\n"
"buffer_store_dwordx2 v[240:241], v195, s[24:27], s36 offen nt\n"
"v_mfma_f32_16x16x32_bf16 v[240:243], a[224:227], v[68:71], 0\n"
"s_waitcnt lgkmcnt(2)\n"
"ds_read_b128 v[68:71], v192 offset:33792\n"
"v_cvt_pk_bf16_f32 v252, v252, v253\n"
"v_mfma_f32_16x16x32_bf16 v[128:131], v[104:107], v[164:167], v[128:131]\n"
"s_waitcnt lgkmcnt(2)\n"
"v_cvt_pk_bf16_f32 v253, v254, v255\n"
"v_cvt_pk_bf16_f32 v208, v208, v209\n"
"v_mfma_f32_16x16x32_bf16 v[112:115], v[108:111], v[166:169], v[112:115]\n"
"s_waitcnt vmcnt(9)\n"
"v_add3_u32 v199, v199, v197, -4\n"
"v_cmp_lt_u32 vcc, 64, v199\n"
"v_mfma_f32_16x16x32_bf16 v[240:243], a[236:239], v[72:75], v[240:243]\n"
"s_add_u32 s36, s36, s33\n"
"ds_read_b128 v[72:75], v192 offset:34816\n"
"ds_read_b64_tr_b16 v[244:245], v182 offset:27136\n"
"v_mfma_f32_16x16x32_bf16 v[232:235], a[240:243], v[76:79], v[232:235]\n"
"s_cbranch_vccnz .Lp3%=\n"
".Lr3%=:\n"
"v_mfma_f32_16x16x32_bf16 v[228:231], a[144:147], v[32:35], 0\n"
"ds_read_b64_tr_b16 v[246:247], v183 offset:27136\n"
"ds_read_b64_tr_b16 v[248:249], v182 offset:27144\n"
"v_mfma_f32_16x16x32_bf16 v[236:239], a[244:247], v[76:79], v[236:239]\n"
"ds_read_b64_tr_b16 v[250:251], v183 offset:27144\n"
"v_cvt_pk_bf16_f32 v209, v210, v211\n"
"v_mfma_f32_16x16x32_bf16 v[228:231], a[156:159], v[36:39], v[228:231]\n"
"ds_read_b64_tr_b16 v[254:255], v183 offset:27080\n"
"v_cvt_pk_bf16_f32 v210, v216, v217\n"
"v_mfma_f32_16x16x32_bf16 v[240:243], a[248:251], v[76:79], v[240:243]\n"
"s_waitcnt lgkmcnt(6)\n"
"ds_read_b128 v[76:79], v192 offset:35840\n"
"v_cvt_pk_bf16_f32 v211, v218, v219\n"
"v_mfma_f32_16x16x32_bf16 v[216:219], a[148:151], v[32:35], 0\n"
"ds_write_b128 v186, v[208:211] offset:6752\n"
"v_mfma_f32_16x16x32_bf16 v[208:211], a[152:155], v[32:35], 0\n"
"v_cvt_pk_bf16_f32 v220, v220, v221\n"
"v_cvt_pk_bf16_f32 v221, v222, v223\n"
"v_mfma_f32_16x16x32_bf16 v[216:219], a[160:163], v[36:39], v[216:219]\n"
"v_max_f32 v116, v116, v124\n"
"v_max_f32 v117, v117, v125\n"
"v_mfma_f32_16x16x32_bf16 v[208:211], a[164:167], v[36:39], v[208:211]\n"
"ds_read_b128 v[32:35], v188 offset:6144\n"
"ds_read_b128 v[36:39], v189 offset:6144\n"
"v_mfma_f32_16x16x32_bf16 v[228:231], a[168:171], v[40:43], v[228:231]\n"
"ds_read_b64_tr_b16 v[124:125], v184 offset:128\n"
"v_max_f32 v118, v118, v126\n"
"v_mfma_f32_16x16x32_bf16 v[232:235], a[252:255], v[68:71], v[232:235]\n"
"s_waitcnt lgkmcnt(10)\n"
"buffer_load_dword v199, off, s[28:31], s39 sc1\n"
"ds_read_b64_tr_b16 v[98:99], v184 offset:13640\n"
"v_mfma_f32_16x16x32_bf16 v[216:219], a[172:175], v[40:43], v[216:219]\n"
"buffer_load_dwordx4 v[56:59], v192, s[28:31], s38 offen sc1\n"
"ds_read_b64_tr_b16 v[100:101], v185 offset:13640\n"
"v_mfma_f32_16x16x32_bf16 v[208:211], a[176:179], v[40:43], v[208:211]\n"
"ds_read_b128 v[40:43], v188 offset:7168\n"
"v_max_f32 v119, v119, v127\n"
"v_mfma_f32_16x16x32_bf16 v[236:239], v[0:3], v[68:71], v[236:239]\n"
"ds_read_b64_tr_b16 v[126:127], v185 offset:128\n"
"v_max_f32 v170, v116, v117\n"
"v_mfma_f32_16x16x32_bf16 v[228:231], a[180:183], v[44:47], v[228:231]\n"
"ds_read_b64_tr_b16 v[106:107], v184 offset:13576\n"
"v_max_f32 v171, v118, v119\n"
"v_mfma_f32_16x16x32_bf16 v[216:219], a[184:187], v[44:47], v[216:219]\n"
"buffer_load_dwordx2 v[60:61], v193, s[28:31], s38 offen offset:1024 sc1\n"
"ds_read_b64_tr_b16 v[108:109], v185 offset:13576\n"
"v_mfma_f32_16x16x32_bf16 v[208:211], a[188:191], v[44:47], v[208:211]\n"
"s_add_u32 s38, s38, 6144\n"
"ds_read_b128 v[44:47], v189 offset:7168\n"
"v_max_f32 v136, v136, v132\n"
"v_mfma_f32_16x16x32_bf16 v[240:243], v[4:7], v[68:71], v[240:243]\n"
"v_max_f32 v137, v137, v133\n"
"v_max_f32 v138, v138, v134\n"
"v_mfma_f32_16x16x32_bf16 v[228:231], a[192:195], v[48:51], v[228:231]\n"
"s_waitcnt lgkmcnt(8)\n"
"ds_read_b64_tr_b16 v[132:133], v184 offset:136\n"
"v_max_f32 v139, v139, v135\n"
"v_mfma_f32_16x16x32_bf16 v[216:219], a[196:199], v[48:51], v[216:219]\n"
"s_add_u32 m0, s34, 6144\n"
"buffer_load_dwordx4 v[62:65], v192, s[28:31], s38 offen sc1\n"
"ds_read_b64_tr_b16 v[134:135], v185 offset:136\n"
"v_mfma_f32_16x16x32_bf16 v[208:211], a[200:203], v[48:51], v[208:211]\n"
"ds_read_b128 v[48:51], v188 offset:8192\n"
"v_max3_f32 v170, v170, v136, v137\n"
"v_mfma_f32_16x16x32_bf16 v[232:235], v[8:11], v[72:75], v[232:235]\n"
"v_max3_f32 v171, v171, v138, v139\n"
"v_max_f32 v120, v120, v140\n"
"v_mfma_f32_16x16x32_bf16 v[228:231], a[204:207], v[52:55], v[228:231]\n"
"v_max_f32 v121, v121, v141\n"
"v_max_f32 v122, v122, v142\n"
"v_mfma_f32_16x16x32_bf16 v[216:219], a[208:211], v[52:55], v[216:219]\n"
"buffer_load_dwordx4 v190, s[8:11], s32 offen lds\n"
"ds_read_b64_tr_b16 v[140:141], v184 offset:72\n"
"v_mfma_f32_16x16x32_bf16 v[236:239], v[12:15], v[72:75], v[236:239]\n"
"v_max_f32 v123, v123, v143\n"
"v_max3_f32 v170, v170, v120, v121\n"
"v_mfma_f32_16x16x32_bf16 v[208:211], a[212:215], v[52:55], v[208:211]\n"
"ds_read_b128 v[52:55], v189 offset:8192\n"
"ds_read_b64_tr_b16 v[142:143], v185 offset:72\n"
"v_mfma_f32_16x16x32_bf16 v[240:243], v[16:19], v[72:75], v[240:243]\n"
"s_waitcnt lgkmcnt(6)\n"
"v_max3_f32 v171, v171, v122, v123\n"
"v_max_f32 v128, v128, v112\n"
"v_mfma_f32_16x16x32_bf16 v[232:235], v[20:23], v[76:79], v[232:235]\n"
"s_add_u32 m0, s34, 9216\n"
"buffer_store_dwordx2 v[252:253], v193, s[28:31], s37 offen offset:1024 sc1\n"
"v_mfma_f32_16x16x32_bf16 v[236:239], v[24:27], v[76:79], v[236:239]\n"
"s_add_u32 s37, s37, 6144\n"
"ds_read_b64_tr_b16 v[252:253], v182 offset:27080\n"
"v_max_f32 v129, v129, v113\n"
"v_mfma_f32_16x16x32_bf16 v[240:243], v[28:31], v[76:79], v[240:243]\n"
"v_max_f32 v130, v130, v114\n"
"v_max_f32 v131, v131, v115\n"
"v_cvt_pk_bf16_f32 v224, v224, v225\n"
"v_max3_f32 v170, v170, v128, v129\n"
"v_cvt_pk_bf16_f32 v225, v226, v227\n"
"v_max3_f32 v171, v171, v130, v131\n"
"v_cvt_pk_bf16_f32 v226, v212, v213\n"
"buffer_load_dwordx4 v191, s[8:11], s32 offen lds\n"
"s_add_u32 s32, s32, s33\n"
"v_cvt_pk_bf16_f32 v227, v214, v215\n"
"v_mfma_f32_16x16x32_bf16 v[212:215], a[0:3], v[32:35], 0\n"
"ds_read_b64_tr_b16 v[112:113], v184 offset:13632\n"
"ds_read_b64_tr_b16 v[114:115], v185 offset:13632\n"
"v_mfma_f32_16x16x32_bf16 v[212:215], a[12:15], v[36:39], v[212:215]\n"
"s_waitcnt lgkmcnt(4)\n"
"ds_write_b128 v186, v[224:227] offset:6880\n"
"v_mfma_f32_16x16x32_bf16 v[212:215], a[24:27], v[40:43], v[212:215]\n"
"s_add_u32 m0, s34, 7168\n"
"buffer_load_dwordx2 v[66:67], v193, s[28:31], s38 offen offset:1024 sc1\n"
"v_cvt_pk_bf16_f32 v222, v204, v205\n"
"v_mfma_f32_16x16x32_bf16 v[212:215], a[36:39], v[44:47], v[212:215]\n"
"s_add_u32 s38, s38, 6144\n"
"v_cvt_pk_bf16_f32 v223, v206, v207\n"
"v_max_f32 v170, v170, v171\n"
"v_mfma_f32_16x16x32_bf16 v[204:207], a[4:7], v[32:35], 0\n"
"ds_write_b128 v186, v[220:223] offset:6816\n"
"v_mfma_f32_16x16x32_bf16 v[220:223], a[8:11], v[32:35], 0\n"
"v_mov_b32 v171, v170\n"
"v_cvt_pk_bf16_f32 v152, v228, v229\n"
"v_mfma_f32_16x16x32_bf16 v[204:207], a[16:19], v[36:39], v[204:207]\n"
"v_permlane16_swap_b32 v170, v171\n"
"v_mfma_f32_16x16x32_bf16 v[220:223], a[20:23], v[36:39], v[220:223]\n"
"v_max_f32 v170, v170, v171\n"
"v_mov_b32 v171, v170\n"
"v_mfma_f32_16x16x32_bf16 v[204:207], a[28:31], v[40:43], v[204:207]\n"
"s_nop 0\n"
"v_permlane32_swap_b32 v170, v171\n"
"v_mfma_f32_16x16x32_bf16 v[220:223], a[32:35], v[40:43], v[220:223]\n"
"v_max_f32 v170, v170, v171\n"
"v_mul_f32 v170, 0xbe553b95, v170\n"
"v_mfma_f32_16x16x32_bf16 v[204:207], a[40:43], v[44:47], v[204:207]\n"
"v_fmamk_f32 v116, v116, 0x3e553b95, v170\n"
"v_fmamk_f32 v117, v117, 0x3e553b95, v170\n"
"v_mfma_f32_16x16x32_bf16 v[220:223], a[44:47], v[44:47], v[220:223]\n"
"v_fmamk_f32 v118, v118, 0x3e553b95, v170\n"
"v_fmamk_f32 v119, v119, 0x3e553b95, v170\n"
"v_mfma_f32_16x16x32_bf16 v[212:215], a[48:51], v[48:51], v[212:215]\n"
"v_exp_f32 v116, v116\n"
"v_mfma_f32_16x16x32_bf16 v[204:207], a[52:55], v[48:51], v[204:207]\n"
"v_exp_f32 v117, v117\n"
"v_mfma_f32_16x16x32_bf16 v[220:223], a[56:59], v[48:51], v[220:223]\n"
"v_exp_f32 v118, v118\n"
"v_mfma_f32_16x16x32_bf16 v[212:215], a[60:63], v[52:55], v[212:215]\n"
"v_exp_f32 v119, v119\n"
"v_mfma_f32_16x16x32_bf16 v[204:207], a[64:67], v[52:55], v[204:207]\n"
"v_cvt_pk_bf16_f32 v82, v116, v117\n"
"v_cvt_pk_bf16_f32 v83, v118, v119\n"
"v_mfma_f32_16x16x32_bf16 v[220:223], a[68:71], v[52:55], v[220:223]\n"
"v_and_b32 v116, v82, v176\n"
"v_and_b32 v117, v83, v177\n"
"v_mfma_f32_16x16x32_bf16 v[224:227], a[72:75], v[32:35], 0\n"
"v_xor_b32 v118, v82, v116\n"
"v_xor_b32 v119, v83, v117\n"
"v_mfma_f32_16x16x32_bf16 v[224:227], a[84:87], v[36:39], v[224:227]\n"
"s_waitcnt lgkmcnt(4)\n"
"v_fmamk_f32 v136, v136, 0x3e553b95, v170\n"
"v_fmamk_f32 v137, v137, 0x3e553b95, v170\n"
"v_mfma_f32_16x16x32_bf16 v[244:247], v[244:247], v[116:119], 0\n"
"v_fmamk_f32 v138, v138, 0x3e553b95, v170\n"
"v_fmamk_f32 v139, v139, 0x3e553b95, v170\n"
"v_mfma_f32_16x16x32_bf16 v[248:251], v[248:251], v[116:119], 0\n"
"v_exp_f32 v136, v136\n"
"v_mfma_f32_16x16x32_bf16 v[252:255], v[252:255], v[116:119], 0\n"
"v_exp_f32 v137, v137\n"
"v_mfma_f32_16x16x32_bf16 v[224:227], a[96:99], v[40:43], v[224:227]\n"
"v_exp_f32 v138, v138\n"
"v_mfma_f32_16x16x32_bf16 v[224:227], a[108:111], v[44:47], v[224:227]\n"
"v_exp_f32 v139, v139\n"
"v_mfma_f32_16x16x32_bf16 v[224:227], a[120:123], v[48:51], v[224:227]\n"
"ds_read_b64_tr_b16 v[116:117], v184 offset:27136\n"
"ds_read_b64_tr_b16 v[118:119], v185 offset:27136\n"
"v_mfma_f32_16x16x32_bf16 v[224:227], a[132:135], v[52:55], v[224:227]\n"
"v_cvt_pk_bf16_f32 v84, v136, v137\n"
"v_cvt_pk_bf16_f32 v85, v138, v139\n"
"v_and_b32 v136, v84, v176\n"
"v_and_b32 v137, v85, v177\n"
"v_xor_b32 v138, v84, v136\n"
"v_xor_b32 v139, v85, v137\n"
"v_mfma_f32_16x16x32_bf16 v[82:85], v[172:175], v[82:85], 0\n"
"v_fmamk_f32 v120, v120, 0x3e553b95, v170\n"
"v_fmamk_f32 v121, v121, 0x3e553b95, v170\n"
"v_mfma_f32_16x16x32_bf16 v[244:247], v[124:127], v[136:139], v[244:247]\n"
"v_fmamk_f32 v122, v122, 0x3e553b95, v170\n"
"v_fmamk_f32 v123, v123, 0x3e553b95, v170\n"
"v_mfma_f32_16x16x32_bf16 v[248:251], v[132:135], v[136:139], v[248:251]\n"
"ds_read_b64_tr_b16 v[124:125], v184 offset:27080\n"
"ds_read_b64_tr_b16 v[126:127], v185 offset:27080\n"
"v_mfma_f32_16x16x32_bf16 v[252:255], v[140:143], v[136:139], v[252:255]\n"
"v_exp_f32 v120, v120\n"
"v_exp_f32 v121, v121\n"
"v_exp_f32 v122, v122\n"
"v_exp_f32 v123, v123\n"
"v_cvt_pk_bf16_f32 v90, v120, v121\n"
"v_cvt_pk_bf16_f32 v91, v122, v123\n"
"v_and_b32 v120, v90, v176\n"
"v_and_b32 v121, v91, v177\n"
"v_xor_b32 v122, v90, v120\n"
"v_xor_b32 v123, v91, v121\n"
"v_fmamk_f32 v128, v128, 0x3e553b95, v170\n"
"v_fmamk_f32 v129, v129, 0x3e553b95, v170\n"
"v_mfma_f32_16x16x32_bf16 v[248:251], v[98:101], v[120:123], v[248:251]\n"
"s_waitcnt lgkmcnt(6)\n"
"ds_read_b64_tr_b16 v[136:137], v184 offset:27144\n"
"ds_read_b64_tr_b16 v[138:139], v185 offset:27144\n"
"v_mfma_f32_16x16x32_bf16 v[244:247], v[112:115], v[120:123], v[244:247]\n"
"buffer_load_dwordx4 v190, s[8:11], s0 offen lds\n"
"v_fmamk_f32 v130, v130, 0x3e553b95, v170\n"
"v_mfma_f32_16x16x32_bf16 v[252:255], v[106:109], v[120:123], v[252:255]\n"
"v_cvt_pk_bf16_f32 v153, v230, v231\n"
"v_fmamk_f32 v131, v131, 0x3e553b95, v170\n"
"v_mfma_f32_16x16x32_bf16 v[228:231], a[76:79], v[32:35], 0\n"
"s_add_u32 m0, s34, 10240\n"
"v_cvt_pk_bf16_f32 v154, v216, v217\n"
"v_cvt_pk_bf16_f32 v155, v218, v219\n"
"v_mfma_f32_16x16x32_bf16 v[216:219], a[80:83], v[32:35], 0\n"
"buffer_load_dwordx4 v191, s[8:11], s0 offen lds\n"
"v_cvt_pk_bf16_f32 v166, v208, v209\n"
"v_mfma_f32_16x16x32_bf16 v[228:231], a[88:91], v[36:39], v[228:231]\n"
"s_add_u32 s0, s0, s33\n"
"v_exp_f32 v128, v128\n"
"v_mfma_f32_16x16x32_bf16 v[216:219], a[92:95], v[36:39], v[216:219]\n"
"v_exp_f32 v129, v129\n"
"v_mfma_f32_16x16x32_bf16 v[228:231], a[100:103], v[40:43], v[228:231]\n"
"s_add_u32 m0, s34, 8192\n"
"v_exp_f32 v130, v130\n"
"v_mfma_f32_16x16x32_bf16 v[216:219], a[104:107], v[40:43], v[216:219]\n"
"buffer_load_dwordx4 v190, s[8:11], s1 offen lds\n"
"v_cvt_pk_bf16_f32 v167, v210, v211\n"
"v_mfma_f32_16x16x32_bf16 v[228:231], a[112:115], v[44:47], v[228:231]\n"
"v_exp_f32 v131, v131\n"
"v_mfma_f32_16x16x32_bf16 v[216:219], a[116:119], v[44:47], v[216:219]\n"
"v_cvt_pk_bf16_f32 v92, v128, v129\n"
"v_cvt_pk_bf16_f32 v93, v130, v131\n"
"v_mfma_f32_16x16x32_bf16 v[228:231], a[124:127], v[48:51], v[228:231]\n"
"s_add_u32 m0, s34, 11264\n"
"v_and_b32 v128, v92, v176\n"
"v_and_b32 v129, v93, v177\n"
"v_mfma_f32_16x16x32_bf16 v[216:219], a[128:131], v[48:51], v[216:219]\n"
"buffer_load_dwordx4 v191, s[8:11], s1 offen lds\n"
"ds_read_b32 v191, v202 offset:36868\n"
"v_mfma_f32_16x16x32_bf16 v[228:231], a[136:139], v[52:55], v[228:231]\n"
"s_add_u32 s1, s1, s33\n"
"v_xor_b32 v130, v92, v128\n"
"v_xor_b32 v131, v93, v129\n"
"v_mfma_f32_16x16x32_bf16 v[90:93], v[172:175], v[90:93], v[82:85]\n"
"s_waitcnt lgkmcnt(1)\n"
"ds_read_b128 v[132:135], v180\n"
"ds_read_b128 v[140:143], v181\n"
"v_mfma_f32_16x16x32_bf16 v[244:247], v[116:119], v[128:131], v[244:247]\n"
"ds_read_b64 v[82:83], v180 offset:64\n"
"v_cvt_pk_bf16_f32 v232, v232, v233\n"
"v_mfma_f32_16x16x32_bf16 v[248:251], v[136:139], v[128:131], v[248:251]\n"
"ds_read_b64 v[84:85], v181 offset:64\n"
"v_cvt_pk_bf16_f32 v233, v234, v235\n"
"v_mfma_f32_16x16x32_bf16 v[252:255], v[124:127], v[128:131], v[252:255]\n"
"ds_read_b128 v[120:123], v180 offset:13504\n"
"v_cvt_pk_bf16_f32 v234, v236, v237\n"
"v_mfma_f32_16x16x32_bf16 v[216:219], a[140:143], v[52:55], v[216:219]\n"
"v_rcp_f32 v171, v90\n"
"v_mfma_f32_16x16x32_bf16 v[208:211], a[144:147], v[32:35], 0\n"
"ds_read_b128 v[112:115], v181 offset:13504\n"
"ds_read_b64 v[90:91], v180 offset:13568\n"
"v_mfma_f32_16x16x32_bf16 v[208:211], a[156:159], v[36:39], v[208:211]\n"
"v_cvt_pk_bf16_f32 v235, v238, v239\n"
"v_cvt_pk_bf16_f32 v240, v240, v241\n"
"v_mfma_f32_16x16x32_bf16 v[208:211], a[168:171], v[40:43], v[208:211]\n"
"buffer_store_dwordx4 v[232:235], v194, s[24:27], s36 offen nt\n"
"v_mfma_f32_16x16x32_bf16 v[208:211], a[180:183], v[44:47], v[208:211]\n"
"s_waitcnt lgkmcnt(5)\n"
"ds_read_b64 v[92:93], v181 offset:13568\n"
"v_cvt_pk_bf16_f32 v241, v242, v243\n"
"v_mfma_f32_16x16x32_bf16 v[132:135], v[132:135], v[144:147], 0\n"
"s_waitcnt lgkmcnt(5)\n"
"ds_read_b128 v[128:131], v180 offset:27008\n"
"ds_read_b128 v[116:119], v181 offset:27008\n"
"v_mfma_f32_16x16x32_bf16 v[140:143], v[140:143], v[144:147], 0\n"
"s_waitcnt lgkmcnt(6)\n"
"v_mul_f32 v244, v171, v244\n"
"v_mul_f32 v245, v171, v245\n"
"v_mfma_f32_16x16x32_bf16 v[132:135], v[80:83], v[156:159], v[132:135]\n"
"s_waitcnt lgkmcnt(5)\n"
"ds_read_b64 v[98:99], v180 offset:27072\n"
"v_mul_f32 v246, v171, v246\n"
"v_mfma_f32_16x16x32_bf16 v[140:143], v[84:87], v[158:161], v[140:143]\n"
"s_waitcnt lgkmcnt(5)\n"
"ds_read_b64 v[100:101], v181 offset:27072\n"
"v_mul_f32 v247, v171, v247\n"
"v_mfma_f32_16x16x32_bf16 v[120:123], v[120:123], v[144:147], 0\n"
"s_waitcnt lgkmcnt(5)\n"
"v_cvt_pk_bf16_f32 v244, v244, v245\n"
"v_cvt_pk_bf16_f32 v245, v246, v247\n"
"v_mfma_f32_16x16x32_bf16 v[112:115], v[112:115], v[144:147], 0\n"
".Lic5%=:\n"
"s_cbranch_execz .Lib1%=\n"
"v_mul_f32 v248, v171, v248\n"
"v_mul_f32 v249, v171, v249\n"
"v_mfma_f32_16x16x32_bf16 v[120:123], v[88:91], v[156:159], v[120:123]\n"
"v_mul_f32 v250, v171, v250\n"
"v_mul_f32 v251, v171, v251\n"
"v_mfma_f32_16x16x32_bf16 v[208:211], a[192:195], v[48:51], v[208:211]\n"
"s_waitcnt lgkmcnt(4)\n"
"v_cvt_pk_bf16_f32 v246, v248, v249\n"
"v_cvt_pk_bf16_f32 v247, v250, v251\n"
"v_mfma_f32_16x16x32_bf16 v[112:115], v[92:95], v[158:161], v[112:115]\n"
"s_waitcnt lgkmcnt(3)\n"
"v_mul_f32 v252, v171, v252\n"
"v_mul_f32 v253, v171, v253\n"
"v_mfma_f32_16x16x32_bf16 v[128:131], v[128:131], v[144:147], 0\n"
"s_waitcnt lgkmcnt(2)\n"
"v_mul_f32 v254, v171, v254\n"
"v_mul_f32 v255, v171, v255\n"
"v_mfma_f32_16x16x32_bf16 v[116:119], v[116:119], v[144:147], 0\n"
"s_waitcnt lgkmcnt(1)\n"
"v_cvt_pk_bf16_f32 v252, v252, v253\n"
"v_cvt_pk_bf16_f32 v253, v254, v255\n"
"v_mfma_f32_16x16x32_bf16 v[128:131], v[96:99], v[156:159], v[128:131]\n"
"s_waitcnt lgkmcnt(0)\n"
"s_mov_b64 exec, s[44:45]\n"
"v_add_u32 v179, 0xc0, v179\n"
"s_mov_b64 exec, -1\n"
"v_mfma_f32_16x16x32_bf16 v[116:119], v[100:103], v[158:161], v[116:119]\n"
"buffer_store_dwordx4 v[244:247], v192, s[28:31], s37 offen sc1\n"
"v_mfma_f32_16x16x32_bf16 v[208:211], a[204:207], v[52:55], v[208:211]\n"
"s_mov_b64 exec, s[46:47]\n"
"v_add_u32 v183, 0xc0, v183\n"
"s_mov_b64 exec, -1\n"
"v_cvt_pk_bf16_f32 v212, v212, v213\n"
"v_cvt_pk_bf16_f32 v213, v214, v215\n"
"v_cvt_pk_bf16_f32 v214, v204, v205\n"
"v_cvt_pk_bf16_f32 v215, v206, v207\n"
"v_mfma_f32_16x16x32_bf16 v[204:207], a[148:151], v[32:35], 0\n"
"buffer_store_dwordx2 v[252:253], v193, s[28:31], s37 offen offset:1024 sc1\n"
"v_mfma_f32_16x16x32_bf16 v[204:207], a[160:163], v[36:39], v[204:207]\n"
"s_add_u32 s37, s37, 6144\n"
"ds_read_b64_tr_b16 v[248:249], v184 offset:128\n"
"ds_read_b64_tr_b16 v[250:251], v185 offset:128\n"
"v_mfma_f32_16x16x32_bf16 v[204:207], a[172:175], v[40:43], v[204:207]\n"
"ds_read_b64_tr_b16 v[244:245], v184 offset:136\n"
"v_cvt_pk_bf16_f32 v220, v220, v221\n"
"v_mfma_f32_16x16x32_bf16 v[204:207], a[184:187], v[44:47], v[204:207]\n"
"ds_read_b64_tr_b16 v[246:247], v185 offset:136\n"
"v_cvt_pk_bf16_f32 v221, v222, v223\n"
"v_mfma_f32_16x16x32_bf16 v[204:207], a[196:199], v[48:51], v[204:207]\n"
"buffer_store_dwordx2 v[240:241], v195, s[24:27], s36 offen nt\n"
"v_mfma_f32_16x16x32_bf16 v[204:207], a[208:211], v[52:55], v[204:207]\n"
"s_add_u32 s36, s36, s33\n"
"ds_read_b64_tr_b16 v[252:253], v184 offset:72\n"
"ds_read_b64_tr_b16 v[254:255], v185 offset:72\n"
"v_max_f32 v132, v132, v140\n"
"v_max_f32 v133, v133, v141\n"
"v_max_f32 v134, v134, v142\n"
"v_max_f32 v135, v135, v143\n"
"v_max_f32 v170, v132, v133\n"
"v_max_f32 v171, v134, v135\n"
"v_max_f32 v120, v120, v112\n"
"v_max_f32 v121, v121, v113\n"
"v_max_f32 v122, v122, v114\n"
"v_max_f32 v123, v123, v115\n"
"v_max3_f32 v170, v170, v120, v121\n"
"v_max3_f32 v171, v171, v122, v123\n"
"v_max_f32 v128, v128, v116\n"
"s_waitcnt vmcnt(13)\n"
"v_add3_u32 v199, v199, v197, -6\n"
"v_cmp_lt_u32 vcc, 64, v199\n"
"ds_write_b128 v202, v[56:59] offset:55296\n"
"s_nop 3\n"
"s_cbranch_vccnz .Lp5%=\n"
".Lr5%=:\n"
"v_max_f32 v129, v129, v117\n"
"v_max_f32 v130, v130, v118\n"
"v_max_f32 v131, v131, v119\n"
"v_max3_f32 v170, v170, v128, v129\n"
"ds_write_b64 v203, v[60:61] offset:55296\n"
"v_max3_f32 v171, v171, v130, v131\n"
"v_cvt_pk_bf16_f32 v224, v224, v225\n"
"v_cvt_pk_bf16_f32 v225, v226, v227\n"
"v_cvt_pk_bf16_f32 v226, v228, v229\n"
"v_cvt_pk_bf16_f32 v227, v230, v231\n"
"v_cvt_pk_bf16_f32 v222, v216, v217\n"
"v_cvt_pk_bf16_f32 v223, v218, v219\n"
"ds_read_b64_tr_b16 v[140:141], v184 offset:13632\n"
"ds_read_b64_tr_b16 v[142:143], v185 offset:13632\n"
"ds_read_b64_tr_b16 v[112:113], v184 offset:13640\n"
"ds_read_b64_tr_b16 v[114:115], v185 offset:13640\n"
"ds_read_b64_tr_b16 v[116:117], v184 offset:13576\n"
"buffer_load_dwordx4 v[56:59], v192, s[28:31], s38 offen sc1\n"
"ds_read_b64_tr_b16 v[118:119], v185 offset:13576\n"
"ds_read_b64_tr_b16 v[98:99], v184 offset:27144\n"
"s_waitcnt lgkmcnt(7)\n"
"s_barrier\n"
"ds_read_b128 v[68:71], v192 offset:55296\n"
"ds_read_b128 v[72:75], v192 offset:56320\n"
"buffer_load_dwordx2 v[60:61], v193, s[28:31], s38 offen offset:1024 sc1\n"
"s_add_u32 s38, s38, 6144\n"
"ds_read_b128 v[76:79], v192 offset:57344\n"
"ds_write_b128 v186, v[212:215] offset:20256\n"
"v_mfma_f32_16x16x32_bf16 v[212:215], a[152:155], v[32:35], 0\n"
"s_waitcnt vmcnt(14)\n"
"v_mfma_f32_16x16x32_bf16 v[212:215], a[164:167], v[36:39], v[212:215]\n"
"ds_read_b128 v[32:35], v188\n"
"v_mfma_f32_16x16x32_bf16 v[212:215], a[176:179], v[40:43], v[212:215]\n"
"ds_read_b128 v[36:39], v189\n"
"ds_read_b128 v[40:43], v188 offset:1024\n"
"v_mfma_f32_16x16x32_bf16 v[212:215], a[188:191], v[44:47], v[212:215]\n"
"ds_read_b128 v[44:47], v189 offset:1024\n"
"v_mfma_f32_16x16x32_bf16 v[212:215], a[200:203], v[48:51], v[212:215]\n"
"s_waitcnt lgkmcnt(7)\n"
"ds_read_b128 v[48:51], v188 offset:2048\n"
"v_mfma_f32_16x16x32_bf16 v[212:215], a[212:215], v[52:55], v[212:215]\n"
"ds_read_b128 v[52:55], v189 offset:2048\n"
"v_mfma_f32_16x16x32_bf16 v[232:235], a[216:219], v[68:71], 0\n"
"s_waitcnt lgkmcnt(8)\n"
"ds_write_b128 v202, v[62:65] offset:30720\n"
"v_mfma_f32_16x16x32_bf16 v[236:239], a[220:223], v[68:71], 0\n"
"buffer_load_dwordx4 v[62:65], v192, s[28:31], s38 offen sc1\n"
"v_mfma_f32_16x16x32_bf16 v[240:243], a[224:227], v[68:71], 0\n"
"s_waitcnt lgkmcnt(6)\n"
"ds_read_b128 v[68:71], v192 offset:58368\n"
"v_mfma_f32_16x16x32_bf16 v[232:235], a[228:231], v[72:75], v[232:235]\n"
"s_waitcnt lgkmcnt(6)\n"
"ds_read_b128 v[136:139], v178\n"
"ds_read_b128 v[124:127], v179\n"
"v_mfma_f32_16x16x32_bf16 v[236:239], a[232:235], v[72:75], v[236:239]\n"
"s_add_u32 m0, s34, 0\n"
"s_nop 0\n"
"buffer_load_dwordx4 v190, s[8:11], s32 offen lds\n"
"v_mfma_f32_16x16x32_bf16 v[240:243], a[236:239], v[72:75], v[240:243]\n"
"s_add_u32 s32, s32, s33\n"
"ds_read_b128 v[72:75], v192 offset:59392\n"
"ds_read_b64 v[106:107], v178 offset:64\n"
"v_mfma_f32_16x16x32_bf16 v[232:235], a[240:243], v[76:79], v[232:235]\n"
"ds_read_b64 v[108:109], v179 offset:64\n"
"v_mfma_f32_16x16x32_bf16 v[236:239], a[244:247], v[76:79], v[236:239]\n"
"s_waitcnt lgkmcnt(7)\n"
"s_add_u32 m0, s34, 3072\n"
"ds_write_b128 v186, v[220:223] offset:20320\n"
"v_mfma_f32_16x16x32_bf16 v[240:243], a[248:251], v[76:79], v[240:243]\n"
"s_waitcnt vmcnt(12)\n"
"buffer_load_dwordx4 v191, s[8:11], s75 offen lds\n"
"v_mfma_f32_16x16x32_bf16 v[228:231], a[0:3], v[32:35], 0\n"
"s_waitcnt lgkmcnt(6)\n"
"ds_read_b128 v[76:79], v192 offset:60416\n"
"v_mfma_f32_16x16x32_bf16 v[216:219], a[4:7], v[32:35], 0\n"
"s_waitcnt lgkmcnt(6)\n"
"ds_write_b64 v203, v[66:67] offset:30720\n"
"v_mfma_f32_16x16x32_bf16 v[136:139], v[136:139], v[144:147], 0\n"
"s_waitcnt lgkmcnt(6)\n"
"buffer_store_dword v198, v196, s[28:31], s40 offen sc1\n"
"v_mfma_f32_16x16x32_bf16 v[124:127], v[124:127], v[144:147], 0\n"
"s_add_u32 m0, s34, 1024\n"
"v_add_u32 v198, 2, v198\n"
"v_cvt_pk_bf16_f32 v144, v208, v209\n"
"v_mfma_f32_16x16x32_bf16 v[232:235], a[252:255], v[68:71], v[232:235]\n"
"s_waitcnt lgkmcnt(4)\n"
"ds_read_b64_tr_b16 v[100:101], v185 offset:27144\n"
"buffer_load_dwordx4 v190, s[8:11], s0 offen lds\n"
"v_mfma_f32_16x16x32_bf16 v[136:139], v[104:107], v[156:159], v[136:139]\n"
"s_waitcnt lgkmcnt(4)\n"
"ds_read_b64_tr_b16 v[106:107], v184 offset:27080\n"
"ds_write_b128 v186, v[224:227] offset:20384\n"
"v_mfma_f32_16x16x32_bf16 v[124:127], v[108:111], v[158:161], v[124:127]\n"
"s_add_u32 s0, s0, s33\n"
"v_cvt_pk_bf16_f32 v145, v210, v211\n"
"v_cvt_pk_bf16_f32 v146, v204, v205\n"
"v_mfma_f32_16x16x32_bf16 v[236:239], v[0:3], v[68:71], v[236:239]\n"
"v_cvt_pk_bf16_f32 v147, v206, v207\n"
"v_cvt_pk_bf16_f32 v158, v212, v213\n"
"v_mfma_f32_16x16x32_bf16 v[240:243], v[4:7], v[68:71], v[240:243]\n"
"ds_read_b64_tr_b16 v[108:109], v185 offset:27080\n"
"v_max_f32 v136, v136, v124\n"
"v_mfma_f32_16x16x32_bf16 v[232:235], v[8:11], v[72:75], v[232:235]\n"
"s_waitcnt lgkmcnt(5)\n"
"v_max_f32 v137, v137, v125\n"
"v_max_f32 v138, v138, v126\n"
"v_mfma_f32_16x16x32_bf16 v[236:239], v[12:15], v[72:75], v[236:239]\n"
"ds_read_b64_tr_b16 v[124:125], v184 offset:27136\n"
"v_max_f32 v139, v139, v127\n"
"v_mfma_f32_16x16x32_bf16 v[240:243], v[16:19], v[72:75], v[240:243]\n"
"ds_read_b64_tr_b16 v[126:127], v185 offset:27136\n"
"v_max3_f32 v170, v170, v136, v137\n"
"v_mfma_f32_16x16x32_bf16 v[232:235], v[20:23], v[76:79], v[232:235]\n"
"v_max3_f32 v171, v171, v138, v139\n"
"v_max_f32 v170, v170, v171\n"
"v_mfma_f32_16x16x32_bf16 v[236:239], v[24:27], v[76:79], v[236:239]\n"
"v_mov_b32 v171, v170\n"
"v_cvt_pk_bf16_f32 v159, v214, v215\n"
"v_mfma_f32_16x16x32_bf16 v[240:243], v[28:31], v[76:79], v[240:243]\n"
"s_waitcnt lgkmcnt(3)\n"
"v_permlane16_swap_b32 v170, v171\n"
"v_mfma_f32_16x16x32_bf16 v[220:223], a[8:11], v[32:35], 0\n"
"s_barrier\n"
"v_mfma_f32_16x16x32_bf16 v[228:231], a[12:15], v[36:39], v[228:231]\n"
"ds_read_b128 v[68:71], v192 offset:30720\n"
"ds_read_b128 v[72:75], v192 offset:31744\n"
"v_mfma_f32_16x16x32_bf16 v[216:219], a[16:19], v[36:39], v[216:219]\n"
"ds_read_b128 v[76:79], v192 offset:32768\n"
"v_max_f32 v170, v170, v171\n"
"v_mfma_f32_16x16x32_bf16 v[220:223], a[20:23], v[36:39], v[220:223]\n"
"v_mov_b32 v171, v170\n"
"v_cvt_pk_bf16_f32 v232, v232, v233\n"
"v_mfma_f32_16x16x32_bf16 v[228:231], a[24:27], v[40:43], v[228:231]\n"
"v_permlane32_swap_b32 v170, v171\n"
"v_mfma_f32_16x16x32_bf16 v[216:219], a[28:31], v[40:43], v[216:219]\n"
"v_max_f32 v170, v170, v171\n"
"v_mul_f32 v170, 0xbe553b95, v170\n"
"v_mfma_f32_16x16x32_bf16 v[220:223], a[32:35], v[40:43], v[220:223]\n"
"v_fmamk_f32 v132, v132, 0x3e553b95, v170\n"
"v_fmamk_f32 v133, v133, 0x3e553b95, v170\n"
"v_mfma_f32_16x16x32_bf16 v[228:231], a[36:39], v[44:47], v[228:231]\n"
"v_fmamk_f32 v134, v134, 0x3e553b95, v170\n"
"v_fmamk_f32 v135, v135, 0x3e553b95, v170\n"
"v_mfma_f32_16x16x32_bf16 v[216:219], a[40:43], v[44:47], v[216:219]\n"
"v_exp_f32 v132, v132\n"
"v_mfma_f32_16x16x32_bf16 v[220:223], a[44:47], v[44:47], v[220:223]\n"
"v_exp_f32 v133, v133\n"
"v_mfma_f32_16x16x32_bf16 v[228:231], a[48:51], v[48:51], v[228:231]\n"
"v_exp_f32 v134, v134\n"
"v_mfma_f32_16x16x32_bf16 v[216:219], a[52:55], v[48:51], v[216:219]\n"
"v_exp_f32 v135, v135\n"
"v_mfma_f32_16x16x32_bf16 v[220:223], a[56:59], v[48:51], v[220:223]\n"
"v_cvt_pk_bf16_f32 v82, v132, v133\n"
"v_cvt_pk_bf16_f32 v83, v134, v135\n"
"v_mfma_f32_16x16x32_bf16 v[228:231], a[60:63], v[52:55], v[228:231]\n"
"v_and_b32 v132, v82, v176\n"
"v_and_b32 v133, v83, v177\n"
"v_mfma_f32_16x16x32_bf16 v[216:219], a[64:67], v[52:55], v[216:219]\n"
"v_xor_b32 v134, v82, v132\n"
"v_xor_b32 v135, v83, v133\n"
"v_mfma_f32_16x16x32_bf16 v[220:223], a[68:71], v[52:55], v[220:223]\n"
"v_fmamk_f32 v120, v120, 0x3e553b95, v170\n"
"v_fmamk_f32 v121, v121, 0x3e553b95, v170\n"
"v_mfma_f32_16x16x32_bf16 v[248:251], v[248:251], v[132:135], 0\n"
"s_add_u32 m0, s34, 4096\n"
"buffer_load_dwordx2 v[66:67], v193, s[28:31], s38 offen offset:1024 sc1\n"
"v_fmamk_f32 v122, v122, 0x3e553b95, v170\n"
"v_mfma_f32_16x16x32_bf16 v[244:247], v[244:247], v[132:135], 0\n"
"s_add_u32 s38, s38, 6144\n"
"v_fmamk_f32 v123, v123, 0x3e553b95, v170\n"
"v_fmamk_f32 v128, v128, 0x3e553b95, v170\n"
"v_mfma_f32_16x16x32_bf16 v[252:255], v[252:255], v[132:135], 0\n"
"v_exp_f32 v120, v120\n"
"v_mfma_f32_16x16x32_bf16 v[224:227], a[72:75], v[32:35], 0\n"
"v_exp_f32 v121, v121\n"
"v_mfma_f32_16x16x32_bf16 v[208:211], a[76:79], v[32:35], 0\n"
"buffer_load_dwordx4 v191, s[8:11], s76 offen lds\n"
"ds_read_b64_tr_b16 v[132:133], v182 offset:128\n"
"v_mfma_f32_16x16x32_bf16 v[204:207], a[80:83], v[32:35], 0\n"
"v_exp_f32 v122, v122\n"
"v_mfma_f32_16x16x32_bf16 v[224:227], a[84:87], v[36:39], v[224:227]\n"
"v_exp_f32 v123, v123\n"
"v_mfma_f32_16x16x32_bf16 v[208:211], a[88:91], v[36:39], v[208:211]\n"
"ds_read_b64_tr_b16 v[134:135], v183 offset:128\n"
"v_cvt_pk_bf16_f32 v84, v120, v121\n"
"v_mfma_f32_16x16x32_bf16 v[204:207], a[92:95], v[36:39], v[204:207]\n"
"v_cvt_pk_bf16_f32 v85, v122, v123\n"
"v_and_b32 v120, v84, v176\n"
"v_mfma_f32_16x16x32_bf16 v[224:227], a[96:99], v[40:43], v[224:227]\n"
"v_and_b32 v121, v85, v177\n"
"v_xor_b32 v122, v84, v120\n"
"v_mfma_f32_16x16x32_bf16 v[208:211], a[100:103], v[40:43], v[208:211]\n"
"v_xor_b32 v123, v85, v121\n"
"v_fmamk_f32 v129, v129, 0x3e553b95, v170\n"
"v_mfma_f32_16x16x32_bf16 v[82:85], v[172:175], v[82:85], 0\n"
"v_fmamk_f32 v130, v130, 0x3e553b95, v170\n"
"v_fmamk_f32 v131, v131, 0x3e553b95, v170\n"
"v_mfma_f32_16x16x32_bf16 v[248:251], v[140:143], v[120:123], v[248:251]\n"
"ds_read_b64_tr_b16 v[140:141], v182 offset:72\n"
"ds_read_b64_tr_b16 v[142:143], v183 offset:72\n"
"v_mfma_f32_16x16x32_bf16 v[244:247], v[112:115], v[120:123], v[244:247]\n"
"v_exp_f32 v128, v128\n"
"v_mfma_f32_16x16x32_bf16 v[252:255], v[116:119], v[120:123], v[252:255]\n"
"ds_read_b64_tr_b16 v[120:121], v182 offset:136\n"
"ds_read_b64_tr_b16 v[122:123], v183 offset:136\n"
"v_mfma_f32_16x16x32_bf16 v[204:207], a[104:107], v[40:43], v[204:207]\n"
"s_add_u32 m0, s34, 2048\n"
"v_exp_f32 v129, v129\n"
"v_mfma_f32_16x16x32_bf16 v[224:227], a[108:111], v[44:47], v[224:227]\n"
"buffer_load_dwordx4 v190, s[8:11], s1 offen lds\n"
"v_cvt_pk_bf16_f32 v90, v128, v129\n"
"v_mfma_f32_16x16x32_bf16 v[208:211], a[112:115], v[44:47], v[208:211]\n"
"s_add_u32 s1, s1, s33\n"
"v_exp_f32 v130, v130\n"
"v_mfma_f32_16x16x32_bf16 v[204:207], a[116:119], v[44:47], v[204:207]\n"
"v_exp_f32 v131, v131\n"
"v_mfma_f32_16x16x32_bf16 v[224:227], a[120:123], v[48:51], v[224:227]\n"
"s_add_u32 m0, s34, 5120\n"
"v_cvt_pk_bf16_f32 v91, v130, v131\n"
"v_and_b32 v128, v90, v176\n"
"v_mfma_f32_16x16x32_bf16 v[208:211], a[124:127], v[48:51], v[208:211]\n"
"buffer_load_dwordx4 v191, s[8:11], s77 offen lds\n"
"v_and_b32 v129, v91, v177\n"
"v_mfma_f32_16x16x32_bf16 v[204:207], a[128:131], v[48:51], v[204:207]\n"
"v_xor_b32 v130, v90, v128\n"
"v_xor_b32 v131, v91, v129\n"
"v_mfma_f32_16x16x32_bf16 v[224:227], a[132:135], v[52:55], v[224:227]\n"
"s_waitcnt lgkmcnt(9)\n"
"v_fmamk_f32 v136, v136, 0x3e553b95, v170\n"
"v_fmamk_f32 v137, v137, 0x3e553b95, v170\n"
"v_mfma_f32_16x16x32_bf16 v[248:251], v[124:127], v[128:131], v[248:251]\n"
"v_fmamk_f32 v138, v138, 0x3e553b95, v170\n"
"v_fmamk_f32 v139, v139, 0x3e553b95, v170\n"
"v_mfma_f32_16x16x32_bf16 v[244:247], v[98:101], v[128:131], v[244:247]\n"
"v_exp_f32 v136, v136\n"
"v_mfma_f32_16x16x32_bf16 v[252:255], v[106:109], v[128:131], v[252:255]\n"
"v_exp_f32 v137, v137\n"
"v_mfma_f32_16x16x32_bf16 v[208:211], a[136:139], v[52:55], v[208:211]\n"
"v_exp_f32 v138, v138\n"
"v_mfma_f32_16x16x32_bf16 v[204:207], a[140:143], v[52:55], v[204:207]\n"
"v_exp_f32 v139, v139\n"
"v_mfma_f32_16x16x32_bf16 v[212:215], a[144:147], v[32:35], 0\n"
"v_cvt_pk_bf16_f32 v92, v136, v137\n"
"v_cvt_pk_bf16_f32 v93, v138, v139\n"
"v_mfma_f32_16x16x32_bf16 v[212:215], a[156:159], v[36:39], v[212:215]\n"
"v_and_b32 v136, v92, v176\n"
"v_and_b32 v137, v93, v177\n"
"v_mfma_f32_16x16x32_bf16 v[212:215], a[168:171], v[40:43], v[212:215]\n"
"v_xor_b32 v138, v92, v136\n"
"v_xor_b32 v139, v93, v137\n"
"v_mfma_f32_16x16x32_bf16 v[90:93], v[172:175], v[90:93], v[82:85]\n"
"s_waitcnt lgkmcnt(0)\n"
"ds_read_b128 v[112:115], v180 offset:13504\n"
"ds_read_b128 v[116:119], v181 offset:13504\n"
"v_mfma_f32_16x16x32_bf16 v[248:251], v[132:135], v[136:139], v[248:251]\n"
"ds_read_b64 v[82:83], v180 offset:13568\n"
"v_cvt_pk_bf16_f32 v233, v234, v235\n"
"v_mfma_f32_16x16x32_bf16 v[244:247], v[120:123], v[136:139], v[244:247]\n"
"ds_read_b64 v[84:85], v181 offset:13568\n"
"v_cvt_pk_bf16_f32 v234, v236, v237\n"
"v_mfma_f32_16x16x32_bf16 v[252:255], v[140:143], v[136:139], v[252:255]\n"
"ds_read_b128 v[128:131], v180 offset:27008\n"
"v_cvt_pk_bf16_f32 v235, v238, v239\n"
"v_mfma_f32_16x16x32_bf16 v[236:239], a[220:223], v[68:71], 0\n"
"buffer_store_dwordx4 v[232:235], v194, s[24:27], s36 offen nt\n"
"s_nop 1\n"
"v_mfma_f32_16x16x32_bf16 v[232:235], a[216:219], v[68:71], 0\n"
"ds_read_b128 v[124:127], v181 offset:27008\n"
"ds_read_b64 v[92:93], v181 offset:27072\n"
"v_mfma_f32_16x16x32_bf16 v[232:235], a[228:231], v[72:75], v[232:235]\n"
"v_rcp_f32 v171, v90\n"
"v_mfma_f32_16x16x32_bf16 v[236:239], a[232:235], v[72:75], v[236:239]\n"
"s_waitcnt lgkmcnt(6)\n"
"ds_read_b64 v[90:91], v180 offset:27072\n"
"ds_read_b128 v[136:139], v178\n"
"v_mfma_f32_16x16x32_bf16 v[112:115], v[112:115], v[148:151], 0\n"
"s_waitcnt lgkmcnt(7)\n"
"v_cvt_pk_bf16_f32 v240, v240, v241\n"
"v_cvt_pk_bf16_f32 v241, v242, v243\n"
"v_mfma_f32_16x16x32_bf16 v[116:119], v[116:119], v[148:151], 0\n"
"s_waitcnt lgkmcnt(6)\n"
"ds_read_b128 v[132:135], v179\n"
"ds_read_b64 v[98:99], v178 offset:64\n"
"v_mfma_f32_16x16x32_bf16 v[112:115], v[80:83], v[160:163], v[112:115]\n"
"s_waitcnt lgkmcnt(7)\n"
"ds_read_b64 v[100:101], v179 offset:64\n"
"v_mul_f32 v248, v171, v248\n"
"v_mfma_f32_16x16x32_bf16 v[116:119], v[84:87], v[162:165], v[116:119]\n"
"s_waitcnt lgkmcnt(7)\n"
"v_mul_f32 v249, v171, v249\n"
"v_mul_f32 v250, v171, v250\n"
"v_mfma_f32_16x16x32_bf16 v[128:131], v[128:131], v[148:151], 0\n"
"ds_read_b128 v[120:123], v178 offset:13504\n"
"v_mul_f32 v251, v171, v251\n"
"v_mfma_f32_16x16x32_bf16 v[232:235], a[240:243], v[76:79], v[232:235]\n"
"s_waitcnt lgkmcnt(7)\n"
"v_cvt_pk_bf16_f32 v248, v248, v249\n"
"v_cvt_pk_bf16_f32 v249, v250, v251\n"
"v_mfma_f32_16x16x32_bf16 v[124:127], v[124:127], v[148:151], 0\n"
"s_waitcnt lgkmcnt(6)\n"
"ds_read_b128 v[140:143], v179 offset:13504\n"
"v_mul_f32 v244, v171, v244\n"
"v_mfma_f32_16x16x32_bf16 v[124:127], v[92:95], v[162:165], v[124:127]\n"
"s_waitcnt lgkmcnt(6)\n"
"ds_read_b64 v[106:107], v178 offset:13568\n"
"v_mul_f32 v245, v171, v245\n"
"v_mfma_f32_16x16x32_bf16 v[128:131], v[88:91], v[160:163], v[128:131]\n"
"s_waitcnt lgkmcnt(6)\n"
"v_mul_f32 v246, v171, v246\n"
"v_mul_f32 v247, v171, v247\n"
"v_mfma_f32_16x16x32_bf16 v[136:139], v[136:139], v[148:151], 0\n"
"s_waitcnt lgkmcnt(5)\n"
"ds_read_b64 v[108:109], v179 offset:13568\n"
"v_cvt_pk_bf16_f32 v250, v244, v245\n"
"v_mfma_f32_16x16x32_bf16 v[132:135], v[132:135], v[148:151], 0\n"
"s_waitcnt lgkmcnt(5)\n"
"v_cvt_pk_bf16_f32 v251, v246, v247\n"
"v_mul_f32 v252, v171, v252\n"
"v_mfma_f32_16x16x32_bf16 v[136:139], v[96:99], v[160:163], v[136:139]\n"
"s_waitcnt lgkmcnt(4)\n"
"buffer_store_dwordx4 v[248:251], v192, s[28:31], s37 offen sc1\n"
"v_mfma_f32_16x16x32_bf16 v[132:135], v[100:103], v[162:165], v[132:135]\n"
"s_waitcnt vmcnt(11) lgkmcnt(3)\n"
"v_mul_f32 v253, v171, v253\n"
"v_mul_f32 v254, v171, v254\n"
"v_mfma_f32_16x16x32_bf16 v[120:123], v[120:123], v[148:151], 0\n"
"s_waitcnt lgkmcnt(2)\n"
"ds_write_b128 v202, v[56:59] offset:55296\n"
"v_mfma_f32_16x16x32_bf16 v[140:143], v[140:143], v[148:151], 0\n"
"s_waitcnt lgkmcnt(2)\n"
"buffer_store_dwordx2 v[240:241], v195, s[24:27], s36 offen nt\n"
"v_mfma_f32_16x16x32_bf16 v[120:123], v[104:107], v[160:163], v[120:123]\n"
"s_waitcnt lgkmcnt(1)\n"
"v_mul_f32 v255, v171, v255\n"
"v_cvt_pk_bf16_f32 v252, v252, v253\n"
"v_mfma_f32_16x16x32_bf16 v[140:143], v[108:111], v[162:165], v[140:143]\n"
"s_add_u32 s36, s36, s33\n"
"v_cvt_pk_bf16_f32 v253, v254, v255\n"
"v_cvt_pk_bf16_f32 v228, v228, v229\n"
"v_mfma_f32_16x16x32_bf16 v[240:243], a[224:227], v[68:71], 0\n"
"ds_read_b128 v[68:71], v192 offset:33792\n"
"ds_read_b64_tr_b16 v[244:245], v184 offset:13632\n"
"v_mfma_f32_16x16x32_bf16 v[240:243], a[236:239], v[72:75], v[240:243]\n"
"ds_read_b128 v[72:75], v192 offset:34816\n"
"v_cvt_pk_bf16_f32 v229, v230, v231\n"
"v_mfma_f32_16x16x32_bf16 v[236:239], a[244:247], v[76:79], v[236:239]\n"
"ds_read_b64_tr_b16 v[246:247], v185 offset:13632\n"
"v_cvt_pk_bf16_f32 v230, v216, v217\n"
"v_mfma_f32_16x16x32_bf16 v[240:243], a[248:251], v[76:79], v[240:243]\n"
"ds_read_b128 v[76:79], v192 offset:35840\n"
"v_cvt_pk_bf16_f32 v231, v218, v219\n"
"v_mfma_f32_16x16x32_bf16 v[216:219], a[148:151], v[32:35], 0\n"
"ds_write_b128 v186, v[228:231] offset:33760\n"
"v_mfma_f32_16x16x32_bf16 v[228:231], a[152:155], v[32:35], 0\n"
"v_cvt_pk_bf16_f32 v220, v220, v221\n"
"v_cvt_pk_bf16_f32 v221, v222, v223\n"
"v_mfma_f32_16x16x32_bf16 v[216:219], a[160:163], v[36:39], v[216:219]\n"
"v_max_f32 v112, v112, v116\n"
"v_max_f32 v113, v113, v117\n"
"v_mfma_f32_16x16x32_bf16 v[228:231], a[164:167], v[36:39], v[228:231]\n"
"s_waitcnt lgkmcnt(5)\n"
"ds_read_b128 v[32:35], v188 offset:6144\n"
"ds_read_b128 v[36:39], v189 offset:6144\n"
"v_mfma_f32_16x16x32_bf16 v[232:235], a[252:255], v[68:71], v[232:235]\n"
"ds_read_b64_tr_b16 v[248:249], v184 offset:13640\n"
"v_max_f32 v114, v114, v118\n"
"v_mfma_f32_16x16x32_bf16 v[216:219], a[172:175], v[40:43], v[216:219]\n"
"s_waitcnt lgkmcnt(6)\n"
"ds_read_b64_tr_b16 v[250:251], v185 offset:13640\n"
"v_max_f32 v115, v115, v119\n"
"v_mfma_f32_16x16x32_bf16 v[228:231], a[176:179], v[40:43], v[228:231]\n"
"ds_read_b128 v[40:43], v188 offset:7168\n"
"v_max_f32 v170, v112, v113\n"
"v_mfma_f32_16x16x32_bf16 v[236:239], v[0:3], v[68:71], v[236:239]\n"
"buffer_store_dwordx2 v[252:253], v193, s[28:31], s37 offen offset:1024 sc1\n"
"v_mfma_f32_16x16x32_bf16 v[212:215], a[180:183], v[44:47], v[212:215]\n"
"s_add_u32 s37, s37, 6144\n"
"ds_read_b64_tr_b16 v[252:253], v184 offset:13576\n"
"ds_read_b64_tr_b16 v[254:255], v185 offset:13576\n"
"v_mfma_f32_16x16x32_bf16 v[216:219], a[184:187], v[44:47], v[216:219]\n"
"ds_read_b64_tr_b16 v[116:117], v184 offset:27136\n"
"v_max_f32 v171, v114, v115\n"
"v_mfma_f32_16x16x32_bf16 v[228:231], a[188:191], v[44:47], v[228:231]\n"
"ds_read_b128 v[44:47], v189 offset:7168\n"
"v_max_f32 v128, v128, v124\n"
"v_mfma_f32_16x16x32_bf16 v[240:243], v[4:7], v[68:71], v[240:243]\n"
"ds_read_b64_tr_b16 v[118:119], v185 offset:27136\n"
"v_max_f32 v129, v129, v125\n"
"v_mfma_f32_16x16x32_bf16 v[212:215], a[192:195], v[48:51], v[212:215]\n"
"ds_read_b64_tr_b16 v[124:125], v184 offset:27144\n"
"v_max_f32 v130, v130, v126\n"
"v_mfma_f32_16x16x32_bf16 v[216:219], a[196:199], v[48:51], v[216:219]\n"
"ds_write_b64 v203, v[60:61] offset:55296\n"
"v_mfma_f32_16x16x32_bf16 v[228:231], a[200:203], v[48:51], v[228:231]\n"
"v_max_f32 v131, v131, v127\n"
"v_max3_f32 v170, v170, v128, v129\n"
"v_mfma_f32_16x16x32_bf16 v[232:235], v[8:11], v[72:75], v[232:235]\n"
"s_waitcnt lgkmcnt(13)\n"
"ds_read_b128 v[48:51], v188 offset:8192\n"
"ds_read_b64_tr_b16 v[126:127], v185 offset:27144\n"
"v_mfma_f32_16x16x32_bf16 v[212:215], a[204:207], v[52:55], v[212:215]\n"
"ds_read_b64_tr_b16 v[98:99], v182 offset:136\n"
"v_max3_f32 v171, v171, v130, v131\n"
"v_mfma_f32_16x16x32_bf16 v[216:219], a[208:211], v[52:55], v[216:219]\n"
"ds_read_b64_tr_b16 v[100:101], v183 offset:136\n"
"v_max_f32 v136, v136, v132\n"
"v_mfma_f32_16x16x32_bf16 v[236:239], v[12:15], v[72:75], v[236:239]\n"
"ds_read_b64_tr_b16 v[106:107], v182 offset:72\n"
"v_max_f32 v137, v137, v133\n"
"v_mfma_f32_16x16x32_bf16 v[228:231], a[212:215], v[52:55], v[228:231]\n"
"ds_read_b128 v[52:55], v189 offset:8192\n"
"v_max_f32 v138, v138, v134\n"
"v_mfma_f32_16x16x32_bf16 v[240:243], v[16:19], v[72:75], v[240:243]\n"
"ds_read_b64_tr_b16 v[132:133], v184 offset:27080\n"
"v_max_f32 v139, v139, v135\n"
"v_mfma_f32_16x16x32_bf16 v[232:235], v[20:23], v[76:79], v[232:235]\n"
"ds_read_b64_tr_b16 v[134:135], v185 offset:27080\n"
"v_max3_f32 v170, v170, v136, v137\n"
"v_mfma_f32_16x16x32_bf16 v[236:239], v[24:27], v[76:79], v[236:239]\n"
"ds_read_b64_tr_b16 v[108:109], v183 offset:72\n"
"v_max3_f32 v171, v171, v138, v139\n"
"v_mfma_f32_16x16x32_bf16 v[240:243], v[28:31], v[76:79], v[240:243]\n"
"v_max_f32 v120, v120, v140\n"
"v_max_f32 v121, v121, v141\n"
"v_max_f32 v122, v122, v142\n"
"v_max_f32 v123, v123, v143\n"
"v_max3_f32 v170, v170, v120, v121\n"
"v_cvt_pk_bf16_f32 v224, v224, v225\n"
"v_max3_f32 v171, v171, v122, v123\n"
"v_cvt_pk_bf16_f32 v225, v226, v227\n"
"v_cvt_pk_bf16_f32 v226, v208, v209\n"
"v_cvt_pk_bf16_f32 v227, v210, v211\n"
"v_cvt_pk_bf16_f32 v222, v204, v205\n"
"v_cvt_pk_bf16_f32 v223, v206, v207\n"
"v_max_f32 v170, v170, v171\n"
"v_mov_b32 v171, v170\n"
"v_cvt_pk_bf16_f32 v148, v212, v213\n"
"v_cvt_pk_bf16_f32 v149, v214, v215\n"
"v_permlane16_swap_b32 v170, v171\n"
"v_max_f32 v170, v170, v171\n"
"v_mov_b32 v171, v170\n"
"v_cvt_pk_bf16_f32 v150, v216, v217\n"
"v_cvt_pk_bf16_f32 v151, v218, v219\n"
"v_permlane32_swap_b32 v170, v171\n"
"v_max_f32 v170, v170, v171\n"
"v_mul_f32 v170, 0xbe553b95, v170\n"
"v_fmamk_f32 v112, v112, 0x3e553b95, v170\n"
"v_fmamk_f32 v113, v113, 0x3e553b95, v170\n"
"v_fmamk_f32 v114, v114, 0x3e553b95, v170\n"
"v_fmamk_f32 v115, v115, 0x3e553b95, v170\n"
"v_exp_f32 v112, v112\n"
"v_exp_f32 v113, v113\n"
"v_exp_f32 v114, v114\n"
"v_exp_f32 v115, v115\n"
"v_cvt_pk_bf16_f32 v82, v112, v113\n"
"v_cvt_pk_bf16_f32 v83, v114, v115\n"
"v_and_b32 v112, v82, v176\n"
"v_and_b32 v113, v83, v177\n"
"v_xor_b32 v114, v82, v112\n"
"v_xor_b32 v115, v83, v113\n"
"v_fmamk_f32 v128, v128, 0x3e553b95, v170\n"
"v_fmamk_f32 v129, v129, 0x3e553b95, v170\n"
"v_mfma_f32_16x16x32_bf16 v[244:247], v[244:247], v[112:115], 0\n"
"s_waitcnt lgkmcnt(14)\n"
"buffer_load_dword v199, off, s[28:31], s39 sc1\n"
"ds_read_b64_tr_b16 v[140:141], v182 offset:128\n"
"v_mfma_f32_16x16x32_bf16 v[248:251], v[248:251], v[112:115], 0\n"
"ds_read_b64_tr_b16 v[142:143], v183 offset:128\n"
"ds_write_b128 v186, v[220:223] offset:33824\n"
"v_mfma_f32_16x16x32_bf16 v[252:255], v[252:255], v[112:115], 0\n"
"v_fmamk_f32 v130, v130, 0x3e553b95, v170\n"
"v_fmamk_f32 v131, v131, 0x3e553b95, v170\n"
"v_mfma_f32_16x16x32_bf16 v[208:211], a[0:3], v[32:35], 0\n"
"v_exp_f32 v128, v128\n"
"v_mfma_f32_16x16x32_bf16 v[204:207], a[4:7], v[32:35], 0\n"
"ds_write_b128 v186, v[224:227] offset:33888\n"
"v_mfma_f32_16x16x32_bf16 v[220:223], a[8:11], v[32:35], 0\n"
"v_exp_f32 v129, v129\n"
"v_mfma_f32_16x16x32_bf16 v[208:211], a[12:15], v[36:39], v[208:211]\n"
"v_exp_f32 v130, v130\n"
"v_mfma_f32_16x16x32_bf16 v[204:207], a[16:19], v[36:39], v[204:207]\n"
"v_exp_f32 v131, v131\n"
"v_mfma_f32_16x16x32_bf16 v[220:223], a[20:23], v[36:39], v[220:223]\n"
"ds_read_b64_tr_b16 v[112:113], v182 offset:13632\n"
"ds_read_b64_tr_b16 v[114:115], v183 offset:13632\n"
"v_mfma_f32_16x16x32_bf16 v[208:211], a[24:27], v[40:43], v[208:211]\n"
"s_waitcnt lgkmcnt(15)\n"
"v_cvt_pk_bf16_f32 v84, v128, v129\n"
"v_cvt_pk_bf16_f32 v85, v130, v131\n"
"v_mfma_f32_16x16x32_bf16 v[204:207], a[28:31], v[40:43], v[204:207]\n"
"v_and_b32 v128, v84, v176\n"
"v_and_b32 v129, v85, v177\n"
"v_mfma_f32_16x16x32_bf16 v[220:223], a[32:35], v[40:43], v[220:223]\n"
"v_xor_b32 v130, v84, v128\n"
"v_xor_b32 v131, v85, v129\n"
"v_mfma_f32_16x16x32_bf16 v[82:85], v[172:175], v[82:85], 0\n"
"s_waitcnt lgkmcnt(2)\n"
"v_fmamk_f32 v136, v136, 0x3e553b95, v170\n"
"v_fmamk_f32 v137, v137, 0x3e553b95, v170\n"
"v_mfma_f32_16x16x32_bf16 v[244:247], v[116:119], v[128:131], v[244:247]\n"
"s_barrier\n"
"v_mfma_f32_16x16x32_bf16 v[248:251], v[124:127], v[128:131], v[248:251]\n"
"s_waitcnt vmcnt(8)\n"
"ds_read_b128 v[68:71], v192 offset:55296\n"
"ds_read_b128 v[72:75], v192 offset:56320\n"
"v_mfma_f32_16x16x32_bf16 v[252:255], v[132:135], v[128:131], v[252:255]\n"
"ds_read_b128 v[76:79], v192 offset:57344\n"
"v_fmamk_f32 v138, v138, 0x3e553b95, v170\n"
"v_mfma_f32_16x16x32_bf16 v[208:211], a[36:39], v[44:47], v[208:211]\n"
"ds_read_b64_tr_b16 v[128:129], v182 offset:13640\n"
"v_fmamk_f32 v139, v139, 0x3e553b95, v170\n"
"v_mfma_f32_16x16x32_bf16 v[204:207], a[40:43], v[44:47], v[204:207]\n"
"ds_read_b64_tr_b16 v[130:131], v183 offset:13640\n"
"v_fmamk_f32 v120, v120, 0x3e553b95, v170\n"
"v_mfma_f32_16x16x32_bf16 v[220:223], a[44:47], v[44:47], v[220:223]\n"
"ds_read_b64_tr_b16 v[116:117], v182 offset:13576\n"
"v_fmamk_f32 v121, v121, 0x3e553b95, v170\n"
"v_mfma_f32_16x16x32_bf16 v[208:211], a[48:51], v[48:51], v[208:211]\n"
"ds_read_b64_tr_b16 v[118:119], v183 offset:13576\n"
"v_fmamk_f32 v122, v122, 0x3e553b95, v170\n"
"v_mfma_f32_16x16x32_bf16 v[204:207], a[52:55], v[48:51], v[204:207]\n"
"v_exp_f32 v136, v136\n"
"v_mfma_f32_16x16x32_bf16 v[220:223], a[56:59], v[48:51], v[220:223]\n"
"v_exp_f32 v137, v137\n"
"v_mfma_f32_16x16x32_bf16 v[208:211], a[60:63], v[52:55], v[208:211]\n"
"v_exp_f32 v138, v138\n"
"v_mfma_f32_16x16x32_bf16 v[204:207], a[64:67], v[52:55], v[204:207]\n"
"v_exp_f32 v139, v139\n"
"v_mfma_f32_16x16x32_bf16 v[220:223], a[68:71], v[52:55], v[220:223]\n"
".Lic6%=:\n"
"s_cbranch_execz .Lib1%=\n"
"v_cvt_pk_bf16_f32 v90, v136, v137\n"
"v_cvt_pk_bf16_f32 v91, v138, v139\n"
"v_mfma_f32_16x16x32_bf16 v[224:227], a[72:75], v[32:35], 0\n"
"v_and_b32 v136, v90, v176\n"
"v_and_b32 v137, v91, v177\n"
"v_mfma_f32_16x16x32_bf16 v[212:215], a[76:79], v[32:35], 0\n"
"v_xor_b32 v138, v90, v136\n"
"v_xor_b32 v139, v91, v137\n"
"v_mfma_f32_16x16x32_bf16 v[216:219], a[80:83], v[32:35], 0\n"
"ds_read_b128 v[32:35], v188\n"
"v_fmamk_f32 v123, v123, 0x3e553b95, v170\n"
"v_mfma_f32_16x16x32_bf16 v[244:247], v[140:143], v[136:139], v[244:247]\n"
"v_exp_f32 v120, v120\n"
"v_mfma_f32_16x16x32_bf16 v[248:251], v[98:101], v[136:139], v[248:251]\n"
"v_exp_f32 v121, v121\n"
"v_mfma_f32_16x16x32_bf16 v[252:255], v[106:109], v[136:139], v[252:255]\n"
"v_cvt_pk_bf16_f32 v162, v228, v229\n"
"v_cvt_pk_bf16_f32 v163, v230, v231\n"
"v_mfma_f32_16x16x32_bf16 v[224:227], a[84:87], v[36:39], v[224:227]\n"
"v_exp_f32 v122, v122\n"
"v_mfma_f32_16x16x32_bf16 v[212:215], a[88:91], v[36:39], v[212:215]\n"
"v_exp_f32 v123, v123\n"
"v_mfma_f32_16x16x32_bf16 v[216:219], a[92:95], v[36:39], v[216:219]\n"
"ds_read_b128 v[36:39], v189\n"
"v_cvt_pk_bf16_f32 v92, v120, v121\n"
"v_mfma_f32_16x16x32_bf16 v[224:227], a[96:99], v[40:43], v[224:227]\n"
"v_cvt_pk_bf16_f32 v93, v122, v123\n"
"v_and_b32 v120, v92, v176\n"
"v_mfma_f32_16x16x32_bf16 v[212:215], a[100:103], v[40:43], v[212:215]\n"
"s_waitcnt lgkmcnt(1)\n"
"v_and_b32 v121, v93, v177\n"
"v_xor_b32 v122, v92, v120\n"
"v_mfma_f32_16x16x32_bf16 v[216:219], a[104:107], v[40:43], v[216:219]\n"
"s_waitcnt vmcnt(7)\n"
"ds_read_b128 v[40:43], v188 offset:1024\n"
"v_xor_b32 v123, v93, v121\n"
"v_mfma_f32_16x16x32_bf16 v[90:93], v[172:175], v[90:93], v[82:85]\n"
"ds_read_b128 v[124:127], v180 offset:27008\n"
"ds_read_b128 v[132:135], v181 offset:27008\n"
"v_mfma_f32_16x16x32_bf16 v[244:247], v[112:115], v[120:123], v[244:247]\n"
"ds_read_b64 v[82:83], v180 offset:27072\n"
"v_cvt_pk_bf16_f32 v232, v232, v233\n"
"v_mfma_f32_16x16x32_bf16 v[248:251], v[128:131], v[120:123], v[248:251]\n"
"ds_read_b64 v[84:85], v181 offset:27072\n"
"v_cvt_pk_bf16_f32 v233, v234, v235\n"
"v_mfma_f32_16x16x32_bf16 v[252:255], v[116:119], v[120:123], v[252:255]\n"
"ds_read_b128 v[136:139], v178\n"
"v_cvt_pk_bf16_f32 v234, v236, v237\n"
"v_mfma_f32_16x16x32_bf16 v[224:227], a[108:111], v[44:47], v[224:227]\n"
"ds_read_b128 v[140:143], v179\n"
"v_cvt_pk_bf16_f32 v235, v238, v239\n"
"v_mfma_f32_16x16x32_bf16 v[236:239], a[220:223], v[68:71], 0\n"
"buffer_store_dwordx4 v[232:235], v194, s[24:27], s36 offen nt\n"
"s_nop 1\n"
"v_mfma_f32_16x16x32_bf16 v[232:235], a[216:219], v[68:71], 0\n"
"ds_read_b64 v[92:93], v179 offset:64\n"
"ds_read_b128 v[120:123], v178 offset:13504\n"
"v_mfma_f32_16x16x32_bf16 v[232:235], a[228:231], v[72:75], v[232:235]\n"
"s_waitcnt lgkmcnt(7)\n"
"v_rcp_f32 v171, v90\n"
"v_mfma_f32_16x16x32_bf16 v[124:127], v[124:127], v[152:155], 0\n"
"s_waitcnt lgkmcnt(6)\n"
"ds_read_b64 v[90:91], v178 offset:64\n"
"ds_read_b128 v[112:115], v179 offset:13504\n"
"v_mfma_f32_16x16x32_bf16 v[132:135], v[132:135], v[152:155], 0\n"
"s_waitcnt lgkmcnt(7)\n"
"v_cvt_pk_bf16_f32 v240, v240, v241\n"
"v_cvt_pk_bf16_f32 v241, v242, v243\n"
"v_mfma_f32_16x16x32_bf16 v[124:127], v[80:83], v[164:167], v[124:127]\n"
"s_waitcnt lgkmcnt(6)\n"
"ds_read_b64 v[98:99], v178 offset:13568\n"
"ds_read_b64 v[100:101], v179 offset:13568\n"
"v_mfma_f32_16x16x32_bf16 v[132:135], v[84:87], v[166:169], v[132:135]\n"
"s_waitcnt lgkmcnt(7)\n"
"v_mul_f32 v244, v171, v244\n"
"v_mul_f32 v245, v171, v245\n"
"v_mfma_f32_16x16x32_bf16 v[136:139], v[136:139], v[152:155], 0\n"
"s_waitcnt lgkmcnt(6)\n"
"ds_write_b128 v202, v[62:65] offset:30720\n"
"v_mfma_f32_16x16x32_bf16 v[140:143], v[140:143], v[152:155], 0\n"
"s_waitcnt lgkmcnt(6)\n"
"v_mul_f32 v246, v171, v246\n"
"v_mul_f32 v247, v171, v247\n"
"v_mfma_f32_16x16x32_bf16 v[140:143], v[92:95], v[166:169], v[140:143]\n"
"s_waitcnt lgkmcnt(5)\n"
"v_cvt_pk_bf16_f32 v244, v244, v245\n"
"v_cvt_pk_bf16_f32 v245, v246, v247\n"
"v_mfma_f32_16x16x32_bf16 v[120:123], v[120:123], v[152:155], 0\n"
"s_waitcnt lgkmcnt(4)\n"
"ds_read_b128 v[128:131], v178 offset:27008\n"
"ds_read_b128 v[116:119], v179 offset:27008\n"
"v_mfma_f32_16x16x32_bf16 v[136:139], v[88:91], v[164:167], v[136:139]\n"
"s_waitcnt lgkmcnt(5)\n"
"v_mul_f32 v248, v171, v248\n"
"v_mul_f32 v249, v171, v249\n"
"v_mfma_f32_16x16x32_bf16 v[112:115], v[112:115], v[152:155], 0\n"
"s_waitcnt lgkmcnt(4)\n"
"ds_read_b64 v[106:107], v178 offset:27072\n"
"v_mul_f32 v250, v171, v250\n"
"v_mfma_f32_16x16x32_bf16 v[120:123], v[96:99], v[164:167], v[120:123]\n"
"s_waitcnt lgkmcnt(4)\n"
"ds_read_b64 v[108:109], v179 offset:27072\n"
"v_mul_f32 v251, v171, v251\n"
"v_mfma_f32_16x16x32_bf16 v[112:115], v[100:103], v[166:169], v[112:115]\n"
"buffer_store_dwordx2 v[240:241], v195, s[24:27], s36 offen nt\n"
"v_mfma_f32_16x16x32_bf16 v[240:243], a[224:227], v[68:71], 0\n"
"s_add_u32 s36, s36, s33\n"
"ds_read_b128 v[68:71], v192 offset:58368\n"
"v_cvt_pk_bf16_f32 v246, v248, v249\n"
"v_mfma_f32_16x16x32_bf16 v[236:239], a[232:235], v[72:75], v[236:239]\n"
"v_cvt_pk_bf16_f32 v247, v250, v251\n"
"v_mul_f32 v252, v171, v252\n"
"v_mfma_f32_16x16x32_bf16 v[240:243], a[236:239], v[72:75], v[240:243]\n"
"s_waitcnt lgkmcnt(4)\n"
"ds_read_b128 v[72:75], v192 offset:59392\n"
"v_mul_f32 v253, v171, v253\n"
"v_mfma_f32_16x16x32_bf16 v[212:215], a[112:115], v[44:47], v[212:215]\n"
"s_waitcnt lgkmcnt(4)\n"
"v_mul_f32 v254, v171, v254\n"
"v_mul_f32 v255, v171, v255\n"
"v_mfma_f32_16x16x32_bf16 v[128:131], v[128:131], v[152:155], 0\n"
"s_waitcnt lgkmcnt(3)\n"
"v_cvt_pk_bf16_f32 v252, v252, v253\n"
"v_cvt_pk_bf16_f32 v253, v254, v255\n"
"v_mfma_f32_16x16x32_bf16 v[116:119], v[116:119], v[152:155], 0\n"
"s_waitcnt lgkmcnt(2)\n"
"ds_read_b64_tr_b16 v[248:249], v184 offset:27136\n"
"ds_read_b64_tr_b16 v[250:251], v185 offset:27136\n"
"v_mfma_f32_16x16x32_bf16 v[128:131], v[104:107], v[164:167], v[128:131]\n"
"ds_read_b64_tr_b16 v[254:255], v185 offset:27080\n"
"v_cvt_pk_bf16_f32 v208, v208, v209\n"
"v_mfma_f32_16x16x32_bf16 v[116:119], v[108:111], v[166:169], v[116:119]\n"
"v_cvt_pk_bf16_f32 v209, v210, v211\n"
"v_cvt_pk_bf16_f32 v210, v204, v205\n"
"v_mfma_f32_16x16x32_bf16 v[232:235], a[240:243], v[76:79], v[232:235]\n"
"s_waitcnt lgkmcnt(4)\n"
"v_cvt_pk_bf16_f32 v211, v206, v207\n"
"v_cvt_pk_bf16_f32 v220, v220, v221\n"
"v_mfma_f32_16x16x32_bf16 v[216:219], a[116:119], v[44:47], v[216:219]\n"
"ds_read_b128 v[44:47], v189 offset:1024\n"
"ds_write_b128 v186, v[208:211] offset:47264\n"
"v_mfma_f32_16x16x32_bf16 v[224:227], a[120:123], v[48:51], v[224:227]\n"
"v_cvt_pk_bf16_f32 v221, v222, v223\n"
"v_max_f32 v124, v124, v132\n"
"v_mfma_f32_16x16x32_bf16 v[236:239], a[244:247], v[76:79], v[236:239]\n"
"v_max_f32 v125, v125, v133\n"
"v_max_f32 v126, v126, v134\n"
"v_mfma_f32_16x16x32_bf16 v[212:215], a[124:127], v[48:51], v[212:215]\n"
"ds_read_b64_tr_b16 v[132:133], v182 offset:128\n"
"ds_write_b64 v203, v[66:67] offset:30720\n"
"v_mfma_f32_16x16x32_bf16 v[240:243], a[248:251], v[76:79], v[240:243]\n"
"buffer_store_dwordx4 v[244:247], v192, s[28:31], s37 offen sc1\n"
"v_mfma_f32_16x16x32_bf16 v[216:219], a[128:131], v[48:51], v[216:219]\n"
"s_waitcnt vmcnt(8)\n"
"ds_read_b128 v[76:79], v192 offset:60416\n"
"ds_read_b128 v[48:51], v188 offset:2048\n"
"v_mfma_f32_16x16x32_bf16 v[224:227], a[132:135], v[52:55], v[224:227]\n"
"ds_read_b64_tr_b16 v[244:245], v184 offset:27144\n"
"v_max_f32 v127, v127, v135\n"
"v_mfma_f32_16x16x32_bf16 v[212:215], a[136:139], v[52:55], v[212:215]\n"
"buffer_store_dwordx2 v[252:253], v193, s[28:31], s37 offen offset:1024 sc1\n"
"v_mfma_f32_16x16x32_bf16 v[232:235], a[252:255], v[68:71], v[232:235]\n"
"s_waitcnt lgkmcnt(10)\n"
"ds_read_b64_tr_b16 v[246:247], v185 offset:27144\n"
"ds_read_b64_tr_b16 v[252:253], v184 offset:27080\n"
"v_mfma_f32_16x16x32_bf16 v[216:219], a[140:143], v[52:55], v[216:219]\n"
"s_waitcnt vmcnt(5)\n"
"ds_read_b128 v[52:55], v189 offset:2048\n"
"v_max_f32 v170, v124, v125\n"
"v_mfma_f32_16x16x32_bf16 v[236:239], v[0:3], v[68:71], v[236:239]\n"
"s_add_u32 s37, s37, 6144\n"
"ds_read_b64_tr_b16 v[134:135], v183 offset:128\n"
"v_max_f32 v171, v126, v127\n"
"v_mfma_f32_16x16x32_bf16 v[240:243], v[4:7], v[68:71], v[240:243]\n"
"buffer_store_dword v198, v196, s[28:31], s40 offen sc1\n"
"v_mfma_f32_16x16x32_bf16 v[232:235], v[8:11], v[72:75], v[232:235]\n"
"s_waitcnt lgkmcnt(6)\n"
"v_add_u32 v198, 1, v198\n"
"v_max_f32 v136, v136, v140\n"
"v_mfma_f32_16x16x32_bf16 v[236:239], v[12:15], v[72:75], v[236:239]\n"
"ds_read_b64_tr_b16 v[98:99], v182 offset:13640\n"
"ds_read_b64_tr_b16 v[100:101], v183 offset:13640\n"
"v_mfma_f32_16x16x32_bf16 v[240:243], v[16:19], v[72:75], v[240:243]\n"
"ds_read_b64_tr_b16 v[106:107], v182 offset:13576\n"
"v_max_f32 v137, v137, v141\n"
"v_mfma_f32_16x16x32_bf16 v[232:235], v[20:23], v[76:79], v[232:235]\n"
"ds_read_b64_tr_b16 v[140:141], v182 offset:136\n"
"v_max_f32 v138, v138, v142\n"
"v_mfma_f32_16x16x32_bf16 v[236:239], v[24:27], v[76:79], v[236:239]\n"
"ds_read_b64_tr_b16 v[108:109], v183 offset:13576\n"
"v_max_f32 v139, v139, v143\n"
"v_mfma_f32_16x16x32_bf16 v[228:231], a[0:3], v[32:35], 0\n"
"ds_read_b64_tr_b16 v[142:143], v183 offset:136\n"
"v_max3_f32 v170, v170, v136, v137\n"
"v_mfma_f32_16x16x32_bf16 v[240:243], v[28:31], v[76:79], v[240:243]\n"
"v_max3_f32 v171, v171, v138, v139\n"
"v_max_f32 v120, v120, v112\n"
"v_mfma_f32_16x16x32_bf16 v[204:207], a[4:7], v[32:35], 0\n"
"v_max_f32 v121, v121, v113\n"
"v_max_f32 v122, v122, v114\n"
"v_mfma_f32_16x16x32_bf16 v[208:211], a[8:11], v[32:35], 0\n"
"ds_read_b64_tr_b16 v[112:113], v182 offset:72\n"
"v_max_f32 v123, v123, v115\n"
"v_mfma_f32_16x16x32_bf16 v[228:231], a[12:15], v[36:39], v[228:231]\n"
"s_waitcnt vmcnt(5)\n"
"ds_read_b64_tr_b16 v[114:115], v183 offset:72\n"
"v_max3_f32 v170, v170, v120, v121\n"
"v_mfma_f32_16x16x32_bf16 v[204:207], a[16:19], v[36:39], v[204:207]\n"
"v_add3_u32 v199, v199, v197, -8\n"
"v_cmp_lt_u32 vcc, 64, v199\n"
"v_mfma_f32_16x16x32_bf16 v[208:211], a[20:23], v[36:39], v[208:211]\n"
"v_max3_f32 v171, v171, v122, v123\n"
"v_max_f32 v128, v128, v116\n"
"v_mfma_f32_16x16x32_bf16 v[228:231], a[24:27], v[40:43], v[228:231]\n"
"s_nop 0\n"
"s_cbranch_vccnz .Lp7%=\n"
".Lr7%=:\n"
"v_mfma_f32_16x16x32_bf16 v[204:207], a[28:31], v[40:43], v[204:207]\n"
"buffer_load_dwordx4 v[56:59], v192, s[28:31], s38 offen sc1\n"
"v_max_f32 v129, v129, v117\n"
"v_mfma_f32_16x16x32_bf16 v[208:211], a[32:35], v[40:43], v[208:211]\n"
"ds_read_b64_tr_b16 v[116:117], v182 offset:13632\n"
"v_max_f32 v130, v130, v118\n"
"v_mfma_f32_16x16x32_bf16 v[228:231], a[36:39], v[44:47], v[228:231]\n"
"s_waitcnt lgkmcnt(14)\n"
"v_max_f32 v131, v131, v119\n"
"v_max3_f32 v170, v170, v128, v129\n"
"v_mfma_f32_16x16x32_bf16 v[204:207], a[40:43], v[44:47], v[204:207]\n"
"buffer_load_dwordx2 v[60:61], v193, s[28:31], s38 offen offset:1024 sc1\n"
"ds_read_b64_tr_b16 v[118:119], v183 offset:13632\n"
"v_mfma_f32_16x16x32_bf16 v[208:211], a[44:47], v[44:47], v[208:211]\n"
"s_add_u32 s38, s38, 6144\n"
"v_max3_f32 v171, v171, v130, v131\n"
"v_max_f32 v170, v170, v171\n"
"v_mfma_f32_16x16x32_bf16 v[228:231], a[48:51], v[48:51], v[228:231]\n"
"s_waitcnt lgkmcnt(11)\n"
"v_mov_b32 v171, v170\n"
"v_cvt_pk_bf16_f32 v224, v224, v225\n"
"v_mfma_f32_16x16x32_bf16 v[204:207], a[52:55], v[48:51], v[204:207]\n"
"v_permlane16_swap_b32 v170, v171\n"
"v_mfma_f32_16x16x32_bf16 v[208:211], a[56:59], v[48:51], v[208:211]\n"
"buffer_load_dwordx4 a[144:147], v192, s[28:31], s38 offen sc1\n"
"v_max_f32 v170, v170, v171\n"
"v_mfma_f32_16x16x32_bf16 v[228:231], a[60:63], v[52:55], v[228:231]\n"
"v_mov_b32 v171, v170\n"
"v_cvt_pk_bf16_f32 v225, v226, v227\n"
"v_mfma_f32_16x16x32_bf16 v[204:207], a[64:67], v[52:55], v[204:207]\n"
"v_permlane32_swap_b32 v170, v171\n"
"v_mfma_f32_16x16x32_bf16 v[208:211], a[68:71], v[52:55], v[208:211]\n"
"v_max_f32 v170, v170, v171\n"
"v_mul_f32 v170, 0xbe553b95, v170\n"
"v_fmamk_f32 v124, v124, 0x3e553b95, v170\n"
"v_fmamk_f32 v125, v125, 0x3e553b95, v170\n"
"buffer_load_dwordx2 a[148:149], v193, s[28:31], s38 offen offset:1024 sc1\n"
"v_fmamk_f32 v126, v126, 0x3e553b95, v170\n"
"v_fmamk_f32 v127, v127, 0x3e553b95, v170\n"
"v_exp_f32 v124, v124\n"
"v_exp_f32 v125, v125\n"
"v_exp_f32 v126, v126\n"
"v_exp_f32 v127, v127\n"
"v_cvt_pk_bf16_f32 v82, v124, v125\n"
"v_cvt_pk_bf16_f32 v83, v126, v127\n"
"v_and_b32 v124, v82, v176\n"
"v_and_b32 v125, v83, v177\n"
"v_xor_b32 v126, v82, v124\n"
"v_xor_b32 v127, v83, v125\n"
"v_fmamk_f32 v136, v136, 0x3e553b95, v170\n"
"v_fmamk_f32 v137, v137, 0x3e553b95, v170\n"
"v_mfma_f32_16x16x32_bf16 v[248:251], v[248:251], v[124:127], 0\n"
"s_add_u32 s38, s38, 6144\n"
"v_fmamk_f32 v138, v138, 0x3e553b95, v170\n"
"v_fmamk_f32 v139, v139, 0x3e553b95, v170\n"
"v_mfma_f32_16x16x32_bf16 v[244:247], v[244:247], v[124:127], 0\n"
"v_exp_f32 v136, v136\n"
"v_mfma_f32_16x16x32_bf16 v[252:255], v[252:255], v[124:127], 0\n"
"v_exp_f32 v137, v137\n"
"v_exp_f32 v138, v138\n"
"v_exp_f32 v139, v139\n"
"v_cvt_pk_bf16_f32 v84, v136, v137\n"
"v_cvt_pk_bf16_f32 v85, v138, v139\n"
"v_and_b32 v136, v84, v176\n"
"v_and_b32 v137, v85, v177\n"
"v_xor_b32 v138, v84, v136\n"
"v_xor_b32 v139, v85, v137\n"
"v_mfma_f32_16x16x32_bf16 v[82:85], v[172:175], v[82:85], 0\n"
"s_waitcnt lgkmcnt(2)\n"
"ds_read_b64_tr_b16 v[124:125], v182 offset:27136\n"
"ds_read_b64_tr_b16 v[126:127], v183 offset:27136\n"
"v_mfma_f32_16x16x32_bf16 v[248:251], v[132:135], v[136:139], v[248:251]\n"
"v_fmamk_f32 v120, v120, 0x3e553b95, v170\n"
"v_cvt_pk_bf16_f32 v226, v212, v213\n"
"v_mfma_f32_16x16x32_bf16 v[244:247], v[140:143], v[136:139], v[244:247]\n"
"v_fmamk_f32 v121, v121, 0x3e553b95, v170\n"
"v_fmamk_f32 v122, v122, 0x3e553b95, v170\n"
"v_mfma_f32_16x16x32_bf16 v[252:255], v[112:115], v[136:139], v[252:255]\n"
"v_cvt_pk_bf16_f32 v227, v214, v215\n"
"v_fmamk_f32 v123, v123, 0x3e553b95, v170\n"
"v_mfma_f32_16x16x32_bf16 v[212:215], a[72:75], v[32:35], 0\n"
"ds_write_b128 v186, v[224:227] offset:47392\n"
"v_mfma_f32_16x16x32_bf16 v[212:215], a[84:87], v[36:39], v[212:215]\n"
"v_exp_f32 v120, v120\n"
"v_mfma_f32_16x16x32_bf16 v[212:215], a[96:99], v[40:43], v[212:215]\n"
"v_cvt_pk_bf16_f32 v222, v216, v217\n"
"v_cvt_pk_bf16_f32 v223, v218, v219\n"
"v_mfma_f32_16x16x32_bf16 v[216:219], a[76:79], v[32:35], 0\n"
"ds_write_b128 v186, v[220:223] offset:47328\n"
"v_mfma_f32_16x16x32_bf16 v[220:223], a[80:83], v[32:35], 0\n"
"v_exp_f32 v121, v121\n"
"v_mfma_f32_16x16x32_bf16 v[216:219], a[88:91], v[36:39], v[216:219]\n"
"v_exp_f32 v122, v122\n"
"v_mfma_f32_16x16x32_bf16 v[220:223], a[92:95], v[36:39], v[220:223]\n"
"ds_read_b64_tr_b16 v[136:137], v182 offset:27144\n"
"ds_read_b64_tr_b16 v[138:139], v183 offset:27144\n"
"v_mfma_f32_16x16x32_bf16 v[216:219], a[100:103], v[40:43], v[216:219]\n"
"ds_read_b64_tr_b16 v[132:133], v182 offset:27080\n"
"v_cvt_pk_bf16_f32 v90, v120, v121\n"
"v_mfma_f32_16x16x32_bf16 v[220:223], a[104:107], v[40:43], v[220:223]\n"
"ds_read_b64_tr_b16 v[134:135], v183 offset:27080\n"
"v_and_b32 v120, v90, v176\n"
"v_mfma_f32_16x16x32_bf16 v[212:215], a[108:111], v[44:47], v[212:215]\n"
"v_exp_f32 v123, v123\n"
"v_mfma_f32_16x16x32_bf16 v[216:219], a[112:115], v[44:47], v[216:219]\n"
"v_cvt_pk_bf16_f32 v91, v122, v123\n"
"v_and_b32 v121, v91, v177\n"
"v_mfma_f32_16x16x32_bf16 v[220:223], a[116:119], v[44:47], v[220:223]\n"
"s_waitcnt lgkmcnt(4)\n"
"v_xor_b32 v122, v90, v120\n"
"v_xor_b32 v123, v91, v121\n"
"v_mfma_f32_16x16x32_bf16 v[212:215], a[120:123], v[48:51], v[212:215]\n"
"s_barrier\n"
"v_mfma_f32_16x16x32_bf16 v[248:251], v[116:119], v[120:123], v[248:251]\n"
"ds_read_b128 v[68:71], v192 offset:30720\n"
"ds_read_b128 v[72:75], v192 offset:31744\n"
"v_mfma_f32_16x16x32_bf16 v[244:247], v[98:101], v[120:123], v[244:247]\n"
"ds_read_b128 v[76:79], v192 offset:32768\n"
"v_fmamk_f32 v128, v128, 0x3e553b95, v170\n"
"v_mfma_f32_16x16x32_bf16 v[252:255], v[106:109], v[120:123], v[252:255]\n"
"v_fmamk_f32 v129, v129, 0x3e553b95, v170\n"
"v_fmamk_f32 v130, v130, 0x3e553b95, v170\n"
"v_mfma_f32_16x16x32_bf16 v[216:219], a[124:127], v[48:51], v[216:219]\n"
"v_fmamk_f32 v131, v131, 0x3e553b95, v170\n"
"v_exp_f32 v128, v128\n"
"v_mfma_f32_16x16x32_bf16 v[220:223], a[128:131], v[48:51], v[220:223]\n"
"v_exp_f32 v129, v129\n"
"v_mfma_f32_16x16x32_bf16 v[212:215], a[132:135], v[52:55], v[212:215]\n"
"v_exp_f32 v130, v130\n"
"v_mfma_f32_16x16x32_bf16 v[216:219], a[136:139], v[52:55], v[216:219]\n"
"v_exp_f32 v131, v131\n"
"v_mfma_f32_16x16x32_bf16 v[220:223], a[140:143], v[52:55], v[220:223]\n"
"v_cvt_pk_bf16_f32 v92, v128, v129\n"
"v_cvt_pk_bf16_f32 v93, v130, v131\n"
"v_and_b32 v128, v92, v176\n"
"v_and_b32 v129, v93, v177\n"
"v_xor_b32 v130, v92, v128\n"
"v_xor_b32 v131, v93, v129\n"
"v_mfma_f32_16x16x32_bf16 v[90:93], v[172:175], v[90:93], v[82:85]\n"
"s_waitcnt lgkmcnt(3)\n"
"ds_read_b128 v[140:143], v178\n"
"ds_read_b128 v[112:115], v179\n"
"v_mfma_f32_16x16x32_bf16 v[248:251], v[124:127], v[128:131], v[248:251]\n"
"ds_read_b64 v[82:83], v178 offset:64\n"
"v_cvt_pk_bf16_f32 v232, v232, v233\n"
"v_mfma_f32_16x16x32_bf16 v[244:247], v[136:139], v[128:131], v[244:247]\n"
"ds_read_b64 v[84:85], v179 offset:64\n"
"v_cvt_pk_bf16_f32 v233, v234, v235\n"
"v_mfma_f32_16x16x32_bf16 v[252:255], v[132:135], v[128:131], v[252:255]\n"
"v_rcp_f32 v171, v90\n"
"v_cvt_pk_bf16_f32 v234, v236, v237\n"
"v_cvt_pk_bf16_f32 v235, v238, v239\n"
"v_cvt_pk_bf16_f32 v240, v240, v241\n"
"v_mul_f32 v248, v171, v248\n"
"v_mul_f32 v249, v171, v249\n"
"v_cvt_pk_bf16_f32 v241, v242, v243\n"
"v_mul_f32 v250, v171, v250\n"
"v_mul_f32 v251, v171, v251\n"
"v_cvt_pk_bf16_f32 v248, v248, v249\n"
"v_cvt_pk_bf16_f32 v249, v250, v251\n"
"v_mul_f32 v244, v171, v244\n"
"v_mul_f32 v245, v171, v245\n"
"v_mul_f32 v246, v171, v246\n"
"v_mul_f32 v247, v171, v247\n"
"v_cvt_pk_bf16_f32 v250, v244, v245\n"
"v_cvt_pk_bf16_f32 v251, v246, v247\n"
"v_mul_f32 v252, v171, v252\n"
"v_mul_f32 v253, v171, v253\n"
"v_mul_f32 v254, v171, v254\n"
"v_mul_f32 v255, v171, v255\n"
"v_cvt_pk_bf16_f32 v252, v252, v253\n"
"v_cvt_pk_bf16_f32 v253, v254, v255\n"
"v_min_u32 v248, -2, v248\n"
"v_min_u32 v249, -2, v249\n"
"v_min_u32 v250, -2, v250\n"
"v_min_u32 v251, -2, v251\n"
"v_min_u32 v252, -2, v252\n"
"v_min_u32 v253, -2, v253\n"
"s_mov_b64 exec, s[44:45]\n"
"v_add_u32 v181, 0xc0, v181\n"
"s_mov_b64 exec, -1\n"
"s_mov_b64 exec, s[46:47]\n"
"v_add_u32 v185, 0xc0, v185\n"
"s_mov_b64 exec, -1\n"
"v_cvt_pk_bf16_f32 v228, v228, v229\n"
"v_cvt_pk_bf16_f32 v229, v230, v231\n"
"v_cvt_pk_bf16_f32 v230, v204, v205\n"
"v_cvt_pk_bf16_f32 v231, v206, v207\n"
"v_cvt_pk_bf16_f32 v208, v208, v209\n"
"v_cvt_pk_bf16_f32 v209, v210, v211\n"
"v_cvt_pk_bf16_f32 v212, v212, v213\n"
"v_cvt_pk_bf16_f32 v213, v214, v215\n"
"v_cvt_pk_bf16_f32 v214, v216, v217\n"
"v_cvt_pk_bf16_f32 v215, v218, v219\n"
"v_cvt_pk_bf16_f32 v210, v220, v221\n"
"v_cvt_pk_bf16_f32 v211, v222, v223\n"
"v_mbcnt_lo_u32_b32 v190, -1, 0\n"
"v_mbcnt_hi_u32_b32 v190, -1, v190\n"
"v_lshrrev_b32 v191, 4, v190\n"
"v_bfe_u32 v186, v190, 1, 2\n"
"v_xor_b32 v186, v186, v191\n"
"v_lshl_add_u32 v186, v186, 4, s6\n"
"v_lshl_add_u32 v188, v190, 4, s6\n"
"v_lshl_add_u32 v189, v190, 3, s6\n"
"s_waitcnt lgkmcnt(2)\n"
"v_mfma_f32_16x16x32_bf16 v[140:143], v[140:143], v[144:147], 0\n"
"s_waitcnt lgkmcnt(1)\n"
"buffer_load_dword v199, off, s[28:31], s39 sc1\n"
"ds_read_b128 v[120:123], v178 offset:13504\n"
"v_mfma_f32_16x16x32_bf16 v[112:115], v[112:115], v[144:147], 0\n"
"s_waitcnt lgkmcnt(1)\n"
"buffer_store_dwordx4 v[248:251], v192, s[28:31], s37 offen sc1\n"
"v_mfma_f32_16x16x32_bf16 v[140:143], v[80:83], v[156:159], v[140:143]\n"
"ds_read_b128 v[116:119], v179 offset:13504\n"
"ds_read_b64 v[90:91], v178 offset:13568\n"
"v_mfma_f32_16x16x32_bf16 v[112:115], v[84:87], v[158:161], v[112:115]\n"
"ds_read_b64 v[92:93], v179 offset:13568\n"
"v_mfma_f32_16x16x32_bf16 v[236:239], a[220:223], v[68:71], 0\n"
"buffer_store_dwordx2 v[252:253], v193, s[28:31], s37 offen offset:1024 sc1\n"
"v_mfma_f32_16x16x32_bf16 v[236:239], a[232:235], v[72:75], v[236:239]\n"
"s_add_u32 s37, s37, 6144\n"
"ds_read_b128 v[128:131], v178 offset:27008\n"
"ds_read_b128 v[124:127], v179 offset:27008\n"
"v_mfma_f32_16x16x32_bf16 v[236:239], a[244:247], v[76:79], v[236:239]\n"
"ds_read_b64 v[98:99], v178 offset:27072\n"
"v_max_f32 v140, v140, v112\n"
"v_max_f32 v141, v141, v113\n"
"v_max_f32 v142, v142, v114\n"
"v_max_f32 v143, v143, v115\n"
"v_max_f32 v170, v140, v141\n"
"v_max_f32 v171, v142, v143\n"
"s_waitcnt lgkmcnt(6)\n"
"v_mfma_f32_16x16x32_bf16 v[120:123], v[120:123], v[144:147], 0\n"
"buffer_store_dwordx4 v[232:235], v194, s[24:27], s36 offen nt\n"
"s_nop 1\n"
"v_mfma_f32_16x16x32_bf16 v[232:235], a[216:219], v[68:71], 0\n"
"s_waitcnt lgkmcnt(4)\n"
"ds_read_b64 v[100:101], v179 offset:27072\n"
"ds_read_b128 v[136:139], v180\n"
"v_mfma_f32_16x16x32_bf16 v[116:119], v[116:119], v[144:147], 0\n"
"s_waitcnt lgkmcnt(5)\n"
"ds_read_b128 v[132:135], v181\n"
"v_mfma_f32_16x16x32_bf16 v[120:123], v[88:91], v[156:159], v[120:123]\n"
"s_waitcnt lgkmcnt(5)\n"
"ds_read_b64 v[106:107], v180 offset:64\n"
"v_mfma_f32_16x16x32_bf16 v[116:119], v[92:95], v[158:161], v[116:119]\n"
"s_waitcnt lgkmcnt(5)\n"
"ds_read_b64 v[108:109], v181 offset:64\n"
"v_mfma_f32_16x16x32_bf16 v[128:131], v[128:131], v[144:147], 0\n"
"s_waitcnt lgkmcnt(5)\n"
"ds_write_b128 v187, v[228:231]\n"
"v_mfma_f32_16x16x32_bf16 v[124:127], v[124:127], v[144:147], 0\n"
"v_mfma_f32_16x16x32_bf16 v[128:131], v[96:99], v[156:159], v[128:131]\n"
"s_nop 0\n"
"v_max_f32 v120, v120, v116\n"
"v_mfma_f32_16x16x32_bf16 v[232:235], a[228:231], v[72:75], v[232:235]\n"
"s_waitcnt vmcnt(9) lgkmcnt(5)\n"
"ds_read_b64_tr_b16 v[244:245], v182 offset:128\n"
"ds_read_b64_tr_b16 v[246:247], v183 offset:128\n"
"v_mfma_f32_16x16x32_bf16 v[124:127], v[100:103], v[158:161], v[124:127]\n"
"s_waitcnt lgkmcnt(6)\n"
"ds_read_b64_tr_b16 v[248:249], v182 offset:136\n"
"v_max_f32 v121, v121, v117\n"
"v_mfma_f32_16x16x32_bf16 v[136:139], v[136:139], v[144:147], 0\n"
"s_waitcnt lgkmcnt(6)\n"
"ds_read_b64_tr_b16 v[250:251], v183 offset:136\n"
"v_max_f32 v122, v122, v118\n"
"v_mfma_f32_16x16x32_bf16 v[132:135], v[132:135], v[144:147], 0\n"
"s_waitcnt lgkmcnt(6)\n"
"ds_read_b64_tr_b16 v[252:253], v182 offset:72\n"
"v_max_f32 v123, v123, v119\n"
"v_mfma_f32_16x16x32_bf16 v[136:139], v[104:107], v[156:159], v[136:139]\n"
"s_waitcnt lgkmcnt(6)\n"
"ds_read_b64_tr_b16 v[254:255], v183 offset:72\n"
"v_max3_f32 v170, v170, v120, v121\n"
"v_mfma_f32_16x16x32_bf16 v[132:135], v[108:111], v[158:161], v[132:135]\n"
"ds_read_b64_tr_b16 v[112:113], v182 offset:13632\n"
"v_max3_f32 v171, v171, v122, v123\n"
"v_mfma_f32_16x16x32_bf16 v[232:235], a[240:243], v[76:79], v[232:235]\n"
"buffer_store_dword v198, v196, s[28:31], s40 offen sc1\n"
"v_max_f32 v128, v128, v124\n"
"v_max_f32 v129, v129, v125\n"
"s_waitcnt vmcnt(8)\n"
"ds_write_b128 v202, v[56:59] offset:55296\n"
"buffer_store_dwordx2 v[240:241], v195, s[24:27], s36 offen nt\n"
"v_mfma_f32_16x16x32_bf16 v[240:243], a[224:227], v[68:71], 0\n"
"s_waitcnt vmcnt(8)\n"
"v_max_f32 v130, v130, v126\n"
"v_max_f32 v131, v131, v127\n"
"v_mfma_f32_16x16x32_bf16 v[240:243], a[236:239], v[72:75], v[240:243]\n"
"s_add_u32 s36, s36, s33\n"
"ds_read_b128 v[68:71], v192 offset:33792\n"
"ds_read_b128 v[72:75], v192 offset:34816\n"
"v_mfma_f32_16x16x32_bf16 v[240:243], a[248:251], v[76:79], v[240:243]\n"
"ds_read_b128 v[76:79], v192 offset:35840\n"
"v_max3_f32 v170, v170, v128, v129\n"
"v_max3_f32 v171, v171, v130, v131\n"
"v_max_f32 v136, v136, v132\n"
"v_max_f32 v137, v137, v133\n"
"v_max_f32 v138, v138, v134\n"
"v_max_f32 v139, v139, v135\n"
"v_max3_f32 v170, v170, v136, v137\n"
"v_max3_f32 v171, v171, v138, v139\n"
"ds_read_b64_tr_b16 v[114:115], v183 offset:13632\n"
"ds_read_b64_tr_b16 v[116:117], v182 offset:13640\n"
"ds_write_b64 v203, v[60:61] offset:55296\n"
"v_max_f32 v170, v170, v171\n"
"v_mov_b32 v171, v170\n"
"ds_read_b64_tr_b16 v[118:119], v183 offset:13640\n"
"ds_read_b64_tr_b16 v[124:125], v182 offset:13576\n"
"v_permlane16_swap_b32 v170, v171\n"
"v_max_f32 v170, v170, v171\n"
"v_mov_b32 v171, v170\n"
"ds_read_b64_tr_b16 v[126:127], v183 offset:13576\n"
"ds_read_b64_tr_b16 v[132:133], v182 offset:27136\n"
"v_permlane32_swap_b32 v170, v171\n"
"v_max_f32 v170, v170, v171\n"
"v_mul_f32 v170, 0xbe553b95, v170\n"
"v_fmamk_f32 v140, v140, 0x3e553b95, v170\n"
"v_fmamk_f32 v141, v141, 0x3e553b95, v170\n"
"v_fmamk_f32 v142, v142, 0x3e553b95, v170\n"
"v_fmamk_f32 v143, v143, 0x3e553b95, v170\n"
"v_exp_f32 v140, v140\n"
"v_exp_f32 v141, v141\n"
"v_exp_f32 v142, v142\n"
"v_exp_f32 v143, v143\n"
"v_cvt_pk_bf16_f32 v82, v140, v141\n"
"v_cvt_pk_bf16_f32 v83, v142, v143\n"
"v_and_b32 v140, v82, v176\n"
"v_and_b32 v141, v83, v177\n"
"v_xor_b32 v142, v82, v140\n"
"v_xor_b32 v143, v83, v141\n"
"v_fmamk_f32 v120, v120, 0x3e553b95, v170\n"
"v_fmamk_f32 v121, v121, 0x3e553b95, v170\n"
"v_fmamk_f32 v122, v122, 0x3e553b95, v170\n"
"v_fmamk_f32 v123, v123, 0x3e553b95, v170\n"
"v_exp_f32 v120, v120\n"
"v_exp_f32 v121, v121\n"
"v_exp_f32 v122, v122\n"
"v_exp_f32 v123, v123\n"
"v_cvt_pk_bf16_f32 v84, v120, v121\n"
"v_cvt_pk_bf16_f32 v85, v122, v123\n"
"v_and_b32 v120, v84, v176\n"
"v_and_b32 v121, v85, v177\n"
"v_xor_b32 v122, v84, v120\n"
"v_xor_b32 v123, v85, v121\n"
"v_mfma_f32_16x16x32_bf16 v[82:85], v[172:175], v[82:85], 0\n"
"s_waitcnt lgkmcnt(9)\n"
"ds_read_b64_tr_b16 v[134:135], v183 offset:27136\n"
"ds_read_b64_tr_b16 v[98:99], v182 offset:27144\n"
"v_mfma_f32_16x16x32_bf16 v[232:235], a[252:255], v[68:71], v[232:235]\n"
"s_waitcnt lgkmcnt(10)\n"
"ds_read_b64_tr_b16 v[100:101], v183 offset:27144\n"
"v_fmamk_f32 v128, v128, 0x3e553b95, v170\n"
"v_mfma_f32_16x16x32_bf16 v[236:239], v[0:3], v[68:71], v[236:239]\n"
"ds_read_b64_tr_b16 v[106:107], v182 offset:27080\n"
"v_fmamk_f32 v129, v129, 0x3e553b95, v170\n"
"v_mfma_f32_16x16x32_bf16 v[240:243], v[4:7], v[68:71], v[240:243]\n"
"ds_read_b64_tr_b16 v[108:109], v183 offset:27080\n"
"v_fmamk_f32 v130, v130, 0x3e553b95, v170\n"
"v_mfma_f32_16x16x32_bf16 v[232:235], v[8:11], v[72:75], v[232:235]\n"
"s_waitcnt lgkmcnt(12)\n"
"ds_write_b128 v187, v[208:211] offset:64\n"
"v_mfma_f32_16x16x32_bf16 v[236:239], v[12:15], v[72:75], v[236:239]\n"
"v_fmamk_f32 v131, v131, 0x3e553b95, v170\n"
"v_fmamk_f32 v136, v136, 0x3e553b95, v170\n"
"v_mfma_f32_16x16x32_bf16 v[240:243], v[16:19], v[72:75], v[240:243]\n"
"s_waitcnt vmcnt(5)\n"
"v_exp_f32 v128, v128\n"
"v_mfma_f32_16x16x32_bf16 v[232:235], v[20:23], v[76:79], v[232:235]\n"
"v_add3_u32 v199, v199, v197, -10\n"
"v_cmp_lt_u32 vcc, 64, v199\n"
"v_mfma_f32_16x16x32_bf16 v[244:247], v[244:247], v[140:143], 0\n"
"s_waitcnt lgkmcnt(12)\n"
"ds_write_b128 v187, v[212:215] offset:128\n"
"v_mfma_f32_16x16x32_bf16 v[248:251], v[248:251], v[140:143], 0\n"
"s_waitcnt lgkmcnt(10)\n"
"v_exp_f32 v129, v129\n"
"v_mfma_f32_16x16x32_bf16 v[252:255], v[252:255], v[140:143], 0\n"
"s_waitcnt lgkmcnt(8)\n"
"v_exp_f32 v130, v130\n"
"v_mfma_f32_16x16x32_bf16 v[236:239], v[24:27], v[76:79], v[236:239]\n"
"s_cbranch_vccnz .Lp9%=\n"
".Lr9%=:\n"
"v_mfma_f32_16x16x32_bf16 v[244:247], v[112:115], v[120:123], v[244:247]\n"
"buffer_load_dwordx4 a[150:153], v192, s[28:31], s38 offen sc1\n"
"ds_read_b64_tr_b16 v[140:141], v184 offset:128\n"
"v_mfma_f32_16x16x32_bf16 v[248:251], v[116:119], v[120:123], v[248:251]\n"
"ds_read_b64_tr_b16 v[142:143], v185 offset:128\n"
"ds_read_b64_tr_b16 v[112:113], v184 offset:72\n"
"v_mfma_f32_16x16x32_bf16 v[240:243], v[28:31], v[76:79], v[240:243]\n"
"ds_read_b64_tr_b16 v[114:115], v185 offset:72\n"
"v_cvt_pk_bf16_f32 v90, v128, v129\n"
"v_mfma_f32_16x16x32_bf16 v[252:255], v[124:127], v[120:123], v[252:255]\n"
"buffer_load_dwordx2 a[154:155], v193, s[28:31], s38 offen offset:1024 sc1\n"
"v_exp_f32 v131, v131\n"
"v_and_b32 v128, v90, v176\n"
"v_cvt_pk_bf16_f32 v91, v130, v131\n"
"v_and_b32 v129, v91, v177\n"
"v_xor_b32 v130, v90, v128\n"
"v_xor_b32 v131, v91, v129\n"
"v_fmamk_f32 v137, v137, 0x3e553b95, v170\n"
"v_fmamk_f32 v138, v138, 0x3e553b95, v170\n"
"v_fmamk_f32 v139, v139, 0x3e553b95, v170\n"
"v_exp_f32 v136, v136\n"
"v_exp_f32 v137, v137\n"
"v_exp_f32 v138, v138\n"
"v_exp_f32 v139, v139\n"
"v_cvt_pk_bf16_f32 v92, v136, v137\n"
"v_cvt_pk_bf16_f32 v93, v138, v139\n"
"v_and_b32 v136, v92, v176\n"
"v_and_b32 v137, v93, v177\n"
"v_xor_b32 v138, v92, v136\n"
"v_xor_b32 v139, v93, v137\n"
".Lic7%=:\n"
"s_cbranch_execz .Lib1%=\n"
"v_mfma_f32_16x16x32_bf16 v[90:93], v[172:175], v[90:93], v[82:85]\n"
"s_waitcnt lgkmcnt(4)\n"
"ds_read_b64_tr_b16 v[120:121], v184 offset:136\n"
"ds_read_b64_tr_b16 v[122:123], v185 offset:136\n"
"v_mfma_f32_16x16x32_bf16 v[244:247], v[132:135], v[128:131], v[244:247]\n"
"s_waitcnt lgkmcnt(4)\n"
"ds_read_b128 v[116:119], v178 offset:13504\n"
"v_cvt_pk_bf16_f32 v232, v232, v233\n"
"v_mfma_f32_16x16x32_bf16 v[248:251], v[98:101], v[128:131], v[248:251]\n"
"s_barrier\n"
"v_mfma_f32_16x16x32_bf16 v[252:255], v[106:109], v[128:131], v[252:255]\n"
"s_waitcnt lgkmcnt(3)\n"
"ds_read_b128 v[68:71], v192 offset:55296\n"
"ds_read_b128 v[72:75], v192 offset:56320\n"
"v_mfma_f32_16x16x32_bf16 v[244:247], v[140:143], v[136:139], v[244:247]\n"
"s_add_u32 s38, s38, 6144\n"
"buffer_load_dwordx4 a[156:159], v192, s[28:31], s38 offen sc1\n"
"ds_read_b128 v[76:79], v192 offset:57344\n"
"v_mfma_f32_16x16x32_bf16 v[252:255], v[112:115], v[136:139], v[252:255]\n"
"v_rcp_f32 v171, v90\n"
"v_cvt_pk_bf16_f32 v233, v234, v235\n"
"v_cvt_pk_bf16_f32 v234, v236, v237\n"
"v_cvt_pk_bf16_f32 v235, v238, v239\n"
"v_cvt_pk_bf16_f32 v240, v240, v241\n"
"v_mul_f32 v244, v171, v244\n"
"v_cvt_pk_bf16_f32 v241, v242, v243\n"
"v_mul_f32 v245, v171, v245\n"
"v_mul_f32 v246, v171, v246\n"
"v_mul_f32 v247, v171, v247\n"
"v_cvt_pk_bf16_f32 v244, v244, v245\n"
"v_cvt_pk_bf16_f32 v245, v246, v247\n"
"v_mul_f32 v252, v171, v252\n"
"v_mul_f32 v253, v171, v253\n"
"v_mul_f32 v254, v171, v254\n"
"v_mul_f32 v255, v171, v255\n"
"v_cvt_pk_bf16_f32 v252, v252, v253\n"
"v_cvt_pk_bf16_f32 v253, v254, v255\n"
"v_min_u32 v244, -2, v244\n"
"v_min_u32 v245, -2, v245\n"
"v_min_u32 v252, -2, v252\n"
"v_min_u32 v253, -2, v253\n"
"v_bfe_u32 v187, v190, 2, 2\n"
"v_xor_b32 v187, v187, v191\n"
"v_lshl_add_u32 v187, v187, 3, s6\n"
"v_and_b32 v190, 15, v190\n"
"v_lshl_add_u32 v186, v190, 6, v186\n"
"v_lshl_add_u32 v187, v190, 5, v187\n"
"s_waitcnt lgkmcnt(2)\n"
"v_mfma_f32_16x16x32_bf16 v[248:251], v[120:123], v[136:139], v[248:251]\n"
"buffer_load_dwordx2 a[160:161], v193, s[28:31], s38 offen offset:1024 sc1\n"
"ds_read_b128 v[124:127], v179 offset:13504\n"
"v_mfma_f32_16x16x32_bf16 v[116:119], v[116:119], v[148:151], 0\n"
"s_add_u32 s38, s38, 6144\n"
"ds_read_b64 v[82:83], v178 offset:13568\n"
"ds_read_b64 v[84:85], v179 offset:13568\n"
"v_mfma_f32_16x16x32_bf16 v[236:239], a[220:223], v[68:71], 0\n"
"s_waitcnt lgkmcnt(4)\n"
"ds_read_b128 v[128:131], v178 offset:27008\n"
"v_mul_f32 v248, v171, v248\n"
"v_mfma_f32_16x16x32_bf16 v[236:239], a[232:235], v[72:75], v[236:239]\n"
"s_waitcnt lgkmcnt(4)\n"
"buffer_store_dwordx2 v[252:253], v193, s[28:31], s37 offen offset:1024 sc1\n"
"v_mfma_f32_16x16x32_bf16 v[236:239], a[244:247], v[76:79], v[236:239]\n"
"v_mul_f32 v249, v171, v249\n"
"v_mul_f32 v250, v171, v250\n"
"v_mul_f32 v251, v171, v251\n"
"v_cvt_pk_bf16_f32 v246, v248, v249\n"
"v_cvt_pk_bf16_f32 v247, v250, v251\n"
"v_min_u32 v246, -2, v246\n"
"v_min_u32 v247, -2, v247\n"
"ds_read_b128 v[132:135], v179 offset:27008\n"
"ds_read_b64 v[90:91], v178 offset:27072\n"
"buffer_store_dwordx4 v[244:247], v192, s[28:31], s37 offen sc1\n"
"ds_read_b64 v[92:93], v179 offset:27072\n"
"ds_read_b128 v[136:139], v180\n"
"s_waitcnt lgkmcnt(7)\n"
"v_mfma_f32_16x16x32_bf16 v[124:127], v[124:127], v[148:151], 0\n"
"s_waitcnt lgkmcnt(5)\n"
"ds_read_b128 v[140:143], v181\n"
"s_add_u32 s37, s37, 6144\n"
"v_mfma_f32_16x16x32_bf16 v[116:119], v[80:83], v[160:163], v[116:119]\n"
"s_waitcnt lgkmcnt(5)\n"
"ds_read_b64 v[98:99], v180 offset:64\n"
"v_mfma_f32_16x16x32_bf16 v[124:127], v[84:87], v[162:165], v[124:127]\n"
"buffer_store_dwordx4 v[232:235], v194, s[24:27], s36 offen nt\n"
"v_mfma_f32_16x16x32_bf16 v[128:131], v[128:131], v[148:151], 0\n"
"ds_read_b64 v[100:101], v181 offset:64\n"
"ds_read_b128 v[120:123], v180 offset:13504\n"
"v_mfma_f32_16x16x32_bf16 v[232:235], a[216:219], v[68:71], 0\n"
"s_waitcnt lgkmcnt(7)\n"
"ds_read_b128 v[112:115], v181 offset:13504\n"
"v_mfma_f32_16x16x32_bf16 v[132:135], v[132:135], v[148:151], 0\n"
"s_waitcnt lgkmcnt(7)\n"
"ds_read_b64 v[106:107], v180 offset:13568\n"
"v_max_f32 v116, v116, v124\n"
"v_mfma_f32_16x16x32_bf16 v[128:131], v[88:91], v[160:163], v[128:131]\n"
"s_waitcnt lgkmcnt(7)\n"
"ds_read_b64 v[108:109], v181 offset:13568\n"
"v_max_f32 v117, v117, v125\n"
"v_mfma_f32_16x16x32_bf16 v[132:135], v[92:95], v[162:165], v[132:135]\n"
"s_waitcnt lgkmcnt(7)\n"
"ds_read_b64_tr_b16 v[248:249], v182 offset:13632\n"
"v_max_f32 v118, v118, v126\n"
"v_mfma_f32_16x16x32_bf16 v[136:139], v[136:139], v[148:151], 0\n"
"s_waitcnt lgkmcnt(7)\n"
"ds_read_b64_tr_b16 v[250:251], v183 offset:13632\n"
"v_max_f32 v119, v119, v127\n"
"v_mfma_f32_16x16x32_bf16 v[140:143], v[140:143], v[148:151], 0\n"
"s_waitcnt lgkmcnt(7)\n"
"ds_read_b64_tr_b16 v[244:245], v182 offset:13640\n"
"v_max_f32 v170, v116, v117\n"
"v_mfma_f32_16x16x32_bf16 v[136:139], v[96:99], v[160:163], v[136:139]\n"
"ds_read_b64_tr_b16 v[246:247], v183 offset:13640\n"
"v_max_f32 v171, v118, v119\n"
"v_mfma_f32_16x16x32_bf16 v[232:235], a[228:231], v[72:75], v[232:235]\n"
"s_waitcnt lgkmcnt(8)\n"
"ds_read_b64_tr_b16 v[252:253], v182 offset:13576\n"
"v_max_f32 v128, v128, v132\n"
"v_mfma_f32_16x16x32_bf16 v[140:143], v[100:103], v[162:165], v[140:143]\n"
"s_waitcnt lgkmcnt(8)\n"
"ds_write_b128 v202, a[144:147]\n"
"v_mfma_f32_16x16x32_bf16 v[120:123], v[120:123], v[148:151], 0\n"
"s_waitcnt lgkmcnt(7)\n"
"v_max_f32 v129, v129, v133\n"
"v_max_f32 v130, v130, v134\n"
"v_mfma_f32_16x16x32_bf16 v[112:115], v[112:115], v[148:151], 0\n"
"s_waitcnt lgkmcnt(6)\n"
"v_max_f32 v131, v131, v135\n"
"v_max3_f32 v170, v170, v128, v129\n"
"v_mfma_f32_16x16x32_bf16 v[120:123], v[104:107], v[160:163], v[120:123]\n"
"ds_read_b64_tr_b16 v[254:255], v183 offset:13576\n"
"ds_write_b64 v203, a[148:149]\n"
"v_mfma_f32_16x16x32_bf16 v[112:115], v[108:111], v[162:165], v[112:115]\n"
"buffer_load_dword v201, off, s[28:31], s39 sc1\n"
"v_max3_f32 v171, v171, v130, v131\n"
"v_mfma_f32_16x16x32_bf16 v[232:235], a[240:243], v[76:79], v[232:235]\n"
"buffer_store_dwordx2 v[240:241], v195, s[24:27], s36 offen nt\n"
"v_mfma_f32_16x16x32_bf16 v[240:243], a[224:227], v[68:71], 0\n"
"s_add_u32 s36, s36, s33\n"
"ds_read_b128 v[68:71], v192 offset:58368\n"
"ds_read_b64_tr_b16 v[124:125], v182 offset:27136\n"
"v_mfma_f32_16x16x32_bf16 v[240:243], a[236:239], v[72:75], v[240:243]\n"
"ds_read_b128 v[72:75], v192 offset:59392\n"
"v_max_f32 v136, v136, v140\n"
"v_mfma_f32_16x16x32_bf16 v[240:243], a[248:251], v[76:79], v[240:243]\n"
"ds_read_b128 v[76:79], v192 offset:60416\n"
"v_max_f32 v137, v137, v141\n"
"v_max_f32 v138, v138, v142\n"
"v_max_f32 v139, v139, v143\n"
"v_max3_f32 v170, v170, v136, v137\n"
"v_max3_f32 v171, v171, v138, v139\n"
"v_max_f32 v120, v120, v112\n"
"v_max_f32 v121, v121, v113\n"
"v_max_f32 v122, v122, v114\n"
"v_max_f32 v123, v123, v115\n"
"v_max3_f32 v170, v170, v120, v121\n"
"v_max3_f32 v171, v171, v122, v123\n"
"ds_write_b32 v186, v194 offset:4608\n"
"v_max_f32 v170, v170, v171\n"
"v_mov_b32 v171, v170\n"
"ds_write_b32 v187, v195 offset:5632\n"
"s_nop 0\n"
"v_permlane16_swap_b32 v170, v171\n"
"v_max_f32 v170, v170, v171\n"
"v_mov_b32 v171, v170\n"
"s_waitcnt lgkmcnt(5)\n"
"v_mfma_f32_16x16x32_bf16 v[232:235], a[252:255], v[68:71], v[232:235]\n"
"s_waitcnt lgkmcnt(3)\n"
"ds_read_b64_tr_b16 v[126:127], v183 offset:27136\n"
"ds_read_b64_tr_b16 v[132:133], v182 offset:27144\n"
"v_mfma_f32_16x16x32_bf16 v[236:239], v[0:3], v[68:71], v[236:239]\n"
"ds_read_b64_tr_b16 v[134:135], v183 offset:27144\n"
"v_permlane32_swap_b32 v170, v171\n"
"v_mfma_f32_16x16x32_bf16 v[240:243], v[4:7], v[68:71], v[240:243]\n"
"ds_read_b64_tr_b16 v[140:141], v182 offset:27080\n"
"ds_read_b64_tr_b16 v[142:143], v183 offset:27080\n"
"v_mfma_f32_16x16x32_bf16 v[232:235], v[8:11], v[72:75], v[232:235]\n"
"s_waitcnt lgkmcnt(6)\n"
"v_max_f32 v170, v170, v171\n"
"v_mul_f32 v170, 0xbe553b95, v170\n"
"v_mfma_f32_16x16x32_bf16 v[236:239], v[12:15], v[72:75], v[236:239]\n"
"ds_read_b64_tr_b16 v[112:113], v184 offset:128\n"
"ds_read_b64_tr_b16 v[114:115], v185 offset:128\n"
"v_mfma_f32_16x16x32_bf16 v[240:243], v[16:19], v[72:75], v[240:243]\n"
"s_waitcnt lgkmcnt(7)\n"
"v_fmamk_f32 v116, v116, 0x3e553b95, v170\n"
"v_fmamk_f32 v117, v117, 0x3e553b95, v170\n"
"v_mfma_f32_16x16x32_bf16 v[232:235], v[20:23], v[76:79], v[232:235]\n"
"ds_read_b64_tr_b16 v[98:99], v184 offset:136\n"
"ds_read_b64_tr_b16 v[100:101], v185 offset:136\n"
"v_mfma_f32_16x16x32_bf16 v[236:239], v[24:27], v[76:79], v[236:239]\n"
"v_fmamk_f32 v118, v118, 0x3e553b95, v170\n"
"v_fmamk_f32 v119, v119, 0x3e553b95, v170\n"
"v_mfma_f32_16x16x32_bf16 v[240:243], v[28:31], v[76:79], v[240:243]\n"
"ds_read_b64_tr_b16 v[106:107], v184 offset:72\n"
"ds_read_b32 v190, v188 offset:4608\n"
"v_exp_f32 v116, v116\n"
"ds_read_b64_tr_b16 v[108:109], v185 offset:72\n"
"ds_read_b32 v191, v189 offset:5632\n"
"v_exp_f32 v117, v117\n"
"v_exp_f32 v118, v118\n"
"v_exp_f32 v119, v119\n"
"v_cvt_pk_bf16_f32 v82, v116, v117\n"
"v_cvt_pk_bf16_f32 v83, v118, v119\n"
"v_and_b32 v116, v82, v176\n"
"v_and_b32 v117, v83, v177\n"
"v_xor_b32 v118, v82, v116\n"
"v_xor_b32 v119, v83, v117\n"
"v_fmamk_f32 v128, v128, 0x3e553b95, v170\n"
"v_fmamk_f32 v129, v129, 0x3e553b95, v170\n"
"v_mfma_f32_16x16x32_bf16 v[248:251], v[248:251], v[116:119], 0\n"
"v_fmamk_f32 v130, v130, 0x3e553b95, v170\n"
"v_fmamk_f32 v131, v131, 0x3e553b95, v170\n"
"v_mfma_f32_16x16x32_bf16 v[244:247], v[244:247], v[116:119], 0\n"
"v_exp_f32 v128, v128\n"
"v_mfma_f32_16x16x32_bf16 v[252:255], v[252:255], v[116:119], 0\n"
"v_exp_f32 v129, v129\n"
"v_exp_f32 v130, v130\n"
"v_exp_f32 v131, v131\n"
"v_cvt_pk_bf16_f32 v84, v128, v129\n"
"v_cvt_pk_bf16_f32 v85, v130, v131\n"
"v_and_b32 v128, v84, v176\n"
"v_and_b32 v129, v85, v177\n"
"v_xor_b32 v130, v84, v128\n"
"v_xor_b32 v131, v85, v129\n"
"v_mfma_f32_16x16x32_bf16 v[82:85], v[172:175], v[82:85], 0\n"
"s_waitcnt lgkmcnt(8)\n"
"ds_read_b64_tr_b16 v[116:117], v184 offset:13632\n"
"ds_read_b64_tr_b16 v[118:119], v185 offset:13632\n"
"v_mfma_f32_16x16x32_bf16 v[248:251], v[124:127], v[128:131], v[248:251]\n"
"v_fmamk_f32 v136, v136, 0x3e553b95, v170\n"
"v_fmamk_f32 v137, v137, 0x3e553b95, v170\n"
"v_mfma_f32_16x16x32_bf16 v[244:247], v[132:135], v[128:131], v[244:247]\n"
"ds_read_b64_tr_b16 v[124:125], v184 offset:13576\n"
"v_fmamk_f32 v138, v138, 0x3e553b95, v170\n"
"v_mfma_f32_16x16x32_bf16 v[252:255], v[140:143], v[128:131], v[252:255]\n"
"v_fmamk_f32 v139, v139, 0x3e553b95, v170\n"
"v_exp_f32 v136, v136\n"
"v_exp_f32 v137, v137\n"
"v_exp_f32 v138, v138\n"
"v_exp_f32 v139, v139\n"
"v_cvt_pk_bf16_f32 v90, v136, v137\n"
"v_cvt_pk_bf16_f32 v91, v138, v139\n"
"v_and_b32 v136, v90, v176\n"
"v_and_b32 v137, v91, v177\n"
"v_xor_b32 v138, v90, v136\n"
"v_xor_b32 v139, v91, v137\n"
"v_fmamk_f32 v120, v120, 0x3e553b95, v170\n"
"v_fmamk_f32 v121, v121, 0x3e553b95, v170\n"
"v_fmamk_f32 v122, v122, 0x3e553b95, v170\n"
"v_fmamk_f32 v123, v123, 0x3e553b95, v170\n"
"v_exp_f32 v120, v120\n"
"v_exp_f32 v121, v121\n"
"v_exp_f32 v122, v122\n"
"v_exp_f32 v123, v123\n"
"v_cvt_pk_bf16_f32 v92, v120, v121\n"
"v_cvt_pk_bf16_f32 v93, v122, v123\n"
"v_and_b32 v120, v92, v176\n"
"v_and_b32 v121, v93, v177\n"
"v_xor_b32 v122, v92, v120\n"
"v_xor_b32 v123, v93, v121\n"
"v_mfma_f32_16x16x32_bf16 v[90:93], v[172:175], v[90:93], v[82:85]\n"
"s_waitcnt vmcnt(8) lgkmcnt(4)\n"
"ds_read_b64_tr_b16 v[128:129], v184 offset:13640\n"
"ds_read_b64_tr_b16 v[130:131], v185 offset:13640\n"
"v_mfma_f32_16x16x32_bf16 v[248:251], v[112:115], v[136:139], v[248:251]\n"
"s_waitcnt lgkmcnt(3)\n"
"ds_write_b128 v202, a[150:153] offset:6144\n"
"v_mfma_f32_16x16x32_bf16 v[244:247], v[98:101], v[136:139], v[244:247]\n"
"s_waitcnt vmcnt(5)\n"
"v_cvt_pk_bf16_f32 v232, v232, v233\n"
"v_cvt_pk_bf16_f32 v233, v234, v235\n"
"v_mfma_f32_16x16x32_bf16 v[252:255], v[106:109], v[136:139], v[252:255]\n"
"v_cvt_pk_bf16_f32 v234, v236, v237\n"
"v_cvt_pk_bf16_f32 v235, v238, v239\n"
"v_mfma_f32_16x16x32_bf16 v[248:251], v[116:119], v[120:123], v[248:251]\n"
"ds_write_b64 v203, a[154:155] offset:6144\n"
"v_rcp_f32 v171, v90\n"
"v_cvt_pk_bf16_f32 v240, v240, v241\n"
"v_cvt_pk_bf16_f32 v241, v242, v243\n"
"ds_write_b128 v202, a[156:159] offset:12288\n"
"s_nop 2\n"
"v_mul_f32 v248, v171, v248\n"
"v_mul_f32 v249, v171, v249\n"
"v_mul_f32 v250, v171, v250\n"
"v_mul_f32 v251, v171, v251\n"
"v_cvt_pk_bf16_f32 v248, v248, v249\n"
"v_cvt_pk_bf16_f32 v249, v250, v251\n"
"v_min_u32 v248, -2, v248\n"
"v_min_u32 v249, -2, v249\n"
"s_waitcnt lgkmcnt(3)\n"
"v_mfma_f32_16x16x32_bf16 v[244:247], v[128:131], v[120:123], v[244:247]\n"
"ds_write_b64 v203, a[160:161] offset:12288\n"
"s_waitcnt vmcnt(1)\n"
"v_add3_u32 v201, v201, v197, -11\n"
"v_cmp_lt_u32 vcc, 64, v201\n"
"ds_read_b64_tr_b16 v[126:127], v185 offset:13576\n"
"ds_write_b128 v186, v[232:235]\n"
"s_nop 2\n"
"s_cbranch_vccnz .Lp10%=\n"
".Lr10%=:\n"
"buffer_load_dwordx4 a[162:165], v192, s[28:31], s38 offen sc1\n"
"v_mul_f32 v244, v171, v244\n"
"v_mul_f32 v245, v171, v245\n"
"v_mul_f32 v246, v171, v246\n"
"ds_write_b64 v187, v[240:241] offset:1024\n"
"v_mul_f32 v247, v171, v247\n"
"v_cvt_pk_bf16_f32 v250, v244, v245\n"
"v_cvt_pk_bf16_f32 v251, v246, v247\n"
"buffer_load_dwordx2 a[166:167], v193, s[28:31], s38 offen offset:1024 sc1\n"
"v_min_u32 v250, -2, v250\n"
"v_min_u32 v251, -2, v251\n"
"s_add_u32 s38, s38, 6144\n"
"s_mov_b32 s48, s38\n"
"s_add_u32 s38, s38, 6144\n"
"s_mov_b32 s49, s38\n"
"s_add_u32 s38, s38, 6144\n"
"s_mov_b32 s50, s38\n"
"s_waitcnt lgkmcnt(2)\n"
"v_mfma_f32_16x16x32_bf16 v[252:255], v[124:127], v[120:123], v[252:255]\n"
"s_barrier\n"
"buffer_store_dwordx4 v[248:251], v192, s[28:31], s37 offen sc1\n"
"ds_read_b128 v[68:71], v192\n"
"ds_read_b128 v[72:75], v192 offset:1024\n"
"ds_read_b128 v[76:79], v192 offset:2048\n"
"s_nop 2\n"
"v_mul_f32 v252, v171, v252\n"
"ds_read_b128 v[32:35], v192 offset:3072\n"
"ds_read_b128 v[36:39], v192 offset:4096\n"
"v_mul_f32 v253, v171, v253\n"
"v_mul_f32 v254, v171, v254\n"
"v_mul_f32 v255, v171, v255\n"
"ds_read_b128 v[40:43], v192 offset:5120\n"
"v_cvt_pk_bf16_f32 v252, v252, v253\n"
"ds_read_b128 v[44:47], v192 offset:6144\n"
"v_cvt_pk_bf16_f32 v253, v254, v255\n"
"v_min_u32 v252, -2, v252\n"
"v_min_u32 v253, -2, v253\n"
"s_add_u32 s38, s38, 6144\n"
"buffer_store_dwordx2 v[252:253], v193, s[28:31], s37 offen offset:1024 sc1\n"
"ds_read_b128 v[48:51], v192 offset:7168\n"
"ds_read_b128 v[52:55], v192 offset:8192\n"
"s_waitcnt lgkmcnt(9)\n"
"ds_read_b128 v[232:235], v188\n"
"s_waitcnt lgkmcnt(9)\n"
"v_mfma_f32_16x16x32_bf16 v[204:207], a[216:219], v[68:71], 0\n"
"s_waitcnt lgkmcnt(8)\n"
"ds_read_b64 v[240:241], v189 offset:1024\n"
"s_add_u32 s37, s37, 6144\n"
"v_mfma_f32_16x16x32_bf16 v[208:211], a[220:223], v[68:71], 0\n"
"v_mfma_f32_16x16x32_bf16 v[204:207], a[228:231], v[72:75], v[204:207]\n"
"s_waitcnt lgkmcnt(8)\n"
"v_mfma_f32_16x16x32_bf16 v[208:211], a[232:235], v[72:75], v[208:211]\n"
"v_mfma_f32_16x16x32_bf16 v[212:215], a[224:227], v[68:71], 0\n"
"buffer_load_dwordx4 v[228:231], v192, s[28:31], s48 offen sc1\n"
"ds_read_b128 v[68:71], v192 offset:9216\n"
"v_mfma_f32_16x16x32_bf16 v[204:207], a[240:243], v[76:79], v[204:207]\n"
"s_waitcnt lgkmcnt(5)\n"
"v_mfma_f32_16x16x32_bf16 v[208:211], a[244:247], v[76:79], v[208:211]\n"
"v_mfma_f32_16x16x32_bf16 v[212:215], a[236:239], v[72:75], v[212:215]\n"
"buffer_load_dwordx2 v[152:153], v193, s[28:31], s48 offen offset:1024 sc1\n"
"ds_read_b128 v[72:75], v192 offset:10240\n"
"v_mfma_f32_16x16x32_bf16 v[204:207], a[252:255], v[32:35], v[204:207]\n"
"s_waitcnt lgkmcnt(2)\n"
"v_mfma_f32_16x16x32_bf16 v[208:211], v[0:3], v[32:35], v[208:211]\n"
"v_mfma_f32_16x16x32_bf16 v[212:215], a[248:251], v[76:79], v[212:215]\n"
"ds_read_b128 v[76:79], v192 offset:11264\n"
"v_mfma_f32_16x16x32_bf16 v[204:207], v[8:11], v[36:39], v[204:207]\n"
"buffer_store_dwordx4 v[232:235], v190, s[24:27], s36 offen nt\n"
"v_mfma_f32_16x16x32_bf16 v[208:211], v[12:15], v[36:39], v[208:211]\n"
"v_mfma_f32_16x16x32_bf16 v[212:215], v[4:7], v[32:35], v[212:215]\n"
"ds_read_b128 v[32:35], v192 offset:12288\n"
"v_mfma_f32_16x16x32_bf16 v[204:207], v[20:23], v[40:43], v[204:207]\n"
"v_mfma_f32_16x16x32_bf16 v[208:211], v[24:27], v[40:43], v[208:211]\n"
"buffer_store_dwordx2 v[240:241], v191, s[24:27], s36 offen nt\n"
"v_mfma_f32_16x16x32_bf16 v[212:215], v[16:19], v[36:39], v[212:215]\n"
"s_add_u32 s36, s36, s33\n"
"ds_read_b128 v[36:39], v192 offset:13312\n"
"v_mfma_f32_16x16x32_bf16 v[212:215], v[28:31], v[40:43], v[212:215]\n"
"ds_read_b128 v[40:43], v192 offset:14336\n"
"v_mfma_f32_16x16x32_bf16 v[216:219], a[216:219], v[44:47], 0\n"
"v_cvt_pk_bf16_f32 v204, v204, v205\n"
"v_cvt_pk_bf16_f32 v205, v206, v207\n"
"v_mfma_f32_16x16x32_bf16 v[220:223], a[220:223], v[44:47], 0\n"
"v_cvt_pk_bf16_f32 v206, v208, v209\n"
"v_cvt_pk_bf16_f32 v207, v210, v211\n"
"v_mfma_f32_16x16x32_bf16 v[216:219], a[228:231], v[48:51], v[216:219]\n"
"s_waitcnt lgkmcnt(2)\n"
"ds_write_b128 v186, v[204:207] offset:1536\n"
"v_mfma_f32_16x16x32_bf16 v[220:223], a[232:235], v[48:51], v[220:223]\n"
"v_cvt_pk_bf16_f32 v212, v212, v213\n"
"v_cvt_pk_bf16_f32 v213, v214, v215\n"
"v_mfma_f32_16x16x32_bf16 v[224:227], a[224:227], v[44:47], 0\n"
"buffer_load_dwordx4 v[62:65], v192, s[28:31], s49 offen sc1\n"
"ds_read_b128 v[44:47], v192 offset:15360\n"
"v_mfma_f32_16x16x32_bf16 v[216:219], a[240:243], v[52:55], v[216:219]\n"
"ds_write_b64 v187, v[212:213] offset:2560\n"
"v_mfma_f32_16x16x32_bf16 v[220:223], a[244:247], v[52:55], v[220:223]\n"
"v_mfma_f32_16x16x32_bf16 v[224:227], a[236:239], v[48:51], v[224:227]\n"
"buffer_load_dwordx2 v[66:67], v193, s[28:31], s49 offen offset:1024 sc1\n"
"ds_read_b128 v[48:51], v192 offset:16384\n"
"v_mfma_f32_16x16x32_bf16 v[216:219], a[252:255], v[68:71], v[216:219]\n"
"v_mfma_f32_16x16x32_bf16 v[220:223], v[0:3], v[68:71], v[220:223]\n"
"v_mfma_f32_16x16x32_bf16 v[224:227], a[248:251], v[52:55], v[224:227]\n"
"s_waitcnt lgkmcnt(3)\n"
"ds_read_b128 v[52:55], v192 offset:17408\n"
"ds_read_b128 v[204:207], v188 offset:1536\n"
"v_mfma_f32_16x16x32_bf16 v[216:219], v[8:11], v[72:75], v[216:219]\n"
"v_mfma_f32_16x16x32_bf16 v[220:223], v[12:15], v[72:75], v[220:223]\n"
"v_mfma_f32_16x16x32_bf16 v[224:227], v[4:7], v[68:71], v[224:227]\n"
"s_waitcnt lgkmcnt(3)\n"
"ds_read_b64 v[212:213], v189 offset:2560\n"
"v_mfma_f32_16x16x32_bf16 v[216:219], v[20:23], v[76:79], v[216:219]\n"
"v_mfma_f32_16x16x32_bf16 v[220:223], v[24:27], v[76:79], v[220:223]\n"
"v_mfma_f32_16x16x32_bf16 v[224:227], v[16:19], v[72:75], v[224:227]\n"
"v_mfma_f32_16x16x32_bf16 v[224:227], v[28:31], v[76:79], v[224:227]\n"
"v_mfma_f32_16x16x32_bf16 v[232:235], a[216:219], v[32:35], 0\n"
"v_mfma_f32_16x16x32_bf16 v[236:239], a[220:223], v[32:35], 0\n"
"s_waitcnt vmcnt(9) lgkmcnt(1)\n"
"ds_write_b128 v202, a[162:165] offset:18432\n"
"v_mfma_f32_16x16x32_bf16 v[232:235], a[228:231], v[36:39], v[232:235]\n"
"s_waitcnt lgkmcnt(1)\n"
"buffer_store_dwordx4 v[204:207], v190, s[24:27], s36 offen \n"
"v_mfma_f32_16x16x32_bf16 v[236:239], a[232:235], v[36:39], v[236:239]\n"
"s_waitcnt vmcnt(9)\n"
"ds_write_b64 v203, a[166:167] offset:18432\n"
"v_mfma_f32_16x16x32_bf16 v[240:243], a[224:227], v[32:35], 0\n"
"v_cvt_pk_bf16_f32 v216, v216, v217\n"
"v_cvt_pk_bf16_f32 v217, v218, v219\n"
"v_mfma_f32_16x16x32_bf16 v[232:235], a[240:243], v[40:43], v[232:235]\n"
"buffer_load_dwordx4 v[56:59], v192, s[28:31], s50 offen sc1\n"
"v_cvt_pk_bf16_f32 v218, v220, v221\n"
"v_mfma_f32_16x16x32_bf16 v[236:239], a[244:247], v[40:43], v[236:239]\n"
"v_cvt_pk_bf16_f32 v219, v222, v223\n"
"v_cvt_pk_bf16_f32 v224, v224, v225\n"
"v_mfma_f32_16x16x32_bf16 v[240:243], a[236:239], v[36:39], v[240:243]\n"
"ds_write_b128 v186, v[216:219] offset:3072\n"
"v_mfma_f32_16x16x32_bf16 v[232:235], a[252:255], v[44:47], v[232:235]\n"
"buffer_load_dwordx2 v[60:61], v193, s[28:31], s50 offen offset:1024 sc1\n"
"v_cvt_pk_bf16_f32 v225, v226, v227\n"
"v_mfma_f32_16x16x32_bf16 v[236:239], v[0:3], v[44:47], v[236:239]\n"
"ds_write_b64 v187, v[224:225] offset:4096\n"
"v_mfma_f32_16x16x32_bf16 v[240:243], a[248:251], v[40:43], v[240:243]\n"
"s_waitcnt lgkmcnt(2)\n"
"s_barrier\n"
"v_mfma_f32_16x16x32_bf16 v[232:235], v[8:11], v[48:51], v[232:235]\n"
"ds_read_b128 v[68:71], v192 offset:18432\n"
"ds_read_b128 v[72:75], v192 offset:19456\n"
"v_mfma_f32_16x16x32_bf16 v[236:239], v[12:15], v[48:51], v[236:239]\n"
"ds_read_b128 v[76:79], v192 offset:20480\n"
"buffer_store_dwordx2 v[212:213], v191, s[24:27], s36 offen \n"
"v_mfma_f32_16x16x32_bf16 v[240:243], v[4:7], v[44:47], v[240:243]\n"
"s_add_u32 s36, s36, s33\n"
"ds_read_b128 v[32:35], v192 offset:21504\n"
"ds_read_b128 v[36:39], v192 offset:22528\n"
"v_mfma_f32_16x16x32_bf16 v[232:235], v[20:23], v[52:55], v[232:235]\n"
"s_waitcnt lgkmcnt(6)\n"
"ds_read_b128 v[40:43], v192 offset:23552\n"
"v_mfma_f32_16x16x32_bf16 v[236:239], v[24:27], v[52:55], v[236:239]\n"
"ds_read_b128 v[216:219], v188 offset:3072\n"
"v_mfma_f32_16x16x32_bf16 v[240:243], v[16:19], v[48:51], v[240:243]\n"
"s_waitcnt lgkmcnt(7)\n"
"ds_read_b64 v[224:225], v189 offset:4096\n"
"v_mfma_f32_16x16x32_bf16 v[240:243], v[28:31], v[52:55], v[240:243]\n"
"v_cvt_pk_bf16_f32 v232, v232, v233\n"
"v_cvt_pk_bf16_f32 v233, v234, v235\n"
"s_nop 0\n"
"v_cvt_pk_bf16_f32 v234, v236, v237\n"
"v_cvt_pk_bf16_f32 v235, v238, v239\n"
"ds_write_b128 v186, v[232:235] offset:4608\n"
"s_waitcnt lgkmcnt(8)\n"
"v_mfma_f32_16x16x32_bf16 v[204:207], a[216:219], v[68:71], 0\n"
"s_waitcnt lgkmcnt(7)\n"
"v_cvt_pk_bf16_f32 v240, v240, v241\n"
"v_cvt_pk_bf16_f32 v241, v242, v243\n"
"v_mfma_f32_16x16x32_bf16 v[208:211], a[220:223], v[68:71], 0\n"
"ds_write_b64 v187, v[240:241] offset:5632\n"
"v_mfma_f32_16x16x32_bf16 v[204:207], a[228:231], v[72:75], v[204:207]\n"
"s_waitcnt lgkmcnt(7)\n"
"v_mfma_f32_16x16x32_bf16 v[208:211], a[232:235], v[72:75], v[208:211]\n"
"s_waitcnt lgkmcnt(3)\n"
"v_mfma_f32_16x16x32_bf16 v[212:215], a[224:227], v[68:71], 0\n"
"s_waitcnt lgkmcnt(2)\n"
"buffer_store_dwordx4 v[216:219], v190, s[24:27], s36 offen \n"
"v_mfma_f32_16x16x32_bf16 v[204:207], a[240:243], v[76:79], v[204:207]\n"
"v_mfma_f32_16x16x32_bf16 v[208:211], a[244:247], v[76:79], v[208:211]\n"
"s_waitcnt lgkmcnt(1)\n"
"ds_read_b128 v[232:235], v188 offset:4608\n"
"v_mfma_f32_16x16x32_bf16 v[212:215], a[236:239], v[72:75], v[212:215]\n"
"buffer_store_dwordx2 v[224:225], v191, s[24:27], s36 offen \n"
"v_mfma_f32_16x16x32_bf16 v[204:207], a[252:255], v[32:35], v[204:207]\n"
"s_waitcnt vmcnt(11) lgkmcnt(1)\n"
"ds_read_b64 v[240:241], v189 offset:5632\n"
"s_add_u32 s36, s36, s33\n"
"v_mfma_f32_16x16x32_bf16 v[208:211], v[0:3], v[32:35], v[208:211]\n"
"v_max3_u32 v199, v228, v229, v230\n"
"s_waitcnt vmcnt(10)\n"
"v_mfma_f32_16x16x32_bf16 v[212:215], a[248:251], v[76:79], v[212:215]\n"
"v_max3_u32 v199, v199, v231, v152\n"
"v_max_u32 v199, v199, v153\n"
"v_mfma_f32_16x16x32_bf16 v[204:207], v[8:11], v[36:39], v[204:207]\n"
"v_cmp_eq_u32 vcc, -1, v199\n"
"v_mfma_f32_16x16x32_bf16 v[208:211], v[12:15], v[36:39], v[208:211]\n"
"v_mfma_f32_16x16x32_bf16 v[212:215], v[4:7], v[32:35], v[212:215]\n"
"s_waitcnt lgkmcnt(1)\n"
"s_nop 1\n"
"s_cbranch_vccnz .Lp11%=\n"
".Lr11%=:\n"
"v_mfma_f32_16x16x32_bf16 v[204:207], v[20:23], v[40:43], v[204:207]\n"
"buffer_store_dwordx4 v[232:235], v190, s[24:27], s36 offen \n"
"v_mfma_f32_16x16x32_bf16 v[208:211], v[24:27], v[40:43], v[208:211]\n"
"s_waitcnt vmcnt(8) lgkmcnt(0)\n"
"ds_write_b128 v202, v[228:231] offset:24576\n"
"v_mfma_f32_16x16x32_bf16 v[212:215], v[16:19], v[36:39], v[212:215]\n"
"buffer_store_dwordx2 v[240:241], v191, s[24:27], s36 offen \n"
"v_mfma_f32_16x16x32_bf16 v[212:215], v[28:31], v[40:43], v[212:215]\n"
"s_add_u32 s36, s36, s33\n"
"v_cvt_pk_bf16_f32 v204, v204, v205\n"
"v_cvt_pk_bf16_f32 v205, v206, v207\n"
"v_cvt_pk_bf16_f32 v206, v208, v209\n"
"v_cvt_pk_bf16_f32 v207, v210, v211\n"
"ds_write_b128 v186, v[204:207]\n"
"s_nop 1\n"
"v_cvt_pk_bf16_f32 v212, v212, v213\n"
"v_cvt_pk_bf16_f32 v213, v214, v215\n"
"v_max3_u32 v199, v62, v63, v64\n"
"s_waitcnt vmcnt(8)\n"
"v_max3_u32 v199, v199, v65, v66\n"
"ds_write_b64 v187, v[212:213] offset:1024\n"
"v_max_u32 v199, v199, v67\n"
"v_cmp_eq_u32 vcc, -1, v199\n"
"ds_write_b64 v203, v[152:153] offset:24576\n"
"s_nop 3\n"
"s_cbranch_vccnz .Lp12%=\n"
".Lr12%=:\n"
"ds_write_b128 v202, v[62:65] offset:36864\n"
"s_waitcnt lgkmcnt(3)\n"
"ds_read_b128 v[204:207], v188\n"
"ds_write_b64 v203, v[66:67] offset:36864\n"
"s_waitcnt lgkmcnt(4)\n"
"ds_read_b64 v[212:213], v189 offset:1024\n"
"s_waitcnt lgkmcnt(4)\n"
"s_barrier\n"
"ds_read_b128 v[44:47], v192 offset:24576\n"
"ds_read_b128 v[48:51], v192 offset:25600\n"
"ds_read_b128 v[52:55], v192 offset:26624\n"
"ds_read_b128 v[68:71], v192 offset:27648\n"
"ds_read_b128 v[72:75], v192 offset:28672\n"
"ds_read_b128 v[76:79], v192 offset:29696\n"
"s_waitcnt lgkmcnt(8)\n"
"buffer_store_dwordx4 v[204:207], v190, s[24:27], s36 offen \n"
"s_waitcnt lgkmcnt(7)\n"
"s_barrier\n"
"ds_read_b128 v[32:35], v192 offset:36864\n"
"ds_read_b128 v[36:39], v192 offset:37888\n"
"ds_read_b128 v[40:43], v192 offset:38912\n"
"s_waitcnt lgkmcnt(9)\n"
"buffer_store_dwordx2 v[212:213], v191, s[24:27], s36 offen \n"
"s_waitcnt lgkmcnt(8)\n"
"v_mfma_f32_16x16x32_bf16 v[216:219], a[216:219], v[44:47], 0\n"
"s_waitcnt lgkmcnt(7)\n"
"s_add_u32 s36, s36, s33\n"
"v_mfma_f32_16x16x32_bf16 v[220:223], a[220:223], v[44:47], 0\n"
"v_mfma_f32_16x16x32_bf16 v[216:219], a[228:231], v[48:51], v[216:219]\n"
"s_waitcnt lgkmcnt(6)\n"
"v_mfma_f32_16x16x32_bf16 v[220:223], a[232:235], v[48:51], v[220:223]\n"
"v_mfma_f32_16x16x32_bf16 v[224:227], a[224:227], v[44:47], 0\n"
"ds_read_b128 v[44:47], v192 offset:39936\n"
"v_mfma_f32_16x16x32_bf16 v[216:219], a[240:243], v[52:55], v[216:219]\n"
"s_waitcnt lgkmcnt(3)\n"
"v_mfma_f32_16x16x32_bf16 v[220:223], a[244:247], v[52:55], v[220:223]\n"
"v_mfma_f32_16x16x32_bf16 v[224:227], a[236:239], v[48:51], v[224:227]\n"
"ds_read_b128 v[48:51], v192 offset:40960\n"
"v_mfma_f32_16x16x32_bf16 v[216:219], a[252:255], v[68:71], v[216:219]\n"
"v_mfma_f32_16x16x32_bf16 v[220:223], v[0:3], v[68:71], v[220:223]\n"
"v_mfma_f32_16x16x32_bf16 v[224:227], a[248:251], v[52:55], v[224:227]\n"
"s_waitcnt vmcnt(8)\n"
"ds_read_b128 v[52:55], v192 offset:41984\n"
"v_max3_u32 v199, v56, v57, v58\n"
"v_mfma_f32_16x16x32_bf16 v[216:219], v[8:11], v[72:75], v[216:219]\n"
"s_waitcnt vmcnt(7)\n"
"v_max3_u32 v199, v199, v59, v60\n"
"v_max_u32 v199, v199, v61\n"
"v_mfma_f32_16x16x32_bf16 v[220:223], v[12:15], v[72:75], v[220:223]\n"
"v_cmp_eq_u32 vcc, -1, v199\n"
"v_mfma_f32_16x16x32_bf16 v[224:227], v[4:7], v[68:71], v[224:227]\n"
"v_mfma_f32_16x16x32_bf16 v[216:219], v[20:23], v[76:79], v[216:219]\n"
"s_nop 2\n"
"s_cbranch_vccnz .Lp13%=\n"
".Lr13%=:\n"
"v_mfma_f32_16x16x32_bf16 v[220:223], v[24:27], v[76:79], v[220:223]\n"
"ds_write_b128 v202, v[56:59] offset:43008\n"
"v_mfma_f32_16x16x32_bf16 v[224:227], v[16:19], v[72:75], v[224:227]\n"
"v_mfma_f32_16x16x32_bf16 v[224:227], v[28:31], v[76:79], v[224:227]\n"
"ds_write_b64 v203, v[60:61] offset:43008\n"
"v_mfma_f32_16x16x32_bf16 v[232:235], a[216:219], v[32:35], 0\n"
"s_waitcnt lgkmcnt(6)\n"
"v_cvt_pk_bf16_f32 v216, v216, v217\n"
"v_cvt_pk_bf16_f32 v217, v218, v219\n"
"v_mfma_f32_16x16x32_bf16 v[236:239], a[220:223], v[32:35], 0\n"
"v_cvt_pk_bf16_f32 v218, v220, v221\n"
"v_cvt_pk_bf16_f32 v219, v222, v223\n"
"v_mfma_f32_16x16x32_bf16 v[232:235], a[228:231], v[36:39], v[232:235]\n"
"s_waitcnt lgkmcnt(5)\n"
"ds_write_b128 v186, v[216:219] offset:1536\n"
"v_mfma_f32_16x16x32_bf16 v[236:239], a[232:235], v[36:39], v[236:239]\n"
"v_cvt_pk_bf16_f32 v224, v224, v225\n"
"v_cvt_pk_bf16_f32 v225, v226, v227\n"
"v_mfma_f32_16x16x32_bf16 v[240:243], a[224:227], v[32:35], 0\n"
"ds_write_b64 v187, v[224:225] offset:2560\n"
"v_mfma_f32_16x16x32_bf16 v[232:235], a[240:243], v[40:43], v[232:235]\n"
"s_waitcnt lgkmcnt(2)\n"
"v_mfma_f32_16x16x32_bf16 v[236:239], a[244:247], v[40:43], v[236:239]\n"
"s_barrier\n"
"v_mfma_f32_16x16x32_bf16 v[240:243], a[236:239], v[36:39], v[240:243]\n"
"ds_read_b128 v[68:71], v192 offset:43008\n"
"ds_read_b128 v[72:75], v192 offset:44032\n"
"v_mfma_f32_16x16x32_bf16 v[232:235], a[252:255], v[44:47], v[232:235]\n"
"ds_read_b128 v[76:79], v192 offset:45056\n"
"v_mfma_f32_16x16x32_bf16 v[236:239], v[0:3], v[44:47], v[236:239]\n"
"s_waitcnt lgkmcnt(4)\n"
"ds_read_b128 v[32:35], v192 offset:46080\n"
"v_mfma_f32_16x16x32_bf16 v[240:243], a[248:251], v[40:43], v[240:243]\n"
"ds_read_b128 v[36:39], v192 offset:47104\n"
"v_mfma_f32_16x16x32_bf16 v[232:235], v[8:11], v[48:51], v[232:235]\n"
"s_waitcnt lgkmcnt(5)\n"
"ds_read_b128 v[40:43], v192 offset:48128\n"
"v_mfma_f32_16x16x32_bf16 v[236:239], v[12:15], v[48:51], v[236:239]\n"
"ds_read_b128 v[216:219], v188 offset:1536\n"
"v_mfma_f32_16x16x32_bf16 v[240:243], v[4:7], v[44:47], v[240:243]\n"
"ds_read_b64 v[224:225], v189 offset:2560\n"
"v_mfma_f32_16x16x32_bf16 v[232:235], v[20:23], v[52:55], v[232:235]\n"
"v_mfma_f32_16x16x32_bf16 v[236:239], v[24:27], v[52:55], v[236:239]\n"
"s_waitcnt lgkmcnt(7)\n"
"v_mfma_f32_16x16x32_bf16 v[240:243], v[16:19], v[48:51], v[240:243]\n"
"v_mfma_f32_16x16x32_bf16 v[240:243], v[28:31], v[52:55], v[240:243]\n"
"v_mfma_f32_16x16x32_bf16 v[204:207], a[216:219], v[68:71], 0\n"
"s_waitcnt lgkmcnt(6)\n"
"v_mfma_f32_16x16x32_bf16 v[208:211], a[220:223], v[68:71], 0\n"
"s_nop 0\n"
"v_cvt_pk_bf16_f32 v232, v232, v233\n"
"v_mfma_f32_16x16x32_bf16 v[204:207], a[228:231], v[72:75], v[204:207]\n"
"s_waitcnt lgkmcnt(5)\n"
"v_cvt_pk_bf16_f32 v233, v234, v235\n"
"v_cvt_pk_bf16_f32 v234, v236, v237\n"
"v_mfma_f32_16x16x32_bf16 v[208:211], a[232:235], v[72:75], v[208:211]\n"
"s_waitcnt lgkmcnt(0)\n"
"buffer_store_dwordx4 v[216:219], v190, s[24:27], s36 offen \n"
"v_mfma_f32_16x16x32_bf16 v[212:215], a[224:227], v[68:71], 0\n"
"v_cvt_pk_bf16_f32 v235, v238, v239\n"
"v_cvt_pk_bf16_f32 v240, v240, v241\n"
"v_mfma_f32_16x16x32_bf16 v[204:207], a[240:243], v[76:79], v[204:207]\n"
"ds_write_b128 v186, v[232:235] offset:3072\n"
"v_mfma_f32_16x16x32_bf16 v[208:211], a[244:247], v[76:79], v[208:211]\n"
"buffer_store_dwordx2 v[224:225], v191, s[24:27], s36 offen \n"
"v_mfma_f32_16x16x32_bf16 v[212:215], a[236:239], v[72:75], v[212:215]\n"
"s_add_u32 s36, s36, s33\n"
"v_cvt_pk_bf16_f32 v241, v242, v243\n"
"v_mfma_f32_16x16x32_bf16 v[204:207], a[252:255], v[32:35], v[204:207]\n"
"ds_write_b64 v187, v[240:241] offset:4096\n"
"v_mfma_f32_16x16x32_bf16 v[208:211], v[0:3], v[32:35], v[208:211]\n"
"v_mfma_f32_16x16x32_bf16 v[212:215], a[248:251], v[76:79], v[212:215]\n"
"v_mfma_f32_16x16x32_bf16 v[204:207], v[8:11], v[36:39], v[204:207]\n"
"v_mfma_f32_16x16x32_bf16 v[208:211], v[12:15], v[36:39], v[208:211]\n"
"s_waitcnt lgkmcnt(1)\n"
"v_mfma_f32_16x16x32_bf16 v[212:215], v[4:7], v[32:35], v[212:215]\n"
"ds_read_b128 v[232:235], v188 offset:3072\n"
"v_mfma_f32_16x16x32_bf16 v[204:207], v[20:23], v[40:43], v[204:207]\n"
"v_mfma_f32_16x16x32_bf16 v[208:211], v[24:27], v[40:43], v[208:211]\n"
"s_waitcnt lgkmcnt(1)\n"
"v_mfma_f32_16x16x32_bf16 v[212:215], v[16:19], v[36:39], v[212:215]\n"
"ds_read_b64 v[240:241], v189 offset:4096\n"
"v_mfma_f32_16x16x32_bf16 v[212:215], v[28:31], v[40:43], v[212:215]\n"
"s_nop 2\n"
"v_cvt_pk_bf16_f32 v204, v204, v205\n"
"v_cvt_pk_bf16_f32 v205, v206, v207\n"
"v_cvt_pk_bf16_f32 v206, v208, v209\n"
"v_cvt_pk_bf16_f32 v207, v210, v211\n"
"ds_write_b128 v186, v[204:207] offset:4608\n"
"v_cvt_pk_bf16_f32 v212, v212, v213\n"
"v_cvt_pk_bf16_f32 v213, v214, v215\n"
"s_waitcnt lgkmcnt(2)\n"
"buffer_store_dwordx4 v[232:235], v190, s[24:27], s36 offen \n"
"ds_write_b64 v187, v[212:213] offset:5632\n"
"s_waitcnt lgkmcnt(2)\n"
"buffer_store_dwordx2 v[240:241], v191, s[24:27], s36 offen \n"
"s_add_u32 s36, s36, s33\n"
"s_waitcnt lgkmcnt(1)\n"
"ds_read_b128 v[204:207], v188 offset:4608\n"
"s_waitcnt lgkmcnt(1)\n"
"ds_read_b64 v[212:213], v189 offset:5632\n"
"s_waitcnt lgkmcnt(1)\n"
"buffer_store_dwordx4 v[204:207], v190, s[24:27], s36 offen \n"
"s_waitcnt lgkmcnt(0)\n"
"buffer_store_dwordx2 v[212:213], v191, s[24:27], s36 offen \n"
"s_add_u32 s36, s36, s33\n"
"s_branch .Lend%=\n"
".Lp1%=:\n"
"s_sub_u32 s42, s42, 1\n"
"s_cbranch_scc1 .Lt1%=\n"
"s_sleep 1\n"
"buffer_load_dword v199, off, s[28:31], s39 sc1\n"
"s_waitcnt vmcnt(0)\n"
"v_add3_u32 v199, v199, v197, -2\n"
"v_cmp_lt_u32 vcc, 64, v199\n"
"s_nop 4\n"
"s_cbranch_vccnz .Lp1%=\n"
"s_branch .Lr1%=\n"
".Lt1%=:\n"
"s_mov_b32 s42, 0\n"
"s_branch .Lr1%=\n"
".Lp3%=:\n"
"s_sub_u32 s42, s42, 1\n"
"s_cbranch_scc1 .Lt3%=\n"
"s_sleep 1\n"
"buffer_load_dword v199, off, s[28:31], s39 sc1\n"
"s_waitcnt vmcnt(0)\n"
"v_add3_u32 v199, v199, v197, -4\n"
"v_cmp_lt_u32 vcc, 64, v199\n"
"s_nop 4\n"
"s_cbranch_vccnz .Lp3%=\n"
"s_branch .Lr3%=\n"
".Lt3%=:\n"
"s_mov_b32 s42, 0\n"
"s_branch .Lr3%=\n"
".Lp5%=:\n"
"s_sub_u32 s42, s42, 1\n"
"s_cbranch_scc1 .Lt5%=\n"
"s_sleep 1\n"
"buffer_load_dword v199, off, s[28:31], s39 sc1\n"
"s_waitcnt vmcnt(0)\n"
"v_add3_u32 v199, v199, v197, -6\n"
"v_cmp_lt_u32 vcc, 64, v199\n"
"s_nop 4\n"
"s_cbranch_vccnz .Lp5%=\n"
"s_branch .Lr5%=\n"
".Lt5%=:\n"
"s_mov_b32 s42, 0\n"
"s_branch .Lr5%=\n"
".Lp7%=:\n"
"s_sub_u32 s42, s42, 1\n"
"s_cbranch_scc1 .Lt7%=\n"
"s_sleep 1\n"
"buffer_load_dword v199, off, s[28:31], s39 sc1\n"
"s_waitcnt vmcnt(0)\n"
"v_add3_u32 v199, v199, v197, -8\n"
"v_cmp_lt_u32 vcc, 64, v199\n"
"s_nop 4\n"
"s_cbranch_vccnz .Lp7%=\n"
"s_branch .Lr7%=\n"
".Lt7%=:\n"
"s_mov_b32 s42, 0\n"
"s_branch .Lr7%=\n"
".Lp9%=:\n"
"s_sub_u32 s42, s42, 1\n"
"s_cbranch_scc1 .Lt9%=\n"
"s_sleep 1\n"
"buffer_load_dword v199, off, s[28:31], s39 sc1\n"
"s_waitcnt vmcnt(0)\n"
"v_add3_u32 v199, v199, v197, -10\n"
"v_cmp_lt_u32 vcc, 64, v199\n"
"s_nop 4\n"
"s_cbranch_vccnz .Lp9%=\n"
"s_branch .Lr9%=\n"
".Lt9%=:\n"
"s_mov_b32 s42, 0\n"
"s_branch .Lr9%=\n"
".Lp10%=:\n"
"s_sub_u32 s42, s42, 1\n"
"s_cbranch_scc1 .Lt10%=\n"
"s_sleep 1\n"
"buffer_load_dword v201, off, s[28:31], s39 sc1\n"
"s_waitcnt vmcnt(0)\n"
"v_add3_u32 v201, v201, v197, -11\n"
"v_cmp_lt_u32 vcc, 64, v201\n"
"s_nop 4\n"
"s_cbranch_vccnz .Lp10%=\n"
"s_branch .Lr10%=\n"
".Lt10%=:\n"
"s_mov_b32 s42, 0\n"
"s_branch .Lr10%=\n"
".Lp11%=:\n"
"s_sub_u32 s42, s42, 1\n"
"s_cbranch_scc1 .Lt11%=\n"
"buffer_load_dwordx4 v[228:231], v192, s[28:31], s48 offen sc1\n"
"buffer_load_dwordx2 v[152:153], v193, s[28:31], s48 offen offset:1024 sc1\n"
"s_waitcnt vmcnt(0)\n"
"v_max3_u32 v199, v228, v229, v230\n"
"v_max3_u32 v199, v199, v231, v152\n"
"v_max_u32 v199, v199, v153\n"
"v_cmp_eq_u32 vcc, -1, v199\n"
"s_nop 4\n"
"s_cbranch_vccnz .Lp11%=\n"
"s_branch .Lr11%=\n"
".Lt11%=:\n"
"s_mov_b32 s42, 0\n"
"s_branch .Lr11%=\n"
".Lp12%=:\n"
"s_sub_u32 s42, s42, 1\n"
"s_cbranch_scc1 .Lt12%=\n"
"buffer_load_dwordx4 v[62:65], v192, s[28:31], s49 offen sc1\n"
"buffer_load_dwordx2 v[66:67], v193, s[28:31], s49 offen offset:1024 sc1\n"
"s_waitcnt vmcnt(0)\n"
"v_max3_u32 v199, v62, v63, v64\n"
"v_max3_u32 v199, v199, v65, v66\n"
"v_max_u32 v199, v199, v67\n"
"v_cmp_eq_u32 vcc, -1, v199\n"
"s_nop 4\n"
"s_cbranch_vccnz .Lp12%=\n"
"s_branch .Lr12%=\n"
".Lt12%=:\n"
"s_mov_b32 s42, 0\n"
"s_branch .Lr12%=\n"
".Lp13%=:\n"
"s_sub_u32 s42, s42, 1\n"
"s_cbranch_scc1 .Lt13%=\n"
"buffer_load_dwordx4 v[56:59], v192, s[28:31], s50 offen sc1\n"
"buffer_load_dwordx2 v[60:61], v193, s[28:31], s50 offen offset:1024 sc1\n"
"s_waitcnt vmcnt(0)\n"
"v_max3_u32 v199, v56, v57, v58\n"
"v_max3_u32 v199, v199, v59, v60\n"
"v_max_u32 v199, v199, v61\n"
"v_cmp_eq_u32 vcc, -1, v199\n"
"s_nop 4\n"
"s_cbranch_vccnz .Lp13%=\n"
"s_branch .Lr13%=\n"
".Lt13%=:\n"
"s_mov_b32 s42, 0\n"
"s_branch .Lr13%=\n"
".Lend%=:\n"
      : "+{v31}"(tid), "+{s2}"(bid), "+{s[0:1]}"(ka)
      : "i"((__attribute__((address_space(3))) char*)smem)
      : "memory", "scc", "vcc", VCLOB, ACLOB, SCLOB);
}

extern "C" void kernel_launch(void* const* d_in, const int* in_sizes, int n_in,
                              void* d_out, int out_size, void* d_ws, size_t ws_size,
                              hipStream_t stream) {
  const uint16_t* x = (const uint16_t*)d_in[0];
  const uint16_t* wqkv = (const uint16_t*)d_in[1];
  const uint16_t* wo = (const uint16_t*)d_in[2];
  const uint16_t* rpb = (const uint16_t*)d_in[3];
  dinat_hs<<<256, 256, 0, stream>>>(x, wqkv, wo, rpb, (uint16_t*)d_out, (char*)d_ws + (2 << 20)
  );
}
```

```cpp
#include <hip/hip_runtime.h>
#include <hip/hip_bf16.h>
#include <stdint.h>

#define VCLOB "v0", "v1", "v2", "v3", "v4", "v5", "v6", "v7", "v8", "v9", "v10", "v11", "v12", "v13", "v14", "v15", "v16", "v17", "v18", "v19", "v20", "v21", "v22", "v23", "v24", "v25", "v26", "v27", "v28", "v29", "v30", "v32", "v33", "v34", "v35", "v36", "v37", "v38", "v39", "v40", "v41", "v42", "v43", "v44", "v45", "v46", "v47", "v48", "v49", "v50", "v51", "v52", "v53", "v54", "v55", "v56", "v57", "v58", "v59", "v60", "v61", "v62", "v63", "v64", "v65", "v66", "v67", "v68", "v69", "v70", "v71", "v72", "v73", "v74", "v75", "v76", "v77", "v78", "v79", "v80", "v81", "v82", "v83", "v84", "v85", "v86", "v87", "v88", "v89", "v90", "v91", "v92", "v93", "v94", "v95", "v96", "v97", "v98", "v99", "v100", "v101", "v102", "v103", "v104", "v105", "v106", "v107", "v108", "v109", "v110", "v111", "v112", "v113", "v114", "v115", "v116", "v117", "v118", "v119", "v120", "v121", "v122", "v123", "v124", "v125", "v126", "v127", "v128", "v129", "v130", "v131", "v132", "v133", "v134", "v135", "v136", "v137", "v138", "v139", "v140", "v141", "v142", "v143", "v144", "v145", "v146", "v147", "v148", "v149", "v150", "v151", "v152", "v153", "v154", "v155", "v156", "v157", "v158", "v159", "v160", "v161", "v162", "v163", "v164", "v165", "v166", "v167", "v168", "v169", "v170", "v171", "v172", "v173", "v174", "v175", "v176", "v177", "v178", "v179", "v180", "v181", "v182", "v183", "v184", "v185", "v186", "v187", "v188", "v189", "v190", "v191", "v192", "v193", "v194", "v195", "v196", "v197", "v198", "v199", "v200", "v201", "v202", "v203", "v204", "v205", "v206", "v207", "v208", "v209", "v210", "v211", "v212", "v213", "v214", "v215", "v216", "v217", "v218", "v219", "v220", "v221", "v222", "v223", "v224", "v225", "v226", "v227", "v228", "v229", "v230", "v231", "v232", "v233", "v234", "v235", "v236", "v237", "v238", "v239", "v240", "v241", "v242", "v243", "v244", "v245", "v246", "v247", "v248", "v249", "v250", "v251", "v252", "v253", "v254", "v255"
#define ACLOB "a0", "a1", "a2", "a3", "a4", "a5", "a6", "a7", "a8", "a9", "a10", "a11", "a12", "a13", "a14", "a15", "a16", "a17", "a18", "a19", "a20", "a21", "a22", "a23", "a24", "a25", "a26", "a27", "a28", "a29", "a30", "a31", "a32", "a33", "a34", "a35", "a36", "a37", "a38", "a39", "a40", "a41", "a42", "a43", "a44", "a45", "a46", "a47", "a48", "a49", "a50", "a51", "a52", "a53", "a54", "a55", "a56", "a57", "a58", "a59", "a60", "a61", "a62", "a63", "a64", "a65", "a66", "a67", "a68", "a69", "a70", "a71", "a72", "a73", "a74", "a75", "a76", "a77", "a78", "a79", "a80", "a81", "a82", "a83", "a84", "a85", "a86", "a87", "a88", "a89", "a90", "a91", "a92", "a93", "a94", "a95", "a96", "a97", "a98", "a99", "a100", "a101", "a102", "a103", "a104", "a105", "a106", "a107", "a108", "a109", "a110", "a111", "a112", "a113", "a114", "a115", "a116", "a117", "a118", "a119", "a120", "a121", "a122", "a123", "a124", "a125", "a126", "a127", "a128", "a129", "a130", "a131", "a132", "a133", "a134", "a135", "a136", "a137", "a138", "a139", "a140", "a141", "a142", "a143", "a144", "a145", "a146", "a147", "a148", "a149", "a150", "a151", "a152", "a153", "a154", "a155", "a156", "a157", "a158", "a159", "a160", "a161", "a162", "a163", "a164", "a165", "a166", "a167", "a168", "a169", "a170", "a171", "a172", "a173", "a174", "a175", "a176", "a177", "a178", "a179", "a180", "a181", "a182", "a183", "a184", "a185", "a186", "a187", "a188", "a189", "a190", "a191", "a192", "a193", "a194", "a195", "a196", "a197", "a198", "a199", "a200", "a201", "a202", "a203", "a204", "a205", "a206", "a207", "a208", "a209", "a210", "a211", "a212", "a213", "a214", "a215", "a216", "a217", "a218", "a219", "a220", "a221", "a222", "a223", "a224", "a225", "a226", "a227", "a228", "a229", "a230", "a231", "a232", "a233", "a234", "a235", "a236", "a237", "a238", "a239", "a240", "a241", "a242", "a243", "a244", "a245", "a246", "a247", "a248", "a249", "a250", "a251", "a252", "a253", "a254", "a255"
#define SCLOB "s3", "s4", "s5", "s6", "s7", "s8", "s9", "s10", "s11", "s12", "s13", "s14", "s15", "s16", "s17", "s18", "s19", "s20", "s21", "s22", "s23", "s24", "s25", "s26", "s27", "s28", "s29", "s30", "s31", "s32", "s33", "s34", "s35", "s36", "s37", "s38", "s39", "s40", "s41", "s42", "s43", "s44", "s45", "s46", "s47", "s48", "s49", "s50", "s51", "s52", "s53", "s54", "s55", "s56", "s57", "s58", "s59", "s60", "s61", "s62", "s63", "s64", "s65", "s66", "s67", "s68", "s69", "s70", "s71", "s72", "s73", "s74", "s75", "s76", "s77", "s78", "s79", "s80", "s81", "s82", "s83", "s84", "s85", "s86", "s87", "s88", "s89", "s90", "s91", "s92", "s93", "s94", "s95", "s96", "s97", "s98", "s99", "s100", "s101"
#define LDS_BYTES 163392

__global__ __launch_bounds__(256, 1) __attribute__((aligned(4096))) void dinat_hs(const uint16_t* __restrict__ x, const uint16_t* __restrict__ wqkv,
                                                   const uint16_t* __restrict__ wo, const uint16_t* __restrict__ rpb,
                                                   uint16_t* __restrict__ out, char* __restrict__ ws
                                                   ) {
  __shared__ __attribute__((aligned(16))) char smem[LDS_BYTES];
  unsigned tid = threadIdx.x, bid = blockIdx.x;
  auto ka = __builtin_amdgcn_kernarg_segment_ptr();
  asm volatile(
"s_load_dwordx2 s[8:9], s[0:1], 0\n"
"s_load_dwordx2 s[12:13], s[0:1], 8\n"
"s_load_dwordx2 s[16:17], s[0:1], 16\n"
"s_load_dwordx2 s[20:21], s[0:1], 24\n"
"s_load_dwordx2 s[24:25], s[0:1], 32\n"
"s_load_dwordx2 s[28:29], s[0:1], 40\n"
"s_getpc_b64 s[4:5]\n"
".Lpc%=:\n"
"s_bfe_u32 s53, s2, 0x20005\n"
"s_and_b32 s54, s2, 1\n"
"s_lshr_b32 s55, s2, 7\n"
"s_mul_i32 s59, s53, 27\n"
"v_lshrrev_b32 v113, 6, v31\n"
"s_max_u32 s59, s59, 1\n"
"s_mul_i32 s58, s55, 4816896\n"
"s_mul_i32 s71, s54, 14278656\n"
"s_nop 0\n"
"v_readfirstlane_b32 s51, v113\n"
"s_xnor_b32 s56, s54, s55\n"
"s_sub_u32 s59, s59, 1\n"
"s_add_u32 s58, s58, s71\n"
"v_and_b32 v112, 63, v31\n"
"s_and_b32 s56, s56, 1\n"
"s_mul_i32 s72, s59, 768\n"
"s_and_b32 s73, s2, 4\n"
"s_lshr_b32 s71, s2, 3\n"
"s_mul_i32 s57, s54, -344064\n"
"s_add_u32 s58, s58, s72\n"
"s_mul_i32 s73, s73, 21504\n"
"s_and_b32 s74, s2, 2\n"
"v_lshlrev_b32 v120, 4, v112\n"
"s_mul_i32 s85, s56, 3\n"
"s_lshl2_add_u32 s71, s71, s51\n"
"v_min_u32 v121, 2, v112\n"
"s_add_u32 s57, s57, 172032\n"
"s_add_u32 s58, s58, s73\n"
"s_mul_i32 s74, s74, 192\n"
"s_lshl_b32 s61, s51, 3\n"
"v_lshrrev_b32 v114, 3, v112\n"
"v_xor_b32 v115, v120, v112\n"
"s_sub_u32 s86, s85, 3\n"
"s_mul_i32 s72, s71, 384\n"
"v_lshlrev_b32 v121, 7, v121\n"
"s_bfe_u32 s52, s2, 0x20003\n"
"s_add_u32 s58, s58, s74\n"
"s_lshr_b32 s84, s2, 5\n"
"v_and_b32 v115, 0x70, v115\n"
"v_add_u32 v114, s61, v114\n"
"s_mul_i32 s86, s86, s57\n"
"v_add_u32 v121, s72, v121\n"
"s_add_u32 s84, s84, s52\n"
"v_xor_b32 v116, 64, v115\n"
"v_mul_u32_u24 v117, 0x300, v114\n"
"s_add_u32 s86, s86, s58\n"
"v_min_u32 v121, .Lend%=-.Lpc%=-4, v121\n"
"v_mul_u32_u24 v123, 0x2aab, v112\n"
"v_add_u32 v124, 64, v112\n"
"s_mul_i32 s84, s84, 2\n"
"v_add3_u32 v119, v117, s86, v116\n"
"s_mov_b32 m0, 163136\n"
"s_mul_i32 s73, s52, 338\n"
"global_load_lds_dword v121, s[4:5]\n"
"s_and_b32 s74, s51, 1\n"
"v_lshrrev_b32 v123, 16, v123\n"
"v_mul_u32_u24 v124, 0x2aab, v124\n"
"s_and_b32 s65, s51, 1\n"
"s_lshr_b32 s75, 0x9249200, s84\n"
"s_lshr_b32 s76, 0x12492480, s84\n"
"s_lshr_b32 s77, 0x4924900, s84\n"
"s_and_b32 s73, s73, -4\n"
"s_mul_i32 s74, s74, 88\n"
"v_lshlrev_b32 v126, 1, v123\n"
"v_lshrrev_b32 v124, 16, v124\n"
"s_lshr_b32 s66, s51, 1\n"
"s_mul_i32 s70, s52, 96\n"
"s_mul_i32 s86, s65, 384\n"
"s_mov_b32 s22, 1352\n"
"s_mov_b32 s23, 0x20000\n"
"s_add_u32 s73, s73, s74\n"
"v_lshlrev_b32 v122, 2, v112\n"
"s_and_b32 s60, s2, 7\n"
"s_lshr_b32 s84, s2, 5\n"
"v_and_b32 v126, 24, v126\n"
"v_lshlrev_b32 v127, 1, v124\n"
"s_add_u32 s86, s86, s70\n"
"s_mul_i32 s71, s66, 36864\n"
"s_movk_i32 s69, 1152\n"
"s_and_b32 s75, s75, 0x180\n"
"s_lshl3_add_u32 s60, s60, s84\n"
"v_and_or_b32 v126, v123, 3, v126\n"
"v_and_b32 v127, 24, v127\n"
"v_add_u32 v125, 128, v112\n"
"s_add_u32 s86, s86, s71\n"
"v_mad_u32_u24 v126, v126, s69, v120\n"
"v_mul_u32_u24 v27, 96, v123\n"
"v_and_or_b32 v127, v124, 3, v127\n"
"v_lshrrev_b32 v128, 2, v124\n"
"v_mul_u32_u24 v125, 0x2aab, v125\n"
"s_add_u32 s86, s86, 384\n"
"s_mul_i32 s65, s65, 3072\n"
"s_mul_i32 s72, s66, 6752\n"
"s_mul_i32 s84, s75, 576\n"
"s_mov_b32 s14, 221184\n"
"s_mov_b32 s15, 0x20000\n"
"v_sub_u32 v27, v126, v27\n"
"v_and_or_b32 v127, v128, 4, v127\n"
"v_lshrrev_b32 v125, 16, v125\n"
"s_add_u32 s72, s72, s65\n"
"s_add_u32 s84, s84, s86\n"
"v_mad_u32_u24 v127, v127, s69, v120\n"
"v_mul_u32_u24 v28, 96, v124\n"
"v_lshlrev_b32 v129, 1, v125\n"
"v_sub_u32 v28, v127, v28\n"
"v_and_b32 v129, 24, v129\n"
"v_add_u32 v28, 1024, v28\n"
"v_and_or_b32 v129, v125, 3, v129\n"
"v_mad_u32_u24 v129, v129, s69, v120\n"
"v_mul_u32_u24 v29, 96, v125\n"
"s_sub_u32 s64, 111, s59\n"
"v_sub_u32 v29, v129, v29\n"
"s_min_u32 s64, s64, 32\n"
"v_max_u32 v118, s85, v114\n"
"v_add_u32 v29, 6656, v29\n"
"s_mul_i32 s64, s64, 768\n"
"v_add_u32 v118, -3, v118\n"
"s_add_u32 s64, s64, s58\n"
"s_mul_i32 s50, s51, 6752\n"
"v_mul_i32_i24 v118, s57, v118\n"
"s_mov_b32 s10, 19267584\n"
"s_mov_b32 s11, 0x20000\n"
"s_add_u32 s50, s50, 27008\n"
"v_add3_u32 v118, v118, s64, v115\n"
"s_mul_i32 s34, s51, 12288\n"
"s_waitcnt lgkmcnt(0)\n"
"s_add_u32 m0, s74, 162768\n"
"s_lshl_b32 s74, s56, 3\n"
"buffer_load_dword v122, s[20:23], s73 offen lds\n"
"s_add_u32 m0, s72, 0\n"
"s_sub_u32 s74, s74, 2\n"
"buffer_load_dwordx4 v27, s[12:15], s84 offen lds\n"
"s_mul_i32 s74, s74, s57\n"
"s_add_u32 s74, s74, s58\n"
"s_add_u32 s34, s34, 81024\n"
"v_add3_u32 v130, v117, s74, v115\n"
"s_add_u32 s73, s74, s57\n"
"s_lshl2_add_u32 s85, s60, s52\n"
"v_add3_u32 v131, v117, s73, v116\n"
"s_and_b32 s76, s76, 0x180\n"
"s_add_u32 m0, s72, 1024\n"
"s_lshl2_add_u32 s39, s85, s51\n"
"s_mul_i32 s85, s76, 576\n"
"s_add_u32 s85, s85, s86\n"
"s_and_b32 s77, s77, 0x180\n"
"s_mul_i32 s71, s77, 576\n"
"buffer_load_dwordx4 v28, s[12:15], s84 offen lds\n"
"s_add_u32 s71, s71, s86\n"
"s_mul_i32 s74, s54, 172032\n"
"s_sub_u32 s36, s58, s74\n"
"s_add_u32 s32, s36, s75\n"
"v_add3_u32 v190, v117, s74, v115\n"
"s_sub_u32 s73, 172032, s74\n"
"v_add3_u32 v191, v117, s73, v116\n"
"s_add_u32 s0, s36, s76\n"
"s_add_u32 s1, s36, s77\n"
"s_add_u32 m0, s72, 2048\n"
"s_mul_i32 s35, s51, 6144\n"
"s_lshl_b32 s33, s57, 1\n"
"s_add_u32 s35, s35, 130176\n"
"v_and_b32 v134, 3, v112\n"
"v_lshrrev_b32 v137, 1, v112\n"
"buffer_load_dwordx4 v29, s[12:15], s84 offen lds\n"
"v_and_or_b32 v137, v137, 4, v134\n"
"v_bfe_u32 v136, v112, 2, 1\n"
"v_lshrrev_b32 v140, 1, v137\n"
"v_lshrrev_b32 v132, 4, v112\n"
"v_lshl_add_u32 v81, v136, 2, v140\n"
"v_xor_b32 v81, v81, v132\n"
"v_lshlrev_b32 v81, 4, v81\n"
"s_add_u32 m0, s50, 0\n"
"v_lshl_add_u32 v81, v137, 7, v81\n"
"v_mul_u32_u24 v25, 0xc00, v136\n"
"v_add_u32 v25, v25, v81\n"
"v_add_u32 v188, s34, v25\n"
"v_xad_u32 v189, v25, 64, s34\n"
"buffer_load_dwordx4 v118, s[8:11], s75 offen lds\n"
"v_xor_b32 v71, 64, v25\n"
"v_add_u32 v70, s35, v25\n"
"v_add_u32 v71, s35, v71\n"
"s_mul_i32 s3, s65, 12\n"
"s_add_u32 s70, s70, s3\n"
"s_mul_i32 s81, s77, 576\n"
"s_add_u32 s81, s81, s70\n"
"s_add_u32 m0, s50, 3072\n"
"s_sub_u32 s3, s76, s75\n"
"s_mul_i32 s3, s3, s66\n"
"s_add_u32 s3, s3, s75\n"
"s_mul_i32 s3, s3, 576\n"
"s_add_u32 s3, s3, s70\n"
"buffer_load_dwordx4 v119, s[8:11], s75 offen lds\n"
"v_lshrrev_b32 v80, 2, v112\n"
"v_mul_u32_u24 v80, 96, v80\n"
"v_lshl_add_u32 v23, v134, 3, v80\n"
"s_mov_b32 s42, 0x80000\n"
"v_add_u32 v72, 54016, v23\n"
"v_add_u32 v142, s61, v137\n"
"v_bfe_u32 v82, v142, 1, 1\n"
"v_bfe_u32 v138, v112, 4, 1\n"
"v_lshrrev_b32 v139, 3, v112\n"
"s_add_u32 m0, s34, 6144\n"
"v_xor_b32 v82, v82, v132\n"
"s_movk_i32 s67, 192\n"
"v_and_b32 v139, 4, v139\n"
"s_mul_i32 s48, s56, 13504\n"
"v_lshlrev_b32 v82, 4, v82\n"
"buffer_load_dwordx4 v130, s[8:11], s75 offen lds\n"
"v_sub_u32 v90, v136, v138\n"
"s_movk_i32 s68, 6752\n"
"s_mul_i32 s49, s56, 272\n"
"v_mad_u32_u24 v141, v142, s67, v82\n"
"s_lshl_b32 s84, s48, 2\n"
"v_mul_i32_i24 v90, 0x88, v90\n"
"v_sub_u32 v91, v137, v139\n"
"s_add_u32 m0, s34, 9216\n"
"v_mad_u32_u24 v186, v136, s68, v141\n"
"s_add_u32 s84, s84, 6752\n"
"v_lshl_add_u32 v90, v91, 3, v90\n"
"v_add_u32 v22, s84, v186\n"
"s_lshl_b32 s84, s66, 10\n"
"buffer_load_dwordx4 v131, s[8:11], s75 offen lds\n"
"v_add_u32 v192, 99456, v120\n"
"s_lshl_b32 s73, s51, 10\n"
"v_add_u32 v202, s73, v192\n"
"v_min_u32 v92, 0xdc, v31\n"
"s_mov_b32 s73, 0xc0000007\n"
"s_cmp_eq_u32 s53, 0\n"
"s_cselect_b32 s73, 0xe0000000, s73\n"
"s_cmp_eq_u32 s53, 3\n"
"s_cselect_b32 s73, 7, s73\n"
"s_add_u32 m0, s72, 13504\n"
"v_mul_u32_u24 v93, 0xf1, v92\n"
"s_mul_i32 s86, s54, -52\n"
"v_lshrrev_b32 v93, 12, v93\n"
"s_sub_u32 s86, s86, 8\n"
"buffer_load_dwordx4 v27, s[12:15], s85 offen lds\n"
"v_lshlrev_b32 v30, 3, v92\n"
"v_mul_i32_i24 v93, s86, v93\n"
"v_lshl_add_u32 v92, v92, 1, v93\n"
"v_xor_b32 v69, 64, v25\n"
"v_add_u32 v30, 160896, v30\n"
"v_add_u32 v68, s50, v25\n"
"v_add_u32 v69, s50, v69\n"
"v_mul_u32_u24 v83, 43, v142\n"
"s_add_u32 m0, s72, 14528\n"
"v_and_b32 v133, 48, v112\n"
"v_lshrrev_b32 v83, 9, v83\n"
"v_mul_u32_u24 v83, 80832, v83\n"
"v_mad_u32_u24 v21, v142, s68, v133\n"
"v_sub_u32 v21, v21, v83\n"
"buffer_load_dwordx4 v28, s[12:15], s85 offen lds\n"
"v_and_b32 v84, 1, v112\n"
"v_add_u32 v21, 6144, v21\n"
"v_add_u32 v187, 74272, v141\n"
"v_add_u32 v198, 60768, v141\n"
"v_mad_u32_u24 v84, v84, s68, v133\n"
"s_mov_b32 s6, 0xf0f0f0f0\n"
"s_mov_b32 s7, 0xf0f0f0f0\n"
"s_add_u32 m0, s72, 15552\n"
"s_mov_b64 exec, s[6:7]\n"
"v_add_u32 v21, s48, v141\n"
"v_mov_b32 v187, v141\n"
"v_add_u32 v198, 60544, v84\n"
"s_mov_b64 exec, -1\n"
"buffer_load_dwordx4 v29, s[12:15], s85 offen lds\n"
"s_add_u32 s85, s49, 161064\n"
"v_add_u32 v20, s85, v90\n"
"s_and_b32 s85, s51, 1\n"
"s_lshl3_add_u32 s84, s85, s84\n"
"s_add_u32 s84, s84, 4096\n"
"v_add_u32 v203, s84, v192\n"
"s_lshl_b32 s84, s51, 6\n"
"v_add_u32 v153, s84, v112\n"
"v_add_u32 v153, 0x100, v153\n"
"s_add_u32 m0, s50, 1024\n"
"v_add_u32 v150, s84, v112\n"
"v_mul_u32_u24 v154, 0xaab, v153\n"
"v_mul_u32_u24 v151, 0xaab, v150\n"
"v_lshrrev_b32 v154, 16, v154\n"
"v_lshrrev_b32 v151, 16, v151\n"
"buffer_load_dwordx4 v118, s[8:11], s76 offen lds\n"
"v_mul_i32_i24 v155, -24, v154\n"
"v_mul_i32_i24 v152, -24, v151\n"
"v_add_u32 v153, v153, v155\n"
"v_add_u32 v150, v150, v152\n"
"v_and_b32 v155, 7, v154\n"
"v_and_b32 v152, 7, v151\n"
"v_xor_b32 v153, v153, v155\n"
"v_xor_b32 v150, v150, v152\n"
"s_add_u32 m0, s50, 4096\n"
"v_lshrrev_b32 v155, 1, v154\n"
"v_lshrrev_b32 v152, 1, v151\n"
"v_and_b32 v155, 4, v155\n"
"s_lshl_b32 s85, s52, 3\n"
"buffer_load_dwordx4 v119, s[8:11], s76 offen lds\n"
"v_and_b32 v152, 4, v152\n"
"v_and_or_b32 v155, v154, 3, v155\n"
"v_and_or_b32 v152, v151, 3, v152\n"
"v_add_u32 v155, s85, v155\n"
"v_add_u32 v152, s85, v152\n"
"v_lshrrev_b32 v65, v155, s73\n"
"v_lshrrev_b32 v66, 4, v154\n"
"v_bfe_u32 v67, v154, 2, 1\n"
"v_lshrrev_b32 v62, v152, s73\n"
"v_lshrrev_b32 v63, 4, v151\n"
"v_bfe_u32 v64, v151, 2, 1\n"
"v_or_b32 v65, v65, v66\n"
"v_xor_b32 v67, s54, v67\n"
"v_or_b32 v62, v62, v63\n"
"v_xor_b32 v64, s54, v64\n"
"v_lshlrev_b32 v65, 31, v65\n"
"v_mul_u32_u24 v67, 172032, v67\n"
"v_mul_u32_u24 v155, 0x300, v155\n"
"v_lshlrev_b32 v62, 31, v62\n"
"v_mul_u32_u24 v64, 172032, v64\n"
"v_mul_u32_u24 v152, 0x300, v152\n"
"v_add3_u32 v65, v65, v67, v155\n"
"v_add3_u32 v62, v62, v64, v152\n"
"v_lshl_add_u32 v195, v153, 4, v65\n"
"s_add_u32 s62, s59, s61\n"
"s_sub_u32 s63, 108, s62\n"
"s_sub_u32 s62, 3, s62\n"
"v_add_u32 v146, 1, v139\n"
"v_add_u32 v147, 2, v139\n"
"v_add_u32 v148, 3, v139\n"
"v_max_i32 v145, s62, v139\n"
"v_max_i32 v146, s62, v146\n"
"v_max_i32 v147, s62, v147\n"
"v_max_i32 v148, s62, v148\n"
"v_max_i32 v102, s62, v137\n"
"s_lshl2_add_u32 s86, s60, s52\n"
"v_mov_b32 v149, 0x7f\n"
"v_min_i32 v145, s63, v145\n"
"v_min_i32 v146, s63, v146\n"
"v_min_i32 v147, s63, v147\n"
"v_min_i32 v148, s63, v148\n"
"v_min_i32 v102, s63, v102\n"
"s_mul_i32 s38, s86, 86016\n"
"v_lshlrev_b32 v145, v145, v149\n"
"v_lshlrev_b32 v146, v146, v149\n"
"v_lshlrev_b32 v147, v147, v149\n"
"v_lshlrev_b32 v148, v148, v149\n"
"v_lshlrev_b32 v102, v102, -1\n"
"v_and_b32 v244, 15, v112\n"
"s_lshl_b32 s86, s51, 2\n"
"v_lshrrev_b32 v145, v137, v145\n"
"v_lshrrev_b32 v146, v137, v146\n"
"v_lshrrev_b32 v147, v137, v147\n"
"v_lshrrev_b32 v148, v137, v148\n"
"v_lshrrev_b32 v102, v139, v102\n"
"v_mul_u32_u24 v244, 0x180, v244\n"
"v_and_b32 v245, 7, v112\n"
"v_add_u32 v246, s86, v132\n"
"s_mov_b32 s74, 0xffff\n"
"v_bfe_i32 v94, v145, 0, 1\n"
"v_bfe_i32 v95, v146, 0, 1\n"
"v_bfe_i32 v96, v147, 0, 1\n"
"v_bfe_i32 v97, v148, 0, 1\n"
"v_bfe_i32 v98, v145, 8, 1\n"
"v_bfe_i32 v99, v146, 8, 1\n"
"v_bfe_i32 v100, v147, 8, 1\n"
"v_bfe_i32 v101, v148, 8, 1\n"
"s_mov_b64 exec, 0\n"
"s_cmp_eq_u32 s51, 0\n"
"s_cbranch_scc1 .Lic0%=\n"
"s_cmp_eq_u32 s51, 1\n"
"s_cbranch_scc1 .Lic1%=\n"
"s_cmp_eq_u32 s51, 2\n"
"s_cbranch_scc1 .Lic2%=\n"
"s_branch .Lic3%=\n"
".Lib0%=:\n"
"s_mov_b64 exec, -1\n"
"v_bfe_i32 v103, v102, 0, 1\n"
"v_bfe_i32 v56, v102, 1, 1\n"
"v_bfe_i32 v57, v102, 2, 1\n"
"v_bfe_i32 v58, v102, 3, 1\n"
"v_add_u32 v244, 99456, v244\n"
"v_xor_b32 v246, v246, v245\n"
"v_lshrrev_b32 v247, 5, v112\n"
"s_add_u32 m0, s34, 7168\n"
"v_bfi_b32 v0, s74, v94, v95\n"
"buffer_load_dwordx4 v130, s[8:11], s76 offen lds\n"
"v_bfi_b32 v1, s74, v96, v97\n"
"v_bfi_b32 v2, s74, v98, v99\n"
"v_bfi_b32 v3, s74, v100, v101\n"
"v_bfi_b32 v176, s74, v103, v56\n"
"v_bfi_b32 v177, s74, v57, v58\n"
"v_lshlrev_b32 v248, 3, v134\n"
"s_and_b32 s84, s52, 1\n"
"v_lshl_add_u32 v196, v246, 4, v244\n"
"s_mul_i32 s74, s52, 1536\n"
"s_lshl2_add_u32 s84, s84, s61\n"
"v_lshl_add_u32 v244, v138, 3, v244\n"
"s_add_u32 m0, s34, 10240\n"
"v_add_u32 v251, s84, v134\n"
"buffer_load_dwordx4 v131, s[8:11], s76 offen lds\n"
"v_mul_u32_u24 v251, 0x300, v251\n"
"v_lshrrev_b32 v252, 2, v112\n"
"v_sub_u32 v253, 11, v112\n"
"v_lshl_add_u32 v251, v252, 7, v251\n"
"v_and_b32 v253, 0x80000000, v253\n"
"v_or_b32 v76, v253, v251\n"
"v_sub_u32 v59, v136, v138\n"
"v_ashrrev_i32 v145, 31, v59\n"
"v_add_u32 v146, -1, v59\n"
"v_not_b32 v145, v145\n"
"v_ashrrev_i32 v146, 31, v146\n"
"s_add_u32 m0, s72, 60768\n"
"v_or_b32 v60, s56, v138\n"
"buffer_load_dwordx4 v27, s[12:15], s71 offen lds\n"
"s_waitcnt vmcnt(12)\n"
"s_barrier\n"
"ds_read_b64_tr_b16 a[0:1], v23\n"
"ds_read_b64_tr_b16 a[2:3], v23 offset:1536\n"
"ds_read_b64_tr_b16 a[4:5], v23 offset:32\n"
"ds_read_b64_tr_b16 a[6:7], v23 offset:1568\n"
"ds_read_b64_tr_b16 a[8:9], v23 offset:64\n"
"ds_read_b64_tr_b16 a[10:11], v23 offset:1600\n"
"ds_read_b64_tr_b16 a[72:73], v23 offset:3072\n"
"s_add_u32 m0, s72, 61792\n"
"ds_read_b64_tr_b16 a[74:75], v23 offset:4608\n"
"buffer_load_dwordx4 v28, s[12:15], s71 offen lds\n"
"ds_read_b64_tr_b16 a[76:77], v23 offset:3104\n"
"v_and_b32 v12, v145, v0\n"
"ds_read_b64_tr_b16 a[78:79], v23 offset:4640\n"
"ds_read_b64_tr_b16 a[80:81], v23 offset:3136\n"
"ds_read_b64_tr_b16 a[82:83], v23 offset:4672\n"
"v_and_b32 v16, v146, v0\n"
"ds_read_b64_tr_b16 a[12:13], v23 offset:6752\n"
"ds_read_b64_tr_b16 a[14:15], v23 offset:8288\n"
"ds_read_b64_tr_b16 a[16:17], v23 offset:6784\n"
"s_add_u32 m0, s72, 62816\n"
"ds_read_b64_tr_b16 a[18:19], v23 offset:8320\n"
"buffer_load_dwordx4 v29, s[12:15], s71 offen lds\n"
"s_waitcnt vmcnt(12)\n"
"ds_read_b128 a[144:147], v68\n"
"s_mul_i32 s72, s54, 312\n"
"s_and_b32 s71, s52, 1\n"
"ds_read_b128 a[148:151], v69\n"
"s_lshl1_add_u32 s72, s71, s72\n"
"s_add_u32 s72, s72, 162760\n"
"v_add_u32 v24, s72, v92\n"
"ds_read_b64_tr_b16 a[20:21], v23 offset:6816\n"
"ds_read_b64_tr_b16 a[22:23], v23 offset:8352\n"
"ds_read_b64_tr_b16 a[84:85], v23 offset:9824\n"
"s_add_u32 m0, s50, 2048\n"
"ds_read_b64_tr_b16 a[86:87], v23 offset:11360\n"
"buffer_load_dwordx4 v118, s[8:11], s77 offen lds\n"
"ds_read_b64_tr_b16 a[88:89], v23 offset:9856\n"
"s_lshl2_add_u32 s71, s60, s51\n"
"ds_read_b64_tr_b16 a[90:91], v23 offset:11392\n"
"s_mul_i32 s72, s51, 1536\n"
"ds_read_b64_tr_b16 a[92:93], v23 offset:9888\n"
"s_lshl2_add_u32 s40, s71, s52\n"
"ds_read_b64_tr_b16 a[94:95], v23 offset:11424\n"
"s_mul_i32 s37, s71, 86016\n"
"ds_read_u16 v64, v24 offset:6\n"
"s_add_u32 s38, s38, s72\n"
"ds_read_u16 v65, v24 offset:4\n"
"s_lshl_b32 s71, s51, 6\n"
"ds_read_u16 v66, v24 offset:2\n"
"v_lshl_add_u32 v249, v23, 2, s71\n"
"v_sub_u32 v249, v249, v248\n"
"v_sub_u32 v249, v249, v248\n"
"v_add_u32 v236, 130176, v249\n"
"v_sub_u32 v250, v236, v248\n"
"v_and_b32 v13, v145, v1\n"
"v_and_b32 v17, v146, v1\n"
"v_and_b32 v14, v145, v2\n"
"v_and_b32 v18, v146, v2\n"
"v_and_b32 v15, v145, v3\n"
"v_and_b32 v19, v146, v3\n"
"v_sub_u32 v60, v136, v60\n"
"v_mov_b32 v160, 0x40de\n"
"v_mov_b32 v161, 0\n"
"v_lshlrev_b32 v61, 4, v134\n"
"v_mov_b64 v[156:157], 0\n"
"v_ashrrev_i32 v145, 31, v60\n"
"v_add_u32 v146, -1, v60\n"
"v_lshl_add_u32 v194, v150, 4, v62\n"
"v_max_u32 v144, 3, v142\n"
"v_add_u32 v144, -3, v144\n"
"v_mov_b32 v143, s48\n"
"v_bfe_u32 v86, v144, 1, 1\n"
"v_mad_u32_u24 v143, v136, s68, v143\n"
"v_xor_b32 v86, v86, v132\n"
"v_lshl_add_u32 v86, v86, 4, v143\n"
"v_mad_u32_u24 v178, v144, s67, v86\n"
"v_add_u32 v144, 5, v142\n"
"v_min_u32 v144, 32, v144\n"
"v_bfe_u32 v87, v144, 1, 1\n"
"v_xor_b32 v87, v87, v132\n"
"v_bfe_u32 v135, v112, 2, 2\n"
"v_lshl_add_u32 v87, v87, 4, v143\n"
"v_mad_u32_u24 v179, v144, s67, v87\n"
"v_cmp_eq_u32 s[44:45], 32, v144\n"
"v_add3_u32 v142, v135, v139, s61\n"
"v_max_u32 v144, 3, v142\n"
"v_add_u32 v144, -3, v144\n"
"v_mov_b32 v143, s48\n"
"v_bfe_u32 v88, v144, 1, 1\n"
"v_mad_u32_u24 v143, v138, s68, v143\n"
"v_xor_b32 v88, v88, v134\n"
"v_lshl_add_u32 v88, v88, 4, v143\n"
"v_mad_u32_u24 v182, v144, s67, v88\n"
"v_add_u32 v144, 5, v142\n"
"v_min_u32 v144, 32, v144\n"
"v_bfe_u32 v89, v144, 1, 1\n"
"v_xor_b32 v89, v89, v134\n"
"v_lshl_add_u32 v89, v89, 4, v143\n"
"v_mad_u32_u24 v183, v144, s67, v89\n"
"v_cmp_eq_u32 s[46:47], 32, v144\n"
"v_not_b32 v145, v145\n"
"v_and_b32 v4, v145, v0\n"
"v_and_b32 v5, v145, v1\n"
"v_and_b32 v6, v145, v2\n"
"v_and_b32 v7, v145, v3\n"
"v_ashrrev_i32 v146, 31, v146\n"
"v_and_b32 v8, v146, v0\n"
"v_and_b32 v9, v146, v1\n"
"v_and_b32 v10, v146, v2\n"
"v_and_b32 v11, v146, v3\n"
"v_and_b32 v85, 1, v114\n"
"v_add_u32 v85, 29, v85\n"
"v_mul_i32_i24 v85, s57, v85\n"
"v_add3_u32 v199, v85, s64, v116\n"
"v_lshlrev_b32 v193, 3, v112\n"
"v_add_u32 v180, 40512, v178\n"
"v_add_u32 v181, 40512, v179\n"
"v_mov_b32 v26, 0xf149f149\n"
"v_add_u32 v184, 40512, v182\n"
"v_add_u32 v185, 40512, v183\n"
"v_mov_b32 v172, 0x3f803f80\n"
"v_mov_b32 v173, v172\n"
"v_mov_b64 v[174:175], v[172:173]\n"
"v_add_u32 v193, 99456, v193\n"
"s_waitcnt vmcnt(11) lgkmcnt(1)\n"
"v_mfma_f32_16x16x32_bf16 v[204:207], a[0:3], a[144:147], 0\n"
"s_add_u32 m0, s50, 5120\n"
"ds_read_b128 a[168:171], v188 offset:6144\n"
"buffer_load_dwordx4 v119, s[8:11], s77 offen lds\n"
"v_mfma_f32_16x16x32_bf16 v[208:211], a[4:7], a[144:147], 0\n"
"s_lshl1_add_u32 s72, s51, 16\n"
"ds_read_b128 a[172:175], v189 offset:6144\n"
"ds_read_u16 v67, v24\n"
"v_mfma_f32_16x16x32_bf16 v[212:215], a[8:11], a[144:147], 0\n"
"s_waitcnt vmcnt(7)\n"
"v_lshl_or_b32 v64, v65, 16, v64\n"
"v_add_u32 v247, s72, v247\n"
"v_mfma_f32_16x16x32_bf16 v[216:219], a[72:75], a[144:147], 0\n"
"s_add_u32 m0, s34, 8192\n"
"ds_read_b128 a[152:155], v68 offset:1024\n"
"buffer_load_dwordx4 v130, s[8:11], s77 offen lds\n"
"v_mfma_f32_16x16x32_bf16 v[220:223], a[76:79], a[144:147], 0\n"
"s_add_u32 s37, s37, s74\n"
"ds_read_b128 a[156:159], v69 offset:1024\n"
"v_xor_b32 v247, v247, v245\n"
"v_mfma_f32_16x16x32_bf16 v[224:227], a[80:83], a[144:147], 0\n"
"s_lshl_b32 s74, s51, 5\n"
"v_lshl_add_u32 v197, v247, 4, v244\n"
"s_mul_i32 s20, s33, 4\n"
"v_mfma_f32_16x16x32_bf16 v[204:207], a[12:15], a[148:151], v[204:207]\n"
"s_add_u32 m0, s34, 11264\n"
"s_sub_u32 s74, 256, s74\n"
"v_add_u32 v235, s74, v250\n"
"v_mfma_f32_16x16x32_bf16 v[208:211], a[16:19], a[148:151], v[208:211]\n"
"s_mov_b32 s6, 0x00f0000f\n"
"s_mov_b32 s7, 0xf0000f00\n"
"buffer_load_dwordx4 v131, s[8:11], s77 offen lds\n"
"v_mfma_f32_16x16x32_bf16 v[212:215], a[20:23], a[148:151], v[212:215]\n"
"s_waitcnt lgkmcnt(4)\n"
"s_mov_b32 s30, 23068672\n"
"s_mul_i32 s21, s33, 5\n"
"v_mfma_f32_16x16x32_bf16 v[216:219], a[84:87], a[148:151], v[216:219]\n"
"s_waitcnt lgkmcnt(2)\n"
"s_mov_b32 s31, 0x20000\n"
"v_mfma_f32_16x16x32_bf16 v[220:223], a[88:91], a[148:151], v[220:223]\n"
"s_waitcnt vmcnt(7)\n"
"v_lshl_or_b32 v65, v67, 16, v66\n"
"v_mfma_f32_16x16x32_bf16 v[224:227], a[92:95], a[148:151], v[224:227]\n"
"s_add_u32 m0, s34, 0\n"
"ds_read_b128 a[176:179], v188 offset:7168\n"
"buffer_load_dwordx4 v190, s[8:11], s32 offen lds\n"
"v_mfma_f32_16x16x32_bf16 v[244:247], a[0:3], a[168:171], 0\n"
"s_lshl_b32 s39, s39, 7\n"
"ds_read_b128 a[180:183], v189 offset:7168\n"
"ds_write_b64 v30, v[64:65]\n"
"v_mfma_f32_16x16x32_bf16 v[248:251], a[4:7], a[168:171], 0\n"
"s_add_u32 s37, s37, 949120\n"
"s_add_u32 s41, s39, 131072\n"
"v_mfma_f32_16x16x32_bf16 v[252:255], a[8:11], a[168:171], 0\n"
"s_add_u32 m0, s34, 3072\n"
"s_lshr_b32 s85, s52, 1\n"
"v_mfma_f32_16x16x32_bf16 v[104:107], a[72:75], a[168:171], 0\n"
"s_mul_i32 s85, s85, 172032\n"
"buffer_load_dwordx4 v191, s[8:11], s32 offen lds\n"
"v_mfma_f32_16x16x32_bf16 v[108:111], a[76:79], a[168:171], 0\n"
"s_add_u32 s32, s32, s33\n"
"s_mov_b32 s18, 73728\n"
"v_mfma_f32_16x16x32_bf16 v[228:231], a[80:83], a[168:171], 0\n"
"s_mul_i32 s79, s51, 3072\n"
"s_mul_i32 s80, s66, 12288\n"
"s_add_u32 s43, s79, 0\n"
"v_mfma_f32_16x16x32_bf16 v[244:247], a[12:15], a[172:175], v[244:247]\n"
"s_add_u32 m0, s34, 1024\n"
"s_add_u32 s85, s85, s36\n"
"v_mfma_f32_16x16x32_bf16 v[248:251], a[16:19], a[172:175], v[248:251]\n"
"s_mul_i32 s78, s33, 6\n"
"buffer_load_dwordx4 v190, s[8:11], s0 offen lds\n"
"v_mfma_f32_16x16x32_bf16 v[252:255], a[20:23], a[172:175], v[252:255]\n"
"s_mov_b32 s26, 19267584\n"
"s_mov_b32 s27, 0x20000\n"
"v_mfma_f32_16x16x32_bf16 v[104:107], a[84:87], a[172:175], v[104:107]\n"
"s_mov_b64 exec, 0\n"
"s_cmp_eq_u32 s51, 0\n"
"s_cbranch_scc1 .Lic4%=\n"
"s_cmp_eq_u32 s51, 1\n"
"s_cbranch_scc1 .Lic5%=\n"
"s_cmp_eq_u32 s51, 2\n"
"s_cbranch_scc1 .Lic6%=\n"
"s_branch .Lic7%=\n"
".Lib1%=:\n"
"s_mov_b64 exec, -1\n"
"v_mfma_f32_16x16x32_bf16 v[108:111], a[88:91], a[172:175], v[108:111]\n"
"s_add_u32 m0, s34, 4096\n"
"s_barrier\n"
"v_mfma_f32_16x16x32_bf16 v[228:231], a[92:95], a[172:175], v[228:231]\n"
"s_waitcnt vmcnt(7)\n"
"buffer_load_dwordx4 v191, s[8:11], s0 offen lds\n"
"s_add_u32 s0, s0, s33\n"
"ds_read_b64_tr_b16 a[24:25], v23 offset:13504\n"
"ds_read_b64_tr_b16 a[26:27], v23 offset:15040\n"
"ds_read_b64_tr_b16 a[28:29], v23 offset:13536\n"
"ds_read_b64_tr_b16 a[30:31], v23 offset:15072\n"
"s_add_u32 m0, s34, 2048\n"
"ds_read_b64_tr_b16 a[32:33], v23 offset:13568\n"
"ds_read_b64_tr_b16 a[34:35], v23 offset:15104\n"
"ds_read_b64_tr_b16 a[96:97], v23 offset:16576\n"
"ds_read_b64_tr_b16 a[98:99], v23 offset:18112\n"
"s_barrier\n"
"buffer_load_dwordx4 v190, s[8:11], s1 offen lds\n"
"ds_read_b64_tr_b16 a[100:101], v23 offset:16608\n"
"ds_read_b64_tr_b16 a[102:103], v23 offset:18144\n"
"ds_read_b64_tr_b16 a[104:105], v23 offset:16640\n"
"ds_read_b64_tr_b16 a[106:107], v23 offset:18176\n"
"s_mov_b64 exec, s[6:7]\n"
"v_lshlrev_b64 v[156:157], v61, v[160:161]\n"
"s_mov_b64 exec, -1\n"
"s_add_u32 m0, s34, 5120\n"
"ds_read_b64_tr_b16 a[36:37], v23 offset:20256\n"
"ds_read_b64_tr_b16 a[38:39], v23 offset:21792\n"
"ds_read_b64_tr_b16 a[40:41], v23 offset:20288\n"
"ds_read_b64_tr_b16 a[42:43], v23 offset:21824\n"
"v_mov_b64 v[160:161], v[156:157]\n"
"v_mov_b64 v[164:165], v[156:157]\n"
"v_mov_b64 v[168:169], v[156:157]\n"
"s_mov_b32 s19, 0x20000\n"
"buffer_load_dwordx4 v191, s[8:11], s1 offen lds\n"
"ds_read_b64_tr_b16 a[44:45], v23 offset:20320\n"
"ds_read_b64_tr_b16 a[46:47], v23 offset:21856\n"
"ds_read_b64_tr_b16 a[108:109], v23 offset:23328\n"
"ds_read_b64_tr_b16 a[110:111], v23 offset:24864\n"
"ds_read_b64_tr_b16 a[112:113], v23 offset:23360\n"
"ds_read_b64_tr_b16 a[114:115], v23 offset:24896\n"
"s_waitcnt lgkmcnt(15)\n"
"v_mfma_f32_16x16x32_bf16 v[204:207], a[24:27], a[152:155], v[204:207]\n"
"ds_read_b64_tr_b16 a[116:117], v23 offset:23392\n"
"ds_read_b64_tr_b16 a[118:119], v23 offset:24928\n"
"v_mfma_f32_16x16x32_bf16 v[208:211], a[28:31], a[152:155], v[208:211]\n"
"s_waitcnt vmcnt(8) lgkmcnt(15)\n"
"ds_read_b64_tr_b16 a[48:49], v23 offset:60768\n"
"ds_read_b64_tr_b16 a[50:51], v23 offset:62304\n"
"v_mfma_f32_16x16x32_bf16 v[212:215], a[32:35], a[152:155], v[212:215]\n"
"s_waitcnt lgkmcnt(15)\n"
"ds_read_b128 a[160:163], v68 offset:2048\n"
"v_add_u32 v68, 130176, v23\n"
"v_mfma_f32_16x16x32_bf16 v[216:219], a[96:99], a[152:155], v[216:219]\n"
"s_waitcnt lgkmcnt(15)\n"
"ds_read_b128 a[164:167], v69 offset:2048\n"
"v_mfma_f32_16x16x32_bf16 v[220:223], a[100:103], a[152:155], v[220:223]\n"
"s_waitcnt lgkmcnt(14)\n"
"ds_read_b64_tr_b16 a[52:53], v23 offset:60800\n"
"ds_read_b64_tr_b16 a[54:55], v23 offset:62336\n"
"v_mfma_f32_16x16x32_bf16 v[224:227], a[104:107], a[152:155], v[224:227]\n"
"s_waitcnt lgkmcnt(14)\n"
"ds_read_b64_tr_b16 a[56:57], v23 offset:60832\n"
"ds_read_b64_tr_b16 a[58:59], v23 offset:62368\n"
"v_mfma_f32_16x16x32_bf16 v[204:207], a[36:39], a[156:159], v[204:207]\n"
"s_waitcnt lgkmcnt(14)\n"
"ds_read_b64_tr_b16 a[120:121], v72 offset:9824\n"
"ds_read_b64_tr_b16 a[122:123], v72 offset:11360\n"
"v_mfma_f32_16x16x32_bf16 v[208:211], a[40:43], a[156:159], v[208:211]\n"
"s_waitcnt lgkmcnt(14)\n"
"ds_read_b64_tr_b16 a[124:125], v72 offset:9856\n"
"ds_read_b64_tr_b16 a[126:127], v72 offset:11392\n"
"v_mfma_f32_16x16x32_bf16 v[212:215], a[44:47], a[156:159], v[212:215]\n"
"s_waitcnt lgkmcnt(14)\n"
"ds_read_b64_tr_b16 a[128:129], v72 offset:9888\n"
"ds_read_b64_tr_b16 a[130:131], v72 offset:11424\n"
"v_mfma_f32_16x16x32_bf16 v[216:219], a[108:111], a[156:159], v[216:219]\n"
"s_waitcnt vmcnt(6) lgkmcnt(14)\n"
"ds_read_b64_tr_b16 a[60:61], v72 offset:13504\n"
"ds_read_b64_tr_b16 a[62:63], v72 offset:15040\n"
"v_mfma_f32_16x16x32_bf16 v[220:223], a[112:115], a[156:159], v[220:223]\n"
"s_add_u32 m0, s35, 0\n"
"ds_read_b128 a[184:187], v188 offset:8192\n"
"buffer_load_dwordx4 v190, s[8:11], s32 offen lds\n"
"v_mfma_f32_16x16x32_bf16 v[224:227], a[116:119], a[156:159], v[224:227]\n"
"s_waitcnt lgkmcnt(14)\n"
"ds_read_b128 a[188:191], v189 offset:8192\n"
"v_mfma_f32_16x16x32_bf16 v[204:207], a[48:51], a[160:163], v[204:207]\n"
"s_add_u32 m0, s35, 3072\n"
"ds_read_b64_tr_b16 a[64:65], v72 offset:13536\n"
"ds_read_b64_tr_b16 a[66:67], v72 offset:15072\n"
"v_mfma_f32_16x16x32_bf16 v[244:247], a[24:27], a[176:179], v[244:247]\n"
"s_waitcnt lgkmcnt(14)\n"
"ds_read_b64_tr_b16 a[68:69], v72 offset:13568\n"
"buffer_load_dwordx4 v191, s[8:11], s32 offen lds\n"
"v_mfma_f32_16x16x32_bf16 v[208:211], a[52:55], a[160:163], v[208:211]\n"
"s_waitcnt lgkmcnt(13)\n"
"ds_read_b64_tr_b16 a[70:71], v72 offset:15104\n"
"ds_read_b64_tr_b16 a[132:133], v72 offset:16576\n"
"v_mfma_f32_16x16x32_bf16 v[212:215], a[56:59], a[160:163], v[212:215]\n"
"s_waitcnt lgkmcnt(13)\n"
"ds_read_b64_tr_b16 a[134:135], v72 offset:18112\n"
"ds_read_b64_tr_b16 a[136:137], v72 offset:16608\n"
"v_mfma_f32_16x16x32_bf16 v[216:219], a[120:123], a[160:163], v[216:219]\n"
"s_waitcnt lgkmcnt(13)\n"
"ds_read_b64_tr_b16 a[138:139], v72 offset:18144\n"
"ds_read_b64_tr_b16 a[140:141], v72 offset:16640\n"
"v_mfma_f32_16x16x32_bf16 v[220:223], a[124:127], a[160:163], v[220:223]\n"
"s_waitcnt lgkmcnt(13)\n"
"ds_read_b64_tr_b16 a[142:143], v72 offset:18176\n"
"v_mfma_f32_16x16x32_bf16 v[224:227], a[128:131], a[160:163], v[224:227]\n"
"s_waitcnt lgkmcnt(12)\n"
"s_add_u32 m0, s35, 1024\n"
"v_mfma_f32_16x16x32_bf16 v[204:207], a[60:63], a[164:167], v[204:207]\n"
"s_add_u32 s1, s1, s33\n"
"buffer_load_dwordx4 v190, s[8:11], s0 offen lds\n"
"v_mfma_f32_16x16x32_bf16 v[248:251], a[28:31], a[176:179], v[248:251]\n"
"s_add_u32 s32, s32, s33\n"
"s_lshl_b32 s40, s40, 7\n"
"v_mfma_f32_16x16x32_bf16 v[252:255], a[32:35], a[176:179], v[252:255]\n"
"s_waitcnt lgkmcnt(8)\n"
"s_add_u32 m0, s35, 4096\n"
"v_cvt_pk_bf16_f32 v204, v204, v205\n"
"v_mfma_f32_16x16x32_bf16 v[208:211], a[64:67], a[164:167], v[208:211]\n"
"s_waitcnt lgkmcnt(6)\n"
"buffer_load_dwordx4 v191, s[8:11], s0 offen lds\n"
"v_cvt_pk_bf16_f32 v205, v206, v207\n"
"v_mfma_f32_16x16x32_bf16 v[212:215], a[68:71], a[164:167], v[212:215]\n"
"s_waitcnt lgkmcnt(4)\n"
"s_add_u32 s0, s0, s33\n"
"v_mfma_f32_16x16x32_bf16 v[216:219], a[132:135], a[164:167], v[216:219]\n"
"s_waitcnt lgkmcnt(2)\n"
"v_cvt_pk_bf16_f32 v206, v208, v209\n"
"v_cvt_pk_bf16_f32 v207, v210, v211\n"
"v_mfma_f32_16x16x32_bf16 v[220:223], a[136:139], a[164:167], v[220:223]\n"
"s_waitcnt lgkmcnt(0)\n"
"v_cvt_pk_bf16_f32 v212, v212, v213\n"
"v_cvt_pk_bf16_f32 v213, v214, v215\n"
"v_mfma_f32_16x16x32_bf16 v[224:227], a[140:143], a[164:167], v[224:227]\n"
"s_add_u32 m0, s35, 2048\n"
"v_cvt_pk_bf16_f32 v216, v216, v217\n"
"v_cvt_pk_bf16_f32 v217, v218, v219\n"
"v_mfma_f32_16x16x32_bf16 v[104:107], a[96:99], a[176:179], v[104:107]\n"
"s_barrier\n"
"v_mfma_f32_16x16x32_bf16 v[108:111], a[100:103], a[176:179], v[108:111]\n"
"s_add_u32 s38, s38, 949120\n"
"buffer_load_dwordx4 v190, s[8:11], s1 offen lds\n"
"ds_write_b128 v21, v[204:207]\n"
"v_mfma_f32_16x16x32_bf16 v[228:231], a[104:107], a[176:179], v[228:231]\n"
"s_add_u32 m0, s35, 5120\n"
"v_cvt_pk_bf16_f32 v218, v220, v221\n"
"v_cvt_pk_bf16_f32 v219, v222, v223\n"
"v_mfma_f32_16x16x32_bf16 v[244:247], a[36:39], a[180:183], v[244:247]\n"
"s_sub_u32 s16, s16, 99456\n"
"s_subb_u32 s17, s17, 0\n"
"buffer_load_dwordx4 v191, s[8:11], s1 offen lds\n"
"v_mfma_f32_16x16x32_bf16 v[248:251], a[40:43], a[180:183], v[248:251]\n"
"s_add_u32 s1, s1, s33\n"
"ds_write_b128 v21, v[216:219] offset:128\n"
"v_mfma_f32_16x16x32_bf16 v[252:255], a[44:47], a[180:183], v[252:255]\n"
"s_mov_b32 s18, 173184\n"
"v_cvt_pk_bf16_f32 v214, v224, v225\n"
"v_cvt_pk_bf16_f32 v215, v226, v227\n"
"v_mfma_f32_16x16x32_bf16 v[104:107], a[108:111], a[180:183], v[104:107]\n"
"s_add_u32 s80, s80, s79\n"
"s_add_u32 s78, s78, s85\n"
"ds_write_b128 v21, v[212:215] offset:64\n"
"v_mfma_f32_16x16x32_bf16 v[108:111], a[112:115], a[180:183], v[108:111]\n"
"v_mfma_f32_16x16x32_bf16 v[228:231], a[116:119], a[180:183], v[228:231]\n"
"s_add_u32 m0, s34, 6144\n"
"s_nop 0\n"
"buffer_load_dwordx4 v190, s[8:11], s32 offen lds\n"
"v_mfma_f32_16x16x32_bf16 v[244:247], a[48:51], a[184:187], v[244:247]\n"
"ds_read_b64 v[80:81], v20 offset:272\n"
"ds_read_b64 v[86:87], v20 offset:336\n"
"v_mfma_f32_16x16x32_bf16 v[248:251], a[52:55], a[184:187], v[248:251]\n"
"ds_read_b64 v[88:89], v20 offset:544\n"
"ds_read_b64 v[94:95], v20 offset:608\n"
"v_mfma_f32_16x16x32_bf16 v[252:255], a[56:59], a[184:187], v[252:255]\n"
"ds_read_b64 v[96:97], v20 offset:816\n"
"ds_read_b64 v[102:103], v20 offset:880\n"
"v_mfma_f32_16x16x32_bf16 v[104:107], a[120:123], a[184:187], v[104:107]\n"
"s_waitcnt vmcnt(11)\n"
"s_add_u32 m0, s34, 9216\n"
"ds_read_b128 v[32:35], v188\n"
"v_mfma_f32_16x16x32_bf16 v[108:111], a[124:127], a[184:187], v[108:111]\n"
"buffer_load_dwordx4 v191, s[8:11], s32 offen lds\n"
"ds_read_b128 v[36:39], v189\n"
"v_mfma_f32_16x16x32_bf16 v[228:231], a[128:131], a[184:187], v[228:231]\n"
"s_add_u32 s32, s32, s33\n"
"v_mfma_f32_16x16x32_bf16 v[244:247], a[60:63], a[188:191], v[244:247]\n"
"v_mfma_f32_16x16x32_bf16 v[248:251], a[64:67], a[188:191], v[248:251]\n"
"s_waitcnt lgkmcnt(7)\n"
"s_add_u32 m0, s34, 7168\n"
"v_mfma_f32_16x16x32_bf16 v[252:255], a[68:71], a[188:191], v[252:255]\n"
"s_waitcnt lgkmcnt(5)\n"
"buffer_load_dwordx4 v190, s[8:11], s0 offen lds\n"
"v_bfi_b32 v80, v4, v80, v26\n"
"v_mfma_f32_16x16x32_bf16 v[104:107], a[132:135], a[188:191], v[104:107]\n"
"s_waitcnt lgkmcnt(3)\n"
"v_cvt_pk_bf16_f32 v244, v244, v245\n"
"v_cvt_pk_bf16_f32 v245, v246, v247\n"
"v_mfma_f32_16x16x32_bf16 v[108:111], a[136:139], a[188:191], v[108:111]\n"
"s_waitcnt lgkmcnt(2)\n"
"v_cvt_pk_bf16_f32 v246, v248, v249\n"
"v_cvt_pk_bf16_f32 v247, v250, v251\n"
"v_mfma_f32_16x16x32_bf16 v[228:231], a[140:143], a[188:191], v[228:231]\n"
"s_waitcnt lgkmcnt(1)\n"
"ds_write_b128 v22, v[244:247]\n"
"v_mfma_f32_16x16x32_bf16 v[204:207], a[0:3], v[32:35], 0\n"
"s_waitcnt vmcnt(11) lgkmcnt(1)\n"
"v_cvt_pk_bf16_f32 v252, v252, v253\n"
"v_cvt_pk_bf16_f32 v253, v254, v255\n"
"v_mfma_f32_16x16x32_bf16 v[208:211], a[4:7], v[32:35], 0\n"
"s_add_u32 m0, s34, 10240\n"
"v_cvt_pk_bf16_f32 v104, v104, v105\n"
"v_cvt_pk_bf16_f32 v105, v106, v107\n"
"v_mfma_f32_16x16x32_bf16 v[212:215], a[8:11], v[32:35], 0\n"
"buffer_load_dwordx4 v191, s[8:11], s0 offen lds\n"
"ds_read_b128 v[40:43], v188 offset:1024\n"
"v_mfma_f32_16x16x32_bf16 v[216:219], a[72:75], v[32:35], 0\n"
"s_add_u32 s0, s0, s33\n"
"ds_read_b128 v[44:47], v189 offset:1024\n"
"v_cvt_pk_bf16_f32 v106, v108, v109\n"
"v_mfma_f32_16x16x32_bf16 v[220:223], a[76:79], v[32:35], 0\n"
"v_cvt_pk_bf16_f32 v107, v110, v111\n"
"v_cvt_pk_bf16_f32 v254, v228, v229\n"
"v_mfma_f32_16x16x32_bf16 v[224:227], a[80:83], v[32:35], 0\n"
"s_add_u32 m0, s34, 8192\n"
"ds_write_b128 v22, v[104:107] offset:128\n"
"v_mfma_f32_16x16x32_bf16 v[204:207], a[12:15], v[36:39], v[204:207]\n"
"buffer_load_dwordx4 v190, s[8:11], s1 offen lds\n"
"v_cvt_pk_bf16_f32 v255, v230, v231\n"
"v_mfma_f32_16x16x32_bf16 v[208:211], a[16:19], v[36:39], v[208:211]\n"
"s_waitcnt vmcnt(11)\n"
"v_bfi_b32 v81, v5, v81, v26\n"
"v_bfi_b32 v86, v6, v86, v26\n"
"v_mfma_f32_16x16x32_bf16 v[212:215], a[20:23], v[36:39], v[212:215]\n"
"s_add_u32 m0, s34, 11264\n"
"ds_read_b128 v[48:51], v188 offset:2048\n"
"ds_read_b128 v[52:55], v189 offset:2048\n"
"v_mfma_f32_16x16x32_bf16 v[216:219], a[84:87], v[36:39], v[216:219]\n"
"s_waitcnt lgkmcnt(4)\n"
"buffer_load_dwordx4 v191, s[8:11], s1 offen lds\n"
"ds_write_b128 v22, v[252:255] offset:64\n"
"v_mfma_f32_16x16x32_bf16 v[220:223], a[88:91], v[36:39], v[220:223]\n"
"s_add_u32 s1, s1, s33\n"
"v_bfi_b32 v87, v7, v87, v26\n"
"v_bfi_b32 v88, v0, v88, v26\n"
"v_mfma_f32_16x16x32_bf16 v[224:227], a[92:95], v[36:39], v[224:227]\n"
"v_bfi_b32 v89, v1, v89, v26\n"
"v_bfi_b32 v94, v2, v94, v26\n"
"v_mfma_f32_16x16x32_bf16 v[204:207], a[24:27], v[40:43], v[204:207]\n"
"s_waitcnt lgkmcnt(4)\n"
"v_bfi_b32 v95, v3, v95, v26\n"
"v_bfi_b32 v96, v0, v96, v26\n"
"v_mfma_f32_16x16x32_bf16 v[208:211], a[28:31], v[40:43], v[208:211]\n"
"v_bfi_b32 v97, v1, v97, v26\n"
"v_bfi_b32 v102, v2, v102, v26\n"
"v_mfma_f32_16x16x32_bf16 v[212:215], a[32:35], v[40:43], v[212:215]\n"
"v_bfi_b32 v103, v3, v103, v26\n"
"v_mfma_f32_16x16x32_bf16 v[216:219], a[96:99], v[40:43], v[216:219]\n"
"v_mfma_f32_16x16x32_bf16 v[220:223], a[100:103], v[40:43], v[220:223]\n"
"v_mfma_f32_16x16x32_bf16 v[224:227], a[104:107], v[40:43], v[224:227]\n"
"v_mfma_f32_16x16x32_bf16 v[204:207], a[36:39], v[44:47], v[204:207]\n"
"s_waitcnt lgkmcnt(2)\n"
"v_mfma_f32_16x16x32_bf16 v[208:211], a[40:43], v[44:47], v[208:211]\n"
"s_add_u32 m0, s65, 154752\n"
"v_mfma_f32_16x16x32_bf16 v[212:215], a[44:47], v[44:47], v[212:215]\n"
"buffer_load_dwordx4 v27, s[12:15], s81 offen lds\n"
"v_mfma_f32_16x16x32_bf16 v[216:219], a[108:111], v[44:47], v[216:219]\n"
"v_mfma_f32_16x16x32_bf16 v[220:223], a[112:115], v[44:47], v[220:223]\n"
"v_mfma_f32_16x16x32_bf16 v[224:227], a[116:119], v[44:47], v[224:227]\n"
"s_waitcnt vmcnt(11)\n"
"s_add_u32 m0, s65, 155776\n"
"v_mfma_f32_16x16x32_bf16 v[204:207], a[48:51], v[48:51], v[204:207]\n"
"s_waitcnt lgkmcnt(1)\n"
"buffer_load_dwordx4 v28, s[12:15], s81 offen lds\n"
"ds_read_b128 a[216:219], v70\n"
"v_mfma_f32_16x16x32_bf16 v[208:211], a[52:55], v[48:51], v[208:211]\n"
"ds_read_b128 a[220:223], v71\n"
"v_mfma_f32_16x16x32_bf16 v[212:215], a[56:59], v[48:51], v[212:215]\n"
"v_mfma_f32_16x16x32_bf16 v[216:219], a[120:123], v[48:51], v[216:219]\n"
"v_mfma_f32_16x16x32_bf16 v[220:223], a[124:127], v[48:51], v[220:223]\n"
"s_add_u32 m0, s65, 156800\n"
"s_nop 0\n"
"buffer_load_dwordx4 v29, s[12:15], s81 offen lds\n"
"v_mfma_f32_16x16x32_bf16 v[224:227], a[128:131], v[48:51], v[224:227]\n"
"v_mfma_f32_16x16x32_bf16 v[204:207], a[60:63], v[52:55], v[204:207]\n"
"v_mfma_f32_16x16x32_bf16 v[208:211], a[64:67], v[52:55], v[208:211]\n"
"v_mfma_f32_16x16x32_bf16 v[212:215], a[68:71], v[52:55], v[212:215]\n"
"s_waitcnt lgkmcnt(1)\n"
"v_mfma_f32_16x16x32_bf16 v[216:219], a[132:135], v[52:55], v[216:219]\n"
"v_mfma_f32_16x16x32_bf16 v[220:223], a[136:139], v[52:55], v[220:223]\n"
"v_mfma_f32_16x16x32_bf16 v[224:227], a[140:143], v[52:55], v[224:227]\n"
"v_mfma_f32_16x16x32_bf16 v[244:247], a[0:3], a[216:219], 0\n"
"s_waitcnt lgkmcnt(0)\n"
"v_cvt_pk_bf16_f32 v204, v204, v205\n"
"v_cvt_pk_bf16_f32 v205, v206, v207\n"
"v_mfma_f32_16x16x32_bf16 v[248:251], a[4:7], a[216:219], 0\n"
"v_cvt_pk_bf16_f32 v206, v208, v209\n"
"v_cvt_pk_bf16_f32 v207, v210, v211\n"
"v_mfma_f32_16x16x32_bf16 v[252:255], a[8:11], a[216:219], 0\n"
"ds_write_b128 v186, v[204:207] offset:20256\n"
"v_mfma_f32_16x16x32_bf16 v[104:107], a[72:75], a[216:219], 0\n"
"v_cvt_pk_bf16_f32 v212, v212, v213\n"
"v_cvt_pk_bf16_f32 v213, v214, v215\n"
"v_mfma_f32_16x16x32_bf16 v[108:111], a[76:79], a[216:219], 0\n"
"v_cvt_pk_bf16_f32 v216, v216, v217\n"
"v_cvt_pk_bf16_f32 v217, v218, v219\n"
"v_mfma_f32_16x16x32_bf16 v[228:231], a[80:83], a[216:219], 0\n"
"v_cvt_pk_bf16_f32 v218, v220, v221\n"
"v_cvt_pk_bf16_f32 v219, v222, v223\n"
"v_mfma_f32_16x16x32_bf16 v[244:247], a[12:15], a[220:223], v[244:247]\n"
"ds_write_b128 v186, v[216:219] offset:20384\n"
"v_mfma_f32_16x16x32_bf16 v[248:251], a[16:19], a[220:223], v[248:251]\n"
"v_cvt_pk_bf16_f32 v214, v224, v225\n"
"v_cvt_pk_bf16_f32 v215, v226, v227\n"
"v_mfma_f32_16x16x32_bf16 v[252:255], a[20:23], a[220:223], v[252:255]\n"
"ds_write_b128 v186, v[212:215] offset:20320\n"
"v_mfma_f32_16x16x32_bf16 v[104:107], a[84:87], a[220:223], v[104:107]\n"
"s_waitcnt vmcnt(11)\n"
"v_mfma_f32_16x16x32_bf16 v[108:111], a[88:91], a[220:223], v[108:111]\n"
"ds_read_b128 a[224:227], v70 offset:1024\n"
"v_mfma_f32_16x16x32_bf16 v[228:231], a[92:95], a[220:223], v[228:231]\n"
"ds_read_b128 a[228:231], v71 offset:1024\n"
"s_waitcnt vmcnt(9)\n"
"ds_read_b128 a[232:235], v70 offset:2048\n"
"ds_read_b128 a[236:239], v71 offset:2048\n"
"s_waitcnt lgkmcnt(3)\n"
"v_mfma_f32_16x16x32_bf16 v[244:247], a[24:27], a[224:227], v[244:247]\n"
"s_waitcnt lgkmcnt(2)\n"
"v_mfma_f32_16x16x32_bf16 v[248:251], a[28:31], a[224:227], v[248:251]\n"
"v_mfma_f32_16x16x32_bf16 v[252:255], a[32:35], a[224:227], v[252:255]\n"
"v_mfma_f32_16x16x32_bf16 v[104:107], a[96:99], a[224:227], v[104:107]\n"
"v_mfma_f32_16x16x32_bf16 v[108:111], a[100:103], a[224:227], v[108:111]\n"
"v_mfma_f32_16x16x32_bf16 v[228:231], a[104:107], a[224:227], v[228:231]\n"
"v_mfma_f32_16x16x32_bf16 v[244:247], a[36:39], a[228:231], v[244:247]\n"
"s_waitcnt lgkmcnt(1)\n"
"v_mfma_f32_16x16x32_bf16 v[248:251], a[40:43], a[228:231], v[248:251]\n"
"s_waitcnt vmcnt(7) lgkmcnt(0)\n"
"s_add_u32 m0, s35, 0\n"
"v_mfma_f32_16x16x32_bf16 v[252:255], a[44:47], a[228:231], v[252:255]\n"
"buffer_load_dwordx4 v27, s[12:15], s3 offen lds\n"
"ds_read_b128 a[240:243], v188 offset:6144\n"
"v_mfma_f32_16x16x32_bf16 v[104:107], a[108:111], a[228:231], v[104:107]\n"
"ds_read_b128 a[244:247], v189 offset:6144\n"
"v_mfma_f32_16x16x32_bf16 v[108:111], a[112:115], a[228:231], v[108:111]\n"
"s_add_u32 m0, s35, 1024\n"
"v_mfma_f32_16x16x32_bf16 v[228:231], a[116:119], a[228:231], v[228:231]\n"
"buffer_load_dwordx4 v28, s[12:15], s3 offen lds\n"
"v_mfma_f32_16x16x32_bf16 v[244:247], a[48:51], a[232:235], v[244:247]\n"
"v_mfma_f32_16x16x32_bf16 v[248:251], a[52:55], a[232:235], v[248:251]\n"
"v_mfma_f32_16x16x32_bf16 v[252:255], a[56:59], a[232:235], v[252:255]\n"
"s_add_u32 m0, s35, 2048\n"
"v_mfma_f32_16x16x32_bf16 v[104:107], a[120:123], a[232:235], v[104:107]\n"
"buffer_load_dwordx4 v29, s[12:15], s3 offen lds\n"
"v_mfma_f32_16x16x32_bf16 v[108:111], a[124:127], a[232:235], v[108:111]\n"
"s_waitcnt lgkmcnt(1)\n"
"v_mfma_f32_16x16x32_bf16 v[228:231], a[128:131], a[232:235], v[228:231]\n"
"v_mfma_f32_16x16x32_bf16 v[244:247], a[60:63], a[236:239], v[244:247]\n"
"s_add_u32 m0, s34, 0\n"
"v_mfma_f32_16x16x32_bf16 v[248:251], a[64:67], a[236:239], v[248:251]\n"
"buffer_load_dwordx4 v190, s[8:11], s32 offen lds\n"
"v_mfma_f32_16x16x32_bf16 v[252:255], a[68:71], a[236:239], v[252:255]\n"
"v_mfma_f32_16x16x32_bf16 v[104:107], a[132:135], a[236:239], v[104:107]\n"
"v_mfma_f32_16x16x32_bf16 v[108:111], a[136:139], a[236:239], v[108:111]\n"
"s_add_u32 m0, s34, 3072\n"
"v_mfma_f32_16x16x32_bf16 v[228:231], a[140:143], a[236:239], v[228:231]\n"
"buffer_load_dwordx4 v191, s[8:11], s32 offen lds\n"
"v_cvt_pk_bf16_f32 v244, v244, v245\n"
"v_mfma_f32_16x16x32_bf16 v[120:123], a[0:3], a[240:243], 0\n"
"s_waitcnt vmcnt(10) lgkmcnt(0)\n"
"v_cvt_pk_bf16_f32 v245, v246, v247\n"
"v_cvt_pk_bf16_f32 v246, v248, v249\n"
"v_mfma_f32_16x16x32_bf16 v[124:127], a[4:7], a[240:243], 0\n"
"s_add_u32 s32, s32, s33\n"
"ds_read_b128 a[248:251], v188 offset:7168\n"
"ds_read_b128 a[252:255], v189 offset:7168\n"
"v_mfma_f32_16x16x32_bf16 v[128:131], a[8:11], a[240:243], 0\n"
"s_add_u32 m0, s34, 1024\n"
"v_cvt_pk_bf16_f32 v247, v250, v251\n"
"v_cvt_pk_bf16_f32 v252, v252, v253\n"
"v_mfma_f32_16x16x32_bf16 v[132:135], a[72:75], a[240:243], 0\n"
"buffer_load_dwordx4 v190, s[8:11], s0 offen lds\n"
"ds_write_b128 v186, v[244:247] offset:33760\n"
"v_mfma_f32_16x16x32_bf16 v[136:139], a[76:79], a[240:243], 0\n"
"v_cvt_pk_bf16_f32 v253, v254, v255\n"
"v_cvt_pk_bf16_f32 v104, v104, v105\n"
"v_mfma_f32_16x16x32_bf16 v[140:143], a[80:83], a[240:243], 0\n"
"s_add_u32 m0, s34, 4096\n"
"v_cvt_pk_bf16_f32 v105, v106, v107\n"
"v_cvt_pk_bf16_f32 v106, v108, v109\n"
"v_mfma_f32_16x16x32_bf16 v[120:123], a[12:15], a[244:247], v[120:123]\n"
"buffer_load_dwordx4 v191, s[8:11], s0 offen lds\n"
"v_cvt_pk_bf16_f32 v107, v110, v111\n"
"v_mfma_f32_16x16x32_bf16 v[124:127], a[16:19], a[244:247], v[124:127]\n"
"s_add_u32 s0, s0, s33\n"
"ds_write_b128 v186, v[104:107] offset:33888\n"
"v_mfma_f32_16x16x32_bf16 v[128:131], a[20:23], a[244:247], v[128:131]\n"
"s_waitcnt vmcnt(10) lgkmcnt(3)\n"
"v_cvt_pk_bf16_f32 v254, v228, v229\n"
"v_cvt_pk_bf16_f32 v255, v230, v231\n"
"v_mfma_f32_16x16x32_bf16 v[132:135], a[84:87], a[244:247], v[132:135]\n"
"s_add_u32 m0, s34, 2048\n"
"s_nop 0\n"
"buffer_load_dwordx4 v190, s[8:11], s1 offen lds\n"
"v_mfma_f32_16x16x32_bf16 v[136:139], a[88:91], a[244:247], v[136:139]\n"
"ds_read_b128 v[56:59], v188 offset:8192\n"
"ds_read_b128 v[60:63], v189 offset:8192\n"
"v_mfma_f32_16x16x32_bf16 v[140:143], a[92:95], a[244:247], v[140:143]\n"
"ds_write_b128 v186, v[252:255] offset:33824\n"
"v_mfma_f32_16x16x32_bf16 v[120:123], a[24:27], a[248:251], v[120:123]\n"
"s_waitcnt lgkmcnt(5)\n"
"s_add_u32 m0, s34, 5120\n"
"v_mfma_f32_16x16x32_bf16 v[124:127], a[28:31], a[248:251], v[124:127]\n"
"buffer_load_dwordx4 v191, s[8:11], s1 offen lds\n"
"ds_read_b64 v[104:105], v20 offset:1088\n"
"v_mfma_f32_16x16x32_bf16 v[128:131], a[32:35], a[248:251], v[128:131]\n"
"s_add_u32 s1, s1, s33\n"
"ds_read_b64 v[110:111], v20 offset:1152\n"
"v_subrev_u32 v20, s49, v20\n"
"v_mfma_f32_16x16x32_bf16 v[132:135], a[96:99], a[248:251], v[132:135]\n"
"s_add_u32 m0, s34, 6144\n"
"v_mfma_f32_16x16x32_bf16 v[136:139], a[100:103], a[248:251], v[136:139]\n"
"v_mfma_f32_16x16x32_bf16 v[140:143], a[104:107], a[248:251], v[140:143]\n"
"buffer_load_dwordx4 v190, s[8:11], s32 offen lds\n"
"v_mfma_f32_16x16x32_bf16 v[120:123], a[36:39], a[252:255], v[120:123]\n"
"s_waitcnt lgkmcnt(4)\n"
"v_mfma_f32_16x16x32_bf16 v[124:127], a[40:43], a[252:255], v[124:127]\n"
"v_mfma_f32_16x16x32_bf16 v[128:131], a[44:47], a[252:255], v[128:131]\n"
"s_waitcnt lgkmcnt(1)\n"
"v_mfma_f32_16x16x32_bf16 v[132:135], a[108:111], a[252:255], v[132:135]\n"
"s_waitcnt lgkmcnt(0)\n"
"v_bfi_b32 v104, v8, v104, v26\n"
"v_bfi_b32 v105, v9, v105, v26\n"
"v_mfma_f32_16x16x32_bf16 v[136:139], a[112:115], a[252:255], v[136:139]\n"
"v_bfi_b32 v110, v10, v110, v26\n"
"v_bfi_b32 v111, v11, v111, v26\n"
"v_mfma_f32_16x16x32_bf16 v[140:143], a[116:119], a[252:255], v[140:143]\n"
"v_mfma_f32_16x16x32_bf16 v[120:123], a[48:51], v[56:59], v[120:123]\n"
"v_mfma_f32_16x16x32_bf16 v[124:127], a[52:55], v[56:59], v[124:127]\n"
"s_add_u32 m0, s34, 9216\n"
"v_mfma_f32_16x16x32_bf16 v[128:131], a[56:59], v[56:59], v[128:131]\n"
"buffer_load_dwordx4 v191, s[8:11], s32 offen lds\n"
"s_add_u32 s32, s32, s33\n"
"v_mfma_f32_16x16x32_bf16 v[132:135], a[120:123], v[56:59], v[132:135]\n"
"v_mfma_f32_16x16x32_bf16 v[136:139], a[124:127], v[56:59], v[136:139]\n"
"v_mfma_f32_16x16x32_bf16 v[140:143], a[128:131], v[56:59], v[140:143]\n"
"s_add_u32 m0, s34, 7168\n"
"v_mfma_f32_16x16x32_bf16 v[120:123], a[60:63], v[60:63], v[120:123]\n"
"buffer_load_dwordx4 v190, s[8:11], s0 offen lds\n"
"v_mfma_f32_16x16x32_bf16 v[124:127], a[64:67], v[60:63], v[124:127]\n"
"v_mfma_f32_16x16x32_bf16 v[128:131], a[68:71], v[60:63], v[128:131]\n"
"v_mfma_f32_16x16x32_bf16 v[132:135], a[132:135], v[60:63], v[132:135]\n"
"s_add_u32 m0, s34, 10240\n"
"v_mfma_f32_16x16x32_bf16 v[136:139], a[136:139], v[60:63], v[136:139]\n"
"buffer_load_dwordx4 v191, s[8:11], s0 offen lds\n"
"s_add_u32 s0, s0, s33\n"
"v_mfma_f32_16x16x32_bf16 v[140:143], a[140:143], v[60:63], v[140:143]\n"
"v_cvt_pk_bf16_f32 v120, v120, v121\n"
"v_cvt_pk_bf16_f32 v121, v122, v123\n"
"v_cvt_pk_bf16_f32 v122, v124, v125\n"
"v_cvt_pk_bf16_f32 v123, v126, v127\n"
"v_cvt_pk_bf16_f32 v128, v128, v129\n"
"v_cvt_pk_bf16_f32 v132, v132, v133\n"
"v_cvt_pk_bf16_f32 v129, v130, v131\n"
"v_cvt_pk_bf16_f32 v133, v134, v135\n"
"v_cvt_pk_bf16_f32 v134, v136, v137\n"
"s_add_u32 m0, s34, 8192\n"
"v_cvt_pk_bf16_f32 v135, v138, v139\n"
"buffer_load_dwordx4 v190, s[8:11], s1 offen lds\n"
"v_cvt_pk_bf16_f32 v130, v140, v141\n"
"v_cvt_pk_bf16_f32 v131, v142, v143\n"
"ds_write_b128 v186, v[120:123] offset:47264\n"
"s_add_u32 m0, s34, 11264\n"
"s_nop 0\n"
"buffer_load_dwordx4 v191, s[8:11], s1 offen lds\n"
"s_add_u32 s1, s1, s33\n"
"s_waitcnt vmcnt(12)\n"
"s_barrier\n"
"ds_read_b64_tr_b16 a[144:145], v68\n"
"ds_read_b64_tr_b16 a[146:147], v68 offset:1536\n"
"ds_read_b64_tr_b16 a[148:149], v68 offset:32\n"
"ds_read_b64_tr_b16 a[150:151], v68 offset:1568\n"
"ds_read_b64_tr_b16 a[152:153], v68 offset:64\n"
"ds_read_b64_tr_b16 a[154:155], v68 offset:1600\n"
"ds_read_b64_tr_b16 a[156:157], v68 offset:6144\n"
"ds_read_b64_tr_b16 a[158:159], v68 offset:7680\n"
"ds_read_b64_tr_b16 a[160:161], v68 offset:6176\n"
"ds_read_b64_tr_b16 a[162:163], v68 offset:7712\n"
"ds_read_b64_tr_b16 a[164:165], v68 offset:6208\n"
"ds_read_b64_tr_b16 a[166:167], v68 offset:7744\n"
"ds_read_b64_tr_b16 a[168:169], v68 offset:12288\n"
"ds_read_b64_tr_b16 a[170:171], v68 offset:13824\n"
"ds_read_b64_tr_b16 a[172:173], v68 offset:12320\n"
"ds_read_b64_tr_b16 a[174:175], v68 offset:13856\n"
"ds_read_b64_tr_b16 a[176:177], v68 offset:12352\n"
"ds_read_b64_tr_b16 a[178:179], v68 offset:13888\n"
"ds_read_b64_tr_b16 a[180:181], v68 offset:18432\n"
"ds_read_b64_tr_b16 a[182:183], v68 offset:19968\n"
"s_waitcnt lgkmcnt(15)\n"
"v_mfma_f32_16x16x32_bf16 v[204:207], a[144:147], v[32:35], 0\n"
"ds_read_b64_tr_b16 a[184:185], v68 offset:18464\n"
"ds_read_b64_tr_b16 a[186:187], v68 offset:20000\n"
"v_mfma_f32_16x16x32_bf16 v[208:211], a[148:151], v[32:35], 0\n"
"s_waitcnt lgkmcnt(15)\n"
"ds_read_b64_tr_b16 a[188:189], v68 offset:18496\n"
"ds_read_b64_tr_b16 a[190:191], v68 offset:20032\n"
"v_mfma_f32_16x16x32_bf16 v[212:215], a[152:155], v[32:35], 0\n"
"s_waitcnt lgkmcnt(15)\n"
"ds_read_b64_tr_b16 a[192:193], v68 offset:24576\n"
"ds_read_b64_tr_b16 a[194:195], v68 offset:26112\n"
"v_mfma_f32_16x16x32_bf16 v[204:207], a[156:159], v[36:39], v[204:207]\n"
"s_waitcnt lgkmcnt(15)\n"
"ds_read_b64_tr_b16 a[196:197], v68 offset:24608\n"
"ds_read_b64_tr_b16 a[198:199], v68 offset:26144\n"
"v_mfma_f32_16x16x32_bf16 v[208:211], a[160:163], v[36:39], v[208:211]\n"
"s_waitcnt lgkmcnt(15)\n"
"ds_read_b64_tr_b16 a[200:201], v68 offset:24640\n"
"ds_read_b64_tr_b16 a[202:203], v68 offset:26176\n"
"v_mfma_f32_16x16x32_bf16 v[212:215], a[164:167], v[36:39], v[212:215]\n"
"s_waitcnt lgkmcnt(15)\n"
"ds_read_b64_tr_b16 a[204:205], v68 offset:27648\n"
"ds_read_b64_tr_b16 a[206:207], v68 offset:29184\n"
"v_mfma_f32_16x16x32_bf16 v[204:207], a[168:171], v[40:43], v[204:207]\n"
"s_waitcnt lgkmcnt(15)\n"
"ds_read_b64_tr_b16 a[208:209], v68 offset:27680\n"
"ds_read_b64_tr_b16 a[210:211], v68 offset:29216\n"
"v_mfma_f32_16x16x32_bf16 v[208:211], a[172:175], v[40:43], v[208:211]\n"
"s_waitcnt lgkmcnt(15)\n"
"ds_read_b64_tr_b16 a[212:213], v68 offset:27712\n"
"ds_read_b64_tr_b16 a[214:215], v68 offset:29248\n"
"v_mfma_f32_16x16x32_bf16 v[212:215], a[176:179], v[40:43], v[212:215]\n"
"s_waitcnt lgkmcnt(15)\n"
"buffer_load_dword v200, off, s[28:31], s39 sc1\n"
"ds_write_b128 v186, v[128:131] offset:47328\n"
"v_mfma_f32_16x16x32_bf16 v[204:207], a[180:183], v[44:47], v[204:207]\n"
"s_waitcnt lgkmcnt(13)\n"
"v_mfma_f32_16x16x32_bf16 v[208:211], a[184:187], v[44:47], v[208:211]\n"
"s_waitcnt lgkmcnt(11)\n"
"ds_write_b128 v186, v[132:135] offset:47392\n"
"v_mfma_f32_16x16x32_bf16 v[212:215], a[188:191], v[44:47], v[212:215]\n"
"s_waitcnt lgkmcnt(8)\n"
"v_mfma_f32_16x16x32_bf16 v[204:207], a[192:195], v[48:51], v[204:207]\n"
"s_waitcnt lgkmcnt(6)\n"
"v_mfma_f32_16x16x32_bf16 v[208:211], a[196:199], v[48:51], v[208:211]\n"
"s_waitcnt lgkmcnt(4)\n"
"v_mfma_f32_16x16x32_bf16 v[212:215], a[200:203], v[48:51], v[212:215]\n"
"s_waitcnt lgkmcnt(2)\n"
"v_mfma_f32_16x16x32_bf16 v[204:207], a[204:207], v[52:55], v[204:207]\n"
"v_mfma_f32_16x16x32_bf16 v[208:211], a[208:211], v[52:55], v[208:211]\n"
"v_mfma_f32_16x16x32_bf16 v[212:215], a[212:215], v[52:55], v[212:215]\n"
"v_mfma_f32_16x16x32_bf16 v[216:219], a[144:147], a[216:219], 0\n"
"s_waitcnt lgkmcnt(0)\n"
"s_barrier\n"
"v_mfma_f32_16x16x32_bf16 v[216:219], a[156:159], a[220:223], v[216:219]\n"
"ds_read_b128 v[112:115], v178\n"
"ds_read_b128 v[116:119], v179\n"
"v_mfma_f32_16x16x32_bf16 v[220:223], a[148:151], a[216:219], 0\n"
"ds_read_b128 v[120:123], v178 offset:13504\n"
"v_cvt_pk_bf16_f32 v144, v204, v205\n"
"v_mfma_f32_16x16x32_bf16 v[216:219], a[168:171], a[224:227], v[216:219]\n"
"ds_read_b128 v[124:127], v179 offset:13504\n"
"v_cvt_pk_bf16_f32 v145, v206, v207\n"
"v_mfma_f32_16x16x32_bf16 v[220:223], a[160:163], a[220:223], v[220:223]\n"
"ds_read_b128 v[128:131], v178 offset:27008\n"
"v_cvt_pk_bf16_f32 v146, v208, v209\n"
"v_mfma_f32_16x16x32_bf16 v[216:219], a[180:183], a[228:231], v[216:219]\n"
"ds_read_b128 v[132:135], v179 offset:27008\n"
"v_cvt_pk_bf16_f32 v147, v210, v211\n"
"v_mfma_f32_16x16x32_bf16 v[220:223], a[172:175], a[224:227], v[220:223]\n"
"ds_read_b128 v[136:139], v180\n"
"v_cvt_pk_bf16_f32 v158, v212, v213\n"
"v_mfma_f32_16x16x32_bf16 v[216:219], a[192:195], a[232:235], v[216:219]\n"
"ds_read_b128 v[140:143], v181\n"
"v_cvt_pk_bf16_f32 v159, v214, v215\n"
"v_mfma_f32_16x16x32_bf16 v[220:223], a[184:187], a[228:231], v[220:223]\n"
"ds_read_b64 v[82:83], v178 offset:64\n"
"v_mov_b64 v[68:69], -1\n"
"v_mfma_f32_16x16x32_bf16 v[216:219], a[204:207], a[236:239], v[216:219]\n"
"s_waitcnt lgkmcnt(8)\n"
"ds_read_b64 v[84:85], v179 offset:64\n"
"v_mov_b64 v[70:71], -1\n"
"v_mfma_f32_16x16x32_bf16 v[112:115], v[112:115], v[144:147], 0\n"
"s_waitcnt lgkmcnt(8)\n"
"ds_read_b64 v[90:91], v178 offset:13568\n"
"v_mfma_f32_16x16x32_bf16 v[116:119], v[116:119], v[144:147], 0\n"
"s_waitcnt lgkmcnt(8)\n"
"ds_read_b64 v[92:93], v179 offset:13568\n"
"v_cvt_pk_bf16_f32 v148, v216, v217\n"
"v_mfma_f32_16x16x32_bf16 v[120:123], v[120:123], v[144:147], 0\n"
"s_waitcnt lgkmcnt(8)\n"
"ds_read_b64 v[98:99], v178 offset:27072\n"
"v_cvt_pk_bf16_f32 v149, v218, v219\n"
"v_mfma_f32_16x16x32_bf16 v[124:127], v[124:127], v[144:147], 0\n"
"s_waitcnt lgkmcnt(8)\n"
"ds_read_b64 v[100:101], v179 offset:27072\n"
"v_subrev_u32 v178, s48, v178\n"
"v_mfma_f32_16x16x32_bf16 v[128:131], v[128:131], v[144:147], 0\n"
"s_waitcnt lgkmcnt(8)\n"
"ds_read_b64 v[106:107], v180 offset:64\n"
"v_subrev_u32 v179, s48, v179\n"
"v_mfma_f32_16x16x32_bf16 v[132:135], v[132:135], v[144:147], 0\n"
"s_waitcnt lgkmcnt(8)\n"
"ds_read_b64 v[108:109], v181 offset:64\n"
"v_subrev_u32 v180, s48, v180\n"
"v_mfma_f32_16x16x32_bf16 v[136:139], v[136:139], v[144:147], 0\n"
"s_waitcnt lgkmcnt(8)\n"
"ds_read_b64_tr_b16 v[244:245], v182 offset:128\n"
"v_subrev_u32 v181, s48, v181\n"
"v_mfma_f32_16x16x32_bf16 v[140:143], v[140:143], v[144:147], 0\n"
"s_waitcnt vmcnt(11) lgkmcnt(8)\n"
".Lic0%=:\n"
"s_cbranch_execz .Lib0%=\n"
"ds_read_b64_tr_b16 v[246:247], v183 offset:128\n"
"v_mfma_f32_16x16x32_bf16 v[112:115], v[80:83], v[156:159], v[112:115]\n"
"s_waitcnt lgkmcnt(8)\n"
"ds_read_b128 v[32:35], v188\n"
"v_mfma_f32_16x16x32_bf16 v[116:119], v[84:87], v[158:161], v[116:119]\n"
"s_waitcnt lgkmcnt(8)\n"
"ds_read_b128 v[36:39], v189\n"
"v_mfma_f32_16x16x32_bf16 v[120:123], v[88:91], v[156:159], v[120:123]\n"
"s_waitcnt lgkmcnt(8)\n"
"ds_read_b64_tr_b16 v[248:249], v182 offset:136\n"
"v_mfma_f32_16x16x32_bf16 v[124:127], v[92:95], v[158:161], v[124:127]\n"
"s_waitcnt lgkmcnt(8)\n"
"ds_read_b64_tr_b16 v[250:251], v183 offset:136\n"
"v_max_f32 v112, v112, v116\n"
"v_mfma_f32_16x16x32_bf16 v[128:131], v[96:99], v[156:159], v[128:131]\n"
"s_waitcnt lgkmcnt(8)\n"
"ds_read_b64_tr_b16 v[252:253], v182 offset:72\n"
"v_max_f32 v113, v113, v117\n"
"v_mfma_f32_16x16x32_bf16 v[132:135], v[100:103], v[158:161], v[132:135]\n"
"s_waitcnt lgkmcnt(8)\n"
"ds_read_b64_tr_b16 v[254:255], v183 offset:72\n"
"v_max_f32 v114, v114, v118\n"
"v_mfma_f32_16x16x32_bf16 v[136:139], v[104:107], v[156:159], v[136:139]\n"
"s_waitcnt lgkmcnt(8)\n"
"ds_read_b64_tr_b16 v[116:117], v182 offset:13632\n"
"v_max_f32 v115, v115, v119\n"
"v_mfma_f32_16x16x32_bf16 v[140:143], v[108:111], v[158:161], v[140:143]\n"
"s_waitcnt vmcnt(9)\n"
"ds_read_b64_tr_b16 v[118:119], v183 offset:13632\n"
"v_max_f32 v170, v112, v113\n"
"v_mfma_f32_16x16x32_bf16 v[220:223], a[196:199], a[232:235], v[220:223]\n"
"ds_read_b128 v[40:43], v188 offset:1024\n"
"v_max_f32 v171, v114, v115\n"
"v_mfma_f32_16x16x32_bf16 v[220:223], a[208:211], a[236:239], v[220:223]\n"
"s_waitcnt lgkmcnt(8)\n"
"ds_read_b128 v[44:47], v189 offset:1024\n"
"v_max_f32 v120, v120, v124\n"
"v_mfma_f32_16x16x32_bf16 v[212:215], a[0:3], v[32:35], 0\n"
"s_waitcnt lgkmcnt(8)\n"
"ds_read_b64_tr_b16 v[98:99], v182 offset:27144\n"
"v_max_f32 v121, v121, v125\n"
"v_mfma_f32_16x16x32_bf16 v[216:219], a[4:7], v[32:35], 0\n"
"s_add_u32 m0, s34, 0\n"
"ds_read_b64_tr_b16 v[124:125], v182 offset:13640\n"
"buffer_load_dwordx4 v190, s[8:11], s32 offen lds\n"
"v_mfma_f32_16x16x32_bf16 v[212:215], a[12:15], v[36:39], v[212:215]\n"
"ds_read_b64_tr_b16 v[100:101], v183 offset:27144\n"
"v_cvt_pk_bf16_f32 v150, v220, v221\n"
"v_mfma_f32_16x16x32_bf16 v[216:219], a[16:19], v[36:39], v[216:219]\n"
"ds_read_b64_tr_b16 v[106:107], v182 offset:27080\n"
"v_cvt_pk_bf16_f32 v151, v222, v223\n"
"v_mfma_f32_16x16x32_bf16 v[220:223], a[8:11], v[32:35], 0\n"
"ds_read_b64_tr_b16 v[108:109], v183 offset:27080\n"
"v_max_f32 v122, v122, v126\n"
"v_mfma_f32_16x16x32_bf16 v[220:223], a[20:23], v[36:39], v[220:223]\n"
"v_max_f32 v123, v123, v127\n"
"v_max3_f32 v170, v170, v120, v121\n"
"v_mfma_f32_16x16x32_bf16 v[224:227], a[152:155], a[216:219], 0\n"
"s_waitcnt vmcnt(8)\n"
"ds_read_b128 v[48:51], v188 offset:2048\n"
"ds_read_b128 v[52:55], v189 offset:2048\n"
"v_mfma_f32_16x16x32_bf16 v[224:227], a[164:167], a[220:223], v[224:227]\n"
"s_waitcnt lgkmcnt(8)\n"
"ds_read_b64_tr_b16 v[126:127], v183 offset:13640\n"
"v_max3_f32 v171, v171, v122, v123\n"
"v_mfma_f32_16x16x32_bf16 v[212:215], a[24:27], v[40:43], v[212:215]\n"
"s_waitcnt lgkmcnt(8)\n"
"v_max_f32 v128, v128, v132\n"
"v_max_f32 v129, v129, v133\n"
"v_mfma_f32_16x16x32_bf16 v[216:219], a[28:31], v[40:43], v[216:219]\n"
"ds_read_b64_tr_b16 v[132:133], v182 offset:13576\n"
"v_max_f32 v130, v130, v134\n"
"v_mfma_f32_16x16x32_bf16 v[220:223], a[32:35], v[40:43], v[220:223]\n"
"v_max_f32 v131, v131, v135\n"
"v_max3_f32 v170, v170, v128, v129\n"
"v_mfma_f32_16x16x32_bf16 v[212:215], a[36:39], v[44:47], v[212:215]\n"
"s_add_u32 m0, s34, 3072\n"
"ds_read_b64_tr_b16 v[134:135], v183 offset:13576\n"
"buffer_load_dwordx4 v191, s[8:11], s32 offen lds\n"
"v_mfma_f32_16x16x32_bf16 v[216:219], a[40:43], v[44:47], v[216:219]\n"
"s_add_u32 s32, s32, s33\n"
"v_max3_f32 v171, v171, v130, v131\n"
"v_max_f32 v136, v136, v140\n"
"v_mfma_f32_16x16x32_bf16 v[220:223], a[44:47], v[44:47], v[220:223]\n"
"v_max_f32 v137, v137, v141\n"
"v_max_f32 v138, v138, v142\n"
"v_mfma_f32_16x16x32_bf16 v[228:231], a[144:147], a[240:243], 0\n"
"s_waitcnt lgkmcnt(4)\n"
"ds_read_b64_tr_b16 v[140:141], v182 offset:27136\n"
"v_max_f32 v139, v139, v143\n"
"v_mfma_f32_16x16x32_bf16 v[212:215], a[48:51], v[48:51], v[212:215]\n"
"s_waitcnt lgkmcnt(4)\n"
"ds_read_b64_tr_b16 v[142:143], v183 offset:27136\n"
"v_max3_f32 v170, v170, v136, v137\n"
"v_mfma_f32_16x16x32_bf16 v[216:219], a[52:55], v[48:51], v[216:219]\n"
"v_max3_f32 v171, v171, v138, v139\n"
"v_max_f32 v170, v170, v171\n"
"v_mfma_f32_16x16x32_bf16 v[220:223], a[56:59], v[48:51], v[220:223]\n"
"v_mov_b32 v171, v170\n"
"v_subrev_u32 v182, s48, v182\n"
"v_mfma_f32_16x16x32_bf16 v[212:215], a[60:63], v[52:55], v[212:215]\n"
"v_permlane16_swap_b32 v170, v171\n"
"v_mfma_f32_16x16x32_bf16 v[216:219], a[64:67], v[52:55], v[216:219]\n"
"v_max_f32 v170, v170, v171\n"
"v_mov_b32 v171, v170\n"
"v_mfma_f32_16x16x32_bf16 v[220:223], a[68:71], v[52:55], v[220:223]\n"
"s_nop 0\n"
"v_permlane32_swap_b32 v170, v171\n"
"v_mfma_f32_16x16x32_bf16 v[224:227], a[176:179], a[224:227], v[224:227]\n"
"s_add_u32 m0, s34, 1024\n"
"v_max_f32 v170, v170, v171\n"
"v_mul_f32 v170, 0xbe553b95, v170\n"
"v_mfma_f32_16x16x32_bf16 v[228:231], a[156:159], a[244:247], v[228:231]\n"
"buffer_load_dwordx4 v190, s[8:11], s0 offen lds\n"
"v_fmamk_f32 v112, v112, 0x3e553b95, v170\n"
"v_mfma_f32_16x16x32_bf16 v[204:207], a[148:151], a[240:243], 0\n"
"v_fmamk_f32 v113, v113, 0x3e553b95, v170\n"
"v_fmamk_f32 v114, v114, 0x3e553b95, v170\n"
"v_mfma_f32_16x16x32_bf16 v[224:227], a[188:191], a[228:231], v[224:227]\n"
"v_fmamk_f32 v115, v115, 0x3e553b95, v170\n"
"v_fmamk_f32 v120, v120, 0x3e553b95, v170\n"
"v_mfma_f32_16x16x32_bf16 v[228:231], a[168:171], a[248:251], v[228:231]\n"
"v_exp_f32 v112, v112\n"
"v_mfma_f32_16x16x32_bf16 v[204:207], a[160:163], a[244:247], v[204:207]\n"
"v_exp_f32 v113, v113\n"
"v_mfma_f32_16x16x32_bf16 v[224:227], a[200:203], a[232:235], v[224:227]\n"
"v_exp_f32 v114, v114\n"
"v_mfma_f32_16x16x32_bf16 v[228:231], a[180:183], a[252:255], v[228:231]\n"
"v_exp_f32 v115, v115\n"
"v_mfma_f32_16x16x32_bf16 v[204:207], a[172:175], a[248:251], v[204:207]\n"
"v_cvt_pk_bf16_f32 v82, v112, v113\n"
"v_cvt_pk_bf16_f32 v83, v114, v115\n"
"v_mfma_f32_16x16x32_bf16 v[224:227], a[212:215], a[236:239], v[224:227]\n"
"v_fmamk_f32 v121, v121, 0x3e553b95, v170\n"
"v_fmamk_f32 v122, v122, 0x3e553b95, v170\n"
"v_mfma_f32_16x16x32_bf16 v[228:231], a[192:195], v[56:59], v[228:231]\n"
"v_fmamk_f32 v123, v123, 0x3e553b95, v170\n"
"v_and_b32 v112, v82, v176\n"
"v_mfma_f32_16x16x32_bf16 v[204:207], a[184:187], a[252:255], v[204:207]\n"
"s_add_u32 m0, s34, 4096\n"
"v_and_b32 v113, v83, v177\n"
"v_xor_b32 v114, v82, v112\n"
"v_mfma_f32_16x16x32_bf16 v[228:231], a[204:207], v[60:63], v[228:231]\n"
"buffer_load_dwordx4 v191, s[8:11], s0 offen lds\n"
"v_xor_b32 v115, v83, v113\n"
"v_mfma_f32_16x16x32_bf16 v[204:207], a[196:199], v[56:59], v[204:207]\n"
"s_waitcnt vmcnt(4)\n"
"v_exp_f32 v120, v120\n"
"v_mfma_f32_16x16x32_bf16 v[244:247], v[244:247], v[112:115], 0\n"
"s_add_u32 s0, s0, s33\n"
"v_exp_f32 v121, v121\n"
"v_mfma_f32_16x16x32_bf16 v[248:251], v[248:251], v[112:115], 0\n"
"v_exp_f32 v122, v122\n"
"v_mfma_f32_16x16x32_bf16 v[252:255], v[252:255], v[112:115], 0\n"
"ds_read_b64_tr_b16 v[112:113], v184 offset:128\n"
"ds_read_b64_tr_b16 v[114:115], v185 offset:128\n"
"v_mfma_f32_16x16x32_bf16 v[204:207], a[208:211], v[60:63], v[204:207]\n"
"v_exp_f32 v123, v123\n"
"v_mfma_f32_16x16x32_bf16 v[208:211], a[152:155], a[240:243], 0\n"
"v_cvt_pk_bf16_f32 v84, v120, v121\n"
"v_cvt_pk_bf16_f32 v85, v122, v123\n"
"v_mfma_f32_16x16x32_bf16 v[208:211], a[164:167], a[244:247], v[208:211]\n"
"v_and_b32 v120, v84, v176\n"
"v_and_b32 v121, v85, v177\n"
"v_mfma_f32_16x16x32_bf16 v[208:211], a[176:179], a[248:251], v[208:211]\n"
"v_xor_b32 v122, v84, v120\n"
"v_xor_b32 v123, v85, v121\n"
"v_mfma_f32_16x16x32_bf16 v[82:85], v[172:175], v[82:85], 0\n"
"s_waitcnt lgkmcnt(4)\n"
"v_fmamk_f32 v128, v128, 0x3e553b95, v170\n"
"v_fmamk_f32 v129, v129, 0x3e553b95, v170\n"
"v_mfma_f32_16x16x32_bf16 v[244:247], v[116:119], v[120:123], v[244:247]\n"
"s_add_u32 m0, s34, 2048\n"
"ds_read_b64_tr_b16 v[116:117], v184 offset:72\n"
"buffer_load_dwordx4 v190, s[8:11], s1 offen lds\n"
"v_mfma_f32_16x16x32_bf16 v[248:251], v[124:127], v[120:123], v[248:251]\n"
"ds_read_b64_tr_b16 v[118:119], v185 offset:72\n"
"v_fmamk_f32 v130, v130, 0x3e553b95, v170\n"
"v_mfma_f32_16x16x32_bf16 v[252:255], v[132:135], v[120:123], v[252:255]\n"
"ds_read_b64_tr_b16 v[120:121], v184 offset:136\n"
"ds_read_b64_tr_b16 v[122:123], v185 offset:136\n"
"v_mfma_f32_16x16x32_bf16 v[208:211], a[188:191], a[252:255], v[208:211]\n"
"v_fmamk_f32 v131, v131, 0x3e553b95, v170\n"
"v_fmamk_f32 v136, v136, 0x3e553b95, v170\n"
"v_mfma_f32_16x16x32_bf16 v[208:211], a[200:203], v[56:59], v[208:211]\n"
"v_exp_f32 v128, v128\n"
"v_mfma_f32_16x16x32_bf16 v[208:211], a[212:215], v[60:63], v[208:211]\n"
"v_exp_f32 v129, v129\n"
"v_exp_f32 v130, v130\n"
"v_exp_f32 v131, v131\n"
"v_fmamk_f32 v137, v137, 0x3e553b95, v170\n"
"v_fmamk_f32 v138, v138, 0x3e553b95, v170\n"
"v_fmamk_f32 v139, v139, 0x3e553b95, v170\n"
"v_cvt_pk_bf16_f32 v90, v128, v129\n"
"v_cvt_pk_bf16_f32 v91, v130, v131\n"
"v_exp_f32 v136, v136\n"
"v_exp_f32 v137, v137\n"
"v_exp_f32 v138, v138\n"
"v_exp_f32 v139, v139\n"
"s_add_u32 m0, s34, 5120\n"
"v_and_b32 v128, v90, v176\n"
"buffer_load_dwordx4 v191, s[8:11], s1 offen lds\n"
"s_add_u32 s1, s1, s33\n"
"v_and_b32 v129, v91, v177\n"
"v_xor_b32 v130, v90, v128\n"
"v_xor_b32 v131, v91, v129\n"
"v_cvt_pk_bf16_f32 v92, v136, v137\n"
"v_cvt_pk_bf16_f32 v93, v138, v139\n"
"v_mfma_f32_16x16x32_bf16 v[248:251], v[98:101], v[128:131], v[248:251]\n"
"s_waitcnt lgkmcnt(6)\n"
"v_and_b32 v136, v92, v176\n"
"v_and_b32 v137, v93, v177\n"
"v_mfma_f32_16x16x32_bf16 v[244:247], v[140:143], v[128:131], v[244:247]\n"
"v_xor_b32 v138, v92, v136\n"
"v_xor_b32 v139, v93, v137\n"
"v_mfma_f32_16x16x32_bf16 v[90:93], v[172:175], v[90:93], v[82:85]\n"
"s_waitcnt lgkmcnt(0)\n"
"v_and_b32 v200, 0xffffff00, v200\n"
"v_add_u32 v200, 0x100, v200\n"
"v_mfma_f32_16x16x32_bf16 v[252:255], v[106:109], v[128:131], v[252:255]\n"
"s_mov_b64 exec, 1\n"
"buffer_store_dword v200, off, s[28:31], s41 sc1\n"
"s_mov_b64 exec, -1\n"
"v_mfma_f32_16x16x32_bf16 v[244:247], v[112:115], v[136:139], v[244:247]\n"
"s_add_u32 s41, s40, 131072\n"
"v_rcp_f32 v171, v90\n"
"v_mfma_f32_16x16x32_bf16 v[248:251], v[120:123], v[136:139], v[248:251]\n"
"v_sub_u32 v201, 0, v200\n"
"v_cvt_pk_bf16_f32 v212, v212, v213\n"
"v_mfma_f32_16x16x32_bf16 v[252:255], v[116:119], v[136:139], v[252:255]\n"
"s_nop 0\n"
"v_readfirstlane_b32 s50, v201\n"
"v_mul_f32 v244, v171, v244\n"
"v_mul_f32 v245, v171, v245\n"
"v_mul_f32 v246, v171, v246\n"
"v_mul_f32 v247, v171, v247\n"
"v_cvt_pk_bf16_f32 v244, v244, v245\n"
"v_cvt_pk_bf16_f32 v245, v246, v247\n"
"v_mul_f32 v248, v171, v248\n"
"v_mul_f32 v249, v171, v249\n"
"v_mul_f32 v250, v171, v250\n"
"v_mul_f32 v251, v171, v251\n"
"v_cvt_pk_bf16_f32 v246, v248, v249\n"
"v_cvt_pk_bf16_f32 v247, v250, v251\n"
"buffer_store_dwordx4 v[244:247], v192, s[28:31], s37 offen sc1\n"
"v_mul_f32 v252, v171, v252\n"
"v_mul_f32 v253, v171, v253\n"
"v_mul_f32 v254, v171, v254\n"
"v_mul_f32 v255, v171, v255\n"
"v_cvt_pk_bf16_f32 v252, v252, v253\n"
"v_cvt_pk_bf16_f32 v253, v254, v255\n"
"v_cvt_pk_bf16_f32 v213, v214, v215\n"
"buffer_store_dwordx2 v[252:253], v193, s[28:31], s37 offen offset:1024 sc1\n"
"s_add_u32 s37, s37, 6144\n"
"s_add_u32 s4, s37, 61440\n"
"v_cvt_pk_bf16_f32 v214, v216, v217\n"
"v_cvt_pk_bf16_f32 v215, v218, v219\n"
"v_mfma_f32_16x16x32_bf16 v[216:219], a[148:151], v[32:35], 0\n"
"ds_write_b128 v186, v[212:215] offset:60768\n"
"v_mfma_f32_16x16x32_bf16 v[212:215], a[152:155], v[32:35], 0\n"
"v_cvt_pk_bf16_f32 v220, v220, v221\n"
"v_cvt_pk_bf16_f32 v221, v222, v223\n"
"v_mfma_f32_16x16x32_bf16 v[216:219], a[160:163], v[36:39], v[216:219]\n"
"v_cvt_pk_bf16_f32 v162, v224, v225\n"
"v_cvt_pk_bf16_f32 v163, v226, v227\n"
"v_mfma_f32_16x16x32_bf16 v[224:227], a[72:75], v[32:35], 0\n"
"v_cvt_pk_bf16_f32 v152, v228, v229\n"
"v_cvt_pk_bf16_f32 v153, v230, v231\n"
"v_mfma_f32_16x16x32_bf16 v[228:231], a[76:79], v[32:35], 0\n"
"v_cvt_pk_bf16_f32 v154, v204, v205\n"
"v_cvt_pk_bf16_f32 v155, v206, v207\n"
"v_mfma_f32_16x16x32_bf16 v[204:207], a[80:83], v[32:35], 0\n"
"ds_read_b128 v[124:127], v178 offset:13504\n"
"ds_read_b128 v[132:135], v179 offset:13504\n"
"v_mfma_f32_16x16x32_bf16 v[224:227], a[84:87], v[36:39], v[224:227]\n"
"ds_read_b64 v[80:81], v20 offset:272\n"
"ds_read_b64 v[86:87], v20 offset:336\n"
"v_mfma_f32_16x16x32_bf16 v[228:231], a[88:91], v[36:39], v[228:231]\n"
"ds_read_b128 v[128:131], v178 offset:27008\n"
"v_cvt_pk_bf16_f32 v166, v208, v209\n"
"v_mfma_f32_16x16x32_bf16 v[204:207], a[92:95], v[36:39], v[204:207]\n"
"ds_read_b128 v[140:143], v179 offset:27008\n"
"v_cvt_pk_bf16_f32 v167, v210, v211\n"
"v_mfma_f32_16x16x32_bf16 v[224:227], a[96:99], v[40:43], v[224:227]\n"
"ds_read_b64 v[88:89], v20 offset:544\n"
"ds_read_b64 v[94:95], v20 offset:608\n"
"v_mfma_f32_16x16x32_bf16 v[228:231], a[100:103], v[40:43], v[228:231]\n"
"ds_read_b128 v[136:139], v180\n"
"v_subrev_u32 v183, s48, v183\n"
"v_mfma_f32_16x16x32_bf16 v[204:207], a[104:107], v[40:43], v[204:207]\n"
"ds_read_b128 v[112:115], v181\n"
"v_subrev_u32 v184, s48, v184\n"
"v_mfma_f32_16x16x32_bf16 v[224:227], a[108:111], v[44:47], v[224:227]\n"
"ds_read_b64 v[96:97], v20 offset:816\n"
"ds_read_b64 v[102:103], v20 offset:880\n"
"v_mfma_f32_16x16x32_bf16 v[228:231], a[112:115], v[44:47], v[228:231]\n"
"s_waitcnt lgkmcnt(11)\n"
"ds_read_b64 v[104:105], v20 offset:1088\n"
"ds_read_b64 v[110:111], v20 offset:1152\n"
"v_mfma_f32_16x16x32_bf16 v[204:207], a[116:119], v[44:47], v[204:207]\n"
"s_waitcnt lgkmcnt(11)\n"
"ds_read_b64 v[82:83], v178 offset:13568\n"
"v_subrev_u32 v185, s48, v185\n"
"v_mfma_f32_16x16x32_bf16 v[224:227], a[120:123], v[48:51], v[224:227]\n"
"s_waitcnt lgkmcnt(11)\n"
"ds_read_b64 v[84:85], v179 offset:13568\n"
"v_bfi_b32 v80, v4, v80, v26\n"
"v_mfma_f32_16x16x32_bf16 v[228:231], a[124:127], v[48:51], v[228:231]\n"
"s_waitcnt lgkmcnt(11)\n"
"ds_read_b64 v[90:91], v178 offset:27072\n"
"v_bfi_b32 v81, v5, v81, v26\n"
"v_mfma_f32_16x16x32_bf16 v[204:207], a[128:131], v[48:51], v[204:207]\n"
"s_waitcnt lgkmcnt(10)\n"
"ds_read_b64 v[92:93], v179 offset:27072\n"
"v_bfi_b32 v86, v6, v86, v26\n"
"v_mfma_f32_16x16x32_bf16 v[224:227], a[132:135], v[52:55], v[224:227]\n"
"s_waitcnt lgkmcnt(10)\n"
"ds_read_b64 v[98:99], v180 offset:64\n"
"v_bfi_b32 v87, v7, v87, v26\n"
"v_mfma_f32_16x16x32_bf16 v[228:231], a[136:139], v[52:55], v[228:231]\n"
"s_waitcnt lgkmcnt(10)\n"
"ds_read_b64 v[100:101], v181 offset:64\n"
"v_bfi_b32 v88, v0, v88, v26\n"
"v_mfma_f32_16x16x32_bf16 v[204:207], a[140:143], v[52:55], v[204:207]\n"
"s_waitcnt lgkmcnt(9)\n"
"v_cvt_pk_bf16_f32 v224, v224, v225\n"
"v_cvt_pk_bf16_f32 v225, v226, v227\n"
"v_mfma_f32_16x16x32_bf16 v[208:211], a[144:147], v[32:35], 0\n"
"s_waitcnt lgkmcnt(7)\n"
"ds_read_b128 v[32:35], v188 offset:6144\n"
"ds_read_b64_tr_b16 v[248:249], v182 offset:13632\n"
"v_mfma_f32_16x16x32_bf16 v[208:211], a[156:159], v[36:39], v[208:211]\n"
"s_waitcnt lgkmcnt(8)\n"
"ds_read_b64_tr_b16 v[250:251], v183 offset:13632\n"
"v_cvt_pk_bf16_f32 v226, v228, v229\n"
"v_mfma_f32_16x16x32_bf16 v[212:215], a[164:167], v[36:39], v[212:215]\n"
"ds_read_b128 v[36:39], v189 offset:6144\n"
"v_cvt_pk_bf16_f32 v227, v230, v231\n"
"v_mfma_f32_16x16x32_bf16 v[208:211], a[168:171], v[40:43], v[208:211]\n"
"ds_write_b128 v186, v[224:227] offset:60896\n"
"v_mfma_f32_16x16x32_bf16 v[216:219], a[172:175], v[40:43], v[216:219]\n"
"v_cvt_pk_bf16_f32 v222, v204, v205\n"
"v_cvt_pk_bf16_f32 v223, v206, v207\n"
"v_mfma_f32_16x16x32_bf16 v[212:215], a[176:179], v[40:43], v[212:215]\n"
"v_bfi_b32 v89, v1, v89, v26\n"
"v_bfi_b32 v94, v2, v94, v26\n"
"v_mfma_f32_16x16x32_bf16 v[124:127], v[124:127], v[148:151], 0\n"
"s_waitcnt lgkmcnt(10)\n"
"ds_read_b128 v[40:43], v188 offset:7168\n"
"ds_write_b128 v186, v[220:223] offset:60832\n"
"v_mfma_f32_16x16x32_bf16 v[132:135], v[132:135], v[148:151], 0\n"
"s_waitcnt lgkmcnt(6)\n"
"v_bfi_b32 v95, v3, v95, v26\n"
"v_bfi_b32 v96, v0, v96, v26\n"
"v_mfma_f32_16x16x32_bf16 v[128:131], v[128:131], v[148:151], 0\n"
"v_bfi_b32 v97, v1, v97, v26\n"
"v_bfi_b32 v102, v2, v102, v26\n"
"v_mfma_f32_16x16x32_bf16 v[140:143], v[140:143], v[148:151], 0\n"
"s_waitcnt lgkmcnt(3)\n"
"ds_read_b64_tr_b16 v[244:245], v182 offset:13640\n"
"ds_read_b64_tr_b16 v[246:247], v183 offset:13640\n"
"v_mfma_f32_16x16x32_bf16 v[136:139], v[136:139], v[148:151], 0\n"
"s_add_u32 m0, s34, 6144\n"
"ds_read_b64_tr_b16 v[252:253], v182 offset:13576\n"
"buffer_load_dwordx4 v190, s[8:11], s32 offen lds\n"
"v_mfma_f32_16x16x32_bf16 v[112:115], v[112:115], v[148:151], 0\n"
"ds_read_b64_tr_b16 v[254:255], v183 offset:13576\n"
"v_bfi_b32 v103, v3, v103, v26\n"
"v_mfma_f32_16x16x32_bf16 v[208:211], a[180:183], v[44:47], v[208:211]\n"
"v_bfi_b32 v104, v8, v104, v26\n"
"v_bfi_b32 v105, v9, v105, v26\n"
"v_mfma_f32_16x16x32_bf16 v[124:127], v[80:83], v[160:163], v[124:127]\n"
"v_bfi_b32 v110, v10, v110, v26\n"
"v_bfi_b32 v111, v11, v111, v26\n"
"v_mfma_f32_16x16x32_bf16 v[132:135], v[84:87], v[162:165], v[132:135]\n"
"ds_read_b64 v[80:81], v20 offset:272\n"
"ds_read_b64 v[86:87], v20 offset:336\n"
"v_mfma_f32_16x16x32_bf16 v[128:131], v[88:91], v[160:163], v[128:131]\n"
"s_waitcnt lgkmcnt(6)\n"
"s_barrier\n"
"v_mfma_f32_16x16x32_bf16 v[140:143], v[92:95], v[162:165], v[140:143]\n"
"ds_read_b128 v[120:123], v180 offset:13504\n"
"ds_read_b128 v[116:119], v181 offset:13504\n"
"v_mfma_f32_16x16x32_bf16 v[136:139], v[96:99], v[160:163], v[136:139]\n"
"ds_read_b64 v[106:107], v180 offset:13568\n"
"v_max_f32 v124, v124, v132\n"
"v_mfma_f32_16x16x32_bf16 v[112:115], v[100:103], v[162:165], v[112:115]\n"
"ds_read_b64 v[108:109], v181 offset:13568\n"
"v_max_f32 v125, v125, v133\n"
"v_mfma_f32_16x16x32_bf16 v[216:219], a[184:187], v[44:47], v[216:219]\n"
"buffer_store_dwordx4 v[68:71], v192, s[28:31], s4 offen sc1\n"
"v_mfma_f32_16x16x32_bf16 v[212:215], a[188:191], v[44:47], v[212:215]\n"
"s_waitcnt lgkmcnt(5)\n"
"ds_read_b128 v[44:47], v189 offset:7168\n"
"ds_read_b64_tr_b16 v[132:133], v182 offset:27136\n"
"v_mfma_f32_16x16x32_bf16 v[208:211], a[192:195], v[48:51], v[208:211]\n"
"s_waitcnt lgkmcnt(6)\n"
"ds_read_b64_tr_b16 v[98:99], v184 offset:136\n"
"v_max_f32 v126, v126, v134\n"
"v_mfma_f32_16x16x32_bf16 v[216:219], a[196:199], v[48:51], v[216:219]\n"
"s_add_u32 m0, s79, 130176\n"
"ds_read_b64_tr_b16 v[100:101], v185 offset:136\n"
"buffer_load_dwordx4 v192, s[16:19], s43 offen lds\n"
"v_mfma_f32_16x16x32_bf16 v[212:215], a[200:203], v[48:51], v[212:215]\n"
"s_waitcnt lgkmcnt(7)\n"
"ds_read_b128 v[48:51], v188 offset:8192\n"
"v_max_f32 v127, v127, v135\n"
"v_mfma_f32_16x16x32_bf16 v[120:123], v[120:123], v[148:151], 0\n"
"s_waitcnt lgkmcnt(7)\n"
"ds_read_b64_tr_b16 v[134:135], v183 offset:27136\n"
"v_max_f32 v170, v124, v125\n"
"v_mfma_f32_16x16x32_bf16 v[116:119], v[116:119], v[148:151], 0\n"
"s_waitcnt lgkmcnt(7)\n"
"v_max_f32 v171, v126, v127\n"
"v_max_f32 v128, v128, v140\n"
"v_mfma_f32_16x16x32_bf16 v[120:123], v[104:107], v[160:163], v[120:123]\n"
"s_waitcnt lgkmcnt(6)\n"
"ds_read_b64_tr_b16 v[106:107], v184 offset:72\n"
"v_max_f32 v129, v129, v141\n"
"v_mfma_f32_16x16x32_bf16 v[116:119], v[108:111], v[162:165], v[116:119]\n"
"ds_read_b64_tr_b16 v[140:141], v182 offset:27144\n"
"ds_read_b64_tr_b16 v[108:109], v185 offset:72\n"
"v_mfma_f32_16x16x32_bf16 v[208:211], a[204:207], v[52:55], v[208:211]\n"
"v_max_f32 v130, v130, v142\n"
"v_max_f32 v131, v131, v143\n"
"v_mfma_f32_16x16x32_bf16 v[216:219], a[208:211], v[52:55], v[216:219]\n"
"ds_read_b64_tr_b16 v[142:143], v183 offset:27144\n"
"v_max3_f32 v170, v170, v128, v129\n"
"v_mfma_f32_16x16x32_bf16 v[212:215], a[212:215], v[52:55], v[212:215]\n"
"ds_read_b128 v[52:55], v189 offset:8192\n"
"v_max3_f32 v171, v171, v130, v131\n"
"v_mfma_f32_16x16x32_bf16 v[228:231], a[0:3], v[32:35], 0\n"
"v_max_f32 v136, v136, v112\n"
"v_max_f32 v137, v137, v113\n"
"v_mfma_f32_16x16x32_bf16 v[204:207], a[4:7], v[32:35], 0\n"
"s_waitcnt lgkmcnt(10)\n"
"ds_read_b64_tr_b16 v[112:113], v182 offset:27080\n"
"v_max_f32 v138, v138, v114\n"
"v_mfma_f32_16x16x32_bf16 v[220:223], a[8:11], v[32:35], 0\n"
"s_add_u32 m0, s79, 130176\n"
"v_max_f32 v139, v139, v115\n"
"v_max3_f32 v170, v170, v136, v137\n"
"v_mfma_f32_16x16x32_bf16 v[228:231], a[12:15], v[36:39], v[228:231]\n"
"buffer_load_dwordx4 v192, s[16:19], s43 offen offset:1024 lds\n"
"ds_read_b64_tr_b16 v[114:115], v183 offset:27080\n"
"v_mfma_f32_16x16x32_bf16 v[204:207], a[16:19], v[36:39], v[204:207]\n"
"v_max3_f32 v171, v171, v138, v139\n"
"v_max_f32 v120, v120, v116\n"
"v_mfma_f32_16x16x32_bf16 v[220:223], a[20:23], v[36:39], v[220:223]\n"
"v_max_f32 v121, v121, v117\n"
"v_max_f32 v122, v122, v118\n"
"v_mfma_f32_16x16x32_bf16 v[228:231], a[24:27], v[40:43], v[228:231]\n"
"ds_read_b64_tr_b16 v[116:117], v184 offset:128\n"
"v_max_f32 v123, v123, v119\n"
"v_mfma_f32_16x16x32_bf16 v[204:207], a[28:31], v[40:43], v[204:207]\n"
"ds_read_b64_tr_b16 v[118:119], v185 offset:128\n"
"v_max3_f32 v170, v170, v120, v121\n"
"v_mfma_f32_16x16x32_bf16 v[220:223], a[32:35], v[40:43], v[220:223]\n"
"v_max3_f32 v171, v171, v122, v123\n"
"v_max_f32 v170, v170, v171\n"
"v_mfma_f32_16x16x32_bf16 v[228:231], a[36:39], v[44:47], v[228:231]\n"
"s_waitcnt lgkmcnt(10)\n"
"v_mov_b32 v171, v170\n"
"v_cvt_pk_bf16_f32 v144, v208, v209\n"
"v_mfma_f32_16x16x32_bf16 v[204:207], a[40:43], v[44:47], v[204:207]\n"
"v_permlane16_swap_b32 v170, v171\n"
"v_mfma_f32_16x16x32_bf16 v[220:223], a[44:47], v[44:47], v[220:223]\n"
"v_max_f32 v170, v170, v171\n"
"v_mov_b32 v171, v170\n"
"v_mfma_f32_16x16x32_bf16 v[228:231], a[48:51], v[48:51], v[228:231]\n"
"s_waitcnt lgkmcnt(4)\n"
"v_permlane32_swap_b32 v170, v171\n"
"v_mfma_f32_16x16x32_bf16 v[204:207], a[52:55], v[48:51], v[204:207]\n"
"s_add_u32 m0, s79, 130176\n"
"v_max_f32 v170, v170, v171\n"
"v_mul_f32 v170, 0xbe553b95, v170\n"
"v_mfma_f32_16x16x32_bf16 v[220:223], a[56:59], v[48:51], v[220:223]\n"
"buffer_load_dwordx4 v192, s[16:19], s43 offen offset:2048 lds\n"
"v_fmamk_f32 v124, v124, 0x3e553b95, v170\n"
"v_mfma_f32_16x16x32_bf16 v[228:231], a[60:63], v[52:55], v[228:231]\n"
"s_add_u32 s43, s79, 18432\n"
"v_fmamk_f32 v125, v125, 0x3e553b95, v170\n"
"v_fmamk_f32 v126, v126, 0x3e553b95, v170\n"
"v_mfma_f32_16x16x32_bf16 v[204:207], a[64:67], v[52:55], v[204:207]\n"
"v_fmamk_f32 v127, v127, 0x3e553b95, v170\n"
"v_fmamk_f32 v128, v128, 0x3e553b95, v170\n"
"v_mfma_f32_16x16x32_bf16 v[220:223], a[68:71], v[52:55], v[220:223]\n"
"v_exp_f32 v124, v124\n"
"v_mfma_f32_16x16x32_bf16 v[224:227], a[72:75], v[32:35], 0\n"
"v_exp_f32 v125, v125\n"
"v_mfma_f32_16x16x32_bf16 v[224:227], a[84:87], v[36:39], v[224:227]\n"
"v_exp_f32 v126, v126\n"
"v_mfma_f32_16x16x32_bf16 v[224:227], a[96:99], v[40:43], v[224:227]\n"
"v_exp_f32 v127, v127\n"
"v_mfma_f32_16x16x32_bf16 v[224:227], a[108:111], v[44:47], v[224:227]\n"
"v_cvt_pk_bf16_f32 v82, v124, v125\n"
"v_cvt_pk_bf16_f32 v83, v126, v127\n"
"v_mfma_f32_16x16x32_bf16 v[224:227], a[120:123], v[48:51], v[224:227]\n"
"v_fmamk_f32 v129, v129, 0x3e553b95, v170\n"
"v_fmamk_f32 v130, v130, 0x3e553b95, v170\n"
"v_mfma_f32_16x16x32_bf16 v[224:227], a[132:135], v[52:55], v[224:227]\n"
"v_fmamk_f32 v131, v131, 0x3e553b95, v170\n"
"v_and_b32 v124, v82, v176\n"
"s_add_u32 m0, s34, 9216\n"
"v_and_b32 v125, v83, v177\n"
"buffer_load_dwordx4 v191, s[8:11], s32 offen lds\n"
"s_add_u32 s32, s32, s33\n"
"v_exp_f32 v128, v128\n"
"v_exp_f32 v129, v129\n"
"v_exp_f32 v130, v130\n"
"v_exp_f32 v131, v131\n"
"v_xor_b32 v126, v82, v124\n"
"v_xor_b32 v127, v83, v125\n"
"v_cvt_pk_bf16_f32 v84, v128, v129\n"
"v_cvt_pk_bf16_f32 v85, v130, v131\n"
"v_mfma_f32_16x16x32_bf16 v[248:251], v[248:251], v[124:127], 0\n"
"v_and_b32 v128, v84, v176\n"
"v_and_b32 v129, v85, v177\n"
"v_mfma_f32_16x16x32_bf16 v[244:247], v[244:247], v[124:127], 0\n"
"v_xor_b32 v130, v84, v128\n"
"v_xor_b32 v131, v85, v129\n"
"v_mfma_f32_16x16x32_bf16 v[252:255], v[252:255], v[124:127], 0\n"
"s_waitcnt lgkmcnt(2)\n"
"ds_read_b64_tr_b16 v[124:125], v184 offset:13632\n"
"ds_read_b64_tr_b16 v[126:127], v185 offset:13632\n"
"v_mfma_f32_16x16x32_bf16 v[248:251], v[132:135], v[128:131], v[248:251]\n"
"ds_read_b64_tr_b16 v[132:133], v184 offset:13576\n"
"v_cvt_pk_bf16_f32 v145, v210, v211\n"
"v_mfma_f32_16x16x32_bf16 v[244:247], v[140:143], v[128:131], v[244:247]\n"
"ds_read_b64_tr_b16 v[134:135], v185 offset:13576\n"
"v_cvt_pk_bf16_f32 v146, v216, v217\n"
"v_mfma_f32_16x16x32_bf16 v[252:255], v[112:115], v[128:131], v[252:255]\n"
"ds_read_b64_tr_b16 v[128:129], v184 offset:13640\n"
"v_cvt_pk_bf16_f32 v147, v218, v219\n"
"v_mfma_f32_16x16x32_bf16 v[82:85], v[172:175], v[82:85], 0\n"
"s_add_u32 m0, s34, 7168\n"
"ds_read_b64_tr_b16 v[130:131], v185 offset:13640\n"
"buffer_load_dwordx4 v190, s[8:11], s0 offen lds\n"
"v_mfma_f32_16x16x32_bf16 v[208:211], a[76:79], v[32:35], 0\n"
"v_fmamk_f32 v136, v136, 0x3e553b95, v170\n"
"v_fmamk_f32 v137, v137, 0x3e553b95, v170\n"
"v_mfma_f32_16x16x32_bf16 v[216:219], a[80:83], v[32:35], 0\n"
"v_fmamk_f32 v138, v138, 0x3e553b95, v170\n"
"v_fmamk_f32 v139, v139, 0x3e553b95, v170\n"
"v_mfma_f32_16x16x32_bf16 v[208:211], a[88:91], v[36:39], v[208:211]\n"
"v_cvt_pk_bf16_f32 v158, v212, v213\n"
"v_fmamk_f32 v120, v120, 0x3e553b95, v170\n"
"v_mfma_f32_16x16x32_bf16 v[216:219], a[92:95], v[36:39], v[216:219]\n"
"v_exp_f32 v136, v136\n"
"v_mfma_f32_16x16x32_bf16 v[208:211], a[100:103], v[40:43], v[208:211]\n"
"v_exp_f32 v137, v137\n"
"v_mfma_f32_16x16x32_bf16 v[216:219], a[104:107], v[40:43], v[216:219]\n"
"v_exp_f32 v138, v138\n"
"v_mfma_f32_16x16x32_bf16 v[208:211], a[112:115], v[44:47], v[208:211]\n"
"v_exp_f32 v139, v139\n"
"v_mfma_f32_16x16x32_bf16 v[216:219], a[116:119], v[44:47], v[216:219]\n"
"v_fmamk_f32 v121, v121, 0x3e553b95, v170\n"
"v_fmamk_f32 v122, v122, 0x3e553b95, v170\n"
"v_mfma_f32_16x16x32_bf16 v[208:211], a[124:127], v[48:51], v[208:211]\n"
"v_fmamk_f32 v123, v123, 0x3e553b95, v170\n"
"v_cvt_pk_bf16_f32 v159, v214, v215\n"
"v_mfma_f32_16x16x32_bf16 v[216:219], a[128:131], v[48:51], v[216:219]\n"
"s_add_u32 m0, s34, 10240\n"
"v_cvt_pk_bf16_f32 v90, v136, v137\n"
"v_cvt_pk_bf16_f32 v91, v138, v139\n"
"v_mfma_f32_16x16x32_bf16 v[208:211], a[136:139], v[52:55], v[208:211]\n"
"buffer_load_dwordx4 v191, s[8:11], s0 offen lds\n"
"v_and_b32 v136, v90, v176\n"
"v_mfma_f32_16x16x32_bf16 v[216:219], a[140:143], v[52:55], v[216:219]\n"
"s_add_u32 s0, s0, s33\n"
"v_exp_f32 v120, v120\n"
"v_mfma_f32_16x16x32_bf16 v[212:215], a[144:147], v[32:35], 0\n"
"v_exp_f32 v121, v121\n"
"v_mfma_f32_16x16x32_bf16 v[212:215], a[156:159], v[36:39], v[212:215]\n"
"v_exp_f32 v122, v122\n"
"v_mfma_f32_16x16x32_bf16 v[212:215], a[168:171], v[40:43], v[212:215]\n"
"v_exp_f32 v123, v123\n"
"v_mfma_f32_16x16x32_bf16 v[212:215], a[180:183], v[44:47], v[212:215]\n"
"v_and_b32 v137, v91, v177\n"
"v_xor_b32 v138, v90, v136\n"
"v_mfma_f32_16x16x32_bf16 v[212:215], a[192:195], v[48:51], v[212:215]\n"
"v_xor_b32 v139, v91, v137\n"
"v_cvt_pk_bf16_f32 v92, v120, v121\n"
"v_mfma_f32_16x16x32_bf16 v[212:215], a[204:207], v[52:55], v[212:215]\n"
"s_waitcnt lgkmcnt(6)\n"
"v_cvt_pk_bf16_f32 v93, v122, v123\n"
"v_and_b32 v120, v92, v176\n"
"v_mfma_f32_16x16x32_bf16 v[248:251], v[116:119], v[136:139], v[248:251]\n"
"v_and_b32 v121, v93, v177\n"
"v_xor_b32 v122, v92, v120\n"
"v_mfma_f32_16x16x32_bf16 v[244:247], v[98:101], v[136:139], v[244:247]\n"
"v_xor_b32 v123, v93, v121\n"
"v_cvt_pk_bf16_f32 v228, v228, v229\n"
"v_mfma_f32_16x16x32_bf16 v[90:93], v[172:175], v[90:93], v[82:85]\n"
"s_waitcnt lgkmcnt(0)\n"
"v_cvt_pk_bf16_f32 v229, v230, v231\n"
"v_cvt_pk_bf16_f32 v230, v204, v205\n"
"v_mfma_f32_16x16x32_bf16 v[252:255], v[106:109], v[136:139], v[252:255]\n"
"s_add_u32 m0, s34, 8192\n"
"v_cvt_pk_bf16_f32 v231, v206, v207\n"
"v_cvt_pk_bf16_f32 v220, v220, v221\n"
"v_mfma_f32_16x16x32_bf16 v[248:251], v[124:127], v[120:123], v[248:251]\n"
".Lic1%=:\n"
"s_cbranch_execz .Lib0%=\n"
"buffer_load_dwordx4 v190, s[8:11], s1 offen lds\n"
"ds_write_b128 v187, v[228:231]\n"
"v_mfma_f32_16x16x32_bf16 v[244:247], v[128:131], v[120:123], v[244:247]\n"
"v_rcp_f32 v171, v90\n"
"v_mfma_f32_16x16x32_bf16 v[252:255], v[132:135], v[120:123], v[252:255]\n"
"v_cvt_pk_bf16_f32 v221, v222, v223\n"
"v_cvt_pk_bf16_f32 v224, v224, v225\n"
"v_mfma_f32_16x16x32_bf16 v[204:207], a[148:151], v[32:35], 0\n"
"v_mul_f32 v248, v171, v248\n"
"v_mul_f32 v249, v171, v249\n"
"v_mfma_f32_16x16x32_bf16 v[228:231], a[152:155], v[32:35], 0\n"
"s_waitcnt vmcnt(16)\n"
"ds_read_b128 v[32:35], v188\n"
"v_mul_f32 v250, v171, v250\n"
"v_mfma_f32_16x16x32_bf16 v[204:207], a[160:163], v[36:39], v[204:207]\n"
"v_mul_f32 v251, v171, v251\n"
"v_cvt_pk_bf16_f32 v248, v248, v249\n"
"v_mfma_f32_16x16x32_bf16 v[228:231], a[164:167], v[36:39], v[228:231]\n"
"ds_read_b128 v[36:39], v189\n"
"v_cvt_pk_bf16_f32 v249, v250, v251\n"
"v_mfma_f32_16x16x32_bf16 v[204:207], a[172:175], v[40:43], v[204:207]\n"
"v_mul_f32 v244, v171, v244\n"
"v_mul_f32 v245, v171, v245\n"
"v_mfma_f32_16x16x32_bf16 v[228:231], a[176:179], v[40:43], v[228:231]\n"
"s_waitcnt vmcnt(14)\n"
"ds_read_b128 v[40:43], v188 offset:1024\n"
"v_mul_f32 v246, v171, v246\n"
"v_mfma_f32_16x16x32_bf16 v[204:207], a[184:187], v[44:47], v[204:207]\n"
"s_add_u32 m0, s34, 11264\n"
"v_mul_f32 v247, v171, v247\n"
"v_cvt_pk_bf16_f32 v250, v244, v245\n"
"v_mfma_f32_16x16x32_bf16 v[228:231], a[188:191], v[44:47], v[228:231]\n"
"buffer_load_dwordx4 v191, s[8:11], s1 offen lds\n"
"ds_read_b128 v[44:47], v189 offset:1024\n"
"v_mfma_f32_16x16x32_bf16 v[204:207], a[196:199], v[48:51], v[204:207]\n"
"s_add_u32 s1, s1, s33\n"
"v_cvt_pk_bf16_f32 v251, v246, v247\n"
"v_mul_f32 v252, v171, v252\n"
"v_mfma_f32_16x16x32_bf16 v[228:231], a[200:203], v[48:51], v[228:231]\n"
"s_waitcnt vmcnt(13)\n"
"ds_read_b128 v[48:51], v188 offset:2048\n"
"v_mul_f32 v253, v171, v253\n"
"v_mfma_f32_16x16x32_bf16 v[204:207], a[208:211], v[52:55], v[204:207]\n"
"buffer_store_dwordx4 v[248:251], v192, s[28:31], s37 offen sc1\n"
"v_mfma_f32_16x16x32_bf16 v[228:231], a[212:215], v[52:55], v[228:231]\n"
"ds_read_b128 v[52:55], v189 offset:2048\n"
"v_mul_f32 v254, v171, v254\n"
"v_mul_f32 v255, v171, v255\n"
"v_cvt_pk_bf16_f32 v252, v252, v253\n"
"v_cvt_pk_bf16_f32 v253, v254, v255\n"
"v_cvt_pk_bf16_f32 v225, v226, v227\n"
"v_cvt_pk_bf16_f32 v226, v208, v209\n"
"v_cvt_pk_bf16_f32 v227, v210, v211\n"
"v_cvt_pk_bf16_f32 v222, v216, v217\n"
"v_cvt_pk_bf16_f32 v223, v218, v219\n"
"v_cvt_pk_bf16_f32 v148, v212, v213\n"
"v_cvt_pk_bf16_f32 v149, v214, v215\n"
"v_cvt_pk_bf16_f32 v150, v204, v205\n"
"v_cvt_pk_bf16_f32 v151, v206, v207\n"
"v_cvt_pk_bf16_f32 v162, v228, v229\n"
"v_cvt_pk_bf16_f32 v163, v230, v231\n"
"v_bfi_b32 v80, v12, v80, v26\n"
"v_bfi_b32 v81, v13, v81, v26\n"
"v_bfi_b32 v86, v14, v86, v26\n"
"v_bfi_b32 v87, v15, v87, v26\n"
"s_waitcnt lgkmcnt(2)\n"
"v_mfma_f32_16x16x32_bf16 v[208:211], a[0:3], v[32:35], 0\n"
"s_add_u32 m0, s34, 0\n"
"buffer_store_dwordx2 v[252:253], v193, s[28:31], s37 offen offset:1024 sc1\n"
"v_mfma_f32_16x16x32_bf16 v[216:219], a[4:7], v[32:35], 0\n"
"s_add_u32 s37, s37, 6144\n"
"ds_write_b128 v187, v[220:223] offset:64\n"
"v_mfma_f32_16x16x32_bf16 v[220:223], a[8:11], v[32:35], 0\n"
"s_add_u32 s4, s37, 55296\n"
"buffer_load_dwordx4 v190, s[8:11], s32 offen lds\n"
"v_mfma_f32_16x16x32_bf16 v[208:211], a[12:15], v[36:39], v[208:211]\n"
"ds_write_b128 v187, v[224:227] offset:128\n"
"v_mfma_f32_16x16x32_bf16 v[216:219], a[16:19], v[36:39], v[216:219]\n"
"v_mfma_f32_16x16x32_bf16 v[220:223], a[20:23], v[36:39], v[220:223]\n"
"ds_read_b128 v[140:143], v178 offset:27008\n"
"v_mfma_f32_16x16x32_bf16 v[208:211], a[24:27], v[40:43], v[208:211]\n"
"ds_read_b128 v[112:115], v179 offset:27008\n"
"ds_read_b128 v[136:139], v180\n"
"v_mfma_f32_16x16x32_bf16 v[216:219], a[28:31], v[40:43], v[216:219]\n"
"ds_read_b128 v[116:119], v181\n"
"v_mfma_f32_16x16x32_bf16 v[220:223], a[32:35], v[40:43], v[220:223]\n"
"ds_read_b128 v[120:123], v180 offset:13504\n"
"v_mfma_f32_16x16x32_bf16 v[208:211], a[36:39], v[44:47], v[208:211]\n"
"s_waitcnt lgkmcnt(8)\n"
"ds_read_b128 v[124:127], v181 offset:13504\n"
"v_mfma_f32_16x16x32_bf16 v[216:219], a[40:43], v[44:47], v[216:219]\n"
"ds_read_b64 v[104:105], v20 offset:1088\n"
"ds_read_b64 v[110:111], v20 offset:1152\n"
"v_mfma_f32_16x16x32_bf16 v[220:223], a[44:47], v[44:47], v[220:223]\n"
"s_waitcnt vmcnt(8) lgkmcnt(8)\n"
"ds_read_b64 v[82:83], v178 offset:27072\n"
"s_add_u32 m0, s34, 3072\n"
"v_mfma_f32_16x16x32_bf16 v[208:211], a[48:51], v[48:51], v[208:211]\n"
"s_barrier\n"
"v_mfma_f32_16x16x32_bf16 v[216:219], a[52:55], v[48:51], v[216:219]\n"
"s_waitcnt lgkmcnt(8)\n"
"buffer_load_dwordx4 v191, s[8:11], s32 offen lds\n"
"ds_read_b128 v[128:131], v180 offset:27008\n"
"v_mfma_f32_16x16x32_bf16 v[220:223], a[56:59], v[48:51], v[220:223]\n"
"s_waitcnt lgkmcnt(8)\n"
"ds_read_b128 v[132:135], v181 offset:27008\n"
"ds_read_b64 v[84:85], v179 offset:27072\n"
"v_mfma_f32_16x16x32_bf16 v[140:143], v[140:143], v[152:155], 0\n"
"s_waitcnt lgkmcnt(9)\n"
"ds_read_b64 v[90:91], v180 offset:64\n"
"s_mov_b64 exec, s[44:45]\n"
"v_add_u32 v179, 0xc0, v179\n"
"s_mov_b64 exec, -1\n"
"v_mfma_f32_16x16x32_bf16 v[112:115], v[112:115], v[152:155], 0\n"
"s_waitcnt lgkmcnt(8)\n"
"buffer_load_dword v201, off, s[28:31], s41 sc1\n"
"ds_read_b64 v[92:93], v181 offset:64\n"
"v_mfma_f32_16x16x32_bf16 v[208:211], a[60:63], v[52:55], v[208:211]\n"
"s_waitcnt lgkmcnt(8)\n"
"ds_read_b64 v[98:99], v180 offset:13568\n"
"ds_read_b64 v[100:101], v181 offset:13568\n"
"v_mfma_f32_16x16x32_bf16 v[136:139], v[136:139], v[152:155], 0\n"
"s_waitcnt lgkmcnt(8)\n"
"ds_read_b64 v[106:107], v180 offset:27072\n"
"v_mfma_f32_16x16x32_bf16 v[116:119], v[116:119], v[152:155], 0\n"
"s_waitcnt lgkmcnt(8)\n"
"v_bfi_b32 v104, v16, v104, v26\n"
"v_bfi_b32 v105, v17, v105, v26\n"
"v_mfma_f32_16x16x32_bf16 v[120:123], v[120:123], v[152:155], 0\n"
"s_waitcnt lgkmcnt(7)\n"
"ds_read_b64 v[108:109], v181 offset:27072\n"
"ds_read_b64_tr_b16 v[244:245], v182 offset:27136\n"
"v_mfma_f32_16x16x32_bf16 v[124:127], v[124:127], v[152:155], 0\n"
"s_waitcnt lgkmcnt(8)\n"
"v_bfi_b32 v110, v18, v110, v26\n"
"v_bfi_b32 v111, v19, v111, v26\n"
"v_mfma_f32_16x16x32_bf16 v[128:131], v[128:131], v[152:155], 0\n"
"s_waitcnt lgkmcnt(7)\n"
"ds_read_b64_tr_b16 v[246:247], v183 offset:27136\n"
"ds_read_b64_tr_b16 v[248:249], v182 offset:27144\n"
"v_mfma_f32_16x16x32_bf16 v[132:135], v[132:135], v[152:155], 0\n"
"s_waitcnt lgkmcnt(8)\n"
"v_cvt_pk_bf16_f32 v208, v208, v209\n"
"v_cvt_pk_bf16_f32 v209, v210, v211\n"
"v_mfma_f32_16x16x32_bf16 v[140:143], v[80:83], v[164:167], v[140:143]\n"
"s_waitcnt lgkmcnt(7)\n"
"ds_read_b64_tr_b16 v[250:251], v183 offset:27144\n"
"ds_read_b64_tr_b16 v[252:253], v182 offset:27080\n"
"v_mfma_f32_16x16x32_bf16 v[112:115], v[84:87], v[166:169], v[112:115]\n"
"s_waitcnt lgkmcnt(8)\n"
"s_add_u32 s41, s40, 131072\n"
"ds_read_b64_tr_b16 v[254:255], v183 offset:27080\n"
"v_mfma_f32_16x16x32_bf16 v[136:139], v[88:91], v[164:167], v[136:139]\n"
"s_waitcnt lgkmcnt(8)\n"
"s_mov_b64 exec, s[46:47]\n"
"v_add_u32 v183, 0xc0, v183\n"
"s_mov_b64 exec, -1\n"
"v_mfma_f32_16x16x32_bf16 v[116:119], v[92:95], v[166:169], v[116:119]\n"
"s_waitcnt lgkmcnt(7)\n"
"ds_read_b64_tr_b16 a[216:217], v236\n"
"v_max_f32 v140, v140, v112\n"
"v_mfma_f32_16x16x32_bf16 v[216:219], a[64:67], v[52:55], v[216:219]\n"
"s_waitcnt lgkmcnt(7)\n"
"v_max_f32 v141, v141, v113\n"
"v_max_f32 v142, v142, v114\n"
"v_mfma_f32_16x16x32_bf16 v[120:123], v[96:99], v[164:167], v[120:123]\n"
"s_add_u32 m0, s79, 142464\n"
"ds_read_b64_tr_b16 v[112:113], v184 offset:128\n"
"buffer_load_dwordx4 v192, s[16:19], s43 offen lds\n"
"v_mfma_f32_16x16x32_bf16 v[124:127], v[100:103], v[166:169], v[124:127]\n"
"s_add_u32 s32, s32, s33\n"
"ds_read_b64_tr_b16 v[98:99], v184 offset:13640\n"
"ds_read_b64_tr_b16 v[100:101], v185 offset:13640\n"
"v_mfma_f32_16x16x32_bf16 v[128:131], v[104:107], v[164:167], v[128:131]\n"
"ds_read_b64_tr_b16 v[106:107], v184 offset:13576\n"
"v_max_f32 v143, v143, v115\n"
"v_mfma_f32_16x16x32_bf16 v[132:135], v[108:111], v[166:169], v[132:135]\n"
"ds_read_b64_tr_b16 v[114:115], v185 offset:128\n"
"v_max_f32 v170, v140, v141\n"
"v_mfma_f32_16x16x32_bf16 v[220:223], a[68:71], v[52:55], v[220:223]\n"
"ds_read_b64_tr_b16 v[108:109], v185 offset:13576\n"
"v_max_f32 v171, v142, v143\n"
"v_mfma_f32_16x16x32_bf16 v[224:227], a[72:75], v[32:35], 0\n"
"v_max_f32 v136, v136, v116\n"
"v_max_f32 v137, v137, v117\n"
"v_mfma_f32_16x16x32_bf16 v[212:215], a[76:79], v[32:35], 0\n"
"ds_read_b64_tr_b16 v[116:117], v184 offset:136\n"
"v_max_f32 v138, v138, v118\n"
"v_mfma_f32_16x16x32_bf16 v[204:207], a[80:83], v[32:35], 0\n"
"v_max_f32 v139, v139, v119\n"
"v_max3_f32 v170, v170, v136, v137\n"
"v_mfma_f32_16x16x32_bf16 v[224:227], a[84:87], v[36:39], v[224:227]\n"
"ds_read_b64_tr_b16 v[118:119], v185 offset:136\n"
"v_max3_f32 v171, v171, v138, v139\n"
"v_mfma_f32_16x16x32_bf16 v[212:215], a[88:91], v[36:39], v[212:215]\n"
"v_max_f32 v120, v120, v124\n"
"v_max_f32 v121, v121, v125\n"
"v_mfma_f32_16x16x32_bf16 v[204:207], a[92:95], v[36:39], v[204:207]\n"
"s_add_u32 m0, s79, 142464\n"
"ds_read_b64_tr_b16 v[124:125], v184 offset:72\n"
"buffer_load_dwordx4 v192, s[16:19], s43 offen offset:1024 lds\n"
"v_mfma_f32_16x16x32_bf16 v[224:227], a[96:99], v[40:43], v[224:227]\n"
"v_max_f32 v122, v122, v126\n"
"v_max_f32 v123, v123, v127\n"
"v_mfma_f32_16x16x32_bf16 v[212:215], a[100:103], v[40:43], v[212:215]\n"
"ds_read_b64_tr_b16 v[126:127], v185 offset:72\n"
"v_max3_f32 v170, v170, v120, v121\n"
"v_mfma_f32_16x16x32_bf16 v[204:207], a[104:107], v[40:43], v[204:207]\n"
"v_max3_f32 v171, v171, v122, v123\n"
"v_max_f32 v128, v128, v132\n"
"v_mfma_f32_16x16x32_bf16 v[224:227], a[108:111], v[44:47], v[224:227]\n"
"v_max_f32 v129, v129, v133\n"
"v_max_f32 v130, v130, v134\n"
"v_mfma_f32_16x16x32_bf16 v[212:215], a[112:115], v[44:47], v[212:215]\n"
"buffer_store_dwordx2 v[68:69], v193, s[28:31], s4 offen offset:1024 sc1\n"
"v_mfma_f32_16x16x32_bf16 v[204:207], a[116:119], v[44:47], v[204:207]\n"
"ds_read_b64_tr_b16 v[132:133], v184 offset:13632\n"
"v_max_f32 v131, v131, v135\n"
"v_mfma_f32_16x16x32_bf16 v[224:227], a[120:123], v[48:51], v[224:227]\n"
"ds_read_b64_tr_b16 v[134:135], v185 offset:13632\n"
"v_max3_f32 v170, v170, v128, v129\n"
"v_mfma_f32_16x16x32_bf16 v[212:215], a[124:127], v[48:51], v[212:215]\n"
"v_max3_f32 v171, v171, v130, v131\n"
"v_max_f32 v170, v170, v171\n"
"v_mfma_f32_16x16x32_bf16 v[204:207], a[128:131], v[48:51], v[204:207]\n"
"v_mov_b32 v171, v170\n"
"v_cvt_pk_bf16_f32 v210, v216, v217\n"
"v_mfma_f32_16x16x32_bf16 v[224:227], a[132:135], v[52:55], v[224:227]\n"
"s_add_u32 m0, s79, 142464\n"
"ds_read_b64_tr_b16 a[218:219], v236 offset:6144\n"
"buffer_load_dwordx4 v192, s[16:19], s43 offen offset:2048 lds\n"
"v_mfma_f32_16x16x32_bf16 v[212:215], a[136:139], v[52:55], v[212:215]\n"
"s_add_u32 s43, s79, 36864\n"
"v_permlane16_swap_b32 v170, v171\n"
"v_mfma_f32_16x16x32_bf16 v[204:207], a[140:143], v[52:55], v[204:207]\n"
"v_cvt_pk_bf16_f32 v211, v218, v219\n"
"v_max_f32 v170, v170, v171\n"
"v_mfma_f32_16x16x32_bf16 v[228:231], a[144:147], v[32:35], 0\n"
"ds_write_b128 v186, v[208:211] offset:6752\n"
"v_mfma_f32_16x16x32_bf16 v[216:219], a[148:151], v[32:35], 0\n"
"v_mov_b32 v171, v170\n"
"v_cvt_pk_bf16_f32 v220, v220, v221\n"
"v_mfma_f32_16x16x32_bf16 v[208:211], a[152:155], v[32:35], 0\n"
"s_waitcnt vmcnt(13)\n"
"v_permlane32_swap_b32 v170, v171\n"
"v_mfma_f32_16x16x32_bf16 v[228:231], a[156:159], v[36:39], v[228:231]\n"
"ds_read_b128 v[32:35], v188 offset:6144\n"
"v_cvt_pk_bf16_f32 v221, v222, v223\n"
"v_mfma_f32_16x16x32_bf16 v[216:219], a[160:163], v[36:39], v[216:219]\n"
"v_max_f32 v170, v170, v171\n"
"v_mul_f32 v170, 0xbe553b95, v170\n"
"v_mfma_f32_16x16x32_bf16 v[208:211], a[164:167], v[36:39], v[208:211]\n"
"ds_read_b128 v[36:39], v189 offset:6144\n"
"v_fmamk_f32 v140, v140, 0x3e553b95, v170\n"
"v_mfma_f32_16x16x32_bf16 v[228:231], a[168:171], v[40:43], v[228:231]\n"
"v_fmamk_f32 v141, v141, 0x3e553b95, v170\n"
"v_fmamk_f32 v142, v142, 0x3e553b95, v170\n"
"v_mfma_f32_16x16x32_bf16 v[216:219], a[172:175], v[40:43], v[216:219]\n"
"s_add_u32 m0, s34, 1024\n"
"v_fmamk_f32 v143, v143, 0x3e553b95, v170\n"
"v_fmamk_f32 v136, v136, 0x3e553b95, v170\n"
"v_mfma_f32_16x16x32_bf16 v[208:211], a[176:179], v[40:43], v[208:211]\n"
"buffer_load_dwordx4 v190, s[8:11], s0 offen lds\n"
"v_fmamk_f32 v137, v137, 0x3e553b95, v170\n"
"v_mfma_f32_16x16x32_bf16 v[228:231], a[180:183], v[44:47], v[228:231]\n"
"v_exp_f32 v140, v140\n"
"v_mfma_f32_16x16x32_bf16 v[216:219], a[184:187], v[44:47], v[216:219]\n"
"s_waitcnt vmcnt(12)\n"
"v_exp_f32 v141, v141\n"
"v_mfma_f32_16x16x32_bf16 v[208:211], a[188:191], v[44:47], v[208:211]\n"
"ds_read_b128 v[40:43], v188 offset:7168\n"
"ds_read_b128 v[44:47], v189 offset:7168\n"
"v_mfma_f32_16x16x32_bf16 v[228:231], a[192:195], v[48:51], v[228:231]\n"
"v_exp_f32 v142, v142\n"
"v_mfma_f32_16x16x32_bf16 v[216:219], a[196:199], v[48:51], v[216:219]\n"
"v_exp_f32 v143, v143\n"
"v_mfma_f32_16x16x32_bf16 v[208:211], a[200:203], v[48:51], v[208:211]\n"
"v_cvt_pk_bf16_f32 v82, v140, v141\n"
"v_cvt_pk_bf16_f32 v83, v142, v143\n"
"v_mfma_f32_16x16x32_bf16 v[228:231], a[204:207], v[52:55], v[228:231]\n"
"v_fmamk_f32 v138, v138, 0x3e553b95, v170\n"
"v_fmamk_f32 v139, v139, 0x3e553b95, v170\n"
"v_mfma_f32_16x16x32_bf16 v[216:219], a[208:211], v[52:55], v[216:219]\n"
"v_and_b32 v140, v82, v176\n"
"v_and_b32 v141, v83, v177\n"
"v_mfma_f32_16x16x32_bf16 v[208:211], a[212:215], v[52:55], v[208:211]\n"
"v_exp_f32 v136, v136\n"
"s_add_u32 m0, s34, 4096\n"
"v_exp_f32 v137, v137\n"
"buffer_load_dwordx4 v191, s[8:11], s0 offen lds\n"
"s_add_u32 s0, s0, s33\n"
"v_exp_f32 v138, v138\n"
"v_exp_f32 v139, v139\n"
"v_xor_b32 v142, v82, v140\n"
"v_xor_b32 v143, v83, v141\n"
"v_cvt_pk_bf16_f32 v84, v136, v137\n"
"v_cvt_pk_bf16_f32 v85, v138, v139\n"
"v_and_b32 v136, v84, v176\n"
"v_and_b32 v137, v85, v177\n"
"v_xor_b32 v138, v84, v136\n"
"v_xor_b32 v139, v85, v137\n"
"v_mfma_f32_16x16x32_bf16 v[82:85], v[172:175], v[82:85], 0\n"
"s_waitcnt lgkmcnt(15)\n"
"ds_read_b64_tr_b16 a[220:221], v236 offset:8\n"
"v_fmamk_f32 v120, v120, 0x3e553b95, v170\n"
"v_mfma_f32_16x16x32_bf16 v[244:247], v[244:247], v[140:143], 0\n"
"s_waitcnt lgkmcnt(14)\n"
"v_fmamk_f32 v121, v121, 0x3e553b95, v170\n"
"v_fmamk_f32 v122, v122, 0x3e553b95, v170\n"
"v_mfma_f32_16x16x32_bf16 v[248:251], v[248:251], v[140:143], 0\n"
"s_waitcnt lgkmcnt(11)\n"
"v_fmamk_f32 v123, v123, 0x3e553b95, v170\n"
"v_fmamk_f32 v128, v128, 0x3e553b95, v170\n"
"v_mfma_f32_16x16x32_bf16 v[252:255], v[252:255], v[140:143], 0\n"
"s_waitcnt lgkmcnt(9)\n"
"ds_read_b64_tr_b16 v[140:141], v184 offset:27136\n"
"ds_read_b64_tr_b16 v[142:143], v185 offset:27136\n"
"v_mfma_f32_16x16x32_bf16 v[244:247], v[112:115], v[136:139], v[244:247]\n"
"ds_read_b64_tr_b16 v[112:113], v184 offset:27080\n"
"v_fmamk_f32 v129, v129, 0x3e553b95, v170\n"
"v_mfma_f32_16x16x32_bf16 v[248:251], v[116:119], v[136:139], v[248:251]\n"
"ds_read_b64_tr_b16 v[114:115], v185 offset:27080\n"
"v_fmamk_f32 v130, v130, 0x3e553b95, v170\n"
"v_mfma_f32_16x16x32_bf16 v[252:255], v[124:127], v[136:139], v[252:255]\n"
"s_waitcnt lgkmcnt(5)\n"
"s_add_u32 m0, s34, 2048\n"
"ds_read_b64_tr_b16 v[136:137], v184 offset:27144\n"
"buffer_load_dwordx4 v190, s[8:11], s1 offen lds\n"
"ds_read_b64_tr_b16 v[138:139], v185 offset:27144\n"
"v_exp_f32 v120, v120\n"
"v_exp_f32 v121, v121\n"
"v_exp_f32 v122, v122\n"
"v_exp_f32 v123, v123\n"
"v_fmamk_f32 v131, v131, 0x3e553b95, v170\n"
"v_cvt_pk_bf16_f32 v90, v120, v121\n"
"v_cvt_pk_bf16_f32 v91, v122, v123\n"
"v_exp_f32 v128, v128\n"
"v_exp_f32 v129, v129\n"
"v_exp_f32 v130, v130\n"
"s_waitcnt vmcnt(12)\n"
"ds_read_b128 v[48:51], v188 offset:8192\n"
"ds_read_b128 v[52:55], v189 offset:8192\n"
"v_exp_f32 v131, v131\n"
"v_and_b32 v120, v90, v176\n"
"v_and_b32 v121, v91, v177\n"
"v_xor_b32 v122, v90, v120\n"
"v_xor_b32 v123, v91, v121\n"
"v_cvt_pk_bf16_f32 v92, v128, v129\n"
"v_cvt_pk_bf16_f32 v93, v130, v131\n"
"v_mfma_f32_16x16x32_bf16 v[244:247], v[132:135], v[120:123], v[244:247]\n"
"v_and_b32 v128, v92, v176\n"
"v_and_b32 v129, v93, v177\n"
"v_mfma_f32_16x16x32_bf16 v[248:251], v[98:101], v[120:123], v[248:251]\n"
"v_xor_b32 v130, v92, v128\n"
"v_xor_b32 v131, v93, v129\n"
"v_mfma_f32_16x16x32_bf16 v[90:93], v[172:175], v[90:93], v[82:85]\n"
"s_waitcnt lgkmcnt(2)\n"
"ds_read_b64_tr_b16 a[222:223], v236 offset:6152\n"
"v_cvt_pk_bf16_f32 v224, v224, v225\n"
"v_mfma_f32_16x16x32_bf16 v[252:255], v[106:109], v[120:123], v[252:255]\n"
"s_add_u32 m0, s34, 5120\n"
"ds_read_b64_tr_b16 a[224:225], v235\n"
"buffer_load_dwordx4 v191, s[8:11], s1 offen lds\n"
"v_mfma_f32_16x16x32_bf16 v[244:247], v[140:143], v[128:131], v[244:247]\n"
"s_add_u32 s1, s1, s33\n"
"v_rcp_f32 v171, v90\n"
"v_mfma_f32_16x16x32_bf16 v[248:251], v[136:139], v[128:131], v[248:251]\n"
"ds_read_b64_tr_b16 a[226:227], v235 offset:6144\n"
"v_cvt_pk_bf16_f32 v225, v226, v227\n"
"v_mfma_f32_16x16x32_bf16 v[252:255], v[112:115], v[128:131], v[252:255]\n"
"s_nop 1\n"
"v_mul_f32 v244, v171, v244\n"
"v_mul_f32 v245, v171, v245\n"
"v_mul_f32 v246, v171, v246\n"
"v_mul_f32 v247, v171, v247\n"
"v_cvt_pk_bf16_f32 v244, v244, v245\n"
"v_cvt_pk_bf16_f32 v245, v246, v247\n"
"v_mul_f32 v248, v171, v248\n"
"v_mul_f32 v249, v171, v249\n"
"v_mul_f32 v250, v171, v250\n"
"v_mul_f32 v251, v171, v251\n"
"v_cvt_pk_bf16_f32 v246, v248, v249\n"
"v_cvt_pk_bf16_f32 v247, v250, v251\n"
"buffer_store_dwordx4 v[244:247], v192, s[28:31], s37 offen sc1\n"
"v_mul_f32 v252, v171, v252\n"
"v_mul_f32 v253, v171, v253\n"
"v_mul_f32 v254, v171, v254\n"
"v_mul_f32 v255, v171, v255\n"
"v_cvt_pk_bf16_f32 v252, v252, v253\n"
"v_cvt_pk_bf16_f32 v253, v254, v255\n"
"s_add_u32 m0, s34, 6144\n"
"buffer_store_dwordx2 v[252:253], v193, s[28:31], s37 offen offset:1024 sc1\n"
"s_add_u32 s37, s37, 6144\n"
"s_add_u32 s4, s37, 55296\n"
"v_cvt_pk_bf16_f32 v226, v212, v213\n"
"v_cvt_pk_bf16_f32 v227, v214, v215\n"
"v_mfma_f32_16x16x32_bf16 v[212:215], a[0:3], v[32:35], 0\n"
"ds_write_b128 v186, v[224:227] offset:6880\n"
"v_mfma_f32_16x16x32_bf16 v[212:215], a[12:15], v[36:39], v[212:215]\n"
"buffer_load_dwordx4 v190, s[8:11], s32 offen lds\n"
"v_cvt_pk_bf16_f32 v222, v204, v205\n"
"v_mfma_f32_16x16x32_bf16 v[212:215], a[24:27], v[40:43], v[212:215]\n"
"v_cvt_pk_bf16_f32 v223, v206, v207\n"
"v_cvt_pk_bf16_f32 v152, v228, v229\n"
"v_mfma_f32_16x16x32_bf16 v[204:207], a[4:7], v[32:35], 0\n"
"ds_write_b128 v186, v[220:223] offset:6816\n"
"v_mfma_f32_16x16x32_bf16 v[220:223], a[8:11], v[32:35], 0\n"
"s_waitcnt vmcnt(11)\n"
"v_cvt_pk_bf16_f32 v153, v230, v231\n"
"v_cvt_pk_bf16_f32 v154, v216, v217\n"
"v_mfma_f32_16x16x32_bf16 v[204:207], a[16:19], v[36:39], v[204:207]\n"
"v_readfirstlane_b32 s6, v201\n"
"v_cvt_pk_bf16_f32 v155, v218, v219\n"
"v_mfma_f32_16x16x32_bf16 v[220:223], a[20:23], v[36:39], v[220:223]\n"
"s_and_b32 s6, s6, 0xffffff00\n"
"v_or_b32 v201, 2, s6\n"
"v_cvt_pk_bf16_f32 v166, v208, v209\n"
"v_mfma_f32_16x16x32_bf16 v[204:207], a[28:31], v[40:43], v[204:207]\n"
"s_mov_b64 exec, 1\n"
"buffer_store_dword v201, off, s[28:31], s40 sc1\n"
"s_mov_b64 exec, -1\n"
"v_mfma_f32_16x16x32_bf16 v[220:223], a[32:35], v[40:43], v[220:223]\n"
"v_cvt_pk_bf16_f32 v167, v210, v211\n"
"v_mfma_f32_16x16x32_bf16 v[212:215], a[36:39], v[44:47], v[212:215]\n"
"s_waitcnt lgkmcnt(6)\n"
"ds_read_b128 v[116:119], v180\n"
"ds_read_b128 v[124:127], v181\n"
"v_mfma_f32_16x16x32_bf16 v[204:207], a[40:43], v[44:47], v[204:207]\n"
"s_waitcnt lgkmcnt(7)\n"
"ds_read_b64 v[82:83], v180 offset:64\n"
"s_add_u32 m0, s34, 9216\n"
"v_mfma_f32_16x16x32_bf16 v[220:223], a[44:47], v[44:47], v[220:223]\n"
"buffer_load_dwordx4 v191, s[8:11], s32 offen lds\n"
"ds_read_b64 v[84:85], v181 offset:64\n"
"v_mfma_f32_16x16x32_bf16 v[212:215], a[48:51], v[48:51], v[212:215]\n"
"s_add_u32 s32, s32, s33\n"
"ds_read_b128 v[120:123], v180 offset:13504\n"
"v_mfma_f32_16x16x32_bf16 v[204:207], a[52:55], v[48:51], v[204:207]\n"
"ds_read_b128 v[132:135], v181 offset:13504\n"
"v_mfma_f32_16x16x32_bf16 v[220:223], a[56:59], v[48:51], v[220:223]\n"
"ds_read_b128 v[128:131], v180 offset:27008\n"
"v_mfma_f32_16x16x32_bf16 v[212:215], a[60:63], v[52:55], v[212:215]\n"
"ds_read_b128 v[140:143], v181 offset:27008\n"
"v_mfma_f32_16x16x32_bf16 v[204:207], a[64:67], v[52:55], v[204:207]\n"
"ds_read_b64 v[90:91], v180 offset:13568\n"
"v_mfma_f32_16x16x32_bf16 v[220:223], a[68:71], v[52:55], v[220:223]\n"
"s_waitcnt lgkmcnt(8)\n"
"ds_read_b64 v[92:93], v181 offset:13568\n"
"v_mfma_f32_16x16x32_bf16 v[116:119], v[116:119], v[144:147], 0\n"
"s_waitcnt lgkmcnt(8)\n"
"ds_read_b64 v[98:99], v180 offset:27072\n"
"v_cvt_pk_bf16_f32 v212, v212, v213\n"
"v_mfma_f32_16x16x32_bf16 v[124:127], v[124:127], v[144:147], 0\n"
"s_waitcnt lgkmcnt(8)\n"
"ds_read_b64 v[100:101], v181 offset:27072\n"
"v_cvt_pk_bf16_f32 v213, v214, v215\n"
"v_mfma_f32_16x16x32_bf16 v[116:119], v[80:83], v[156:159], v[116:119]\n"
"s_waitcnt lgkmcnt(8)\n"
"ds_read_b64_tr_b16 v[248:249], v184 offset:128\n"
"v_cvt_pk_bf16_f32 v214, v204, v205\n"
"v_mfma_f32_16x16x32_bf16 v[124:127], v[84:87], v[158:161], v[124:127]\n"
"s_waitcnt lgkmcnt(8)\n"
"ds_read_b64_tr_b16 v[250:251], v185 offset:128\n"
"v_cvt_pk_bf16_f32 v215, v206, v207\n"
"v_mfma_f32_16x16x32_bf16 v[120:123], v[120:123], v[144:147], 0\n"
"s_waitcnt lgkmcnt(8)\n"
"ds_read_b64_tr_b16 v[244:245], v184 offset:136\n"
"v_cvt_pk_bf16_f32 v220, v220, v221\n"
"v_mfma_f32_16x16x32_bf16 v[132:135], v[132:135], v[144:147], 0\n"
"s_waitcnt lgkmcnt(8)\n"
"ds_read_b64_tr_b16 v[246:247], v185 offset:136\n"
"v_cvt_pk_bf16_f32 v221, v222, v223\n"
"v_mfma_f32_16x16x32_bf16 v[128:131], v[128:131], v[144:147], 0\n"
"s_waitcnt lgkmcnt(8)\n"
"ds_read_b64_tr_b16 v[252:253], v184 offset:72\n"
"v_max_f32 v116, v116, v124\n"
"v_mfma_f32_16x16x32_bf16 v[140:143], v[140:143], v[144:147], 0\n"
"s_waitcnt lgkmcnt(8)\n"
"ds_read_b64_tr_b16 v[254:255], v185 offset:72\n"
"v_max_f32 v117, v117, v125\n"
"v_mfma_f32_16x16x32_bf16 v[120:123], v[88:91], v[156:159], v[120:123]\n"
"s_waitcnt lgkmcnt(8)\n"
"ds_read_b64_tr_b16 v[124:125], v184 offset:13632\n"
"v_max_f32 v118, v118, v126\n"
"v_mfma_f32_16x16x32_bf16 v[132:135], v[92:95], v[158:161], v[132:135]\n"
"s_waitcnt lgkmcnt(8)\n"
"v_max_f32 v119, v119, v127\n"
"v_max_f32 v170, v116, v117\n"
"v_mfma_f32_16x16x32_bf16 v[128:131], v[96:99], v[156:159], v[128:131]\n"
"s_waitcnt lgkmcnt(7)\n"
"ds_read_b64_tr_b16 v[126:127], v185 offset:13632\n"
"ds_read_b64_tr_b16 v[98:99], v184 offset:27144\n"
"v_mfma_f32_16x16x32_bf16 v[140:143], v[100:103], v[158:161], v[140:143]\n"
"s_waitcnt vmcnt(9)\n"
"ds_read_b64_tr_b16 v[100:101], v185 offset:27144\n"
"v_max_f32 v171, v118, v119\n"
"v_mfma_f32_16x16x32_bf16 v[224:227], a[72:75], v[32:35], 0\n"
"s_add_u32 m0, s34, 7168\n"
"v_max_f32 v120, v120, v132\n"
"v_max_f32 v121, v121, v133\n"
"v_mfma_f32_16x16x32_bf16 v[228:231], a[76:79], v[32:35], 0\n"
"s_barrier\n"
"v_mfma_f32_16x16x32_bf16 v[216:219], a[80:83], v[32:35], 0\n"
"buffer_load_dwordx4 v190, s[8:11], s0 offen lds\n"
"ds_read_b128 v[136:139], v178\n"
"v_mfma_f32_16x16x32_bf16 v[224:227], a[84:87], v[36:39], v[224:227]\n"
"ds_read_b128 v[112:115], v179\n"
"ds_read_b64 v[106:107], v178 offset:64\n"
"v_mfma_f32_16x16x32_bf16 v[228:231], a[88:91], v[36:39], v[228:231]\n"
"ds_read_b64 v[108:109], v179 offset:64\n"
"v_max_f32 v122, v122, v134\n"
"v_mfma_f32_16x16x32_bf16 v[216:219], a[92:95], v[36:39], v[216:219]\n"
"ds_write_b128 v186, v[212:215] offset:20256\n"
"v_mfma_f32_16x16x32_bf16 v[224:227], a[96:99], v[40:43], v[224:227]\n"
"v_max_f32 v123, v123, v135\n"
"v_max3_f32 v170, v170, v120, v121\n"
"v_mfma_f32_16x16x32_bf16 v[228:231], a[100:103], v[40:43], v[228:231]\n"
"v_max3_f32 v171, v171, v122, v123\n"
"v_max_f32 v128, v128, v140\n"
"v_mfma_f32_16x16x32_bf16 v[216:219], a[104:107], v[40:43], v[216:219]\n"
"ds_read_b64_tr_b16 v[132:133], v184 offset:13640\n"
"ds_read_b64_tr_b16 v[134:135], v185 offset:13640\n"
"v_mfma_f32_16x16x32_bf16 v[224:227], a[108:111], v[44:47], v[224:227]\n"
"s_waitcnt lgkmcnt(6)\n"
"ds_read_b64_tr_b16 a[228:229], v236 offset:12288\n"
"v_max_f32 v129, v129, v141\n"
"v_mfma_f32_16x16x32_bf16 v[136:139], v[136:139], v[144:147], 0\n"
"s_waitcnt lgkmcnt(6)\n"
"buffer_store_dwordx4 v[68:71], v192, s[28:31], s4 offen sc1\n"
"v_mfma_f32_16x16x32_bf16 v[112:115], v[112:115], v[144:147], 0\n"
"s_waitcnt lgkmcnt(4)\n"
"ds_read_b64_tr_b16 v[140:141], v184 offset:13576\n"
"ds_read_b64_tr_b16 a[230:231], v236 offset:18432\n"
"v_mfma_f32_16x16x32_bf16 v[136:139], v[104:107], v[156:159], v[136:139]\n"
"s_add_u32 m0, s79, 130176\n"
"v_max_f32 v130, v130, v142\n"
"v_max_f32 v131, v131, v143\n"
"v_mfma_f32_16x16x32_bf16 v[112:115], v[108:111], v[158:161], v[112:115]\n"
"buffer_load_dwordx4 v192, s[16:19], s43 offen lds\n"
"ds_read_b64_tr_b16 v[142:143], v185 offset:13576\n"
"v_mfma_f32_16x16x32_bf16 v[228:231], a[112:115], v[44:47], v[228:231]\n"
"ds_read_b64_tr_b16 v[106:107], v184 offset:27080\n"
"v_max3_f32 v170, v170, v128, v129\n"
"v_mfma_f32_16x16x32_bf16 v[216:219], a[116:119], v[44:47], v[216:219]\n"
"ds_read_b64_tr_b16 v[108:109], v185 offset:27080\n"
"v_max3_f32 v171, v171, v130, v131\n"
"v_mfma_f32_16x16x32_bf16 v[224:227], a[120:123], v[48:51], v[224:227]\n"
"v_max_f32 v136, v136, v112\n"
"v_max_f32 v137, v137, v113\n"
"v_mfma_f32_16x16x32_bf16 v[228:231], a[124:127], v[48:51], v[228:231]\n"
"ds_read_b64_tr_b16 v[112:113], v184 offset:27136\n"
"v_max_f32 v138, v138, v114\n"
"v_mfma_f32_16x16x32_bf16 v[216:219], a[128:131], v[48:51], v[216:219]\n"
"v_max_f32 v139, v139, v115\n"
"v_max3_f32 v170, v170, v136, v137\n"
"v_mfma_f32_16x16x32_bf16 v[224:227], a[132:135], v[52:55], v[224:227]\n"
"ds_read_b64_tr_b16 v[114:115], v185 offset:27136\n"
"v_max3_f32 v171, v171, v138, v139\n"
"v_mfma_f32_16x16x32_bf16 v[228:231], a[136:139], v[52:55], v[228:231]\n"
"v_max_f32 v170, v170, v171\n"
"v_mov_b32 v171, v170\n"
"v_mfma_f32_16x16x32_bf16 v[216:219], a[140:143], v[52:55], v[216:219]\n"
"s_nop 0\n"
"v_permlane16_swap_b32 v170, v171\n"
"v_mfma_f32_16x16x32_bf16 v[208:211], a[144:147], v[32:35], 0\n"
"ds_read_b64_tr_b16 a[232:233], v236 offset:12296\n"
"v_max_f32 v170, v170, v171\n"
"v_mfma_f32_16x16x32_bf16 v[204:207], a[148:151], v[32:35], 0\n"
"s_add_u32 m0, s79, 130176\n"
"v_mov_b32 v171, v170\n"
"v_cvt_pk_bf16_f32 v224, v224, v225\n"
"v_mfma_f32_16x16x32_bf16 v[212:215], a[152:155], v[32:35], 0\n"
"buffer_load_dwordx4 v192, s[16:19], s43 offen offset:1024 lds\n"
"ds_read_b128 v[32:35], v188\n"
"v_mfma_f32_16x16x32_bf16 v[208:211], a[156:159], v[36:39], v[208:211]\n"
"v_permlane32_swap_b32 v170, v171\n"
"v_mfma_f32_16x16x32_bf16 v[204:207], a[160:163], v[36:39], v[204:207]\n"
"v_max_f32 v170, v170, v171\n"
"v_mul_f32 v170, 0xbe553b95, v170\n"
"v_mfma_f32_16x16x32_bf16 v[212:215], a[164:167], v[36:39], v[212:215]\n"
"ds_read_b128 v[36:39], v189\n"
"v_fmamk_f32 v116, v116, 0x3e553b95, v170\n"
"v_mfma_f32_16x16x32_bf16 v[208:211], a[168:171], v[40:43], v[208:211]\n"
"v_fmamk_f32 v117, v117, 0x3e553b95, v170\n"
"v_fmamk_f32 v118, v118, 0x3e553b95, v170\n"
"v_mfma_f32_16x16x32_bf16 v[204:207], a[172:175], v[40:43], v[204:207]\n"
"v_fmamk_f32 v119, v119, 0x3e553b95, v170\n"
"v_fmamk_f32 v120, v120, 0x3e553b95, v170\n"
"v_mfma_f32_16x16x32_bf16 v[212:215], a[176:179], v[40:43], v[212:215]\n"
"v_exp_f32 v116, v116\n"
"v_mfma_f32_16x16x32_bf16 v[208:211], a[180:183], v[44:47], v[208:211]\n"
"v_exp_f32 v117, v117\n"
"v_mfma_f32_16x16x32_bf16 v[204:207], a[184:187], v[44:47], v[204:207]\n"
"s_waitcnt vmcnt(11)\n"
"v_exp_f32 v118, v118\n"
"v_mfma_f32_16x16x32_bf16 v[212:215], a[188:191], v[44:47], v[212:215]\n"
"ds_read_b128 v[40:43], v188 offset:1024\n"
"ds_read_b128 v[44:47], v189 offset:1024\n"
"v_mfma_f32_16x16x32_bf16 v[208:211], a[192:195], v[48:51], v[208:211]\n"
"s_add_u32 m0, s79, 130176\n"
"v_exp_f32 v119, v119\n"
"v_mfma_f32_16x16x32_bf16 v[204:207], a[196:199], v[48:51], v[204:207]\n"
".Lic2%=:\n"
"s_cbranch_execz .Lib0%=\n"
"buffer_load_dwordx4 v192, s[16:19], s43 offen offset:2048 lds\n"
"v_cvt_pk_bf16_f32 v82, v116, v117\n"
"v_mfma_f32_16x16x32_bf16 v[212:215], a[200:203], v[48:51], v[212:215]\n"
"s_add_u32 s43, s79, 55296\n"
"v_cvt_pk_bf16_f32 v83, v118, v119\n"
"v_fmamk_f32 v121, v121, 0x3e553b95, v170\n"
"v_mfma_f32_16x16x32_bf16 v[208:211], a[204:207], v[52:55], v[208:211]\n"
"v_fmamk_f32 v122, v122, 0x3e553b95, v170\n"
"v_fmamk_f32 v123, v123, 0x3e553b95, v170\n"
"v_mfma_f32_16x16x32_bf16 v[204:207], a[208:211], v[52:55], v[204:207]\n"
"v_and_b32 v116, v82, v176\n"
"v_and_b32 v117, v83, v177\n"
"v_mfma_f32_16x16x32_bf16 v[212:215], a[212:215], v[52:55], v[212:215]\n"
"v_exp_f32 v120, v120\n"
"v_exp_f32 v121, v121\n"
"v_exp_f32 v122, v122\n"
"v_exp_f32 v123, v123\n"
"v_xor_b32 v118, v82, v116\n"
"v_xor_b32 v119, v83, v117\n"
"v_cvt_pk_bf16_f32 v84, v120, v121\n"
"v_cvt_pk_bf16_f32 v85, v122, v123\n"
"v_mfma_f32_16x16x32_bf16 v[248:251], v[248:251], v[116:119], 0\n"
"v_and_b32 v120, v84, v176\n"
"v_and_b32 v121, v85, v177\n"
"v_mfma_f32_16x16x32_bf16 v[244:247], v[244:247], v[116:119], 0\n"
"v_xor_b32 v122, v84, v120\n"
"v_xor_b32 v123, v85, v121\n"
"v_mfma_f32_16x16x32_bf16 v[252:255], v[252:255], v[116:119], 0\n"
"s_waitcnt lgkmcnt(9)\n"
"ds_read_b64_tr_b16 v[116:117], v182 offset:128\n"
"ds_read_b64_tr_b16 v[118:119], v183 offset:128\n"
"v_mfma_f32_16x16x32_bf16 v[248:251], v[124:127], v[120:123], v[248:251]\n"
"s_waitcnt lgkmcnt(4)\n"
"ds_read_b64_tr_b16 v[124:125], v182 offset:72\n"
"v_fmamk_f32 v128, v128, 0x3e553b95, v170\n"
"v_mfma_f32_16x16x32_bf16 v[244:247], v[132:135], v[120:123], v[244:247]\n"
"s_add_u32 m0, s34, 10240\n"
"ds_read_b64_tr_b16 v[126:127], v183 offset:72\n"
"buffer_load_dwordx4 v191, s[8:11], s0 offen lds\n"
"v_mfma_f32_16x16x32_bf16 v[252:255], v[140:143], v[120:123], v[252:255]\n"
"s_add_u32 s0, s0, s33\n"
"ds_read_b64_tr_b16 v[120:121], v182 offset:136\n"
"v_fmamk_f32 v129, v129, 0x3e553b95, v170\n"
"v_mfma_f32_16x16x32_bf16 v[82:85], v[172:175], v[82:85], 0\n"
"ds_read_b64_tr_b16 v[122:123], v183 offset:136\n"
"v_fmamk_f32 v130, v130, 0x3e553b95, v170\n"
"v_fmamk_f32 v131, v131, 0x3e553b95, v170\n"
"v_exp_f32 v128, v128\n"
"v_exp_f32 v129, v129\n"
"v_exp_f32 v130, v130\n"
"v_exp_f32 v131, v131\n"
"v_fmamk_f32 v136, v136, 0x3e553b95, v170\n"
"v_fmamk_f32 v137, v137, 0x3e553b95, v170\n"
"v_fmamk_f32 v138, v138, 0x3e553b95, v170\n"
"v_fmamk_f32 v139, v139, 0x3e553b95, v170\n"
"v_cvt_pk_bf16_f32 v90, v128, v129\n"
"v_cvt_pk_bf16_f32 v91, v130, v131\n"
"v_exp_f32 v136, v136\n"
"v_exp_f32 v137, v137\n"
"v_exp_f32 v138, v138\n"
"s_waitcnt vmcnt(11)\n"
"ds_read_b128 v[48:51], v188 offset:2048\n"
"ds_read_b128 v[52:55], v189 offset:2048\n"
"v_exp_f32 v139, v139\n"
"v_and_b32 v128, v90, v176\n"
"v_and_b32 v129, v91, v177\n"
"s_add_u32 m0, s34, 8192\n"
"v_xor_b32 v130, v90, v128\n"
"buffer_load_dwordx4 v190, s[8:11], s1 offen lds\n"
"v_xor_b32 v131, v91, v129\n"
"v_cvt_pk_bf16_f32 v92, v136, v137\n"
"v_cvt_pk_bf16_f32 v93, v138, v139\n"
"v_mfma_f32_16x16x32_bf16 v[248:251], v[112:115], v[128:131], v[248:251]\n"
"ds_read_b64_tr_b16 a[234:235], v236 offset:18440\n"
"v_and_b32 v136, v92, v176\n"
"v_mfma_f32_16x16x32_bf16 v[244:247], v[98:101], v[128:131], v[244:247]\n"
"v_and_b32 v137, v93, v177\n"
"v_xor_b32 v138, v92, v136\n"
"v_mfma_f32_16x16x32_bf16 v[252:255], v[106:109], v[128:131], v[252:255]\n"
"v_xor_b32 v139, v93, v137\n"
"v_cvt_pk_bf16_f32 v225, v226, v227\n"
"v_mfma_f32_16x16x32_bf16 v[90:93], v[172:175], v[90:93], v[82:85]\n"
"s_waitcnt lgkmcnt(3)\n"
"ds_read_b64_tr_b16 a[236:237], v235 offset:12288\n"
"v_cvt_pk_bf16_f32 v226, v228, v229\n"
"v_mfma_f32_16x16x32_bf16 v[248:251], v[116:119], v[136:139], v[248:251]\n"
"v_cvt_pk_bf16_f32 v227, v230, v231\n"
"v_cvt_pk_bf16_f32 v222, v216, v217\n"
"v_mfma_f32_16x16x32_bf16 v[244:247], v[120:123], v[136:139], v[244:247]\n"
"ds_write_b128 v186, v[224:227] offset:20384\n"
"v_mfma_f32_16x16x32_bf16 v[252:255], v[124:127], v[136:139], v[252:255]\n"
"v_rcp_f32 v171, v90\n"
"v_mfma_f32_16x16x32_bf16 v[228:231], a[0:3], v[32:35], 0\n"
"v_cvt_pk_bf16_f32 v223, v218, v219\n"
"v_mul_f32 v248, v171, v248\n"
"v_mfma_f32_16x16x32_bf16 v[216:219], a[4:7], v[32:35], 0\n"
"s_add_u32 m0, s34, 11264\n"
"ds_write_b128 v186, v[220:223] offset:20320\n"
"v_mfma_f32_16x16x32_bf16 v[220:223], a[8:11], v[32:35], 0\n"
"buffer_load_dwordx4 v191, s[8:11], s1 offen lds\n"
"v_mul_f32 v249, v171, v249\n"
"v_mfma_f32_16x16x32_bf16 v[228:231], a[12:15], v[36:39], v[228:231]\n"
"s_add_u32 s1, s1, s33\n"
"v_mul_f32 v250, v171, v250\n"
"v_mul_f32 v251, v171, v251\n"
"v_mfma_f32_16x16x32_bf16 v[216:219], a[16:19], v[36:39], v[216:219]\n"
"v_cvt_pk_bf16_f32 v248, v248, v249\n"
"v_cvt_pk_bf16_f32 v249, v250, v251\n"
"v_mfma_f32_16x16x32_bf16 v[220:223], a[20:23], v[36:39], v[220:223]\n"
"buffer_load_dword v200, off, s[28:31], s39 sc1\n"
"v_mul_f32 v244, v171, v244\n"
"v_mfma_f32_16x16x32_bf16 v[228:231], a[24:27], v[40:43], v[228:231]\n"
"v_mul_f32 v245, v171, v245\n"
"v_mul_f32 v246, v171, v246\n"
"v_mfma_f32_16x16x32_bf16 v[216:219], a[28:31], v[40:43], v[216:219]\n"
"v_mul_f32 v247, v171, v247\n"
"v_cvt_pk_bf16_f32 v250, v244, v245\n"
"v_mfma_f32_16x16x32_bf16 v[220:223], a[32:35], v[40:43], v[220:223]\n"
"v_cvt_pk_bf16_f32 v251, v246, v247\n"
"v_mul_f32 v252, v171, v252\n"
"v_mfma_f32_16x16x32_bf16 v[228:231], a[36:39], v[44:47], v[228:231]\n"
"s_waitcnt lgkmcnt(5)\n"
"buffer_store_dwordx4 v[248:251], v192, s[28:31], s37 offen sc1\n"
"v_mfma_f32_16x16x32_bf16 v[216:219], a[40:43], v[44:47], v[216:219]\n"
"v_mul_f32 v253, v171, v253\n"
"v_mul_f32 v254, v171, v254\n"
"v_mfma_f32_16x16x32_bf16 v[220:223], a[44:47], v[44:47], v[220:223]\n"
"s_waitcnt lgkmcnt(4)\n"
"v_mul_f32 v255, v171, v255\n"
"v_cvt_pk_bf16_f32 v252, v252, v253\n"
"v_mfma_f32_16x16x32_bf16 v[228:231], a[48:51], v[48:51], v[228:231]\n"
"s_add_u32 m0, s34, 0\n"
"v_cvt_pk_bf16_f32 v253, v254, v255\n"
"v_cvt_pk_bf16_f32 v144, v208, v209\n"
"v_mfma_f32_16x16x32_bf16 v[216:219], a[52:55], v[48:51], v[216:219]\n"
"buffer_load_dwordx4 v190, s[8:11], s32 offen lds\n"
"ds_read_b64_tr_b16 a[238:239], v235 offset:18432\n"
"v_mfma_f32_16x16x32_bf16 v[220:223], a[56:59], v[48:51], v[220:223]\n"
"ds_read_b128 v[132:135], v180 offset:13504\n"
"v_cvt_pk_bf16_f32 v145, v210, v211\n"
"v_mfma_f32_16x16x32_bf16 v[228:231], a[60:63], v[52:55], v[228:231]\n"
"ds_read_b128 v[140:143], v181 offset:13504\n"
"v_cvt_pk_bf16_f32 v146, v204, v205\n"
"v_mfma_f32_16x16x32_bf16 v[216:219], a[64:67], v[52:55], v[216:219]\n"
"buffer_store_dwordx2 v[252:253], v193, s[28:31], s37 offen offset:1024 sc1\n"
"v_mfma_f32_16x16x32_bf16 v[220:223], a[68:71], v[52:55], v[220:223]\n"
"s_add_u32 s37, s37, 6144\n"
"ds_read_b128 v[128:131], v180 offset:27008\n"
"ds_read_b128 v[112:115], v181 offset:27008\n"
"v_mfma_f32_16x16x32_bf16 v[224:227], a[72:75], v[32:35], 0\n"
"s_add_u32 s4, s37, 49152\n"
"ds_read_b128 v[136:139], v178\n"
"v_cvt_pk_bf16_f32 v147, v206, v207\n"
"v_mfma_f32_16x16x32_bf16 v[208:211], a[76:79], v[32:35], 0\n"
"ds_read_b128 v[116:119], v179\n"
"v_cvt_pk_bf16_f32 v158, v212, v213\n"
"v_mfma_f32_16x16x32_bf16 v[204:207], a[80:83], v[32:35], 0\n"
"ds_read_b64 v[82:83], v180 offset:13568\n"
"v_cvt_pk_bf16_f32 v159, v214, v215\n"
"v_mfma_f32_16x16x32_bf16 v[224:227], a[84:87], v[36:39], v[224:227]\n"
"ds_read_b64 v[84:85], v181 offset:13568\n"
"v_cvt_pk_bf16_f32 v228, v228, v229\n"
"v_mfma_f32_16x16x32_bf16 v[208:211], a[88:91], v[36:39], v[208:211]\n"
"ds_read_b64 v[90:91], v180 offset:27072\n"
"v_cvt_pk_bf16_f32 v229, v230, v231\n"
"v_mfma_f32_16x16x32_bf16 v[204:207], a[92:95], v[36:39], v[204:207]\n"
"s_waitcnt lgkmcnt(8)\n"
"ds_read_b64 v[92:93], v181 offset:27072\n"
"v_cvt_pk_bf16_f32 v230, v216, v217\n"
"v_mfma_f32_16x16x32_bf16 v[132:135], v[132:135], v[148:151], 0\n"
"s_waitcnt lgkmcnt(8)\n"
"ds_read_b64 v[98:99], v178 offset:64\n"
"v_cvt_pk_bf16_f32 v231, v218, v219\n"
"v_mfma_f32_16x16x32_bf16 v[140:143], v[140:143], v[148:151], 0\n"
"s_waitcnt lgkmcnt(8)\n"
"ds_read_b64 v[100:101], v179 offset:64\n"
"v_cvt_pk_bf16_f32 v220, v220, v221\n"
"v_mfma_f32_16x16x32_bf16 v[128:131], v[128:131], v[148:151], 0\n"
"s_waitcnt lgkmcnt(8)\n"
"ds_read_b64_tr_b16 v[244:245], v184 offset:13632\n"
"v_cvt_pk_bf16_f32 v221, v222, v223\n"
"v_mfma_f32_16x16x32_bf16 v[112:115], v[112:115], v[148:151], 0\n"
"s_waitcnt lgkmcnt(8)\n"
"ds_read_b64_tr_b16 v[246:247], v185 offset:13632\n"
"s_add_u32 m0, s34, 3072\n"
"v_mfma_f32_16x16x32_bf16 v[136:139], v[136:139], v[148:151], 0\n"
"s_waitcnt lgkmcnt(8)\n"
"buffer_load_dwordx4 v191, s[8:11], s32 offen lds\n"
"ds_read_b64_tr_b16 v[248:249], v184 offset:13640\n"
"v_mfma_f32_16x16x32_bf16 v[116:119], v[116:119], v[148:151], 0\n"
"s_waitcnt lgkmcnt(8)\n"
"ds_read_b64_tr_b16 v[250:251], v185 offset:13640\n"
"s_add_u32 s32, s32, s33\n"
"v_mfma_f32_16x16x32_bf16 v[132:135], v[80:83], v[160:163], v[132:135]\n"
"s_waitcnt lgkmcnt(8)\n"
"ds_read_b64_tr_b16 v[252:253], v184 offset:13576\n"
"v_mfma_f32_16x16x32_bf16 v[140:143], v[84:87], v[162:165], v[140:143]\n"
"s_waitcnt lgkmcnt(8)\n"
"ds_read_b64_tr_b16 v[254:255], v185 offset:13576\n"
"v_mfma_f32_16x16x32_bf16 v[128:131], v[88:91], v[160:163], v[128:131]\n"
"s_waitcnt lgkmcnt(8)\n"
"v_mfma_f32_16x16x32_bf16 v[112:115], v[92:95], v[162:165], v[112:115]\n"
"s_waitcnt lgkmcnt(7)\n"
"v_mfma_f32_16x16x32_bf16 v[136:139], v[96:99], v[160:163], v[136:139]\n"
"s_waitcnt vmcnt(8) lgkmcnt(6)\n"
"ds_read_b64_tr_b16 v[98:99], v182 offset:136\n"
"v_max_f32 v132, v132, v140\n"
"v_mfma_f32_16x16x32_bf16 v[116:119], v[100:103], v[162:165], v[116:119]\n"
"s_barrier\n"
"v_mfma_f32_16x16x32_bf16 v[224:227], a[96:99], v[40:43], v[224:227]\n"
"ds_read_b128 v[120:123], v178 offset:13504\n"
"ds_read_b128 v[124:127], v179 offset:13504\n"
"v_mfma_f32_16x16x32_bf16 v[208:211], a[100:103], v[40:43], v[208:211]\n"
"ds_read_b64 v[106:107], v178 offset:13568\n"
"v_max_f32 v133, v133, v141\n"
"v_mfma_f32_16x16x32_bf16 v[204:207], a[104:107], v[40:43], v[204:207]\n"
"s_add_u32 m0, s79, 142464\n"
"ds_read_b64 v[108:109], v179 offset:13568\n"
"buffer_load_dwordx4 v192, s[16:19], s43 offen lds\n"
"v_mfma_f32_16x16x32_bf16 v[224:227], a[108:111], v[44:47], v[224:227]\n"
"ds_read_b64_tr_b16 v[140:141], v184 offset:27136\n"
"v_max_f32 v134, v134, v142\n"
"v_mfma_f32_16x16x32_bf16 v[208:211], a[112:115], v[44:47], v[208:211]\n"
"ds_write_b128 v186, v[228:231] offset:33760\n"
"v_mfma_f32_16x16x32_bf16 v[204:207], a[116:119], v[44:47], v[204:207]\n"
"v_max_f32 v135, v135, v143\n"
"v_max_f32 v170, v132, v133\n"
"v_mfma_f32_16x16x32_bf16 v[224:227], a[120:123], v[48:51], v[224:227]\n"
"v_max_f32 v171, v134, v135\n"
"v_max_f32 v128, v128, v112\n"
"v_mfma_f32_16x16x32_bf16 v[208:211], a[124:127], v[48:51], v[208:211]\n"
"s_waitcnt lgkmcnt(5)\n"
"ds_read_b64_tr_b16 v[142:143], v185 offset:27136\n"
"ds_read_b64_tr_b16 v[100:101], v183 offset:136\n"
"v_mfma_f32_16x16x32_bf16 v[120:123], v[120:123], v[148:151], 0\n"
"s_waitcnt lgkmcnt(6)\n"
"ds_read_b64_tr_b16 a[240:241], v236\n"
"v_max_f32 v129, v129, v113\n"
"v_mfma_f32_16x16x32_bf16 v[124:127], v[124:127], v[148:151], 0\n"
"s_waitcnt lgkmcnt(6)\n"
"v_max_f32 v130, v130, v114\n"
"v_max_f32 v131, v131, v115\n"
"v_mfma_f32_16x16x32_bf16 v[120:123], v[104:107], v[160:163], v[120:123]\n"
"s_waitcnt lgkmcnt(5)\n"
"ds_read_b64_tr_b16 v[112:113], v184 offset:27144\n"
"v_max3_f32 v170, v170, v128, v129\n"
"v_mfma_f32_16x16x32_bf16 v[124:127], v[108:111], v[162:165], v[124:127]\n"
"ds_read_b64_tr_b16 v[114:115], v185 offset:27144\n"
"v_max3_f32 v171, v171, v130, v131\n"
"v_mfma_f32_16x16x32_bf16 v[204:207], a[128:131], v[48:51], v[204:207]\n"
"s_add_u32 m0, s79, 142464\n"
"ds_read_b64_tr_b16 v[106:107], v182 offset:72\n"
"buffer_load_dwordx4 v192, s[16:19], s43 offen offset:1024 lds\n"
"v_mfma_f32_16x16x32_bf16 v[224:227], a[132:135], v[52:55], v[224:227]\n"
"ds_read_b64_tr_b16 v[108:109], v183 offset:72\n"
"v_max_f32 v136, v136, v116\n"
"v_mfma_f32_16x16x32_bf16 v[208:211], a[136:139], v[52:55], v[208:211]\n"
"v_max_f32 v137, v137, v117\n"
"v_max_f32 v138, v138, v118\n"
"v_mfma_f32_16x16x32_bf16 v[204:207], a[140:143], v[52:55], v[204:207]\n"
"ds_read_b64_tr_b16 v[116:117], v184 offset:27080\n"
"v_max_f32 v139, v139, v119\n"
"v_mfma_f32_16x16x32_bf16 v[212:215], a[144:147], v[32:35], 0\n"
"ds_read_b64_tr_b16 v[118:119], v185 offset:27080\n"
"v_max3_f32 v170, v170, v136, v137\n"
"v_mfma_f32_16x16x32_bf16 v[216:219], a[148:151], v[32:35], 0\n"
"v_max3_f32 v171, v171, v138, v139\n"
"v_max_f32 v120, v120, v124\n"
"v_mfma_f32_16x16x32_bf16 v[228:231], a[152:155], v[32:35], 0\n"
"ds_read_b128 v[32:35], v188 offset:6144\n"
"v_max_f32 v121, v121, v125\n"
"v_mfma_f32_16x16x32_bf16 v[212:215], a[156:159], v[36:39], v[212:215]\n"
"ds_read_b64_tr_b16 v[124:125], v182 offset:128\n"
"v_max_f32 v122, v122, v126\n"
"v_mfma_f32_16x16x32_bf16 v[216:219], a[160:163], v[36:39], v[216:219]\n"
"v_max_f32 v123, v123, v127\n"
"v_max3_f32 v170, v170, v120, v121\n"
"v_mfma_f32_16x16x32_bf16 v[228:231], a[164:167], v[36:39], v[228:231]\n"
"ds_read_b128 v[36:39], v189 offset:6144\n"
"ds_read_b64_tr_b16 v[126:127], v183 offset:128\n"
"v_mfma_f32_16x16x32_bf16 v[212:215], a[168:171], v[40:43], v[212:215]\n"
"s_add_u32 m0, s79, 142464\n"
"v_max3_f32 v171, v171, v122, v123\n"
"v_max_f32 v170, v170, v171\n"
"v_mfma_f32_16x16x32_bf16 v[216:219], a[172:175], v[40:43], v[216:219]\n"
"buffer_load_dwordx4 v192, s[16:19], s43 offen offset:2048 lds\n"
"v_mov_b32 v171, v170\n"
"v_mfma_f32_16x16x32_bf16 v[228:231], a[176:179], v[40:43], v[228:231]\n"
"s_add_u32 s43, s80, 12288\n"
"ds_read_b64_tr_b16 a[242:243], v236 offset:6144\n"
"v_permlane16_swap_b32 v170, v171\n"
"v_mfma_f32_16x16x32_bf16 v[212:215], a[180:183], v[44:47], v[212:215]\n"
"v_max_f32 v170, v170, v171\n"
"v_mov_b32 v171, v170\n"
"v_mfma_f32_16x16x32_bf16 v[216:219], a[184:187], v[44:47], v[216:219]\n"
"buffer_store_dwordx2 v[68:69], v193, s[28:31], s4 offen offset:1024 sc1\n"
"v_mfma_f32_16x16x32_bf16 v[228:231], a[188:191], v[44:47], v[228:231]\n"
"v_permlane32_swap_b32 v170, v171\n"
"v_mfma_f32_16x16x32_bf16 v[212:215], a[192:195], v[48:51], v[212:215]\n"
"v_max_f32 v170, v170, v171\n"
"v_mul_f32 v170, 0xbe553b95, v170\n"
"v_mfma_f32_16x16x32_bf16 v[216:219], a[196:199], v[48:51], v[216:219]\n"
"v_fmamk_f32 v132, v132, 0x3e553b95, v170\n"
"v_fmamk_f32 v133, v133, 0x3e553b95, v170\n"
"v_mfma_f32_16x16x32_bf16 v[228:231], a[200:203], v[48:51], v[228:231]\n"
"v_fmamk_f32 v134, v134, 0x3e553b95, v170\n"
"v_fmamk_f32 v135, v135, 0x3e553b95, v170\n"
"v_mfma_f32_16x16x32_bf16 v[212:215], a[204:207], v[52:55], v[212:215]\n"
"v_exp_f32 v132, v132\n"
"v_mfma_f32_16x16x32_bf16 v[216:219], a[208:211], v[52:55], v[216:219]\n"
"v_exp_f32 v133, v133\n"
"v_mfma_f32_16x16x32_bf16 v[228:231], a[212:215], v[52:55], v[228:231]\n"
"s_waitcnt lgkmcnt(2)\n"
"s_add_u32 m0, s34, 1024\n"
"v_exp_f32 v134, v134\n"
"buffer_load_dwordx4 v190, s[8:11], s0 offen lds\n"
"v_exp_f32 v135, v135\n"
"v_cvt_pk_bf16_f32 v82, v132, v133\n"
"v_cvt_pk_bf16_f32 v83, v134, v135\n"
"v_fmamk_f32 v128, v128, 0x3e553b95, v170\n"
"v_fmamk_f32 v129, v129, 0x3e553b95, v170\n"
"v_fmamk_f32 v130, v130, 0x3e553b95, v170\n"
"v_fmamk_f32 v131, v131, 0x3e553b95, v170\n"
"v_and_b32 v132, v82, v176\n"
"v_and_b32 v133, v83, v177\n"
"v_exp_f32 v128, v128\n"
"v_exp_f32 v129, v129\n"
"v_exp_f32 v130, v130\n"
"v_exp_f32 v131, v131\n"
"v_xor_b32 v134, v82, v132\n"
"v_xor_b32 v135, v83, v133\n"
"v_cvt_pk_bf16_f32 v84, v128, v129\n"
"v_cvt_pk_bf16_f32 v85, v130, v131\n"
"v_mfma_f32_16x16x32_bf16 v[244:247], v[244:247], v[132:135], 0\n"
"s_waitcnt vmcnt(12)\n"
"v_and_b32 v128, v84, v176\n"
"v_and_b32 v129, v85, v177\n"
"v_mfma_f32_16x16x32_bf16 v[248:251], v[248:251], v[132:135], 0\n"
"ds_read_b128 v[40:43], v188 offset:7168\n"
"ds_read_b128 v[44:47], v189 offset:7168\n"
"v_mfma_f32_16x16x32_bf16 v[252:255], v[252:255], v[132:135], 0\n"
"v_xor_b32 v130, v84, v128\n"
"v_xor_b32 v131, v85, v129\n"
"v_mfma_f32_16x16x32_bf16 v[82:85], v[172:175], v[82:85], 0\n"
"ds_read_b64_tr_b16 v[132:133], v182 offset:13632\n"
"ds_read_b64_tr_b16 v[134:135], v183 offset:13632\n"
"v_mfma_f32_16x16x32_bf16 v[244:247], v[140:143], v[128:131], v[244:247]\n"
"s_add_u32 m0, s34, 4096\n"
"ds_read_b64_tr_b16 v[140:141], v182 offset:13576\n"
"buffer_load_dwordx4 v191, s[8:11], s0 offen lds\n"
"v_mfma_f32_16x16x32_bf16 v[248:251], v[112:115], v[128:131], v[248:251]\n"
"s_add_u32 s0, s0, s33\n"
"ds_read_b64_tr_b16 v[142:143], v183 offset:13576\n"
"v_fmamk_f32 v136, v136, 0x3e553b95, v170\n"
"v_mfma_f32_16x16x32_bf16 v[252:255], v[116:119], v[128:131], v[252:255]\n"
"ds_read_b64_tr_b16 v[128:129], v182 offset:13640\n"
"v_fmamk_f32 v137, v137, 0x3e553b95, v170\n"
"v_fmamk_f32 v138, v138, 0x3e553b95, v170\n"
"v_fmamk_f32 v139, v139, 0x3e553b95, v170\n"
"v_exp_f32 v136, v136\n"
"v_exp_f32 v137, v137\n"
"v_exp_f32 v138, v138\n"
"v_exp_f32 v139, v139\n"
"v_fmamk_f32 v120, v120, 0x3e553b95, v170\n"
"v_fmamk_f32 v121, v121, 0x3e553b95, v170\n"
"v_fmamk_f32 v122, v122, 0x3e553b95, v170\n"
"v_fmamk_f32 v123, v123, 0x3e553b95, v170\n"
"ds_read_b64_tr_b16 v[130:131], v183 offset:13640\n"
"v_cvt_pk_bf16_f32 v90, v136, v137\n"
"v_cvt_pk_bf16_f32 v91, v138, v139\n"
"v_exp_f32 v120, v120\n"
"v_exp_f32 v121, v121\n"
"v_exp_f32 v122, v122\n"
"v_exp_f32 v123, v123\n"
"v_and_b32 v136, v90, v176\n"
"v_and_b32 v137, v91, v177\n"
"s_add_u32 m0, s34, 2048\n"
"v_xor_b32 v138, v90, v136\n"
"buffer_load_dwordx4 v190, s[8:11], s1 offen lds\n"
"v_xor_b32 v139, v91, v137\n"
"v_cvt_pk_bf16_f32 v92, v120, v121\n"
"v_cvt_pk_bf16_f32 v93, v122, v123\n"
"v_mfma_f32_16x16x32_bf16 v[248:251], v[98:101], v[136:139], v[248:251]\n"
"s_waitcnt lgkmcnt(9)\n"
"ds_read_b64_tr_b16 a[244:245], v236 offset:8\n"
"v_and_b32 v120, v92, v176\n"
"v_mfma_f32_16x16x32_bf16 v[244:247], v[124:127], v[136:139], v[244:247]\n"
"v_and_b32 v121, v93, v177\n"
"v_xor_b32 v122, v92, v120\n"
"v_mfma_f32_16x16x32_bf16 v[252:255], v[106:109], v[136:139], v[252:255]\n"
"v_xor_b32 v123, v93, v121\n"
"v_cvt_pk_bf16_f32 v224, v224, v225\n"
"v_mfma_f32_16x16x32_bf16 v[90:93], v[172:175], v[90:93], v[82:85]\n"
"s_waitcnt lgkmcnt(1)\n"
"ds_read_b64_tr_b16 a[246:247], v236 offset:6152\n"
"v_cvt_pk_bf16_f32 v225, v226, v227\n"
"v_mfma_f32_16x16x32_bf16 v[244:247], v[132:135], v[120:123], v[244:247]\n"
"ds_read_b64_tr_b16 a[248:249], v235\n"
"v_cvt_pk_bf16_f32 v226, v208, v209\n"
"v_mfma_f32_16x16x32_bf16 v[248:251], v[128:131], v[120:123], v[248:251]\n"
"v_cvt_pk_bf16_f32 v227, v210, v211\n"
"v_rcp_f32 v171, v90\n"
"v_mfma_f32_16x16x32_bf16 v[252:255], v[140:143], v[120:123], v[252:255]\n"
"s_waitcnt vmcnt(11)\n"
"ds_write_b128 v186, v[224:227] offset:33888\n"
"v_mfma_f32_16x16x32_bf16 v[208:211], a[0:3], v[32:35], 0\n"
"v_add3_u32 v200, v200, s50, -2\n"
"v_cmp_lt_u32 vcc, 64, v200\n"
"v_mfma_f32_16x16x32_bf16 v[208:211], a[12:15], v[36:39], v[208:211]\n"
"s_add_u32 m0, s34, 5120\n"
"v_mul_f32 v244, v171, v244\n"
"v_mul_f32 v245, v171, v245\n"
"v_mfma_f32_16x16x32_bf16 v[208:211], a[24:27], v[40:43], v[208:211]\n"
"s_cbranch_vccnz .Lp1%=\n"
".Lr1%=:\n"
"v_mfma_f32_16x16x32_bf16 v[208:211], a[36:39], v[44:47], v[208:211]\n"
"buffer_load_dwordx4 v191, s[8:11], s1 offen lds\n"
"ds_read_b128 v[48:51], v188 offset:8192\n"
"v_mfma_f32_16x16x32_bf16 v[224:227], a[72:75], v[32:35], 0\n"
"s_add_u32 s1, s1, s33\n"
"ds_read_b128 v[52:55], v189 offset:8192\n"
"v_mul_f32 v246, v171, v246\n"
"v_mfma_f32_16x16x32_bf16 v[224:227], a[84:87], v[36:39], v[224:227]\n"
"v_mul_f32 v247, v171, v247\n"
"v_cvt_pk_bf16_f32 v244, v244, v245\n"
"v_mfma_f32_16x16x32_bf16 v[224:227], a[96:99], v[40:43], v[224:227]\n"
"buffer_load_dword v201, off, s[28:31], s41 sc1\n"
"v_cvt_pk_bf16_f32 v245, v246, v247\n"
"v_mfma_f32_16x16x32_bf16 v[224:227], a[108:111], v[44:47], v[224:227]\n"
"v_mul_f32 v248, v171, v248\n"
"v_mul_f32 v249, v171, v249\n"
"buffer_load_dwordx4 v[56:59], v192, s[28:31], s38 offen sc1\n"
"v_mul_f32 v250, v171, v250\n"
"v_mul_f32 v251, v171, v251\n"
"v_cvt_pk_bf16_f32 v246, v248, v249\n"
"v_cvt_pk_bf16_f32 v247, v250, v251\n"
"v_mul_f32 v252, v171, v252\n"
"v_mul_f32 v253, v171, v253\n"
"v_mul_f32 v254, v171, v254\n"
"v_mul_f32 v255, v171, v255\n"
"v_cvt_pk_bf16_f32 v252, v252, v253\n"
"v_cvt_pk_bf16_f32 v253, v254, v255\n"
"v_cvt_pk_bf16_f32 v222, v204, v205\n"
"v_cvt_pk_bf16_f32 v223, v206, v207\n"
"v_mfma_f32_16x16x32_bf16 v[204:207], a[4:7], v[32:35], 0\n"
"s_waitcnt lgkmcnt(1)\n"
"buffer_store_dwordx4 v[244:247], v192, s[28:31], s37 offen sc1\n"
"v_mfma_f32_16x16x32_bf16 v[204:207], a[16:19], v[36:39], v[204:207]\n"
"ds_write_b128 v186, v[220:223] offset:33824\n"
"v_mfma_f32_16x16x32_bf16 v[220:223], a[8:11], v[32:35], 0\n"
"s_waitcnt lgkmcnt(1)\n"
"v_cvt_pk_bf16_f32 v148, v212, v213\n"
"v_cvt_pk_bf16_f32 v149, v214, v215\n"
"v_mfma_f32_16x16x32_bf16 v[220:223], a[20:23], v[36:39], v[220:223]\n"
"s_add_u32 m0, s34, 6144\n"
"buffer_store_dwordx2 v[252:253], v193, s[28:31], s37 offen offset:1024 sc1\n"
"v_mfma_f32_16x16x32_bf16 v[204:207], a[28:31], v[40:43], v[204:207]\n"
"s_add_u32 s37, s37, 6144\n"
"ds_read_b64_tr_b16 a[250:251], v235 offset:6144\n"
"v_cvt_pk_bf16_f32 v150, v216, v217\n"
"v_mfma_f32_16x16x32_bf16 v[220:223], a[32:35], v[40:43], v[220:223]\n"
"s_add_u32 s4, s37, 49152\n"
"v_cvt_pk_bf16_f32 v151, v218, v219\n"
"v_cvt_pk_bf16_f32 v162, v228, v229\n"
"v_mfma_f32_16x16x32_bf16 v[204:207], a[40:43], v[44:47], v[204:207]\n"
"buffer_load_dwordx4 v190, s[8:11], s32 offen lds\n"
"v_cvt_pk_bf16_f32 v163, v230, v231\n"
"v_mfma_f32_16x16x32_bf16 v[220:223], a[44:47], v[44:47], v[220:223]\n"
"ds_read_b128 v[112:115], v180 offset:27008\n"
"ds_read_b128 v[116:119], v181 offset:27008\n"
"v_mfma_f32_16x16x32_bf16 v[208:211], a[48:51], v[48:51], v[208:211]\n"
"ds_read_b64 v[82:83], v180 offset:27072\n"
"v_mfma_f32_16x16x32_bf16 v[204:207], a[52:55], v[48:51], v[204:207]\n"
"ds_read_b64 v[84:85], v181 offset:27072\n"
"s_mov_b64 exec, s[44:45]\n"
"v_add_u32 v181, 0xc0, v181\n"
"s_mov_b64 exec, -1\n"
"v_mfma_f32_16x16x32_bf16 v[220:223], a[56:59], v[48:51], v[220:223]\n"
"buffer_load_dwordx2 v[60:61], v193, s[28:31], s38 offen offset:1024 sc1\n"
"ds_read_b128 v[136:139], v178\n"
"v_mfma_f32_16x16x32_bf16 v[208:211], a[60:63], v[52:55], v[208:211]\n"
"s_add_u32 s38, s38, 6144\n"
"ds_read_b128 v[124:127], v179\n"
"ds_read_b128 v[120:123], v178 offset:13504\n"
"v_mfma_f32_16x16x32_bf16 v[204:207], a[64:67], v[52:55], v[204:207]\n"
"s_waitcnt vmcnt(11) lgkmcnt(7)\n"
"ds_read_b128 v[132:135], v179 offset:13504\n"
"v_mfma_f32_16x16x32_bf16 v[220:223], a[68:71], v[52:55], v[220:223]\n"
"s_barrier\n"
"v_mfma_f32_16x16x32_bf16 v[212:215], a[76:79], v[32:35], 0\n"
"s_waitcnt lgkmcnt(7)\n"
"buffer_load_dwordx4 v[62:65], v192, s[28:31], s38 offen sc1\n"
"ds_read_b128 v[128:131], v178 offset:27008\n"
"v_mfma_f32_16x16x32_bf16 v[112:115], v[112:115], v[152:155], 0\n"
"s_waitcnt lgkmcnt(7)\n"
"ds_read_b128 v[140:143], v179 offset:27008\n"
"ds_read_b64 v[90:91], v178 offset:64\n"
"v_mfma_f32_16x16x32_bf16 v[116:119], v[116:119], v[152:155], 0\n"
"s_waitcnt lgkmcnt(8)\n"
"ds_read_b64 v[92:93], v179 offset:64\n"
"v_cvt_pk_bf16_f32 v208, v208, v209\n"
"v_mfma_f32_16x16x32_bf16 v[112:115], v[80:83], v[164:167], v[112:115]\n"
"s_waitcnt lgkmcnt(8)\n"
"ds_read_b64 v[98:99], v178 offset:13568\n"
"v_cvt_pk_bf16_f32 v209, v210, v211\n"
"v_mfma_f32_16x16x32_bf16 v[116:119], v[84:87], v[166:169], v[116:119]\n"
"s_waitcnt lgkmcnt(8)\n"
"buffer_load_dwordx2 v[66:67], v193, s[28:31], s38 offen offset:1024 sc1\n"
"ds_read_b64 v[100:101], v179 offset:13568\n"
"v_mfma_f32_16x16x32_bf16 v[136:139], v[136:139], v[152:155], 0\n"
"s_waitcnt lgkmcnt(8)\n"
"ds_read_b64 v[106:107], v178 offset:27072\n"
"v_cvt_pk_bf16_f32 v210, v204, v205\n"
"v_mfma_f32_16x16x32_bf16 v[124:127], v[124:127], v[152:155], 0\n"
"s_waitcnt lgkmcnt(8)\n"
"ds_read_b64 v[108:109], v179 offset:27072\n"
"v_cvt_pk_bf16_f32 v211, v206, v207\n"
"v_mfma_f32_16x16x32_bf16 v[120:123], v[120:123], v[152:155], 0\n"
"s_waitcnt lgkmcnt(8)\n"
"ds_write_b128 v186, v[208:211] offset:47264\n"
"v_mfma_f32_16x16x32_bf16 v[132:135], v[132:135], v[152:155], 0\n"
"s_waitcnt lgkmcnt(7)\n"
"v_cvt_pk_bf16_f32 v220, v220, v221\n"
"v_cvt_pk_bf16_f32 v221, v222, v223\n"
"v_mfma_f32_16x16x32_bf16 v[128:131], v[128:131], v[152:155], 0\n"
"s_waitcnt lgkmcnt(6)\n"
"v_max_f32 v112, v112, v116\n"
"v_max_f32 v113, v113, v117\n"
"v_mfma_f32_16x16x32_bf16 v[140:143], v[140:143], v[152:155], 0\n"
"s_waitcnt lgkmcnt(5)\n"
"ds_read_b64_tr_b16 v[248:249], v184 offset:27136\n"
"ds_read_b64_tr_b16 v[250:251], v185 offset:27136\n"
"v_mfma_f32_16x16x32_bf16 v[136:139], v[88:91], v[164:167], v[136:139]\n"
"s_waitcnt lgkmcnt(6)\n"
"ds_read_b64_tr_b16 v[244:245], v184 offset:27144\n"
"v_max_f32 v114, v114, v118\n"
"v_mfma_f32_16x16x32_bf16 v[124:127], v[92:95], v[166:169], v[124:127]\n"
"s_waitcnt lgkmcnt(6)\n"
"ds_read_b64_tr_b16 v[246:247], v185 offset:27144\n"
"v_max_f32 v115, v115, v119\n"
"v_mfma_f32_16x16x32_bf16 v[120:123], v[96:99], v[164:167], v[120:123]\n"
"s_waitcnt lgkmcnt(6)\n"
"ds_read_b64_tr_b16 v[252:253], v184 offset:27080\n"
"v_max_f32 v170, v112, v113\n"
"v_mfma_f32_16x16x32_bf16 v[132:135], v[100:103], v[166:169], v[132:135]\n"
"s_waitcnt lgkmcnt(6)\n"
"ds_read_b64_tr_b16 v[254:255], v185 offset:27080\n"
"v_max_f32 v171, v114, v115\n"
"v_mfma_f32_16x16x32_bf16 v[128:131], v[104:107], v[164:167], v[128:131]\n"
"s_add_u32 m0, s79, 130176\n"
"ds_read_b64_tr_b16 v[116:117], v182 offset:128\n"
"buffer_load_dwordx4 v192, s[16:19], s43 offen lds\n"
"v_mfma_f32_16x16x32_bf16 v[140:143], v[108:111], v[166:169], v[140:143]\n"
"s_add_u32 s38, s38, 6144\n"
"ds_read_b64_tr_b16 v[118:119], v183 offset:128\n"
"v_max_f32 v136, v136, v124\n"
"v_mfma_f32_16x16x32_bf16 v[216:219], a[80:83], v[32:35], 0\n"
"ds_read_b64_tr_b16 v[98:99], v182 offset:13640\n"
"v_max_f32 v137, v137, v125\n"
"v_mfma_f32_16x16x32_bf16 v[212:215], a[88:91], v[36:39], v[212:215]\n"
"ds_read_b64_tr_b16 v[124:125], v182 offset:136\n"
"v_max_f32 v138, v138, v126\n"
"v_mfma_f32_16x16x32_bf16 v[216:219], a[92:95], v[36:39], v[216:219]\n"
"ds_read_b64_tr_b16 v[100:101], v183 offset:13640\n"
"v_max_f32 v139, v139, v127\n"
"v_mfma_f32_16x16x32_bf16 v[212:215], a[100:103], v[40:43], v[212:215]\n"
"ds_read_b64_tr_b16 v[126:127], v183 offset:136\n"
"v_max3_f32 v170, v170, v136, v137\n"
"v_mfma_f32_16x16x32_bf16 v[216:219], a[104:107], v[40:43], v[216:219]\n"
"ds_read_b64_tr_b16 v[106:107], v182 offset:13576\n"
"v_max3_f32 v171, v171, v138, v139\n"
"v_mfma_f32_16x16x32_bf16 v[212:215], a[112:115], v[44:47], v[212:215]\n"
"ds_read_b64_tr_b16 v[108:109], v183 offset:13576\n"
"v_max_f32 v120, v120, v132\n"
"v_mfma_f32_16x16x32_bf16 v[216:219], a[116:119], v[44:47], v[216:219]\n"
"ds_read_b64_tr_b16 a[252:253], v236 offset:12288\n"
"v_max_f32 v121, v121, v133\n"
"v_mfma_f32_16x16x32_bf16 v[224:227], a[120:123], v[48:51], v[224:227]\n"
"v_max_f32 v122, v122, v134\n"
"v_max_f32 v123, v123, v135\n"
"v_mfma_f32_16x16x32_bf16 v[212:215], a[124:127], v[48:51], v[212:215]\n"
"s_add_u32 m0, s79, 130176\n"
"ds_read_b64_tr_b16 v[132:133], v182 offset:72\n"
"buffer_load_dwordx4 v192, s[16:19], s43 offen offset:1024 lds\n"
"v_mfma_f32_16x16x32_bf16 v[216:219], a[128:131], v[48:51], v[216:219]\n"
"ds_read_b64_tr_b16 v[134:135], v183 offset:72\n"
"v_max3_f32 v170, v170, v120, v121\n"
"v_mfma_f32_16x16x32_bf16 v[224:227], a[132:135], v[52:55], v[224:227]\n"
"ds_read_b64_tr_b16 a[254:255], v236 offset:18432\n"
"v_max3_f32 v171, v171, v122, v123\n"
"v_mfma_f32_16x16x32_bf16 v[212:215], a[136:139], v[52:55], v[212:215]\n"
"v_max_f32 v128, v128, v140\n"
"v_max_f32 v129, v129, v141\n"
"v_mfma_f32_16x16x32_bf16 v[216:219], a[140:143], v[52:55], v[216:219]\n"
"ds_read_b64_tr_b16 v[140:141], v182 offset:13632\n"
"v_max_f32 v130, v130, v142\n"
"v_mfma_f32_16x16x32_bf16 v[228:231], a[144:147], v[32:35], 0\n"
"v_max_f32 v131, v131, v143\n"
"v_max3_f32 v170, v170, v128, v129\n"
"v_mfma_f32_16x16x32_bf16 v[204:207], a[148:151], v[32:35], 0\n"
"ds_read_b64_tr_b16 v[142:143], v183 offset:13632\n"
"v_max3_f32 v171, v171, v130, v131\n"
"v_mfma_f32_16x16x32_bf16 v[208:211], a[152:155], v[32:35], 0\n"
"ds_read_b128 v[32:35], v188\n"
"v_max_f32 v170, v170, v171\n"
"v_mfma_f32_16x16x32_bf16 v[228:231], a[156:159], v[36:39], v[228:231]\n"
".Lic3%=:\n"
"s_cbranch_execz .Lib0%=\n"
"v_mov_b32 v171, v170\n"
"v_cvt_pk_bf16_f32 v224, v224, v225\n"
"v_mfma_f32_16x16x32_bf16 v[204:207], a[160:163], v[36:39], v[204:207]\n"
"buffer_store_dwordx4 v[68:71], v192, s[28:31], s4 offen sc1\n"
"v_mfma_f32_16x16x32_bf16 v[208:211], a[164:167], v[36:39], v[208:211]\n"
"s_add_u32 m0, s79, 130176\n"
"ds_read_b128 v[36:39], v189\n"
"ds_read_b64_tr_b16 v[0:1], v236 offset:12296\n"
"v_mfma_f32_16x16x32_bf16 v[228:231], a[168:171], v[40:43], v[228:231]\n"
"v_permlane16_swap_b32 v170, v171\n"
"v_mfma_f32_16x16x32_bf16 v[204:207], a[172:175], v[40:43], v[204:207]\n"
"buffer_load_dwordx4 v192, s[16:19], s43 offen offset:2048 lds\n"
"v_max_f32 v170, v170, v171\n"
"v_mfma_f32_16x16x32_bf16 v[208:211], a[176:179], v[40:43], v[208:211]\n"
"s_add_u32 s43, s80, 49152\n"
"v_mov_b32 v171, v170\n"
"v_cvt_pk_bf16_f32 v225, v226, v227\n"
"v_mfma_f32_16x16x32_bf16 v[228:231], a[180:183], v[44:47], v[228:231]\n"
"v_permlane32_swap_b32 v170, v171\n"
"v_mfma_f32_16x16x32_bf16 v[204:207], a[184:187], v[44:47], v[204:207]\n"
"v_max_f32 v170, v170, v171\n"
"v_mul_f32 v170, 0xbe553b95, v170\n"
"v_mfma_f32_16x16x32_bf16 v[208:211], a[188:191], v[44:47], v[208:211]\n"
"v_fmamk_f32 v112, v112, 0x3e553b95, v170\n"
"v_fmamk_f32 v113, v113, 0x3e553b95, v170\n"
"v_mfma_f32_16x16x32_bf16 v[228:231], a[192:195], v[48:51], v[228:231]\n"
"v_fmamk_f32 v114, v114, 0x3e553b95, v170\n"
"v_fmamk_f32 v115, v115, 0x3e553b95, v170\n"
"v_mfma_f32_16x16x32_bf16 v[204:207], a[196:199], v[48:51], v[204:207]\n"
"v_exp_f32 v112, v112\n"
"v_mfma_f32_16x16x32_bf16 v[208:211], a[200:203], v[48:51], v[208:211]\n"
"v_exp_f32 v113, v113\n"
"v_mfma_f32_16x16x32_bf16 v[228:231], a[204:207], v[52:55], v[228:231]\n"
"v_exp_f32 v114, v114\n"
"v_mfma_f32_16x16x32_bf16 v[204:207], a[208:211], v[52:55], v[204:207]\n"
"v_exp_f32 v115, v115\n"
"v_mfma_f32_16x16x32_bf16 v[208:211], a[212:215], v[52:55], v[208:211]\n"
"s_add_u32 m0, s34, 9216\n"
"v_cvt_pk_bf16_f32 v82, v112, v113\n"
"buffer_load_dwordx4 v191, s[8:11], s32 offen lds\n"
"s_add_u32 s32, s32, s33\n"
"v_cvt_pk_bf16_f32 v83, v114, v115\n"
"v_fmamk_f32 v136, v136, 0x3e553b95, v170\n"
"v_fmamk_f32 v137, v137, 0x3e553b95, v170\n"
"v_fmamk_f32 v138, v138, 0x3e553b95, v170\n"
"v_fmamk_f32 v139, v139, 0x3e553b95, v170\n"
"v_and_b32 v112, v82, v176\n"
"v_and_b32 v113, v83, v177\n"
"v_exp_f32 v136, v136\n"
"s_waitcnt vmcnt(15)\n"
"ds_read_b128 v[40:43], v188 offset:1024\n"
"ds_read_b128 v[44:47], v189 offset:1024\n"
"v_exp_f32 v137, v137\n"
"v_exp_f32 v138, v138\n"
"v_exp_f32 v139, v139\n"
"v_xor_b32 v114, v82, v112\n"
"v_xor_b32 v115, v83, v113\n"
"v_cvt_pk_bf16_f32 v84, v136, v137\n"
"v_cvt_pk_bf16_f32 v85, v138, v139\n"
"v_and_b32 v136, v84, v176\n"
"v_and_b32 v137, v85, v177\n"
"v_xor_b32 v138, v84, v136\n"
"v_xor_b32 v139, v85, v137\n"
"v_mfma_f32_16x16x32_bf16 v[82:85], v[172:175], v[82:85], 0\n"
"s_waitcnt lgkmcnt(15)\n"
"ds_read_b64_tr_b16 v[2:3], v236 offset:18440\n"
"v_fmamk_f32 v120, v120, 0x3e553b95, v170\n"
"v_mfma_f32_16x16x32_bf16 v[248:251], v[248:251], v[112:115], 0\n"
"v_fmamk_f32 v121, v121, 0x3e553b95, v170\n"
"v_fmamk_f32 v122, v122, 0x3e553b95, v170\n"
"v_mfma_f32_16x16x32_bf16 v[244:247], v[244:247], v[112:115], 0\n"
"s_waitcnt lgkmcnt(4)\n"
"v_fmamk_f32 v123, v123, 0x3e553b95, v170\n"
"v_fmamk_f32 v128, v128, 0x3e553b95, v170\n"
"v_mfma_f32_16x16x32_bf16 v[252:255], v[252:255], v[112:115], 0\n"
"s_add_u32 m0, s34, 7168\n"
"ds_read_b64_tr_b16 v[112:113], v182 offset:27136\n"
"buffer_load_dwordx4 v190, s[8:11], s0 offen lds\n"
"v_mfma_f32_16x16x32_bf16 v[248:251], v[116:119], v[136:139], v[248:251]\n"
"ds_read_b64_tr_b16 v[114:115], v183 offset:27136\n"
"ds_read_b64_tr_b16 v[116:117], v182 offset:27080\n"
"v_mfma_f32_16x16x32_bf16 v[244:247], v[124:127], v[136:139], v[244:247]\n"
"ds_read_b64_tr_b16 v[118:119], v183 offset:27080\n"
"v_fmamk_f32 v129, v129, 0x3e553b95, v170\n"
"v_mfma_f32_16x16x32_bf16 v[252:255], v[132:135], v[136:139], v[252:255]\n"
"ds_read_b64_tr_b16 v[136:137], v182 offset:27144\n"
"v_exp_f32 v120, v120\n"
"v_exp_f32 v121, v121\n"
"v_exp_f32 v122, v122\n"
"v_exp_f32 v123, v123\n"
"v_fmamk_f32 v130, v130, 0x3e553b95, v170\n"
"v_fmamk_f32 v131, v131, 0x3e553b95, v170\n"
"ds_read_b64_tr_b16 v[138:139], v183 offset:27144\n"
"v_cvt_pk_bf16_f32 v90, v120, v121\n"
"v_cvt_pk_bf16_f32 v91, v122, v123\n"
"v_exp_f32 v128, v128\n"
"v_exp_f32 v129, v129\n"
"v_exp_f32 v130, v130\n"
"v_exp_f32 v131, v131\n"
"v_and_b32 v120, v90, v176\n"
"v_and_b32 v121, v91, v177\n"
"v_xor_b32 v122, v90, v120\n"
"s_add_u32 m0, s34, 10240\n"
"v_xor_b32 v123, v91, v121\n"
"v_cvt_pk_bf16_f32 v92, v128, v129\n"
"v_cvt_pk_bf16_f32 v93, v130, v131\n"
"v_mfma_f32_16x16x32_bf16 v[248:251], v[140:143], v[120:123], v[248:251]\n"
"buffer_load_dwordx4 v191, s[8:11], s0 offen lds\n"
"v_and_b32 v128, v92, v176\n"
"v_mfma_f32_16x16x32_bf16 v[244:247], v[98:101], v[120:123], v[244:247]\n"
"s_add_u32 s0, s0, s33\n"
"v_and_b32 v129, v93, v177\n"
"v_xor_b32 v130, v92, v128\n"
"v_mfma_f32_16x16x32_bf16 v[252:255], v[106:109], v[120:123], v[252:255]\n"
"v_xor_b32 v131, v93, v129\n"
"v_cvt_pk_bf16_f32 v226, v212, v213\n"
"v_mfma_f32_16x16x32_bf16 v[90:93], v[172:175], v[90:93], v[82:85]\n"
"s_waitcnt vmcnt(13) lgkmcnt(0)\n"
"ds_read_b64_tr_b16 v[4:5], v235 offset:12288\n"
"v_cvt_pk_bf16_f32 v227, v214, v215\n"
"v_mfma_f32_16x16x32_bf16 v[248:251], v[112:115], v[128:131], v[248:251]\n"
"v_readfirstlane_b32 s6, v201\n"
"v_cvt_pk_bf16_f32 v222, v216, v217\n"
"v_mfma_f32_16x16x32_bf16 v[244:247], v[136:139], v[128:131], v[244:247]\n"
"s_and_b32 s6, s6, 0xffffff00\n"
"v_or_b32 v201, 4, s6\n"
"v_cvt_pk_bf16_f32 v223, v218, v219\n"
"v_mfma_f32_16x16x32_bf16 v[252:255], v[116:119], v[128:131], v[252:255]\n"
"s_mov_b64 exec, 1\n"
"buffer_store_dword v201, off, s[28:31], s40 sc1\n"
"s_mov_b64 exec, -1\n"
"v_mfma_f32_16x16x32_bf16 v[212:215], a[0:3], v[32:35], 0\n"
"ds_read_b128 v[48:51], v188 offset:2048\n"
"ds_read_b128 v[52:55], v189 offset:2048\n"
"v_mfma_f32_16x16x32_bf16 v[216:219], a[4:7], v[32:35], 0\n"
"ds_write_b128 v186, v[220:223] offset:47328\n"
"v_mfma_f32_16x16x32_bf16 v[220:223], a[8:11], v[32:35], 0\n"
"s_waitcnt vmcnt(12)\n"
"v_rcp_f32 v171, v90\n"
"v_mfma_f32_16x16x32_bf16 v[212:215], a[12:15], v[36:39], v[212:215]\n"
"s_add_u32 m0, s34, 8192\n"
"v_or_b32 v201, 5, s6\n"
"v_mul_f32 v248, v171, v248\n"
"v_mfma_f32_16x16x32_bf16 v[216:219], a[16:19], v[36:39], v[216:219]\n"
"buffer_load_dwordx4 v190, s[8:11], s1 offen lds\n"
"ds_write_b128 v186, v[224:227] offset:47392\n"
"v_mfma_f32_16x16x32_bf16 v[220:223], a[20:23], v[36:39], v[220:223]\n"
"v_mul_f32 v249, v171, v249\n"
"v_mul_f32 v250, v171, v250\n"
"v_mfma_f32_16x16x32_bf16 v[212:215], a[24:27], v[40:43], v[212:215]\n"
"s_mov_b64 exec, 1\n"
"buffer_store_dword v201, off, s[28:31], s40 sc1\n"
"s_mov_b64 exec, -1\n"
"v_mfma_f32_16x16x32_bf16 v[216:219], a[28:31], v[40:43], v[216:219]\n"
"v_mul_f32 v251, v171, v251\n"
"v_cvt_pk_bf16_f32 v248, v248, v249\n"
"v_mfma_f32_16x16x32_bf16 v[220:223], a[32:35], v[40:43], v[220:223]\n"
"v_cvt_pk_bf16_f32 v249, v250, v251\n"
"v_mul_f32 v244, v171, v244\n"
"v_mfma_f32_16x16x32_bf16 v[212:215], a[36:39], v[44:47], v[212:215]\n"
"s_waitcnt lgkmcnt(3)\n"
"v_mul_f32 v245, v171, v245\n"
"v_mul_f32 v246, v171, v246\n"
"v_mfma_f32_16x16x32_bf16 v[216:219], a[40:43], v[44:47], v[216:219]\n"
"s_waitcnt vmcnt(12)\n"
"v_mul_f32 v247, v171, v247\n"
"v_cvt_pk_bf16_f32 v250, v244, v245\n"
"v_mfma_f32_16x16x32_bf16 v[220:223], a[44:47], v[44:47], v[220:223]\n"
"v_cvt_pk_bf16_f32 v251, v246, v247\n"
"v_mul_f32 v252, v171, v252\n"
"v_mfma_f32_16x16x32_bf16 v[212:215], a[48:51], v[48:51], v[212:215]\n"
"s_waitcnt lgkmcnt(2)\n"
"buffer_store_dwordx4 v[248:251], v192, s[28:31], s37 offen sc1\n"
"v_mfma_f32_16x16x32_bf16 v[216:219], a[52:55], v[48:51], v[216:219]\n"
"s_add_u32 m0, s34, 11264\n"
"v_mul_f32 v253, v171, v253\n"
"v_mul_f32 v254, v171, v254\n"
"v_mfma_f32_16x16x32_bf16 v[220:223], a[56:59], v[48:51], v[220:223]\n"
"v_mul_f32 v255, v171, v255\n"
"v_cvt_pk_bf16_f32 v252, v252, v253\n"
"v_mfma_f32_16x16x32_bf16 v[212:215], a[60:63], v[52:55], v[212:215]\n"
"buffer_load_dwordx4 v191, s[8:11], s1 offen lds\n"
"v_cvt_pk_bf16_f32 v253, v254, v255\n"
"v_mfma_f32_16x16x32_bf16 v[216:219], a[64:67], v[52:55], v[216:219]\n"
"s_add_u32 s1, s1, s33\n"
"ds_read_b64_tr_b16 v[6:7], v235 offset:18432\n"
"v_cvt_pk_bf16_f32 v152, v228, v229\n"
"v_mfma_f32_16x16x32_bf16 v[220:223], a[68:71], v[52:55], v[220:223]\n"
"v_cvt_pk_bf16_f32 v153, v230, v231\n"
"v_cvt_pk_bf16_f32 v154, v204, v205\n"
"v_mfma_f32_16x16x32_bf16 v[224:227], a[72:75], v[32:35], 0\n"
"v_cvt_pk_bf16_f32 v155, v206, v207\n"
"v_cvt_pk_bf16_f32 v166, v208, v209\n"
"v_mfma_f32_16x16x32_bf16 v[228:231], a[76:79], v[32:35], 0\n"
"buffer_store_dwordx2 v[252:253], v193, s[28:31], s37 offen offset:1024 sc1\n"
"v_mfma_f32_16x16x32_bf16 v[204:207], a[80:83], v[32:35], 0\n"
"s_add_u32 s37, s37, 6144\n"
"v_cvt_pk_bf16_f32 v167, v210, v211\n"
"s_mov_b64 exec, s[46:47]\n"
"v_add_u32 v185, 0xc0, v185\n"
"s_mov_b64 exec, -1\n"
"v_mfma_f32_16x16x32_bf16 v[224:227], a[84:87], v[36:39], v[224:227]\n"
"s_add_u32 s4, s37, 43008\n"
"ds_read_b128 v[124:127], v178\n"
"ds_read_b128 v[132:135], v179\n"
"v_mfma_f32_16x16x32_bf16 v[228:231], a[88:91], v[36:39], v[228:231]\n"
"ds_read_b64 v[82:83], v178 offset:64\n"
"v_cvt_pk_bf16_f32 v212, v212, v213\n"
"v_mfma_f32_16x16x32_bf16 v[204:207], a[92:95], v[36:39], v[204:207]\n"
"ds_read_b64 v[84:85], v179 offset:64\n"
"v_cvt_pk_bf16_f32 v213, v214, v215\n"
"v_mfma_f32_16x16x32_bf16 v[224:227], a[96:99], v[40:43], v[224:227]\n"
"s_add_u32 m0, s34, 0\n"
"ds_read_b128 v[120:123], v178 offset:13504\n"
"buffer_load_dwordx4 v190, s[8:11], s32 offen lds\n"
"v_mfma_f32_16x16x32_bf16 v[228:231], a[100:103], v[40:43], v[228:231]\n"
"ds_read_b128 v[140:143], v179 offset:13504\n"
"v_cvt_pk_bf16_f32 v214, v216, v217\n"
"v_mfma_f32_16x16x32_bf16 v[204:207], a[104:107], v[40:43], v[204:207]\n"
"ds_read_b64 v[90:91], v178 offset:13568\n"
"v_cvt_pk_bf16_f32 v215, v218, v219\n"
"v_mfma_f32_16x16x32_bf16 v[224:227], a[108:111], v[44:47], v[224:227]\n"
"ds_read_b64 v[92:93], v179 offset:13568\n"
"v_cvt_pk_bf16_f32 v220, v220, v221\n"
"v_mfma_f32_16x16x32_bf16 v[228:231], a[112:115], v[44:47], v[228:231]\n"
"ds_read_b128 v[128:131], v178 offset:27008\n"
"v_cvt_pk_bf16_f32 v221, v222, v223\n"
"v_mfma_f32_16x16x32_bf16 v[204:207], a[116:119], v[44:47], v[204:207]\n"
"s_waitcnt lgkmcnt(8)\n"
"ds_write_b128 v202, v[56:59] offset:55296\n"
"v_mfma_f32_16x16x32_bf16 v[124:127], v[124:127], v[144:147], 0\n"
"s_waitcnt lgkmcnt(7)\n"
"v_mfma_f32_16x16x32_bf16 v[132:135], v[132:135], v[144:147], 0\n"
"s_waitcnt lgkmcnt(6)\n"
"ds_read_b128 v[112:115], v179 offset:27008\n"
"v_mfma_f32_16x16x32_bf16 v[124:127], v[80:83], v[156:159], v[124:127]\n"
"s_waitcnt lgkmcnt(6)\n"
"ds_read_b64 v[98:99], v178 offset:27072\n"
"ds_read_b64 v[100:101], v179 offset:27072\n"
"v_mfma_f32_16x16x32_bf16 v[132:135], v[84:87], v[158:161], v[132:135]\n"
"s_waitcnt lgkmcnt(7)\n"
"ds_read_b64_tr_b16 v[244:245], v182 offset:128\n"
"s_add_u32 m0, s34, 3072\n"
"v_mfma_f32_16x16x32_bf16 v[120:123], v[120:123], v[144:147], 0\n"
"s_waitcnt lgkmcnt(7)\n"
"buffer_load_dwordx4 v191, s[8:11], s32 offen lds\n"
"ds_read_b64_tr_b16 v[246:247], v183 offset:128\n"
"v_mfma_f32_16x16x32_bf16 v[140:143], v[140:143], v[144:147], 0\n"
"s_waitcnt lgkmcnt(7)\n"
"ds_read_b64_tr_b16 v[248:249], v182 offset:136\n"
"v_max_f32 v124, v124, v132\n"
"v_mfma_f32_16x16x32_bf16 v[120:123], v[88:91], v[156:159], v[120:123]\n"
"s_waitcnt lgkmcnt(7)\n"
"ds_read_b64_tr_b16 v[250:251], v183 offset:136\n"
"v_max_f32 v125, v125, v133\n"
"v_mfma_f32_16x16x32_bf16 v[140:143], v[92:95], v[158:161], v[140:143]\n"
"s_add_u32 s32, s32, s33\n"
"ds_read_b64_tr_b16 v[252:253], v182 offset:72\n"
"v_max_f32 v126, v126, v134\n"
"v_mfma_f32_16x16x32_bf16 v[128:131], v[128:131], v[144:147], 0\n"
"ds_read_b64_tr_b16 v[254:255], v183 offset:72\n"
"v_max_f32 v127, v127, v135\n"
"v_mfma_f32_16x16x32_bf16 v[224:227], a[120:123], v[48:51], v[224:227]\n"
"s_waitcnt lgkmcnt(8)\n"
"ds_write_b64 v203, v[60:61] offset:55296\n"
"v_mfma_f32_16x16x32_bf16 v[112:115], v[112:115], v[144:147], 0\n"
"s_waitcnt lgkmcnt(8)\n"
"v_max_f32 v170, v124, v125\n"
"v_max_f32 v171, v126, v127\n"
"v_mfma_f32_16x16x32_bf16 v[128:131], v[96:99], v[156:159], v[128:131]\n"
"s_waitcnt lgkmcnt(7)\n"
"ds_read_b64_tr_b16 v[132:133], v182 offset:13632\n"
"ds_read_b64_tr_b16 v[134:135], v183 offset:13632\n"
"v_mfma_f32_16x16x32_bf16 v[112:115], v[100:103], v[158:161], v[112:115]\n"
"ds_read_b64_tr_b16 v[98:99], v182 offset:27144\n"
"v_max_f32 v120, v120, v140\n"
"v_mfma_f32_16x16x32_bf16 v[228:231], a[124:127], v[48:51], v[228:231]\n"
"ds_read_b64_tr_b16 v[100:101], v183 offset:27144\n"
"v_max_f32 v121, v121, v141\n"
"v_mfma_f32_16x16x32_bf16 v[204:207], a[128:131], v[48:51], v[204:207]\n"
"s_add_u32 m0, s34, 1024\n"
"ds_read_b64_tr_b16 v[140:141], v182 offset:13640\n"
"buffer_load_dwordx4 v190, s[8:11], s0 offen lds\n"
"v_mfma_f32_16x16x32_bf16 v[224:227], a[132:135], v[52:55], v[224:227]\n"
"s_waitcnt vmcnt(12)\n"
"v_max_f32 v122, v122, v142\n"
"v_max_f32 v123, v123, v143\n"
"v_mfma_f32_16x16x32_bf16 v[228:231], a[136:139], v[52:55], v[228:231]\n"
"s_barrier\n"
"v_mfma_f32_16x16x32_bf16 v[204:207], a[140:143], v[52:55], v[204:207]\n"
"ds_write_b128 v186, v[212:215] offset:60768\n"
"v_mfma_f32_16x16x32_bf16 v[208:211], a[144:147], v[32:35], 0\n"
"v_max3_f32 v170, v170, v120, v121\n"
"v_max3_f32 v171, v171, v122, v123\n"
"v_mfma_f32_16x16x32_bf16 v[216:219], a[148:151], v[32:35], 0\n"
"v_max_f32 v128, v128, v112\n"
"v_max_f32 v129, v129, v113\n"
"v_mfma_f32_16x16x32_bf16 v[212:215], a[152:155], v[32:35], 0\n"
"ds_read_b128 v[136:139], v180\n"
"ds_read_b128 v[116:119], v181\n"
"v_mfma_f32_16x16x32_bf16 v[208:211], a[156:159], v[36:39], v[208:211]\n"
"ds_read_b64 v[106:107], v180 offset:64\n"
"v_max_f32 v130, v130, v114\n"
"v_mfma_f32_16x16x32_bf16 v[216:219], a[160:163], v[36:39], v[216:219]\n"
"ds_read_b64 v[108:109], v181 offset:64\n"
"v_max_f32 v131, v131, v115\n"
"v_mfma_f32_16x16x32_bf16 v[212:215], a[164:167], v[36:39], v[212:215]\n"
"ds_read_b64_tr_b16 v[8:9], v236\n"
"v_max3_f32 v170, v170, v128, v129\n"
"v_mfma_f32_16x16x32_bf16 v[208:211], a[168:171], v[40:43], v[208:211]\n"
"s_add_u32 m0, s79, 142464\n"
"v_max3_f32 v171, v171, v130, v131\n"
"v_cvt_pk_bf16_f32 v224, v224, v225\n"
"v_mfma_f32_16x16x32_bf16 v[216:219], a[172:175], v[40:43], v[216:219]\n"
"buffer_load_dwordx4 v192, s[16:19], s43 offen lds\n"
"ds_read_b64_tr_b16 v[10:11], v236 offset:6144\n"
"v_mfma_f32_16x16x32_bf16 v[212:215], a[176:179], v[40:43], v[212:215]\n"
"v_cvt_pk_bf16_f32 v225, v226, v227\n"
"v_cvt_pk_bf16_f32 v226, v228, v229\n"
"v_mfma_f32_16x16x32_bf16 v[208:211], a[180:183], v[44:47], v[208:211]\n"
"ds_read_b64_tr_b16 v[142:143], v183 offset:13640\n"
"v_cvt_pk_bf16_f32 v227, v230, v231\n"
"v_mfma_f32_16x16x32_bf16 v[216:219], a[184:187], v[44:47], v[216:219]\n"
"s_waitcnt lgkmcnt(6)\n"
"ds_read_b64_tr_b16 v[112:113], v182 offset:13576\n"
"v_cvt_pk_bf16_f32 v222, v204, v205\n"
"v_mfma_f32_16x16x32_bf16 v[136:139], v[136:139], v[144:147], 0\n"
"s_waitcnt lgkmcnt(6)\n"
"ds_read_b64_tr_b16 v[114:115], v183 offset:13576\n"
"v_cvt_pk_bf16_f32 v223, v206, v207\n"
"v_mfma_f32_16x16x32_bf16 v[116:119], v[116:119], v[144:147], 0\n"
"s_waitcnt lgkmcnt(6)\n"
"ds_write_b128 v186, v[220:223] offset:60832\n"
"v_mfma_f32_16x16x32_bf16 v[136:139], v[104:107], v[156:159], v[136:139]\n"
"s_waitcnt lgkmcnt(6)\n"
"v_mfma_f32_16x16x32_bf16 v[116:119], v[108:111], v[158:161], v[116:119]\n"
"buffer_store_dwordx2 v[68:69], v193, s[28:31], s4 offen offset:1024 sc1\n"
"v_mfma_f32_16x16x32_bf16 v[212:215], a[188:191], v[44:47], v[212:215]\n"
"ds_read_b64_tr_b16 v[106:107], v182 offset:27080\n"
"ds_write_b128 v186, v[224:227] offset:60896\n"
"v_mfma_f32_16x16x32_bf16 v[208:211], a[192:195], v[48:51], v[208:211]\n"
"s_waitcnt vmcnt(13)\n"
"s_add_u32 m0, s79, 142464\n"
"v_mfma_f32_16x16x32_bf16 v[216:219], a[196:199], v[48:51], v[216:219]\n"
"buffer_load_dwordx4 v192, s[16:19], s43 offen offset:1024 lds\n"
"ds_read_b128 v[32:35], v188 offset:6144\n"
"v_mfma_f32_16x16x32_bf16 v[212:215], a[200:203], v[48:51], v[212:215]\n"
"ds_read_b128 v[36:39], v189 offset:6144\n"
"ds_read_b64_tr_b16 v[108:109], v183 offset:27080\n"
"v_mfma_f32_16x16x32_bf16 v[208:211], a[204:207], v[52:55], v[208:211]\n"
"ds_read_b64_tr_b16 v[12:13], v236 offset:8\n"
"v_max_f32 v136, v136, v116\n"
"v_mfma_f32_16x16x32_bf16 v[216:219], a[208:211], v[52:55], v[216:219]\n"
"v_max_f32 v137, v137, v117\n"
"v_max_f32 v138, v138, v118\n"
"v_mfma_f32_16x16x32_bf16 v[212:215], a[212:215], v[52:55], v[212:215]\n"
"v_max_f32 v139, v139, v119\n"
"v_max3_f32 v170, v170, v136, v137\n"
"v_max3_f32 v171, v171, v138, v139\n"
"v_max_f32 v170, v170, v171\n"
"v_mov_b32 v171, v170\n"
"ds_read_b64_tr_b16 v[116:117], v182 offset:27136\n"
"ds_read_b64_tr_b16 v[118:119], v183 offset:27136\n"
"ds_read_b64_tr_b16 v[14:15], v236 offset:6152\n"
"v_permlane16_swap_b32 v170, v171\n"
"v_max_f32 v170, v170, v171\n"
"v_mov_b32 v171, v170\n"
"v_cvt_pk_bf16_f32 v144, v208, v209\n"
"v_cvt_pk_bf16_f32 v145, v210, v211\n"
"v_permlane32_swap_b32 v170, v171\n"
"v_max_f32 v170, v170, v171\n"
"v_mul_f32 v170, 0xbe553b95, v170\n"
"v_fmamk_f32 v124, v124, 0x3e553b95, v170\n"
"v_fmamk_f32 v125, v125, 0x3e553b95, v170\n"
"v_fmamk_f32 v126, v126, 0x3e553b95, v170\n"
"v_fmamk_f32 v127, v127, 0x3e553b95, v170\n"
"v_exp_f32 v124, v124\n"
"v_exp_f32 v125, v125\n"
"v_exp_f32 v126, v126\n"
"v_exp_f32 v127, v127\n"
"v_cvt_pk_bf16_f32 v82, v124, v125\n"
"v_cvt_pk_bf16_f32 v83, v126, v127\n"
"v_and_b32 v124, v82, v176\n"
"v_and_b32 v125, v83, v177\n"
"v_xor_b32 v126, v82, v124\n"
"v_xor_b32 v127, v83, v125\n"
"v_fmamk_f32 v120, v120, 0x3e553b95, v170\n"
"v_fmamk_f32 v121, v121, 0x3e553b95, v170\n"
"v_mfma_f32_16x16x32_bf16 v[244:247], v[244:247], v[124:127], 0\n"
"s_waitcnt lgkmcnt(5)\n"
"ds_read_b64_tr_b16 v[16:17], v235\n"
"v_fmamk_f32 v122, v122, 0x3e553b95, v170\n"
"v_mfma_f32_16x16x32_bf16 v[228:231], a[0:3], v[32:35], 0\n"
"s_add_u32 m0, s79, 142464\n"
"ds_read_b64_tr_b16 v[18:19], v235 offset:6144\n"
"buffer_load_dwordx4 v192, s[16:19], s43 offen offset:2048 lds\n"
"v_mfma_f32_16x16x32_bf16 v[248:251], v[248:251], v[124:127], 0\n"
"v_fmamk_f32 v123, v123, 0x3e553b95, v170\n"
"v_fmamk_f32 v128, v128, 0x3e553b95, v170\n"
"v_mfma_f32_16x16x32_bf16 v[204:207], a[4:7], v[32:35], 0\n"
"v_exp_f32 v120, v120\n"
"v_mfma_f32_16x16x32_bf16 v[252:255], v[252:255], v[124:127], 0\n"
"ds_read_b64_tr_b16 v[124:125], v184 offset:128\n"
"ds_read_b64_tr_b16 v[126:127], v185 offset:128\n"
"v_mfma_f32_16x16x32_bf16 v[220:223], a[8:11], v[32:35], 0\n"
"s_waitcnt vmcnt(13)\n"
"v_exp_f32 v121, v121\n"
"v_mfma_f32_16x16x32_bf16 v[228:231], a[12:15], v[36:39], v[228:231]\n"
"buffer_load_dword v200, off, s[28:31], s39 sc1\n"
"ds_read_b128 v[40:43], v188 offset:7168\n"
"v_mfma_f32_16x16x32_bf16 v[204:207], a[16:19], v[36:39], v[204:207]\n"
"ds_read_b128 v[44:47], v189 offset:7168\n"
"v_cvt_pk_bf16_f32 v84, v120, v121\n"
"v_mfma_f32_16x16x32_bf16 v[220:223], a[20:23], v[36:39], v[220:223]\n"
"v_exp_f32 v122, v122\n"
"v_mfma_f32_16x16x32_bf16 v[224:227], a[72:75], v[32:35], 0\n"
"v_exp_f32 v123, v123\n"
"v_mfma_f32_16x16x32_bf16 v[208:211], a[76:79], v[32:35], 0\n"
"v_cvt_pk_bf16_f32 v85, v122, v123\n"
"v_and_b32 v120, v84, v176\n"
"v_mfma_f32_16x16x32_bf16 v[224:227], a[84:87], v[36:39], v[224:227]\n"
"s_add_u32 m0, s34, 4096\n"
"v_and_b32 v121, v85, v177\n"
"v_xor_b32 v122, v84, v120\n"
"v_mfma_f32_16x16x32_bf16 v[208:211], a[88:91], v[36:39], v[208:211]\n"
"buffer_load_dwordx4 v191, s[8:11], s0 offen lds\n"
"v_xor_b32 v123, v85, v121\n"
"v_mfma_f32_16x16x32_bf16 v[82:85], v[172:175], v[82:85], 0\n"
"s_add_u32 s0, s0, s33\n"
"v_fmamk_f32 v129, v129, 0x3e553b95, v170\n"
"v_fmamk_f32 v130, v130, 0x3e553b95, v170\n"
"v_mfma_f32_16x16x32_bf16 v[244:247], v[132:135], v[120:123], v[244:247]\n"
"s_waitcnt lgkmcnt(1)\n"
"ds_read_b64_tr_b16 v[132:133], v184 offset:72\n"
"ds_read_b64_tr_b16 v[134:135], v185 offset:72\n"
"v_mfma_f32_16x16x32_bf16 v[248:251], v[140:143], v[120:123], v[248:251]\n"
"v_fmamk_f32 v131, v131, 0x3e553b95, v170\n"
"v_fmamk_f32 v136, v136, 0x3e553b95, v170\n"
"v_mfma_f32_16x16x32_bf16 v[252:255], v[112:115], v[120:123], v[252:255]\n"
"ds_read_b64_tr_b16 v[120:121], v184 offset:136\n"
"ds_read_b64_tr_b16 v[122:123], v185 offset:136\n"
"v_mfma_f32_16x16x32_bf16 v[228:231], a[24:27], v[40:43], v[228:231]\n"
"s_waitcnt lgkmcnt(4)\n"
"v_exp_f32 v128, v128\n"
"v_mfma_f32_16x16x32_bf16 v[204:207], a[28:31], v[40:43], v[204:207]\n"
"v_exp_f32 v129, v129\n"
"v_mfma_f32_16x16x32_bf16 v[220:223], a[32:35], v[40:43], v[220:223]\n"
"v_exp_f32 v130, v130\n"
"v_mfma_f32_16x16x32_bf16 v[228:231], a[36:39], v[44:47], v[228:231]\n"
"v_exp_f32 v131, v131\n"
"v_mfma_f32_16x16x32_bf16 v[204:207], a[40:43], v[44:47], v[204:207]\n"
"v_cvt_pk_bf16_f32 v90, v128, v129\n"
"v_cvt_pk_bf16_f32 v91, v130, v131\n"
"v_mfma_f32_16x16x32_bf16 v[220:223], a[44:47], v[44:47], v[220:223]\n"
"s_add_u32 m0, s34, 2048\n"
"v_and_b32 v128, v90, v176\n"
"v_and_b32 v129, v91, v177\n"
"v_mfma_f32_16x16x32_bf16 v[224:227], a[96:99], v[40:43], v[224:227]\n"
"buffer_load_dwordx4 v190, s[8:11], s1 offen lds\n"
"v_xor_b32 v130, v90, v128\n"
"v_mfma_f32_16x16x32_bf16 v[208:211], a[100:103], v[40:43], v[208:211]\n"
"v_xor_b32 v131, v91, v129\n"
"v_fmamk_f32 v137, v137, 0x3e553b95, v170\n"
"v_mfma_f32_16x16x32_bf16 v[224:227], a[108:111], v[44:47], v[224:227]\n"
"v_cvt_pk_bf16_f32 v146, v216, v217\n"
"v_fmamk_f32 v138, v138, 0x3e553b95, v170\n"
"v_mfma_f32_16x16x32_bf16 v[244:247], v[116:119], v[128:131], v[244:247]\n"
"v_fmamk_f32 v139, v139, 0x3e553b95, v170\n"
"v_cvt_pk_bf16_f32 v147, v218, v219\n"
"v_mfma_f32_16x16x32_bf16 v[248:251], v[98:101], v[128:131], v[248:251]\n"
"v_exp_f32 v136, v136\n"
"v_mfma_f32_16x16x32_bf16 v[252:255], v[106:109], v[128:131], v[252:255]\n"
"v_cvt_pk_bf16_f32 v158, v212, v213\n"
"v_cvt_pk_bf16_f32 v159, v214, v215\n"
"v_mfma_f32_16x16x32_bf16 v[216:219], a[80:83], v[32:35], 0\n"
"v_exp_f32 v137, v137\n"
"v_mfma_f32_16x16x32_bf16 v[216:219], a[92:95], v[36:39], v[216:219]\n"
"v_exp_f32 v138, v138\n"
"v_mfma_f32_16x16x32_bf16 v[216:219], a[104:107], v[40:43], v[216:219]\n"
"s_waitcnt vmcnt(11)\n"
"ds_read_b128 v[48:51], v188 offset:8192\n"
"ds_read_b128 v[52:55], v189 offset:8192\n"
"v_mfma_f32_16x16x32_bf16 v[208:211], a[112:115], v[44:47], v[208:211]\n"
"ds_read_b128 v[140:143], v178 offset:13504\n"
"v_cvt_pk_bf16_f32 v92, v136, v137\n"
"v_mfma_f32_16x16x32_bf16 v[216:219], a[116:119], v[44:47], v[216:219]\n"
"s_add_u32 m0, s34, 5120\n"
"v_exp_f32 v139, v139\n"
"v_mfma_f32_16x16x32_bf16 v[212:215], a[144:147], v[32:35], 0\n"
"buffer_load_dwordx4 v191, s[8:11], s1 offen lds\n"
"ds_read_b128 v[112:115], v179 offset:13504\n"
"v_mfma_f32_16x16x32_bf16 v[212:215], a[156:159], v[36:39], v[212:215]\n"
"s_add_u32 s1, s1, s33\n"
"v_cvt_pk_bf16_f32 v93, v138, v139\n"
"v_and_b32 v136, v92, v176\n"
"v_mfma_f32_16x16x32_bf16 v[212:215], a[168:171], v[40:43], v[212:215]\n"
"v_and_b32 v137, v93, v177\n"
"v_xor_b32 v138, v92, v136\n"
"v_mfma_f32_16x16x32_bf16 v[212:215], a[180:183], v[44:47], v[212:215]\n"
"v_xor_b32 v139, v93, v137\n"
"v_mfma_f32_16x16x32_bf16 v[90:93], v[172:175], v[90:93], v[82:85]\n"
"s_waitcnt lgkmcnt(4)\n"
"ds_read_b128 v[128:131], v178 offset:27008\n"
"ds_read_b128 v[116:119], v179 offset:27008\n"
"v_mfma_f32_16x16x32_bf16 v[244:247], v[124:127], v[136:139], v[244:247]\n"
"s_waitcnt lgkmcnt(5)\n"
"ds_read_b64 v[82:83], v178 offset:13568\n"
"v_mfma_f32_16x16x32_bf16 v[248:251], v[120:123], v[136:139], v[248:251]\n"
"ds_read_b64 v[84:85], v179 offset:13568\n"
"v_rcp_f32 v171, v90\n"
"v_mfma_f32_16x16x32_bf16 v[252:255], v[132:135], v[136:139], v[252:255]\n"
"s_waitcnt lgkmcnt(5)\n"
"ds_read_b64 v[90:91], v178 offset:27072\n"
"ds_read_b64 v[92:93], v179 offset:27072\n"
"v_mfma_f32_16x16x32_bf16 v[228:231], a[48:51], v[48:51], v[228:231]\n"
"s_waitcnt lgkmcnt(6)\n"
"v_mul_f32 v244, v171, v244\n"
"v_mul_f32 v245, v171, v245\n"
"v_mfma_f32_16x16x32_bf16 v[204:207], a[52:55], v[48:51], v[204:207]\n"
"ds_read_b128 v[136:139], v180\n"
"ds_read_b128 v[124:127], v181\n"
"v_mfma_f32_16x16x32_bf16 v[220:223], a[56:59], v[48:51], v[220:223]\n"
"s_add_u32 m0, s34, 6144\n"
"v_mul_f32 v246, v171, v246\n"
"v_mul_f32 v247, v171, v247\n"
"v_mfma_f32_16x16x32_bf16 v[228:231], a[60:63], v[52:55], v[228:231]\n"
"s_waitcnt lgkmcnt(7)\n"
"buffer_load_dwordx4 v190, s[8:11], s32 offen lds\n"
"ds_read_b64 v[98:99], v180 offset:64\n"
"v_mfma_f32_16x16x32_bf16 v[204:207], a[64:67], v[52:55], v[204:207]\n"
"s_waitcnt lgkmcnt(7)\n"
"ds_read_b64 v[100:101], v181 offset:64\n"
"v_cvt_pk_bf16_f32 v244, v244, v245\n"
"v_mfma_f32_16x16x32_bf16 v[220:223], a[68:71], v[52:55], v[220:223]\n"
"v_cvt_pk_bf16_f32 v245, v246, v247\n"
"v_mul_f32 v248, v171, v248\n"
"v_mfma_f32_16x16x32_bf16 v[140:143], v[140:143], v[148:151], 0\n"
"s_waitcnt lgkmcnt(7)\n"
"v_mul_f32 v249, v171, v249\n"
"v_mul_f32 v250, v171, v250\n"
"v_mfma_f32_16x16x32_bf16 v[112:115], v[112:115], v[148:151], 0\n"
"s_waitcnt lgkmcnt(6)\n"
"v_mul_f32 v251, v171, v251\n"
"v_cvt_pk_bf16_f32 v246, v248, v249\n"
"v_mfma_f32_16x16x32_bf16 v[140:143], v[80:83], v[160:163], v[140:143]\n"
"v_cvt_pk_bf16_f32 v247, v250, v251\n"
"v_mul_f32 v252, v171, v252\n"
"v_mfma_f32_16x16x32_bf16 v[112:115], v[84:87], v[162:165], v[112:115]\n"
"s_waitcnt lgkmcnt(3)\n"
"buffer_store_dwordx4 v[244:247], v192, s[28:31], s37 offen sc1\n"
"v_mfma_f32_16x16x32_bf16 v[128:131], v[128:131], v[148:151], 0\n"
"s_waitcnt lgkmcnt(2)\n"
"v_mul_f32 v253, v171, v253\n"
"v_mul_f32 v254, v171, v254\n"
"v_mfma_f32_16x16x32_bf16 v[116:119], v[116:119], v[148:151], 0\n"
"v_mul_f32 v255, v171, v255\n"
"v_cvt_pk_bf16_f32 v252, v252, v253\n"
"v_mfma_f32_16x16x32_bf16 v[128:131], v[88:91], v[160:163], v[128:131]\n"
"s_add_u32 m0, s34, 9216\n"
"v_cvt_pk_bf16_f32 v253, v254, v255\n"
"v_cvt_pk_bf16_f32 v228, v228, v229\n"
"v_mfma_f32_16x16x32_bf16 v[116:119], v[92:95], v[162:165], v[116:119]\n"
"buffer_load_dwordx4 v191, s[8:11], s32 offen lds\n"
"ds_read_b64_tr_b16 v[248:249], v182 offset:13632\n"
"v_mfma_f32_16x16x32_bf16 v[136:139], v[136:139], v[148:151], 0\n"
"s_waitcnt lgkmcnt(2)\n"
"ds_read_b64_tr_b16 v[250:251], v183 offset:13632\n"
"ds_read_b64_tr_b16 v[244:245], v182 offset:13640\n"
"v_mfma_f32_16x16x32_bf16 v[124:127], v[124:127], v[148:151], 0\n"
"s_waitcnt lgkmcnt(3)\n"
"ds_read_b64_tr_b16 v[246:247], v183 offset:13640\n"
"v_cvt_pk_bf16_f32 v229, v230, v231\n"
"v_mfma_f32_16x16x32_bf16 v[136:139], v[96:99], v[160:163], v[136:139]\n"
"s_add_u32 s32, s32, s33\n"
"buffer_store_dwordx2 v[252:253], v193, s[28:31], s37 offen offset:1024 sc1\n"
"v_mfma_f32_16x16x32_bf16 v[124:127], v[100:103], v[162:165], v[124:127]\n"
"s_add_u32 s37, s37, 6144\n"
"ds_read_b64_tr_b16 v[252:253], v182 offset:13576\n"
"ds_read_b64_tr_b16 v[254:255], v183 offset:13576\n"
"v_mfma_f32_16x16x32_bf16 v[224:227], a[120:123], v[48:51], v[224:227]\n"
"v_cvt_pk_bf16_f32 v230, v204, v205\n"
"v_cvt_pk_bf16_f32 v231, v206, v207\n"
"v_mfma_f32_16x16x32_bf16 v[208:211], a[124:127], v[48:51], v[208:211]\n"
"v_cvt_pk_bf16_f32 v220, v220, v221\n"
"v_cvt_pk_bf16_f32 v221, v222, v223\n"
"v_mfma_f32_16x16x32_bf16 v[216:219], a[128:131], v[48:51], v[216:219]\n"
"ds_read_b64_tr_b16 v[98:99], v184 offset:136\n"
"ds_read_b64_tr_b16 v[100:101], v185 offset:136\n"
"v_mfma_f32_16x16x32_bf16 v[224:227], a[132:135], v[52:55], v[224:227]\n"
"v_max_f32 v140, v140, v112\n"
"v_max_f32 v141, v141, v113\n"
"v_mfma_f32_16x16x32_bf16 v[208:211], a[136:139], v[52:55], v[208:211]\n"
"ds_read_b64_tr_b16 v[112:113], v182 offset:27136\n"
"v_max_f32 v142, v142, v114\n"
"v_mfma_f32_16x16x32_bf16 v[216:219], a[140:143], v[52:55], v[216:219]\n"
"s_add_u32 m0, s34, 7168\n"
"v_max_f32 v143, v143, v115\n"
"v_max_f32 v170, v140, v141\n"
"v_mfma_f32_16x16x32_bf16 v[204:207], a[148:151], v[32:35], 0\n"
"buffer_load_dwordx4 v190, s[8:11], s0 offen lds\n"
"ds_read_b64_tr_b16 v[114:115], v183 offset:27136\n"
"v_mfma_f32_16x16x32_bf16 v[204:207], a[160:163], v[36:39], v[204:207]\n"
"v_max_f32 v171, v142, v143\n"
"v_max_f32 v128, v128, v116\n"
"v_mfma_f32_16x16x32_bf16 v[204:207], a[172:175], v[40:43], v[204:207]\n"
".Lic4%=:\n"
"s_cbranch_execz .Lib1%=\n"
"v_max_f32 v129, v129, v117\n"
"v_max_f32 v130, v130, v118\n"
"v_mfma_f32_16x16x32_bf16 v[204:207], a[184:187], v[44:47], v[204:207]\n"
"ds_read_b64_tr_b16 v[116:117], v182 offset:27144\n"
"v_max_f32 v131, v131, v119\n"
"v_mfma_f32_16x16x32_bf16 v[212:215], a[192:195], v[48:51], v[212:215]\n"
"ds_read_b64_tr_b16 v[118:119], v183 offset:27144\n"
"v_max3_f32 v170, v170, v128, v129\n"
"v_mfma_f32_16x16x32_bf16 v[204:207], a[196:199], v[48:51], v[204:207]\n"
"v_max3_f32 v171, v171, v130, v131\n"
"v_max_f32 v136, v136, v124\n"
"v_mfma_f32_16x16x32_bf16 v[212:215], a[204:207], v[52:55], v[212:215]\n"
"v_max_f32 v137, v137, v125\n"
"v_max_f32 v138, v138, v126\n"
"v_mfma_f32_16x16x32_bf16 v[204:207], a[208:211], v[52:55], v[204:207]\n"
"s_waitcnt vmcnt(9)\n"
"v_max_f32 v139, v139, v127\n"
"v_max3_f32 v170, v170, v136, v137\n"
"v_max3_f32 v171, v171, v138, v139\n"
"s_barrier\n"
"ds_read_b128 v[68:71], v192 offset:55296\n"
"ds_read_b128 v[72:75], v192 offset:56320\n"
"ds_read_b128 v[76:79], v192 offset:57344\n"
"v_cvt_pk_bf16_f32 v224, v224, v225\n"
"s_add_u32 m0, s34, 10240\n"
"v_cvt_pk_bf16_f32 v225, v226, v227\n"
"buffer_load_dwordx4 v191, s[8:11], s0 offen lds\n"
"s_add_u32 s0, s0, s33\n"
"ds_write_b128 v187, v[228:231]\n"
"v_mfma_f32_16x16x32_bf16 v[228:231], a[152:155], v[32:35], 0\n"
"v_cvt_pk_bf16_f32 v226, v208, v209\n"
"v_cvt_pk_bf16_f32 v227, v210, v211\n"
"v_mfma_f32_16x16x32_bf16 v[228:231], a[164:167], v[36:39], v[228:231]\n"
"s_waitcnt vmcnt(9)\n"
"v_add3_u32 v200, v200, s50, -4\n"
"v_cmp_lt_u32 vcc, 64, v200\n"
"v_mfma_f32_16x16x32_bf16 v[228:231], a[176:179], v[40:43], v[228:231]\n"
"ds_read_b128 v[32:35], v188\n"
"ds_read_b128 v[36:39], v189\n"
"v_mfma_f32_16x16x32_bf16 v[228:231], a[188:191], v[44:47], v[228:231]\n"
"s_waitcnt lgkmcnt(5)\n"
"ds_write_b128 v202, v[62:65] offset:36864\n"
"v_mfma_f32_16x16x32_bf16 v[240:243], a[224:227], v[68:71], 0\n"
"s_waitcnt lgkmcnt(5)\n"
"v_cvt_pk_bf16_f32 v222, v216, v217\n"
"v_cvt_pk_bf16_f32 v223, v218, v219\n"
"v_mfma_f32_16x16x32_bf16 v[240:243], a[236:239], v[72:75], v[240:243]\n"
"s_waitcnt lgkmcnt(4)\n"
"s_cbranch_vccnz .Lp3%=\n"
".Lr3%=:\n"
"v_mfma_f32_16x16x32_bf16 v[240:243], a[248:251], v[76:79], v[240:243]\n"
"buffer_load_dword v200, off, s[28:31], s39 sc1\n"
"ds_read_b128 v[120:123], v180 offset:13504\n"
"v_mfma_f32_16x16x32_bf16 v[228:231], a[200:203], v[48:51], v[228:231]\n"
"s_add_u32 m0, s34, 8192\n"
"buffer_load_dwordx4 v[56:59], v192, s[28:31], s38 offen sc1\n"
"ds_read_b128 v[132:135], v181 offset:13504\n"
"v_mfma_f32_16x16x32_bf16 v[228:231], a[212:215], v[52:55], v[228:231]\n"
"ds_read_b64 v[106:107], v180 offset:13568\n"
"ds_read_b64 v[108:109], v181 offset:13568\n"
"ds_read_b64_tr_b16 v[20:21], v236 offset:12288\n"
"s_waitcnt lgkmcnt(7)\n"
"v_mfma_f32_16x16x32_bf16 v[208:211], a[0:3], v[32:35], 0\n"
"s_waitcnt lgkmcnt(6)\n"
"v_mfma_f32_16x16x32_bf16 v[216:219], a[4:7], v[32:35], 0\n"
"buffer_load_dwordx4 v190, s[8:11], s1 offen lds\n"
"ds_read_b64_tr_b16 v[22:23], v236 offset:18432\n"
"v_mfma_f32_16x16x32_bf16 v[208:211], a[12:15], v[36:39], v[208:211]\n"
"v_mfma_f32_16x16x32_bf16 v[216:219], a[16:19], v[36:39], v[216:219]\n"
"ds_read_b64_tr_b16 v[24:25], v236 offset:12296\n"
"s_waitcnt lgkmcnt(6)\n"
"v_mfma_f32_16x16x32_bf16 v[120:123], v[120:123], v[148:151], 0\n"
"s_waitcnt lgkmcnt(5)\n"
"buffer_load_dwordx2 v[60:61], v193, s[28:31], s38 offen offset:1024 sc1\n"
"ds_read_b64_tr_b16 v[26:27], v236 offset:18440\n"
"v_mfma_f32_16x16x32_bf16 v[132:135], v[132:135], v[148:151], 0\n"
"s_waitcnt lgkmcnt(5)\n"
"v_cvt_pk_bf16_f32 v148, v212, v213\n"
"v_cvt_pk_bf16_f32 v149, v214, v215\n"
"v_mfma_f32_16x16x32_bf16 v[120:123], v[104:107], v[160:163], v[120:123]\n"
"s_waitcnt lgkmcnt(4)\n"
"ds_read_b64_tr_b16 v[28:29], v235 offset:12288\n"
"v_cvt_pk_bf16_f32 v150, v204, v205\n"
"v_mfma_f32_16x16x32_bf16 v[132:135], v[108:111], v[162:165], v[132:135]\n"
"s_add_u32 s38, s38, 6144\n"
"v_cvt_pk_bf16_f32 v151, v206, v207\n"
"v_cvt_pk_bf16_f32 v162, v228, v229\n"
"v_mfma_f32_16x16x32_bf16 v[236:239], a[220:223], v[68:71], 0\n"
"buffer_load_dwordx4 v[62:65], v192, s[28:31], s38 offen sc1\n"
"ds_read_b64_tr_b16 v[30:31], v235 offset:18432\n"
"v_mfma_f32_16x16x32_bf16 v[232:235], a[216:219], v[68:71], 0\n"
"v_cvt_pk_bf16_f32 v163, v230, v231\n"
"v_max_f32 v120, v120, v132\n"
"v_mfma_f32_16x16x32_bf16 v[232:235], a[228:231], v[72:75], v[232:235]\n"
"ds_read_b128 v[68:71], v192 offset:58368\n"
"v_max_f32 v121, v121, v133\n"
"v_mfma_f32_16x16x32_bf16 v[236:239], a[232:235], v[72:75], v[236:239]\n"
"s_add_u32 m0, s34, 11264\n"
"ds_read_b128 v[72:75], v192 offset:59392\n"
"v_max_f32 v122, v122, v134\n"
"v_mfma_f32_16x16x32_bf16 v[232:235], a[240:243], v[76:79], v[232:235]\n"
"buffer_load_dwordx4 v191, s[8:11], s1 offen lds\n"
"ds_write_b64 v203, v[66:67] offset:36864\n"
"v_mfma_f32_16x16x32_bf16 v[236:239], a[244:247], v[76:79], v[236:239]\n"
"s_add_u32 s1, s1, s33\n"
"v_max_f32 v123, v123, v135\n"
"v_max3_f32 v170, v170, v120, v121\n"
"v_mfma_f32_16x16x32_bf16 v[212:215], a[76:79], v[32:35], 0\n"
"ds_read_b128 v[76:79], v192 offset:60416\n"
"ds_read_b64_tr_b16 v[124:125], v182 offset:27080\n"
"v_mfma_f32_16x16x32_bf16 v[204:207], a[80:83], v[32:35], 0\n"
"buffer_load_dwordx2 v[66:67], v193, s[28:31], s38 offen offset:1024 sc1\n"
"ds_read_b64_tr_b16 v[126:127], v183 offset:27080\n"
"v_mfma_f32_16x16x32_bf16 v[212:215], a[88:91], v[36:39], v[212:215]\n"
"s_add_u32 s38, s38, 6144\n"
"ds_read_b64_tr_b16 v[132:133], v184 offset:128\n"
"v_max3_f32 v171, v171, v122, v123\n"
"v_mfma_f32_16x16x32_bf16 v[204:207], a[92:95], v[36:39], v[204:207]\n"
"ds_write_b128 v187, v[220:223] offset:64\n"
"v_mfma_f32_16x16x32_bf16 v[220:223], a[8:11], v[32:35], 0\n"
"s_waitcnt vmcnt(15) lgkmcnt(7)\n"
"v_max_f32 v170, v170, v171\n"
"v_mov_b32 v171, v170\n"
"v_mfma_f32_16x16x32_bf16 v[232:235], a[252:255], v[68:71], v[232:235]\n"
"s_waitcnt lgkmcnt(6)\n"
"v_permlane16_swap_b32 v170, v171\n"
"v_mfma_f32_16x16x32_bf16 v[236:239], v[0:3], v[68:71], v[236:239]\n"
"ds_read_b128 v[40:43], v188 offset:1024\n"
"ds_read_b128 v[44:47], v189 offset:1024\n"
"v_mfma_f32_16x16x32_bf16 v[240:243], v[4:7], v[68:71], v[240:243]\n"
"s_add_u32 m0, s34, 0\n"
"ds_read_b64_tr_b16 v[134:135], v185 offset:128\n"
"buffer_load_dwordx4 v190, s[8:11], s32 offen lds\n"
"v_mfma_f32_16x16x32_bf16 v[232:235], v[8:11], v[72:75], v[232:235]\n"
"s_waitcnt lgkmcnt(7)\n"
"ds_read_b64_tr_b16 v[106:107], v184 offset:72\n"
"v_max_f32 v170, v170, v171\n"
"v_mfma_f32_16x16x32_bf16 v[240:243], v[16:19], v[72:75], v[240:243]\n"
"ds_read_b64_tr_b16 v[108:109], v185 offset:72\n"
"v_mov_b32 v171, v170\n"
"v_mfma_f32_16x16x32_bf16 v[236:239], v[12:15], v[72:75], v[236:239]\n"
"ds_write_b128 v187, v[224:227] offset:128\n"
"v_mfma_f32_16x16x32_bf16 v[232:235], v[20:23], v[76:79], v[232:235]\n"
"v_permlane32_swap_b32 v170, v171\n"
"v_mfma_f32_16x16x32_bf16 v[240:243], v[28:31], v[76:79], v[240:243]\n"
"v_max_f32 v170, v170, v171\n"
"v_mul_f32 v170, 0xbe553b95, v170\n"
"v_mfma_f32_16x16x32_bf16 v[236:239], v[24:27], v[76:79], v[236:239]\n"
"s_waitcnt lgkmcnt(5)\n"
"v_fmamk_f32 v140, v140, 0x3e553b95, v170\n"
"v_fmamk_f32 v141, v141, 0x3e553b95, v170\n"
"v_mfma_f32_16x16x32_bf16 v[220:223], a[20:23], v[36:39], v[220:223]\n"
"v_fmamk_f32 v142, v142, 0x3e553b95, v170\n"
"v_fmamk_f32 v143, v143, 0x3e553b95, v170\n"
"v_mfma_f32_16x16x32_bf16 v[208:211], a[24:27], v[40:43], v[208:211]\n"
"s_waitcnt lgkmcnt(4)\n"
"v_exp_f32 v140, v140\n"
"v_mfma_f32_16x16x32_bf16 v[216:219], a[28:31], v[40:43], v[216:219]\n"
"v_exp_f32 v141, v141\n"
"v_mfma_f32_16x16x32_bf16 v[220:223], a[32:35], v[40:43], v[220:223]\n"
"s_add_u32 m0, s34, 3072\n"
"v_exp_f32 v142, v142\n"
"v_mfma_f32_16x16x32_bf16 v[208:211], a[36:39], v[44:47], v[208:211]\n"
"buffer_load_dwordx4 v191, s[8:11], s32 offen lds\n"
"v_cvt_pk_bf16_f32 v82, v140, v141\n"
"v_mfma_f32_16x16x32_bf16 v[216:219], a[40:43], v[44:47], v[216:219]\n"
"s_add_u32 s32, s32, s33\n"
"v_exp_f32 v143, v143\n"
"v_mfma_f32_16x16x32_bf16 v[220:223], a[44:47], v[44:47], v[220:223]\n"
"v_cvt_pk_bf16_f32 v83, v142, v143\n"
"v_and_b32 v140, v82, v176\n"
"v_mfma_f32_16x16x32_bf16 v[224:227], a[72:75], v[32:35], 0\n"
"v_and_b32 v141, v83, v177\n"
"v_xor_b32 v142, v82, v140\n"
"v_mfma_f32_16x16x32_bf16 v[224:227], a[84:87], v[36:39], v[224:227]\n"
"v_xor_b32 v143, v83, v141\n"
"v_fmamk_f32 v128, v128, 0x3e553b95, v170\n"
"v_mfma_f32_16x16x32_bf16 v[224:227], a[96:99], v[40:43], v[224:227]\n"
"v_fmamk_f32 v129, v129, 0x3e553b95, v170\n"
"v_fmamk_f32 v130, v130, 0x3e553b95, v170\n"
"v_mfma_f32_16x16x32_bf16 v[248:251], v[248:251], v[140:143], 0\n"
"v_fmamk_f32 v131, v131, 0x3e553b95, v170\n"
"v_fmamk_f32 v136, v136, 0x3e553b95, v170\n"
"v_mfma_f32_16x16x32_bf16 v[244:247], v[244:247], v[140:143], 0\n"
"v_exp_f32 v128, v128\n"
"v_mfma_f32_16x16x32_bf16 v[252:255], v[252:255], v[140:143], 0\n"
"s_waitcnt vmcnt(15)\n"
"ds_read_b128 v[48:51], v188 offset:2048\n"
"ds_read_b128 v[52:55], v189 offset:2048\n"
"v_mfma_f32_16x16x32_bf16 v[212:215], a[100:103], v[40:43], v[212:215]\n"
"v_exp_f32 v129, v129\n"
"v_mfma_f32_16x16x32_bf16 v[204:207], a[104:107], v[40:43], v[204:207]\n"
"s_add_u32 m0, s34, 1024\n"
"v_exp_f32 v130, v130\n"
"v_mfma_f32_16x16x32_bf16 v[224:227], a[108:111], v[44:47], v[224:227]\n"
"buffer_load_dwordx4 v190, s[8:11], s0 offen lds\n"
"ds_read_b64_tr_b16 v[140:141], v184 offset:13632\n"
"v_mfma_f32_16x16x32_bf16 v[212:215], a[112:115], v[44:47], v[212:215]\n"
"v_exp_f32 v131, v131\n"
"v_mfma_f32_16x16x32_bf16 v[204:207], a[116:119], v[44:47], v[204:207]\n"
"ds_read_b64_tr_b16 v[142:143], v185 offset:13632\n"
"v_cvt_pk_bf16_f32 v84, v128, v129\n"
"v_mfma_f32_16x16x32_bf16 v[228:231], a[144:147], v[32:35], 0\n"
"v_cvt_pk_bf16_f32 v85, v130, v131\n"
"v_and_b32 v128, v84, v176\n"
"v_mfma_f32_16x16x32_bf16 v[228:231], a[156:159], v[36:39], v[228:231]\n"
"v_and_b32 v129, v85, v177\n"
"v_xor_b32 v130, v84, v128\n"
"v_mfma_f32_16x16x32_bf16 v[228:231], a[168:171], v[40:43], v[228:231]\n"
"s_waitcnt lgkmcnt(3)\n"
"v_xor_b32 v131, v85, v129\n"
"v_fmamk_f32 v137, v137, 0x3e553b95, v170\n"
"v_mfma_f32_16x16x32_bf16 v[82:85], v[172:175], v[82:85], 0\n"
"v_fmamk_f32 v138, v138, 0x3e553b95, v170\n"
"v_fmamk_f32 v139, v139, 0x3e553b95, v170\n"
"v_mfma_f32_16x16x32_bf16 v[248:251], v[112:115], v[128:131], v[248:251]\n"
"ds_read_b64_tr_b16 v[112:113], v184 offset:13576\n"
"ds_read_b64_tr_b16 v[114:115], v185 offset:13576\n"
"v_mfma_f32_16x16x32_bf16 v[244:247], v[116:119], v[128:131], v[244:247]\n"
"v_exp_f32 v136, v136\n"
"v_mfma_f32_16x16x32_bf16 v[252:255], v[124:127], v[128:131], v[252:255]\n"
"ds_read_b64_tr_b16 v[128:129], v184 offset:13640\n"
"ds_read_b64_tr_b16 v[130:131], v185 offset:13640\n"
"v_mfma_f32_16x16x32_bf16 v[208:211], a[48:51], v[48:51], v[208:211]\n"
"s_waitcnt lgkmcnt(6)\n"
"v_exp_f32 v137, v137\n"
"v_mfma_f32_16x16x32_bf16 v[216:219], a[52:55], v[48:51], v[216:219]\n"
"s_add_u32 m0, s34, 4096\n"
"v_exp_f32 v138, v138\n"
"v_mfma_f32_16x16x32_bf16 v[220:223], a[56:59], v[48:51], v[220:223]\n"
"buffer_load_dwordx4 v191, s[8:11], s0 offen lds\n"
"v_cvt_pk_bf16_f32 v90, v136, v137\n"
"v_mfma_f32_16x16x32_bf16 v[208:211], a[60:63], v[52:55], v[208:211]\n"
"s_add_u32 s0, s0, s33\n"
"v_exp_f32 v139, v139\n"
"v_mfma_f32_16x16x32_bf16 v[216:219], a[64:67], v[52:55], v[216:219]\n"
"v_cvt_pk_bf16_f32 v232, v232, v233\n"
"v_cvt_pk_bf16_f32 v91, v138, v139\n"
"v_mfma_f32_16x16x32_bf16 v[220:223], a[68:71], v[52:55], v[220:223]\n"
"v_cvt_pk_bf16_f32 v233, v234, v235\n"
"v_cvt_pk_bf16_f32 v240, v240, v241\n"
"v_mfma_f32_16x16x32_bf16 v[224:227], a[120:123], v[48:51], v[224:227]\n"
"v_cvt_pk_bf16_f32 v234, v236, v237\n"
"v_cvt_pk_bf16_f32 v235, v238, v239\n"
"v_mfma_f32_16x16x32_bf16 v[212:215], a[124:127], v[48:51], v[212:215]\n"
"ds_write_b128 v196, v[232:235] offset:30720\n"
"v_mfma_f32_16x16x32_bf16 v[204:207], a[128:131], v[48:51], v[204:207]\n"
"s_waitcnt vmcnt(13)\n"
"v_cvt_pk_bf16_f32 v241, v242, v243\n"
"v_or_b32 v201, 7, s6\n"
"v_mfma_f32_16x16x32_bf16 v[224:227], a[132:135], v[52:55], v[224:227]\n"
"s_mov_b64 exec, 1\n"
"buffer_store_dword v201, off, s[28:31], s40 sc1\n"
"s_mov_b64 exec, -1\n"
"v_mfma_f32_16x16x32_bf16 v[212:215], a[136:139], v[52:55], v[212:215]\n"
"ds_write_b64 v197, v[240:241] offset:30720\n"
"v_mfma_f32_16x16x32_bf16 v[204:207], a[140:143], v[52:55], v[204:207]\n"
"s_add_u32 m0, s34, 2048\n"
"v_and_b32 v136, v90, v176\n"
"v_and_b32 v137, v91, v177\n"
"v_mfma_f32_16x16x32_bf16 v[228:231], a[180:183], v[44:47], v[228:231]\n"
"buffer_load_dwordx4 v190, s[8:11], s1 offen lds\n"
"v_xor_b32 v138, v90, v136\n"
"v_mfma_f32_16x16x32_bf16 v[228:231], a[192:195], v[48:51], v[228:231]\n"
"v_xor_b32 v139, v91, v137\n"
"v_fmamk_f32 v120, v120, 0x3e553b95, v170\n"
"v_mfma_f32_16x16x32_bf16 v[228:231], a[204:207], v[52:55], v[228:231]\n"
"v_fmamk_f32 v121, v121, 0x3e553b95, v170\n"
"v_fmamk_f32 v122, v122, 0x3e553b95, v170\n"
"v_mfma_f32_16x16x32_bf16 v[248:251], v[132:135], v[136:139], v[248:251]\n"
"v_fmamk_f32 v123, v123, 0x3e553b95, v170\n"
"v_exp_f32 v120, v120\n"
"v_mfma_f32_16x16x32_bf16 v[244:247], v[98:101], v[136:139], v[244:247]\n"
"v_exp_f32 v121, v121\n"
"v_mfma_f32_16x16x32_bf16 v[252:255], v[106:109], v[136:139], v[252:255]\n"
"s_waitcnt lgkmcnt(0)\n"
"v_exp_f32 v122, v122\n"
"s_barrier\n"
"ds_read_b128 v[76:79], v192 offset:36864\n"
"v_exp_f32 v123, v123\n"
"v_cvt_pk_bf16_f32 v92, v120, v121\n"
"v_cvt_pk_bf16_f32 v93, v122, v123\n"
"v_and_b32 v120, v92, v176\n"
"v_and_b32 v121, v93, v177\n"
"v_xor_b32 v122, v92, v120\n"
"v_xor_b32 v123, v93, v121\n"
"v_mfma_f32_16x16x32_bf16 v[90:93], v[172:175], v[90:93], v[82:85]\n"
"ds_read_b128 v[116:119], v178 offset:27008\n"
"ds_read_b128 v[124:127], v179 offset:27008\n"
"v_mfma_f32_16x16x32_bf16 v[248:251], v[140:143], v[120:123], v[248:251]\n"
"s_add_u32 m0, s34, 5120\n"
"ds_read_b64 v[82:83], v178 offset:27072\n"
"buffer_load_dwordx4 v191, s[8:11], s1 offen lds\n"
"v_mfma_f32_16x16x32_bf16 v[244:247], v[128:131], v[120:123], v[244:247]\n"
"s_add_u32 s1, s1, s33\n"
"ds_read_b64 v[84:85], v179 offset:27072\n"
"v_rcp_f32 v171, v90\n"
"v_mfma_f32_16x16x32_bf16 v[252:255], v[112:115], v[120:123], v[252:255]\n"
"ds_read_b128 v[136:139], v180\n"
"ds_read_b128 v[132:135], v181\n"
"v_mul_f32 v248, v171, v248\n"
"v_mul_f32 v249, v171, v249\n"
"v_mul_f32 v250, v171, v250\n"
"v_mul_f32 v251, v171, v251\n"
"v_cvt_pk_bf16_f32 v248, v248, v249\n"
"v_cvt_pk_bf16_f32 v249, v250, v251\n"
"v_mul_f32 v244, v171, v244\n"
"v_mul_f32 v245, v171, v245\n"
"v_mul_f32 v246, v171, v246\n"
"v_mul_f32 v247, v171, v247\n"
"v_cvt_pk_bf16_f32 v250, v244, v245\n"
"v_cvt_pk_bf16_f32 v251, v246, v247\n"
"v_mul_f32 v252, v171, v252\n"
"v_mul_f32 v253, v171, v253\n"
"v_mul_f32 v254, v171, v254\n"
"v_mul_f32 v255, v171, v255\n"
"v_cvt_pk_bf16_f32 v252, v252, v253\n"
"v_cvt_pk_bf16_f32 v253, v254, v255\n"
"v_cvt_pk_bf16_f32 v208, v208, v209\n"
"v_cvt_pk_bf16_f32 v209, v210, v211\n"
"v_cvt_pk_bf16_f32 v210, v216, v217\n"
"v_cvt_pk_bf16_f32 v211, v218, v219\n"
"v_mfma_f32_16x16x32_bf16 v[216:219], a[148:151], v[32:35], 0\n"
"s_waitcnt vmcnt(13) lgkmcnt(4)\n"
"v_add3_u32 v200, v200, s50, -5\n"
"v_cmp_lt_u32 vcc, 64, v200\n"
"v_mfma_f32_16x16x32_bf16 v[116:119], v[116:119], v[152:155], 0\n"
"s_waitcnt lgkmcnt(3)\n"
"buffer_store_dwordx4 v[248:251], v192, s[28:31], s37 offen sc1\n"
"v_mfma_f32_16x16x32_bf16 v[124:127], v[124:127], v[152:155], 0\n"
"s_waitcnt vmcnt(13) lgkmcnt(0)\n"
"ds_read_b64 v[90:91], v180 offset:64\n"
"ds_read_b64 v[92:93], v181 offset:64\n"
"v_mfma_f32_16x16x32_bf16 v[116:119], v[80:83], v[164:167], v[116:119]\n"
"s_cbranch_vccnz .Lp4%=\n"
".Lr4%=:\n"
"v_mfma_f32_16x16x32_bf16 v[124:127], v[84:87], v[166:169], v[124:127]\n"
"buffer_store_dwordx2 v[252:253], v193, s[28:31], s37 offen offset:1024 sc1\n"
"v_mfma_f32_16x16x32_bf16 v[136:139], v[136:139], v[152:155], 0\n"
"s_add_u32 s37, s37, 6144\n"
"ds_write_b128 v186, v[208:211] offset:6752\n"
"v_mfma_f32_16x16x32_bf16 v[132:135], v[132:135], v[152:155], 0\n"
"v_cvt_pk_bf16_f32 v220, v220, v221\n"
"v_cvt_pk_bf16_f32 v221, v222, v223\n"
"v_mfma_f32_16x16x32_bf16 v[232:235], a[216:219], v[76:79], 0\n"
"v_max_f32 v116, v116, v124\n"
"v_max_f32 v117, v117, v125\n"
"v_mfma_f32_16x16x32_bf16 v[236:239], a[220:223], v[76:79], 0\n"
"ds_read_b128 v[120:123], v180 offset:13504\n"
"ds_read_b128 v[140:143], v181 offset:13504\n"
"v_mfma_f32_16x16x32_bf16 v[240:243], a[224:227], v[76:79], 0\n"
"s_waitcnt lgkmcnt(4)\n"
"ds_read_b128 v[76:79], v192 offset:39936\n"
"v_max_f32 v118, v118, v126\n"
"v_mfma_f32_16x16x32_bf16 v[136:139], v[88:91], v[164:167], v[136:139]\n"
"s_waitcnt vmcnt(12) lgkmcnt(4)\n"
"ds_read_b64 v[98:99], v180 offset:13568\n"
"v_max_f32 v119, v119, v127\n"
"v_mfma_f32_16x16x32_bf16 v[132:135], v[92:95], v[166:169], v[132:135]\n"
"ds_read_b64 v[100:101], v181 offset:13568\n"
"v_max_f32 v170, v116, v117\n"
"v_mfma_f32_16x16x32_bf16 v[208:211], a[152:155], v[32:35], 0\n"
"ds_read_b128 v[32:35], v188 offset:6144\n"
"v_max_f32 v171, v118, v119\n"
"v_mfma_f32_16x16x32_bf16 v[216:219], a[160:163], v[36:39], v[216:219]\n"
"ds_write_b128 v202, v[56:59] offset:43008\n"
"v_mfma_f32_16x16x32_bf16 v[208:211], a[164:167], v[36:39], v[208:211]\n"
"buffer_load_dwordx4 v[56:59], v192, s[28:31], s38 offen sc1\n"
"v_max_f32 v136, v136, v132\n"
"v_mfma_f32_16x16x32_bf16 v[216:219], a[172:175], v[40:43], v[216:219]\n"
"s_waitcnt lgkmcnt(6)\n"
"v_max_f32 v137, v137, v133\n"
"v_max_f32 v138, v138, v134\n"
"v_mfma_f32_16x16x32_bf16 v[120:123], v[120:123], v[152:155], 0\n"
"s_waitcnt lgkmcnt(5)\n"
"ds_read_b128 v[36:39], v189 offset:6144\n"
"ds_read_b128 v[128:131], v180 offset:27008\n"
"v_mfma_f32_16x16x32_bf16 v[140:143], v[140:143], v[152:155], 0\n"
"ds_read_b128 v[112:115], v181 offset:27008\n"
"v_max_f32 v139, v139, v135\n"
"v_mfma_f32_16x16x32_bf16 v[208:211], a[176:179], v[40:43], v[208:211]\n"
"s_waitcnt lgkmcnt(6)\n"
"ds_read_b128 v[40:43], v188 offset:7168\n"
"v_max3_f32 v170, v170, v136, v137\n"
"v_mfma_f32_16x16x32_bf16 v[120:123], v[96:99], v[164:167], v[120:123]\n"
"s_waitcnt lgkmcnt(6)\n"
"ds_read_b64 v[106:107], v180 offset:27072\n"
"v_max3_f32 v171, v171, v138, v139\n"
"v_mfma_f32_16x16x32_bf16 v[140:143], v[100:103], v[166:169], v[140:143]\n"
"ds_read_b64 v[108:109], v181 offset:27072\n"
"v_cvt_pk_bf16_f32 v224, v224, v225\n"
"v_mfma_f32_16x16x32_bf16 v[216:219], a[184:187], v[44:47], v[216:219]\n"
"ds_read_b128 v[68:71], v202 offset:30720\n"
"v_cvt_pk_bf16_f32 v225, v226, v227\n"
"v_mfma_f32_16x16x32_bf16 v[208:211], a[188:191], v[44:47], v[208:211]\n"
"ds_read_b128 v[44:47], v189 offset:7168\n"
"v_cvt_pk_bf16_f32 v226, v212, v213\n"
"v_mfma_f32_16x16x32_bf16 v[216:219], a[196:199], v[48:51], v[216:219]\n"
"ds_read_b64_tr_b16 v[244:245], v182 offset:27136\n"
"v_max_f32 v120, v120, v140\n"
"v_mfma_f32_16x16x32_bf16 v[208:211], a[200:203], v[48:51], v[208:211]\n"
"s_waitcnt vmcnt(11) lgkmcnt(8)\n"
"ds_read_b128 v[48:51], v188 offset:8192\n"
"v_max_f32 v121, v121, v141\n"
"v_mfma_f32_16x16x32_bf16 v[216:219], a[208:211], v[52:55], v[216:219]\n"
"s_waitcnt lgkmcnt(8)\n"
"ds_read_b64_tr_b16 v[246:247], v183 offset:27136\n"
"v_max_f32 v122, v122, v142\n"
"v_mfma_f32_16x16x32_bf16 v[128:131], v[128:131], v[152:155], 0\n"
"s_waitcnt lgkmcnt(8)\n"
"ds_read_b64_tr_b16 v[248:249], v182 offset:27144\n"
"v_max_f32 v123, v123, v143\n"
"v_mfma_f32_16x16x32_bf16 v[112:115], v[112:115], v[152:155], 0\n"
"s_add_u32 m0, s34, 6144\n"
"ds_read_b64_tr_b16 v[250:251], v183 offset:27144\n"
"buffer_load_dwordx4 v190, s[8:11], s32 offen lds\n"
"v_mfma_f32_16x16x32_bf16 v[208:211], a[212:215], v[52:55], v[208:211]\n"
"s_waitcnt lgkmcnt(8)\n"
"ds_read_b128 v[52:55], v189 offset:8192\n"
"v_max3_f32 v170, v170, v120, v121\n"
"v_mfma_f32_16x16x32_bf16 v[128:131], v[104:107], v[164:167], v[128:131]\n"
"s_waitcnt lgkmcnt(8)\n"
"ds_read_b64_tr_b16 v[252:253], v182 offset:27080\n"
"v_max3_f32 v171, v171, v122, v123\n"
"v_mfma_f32_16x16x32_bf16 v[112:115], v[108:111], v[166:169], v[112:115]\n"
"s_waitcnt lgkmcnt(8)\n"
"ds_read_b128 v[72:75], v202 offset:34816\n"
"buffer_store_dwordx4 v[68:71], v194, s[24:27], s36 offen sc1\n"
"s_nop 1\n"
"ds_read_b128 v[68:71], v192 offset:37888\n"
"ds_read_b64_tr_b16 v[254:255], v183 offset:27080\n"
"v_cvt_pk_bf16_f32 v227, v214, v215\n"
"v_mfma_f32_16x16x32_bf16 v[212:215], a[0:3], v[32:35], 0\n"
"ds_read_b64_tr_b16 v[124:125], v184 offset:128\n"
"v_max_f32 v128, v128, v112\n"
"v_mfma_f32_16x16x32_bf16 v[212:215], a[12:15], v[36:39], v[212:215]\n"
"ds_read_b64_tr_b16 v[126:127], v185 offset:128\n"
"v_max_f32 v129, v129, v113\n"
"v_mfma_f32_16x16x32_bf16 v[212:215], a[24:27], v[40:43], v[212:215]\n"
"s_waitcnt lgkmcnt(12)\n"
"ds_read_b64_tr_b16 v[132:133], v184 offset:136\n"
"v_max_f32 v130, v130, v114\n"
"v_mfma_f32_16x16x32_bf16 v[212:215], a[36:39], v[44:47], v[212:215]\n"
"s_waitcnt lgkmcnt(11)\n"
"ds_read_b64_tr_b16 v[134:135], v185 offset:136\n"
"v_max_f32 v131, v131, v115\n"
"v_mfma_f32_16x16x32_bf16 v[212:215], a[48:51], v[48:51], v[212:215]\n"
"s_waitcnt lgkmcnt(8)\n"
"ds_write_b64 v203, v[60:61] offset:43008\n"
"v_mfma_f32_16x16x32_bf16 v[212:215], a[60:63], v[52:55], v[212:215]\n"
"s_add_u32 m0, s34, 9216\n"
"buffer_load_dwordx2 v[60:61], v193, s[28:31], s38 offen offset:1024 sc1\n"
"v_max3_f32 v170, v170, v128, v129\n"
"v_max3_f32 v171, v171, v130, v131\n"
"v_cvt_pk_bf16_f32 v222, v204, v205\n"
"v_cvt_pk_bf16_f32 v223, v206, v207\n"
"v_mfma_f32_16x16x32_bf16 v[204:207], a[4:7], v[32:35], 0\n"
"s_waitcnt lgkmcnt(6)\n"
"ds_read_b64_tr_b16 v[140:141], v184 offset:72\n"
"ds_read_b64_tr_b16 v[142:143], v185 offset:72\n"
"v_mfma_f32_16x16x32_bf16 v[232:235], a[228:231], v[68:71], v[232:235]\n"
"s_add_u32 s38, s38, 6144\n"
"ds_read_b64_tr_b16 v[112:113], v184 offset:13632\n"
"buffer_load_dwordx4 v191, s[8:11], s32 offen lds\n"
"v_mfma_f32_16x16x32_bf16 v[236:239], a[232:235], v[68:71], v[236:239]\n"
"s_add_u32 s32, s32, s33\n"
"ds_write_b128 v186, v[220:223] offset:6816\n"
"v_mfma_f32_16x16x32_bf16 v[240:243], a[236:239], v[68:71], v[240:243]\n"
"v_max_f32 v170, v170, v171\n"
"v_mov_b32 v171, v170\n"
"v_mfma_f32_16x16x32_bf16 v[220:223], a[8:11], v[32:35], 0\n"
"s_nop 0\n"
"v_permlane16_swap_b32 v170, v171\n"
"v_mfma_f32_16x16x32_bf16 v[204:207], a[16:19], v[36:39], v[204:207]\n"
"ds_read_b128 v[68:71], v192 offset:40960\n"
"buffer_store_dwordx4 v[72:75], v195, s[24:27], s36 offen sc1\n"
"v_mfma_f32_16x16x32_bf16 v[220:223], a[20:23], v[36:39], v[220:223]\n"
"s_add_u32 s36, s36, s33\n"
"ds_read_b128 v[72:75], v192 offset:38912\n"
"ds_read_b64_tr_b16 v[114:115], v185 offset:13632\n"
"v_mfma_f32_16x16x32_bf16 v[204:207], a[28:31], v[40:43], v[204:207]\n"
"ds_read_b64_tr_b16 v[98:99], v184 offset:13640\n"
"v_max_f32 v170, v170, v171\n"
"v_mfma_f32_16x16x32_bf16 v[220:223], a[32:35], v[40:43], v[220:223]\n"
"ds_read_b64_tr_b16 v[100:101], v185 offset:13640\n"
"v_mov_b32 v171, v170\n"
"v_mfma_f32_16x16x32_bf16 v[204:207], a[40:43], v[44:47], v[204:207]\n"
"ds_read_b64_tr_b16 v[106:107], v184 offset:13576\n"
"v_permlane32_swap_b32 v170, v171\n"
"v_mfma_f32_16x16x32_bf16 v[220:223], a[44:47], v[44:47], v[220:223]\n"
"s_add_u32 m0, s34, 7168\n"
"ds_read_b64_tr_b16 v[108:109], v185 offset:13576\n"
"buffer_load_dwordx4 v190, s[8:11], s0 offen lds\n"
"v_mfma_f32_16x16x32_bf16 v[204:207], a[52:55], v[48:51], v[204:207]\n"
"ds_write_b128 v186, v[224:227] offset:6880\n"
"v_mfma_f32_16x16x32_bf16 v[220:223], a[56:59], v[48:51], v[220:223]\n"
"v_max_f32 v170, v170, v171\n"
"v_mul_f32 v170, 0xbe553b95, v170\n"
"v_mfma_f32_16x16x32_bf16 v[204:207], a[64:67], v[52:55], v[204:207]\n"
"s_waitcnt lgkmcnt(6)\n"
"v_fmamk_f32 v116, v116, 0x3e553b95, v170\n"
"v_fmamk_f32 v117, v117, 0x3e553b95, v170\n"
"v_mfma_f32_16x16x32_bf16 v[232:235], a[240:243], v[72:75], v[232:235]\n"
"v_fmamk_f32 v118, v118, 0x3e553b95, v170\n"
"v_fmamk_f32 v119, v119, 0x3e553b95, v170\n"
"v_mfma_f32_16x16x32_bf16 v[236:239], a[244:247], v[72:75], v[236:239]\n"
"v_exp_f32 v116, v116\n"
"v_mfma_f32_16x16x32_bf16 v[240:243], a[248:251], v[72:75], v[240:243]\n"
"ds_read_b128 v[72:75], v192 offset:41984\n"
"v_fmamk_f32 v136, v136, 0x3e553b95, v170\n"
"v_mfma_f32_16x16x32_bf16 v[232:235], a[252:255], v[76:79], v[232:235]\n"
"v_exp_f32 v117, v117\n"
"v_mfma_f32_16x16x32_bf16 v[240:243], v[4:7], v[76:79], v[240:243]\n"
"v_exp_f32 v118, v118\n"
"v_mfma_f32_16x16x32_bf16 v[236:239], v[0:3], v[76:79], v[236:239]\n"
"v_exp_f32 v119, v119\n"
"v_mfma_f32_16x16x32_bf16 v[232:235], v[8:11], v[68:71], v[232:235]\n"
"s_add_u32 m0, s34, 10240\n"
"v_cvt_pk_bf16_f32 v82, v116, v117\n"
"v_cvt_pk_bf16_f32 v83, v118, v119\n"
"v_mfma_f32_16x16x32_bf16 v[240:243], v[16:19], v[68:71], v[240:243]\n"
"buffer_load_dwordx4 v191, s[8:11], s0 offen lds\n"
"v_and_b32 v116, v82, v176\n"
"v_mfma_f32_16x16x32_bf16 v[236:239], v[12:15], v[68:71], v[236:239]\n"
"s_add_u32 s0, s0, s33\n"
"v_and_b32 v117, v83, v177\n"
"v_xor_b32 v118, v82, v116\n"
"v_mfma_f32_16x16x32_bf16 v[220:223], a[68:71], v[52:55], v[220:223]\n"
"v_xor_b32 v119, v83, v117\n"
"v_fmamk_f32 v137, v137, 0x3e553b95, v170\n"
"v_mfma_f32_16x16x32_bf16 v[224:227], a[72:75], v[32:35], 0\n"
"s_waitcnt lgkmcnt(0)\n"
"v_fmamk_f32 v138, v138, 0x3e553b95, v170\n"
"v_fmamk_f32 v139, v139, 0x3e553b95, v170\n"
"v_mfma_f32_16x16x32_bf16 v[232:235], v[20:23], v[72:75], v[232:235]\n"
"v_exp_f32 v136, v136\n"
"v_mfma_f32_16x16x32_bf16 v[240:243], v[28:31], v[72:75], v[240:243]\n"
"v_exp_f32 v137, v137\n"
"v_mfma_f32_16x16x32_bf16 v[236:239], v[24:27], v[72:75], v[236:239]\n"
"buffer_load_dword v200, off, s[28:31], s39 sc1\n"
"v_cvt_pk_bf16_f32 v84, v136, v137\n"
"v_mfma_f32_16x16x32_bf16 v[244:247], v[244:247], v[116:119], 0\n"
"v_exp_f32 v138, v138\n"
"v_mfma_f32_16x16x32_bf16 v[248:251], v[248:251], v[116:119], 0\n"
"v_exp_f32 v139, v139\n"
"v_mfma_f32_16x16x32_bf16 v[252:255], v[252:255], v[116:119], 0\n"
"ds_read_b64_tr_b16 v[116:117], v184 offset:27136\n"
"ds_read_b64_tr_b16 v[118:119], v185 offset:27136\n"
"v_mfma_f32_16x16x32_bf16 v[224:227], a[84:87], v[36:39], v[224:227]\n"
"s_add_u32 m0, s34, 8192\n"
"v_cvt_pk_bf16_f32 v85, v138, v139\n"
"v_and_b32 v136, v84, v176\n"
"v_mfma_f32_16x16x32_bf16 v[224:227], a[96:99], v[40:43], v[224:227]\n"
"buffer_load_dwordx4 v190, s[8:11], s1 offen lds\n"
"v_and_b32 v137, v85, v177\n"
"v_mfma_f32_16x16x32_bf16 v[224:227], a[108:111], v[44:47], v[224:227]\n"
"v_xor_b32 v138, v84, v136\n"
"v_xor_b32 v139, v85, v137\n"
"v_mfma_f32_16x16x32_bf16 v[82:85], v[172:175], v[82:85], 0\n"
"v_fmamk_f32 v120, v120, 0x3e553b95, v170\n"
"v_fmamk_f32 v121, v121, 0x3e553b95, v170\n"
"v_mfma_f32_16x16x32_bf16 v[244:247], v[124:127], v[136:139], v[244:247]\n"
"v_fmamk_f32 v122, v122, 0x3e553b95, v170\n"
"v_fmamk_f32 v123, v123, 0x3e553b95, v170\n"
"v_mfma_f32_16x16x32_bf16 v[248:251], v[132:135], v[136:139], v[248:251]\n"
"ds_read_b64_tr_b16 v[124:125], v184 offset:27080\n"
"ds_read_b64_tr_b16 v[126:127], v185 offset:27080\n"
"v_mfma_f32_16x16x32_bf16 v[252:255], v[140:143], v[136:139], v[252:255]\n"
"ds_read_b64_tr_b16 v[136:137], v184 offset:27144\n"
"v_cvt_pk_bf16_f32 v232, v232, v233\n"
"v_mfma_f32_16x16x32_bf16 v[224:227], a[120:123], v[48:51], v[224:227]\n"
"ds_read_b64_tr_b16 v[138:139], v185 offset:27144\n"
"v_cvt_pk_bf16_f32 v233, v234, v235\n"
"v_mfma_f32_16x16x32_bf16 v[224:227], a[132:135], v[52:55], v[224:227]\n"
"v_exp_f32 v120, v120\n"
"v_exp_f32 v121, v121\n"
"v_exp_f32 v122, v122\n"
"v_exp_f32 v123, v123\n"
"v_cvt_pk_bf16_f32 v90, v120, v121\n"
"v_cvt_pk_bf16_f32 v91, v122, v123\n"
"s_add_u32 m0, s34, 11264\n"
"v_cvt_pk_bf16_f32 v240, v240, v241\n"
"buffer_load_dwordx4 v191, s[8:11], s1 offen lds\n"
"s_add_u32 s1, s1, s33\n"
"v_cvt_pk_bf16_f32 v234, v236, v237\n"
"v_cvt_pk_bf16_f32 v235, v238, v239\n"
"v_cvt_pk_bf16_f32 v241, v242, v243\n"
"v_and_b32 v120, v90, v176\n"
"v_and_b32 v121, v91, v177\n"
"v_xor_b32 v122, v90, v120\n"
"v_xor_b32 v123, v91, v121\n"
"v_fmamk_f32 v128, v128, 0x3e553b95, v170\n"
"v_cvt_pk_bf16_f32 v152, v228, v229\n"
"v_mfma_f32_16x16x32_bf16 v[244:247], v[112:115], v[120:123], v[244:247]\n"
"ds_write_b128 v196, v[232:235] offset:55296\n"
"v_mfma_f32_16x16x32_bf16 v[248:251], v[98:101], v[120:123], v[248:251]\n"
"v_fmamk_f32 v129, v129, 0x3e553b95, v170\n"
"v_fmamk_f32 v130, v130, 0x3e553b95, v170\n"
"v_mfma_f32_16x16x32_bf16 v[252:255], v[106:109], v[120:123], v[252:255]\n"
".Lic5%=:\n"
"s_cbranch_execz .Lib1%=\n"
"v_cvt_pk_bf16_f32 v153, v230, v231\n"
"v_fmamk_f32 v131, v131, 0x3e553b95, v170\n"
"v_mfma_f32_16x16x32_bf16 v[228:231], a[76:79], v[32:35], 0\n"
"ds_write_b64 v197, v[240:241] offset:55296\n"
"v_mfma_f32_16x16x32_bf16 v[228:231], a[88:91], v[36:39], v[228:231]\n"
"v_cvt_pk_bf16_f32 v154, v216, v217\n"
"v_cvt_pk_bf16_f32 v155, v218, v219\n"
"v_mfma_f32_16x16x32_bf16 v[216:219], a[80:83], v[32:35], 0\n"
"v_exp_f32 v128, v128\n"
"v_mfma_f32_16x16x32_bf16 v[216:219], a[92:95], v[36:39], v[216:219]\n"
"s_waitcnt vmcnt(11)\n"
"v_exp_f32 v129, v129\n"
"v_mfma_f32_16x16x32_bf16 v[228:231], a[100:103], v[40:43], v[228:231]\n"
"v_or_b32 v201, 8, s6\n"
"v_cvt_pk_bf16_f32 v166, v208, v209\n"
"v_mfma_f32_16x16x32_bf16 v[216:219], a[104:107], v[40:43], v[216:219]\n"
"s_mov_b64 exec, 1\n"
"buffer_store_dword v201, off, s[28:31], s40 sc1\n"
"s_mov_b64 exec, -1\n"
"v_mfma_f32_16x16x32_bf16 v[228:231], a[112:115], v[44:47], v[228:231]\n"
"v_exp_f32 v130, v130\n"
"v_mfma_f32_16x16x32_bf16 v[216:219], a[116:119], v[44:47], v[216:219]\n"
"s_waitcnt lgkmcnt(0)\n"
"v_cvt_pk_bf16_f32 v167, v210, v211\n"
"v_cvt_pk_bf16_f32 v92, v128, v129\n"
"v_mfma_f32_16x16x32_bf16 v[228:231], a[124:127], v[48:51], v[228:231]\n"
"s_barrier\n"
"v_mfma_f32_16x16x32_bf16 v[216:219], a[128:131], v[48:51], v[216:219]\n"
"ds_read_b128 v[72:75], v192 offset:43008\n"
"v_and_b32 v128, v92, v176\n"
"v_mfma_f32_16x16x32_bf16 v[228:231], a[136:139], v[52:55], v[228:231]\n"
"v_exp_f32 v131, v131\n"
"v_mfma_f32_16x16x32_bf16 v[216:219], a[140:143], v[52:55], v[216:219]\n"
"v_cvt_pk_bf16_f32 v93, v130, v131\n"
"v_and_b32 v129, v93, v177\n"
"v_mfma_f32_16x16x32_bf16 v[208:211], a[144:147], v[32:35], 0\n"
"v_xor_b32 v130, v92, v128\n"
"v_xor_b32 v131, v93, v129\n"
"v_mfma_f32_16x16x32_bf16 v[90:93], v[172:175], v[90:93], v[82:85]\n"
"ds_read_b128 v[132:135], v180\n"
"ds_read_b128 v[140:143], v181\n"
"v_mfma_f32_16x16x32_bf16 v[244:247], v[116:119], v[128:131], v[244:247]\n"
"ds_read_b64 v[82:83], v180 offset:64\n"
"s_mov_b64 exec, s[44:45]\n"
"v_add_u32 v179, 0xc0, v179\n"
"s_mov_b64 exec, -1\n"
"v_mfma_f32_16x16x32_bf16 v[248:251], v[136:139], v[128:131], v[248:251]\n"
"ds_read_b64 v[84:85], v181 offset:64\n"
"ds_read_b128 v[120:123], v180 offset:13504\n"
"v_mfma_f32_16x16x32_bf16 v[252:255], v[124:127], v[128:131], v[252:255]\n"
"s_waitcnt lgkmcnt(5)\n"
"v_rcp_f32 v171, v90\n"
"v_mfma_f32_16x16x32_bf16 v[232:235], a[216:219], v[72:75], 0\n"
"ds_read_b128 v[112:115], v181 offset:13504\n"
"ds_read_b64 v[90:91], v180 offset:13568\n"
"v_mfma_f32_16x16x32_bf16 v[236:239], a[220:223], v[72:75], 0\n"
"ds_read_b64 v[92:93], v181 offset:13568\n"
"v_mul_f32 v244, v171, v244\n"
"v_mfma_f32_16x16x32_bf16 v[240:243], a[224:227], v[72:75], 0\n"
"ds_read_b128 v[72:75], v192 offset:46080\n"
"v_mul_f32 v245, v171, v245\n"
"v_mfma_f32_16x16x32_bf16 v[208:211], a[156:159], v[36:39], v[208:211]\n"
"s_waitcnt lgkmcnt(8)\n"
"ds_read_b128 v[128:131], v180 offset:27008\n"
"v_mul_f32 v246, v171, v246\n"
"v_mfma_f32_16x16x32_bf16 v[132:135], v[132:135], v[144:147], 0\n"
"s_waitcnt lgkmcnt(8)\n"
"ds_read_b128 v[116:119], v181 offset:27008\n"
"v_mul_f32 v247, v171, v247\n"
"v_mfma_f32_16x16x32_bf16 v[140:143], v[140:143], v[144:147], 0\n"
"s_waitcnt lgkmcnt(8)\n"
"ds_write_b128 v202, v[62:65] offset:30720\n"
"v_mfma_f32_16x16x32_bf16 v[132:135], v[80:83], v[156:159], v[132:135]\n"
"s_waitcnt lgkmcnt(7)\n"
"v_cvt_pk_bf16_f32 v244, v244, v245\n"
"v_cvt_pk_bf16_f32 v245, v246, v247\n"
"v_mfma_f32_16x16x32_bf16 v[140:143], v[84:87], v[158:161], v[140:143]\n"
"s_waitcnt lgkmcnt(6)\n"
"v_mul_f32 v248, v171, v248\n"
"v_mul_f32 v249, v171, v249\n"
"v_mfma_f32_16x16x32_bf16 v[120:123], v[120:123], v[144:147], 0\n"
"s_waitcnt lgkmcnt(5)\n"
"ds_read_b64 v[98:99], v180 offset:27072\n"
"ds_read_b64 v[100:101], v181 offset:27072\n"
"v_mfma_f32_16x16x32_bf16 v[112:115], v[112:115], v[144:147], 0\n"
"s_waitcnt lgkmcnt(6)\n"
"v_mul_f32 v250, v171, v250\n"
"v_mul_f32 v251, v171, v251\n"
"v_mfma_f32_16x16x32_bf16 v[120:123], v[88:91], v[156:159], v[120:123]\n"
"ds_read_b128 v[136:139], v178\n"
"ds_read_b128 v[124:127], v179\n"
"v_mfma_f32_16x16x32_bf16 v[112:115], v[92:95], v[158:161], v[112:115]\n"
"s_waitcnt lgkmcnt(6)\n"
"v_cvt_pk_bf16_f32 v246, v248, v249\n"
"v_cvt_pk_bf16_f32 v247, v250, v251\n"
"v_mfma_f32_16x16x32_bf16 v[208:211], a[168:171], v[40:43], v[208:211]\n"
"s_waitcnt lgkmcnt(5)\n"
"buffer_store_dwordx4 v[244:247], v192, s[28:31], s37 offen sc1\n"
"v_mfma_f32_16x16x32_bf16 v[128:131], v[128:131], v[144:147], 0\n"
"ds_read_b64 v[106:107], v178 offset:64\n"
"v_mul_f32 v252, v171, v252\n"
"v_mfma_f32_16x16x32_bf16 v[116:119], v[116:119], v[144:147], 0\n"
"ds_read_b128 v[76:79], v202 offset:55296\n"
"v_mul_f32 v253, v171, v253\n"
"v_mfma_f32_16x16x32_bf16 v[208:211], a[180:183], v[44:47], v[208:211]\n"
"s_waitcnt lgkmcnt(5)\n"
"ds_read_b64 v[108:109], v179 offset:64\n"
"v_mul_f32 v254, v171, v254\n"
"v_mfma_f32_16x16x32_bf16 v[128:131], v[96:99], v[156:159], v[128:131]\n"
"s_waitcnt lgkmcnt(5)\n"
"v_mul_f32 v255, v171, v255\n"
"v_cvt_pk_bf16_f32 v252, v252, v253\n"
"v_mfma_f32_16x16x32_bf16 v[116:119], v[100:103], v[158:161], v[116:119]\n"
"s_waitcnt lgkmcnt(4)\n"
"v_cvt_pk_bf16_f32 v253, v254, v255\n"
"s_mov_b64 exec, s[46:47]\n"
"v_add_u32 v183, 0xc0, v183\n"
"s_mov_b64 exec, -1\n"
"v_mfma_f32_16x16x32_bf16 v[136:139], v[136:139], v[144:147], 0\n"
"s_waitcnt lgkmcnt(3)\n"
"buffer_store_dwordx2 v[252:253], v193, s[28:31], s37 offen offset:1024 sc1\n"
"v_mfma_f32_16x16x32_bf16 v[124:127], v[124:127], v[144:147], 0\n"
"s_add_u32 s37, s37, 6144\n"
"v_cvt_pk_bf16_f32 v212, v212, v213\n"
"v_cvt_pk_bf16_f32 v213, v214, v215\n"
"v_mfma_f32_16x16x32_bf16 v[208:211], a[192:195], v[48:51], v[208:211]\n"
"s_waitcnt lgkmcnt(2)\n"
"ds_read_b128 v[68:71], v202 offset:59392\n"
"ds_read_b64_tr_b16 v[248:249], v184 offset:128\n"
"v_mfma_f32_16x16x32_bf16 v[136:139], v[104:107], v[156:159], v[136:139]\n"
"s_waitcnt lgkmcnt(3)\n"
"ds_read_b64_tr_b16 v[250:251], v185 offset:128\n"
"buffer_store_dwordx4 v[76:79], v194, s[24:27], s36 offen sc1\n"
"v_mfma_f32_16x16x32_bf16 v[208:211], a[204:207], v[52:55], v[208:211]\n"
"s_waitcnt lgkmcnt(3)\n"
"ds_read_b128 v[76:79], v192 offset:44032\n"
"ds_read_b64_tr_b16 v[244:245], v184 offset:136\n"
"v_mfma_f32_16x16x32_bf16 v[124:127], v[108:111], v[158:161], v[124:127]\n"
"v_cvt_pk_bf16_f32 v214, v204, v205\n"
"v_cvt_pk_bf16_f32 v215, v206, v207\n"
"v_mfma_f32_16x16x32_bf16 v[204:207], a[148:151], v[32:35], 0\n"
"ds_write_b128 v186, v[212:215] offset:20256\n"
"v_mfma_f32_16x16x32_bf16 v[212:215], a[152:155], v[32:35], 0\n"
"v_cvt_pk_bf16_f32 v220, v220, v221\n"
"v_cvt_pk_bf16_f32 v221, v222, v223\n"
"v_mfma_f32_16x16x32_bf16 v[204:207], a[160:163], v[36:39], v[204:207]\n"
"s_waitcnt lgkmcnt(5)\n"
"v_max_f32 v132, v132, v140\n"
"v_max_f32 v133, v133, v141\n"
"v_mfma_f32_16x16x32_bf16 v[212:215], a[164:167], v[36:39], v[212:215]\n"
"ds_read_b128 v[32:35], v188\n"
"ds_read_b128 v[36:39], v189\n"
"v_mfma_f32_16x16x32_bf16 v[204:207], a[172:175], v[40:43], v[204:207]\n"
"ds_read_b64_tr_b16 v[246:247], v185 offset:136\n"
"buffer_store_dwordx4 v[68:71], v195, s[24:27], s36 offen sc1\n"
"v_mfma_f32_16x16x32_bf16 v[212:215], a[176:179], v[40:43], v[212:215]\n"
"s_waitcnt lgkmcnt(5)\n"
"ds_read_b128 v[68:71], v192 offset:45056\n"
"ds_read_b128 v[40:43], v188 offset:1024\n"
"v_mfma_f32_16x16x32_bf16 v[232:235], a[228:231], v[76:79], v[232:235]\n"
"s_add_u32 s36, s36, s33\n"
"ds_read_b64_tr_b16 v[252:253], v184 offset:72\n"
"v_max_f32 v134, v134, v142\n"
"v_mfma_f32_16x16x32_bf16 v[236:239], a[232:235], v[76:79], v[236:239]\n"
"ds_read_b64_tr_b16 v[254:255], v185 offset:72\n"
"v_max_f32 v135, v135, v143\n"
"v_mfma_f32_16x16x32_bf16 v[240:243], a[236:239], v[76:79], v[240:243]\n"
"ds_read_b128 v[76:79], v192 offset:47104\n"
"v_max_f32 v170, v132, v133\n"
"v_mfma_f32_16x16x32_bf16 v[204:207], a[184:187], v[44:47], v[204:207]\n"
"ds_read_b64_tr_b16 v[140:141], v184 offset:13632\n"
"v_max_f32 v171, v134, v135\n"
"v_mfma_f32_16x16x32_bf16 v[212:215], a[188:191], v[44:47], v[212:215]\n"
"ds_read_b128 v[44:47], v189 offset:1024\n"
"v_max_f32 v120, v120, v112\n"
"v_mfma_f32_16x16x32_bf16 v[204:207], a[196:199], v[48:51], v[204:207]\n"
"s_waitcnt lgkmcnt(8)\n"
"ds_write_b64 v203, v[66:67] offset:30720\n"
"v_mfma_f32_16x16x32_bf16 v[212:215], a[200:203], v[48:51], v[212:215]\n"
"s_waitcnt lgkmcnt(7)\n"
"v_max_f32 v121, v121, v113\n"
"v_max_f32 v122, v122, v114\n"
"v_mfma_f32_16x16x32_bf16 v[204:207], a[208:211], v[52:55], v[204:207]\n"
"s_add_u32 m0, s34, 0\n"
"ds_read_b128 v[48:51], v188 offset:2048\n"
"buffer_load_dwordx4 v190, s[8:11], s32 offen lds\n"
"v_mfma_f32_16x16x32_bf16 v[232:235], a[240:243], v[68:71], v[232:235]\n"
"s_add_u32 s32, s32, s33\n"
"ds_read_b64_tr_b16 v[142:143], v185 offset:13632\n"
"ds_read_b64_tr_b16 v[112:113], v184 offset:13640\n"
"v_mfma_f32_16x16x32_bf16 v[236:239], a[244:247], v[68:71], v[236:239]\n"
"ds_read_b64_tr_b16 v[98:99], v184 offset:27144\n"
"v_max_f32 v123, v123, v115\n"
"v_mfma_f32_16x16x32_bf16 v[240:243], a[248:251], v[68:71], v[240:243]\n"
"ds_read_b128 v[68:71], v192 offset:48128\n"
"v_max3_f32 v170, v170, v120, v121\n"
"v_mfma_f32_16x16x32_bf16 v[232:235], a[252:255], v[72:75], v[232:235]\n"
"s_waitcnt lgkmcnt(8)\n"
"ds_read_b64_tr_b16 v[114:115], v185 offset:13640\n"
"v_max3_f32 v171, v171, v122, v123\n"
"v_mfma_f32_16x16x32_bf16 v[240:243], v[4:7], v[72:75], v[240:243]\n"
"ds_read_b64_tr_b16 v[100:101], v185 offset:27144\n"
"v_max_f32 v128, v128, v116\n"
"v_mfma_f32_16x16x32_bf16 v[236:239], v[0:3], v[72:75], v[236:239]\n"
"ds_read_b64_tr_b16 v[106:107], v184 offset:27080\n"
"v_max_f32 v129, v129, v117\n"
"v_mfma_f32_16x16x32_bf16 v[232:235], v[8:11], v[76:79], v[232:235]\n"
"ds_read_b64_tr_b16 v[116:117], v184 offset:13576\n"
"v_max_f32 v130, v130, v118\n"
"v_mfma_f32_16x16x32_bf16 v[240:243], v[16:19], v[76:79], v[240:243]\n"
"ds_read_b64_tr_b16 v[108:109], v185 offset:27080\n"
"v_max_f32 v131, v131, v119\n"
"v_mfma_f32_16x16x32_bf16 v[236:239], v[12:15], v[76:79], v[236:239]\n"
"ds_read_b64_tr_b16 v[118:119], v185 offset:13576\n"
"v_max3_f32 v170, v170, v128, v129\n"
"v_mfma_f32_16x16x32_bf16 v[212:215], a[212:215], v[52:55], v[212:215]\n"
"s_waitcnt lgkmcnt(12)\n"
"ds_read_b128 v[52:55], v189 offset:2048\n"
"v_max3_f32 v171, v171, v130, v131\n"
"v_cvt_pk_bf16_f32 v224, v224, v225\n"
"v_max_f32 v136, v136, v124\n"
"v_cvt_pk_bf16_f32 v225, v226, v227\n"
"v_max_f32 v137, v137, v125\n"
"v_max_f32 v138, v138, v126\n"
"v_cvt_pk_bf16_f32 v226, v228, v229\n"
"v_max_f32 v139, v139, v127\n"
"v_cvt_pk_bf16_f32 v227, v230, v231\n"
"v_mfma_f32_16x16x32_bf16 v[228:231], a[0:3], v[32:35], 0\n"
"s_waitcnt vmcnt(8) lgkmcnt(7)\n"
"v_add3_u32 v200, v200, s50, -7\n"
"v_cmp_lt_u32 vcc, 64, v200\n"
"v_mfma_f32_16x16x32_bf16 v[232:235], v[20:23], v[68:71], v[232:235]\n"
"s_add_u32 m0, s34, 3072\n"
"ds_read_b64_tr_b16 v[124:125], v184 offset:27136\n"
"buffer_load_dwordx4 v199, s[8:11], s75 offen lds\n"
"v_mfma_f32_16x16x32_bf16 v[240:243], v[28:31], v[68:71], v[240:243]\n"
"s_cbranch_vccnz .Lp6%=\n"
".Lr6%=:\n"
"v_mfma_f32_16x16x32_bf16 v[236:239], v[24:27], v[68:71], v[236:239]\n"
"ds_read_b64_tr_b16 v[126:127], v185 offset:27136\n"
"ds_write_b128 v186, v[224:227] offset:20384\n"
"v_mfma_f32_16x16x32_bf16 v[228:231], a[12:15], v[36:39], v[228:231]\n"
"buffer_load_dwordx4 v[62:65], v192, s[28:31], s38 offen sc1\n"
"v_max3_f32 v170, v170, v136, v137\n"
"v_mfma_f32_16x16x32_bf16 v[228:231], a[24:27], v[40:43], v[228:231]\n"
"v_max3_f32 v171, v171, v138, v139\n"
"v_cvt_pk_bf16_f32 v222, v216, v217\n"
"v_mfma_f32_16x16x32_bf16 v[228:231], a[36:39], v[44:47], v[228:231]\n"
"v_cvt_pk_bf16_f32 v223, v218, v219\n"
"v_max_f32 v170, v170, v171\n"
"v_mfma_f32_16x16x32_bf16 v[216:219], a[4:7], v[32:35], 0\n"
"ds_write_b128 v186, v[220:223] offset:20320\n"
"v_mfma_f32_16x16x32_bf16 v[220:223], a[8:11], v[32:35], 0\n"
"buffer_load_dwordx2 v[66:67], v193, s[28:31], s38 offen offset:1024 sc1\n"
"v_mov_b32 v171, v170\n"
"v_mfma_f32_16x16x32_bf16 v[216:219], a[16:19], v[36:39], v[216:219]\n"
"s_add_u32 s38, s38, 6144\n"
"v_permlane16_swap_b32 v170, v171\n"
"v_mfma_f32_16x16x32_bf16 v[220:223], a[20:23], v[36:39], v[220:223]\n"
"s_waitcnt lgkmcnt(4)\n"
"v_max_f32 v170, v170, v171\n"
"v_mov_b32 v171, v170\n"
"v_mfma_f32_16x16x32_bf16 v[216:219], a[28:31], v[40:43], v[216:219]\n"
"s_add_u32 m0, s34, 1024\n"
"v_permlane32_swap_b32 v170, v171\n"
"v_mfma_f32_16x16x32_bf16 v[220:223], a[32:35], v[40:43], v[220:223]\n"
"buffer_load_dwordx4 v190, s[8:11], s0 offen lds\n"
"v_max_f32 v170, v170, v171\n"
"v_mfma_f32_16x16x32_bf16 v[216:219], a[40:43], v[44:47], v[216:219]\n"
"s_add_u32 s0, s0, s33\n"
"v_mul_f32 v170, 0xbe553b95, v170\n"
"v_fmamk_f32 v132, v132, 0x3e553b95, v170\n"
"v_mfma_f32_16x16x32_bf16 v[220:223], a[44:47], v[44:47], v[220:223]\n"
"v_fmamk_f32 v133, v133, 0x3e553b95, v170\n"
"v_fmamk_f32 v134, v134, 0x3e553b95, v170\n"
"v_mfma_f32_16x16x32_bf16 v[228:231], a[48:51], v[48:51], v[228:231]\n"
"v_fmamk_f32 v135, v135, 0x3e553b95, v170\n"
"v_fmamk_f32 v120, v120, 0x3e553b95, v170\n"
"v_mfma_f32_16x16x32_bf16 v[216:219], a[52:55], v[48:51], v[216:219]\n"
"v_exp_f32 v132, v132\n"
"v_mfma_f32_16x16x32_bf16 v[220:223], a[56:59], v[48:51], v[220:223]\n"
"v_exp_f32 v133, v133\n"
"v_mfma_f32_16x16x32_bf16 v[228:231], a[60:63], v[52:55], v[228:231]\n"
"v_exp_f32 v134, v134\n"
"v_mfma_f32_16x16x32_bf16 v[216:219], a[64:67], v[52:55], v[216:219]\n"
"v_exp_f32 v135, v135\n"
"v_mfma_f32_16x16x32_bf16 v[220:223], a[68:71], v[52:55], v[220:223]\n"
"v_cvt_pk_bf16_f32 v82, v132, v133\n"
"v_cvt_pk_bf16_f32 v83, v134, v135\n"
"v_mfma_f32_16x16x32_bf16 v[224:227], a[72:75], v[32:35], 0\n"
"v_and_b32 v132, v82, v176\n"
"v_and_b32 v133, v83, v177\n"
"v_mfma_f32_16x16x32_bf16 v[224:227], a[84:87], v[36:39], v[224:227]\n"
"s_add_u32 m0, s34, 4096\n"
"v_xor_b32 v134, v82, v132\n"
"v_xor_b32 v135, v83, v133\n"
"v_mfma_f32_16x16x32_bf16 v[224:227], a[96:99], v[40:43], v[224:227]\n"
"buffer_load_dwordx4 v199, s[8:11], s76 offen lds\n"
"v_fmamk_f32 v121, v121, 0x3e553b95, v170\n"
"v_mfma_f32_16x16x32_bf16 v[248:251], v[248:251], v[132:135], 0\n"
"v_fmamk_f32 v122, v122, 0x3e553b95, v170\n"
"v_fmamk_f32 v123, v123, 0x3e553b95, v170\n"
"v_mfma_f32_16x16x32_bf16 v[244:247], v[244:247], v[132:135], 0\n"
"v_exp_f32 v120, v120\n"
"v_mfma_f32_16x16x32_bf16 v[252:255], v[252:255], v[132:135], 0\n"
"buffer_load_dword v200, off, s[28:31], s39 sc1\n"
"ds_read_b64_tr_b16 v[132:133], v182 offset:128\n"
"v_mfma_f32_16x16x32_bf16 v[224:227], a[108:111], v[44:47], v[224:227]\n"
"v_exp_f32 v121, v121\n"
"v_mfma_f32_16x16x32_bf16 v[224:227], a[120:123], v[48:51], v[224:227]\n"
"v_exp_f32 v122, v122\n"
"v_mfma_f32_16x16x32_bf16 v[224:227], a[132:135], v[52:55], v[224:227]\n"
"v_exp_f32 v123, v123\n"
"v_cvt_pk_bf16_f32 v84, v120, v121\n"
"v_cvt_pk_bf16_f32 v85, v122, v123\n"
"v_and_b32 v120, v84, v176\n"
"v_and_b32 v121, v85, v177\n"
"v_xor_b32 v122, v84, v120\n"
"v_xor_b32 v123, v85, v121\n"
"v_mfma_f32_16x16x32_bf16 v[82:85], v[172:175], v[82:85], 0\n"
"ds_read_b64_tr_b16 v[134:135], v183 offset:128\n"
"v_fmamk_f32 v128, v128, 0x3e553b95, v170\n"
"v_mfma_f32_16x16x32_bf16 v[248:251], v[140:143], v[120:123], v[248:251]\n"
"v_fmamk_f32 v129, v129, 0x3e553b95, v170\n"
"v_fmamk_f32 v130, v130, 0x3e553b95, v170\n"
"v_mfma_f32_16x16x32_bf16 v[244:247], v[112:115], v[120:123], v[244:247]\n"
"s_add_u32 m0, s34, 2048\n"
"ds_read_b64_tr_b16 v[140:141], v182 offset:72\n"
"buffer_load_dwordx4 v190, s[8:11], s1 offen lds\n"
"v_mfma_f32_16x16x32_bf16 v[252:255], v[116:119], v[120:123], v[252:255]\n"
"s_add_u32 s1, s1, s33\n"
"v_fmamk_f32 v131, v131, 0x3e553b95, v170\n"
"v_exp_f32 v128, v128\n"
"v_exp_f32 v129, v129\n"
"v_exp_f32 v130, v130\n"
"v_exp_f32 v131, v131\n"
"v_cvt_pk_bf16_f32 v232, v232, v233\n"
"v_cvt_pk_bf16_f32 v233, v234, v235\n"
"v_cvt_pk_bf16_f32 v240, v240, v241\n"
"v_cvt_pk_bf16_f32 v234, v236, v237\n"
"v_cvt_pk_bf16_f32 v235, v238, v239\n"
"v_cvt_pk_bf16_f32 v241, v242, v243\n"
"v_cvt_pk_bf16_f32 v90, v128, v129\n"
"v_cvt_pk_bf16_f32 v91, v130, v131\n"
"v_and_b32 v128, v90, v176\n"
"v_and_b32 v129, v91, v177\n"
"v_xor_b32 v130, v90, v128\n"
"v_cvt_pk_bf16_f32 v144, v208, v209\n"
"v_xor_b32 v131, v91, v129\n"
"v_fmamk_f32 v136, v136, 0x3e553b95, v170\n"
"v_cvt_pk_bf16_f32 v145, v210, v211\n"
"v_mfma_f32_16x16x32_bf16 v[244:247], v[98:101], v[128:131], v[244:247]\n"
"s_waitcnt lgkmcnt(5)\n"
"ds_write_b128 v196, v[232:235] offset:49152\n"
"v_mfma_f32_16x16x32_bf16 v[248:251], v[124:127], v[128:131], v[248:251]\n"
"v_fmamk_f32 v137, v137, 0x3e553b95, v170\n"
"v_cvt_pk_bf16_f32 v146, v204, v205\n"
"v_mfma_f32_16x16x32_bf16 v[252:255], v[106:109], v[128:131], v[252:255]\n"
"s_add_u32 m0, s34, 5120\n"
"v_fmamk_f32 v138, v138, 0x3e553b95, v170\n"
"v_fmamk_f32 v139, v139, 0x3e553b95, v170\n"
"v_mfma_f32_16x16x32_bf16 v[208:211], a[76:79], v[32:35], 0\n"
"buffer_load_dwordx4 v199, s[8:11], s77 offen lds\n"
"ds_write_b64 v197, v[240:241] offset:49152\n"
"v_mfma_f32_16x16x32_bf16 v[208:211], a[88:91], v[36:39], v[208:211]\n"
"s_mul_i32 s8, s33, 0\n"
"v_cvt_pk_bf16_f32 v147, v206, v207\n"
"v_cvt_pk_bf16_f32 v158, v212, v213\n"
"v_mfma_f32_16x16x32_bf16 v[204:207], a[80:83], v[32:35], 0\n"
"s_mul_i32 s9, s33, 1\n"
"ds_read_b64_tr_b16 v[120:121], v182 offset:136\n"
"ds_read_b64_tr_b16 v[122:123], v183 offset:136\n"
"v_mfma_f32_16x16x32_bf16 v[204:207], a[92:95], v[36:39], v[204:207]\n"
"s_mul_i32 s10, s33, 3\n"
"ds_read_b64_tr_b16 v[142:143], v183 offset:72\n"
"v_cvt_pk_bf16_f32 v159, v214, v215\n"
"v_mfma_f32_16x16x32_bf16 v[208:211], a[100:103], v[40:43], v[208:211]\n"
"s_mul_i32 s11, s33, 2\n"
"v_exp_f32 v136, v136\n"
"v_mfma_f32_16x16x32_bf16 v[204:207], a[104:107], v[40:43], v[204:207]\n"
"v_exp_f32 v137, v137\n"
"v_mfma_f32_16x16x32_bf16 v[208:211], a[112:115], v[44:47], v[208:211]\n"
"v_exp_f32 v138, v138\n"
"v_mfma_f32_16x16x32_bf16 v[204:207], a[116:119], v[44:47], v[204:207]\n"
"s_waitcnt lgkmcnt(3)\n"
"v_exp_f32 v139, v139\n"
"v_mfma_f32_16x16x32_bf16 v[208:211], a[124:127], v[48:51], v[208:211]\n"
"s_barrier\n"
"v_mfma_f32_16x16x32_bf16 v[204:207], a[128:131], v[48:51], v[204:207]\n"
"ds_read_b128 v[68:71], v192 offset:30720\n"
"v_cvt_pk_bf16_f32 v92, v136, v137\n"
"v_mfma_f32_16x16x32_bf16 v[208:211], a[136:139], v[52:55], v[208:211]\n"
"v_cvt_pk_bf16_f32 v93, v138, v139\n"
"v_and_b32 v136, v92, v176\n"
"v_mfma_f32_16x16x32_bf16 v[204:207], a[140:143], v[52:55], v[204:207]\n"
"v_and_b32 v137, v93, v177\n"
"v_xor_b32 v138, v92, v136\n"
"v_mfma_f32_16x16x32_bf16 v[212:215], a[144:147], v[32:35], 0\n"
"v_xor_b32 v139, v93, v137\n"
"v_cvt_pk_bf16_f32 v228, v228, v229\n"
"v_mfma_f32_16x16x32_bf16 v[90:93], v[172:175], v[90:93], v[82:85]\n"
"s_waitcnt lgkmcnt(1)\n"
"ds_read_b128 v[112:115], v180 offset:13504\n"
"ds_read_b128 v[116:119], v181 offset:13504\n"
"v_mfma_f32_16x16x32_bf16 v[248:251], v[132:135], v[136:139], v[248:251]\n"
"ds_read_b64 v[82:83], v180 offset:13568\n"
"v_cvt_pk_bf16_f32 v229, v230, v231\n"
"v_mfma_f32_16x16x32_bf16 v[244:247], v[120:123], v[136:139], v[244:247]\n"
"ds_read_b64 v[84:85], v181 offset:13568\n"
"v_rcp_f32 v171, v90\n"
"v_mfma_f32_16x16x32_bf16 v[252:255], v[140:143], v[136:139], v[252:255]\n"
"s_waitcnt lgkmcnt(4)\n"
"ds_read_b128 v[128:131], v180 offset:27008\n"
"ds_read_b128 v[124:127], v181 offset:27008\n"
"v_mfma_f32_16x16x32_bf16 v[212:215], a[156:159], v[36:39], v[212:215]\n"
"v_mul_f32 v248, v171, v248\n"
"v_mul_f32 v249, v171, v249\n"
"v_mfma_f32_16x16x32_bf16 v[232:235], a[216:219], v[68:71], 0\n"
"ds_read_b64 v[90:91], v180 offset:27072\n"
"ds_read_b64 v[92:93], v181 offset:27072\n"
"v_mfma_f32_16x16x32_bf16 v[236:239], a[220:223], v[68:71], 0\n"
"v_mul_f32 v250, v171, v250\n"
"v_mul_f32 v251, v171, v251\n"
"v_mfma_f32_16x16x32_bf16 v[240:243], a[224:227], v[68:71], 0\n"
"s_waitcnt lgkmcnt(7)\n"
"ds_read_b128 v[68:71], v192 offset:33792\n"
"ds_read_b128 v[136:139], v178\n"
"v_mfma_f32_16x16x32_bf16 v[212:215], a[168:171], v[40:43], v[212:215]\n"
"s_waitcnt lgkmcnt(8)\n"
"v_cvt_pk_bf16_f32 v248, v248, v249\n"
"v_cvt_pk_bf16_f32 v249, v250, v251\n"
"v_mfma_f32_16x16x32_bf16 v[112:115], v[112:115], v[148:151], 0\n"
"s_waitcnt lgkmcnt(7)\n"
"ds_read_b128 v[132:135], v179\n"
"ds_read_b64 v[98:99], v178 offset:64\n"
"v_mfma_f32_16x16x32_bf16 v[116:119], v[116:119], v[148:151], 0\n"
"s_waitcnt lgkmcnt(8)\n"
"v_mul_f32 v244, v171, v244\n"
"v_mul_f32 v245, v171, v245\n"
"v_mfma_f32_16x16x32_bf16 v[112:115], v[80:83], v[160:163], v[112:115]\n"
"s_waitcnt lgkmcnt(7)\n"
"ds_read_b64 v[100:101], v179 offset:64\n"
"ds_write_b128 v202, v[56:59] offset:55296\n"
"v_mfma_f32_16x16x32_bf16 v[116:119], v[84:87], v[162:165], v[116:119]\n"
"s_waitcnt lgkmcnt(8)\n"
"v_mul_f32 v246, v171, v246\n"
"v_mul_f32 v247, v171, v247\n"
"v_mfma_f32_16x16x32_bf16 v[128:131], v[128:131], v[148:151], 0\n"
"s_waitcnt lgkmcnt(7)\n"
"v_cvt_pk_bf16_f32 v250, v244, v245\n"
"v_cvt_pk_bf16_f32 v251, v246, v247\n"
"v_mfma_f32_16x16x32_bf16 v[124:127], v[124:127], v[148:151], 0\n"
"s_waitcnt lgkmcnt(6)\n"
"buffer_store_dwordx4 v[248:251], v192, s[28:31], s37 offen sc1\n"
"v_mfma_f32_16x16x32_bf16 v[128:131], v[88:91], v[160:163], v[128:131]\n"
"s_waitcnt lgkmcnt(4)\n"
"ds_read_b128 v[120:123], v178 offset:13504\n"
"ds_read_b128 v[140:143], v179 offset:13504\n"
"v_mfma_f32_16x16x32_bf16 v[124:127], v[92:95], v[162:165], v[124:127]\n"
"s_waitcnt lgkmcnt(5)\n"
"ds_read_b64 v[106:107], v178 offset:13568\n"
"v_mul_f32 v252, v171, v252\n"
"v_mfma_f32_16x16x32_bf16 v[136:139], v[136:139], v[148:151], 0\n"
"s_waitcnt lgkmcnt(5)\n"
"ds_read_b64 v[108:109], v179 offset:13568\n"
"v_mul_f32 v253, v171, v253\n"
"v_mfma_f32_16x16x32_bf16 v[132:135], v[132:135], v[148:151], 0\n"
"s_waitcnt lgkmcnt(5)\n"
"ds_read_b128 v[72:75], v202 offset:49152\n"
"v_mul_f32 v254, v171, v254\n"
"v_mfma_f32_16x16x32_bf16 v[136:139], v[96:99], v[160:163], v[136:139]\n"
"v_mul_f32 v255, v171, v255\n"
"v_cvt_pk_bf16_f32 v252, v252, v253\n"
"v_mfma_f32_16x16x32_bf16 v[132:135], v[100:103], v[162:165], v[132:135]\n"
"v_cvt_pk_bf16_f32 v253, v254, v255\n"
"v_cvt_pk_bf16_f32 v230, v216, v217\n"
"v_mfma_f32_16x16x32_bf16 v[212:215], a[180:183], v[44:47], v[212:215]\n"
"buffer_store_dwordx2 v[252:253], v193, s[28:31], s37 offen offset:1024 sc1\n"
"v_mfma_f32_16x16x32_bf16 v[212:215], a[192:195], v[48:51], v[212:215]\n"
"s_waitcnt lgkmcnt(4)\n"
"ds_read_b64_tr_b16 v[244:245], v184 offset:13632\n"
"ds_read_b64_tr_b16 v[246:247], v185 offset:13632\n"
"v_mfma_f32_16x16x32_bf16 v[212:215], a[204:207], v[52:55], v[212:215]\n"
"s_waitcnt lgkmcnt(5)\n"
"ds_read_b64_tr_b16 v[248:249], v184 offset:13640\n"
"v_cvt_pk_bf16_f32 v231, v218, v219\n"
"v_mfma_f32_16x16x32_bf16 v[120:123], v[120:123], v[148:151], 0\n"
"s_waitcnt lgkmcnt(5)\n"
"buffer_load_dwordx4 a[204:207], v192, s[28:31], s38 offen sc1\n"
"ds_write_b128 v186, v[228:231] offset:33760\n"
"v_mfma_f32_16x16x32_bf16 v[140:143], v[140:143], v[148:151], 0\n"
"s_waitcnt lgkmcnt(4)\n"
"v_cvt_pk_bf16_f32 v220, v220, v221\n"
"v_cvt_pk_bf16_f32 v221, v222, v223\n"
"v_mfma_f32_16x16x32_bf16 v[120:123], v[104:107], v[160:163], v[120:123]\n"
"s_add_u32 s37, s37, 6144\n"
"v_max_f32 v112, v112, v116\n"
"v_max_f32 v113, v113, v117\n"
"v_mfma_f32_16x16x32_bf16 v[140:143], v[108:111], v[162:165], v[140:143]\n"
"buffer_store_dwordx4 v[72:75], v194, s[24:27], s36 offen sc1\n"
"v_mfma_f32_16x16x32_bf16 v[216:219], a[148:151], v[32:35], 0\n"
"s_nop 0\n"
"ds_read_b128 v[72:75], v192 offset:31744\n"
"ds_read_b64_tr_b16 v[250:251], v185 offset:13640\n"
"v_mfma_f32_16x16x32_bf16 v[228:231], a[152:155], v[32:35], 0\n"
"ds_read_b128 v[32:35], v188 offset:6144\n"
"v_max_f32 v114, v114, v118\n"
"v_mfma_f32_16x16x32_bf16 v[216:219], a[160:163], v[36:39], v[216:219]\n"
"ds_read_b64_tr_b16 v[252:253], v184 offset:13576\n"
"v_max_f32 v115, v115, v119\n"
"v_mfma_f32_16x16x32_bf16 v[228:231], a[164:167], v[36:39], v[228:231]\n"
"ds_read_b128 v[36:39], v189 offset:6144\n"
"v_max_f32 v170, v112, v113\n"
"v_mfma_f32_16x16x32_bf16 v[216:219], a[172:175], v[40:43], v[216:219]\n"
"ds_read_b128 v[76:79], v202 offset:53248\n"
"v_max_f32 v171, v114, v115\n"
"v_mfma_f32_16x16x32_bf16 v[228:231], a[176:179], v[40:43], v[228:231]\n"
"ds_read_b128 v[40:43], v188 offset:7168\n"
"v_max_f32 v128, v128, v124\n"
"v_mfma_f32_16x16x32_bf16 v[216:219], a[184:187], v[44:47], v[216:219]\n"
"ds_read_b64_tr_b16 v[254:255], v185 offset:13576\n"
"v_max_f32 v129, v129, v125\n"
"v_mfma_f32_16x16x32_bf16 v[228:231], a[188:191], v[44:47], v[228:231]\n"
"ds_read_b128 v[44:47], v189 offset:7168\n"
"v_max_f32 v130, v130, v126\n"
"v_mfma_f32_16x16x32_bf16 v[216:219], a[196:199], v[48:51], v[216:219]\n"
"s_waitcnt lgkmcnt(8)\n"
"ds_read_b64_tr_b16 v[116:117], v184 offset:27136\n"
"v_max_f32 v131, v131, v127\n"
"v_mfma_f32_16x16x32_bf16 v[232:235], a[228:231], v[72:75], v[232:235]\n"
"ds_read_b64_tr_b16 v[118:119], v185 offset:27136\n"
"v_max3_f32 v170, v170, v128, v129\n"
"v_mfma_f32_16x16x32_bf16 v[236:239], a[232:235], v[72:75], v[236:239]\n"
"ds_read_b64_tr_b16 v[124:125], v184 offset:27144\n"
"v_max3_f32 v171, v171, v130, v131\n"
"v_mfma_f32_16x16x32_bf16 v[240:243], a[236:239], v[72:75], v[240:243]\n"
"ds_read_b128 v[72:75], v192 offset:34816\n"
"v_max_f32 v136, v136, v132\n"
"v_mfma_f32_16x16x32_bf16 v[228:231], a[200:203], v[48:51], v[228:231]\n"
"s_waitcnt vmcnt(18)\n"
"ds_read_b128 v[48:51], v188 offset:8192\n"
"v_max_f32 v137, v137, v133\n"
"v_mfma_f32_16x16x32_bf16 v[216:219], a[208:211], v[52:55], v[216:219]\n"
"s_waitcnt lgkmcnt(8)\n"
"buffer_load_dwordx2 a[208:209], v193, s[28:31], s38 offen offset:1024 sc1\n"
"ds_read_b64_tr_b16 v[126:127], v185 offset:27144\n"
"v_mfma_f32_16x16x32_bf16 v[228:231], a[212:215], v[52:55], v[228:231]\n"
"s_add_u32 s38, s38, 6144\n"
"ds_read_b128 v[52:55], v189 offset:8192\n"
"v_max_f32 v138, v138, v134\n"
"v_max_f32 v139, v139, v135\n"
"v_max3_f32 v170, v170, v136, v137\n"
"v_max3_f32 v171, v171, v138, v139\n"
"v_max_f32 v120, v120, v140\n"
"v_max_f32 v121, v121, v141\n"
"v_max_f32 v122, v122, v142\n"
"buffer_store_dwordx4 v[76:79], v195, s[24:27], s36 offen sc1\n"
"s_nop 1\n"
"ds_read_b128 v[76:79], v192 offset:32768\n"
"v_max_f32 v123, v123, v143\n"
"v_cvt_pk_bf16_f32 v224, v224, v225\n"
"v_max3_f32 v170, v170, v120, v121\n"
"v_cvt_pk_bf16_f32 v225, v226, v227\n"
"v_max3_f32 v171, v171, v122, v123\n"
"v_cvt_pk_bf16_f32 v226, v208, v209\n"
"v_cvt_pk_bf16_f32 v227, v210, v211\n"
"v_mfma_f32_16x16x32_bf16 v[208:211], a[0:3], v[32:35], 0\n"
"s_add_u32 s36, s36, s33\n"
"ds_write_b64 v203, v[60:61] offset:55296\n"
"v_mfma_f32_16x16x32_bf16 v[208:211], a[12:15], v[36:39], v[208:211]\n"
"s_waitcnt lgkmcnt(11)\n"
"v_cvt_pk_bf16_f32 v222, v204, v205\n"
"v_cvt_pk_bf16_f32 v223, v206, v207\n"
"v_mfma_f32_16x16x32_bf16 v[204:207], a[4:7], v[32:35], 0\n"
"ds_read_b64_tr_b16 v[132:133], v184 offset:27080\n"
"ds_read_b64_tr_b16 v[134:135], v185 offset:27080\n"
"v_mfma_f32_16x16x32_bf16 v[204:207], a[16:19], v[36:39], v[204:207]\n"
"ds_read_b64_tr_b16 v[140:141], v182 offset:128\n"
"v_max_f32 v170, v170, v171\n"
"v_mfma_f32_16x16x32_bf16 v[208:211], a[24:27], v[40:43], v[208:211]\n"
"s_waitcnt lgkmcnt(12)\n"
"ds_write_b128 v186, v[220:223] offset:33824\n"
"v_mfma_f32_16x16x32_bf16 v[220:223], a[8:11], v[32:35], 0\n"
"s_waitcnt lgkmcnt(5)\n"
"v_mov_b32 v171, v170\n"
"v_cvt_pk_bf16_f32 v148, v212, v213\n"
"v_mfma_f32_16x16x32_bf16 v[232:235], a[240:243], v[76:79], v[232:235]\n"
"v_permlane16_swap_b32 v170, v171\n"
"v_mfma_f32_16x16x32_bf16 v[236:239], a[244:247], v[76:79], v[236:239]\n"
"ds_read_b64_tr_b16 v[142:143], v183 offset:128\n"
"ds_read_b64_tr_b16 v[98:99], v182 offset:136\n"
"v_mfma_f32_16x16x32_bf16 v[240:243], a[248:251], v[76:79], v[240:243]\n"
"ds_read_b128 v[76:79], v192 offset:35840\n"
"v_max_f32 v170, v170, v171\n"
"v_mfma_f32_16x16x32_bf16 v[232:235], a[252:255], v[68:71], v[232:235]\n"
"ds_read_b64_tr_b16 v[100:101], v183 offset:136\n"
"v_mov_b32 v171, v170\n"
"v_mfma_f32_16x16x32_bf16 v[240:243], v[4:7], v[68:71], v[240:243]\n"
"ds_read_b64_tr_b16 v[106:107], v182 offset:72\n"
"v_permlane32_swap_b32 v170, v171\n"
"v_mfma_f32_16x16x32_bf16 v[236:239], v[0:3], v[68:71], v[236:239]\n"
"s_waitcnt vmcnt(8)\n"
"ds_read_b64_tr_b16 v[108:109], v183 offset:72\n"
"v_add3_u32 v200, v200, s50, -8\n"
"v_mfma_f32_16x16x32_bf16 v[232:235], v[8:11], v[72:75], v[232:235]\n"
"v_cmp_lt_u32 vcc, 64, v200\n"
"v_max_f32 v170, v170, v171\n"
"v_mfma_f32_16x16x32_bf16 v[240:243], v[16:19], v[72:75], v[240:243]\n"
"ds_write_b128 v186, v[224:227] offset:33888\n"
"v_mfma_f32_16x16x32_bf16 v[236:239], v[12:15], v[72:75], v[236:239]\n"
"s_nop 0\n"
"s_cbranch_vccnz .Lp7%=\n"
".Lr7%=:\n"
"v_mfma_f32_16x16x32_bf16 v[220:223], a[20:23], v[36:39], v[220:223]\n"
".Lic6%=:\n"
"s_cbranch_execz .Lib1%=\n"
"buffer_load_dwordx4 a[210:213], v192, s[28:31], s38 offen sc1\n"
"v_mul_f32 v170, 0xbe553b95, v170\n"
"v_mfma_f32_16x16x32_bf16 v[204:207], a[28:31], v[40:43], v[204:207]\n"
"s_waitcnt lgkmcnt(4)\n"
"v_fmamk_f32 v112, v112, 0x3e553b95, v170\n"
"v_fmamk_f32 v113, v113, 0x3e553b95, v170\n"
"v_mfma_f32_16x16x32_bf16 v[232:235], v[20:23], v[76:79], v[232:235]\n"
"v_fmamk_f32 v114, v114, 0x3e553b95, v170\n"
"v_fmamk_f32 v115, v115, 0x3e553b95, v170\n"
"v_mfma_f32_16x16x32_bf16 v[240:243], v[28:31], v[76:79], v[240:243]\n"
"buffer_load_dwordx2 a[214:215], v193, s[28:31], s38 offen offset:1024 sc1\n"
"v_fmamk_f32 v128, v128, 0x3e553b95, v170\n"
"v_mfma_f32_16x16x32_bf16 v[236:239], v[24:27], v[76:79], v[236:239]\n"
"s_add_u32 s38, s38, 6144\n"
"v_exp_f32 v112, v112\n"
"v_mfma_f32_16x16x32_bf16 v[220:223], a[32:35], v[40:43], v[220:223]\n"
"s_add_u32 s12, s38, 0\n"
"v_exp_f32 v113, v113\n"
"v_mfma_f32_16x16x32_bf16 v[208:211], a[36:39], v[44:47], v[208:211]\n"
"s_add_u32 s13, s38, 6144\n"
"v_exp_f32 v114, v114\n"
"v_mfma_f32_16x16x32_bf16 v[204:207], a[40:43], v[44:47], v[204:207]\n"
"s_add_u32 s15, s38, 12288\n"
"v_exp_f32 v115, v115\n"
"v_mfma_f32_16x16x32_bf16 v[220:223], a[44:47], v[44:47], v[220:223]\n"
"s_add_u32 s14, s38, 18432\n"
"v_cvt_pk_bf16_f32 v82, v112, v113\n"
"v_cvt_pk_bf16_f32 v83, v114, v115\n"
"v_mfma_f32_16x16x32_bf16 v[208:211], a[48:51], v[48:51], v[208:211]\n"
"s_add_u32 s16, s38, 24576\n"
"v_and_b32 v112, v82, v176\n"
"v_and_b32 v113, v83, v177\n"
"v_mfma_f32_16x16x32_bf16 v[204:207], a[52:55], v[48:51], v[204:207]\n"
"s_add_u32 s17, s38, 30720\n"
"v_xor_b32 v114, v82, v112\n"
"v_xor_b32 v115, v83, v113\n"
"v_mfma_f32_16x16x32_bf16 v[220:223], a[56:59], v[48:51], v[220:223]\n"
"v_fmamk_f32 v129, v129, 0x3e553b95, v170\n"
"v_fmamk_f32 v130, v130, 0x3e553b95, v170\n"
"v_mfma_f32_16x16x32_bf16 v[244:247], v[244:247], v[112:115], 0\n"
"v_fmamk_f32 v131, v131, 0x3e553b95, v170\n"
"v_fmamk_f32 v136, v136, 0x3e553b95, v170\n"
"v_mfma_f32_16x16x32_bf16 v[248:251], v[248:251], v[112:115], 0\n"
"v_exp_f32 v128, v128\n"
"v_mfma_f32_16x16x32_bf16 v[252:255], v[252:255], v[112:115], 0\n"
"v_exp_f32 v129, v129\n"
"v_mfma_f32_16x16x32_bf16 v[208:211], a[60:63], v[52:55], v[208:211]\n"
"v_exp_f32 v130, v130\n"
"v_mfma_f32_16x16x32_bf16 v[204:207], a[64:67], v[52:55], v[204:207]\n"
"v_exp_f32 v131, v131\n"
"v_mfma_f32_16x16x32_bf16 v[220:223], a[68:71], v[52:55], v[220:223]\n"
"ds_read_b64_tr_b16 v[112:113], v182 offset:13632\n"
"ds_read_b64_tr_b16 v[114:115], v183 offset:13632\n"
"v_mfma_f32_16x16x32_bf16 v[224:227], a[72:75], v[32:35], 0\n"
"v_cvt_pk_bf16_f32 v84, v128, v129\n"
"v_cvt_pk_bf16_f32 v85, v130, v131\n"
"v_mfma_f32_16x16x32_bf16 v[224:227], a[84:87], v[36:39], v[224:227]\n"
"v_and_b32 v128, v84, v176\n"
"v_and_b32 v129, v85, v177\n"
"v_mfma_f32_16x16x32_bf16 v[224:227], a[96:99], v[40:43], v[224:227]\n"
"v_xor_b32 v130, v84, v128\n"
"v_xor_b32 v131, v85, v129\n"
"v_mfma_f32_16x16x32_bf16 v[82:85], v[172:175], v[82:85], 0\n"
"v_fmamk_f32 v137, v137, 0x3e553b95, v170\n"
"v_fmamk_f32 v138, v138, 0x3e553b95, v170\n"
"v_mfma_f32_16x16x32_bf16 v[244:247], v[116:119], v[128:131], v[244:247]\n"
"ds_read_b64_tr_b16 v[116:117], v182 offset:13576\n"
"v_fmamk_f32 v139, v139, 0x3e553b95, v170\n"
"v_mfma_f32_16x16x32_bf16 v[248:251], v[124:127], v[128:131], v[248:251]\n"
"ds_read_b64_tr_b16 v[118:119], v183 offset:13576\n"
"v_cvt_pk_bf16_f32 v232, v232, v233\n"
"v_mfma_f32_16x16x32_bf16 v[252:255], v[132:135], v[128:131], v[252:255]\n"
"ds_read_b64_tr_b16 v[128:129], v182 offset:13640\n"
"ds_read_b64_tr_b16 v[130:131], v183 offset:13640\n"
"v_mfma_f32_16x16x32_bf16 v[224:227], a[108:111], v[44:47], v[224:227]\n"
"v_exp_f32 v136, v136\n"
"v_mfma_f32_16x16x32_bf16 v[224:227], a[120:123], v[48:51], v[224:227]\n"
"v_exp_f32 v137, v137\n"
"v_mfma_f32_16x16x32_bf16 v[224:227], a[132:135], v[52:55], v[224:227]\n"
"v_exp_f32 v138, v138\n"
"v_exp_f32 v139, v139\n"
"v_cvt_pk_bf16_f32 v90, v136, v137\n"
"v_cvt_pk_bf16_f32 v91, v138, v139\n"
"v_cvt_pk_bf16_f32 v233, v234, v235\n"
"s_waitcnt vmcnt(6)\n"
"v_or_b32 v201, 10, s6\n"
"s_mov_b64 exec, 1\n"
"buffer_store_dword v201, off, s[28:31], s40 sc1\n"
"s_mov_b64 exec, -1\n"
"v_cvt_pk_bf16_f32 v240, v240, v241\n"
"v_cvt_pk_bf16_f32 v234, v236, v237\n"
"v_cvt_pk_bf16_f32 v235, v238, v239\n"
"v_cvt_pk_bf16_f32 v241, v242, v243\n"
"v_and_b32 v136, v90, v176\n"
"v_and_b32 v137, v91, v177\n"
"v_xor_b32 v138, v90, v136\n"
"v_xor_b32 v139, v91, v137\n"
"v_fmamk_f32 v120, v120, 0x3e553b95, v170\n"
"v_fmamk_f32 v121, v121, 0x3e553b95, v170\n"
"v_mfma_f32_16x16x32_bf16 v[244:247], v[140:143], v[136:139], v[244:247]\n"
"s_waitcnt lgkmcnt(7)\n"
"ds_write_b128 v196, v[232:235] offset:36864\n"
"v_mfma_f32_16x16x32_bf16 v[248:251], v[98:101], v[136:139], v[248:251]\n"
"v_fmamk_f32 v122, v122, 0x3e553b95, v170\n"
"v_cvt_pk_bf16_f32 v149, v214, v215\n"
"v_mfma_f32_16x16x32_bf16 v[252:255], v[106:109], v[136:139], v[252:255]\n"
"v_fmamk_f32 v123, v123, 0x3e553b95, v170\n"
"v_cvt_pk_bf16_f32 v150, v216, v217\n"
"v_mfma_f32_16x16x32_bf16 v[212:215], a[76:79], v[32:35], 0\n"
"ds_write_b64 v197, v[240:241] offset:36864\n"
"v_mfma_f32_16x16x32_bf16 v[212:215], a[88:91], v[36:39], v[212:215]\n"
"v_exp_f32 v120, v120\n"
"v_mfma_f32_16x16x32_bf16 v[212:215], a[100:103], v[40:43], v[212:215]\n"
"v_cvt_pk_bf16_f32 v151, v218, v219\n"
"v_cvt_pk_bf16_f32 v162, v228, v229\n"
"v_mfma_f32_16x16x32_bf16 v[216:219], a[80:83], v[32:35], 0\n"
"ds_read_b128 v[32:35], v188\n"
"v_cvt_pk_bf16_f32 v163, v230, v231\n"
"v_mfma_f32_16x16x32_bf16 v[216:219], a[92:95], v[36:39], v[216:219]\n"
"ds_read_b128 v[36:39], v189\n"
"ds_read_b128 v[124:127], v180 offset:27008\n"
"v_mfma_f32_16x16x32_bf16 v[216:219], a[104:107], v[40:43], v[216:219]\n"
"ds_read_b128 v[40:43], v188 offset:1024\n"
"v_exp_f32 v121, v121\n"
"v_mfma_f32_16x16x32_bf16 v[212:215], a[112:115], v[44:47], v[212:215]\n"
"v_exp_f32 v122, v122\n"
"v_mfma_f32_16x16x32_bf16 v[216:219], a[116:119], v[44:47], v[216:219]\n"
"s_waitcnt lgkmcnt(4)\n"
"ds_read_b128 v[44:47], v189 offset:1024\n"
"v_cvt_pk_bf16_f32 v92, v120, v121\n"
"v_mfma_f32_16x16x32_bf16 v[212:215], a[124:127], v[48:51], v[212:215]\n"
"s_barrier\n"
"v_mfma_f32_16x16x32_bf16 v[216:219], a[128:131], v[48:51], v[216:219]\n"
"ds_read_b128 v[76:79], v192 offset:55296\n"
"ds_read_b128 v[48:51], v188 offset:2048\n"
"v_mfma_f32_16x16x32_bf16 v[212:215], a[136:139], v[52:55], v[212:215]\n"
"s_waitcnt lgkmcnt(6)\n"
"v_exp_f32 v123, v123\n"
"v_mfma_f32_16x16x32_bf16 v[216:219], a[140:143], v[52:55], v[216:219]\n"
"ds_read_b128 v[52:55], v189 offset:2048\n"
"v_cvt_pk_bf16_f32 v93, v122, v123\n"
"v_mfma_f32_16x16x32_bf16 v[228:231], a[0:3], v[32:35], 0\n"
"s_waitcnt lgkmcnt(5)\n"
"v_and_b32 v120, v92, v176\n"
"v_and_b32 v121, v93, v177\n"
"v_mfma_f32_16x16x32_bf16 v[228:231], a[12:15], v[36:39], v[228:231]\n"
"s_waitcnt lgkmcnt(4)\n"
"v_xor_b32 v122, v92, v120\n"
"v_xor_b32 v123, v93, v121\n"
"v_mfma_f32_16x16x32_bf16 v[90:93], v[172:175], v[90:93], v[82:85]\n"
"ds_read_b128 v[132:135], v181 offset:27008\n"
"ds_read_b128 v[136:139], v178\n"
"v_mfma_f32_16x16x32_bf16 v[244:247], v[112:115], v[120:123], v[244:247]\n"
"ds_read_b64 v[82:83], v180 offset:27072\n"
"v_cvt_pk_bf16_f32 v208, v208, v209\n"
"v_mfma_f32_16x16x32_bf16 v[248:251], v[128:131], v[120:123], v[248:251]\n"
"ds_read_b64 v[84:85], v181 offset:27072\n"
"v_cvt_pk_bf16_f32 v209, v210, v211\n"
"v_mfma_f32_16x16x32_bf16 v[252:255], v[116:119], v[120:123], v[252:255]\n"
"s_waitcnt lgkmcnt(6)\n"
"ds_read_b128 v[140:143], v179\n"
"v_rcp_f32 v171, v90\n"
"v_mfma_f32_16x16x32_bf16 v[124:127], v[124:127], v[152:155], 0\n"
"ds_read_b64 v[90:91], v178 offset:64\n"
"ds_read_b64 v[92:93], v179 offset:64\n"
"v_mfma_f32_16x16x32_bf16 v[232:235], a[216:219], v[76:79], 0\n"
"v_mul_f32 v244, v171, v244\n"
"v_mul_f32 v245, v171, v245\n"
"v_mfma_f32_16x16x32_bf16 v[236:239], a[220:223], v[76:79], 0\n"
"ds_read_b128 v[120:123], v178 offset:13504\n"
"ds_read_b128 v[112:115], v179 offset:13504\n"
"v_mfma_f32_16x16x32_bf16 v[240:243], a[224:227], v[76:79], 0\n"
"s_waitcnt lgkmcnt(8)\n"
"v_mul_f32 v246, v171, v246\n"
"v_mul_f32 v247, v171, v247\n"
"v_mfma_f32_16x16x32_bf16 v[228:231], a[24:27], v[40:43], v[228:231]\n"
"s_waitcnt lgkmcnt(7)\n"
"ds_read_b128 v[76:79], v192 offset:58368\n"
"ds_read_b64 v[98:99], v178 offset:13568\n"
"v_mfma_f32_16x16x32_bf16 v[132:135], v[132:135], v[152:155], 0\n"
"s_waitcnt lgkmcnt(8)\n"
"v_cvt_pk_bf16_f32 v244, v244, v245\n"
"v_cvt_pk_bf16_f32 v245, v246, v247\n"
"v_mfma_f32_16x16x32_bf16 v[136:139], v[136:139], v[152:155], 0\n"
"s_waitcnt lgkmcnt(7)\n"
"ds_read_b64 v[100:101], v179 offset:13568\n"
"ds_write_b128 v202, v[62:65] offset:30720\n"
"v_mfma_f32_16x16x32_bf16 v[124:127], v[80:83], v[164:167], v[124:127]\n"
"s_waitcnt lgkmcnt(7)\n"
"v_mul_f32 v248, v171, v248\n"
"v_mul_f32 v249, v171, v249\n"
"v_mfma_f32_16x16x32_bf16 v[132:135], v[84:87], v[166:169], v[132:135]\n"
"v_mul_f32 v250, v171, v250\n"
"v_mul_f32 v251, v171, v251\n"
"v_mfma_f32_16x16x32_bf16 v[140:143], v[140:143], v[152:155], 0\n"
"s_waitcnt lgkmcnt(5)\n"
"ds_read_b128 v[128:131], v178 offset:27008\n"
"ds_read_b128 v[116:119], v179 offset:27008\n"
"v_mfma_f32_16x16x32_bf16 v[136:139], v[88:91], v[164:167], v[136:139]\n"
"s_waitcnt lgkmcnt(6)\n"
"ds_read_b64 v[106:107], v178 offset:27072\n"
"v_cvt_pk_bf16_f32 v246, v248, v249\n"
"v_mfma_f32_16x16x32_bf16 v[140:143], v[92:95], v[166:169], v[140:143]\n"
"ds_read_b64 v[108:109], v179 offset:27072\n"
"v_cvt_pk_bf16_f32 v247, v250, v251\n"
"v_mfma_f32_16x16x32_bf16 v[120:123], v[120:123], v[152:155], 0\n"
"s_waitcnt lgkmcnt(6)\n"
"buffer_store_dwordx4 v[244:247], v192, s[28:31], s37 offen sc1\n"
"v_mfma_f32_16x16x32_bf16 v[112:115], v[112:115], v[152:155], 0\n"
"s_waitcnt lgkmcnt(5)\n"
"v_mul_f32 v252, v171, v252\n"
"v_mul_f32 v253, v171, v253\n"
"v_mfma_f32_16x16x32_bf16 v[120:123], v[96:99], v[164:167], v[120:123]\n"
"ds_read_b128 v[68:71], v202 offset:36864\n"
"v_mul_f32 v254, v171, v254\n"
"v_mfma_f32_16x16x32_bf16 v[112:115], v[100:103], v[166:169], v[112:115]\n"
"v_mul_f32 v255, v171, v255\n"
"v_cvt_pk_bf16_f32 v252, v252, v253\n"
"v_mfma_f32_16x16x32_bf16 v[228:231], a[36:39], v[44:47], v[228:231]\n"
"s_waitcnt lgkmcnt(4)\n"
"v_cvt_pk_bf16_f32 v253, v254, v255\n"
"v_cvt_pk_bf16_f32 v210, v204, v205\n"
"v_mfma_f32_16x16x32_bf16 v[128:131], v[128:131], v[152:155], 0\n"
"s_waitcnt lgkmcnt(3)\n"
"buffer_store_dwordx2 v[252:253], v193, s[28:31], s37 offen offset:1024 sc1\n"
"v_mfma_f32_16x16x32_bf16 v[116:119], v[116:119], v[152:155], 0\n"
"s_waitcnt lgkmcnt(2)\n"
"ds_read_b64_tr_b16 v[248:249], v184 offset:27136\n"
"ds_read_b64_tr_b16 v[250:251], v185 offset:27136\n"
"v_mfma_f32_16x16x32_bf16 v[128:131], v[104:107], v[164:167], v[128:131]\n"
"s_waitcnt lgkmcnt(3)\n"
"ds_read_b64_tr_b16 v[244:245], v184 offset:27144\n"
"v_cvt_pk_bf16_f32 v211, v206, v207\n"
"v_mfma_f32_16x16x32_bf16 v[116:119], v[108:111], v[166:169], v[116:119]\n"
"s_add_u32 s37, s37, 6144\n"
"ds_write_b128 v186, v[208:211] offset:47264\n"
"v_mfma_f32_16x16x32_bf16 v[204:207], a[4:7], v[32:35], 0\n"
"s_waitcnt lgkmcnt(4)\n"
"buffer_store_dwordx4 v[68:71], v194, s[24:27], s36 offen sc1\n"
"v_mfma_f32_16x16x32_bf16 v[208:211], a[8:11], v[32:35], 0\n"
"s_nop 0\n"
"ds_read_b128 v[68:71], v192 offset:56320\n"
"ds_read_b64_tr_b16 v[246:247], v185 offset:27144\n"
"v_mfma_f32_16x16x32_bf16 v[204:207], a[16:19], v[36:39], v[204:207]\n"
"ds_read_b64_tr_b16 v[252:253], v184 offset:27080\n"
"v_cvt_pk_bf16_f32 v220, v220, v221\n"
"v_mfma_f32_16x16x32_bf16 v[208:211], a[20:23], v[36:39], v[208:211]\n"
"ds_read_b64_tr_b16 v[254:255], v185 offset:27080\n"
"v_cvt_pk_bf16_f32 v221, v222, v223\n"
"v_mfma_f32_16x16x32_bf16 v[204:207], a[28:31], v[40:43], v[204:207]\n"
"ds_read_b128 v[72:75], v202 offset:40960\n"
"v_max_f32 v124, v124, v132\n"
"v_mfma_f32_16x16x32_bf16 v[208:211], a[32:35], v[40:43], v[208:211]\n"
"ds_write_b64 v203, v[66:67] offset:30720\n"
"v_mfma_f32_16x16x32_bf16 v[204:207], a[40:43], v[44:47], v[204:207]\n"
"v_max_f32 v125, v125, v133\n"
"v_max_f32 v126, v126, v134\n"
"v_mfma_f32_16x16x32_bf16 v[208:211], a[44:47], v[44:47], v[208:211]\n"
"ds_read_b64_tr_b16 v[132:133], v182 offset:128\n"
"ds_read_b64_tr_b16 v[98:99], v182 offset:13640\n"
"v_mfma_f32_16x16x32_bf16 v[228:231], a[48:51], v[48:51], v[228:231]\n"
"s_waitcnt lgkmcnt(7)\n"
"ds_read_b64_tr_b16 v[100:101], v183 offset:13640\n"
"v_max_f32 v127, v127, v135\n"
"v_mfma_f32_16x16x32_bf16 v[204:207], a[52:55], v[48:51], v[204:207]\n"
"ds_read_b64_tr_b16 v[134:135], v183 offset:128\n"
"v_max_f32 v170, v124, v125\n"
"v_mfma_f32_16x16x32_bf16 v[232:235], a[228:231], v[68:71], v[232:235]\n"
"ds_read_b64_tr_b16 v[106:107], v182 offset:13576\n"
"v_max_f32 v171, v126, v127\n"
"v_mfma_f32_16x16x32_bf16 v[236:239], a[232:235], v[68:71], v[236:239]\n"
"ds_read_b64_tr_b16 v[108:109], v183 offset:13576\n"
"v_max_f32 v136, v136, v140\n"
"v_mfma_f32_16x16x32_bf16 v[240:243], a[236:239], v[68:71], v[240:243]\n"
"s_waitcnt lgkmcnt(7)\n"
"ds_read_b128 v[68:71], v192 offset:59392\n"
"v_max_f32 v137, v137, v141\n"
"v_mfma_f32_16x16x32_bf16 v[208:211], a[56:59], v[48:51], v[208:211]\n"
"buffer_store_dwordx4 v[72:75], v195, s[24:27], s36 offen sc1\n"
"v_mfma_f32_16x16x32_bf16 v[228:231], a[60:63], v[52:55], v[228:231]\n"
"s_add_u32 s36, s36, s33\n"
"ds_read_b128 v[72:75], v192 offset:57344\n"
"ds_read_b64_tr_b16 v[140:141], v182 offset:136\n"
"v_mfma_f32_16x16x32_bf16 v[204:207], a[64:67], v[52:55], v[204:207]\n"
"v_max_f32 v138, v138, v142\n"
"v_max_f32 v139, v139, v143\n"
"v_mfma_f32_16x16x32_bf16 v[208:211], a[68:71], v[52:55], v[208:211]\n"
"v_max3_f32 v170, v170, v136, v137\n"
"v_max3_f32 v171, v171, v138, v139\n"
"v_max_f32 v120, v120, v112\n"
"v_max_f32 v121, v121, v113\n"
"v_max_f32 v122, v122, v114\n"
"v_max_f32 v123, v123, v115\n"
"v_max3_f32 v170, v170, v120, v121\n"
"v_max3_f32 v171, v171, v122, v123\n"
"v_max_f32 v128, v128, v116\n"
"v_max_f32 v129, v129, v117\n"
"v_max_f32 v130, v130, v118\n"
"v_max_f32 v131, v131, v119\n"
"v_max3_f32 v170, v170, v128, v129\n"
"v_max3_f32 v171, v171, v130, v131\n"
"v_max_f32 v170, v170, v171\n"
"v_mov_b32 v171, v170\n"
"v_cvt_pk_bf16_f32 v224, v224, v225\n"
"v_cvt_pk_bf16_f32 v225, v226, v227\n"
"v_permlane16_swap_b32 v170, v171\n"
"v_max_f32 v170, v170, v171\n"
"v_mov_b32 v171, v170\n"
"v_cvt_pk_bf16_f32 v226, v212, v213\n"
"v_cvt_pk_bf16_f32 v227, v214, v215\n"
"v_mfma_f32_16x16x32_bf16 v[212:215], a[72:75], v[32:35], 0\n"
"s_waitcnt lgkmcnt(1)\n"
"ds_read_b64_tr_b16 v[142:143], v183 offset:136\n"
"ds_read_b64_tr_b16 v[112:113], v182 offset:72\n"
"v_mfma_f32_16x16x32_bf16 v[232:235], a[240:243], v[72:75], v[232:235]\n"
"ds_read_b64_tr_b16 v[114:115], v183 offset:72\n"
"v_permlane32_swap_b32 v170, v171\n"
"v_mfma_f32_16x16x32_bf16 v[236:239], a[244:247], v[72:75], v[236:239]\n"
"ds_read_b64_tr_b16 v[116:117], v182 offset:13632\n"
"ds_read_b64_tr_b16 v[118:119], v183 offset:13632\n"
"v_mfma_f32_16x16x32_bf16 v[240:243], a[248:251], v[72:75], v[240:243]\n"
"v_max_f32 v170, v170, v171\n"
"v_mul_f32 v170, 0xbe553b95, v170\n"
"v_mfma_f32_16x16x32_bf16 v[232:235], a[252:255], v[76:79], v[232:235]\n"
"ds_read_b128 v[72:75], v192 offset:60416\n"
"v_fmamk_f32 v124, v124, 0x3e553b95, v170\n"
"v_mfma_f32_16x16x32_bf16 v[240:243], v[4:7], v[76:79], v[240:243]\n"
"v_fmamk_f32 v125, v125, 0x3e553b95, v170\n"
"v_fmamk_f32 v126, v126, 0x3e553b95, v170\n"
"v_mfma_f32_16x16x32_bf16 v[236:239], v[0:3], v[76:79], v[236:239]\n"
"v_fmamk_f32 v127, v127, 0x3e553b95, v170\n"
"v_fmamk_f32 v136, v136, 0x3e553b95, v170\n"
"v_mfma_f32_16x16x32_bf16 v[232:235], v[8:11], v[68:71], v[232:235]\n"
"buffer_load_dword v200, off, s[28:31], s39 sc1\n"
"ds_write_b128 v186, v[224:227] offset:47392\n"
"v_mfma_f32_16x16x32_bf16 v[240:243], v[16:19], v[68:71], v[240:243]\n"
"v_exp_f32 v124, v124\n"
"v_mfma_f32_16x16x32_bf16 v[236:239], v[12:15], v[68:71], v[236:239]\n"
"v_exp_f32 v125, v125\n"
"v_mfma_f32_16x16x32_bf16 v[212:215], a[84:87], v[36:39], v[212:215]\n"
"v_exp_f32 v126, v126\n"
"v_mfma_f32_16x16x32_bf16 v[212:215], a[96:99], v[40:43], v[212:215]\n"
"s_waitcnt lgkmcnt(1)\n"
"v_exp_f32 v127, v127\n"
"v_mfma_f32_16x16x32_bf16 v[212:215], a[108:111], v[44:47], v[212:215]\n"
"v_cvt_pk_bf16_f32 v82, v124, v125\n"
"v_cvt_pk_bf16_f32 v83, v126, v127\n"
"v_mfma_f32_16x16x32_bf16 v[232:235], v[20:23], v[72:75], v[232:235]\n"
"v_and_b32 v124, v82, v176\n"
"v_and_b32 v125, v83, v177\n"
"v_mfma_f32_16x16x32_bf16 v[240:243], v[28:31], v[72:75], v[240:243]\n"
"v_xor_b32 v126, v82, v124\n"
"v_xor_b32 v127, v83, v125\n"
"v_mfma_f32_16x16x32_bf16 v[236:239], v[24:27], v[72:75], v[236:239]\n"
"v_fmamk_f32 v137, v137, 0x3e553b95, v170\n"
"v_fmamk_f32 v138, v138, 0x3e553b95, v170\n"
"v_mfma_f32_16x16x32_bf16 v[248:251], v[248:251], v[124:127], 0\n"
"v_fmamk_f32 v139, v139, 0x3e553b95, v170\n"
"v_fmamk_f32 v120, v120, 0x3e553b95, v170\n"
"v_mfma_f32_16x16x32_bf16 v[244:247], v[244:247], v[124:127], 0\n"
"v_exp_f32 v136, v136\n"
"v_mfma_f32_16x16x32_bf16 v[252:255], v[252:255], v[124:127], 0\n"
"v_exp_f32 v137, v137\n"
"v_mfma_f32_16x16x32_bf16 v[212:215], a[120:123], v[48:51], v[212:215]\n"
"v_exp_f32 v138, v138\n"
"v_mfma_f32_16x16x32_bf16 v[212:215], a[132:135], v[52:55], v[212:215]\n"
"v_exp_f32 v139, v139\n"
"v_cvt_pk_bf16_f32 v84, v136, v137\n"
"v_cvt_pk_bf16_f32 v85, v138, v139\n"
"v_and_b32 v136, v84, v176\n"
"v_and_b32 v137, v85, v177\n"
"v_xor_b32 v138, v84, v136\n"
"v_xor_b32 v139, v85, v137\n"
"v_mfma_f32_16x16x32_bf16 v[82:85], v[172:175], v[82:85], 0\n"
"ds_read_b64_tr_b16 v[124:125], v182 offset:27136\n"
"ds_read_b64_tr_b16 v[126:127], v183 offset:27136\n"
"v_mfma_f32_16x16x32_bf16 v[248:251], v[132:135], v[136:139], v[248:251]\n"
"v_fmamk_f32 v121, v121, 0x3e553b95, v170\n"
"v_fmamk_f32 v122, v122, 0x3e553b95, v170\n"
"v_mfma_f32_16x16x32_bf16 v[244:247], v[140:143], v[136:139], v[244:247]\n"
"ds_read_b64_tr_b16 v[132:133], v182 offset:27080\n"
"ds_read_b64_tr_b16 v[134:135], v183 offset:27080\n"
"v_mfma_f32_16x16x32_bf16 v[252:255], v[112:115], v[136:139], v[252:255]\n"
"v_fmamk_f32 v123, v123, 0x3e553b95, v170\n"
"v_exp_f32 v120, v120\n"
"v_cvt_pk_bf16_f32 v222, v216, v217\n"
"v_exp_f32 v121, v121\n"
"v_cvt_pk_bf16_f32 v223, v218, v219\n"
"v_mfma_f32_16x16x32_bf16 v[216:219], a[76:79], v[32:35], 0\n"
"ds_write_b128 v186, v[220:223] offset:47328\n"
"v_mfma_f32_16x16x32_bf16 v[220:223], a[80:83], v[32:35], 0\n"
"v_exp_f32 v122, v122\n"
"v_mfma_f32_16x16x32_bf16 v[216:219], a[88:91], v[36:39], v[216:219]\n"
"v_exp_f32 v123, v123\n"
"v_mfma_f32_16x16x32_bf16 v[220:223], a[92:95], v[36:39], v[220:223]\n"
"ds_read_b64_tr_b16 v[136:137], v182 offset:27144\n"
"ds_read_b64_tr_b16 v[138:139], v183 offset:27144\n"
"v_mfma_f32_16x16x32_bf16 v[216:219], a[100:103], v[40:43], v[216:219]\n"
"v_cvt_pk_bf16_f32 v90, v120, v121\n"
"v_cvt_pk_bf16_f32 v91, v122, v123\n"
"v_mfma_f32_16x16x32_bf16 v[220:223], a[104:107], v[40:43], v[220:223]\n"
"v_and_b32 v120, v90, v176\n"
"v_and_b32 v121, v91, v177\n"
"v_mfma_f32_16x16x32_bf16 v[216:219], a[112:115], v[44:47], v[216:219]\n"
"v_xor_b32 v122, v90, v120\n"
"v_cvt_pk_bf16_f32 v232, v232, v233\n"
"v_mfma_f32_16x16x32_bf16 v[220:223], a[116:119], v[44:47], v[220:223]\n"
"v_xor_b32 v123, v91, v121\n"
"v_cvt_pk_bf16_f32 v233, v234, v235\n"
"v_mfma_f32_16x16x32_bf16 v[216:219], a[124:127], v[48:51], v[216:219]\n"
"v_cvt_pk_bf16_f32 v240, v240, v241\n"
"v_cvt_pk_bf16_f32 v234, v236, v237\n"
"v_mfma_f32_16x16x32_bf16 v[248:251], v[116:119], v[120:123], v[248:251]\n"
"v_cvt_pk_bf16_f32 v235, v238, v239\n"
"v_cvt_pk_bf16_f32 v241, v242, v243\n"
"v_mfma_f32_16x16x32_bf16 v[244:247], v[98:101], v[120:123], v[244:247]\n"
"ds_write_b128 v196, v[232:235] offset:43008\n"
"v_mfma_f32_16x16x32_bf16 v[252:255], v[106:109], v[120:123], v[252:255]\n"
"v_fmamk_f32 v128, v128, 0x3e553b95, v170\n"
"v_fmamk_f32 v129, v129, 0x3e553b95, v170\n"
"v_mfma_f32_16x16x32_bf16 v[220:223], a[128:131], v[48:51], v[220:223]\n"
"v_fmamk_f32 v130, v130, 0x3e553b95, v170\n"
"v_fmamk_f32 v131, v131, 0x3e553b95, v170\n"
"v_mfma_f32_16x16x32_bf16 v[216:219], a[136:139], v[52:55], v[216:219]\n"
"ds_write_b64 v197, v[240:241] offset:43008\n"
"v_mfma_f32_16x16x32_bf16 v[220:223], a[140:143], v[52:55], v[220:223]\n"
"v_exp_f32 v128, v128\n"
"v_exp_f32 v129, v129\n"
"s_waitcnt vmcnt(3)\n"
"v_or_b32 v201, 11, s6\n"
"s_mov_b64 exec, 1\n"
"buffer_store_dword v201, off, s[28:31], s40 sc1\n"
"s_mov_b64 exec, -1\n"
"v_exp_f32 v130, v130\n"
"v_exp_f32 v131, v131\n"
"v_cvt_pk_bf16_f32 v92, v128, v129\n"
"v_cvt_pk_bf16_f32 v93, v130, v131\n"
"v_and_b32 v128, v92, v176\n"
"v_and_b32 v129, v93, v177\n"
"v_xor_b32 v130, v92, v128\n"
"v_xor_b32 v131, v93, v129\n"
"v_mfma_f32_16x16x32_bf16 v[90:93], v[172:175], v[90:93], v[82:85]\n"
"s_waitcnt lgkmcnt(0)\n"
"ds_read_b128 v[140:143], v178\n"
"ds_read_b128 v[112:115], v179\n"
"v_mfma_f32_16x16x32_bf16 v[248:251], v[124:127], v[128:131], v[248:251]\n"
"s_barrier\n"
"v_mfma_f32_16x16x32_bf16 v[244:247], v[136:139], v[128:131], v[244:247]\n"
"ds_read_b128 v[72:75], v192 offset:30720\n"
"ds_read_b64 v[82:83], v178 offset:64\n"
"v_mfma_f32_16x16x32_bf16 v[252:255], v[132:135], v[128:131], v[252:255]\n"
"v_rcp_f32 v171, v90\n"
"s_mov_b64 exec, s[44:45]\n"
"v_add_u32 v181, 0xc0, v181\n"
"s_mov_b64 exec, -1\n"
"v_mul_f32 v248, v171, v248\n"
"v_mul_f32 v249, v171, v249\n"
"v_mul_f32 v250, v171, v250\n"
"v_mul_f32 v251, v171, v251\n"
"v_cvt_pk_bf16_f32 v248, v248, v249\n"
"v_cvt_pk_bf16_f32 v249, v250, v251\n"
"v_mul_f32 v244, v171, v244\n"
"v_mul_f32 v245, v171, v245\n"
"v_mul_f32 v246, v171, v246\n"
"v_mul_f32 v247, v171, v247\n"
"v_cvt_pk_bf16_f32 v250, v244, v245\n"
"v_cvt_pk_bf16_f32 v251, v246, v247\n"
"v_mul_f32 v252, v171, v252\n"
"v_mul_f32 v253, v171, v253\n"
"v_mul_f32 v254, v171, v254\n"
"v_mul_f32 v255, v171, v255\n"
"v_cvt_pk_bf16_f32 v252, v252, v253\n"
"v_cvt_pk_bf16_f32 v253, v254, v255\n"
"v_min_u32 v248, -2, v248\n"
"v_min_u32 v249, -2, v249\n"
"v_min_u32 v250, -2, v250\n"
"v_min_u32 v251, -2, v251\n"
"v_min_u32 v252, -2, v252\n"
"v_min_u32 v253, -2, v253\n"
"s_mov_b64 exec, s[46:47]\n"
"v_add_u32 v185, 0xc0, v185\n"
"s_mov_b64 exec, -1\n"
"v_cvt_pk_bf16_f32 v228, v228, v229\n"
"v_cvt_pk_bf16_f32 v229, v230, v231\n"
"v_cvt_pk_bf16_f32 v230, v204, v205\n"
"v_cvt_pk_bf16_f32 v231, v206, v207\n"
"v_cvt_pk_bf16_f32 v208, v208, v209\n"
"v_cvt_pk_bf16_f32 v209, v210, v211\n"
"v_cvt_pk_bf16_f32 v212, v212, v213\n"
"v_cvt_pk_bf16_f32 v213, v214, v215\n"
"v_cvt_pk_bf16_f32 v214, v216, v217\n"
"v_cvt_pk_bf16_f32 v215, v218, v219\n"
"v_cvt_pk_bf16_f32 v210, v220, v221\n"
"v_cvt_pk_bf16_f32 v211, v222, v223\n"
"s_waitcnt lgkmcnt(1)\n"
"v_mfma_f32_16x16x32_bf16 v[140:143], v[140:143], v[144:147], 0\n"
"s_waitcnt lgkmcnt(0)\n"
"buffer_store_dwordx4 v[248:251], v192, s[28:31], s37 offen sc1\n"
"v_mfma_f32_16x16x32_bf16 v[112:115], v[112:115], v[144:147], 0\n"
"ds_read_b64 v[84:85], v179 offset:64\n"
"ds_read_b128 v[120:123], v178 offset:13504\n"
"v_mfma_f32_16x16x32_bf16 v[140:143], v[80:83], v[156:159], v[140:143]\n"
"ds_read_b128 v[116:119], v179 offset:13504\n"
"buffer_store_dwordx2 v[252:253], v193, s[28:31], s37 offen offset:1024 sc1\n"
"v_mfma_f32_16x16x32_bf16 v[232:235], a[216:219], v[72:75], 0\n"
"s_add_u32 s37, s37, 6144\n"
"ds_read_b64 v[90:91], v178 offset:13568\n"
"ds_write_b128 v202, a[204:207] offset:55296\n"
"v_mfma_f32_16x16x32_bf16 v[236:239], a[220:223], v[72:75], 0\n"
"v_mfma_f32_16x16x32_bf16 v[240:243], a[224:227], v[72:75], 0\n"
"ds_read_b128 v[72:75], v192 offset:33792\n"
"ds_read_b64 v[92:93], v179 offset:13568\n"
"ds_read_b128 v[128:131], v178 offset:27008\n"
"ds_read_b128 v[124:127], v179 offset:27008\n"
"s_waitcnt lgkmcnt(8)\n"
"v_mfma_f32_16x16x32_bf16 v[112:115], v[84:87], v[158:161], v[112:115]\n"
"s_waitcnt lgkmcnt(7)\n"
"ds_read_b64 v[98:99], v178 offset:27072\n"
"v_mfma_f32_16x16x32_bf16 v[120:123], v[120:123], v[144:147], 0\n"
"s_waitcnt lgkmcnt(7)\n"
"ds_read_b64 v[100:101], v179 offset:27072\n"
"v_mfma_f32_16x16x32_bf16 v[116:119], v[116:119], v[144:147], 0\n"
"s_waitcnt lgkmcnt(7)\n"
"ds_read_b128 v[136:139], v180\n"
"v_max_f32 v140, v140, v112\n"
"v_mfma_f32_16x16x32_bf16 v[120:123], v[88:91], v[156:159], v[120:123]\n"
"ds_read_b128 v[132:135], v181\n"
"v_max_f32 v141, v141, v113\n"
"v_max_f32 v142, v142, v114\n"
"v_max_f32 v143, v143, v115\n"
"v_max_f32 v170, v140, v141\n"
"v_max_f32 v171, v142, v143\n"
"ds_read_b64 v[106:107], v180 offset:64\n"
"ds_read_b64 v[108:109], v181 offset:64\n"
"ds_read_b128 v[76:79], v202 offset:43008\n"
"s_waitcnt lgkmcnt(9)\n"
"v_mfma_f32_16x16x32_bf16 v[116:119], v[92:95], v[158:161], v[116:119]\n"
"s_waitcnt vmcnt(3) lgkmcnt(8)\n"
"ds_write_b128 v198, v[228:231]\n"
"v_mfma_f32_16x16x32_bf16 v[128:131], v[128:131], v[144:147], 0\n"
"s_waitcnt lgkmcnt(7)\n"
"v_add3_u32 v200, v200, s50, -10\n"
"v_cmp_lt_u32 vcc, 64, v200\n"
"v_mfma_f32_16x16x32_bf16 v[124:127], v[124:127], v[144:147], 0\n"
"s_waitcnt lgkmcnt(6)\n"
"v_max_f32 v120, v120, v116\n"
"v_max_f32 v121, v121, v117\n"
"v_mfma_f32_16x16x32_bf16 v[128:131], v[96:99], v[156:159], v[128:131]\n"
"s_waitcnt lgkmcnt(5)\n"
"ds_write_b64 v203, a[208:209] offset:55296\n"
"v_mfma_f32_16x16x32_bf16 v[124:127], v[100:103], v[158:161], v[124:127]\n"
"s_waitcnt lgkmcnt(5)\n"
"v_max_f32 v122, v122, v118\n"
"v_max_f32 v123, v123, v119\n"
"v_mfma_f32_16x16x32_bf16 v[136:139], v[136:139], v[144:147], 0\n"
"s_waitcnt lgkmcnt(4)\n"
"ds_read_b64_tr_b16 v[244:245], v182 offset:128\n"
"ds_read_b64_tr_b16 v[246:247], v183 offset:128\n"
"v_mfma_f32_16x16x32_bf16 v[132:135], v[132:135], v[144:147], 0\n"
"s_waitcnt lgkmcnt(5)\n"
"ds_read_b128 v[68:71], v202 offset:47104\n"
"v_max3_f32 v170, v170, v120, v121\n"
"v_mfma_f32_16x16x32_bf16 v[136:139], v[104:107], v[156:159], v[136:139]\n"
"s_waitcnt lgkmcnt(5)\n"
"ds_read_b64_tr_b16 v[248:249], v182 offset:136\n"
"buffer_store_dwordx4 v[76:79], v194, s[24:27], s36 offen sc1\n"
"v_mfma_f32_16x16x32_bf16 v[132:135], v[108:111], v[158:161], v[132:135]\n"
"s_cbranch_vccnz .Lp9%=\n"
".Lr9%=:\n"
"ds_read_b128 v[76:79], v192 offset:31744\n"
"ds_read_b64_tr_b16 v[250:251], v183 offset:136\n"
"ds_read_b64_tr_b16 v[252:253], v182 offset:72\n"
"v_max3_f32 v171, v171, v122, v123\n"
"v_max_f32 v128, v128, v124\n"
"buffer_load_dwordx4 a[144:147], v192, s[28:31], s12 offen sc1\n"
"ds_read_b64_tr_b16 v[254:255], v183 offset:72\n"
"v_max_f32 v129, v129, v125\n"
"v_max_f32 v130, v130, v126\n"
"v_max_f32 v131, v131, v127\n"
"v_max3_f32 v170, v170, v128, v129\n"
"v_max3_f32 v171, v171, v130, v131\n"
"v_max_f32 v136, v136, v132\n"
"v_max_f32 v137, v137, v133\n"
"v_max_f32 v138, v138, v134\n"
"v_max_f32 v139, v139, v135\n"
"v_max3_f32 v170, v170, v136, v137\n"
"ds_write_b128 v202, a[210:213] offset:36864\n"
"buffer_load_dwordx2 a[148:149], v193, s[28:31], s12 offen offset:1024 sc1\n"
"v_max3_f32 v171, v171, v138, v139\n"
"v_max_f32 v170, v170, v171\n"
"v_mov_b32 v171, v170\n"
"s_nop 1\n"
"v_permlane16_swap_b32 v170, v171\n"
"v_max_f32 v170, v170, v171\n"
"v_mov_b32 v171, v170\n"
"s_waitcnt lgkmcnt(4)\n"
"v_mfma_f32_16x16x32_bf16 v[232:235], a[228:231], v[76:79], v[232:235]\n"
"buffer_load_dwordx4 a[150:153], v192, s[28:31], s13 offen sc1\n"
"ds_read_b64_tr_b16 v[112:113], v182 offset:13632\n"
"v_mfma_f32_16x16x32_bf16 v[236:239], a[232:235], v[76:79], v[236:239]\n"
"ds_read_b64_tr_b16 v[114:115], v183 offset:13632\n"
"ds_read_b64_tr_b16 v[116:117], v182 offset:13640\n"
"v_mfma_f32_16x16x32_bf16 v[240:243], a[236:239], v[76:79], v[240:243]\n"
"ds_read_b128 v[76:79], v192 offset:34816\n"
"v_permlane32_swap_b32 v170, v171\n"
"ds_read_b64_tr_b16 v[118:119], v183 offset:13640\n"
"v_max_f32 v170, v170, v171\n"
"buffer_load_dwordx2 a[154:155], v193, s[28:31], s13 offen offset:1024 sc1\n"
"ds_read_b64_tr_b16 v[124:125], v182 offset:13576\n"
"ds_read_b64_tr_b16 v[126:127], v183 offset:13576\n"
"v_mul_f32 v170, 0xbe553b95, v170\n"
"v_fmamk_f32 v140, v140, 0x3e553b95, v170\n"
"v_fmamk_f32 v141, v141, 0x3e553b95, v170\n"
"ds_read_b64_tr_b16 v[132:133], v182 offset:27136\n"
"v_fmamk_f32 v142, v142, 0x3e553b95, v170\n"
"ds_read_b64_tr_b16 v[134:135], v183 offset:27136\n"
"v_fmamk_f32 v143, v143, 0x3e553b95, v170\n"
".Lic7%=:\n"
"s_cbranch_execz .Lib1%=\n"
"buffer_store_dwordx4 v[68:71], v195, s[24:27], s36 offen sc1\n"
"s_nop 1\n"
"ds_read_b128 v[68:71], v192 offset:32768\n"
"s_add_u32 s36, s36, s33\n"
"ds_read_b64_tr_b16 v[98:99], v182 offset:27144\n"
"ds_read_b64_tr_b16 v[100:101], v183 offset:27144\n"
"v_exp_f32 v140, v140\n"
"ds_read_b64_tr_b16 v[106:107], v182 offset:27080\n"
"v_exp_f32 v141, v141\n"
"ds_read_b64_tr_b16 v[108:109], v183 offset:27080\n"
"ds_write_b64 v203, a[214:215] offset:36864\n"
"v_exp_f32 v142, v142\n"
"v_exp_f32 v143, v143\n"
"v_cvt_pk_bf16_f32 v82, v140, v141\n"
"v_cvt_pk_bf16_f32 v83, v142, v143\n"
"v_and_b32 v140, v82, v176\n"
"v_and_b32 v141, v83, v177\n"
"v_xor_b32 v142, v82, v140\n"
"v_xor_b32 v143, v83, v141\n"
"v_fmamk_f32 v120, v120, 0x3e553b95, v170\n"
"v_fmamk_f32 v121, v121, 0x3e553b95, v170\n"
"v_mfma_f32_16x16x32_bf16 v[244:247], v[244:247], v[140:143], 0\n"
"s_waitcnt lgkmcnt(5)\n"
"ds_write_b128 v198, v[208:211] offset:64\n"
"v_mfma_f32_16x16x32_bf16 v[232:235], a[240:243], v[68:71], v[232:235]\n"
"v_fmamk_f32 v122, v122, 0x3e553b95, v170\n"
"v_fmamk_f32 v123, v123, 0x3e553b95, v170\n"
"v_mfma_f32_16x16x32_bf16 v[236:239], a[244:247], v[68:71], v[236:239]\n"
"v_exp_f32 v120, v120\n"
"v_mfma_f32_16x16x32_bf16 v[240:243], a[248:251], v[68:71], v[240:243]\n"
"ds_read_b128 v[68:71], v192 offset:35840\n"
"ds_write_b128 v198, v[212:215] offset:128\n"
"v_mfma_f32_16x16x32_bf16 v[232:235], a[252:255], v[72:75], v[232:235]\n"
"v_exp_f32 v121, v121\n"
"v_mfma_f32_16x16x32_bf16 v[236:239], v[0:3], v[72:75], v[236:239]\n"
"v_exp_f32 v122, v122\n"
"v_mfma_f32_16x16x32_bf16 v[240:243], v[4:7], v[72:75], v[240:243]\n"
"buffer_load_dword v201, off, s[28:31], s39 sc1\n"
"v_cvt_pk_bf16_f32 v84, v120, v121\n"
"v_mfma_f32_16x16x32_bf16 v[232:235], v[8:11], v[76:79], v[232:235]\n"
"v_exp_f32 v123, v123\n"
"v_mfma_f32_16x16x32_bf16 v[236:239], v[12:15], v[76:79], v[236:239]\n"
"v_cvt_pk_bf16_f32 v85, v122, v123\n"
"v_and_b32 v120, v84, v176\n"
"v_mfma_f32_16x16x32_bf16 v[240:243], v[16:19], v[76:79], v[240:243]\n"
"v_and_b32 v121, v85, v177\n"
"v_xor_b32 v122, v84, v120\n"
"v_mfma_f32_16x16x32_bf16 v[248:251], v[248:251], v[140:143], 0\n"
"s_waitcnt lgkmcnt(1)\n"
"v_xor_b32 v123, v85, v121\n"
"v_fmamk_f32 v128, v128, 0x3e553b95, v170\n"
"v_mfma_f32_16x16x32_bf16 v[252:255], v[252:255], v[140:143], 0\n"
"ds_read_b64_tr_b16 v[140:141], v184 offset:128\n"
"ds_read_b64_tr_b16 v[142:143], v185 offset:128\n"
"v_mfma_f32_16x16x32_bf16 v[244:247], v[112:115], v[120:123], v[244:247]\n"
"s_waitcnt lgkmcnt(2)\n"
"ds_read_b64_tr_b16 v[112:113], v184 offset:72\n"
"v_fmamk_f32 v129, v129, 0x3e553b95, v170\n"
"v_mfma_f32_16x16x32_bf16 v[248:251], v[116:119], v[120:123], v[248:251]\n"
"s_barrier\n"
"v_mfma_f32_16x16x32_bf16 v[232:235], v[20:23], v[68:71], v[232:235]\n"
"ds_read_b128 v[72:75], v192 offset:55296\n"
"ds_read_b128 v[76:79], v192 offset:56320\n"
"v_mfma_f32_16x16x32_bf16 v[252:255], v[124:127], v[120:123], v[252:255]\n"
"ds_read_b64_tr_b16 v[120:121], v184 offset:136\n"
"v_fmamk_f32 v130, v130, 0x3e553b95, v170\n"
"v_mfma_f32_16x16x32_bf16 v[82:85], v[172:175], v[82:85], 0\n"
"ds_read_b64_tr_b16 v[122:123], v185 offset:136\n"
"v_fmamk_f32 v131, v131, 0x3e553b95, v170\n"
"v_mfma_f32_16x16x32_bf16 v[236:239], v[24:27], v[68:71], v[236:239]\n"
"ds_read_b64_tr_b16 v[114:115], v185 offset:72\n"
"v_fmamk_f32 v136, v136, 0x3e553b95, v170\n"
"v_mfma_f32_16x16x32_bf16 v[240:243], v[28:31], v[68:71], v[240:243]\n"
"ds_read_b128 v[68:71], v192 offset:57344\n"
"v_exp_f32 v128, v128\n"
"v_exp_f32 v129, v129\n"
"v_exp_f32 v130, v130\n"
"v_exp_f32 v131, v131\n"
"v_cvt_pk_bf16_f32 v90, v128, v129\n"
"v_cvt_pk_bf16_f32 v91, v130, v131\n"
"v_and_b32 v128, v90, v176\n"
"v_and_b32 v129, v91, v177\n"
"v_xor_b32 v130, v90, v128\n"
"v_xor_b32 v131, v91, v129\n"
"v_fmamk_f32 v137, v137, 0x3e553b95, v170\n"
"v_fmamk_f32 v138, v138, 0x3e553b95, v170\n"
"v_mfma_f32_16x16x32_bf16 v[244:247], v[132:135], v[128:131], v[244:247]\n"
"v_fmamk_f32 v139, v139, 0x3e553b95, v170\n"
"v_exp_f32 v136, v136\n"
"v_mfma_f32_16x16x32_bf16 v[248:251], v[98:101], v[128:131], v[248:251]\n"
"v_exp_f32 v137, v137\n"
"v_mfma_f32_16x16x32_bf16 v[252:255], v[106:109], v[128:131], v[252:255]\n"
"v_exp_f32 v138, v138\n"
"v_exp_f32 v139, v139\n"
"v_cvt_pk_bf16_f32 v92, v136, v137\n"
"v_cvt_pk_bf16_f32 v93, v138, v139\n"
"v_and_b32 v136, v92, v176\n"
"v_and_b32 v137, v93, v177\n"
"v_xor_b32 v138, v92, v136\n"
"v_xor_b32 v139, v93, v137\n"
"v_mfma_f32_16x16x32_bf16 v[90:93], v[172:175], v[90:93], v[82:85]\n"
"s_waitcnt lgkmcnt(1)\n"
"buffer_load_dwordx4 v[228:231], v192, s[28:31], s14 offen sc1\n"
"ds_read_b128 v[116:119], v178 offset:13504\n"
"v_mfma_f32_16x16x32_bf16 v[244:247], v[140:143], v[136:139], v[244:247]\n"
"ds_read_b128 v[124:127], v179 offset:13504\n"
"ds_read_b64 v[82:83], v178 offset:13568\n"
"v_mfma_f32_16x16x32_bf16 v[248:251], v[120:123], v[136:139], v[248:251]\n"
"ds_read_b64 v[84:85], v179 offset:13568\n"
"v_cvt_pk_bf16_f32 v232, v232, v233\n"
"v_mfma_f32_16x16x32_bf16 v[252:255], v[112:115], v[136:139], v[252:255]\n"
"buffer_load_dwordx2 v[152:153], v193, s[28:31], s14 offen offset:1024 sc1\n"
"v_rcp_f32 v171, v90\n"
"v_cvt_pk_bf16_f32 v233, v234, v235\n"
"v_cvt_pk_bf16_f32 v234, v236, v237\n"
"v_mul_f32 v244, v171, v244\n"
"v_cvt_pk_bf16_f32 v235, v238, v239\n"
"v_mfma_f32_16x16x32_bf16 v[236:239], a[220:223], v[72:75], 0\n"
"ds_write_b128 v196, v[232:235] offset:49152\n"
"v_mfma_f32_16x16x32_bf16 v[232:235], a[216:219], v[72:75], 0\n"
"v_mul_f32 v245, v171, v245\n"
"v_mul_f32 v246, v171, v246\n"
"v_mfma_f32_16x16x32_bf16 v[232:235], a[228:231], v[76:79], v[232:235]\n"
"s_waitcnt lgkmcnt(4)\n"
"v_mul_f32 v247, v171, v247\n"
"v_cvt_pk_bf16_f32 v240, v240, v241\n"
"v_mfma_f32_16x16x32_bf16 v[116:119], v[116:119], v[148:151], 0\n"
"s_waitcnt lgkmcnt(3)\n"
"ds_read_b128 v[128:131], v178 offset:27008\n"
"ds_read_b128 v[132:135], v179 offset:27008\n"
"v_mfma_f32_16x16x32_bf16 v[124:127], v[124:127], v[148:151], 0\n"
"s_waitcnt lgkmcnt(4)\n"
"ds_read_b64 v[90:91], v178 offset:27072\n"
"v_cvt_pk_bf16_f32 v244, v244, v245\n"
"v_mfma_f32_16x16x32_bf16 v[116:119], v[80:83], v[160:163], v[116:119]\n"
"s_waitcnt lgkmcnt(4)\n"
"ds_read_b64 v[92:93], v179 offset:27072\n"
"v_cvt_pk_bf16_f32 v245, v246, v247\n"
"v_mfma_f32_16x16x32_bf16 v[124:127], v[84:87], v[162:165], v[124:127]\n"
"ds_read_b128 v[136:139], v180\n"
"v_cvt_pk_bf16_f32 v241, v242, v243\n"
"v_mfma_f32_16x16x32_bf16 v[236:239], a[232:235], v[76:79], v[236:239]\n"
"ds_write_b64 v197, v[240:241] offset:49152\n"
"v_mfma_f32_16x16x32_bf16 v[240:243], a[224:227], v[72:75], 0\n"
"v_mul_f32 v248, v171, v248\n"
"v_mul_f32 v249, v171, v249\n"
"v_mfma_f32_16x16x32_bf16 v[240:243], a[236:239], v[76:79], v[240:243]\n"
"ds_read_b128 v[72:75], v192 offset:58368\n"
"ds_read_b128 v[76:79], v192 offset:59392\n"
"v_mfma_f32_16x16x32_bf16 v[232:235], a[240:243], v[68:71], v[232:235]\n"
"s_waitcnt lgkmcnt(7)\n"
"ds_read_b128 v[140:143], v181\n"
"v_mul_f32 v250, v171, v250\n"
"v_mfma_f32_16x16x32_bf16 v[236:239], a[244:247], v[68:71], v[236:239]\n"
"s_waitcnt lgkmcnt(7)\n"
"ds_read_b64 v[98:99], v180 offset:64\n"
"v_mul_f32 v251, v171, v251\n"
"v_mfma_f32_16x16x32_bf16 v[128:131], v[128:131], v[148:151], 0\n"
"s_waitcnt lgkmcnt(7)\n"
"ds_read_b64 v[100:101], v181 offset:64\n"
"v_cvt_pk_bf16_f32 v246, v248, v249\n"
"v_mfma_f32_16x16x32_bf16 v[132:135], v[132:135], v[148:151], 0\n"
"s_waitcnt lgkmcnt(7)\n"
"ds_read_b128 v[120:123], v180 offset:13504\n"
"v_cvt_pk_bf16_f32 v247, v250, v251\n"
"v_mfma_f32_16x16x32_bf16 v[240:243], a[248:251], v[68:71], v[240:243]\n"
"s_waitcnt lgkmcnt(7)\n"
"ds_read_b128 v[68:71], v192 offset:60416\n"
"v_mul_f32 v252, v171, v252\n"
"v_mfma_f32_16x16x32_bf16 v[128:131], v[88:91], v[160:163], v[128:131]\n"
"ds_read_b128 v[112:115], v181 offset:13504\n"
"v_mul_f32 v253, v171, v253\n"
"v_mfma_f32_16x16x32_bf16 v[132:135], v[92:95], v[162:165], v[132:135]\n"
"ds_read_b64 v[106:107], v180 offset:13568\n"
"v_mul_f32 v254, v171, v254\n"
"v_mfma_f32_16x16x32_bf16 v[136:139], v[136:139], v[148:151], 0\n"
"s_waitcnt lgkmcnt(8)\n"
"ds_read_b64 v[108:109], v181 offset:13568\n"
"v_mul_f32 v255, v171, v255\n"
"v_mfma_f32_16x16x32_bf16 v[232:235], a[252:255], v[72:75], v[232:235]\n"
"s_waitcnt lgkmcnt(8)\n"
"v_cvt_pk_bf16_f32 v252, v252, v253\n"
"v_cvt_pk_bf16_f32 v253, v254, v255\n"
"v_mfma_f32_16x16x32_bf16 v[236:239], v[0:3], v[72:75], v[236:239]\n"
"s_waitcnt lgkmcnt(7)\n"
"v_min_u32 v244, -2, v244\n"
"v_min_u32 v245, -2, v245\n"
"v_mfma_f32_16x16x32_bf16 v[240:243], v[4:7], v[72:75], v[240:243]\n"
"s_waitcnt lgkmcnt(6)\n"
"ds_read_b128 v[72:75], v192 offset:36864\n"
"v_min_u32 v246, -2, v246\n"
"v_mfma_f32_16x16x32_bf16 v[140:143], v[140:143], v[148:151], 0\n"
"s_waitcnt lgkmcnt(6)\n"
"v_min_u32 v247, -2, v247\n"
"v_min_u32 v252, -2, v252\n"
"v_mfma_f32_16x16x32_bf16 v[232:235], v[8:11], v[76:79], v[232:235]\n"
"s_waitcnt lgkmcnt(5)\n"
"buffer_store_dwordx4 v[244:247], v192, s[28:31], s37 offen sc1\n"
"v_mfma_f32_16x16x32_bf16 v[236:239], v[12:15], v[76:79], v[236:239]\n"
"s_waitcnt lgkmcnt(3)\n"
"v_min_u32 v253, -2, v253\n"
"v_max_f32 v116, v116, v124\n"
"v_mfma_f32_16x16x32_bf16 v[240:243], v[16:19], v[76:79], v[240:243]\n"
"ds_read_b128 v[76:79], v192 offset:37888\n"
"buffer_store_dwordx2 v[252:253], v193, s[28:31], s37 offen offset:1024 sc1\n"
"v_mfma_f32_16x16x32_bf16 v[136:139], v[96:99], v[160:163], v[136:139]\n"
"s_add_u32 s37, s37, 6144\n"
"ds_read_b64_tr_b16 v[248:249], v182 offset:13632\n"
"ds_read_b64_tr_b16 v[250:251], v183 offset:13632\n"
"v_mfma_f32_16x16x32_bf16 v[232:235], v[20:23], v[68:71], v[232:235]\n"
"s_waitcnt lgkmcnt(3)\n"
"ds_read_b64_tr_b16 v[244:245], v182 offset:13640\n"
"v_max_f32 v117, v117, v125\n"
"v_mfma_f32_16x16x32_bf16 v[236:239], v[24:27], v[68:71], v[236:239]\n"
"ds_read_b64_tr_b16 v[246:247], v183 offset:13640\n"
"v_max_f32 v118, v118, v126\n"
"v_mfma_f32_16x16x32_bf16 v[240:243], v[28:31], v[68:71], v[240:243]\n"
"ds_read_b128 v[68:71], v192 offset:38912\n"
"v_max_f32 v119, v119, v127\n"
"v_mfma_f32_16x16x32_bf16 v[140:143], v[100:103], v[162:165], v[140:143]\n"
"ds_read_b64_tr_b16 v[252:253], v182 offset:13576\n"
"v_max_f32 v170, v116, v117\n"
"v_mfma_f32_16x16x32_bf16 v[120:123], v[120:123], v[148:151], 0\n"
"ds_read_b64_tr_b16 v[254:255], v183 offset:13576\n"
"v_max_f32 v171, v118, v119\n"
"v_mfma_f32_16x16x32_bf16 v[112:115], v[112:115], v[148:151], 0\n"
"s_waitcnt vmcnt(9)\n"
"ds_read_b64_tr_b16 v[124:125], v182 offset:27136\n"
"v_max_f32 v128, v128, v132\n"
"v_mfma_f32_16x16x32_bf16 v[204:207], a[216:219], v[72:75], 0\n"
"s_waitcnt lgkmcnt(8)\n"
"ds_write_b128 v202, a[144:147] offset:24576\n"
"v_mfma_f32_16x16x32_bf16 v[208:211], a[220:223], v[72:75], 0\n"
"v_max_f32 v129, v129, v133\n"
"v_max_f32 v130, v130, v134\n"
"v_mfma_f32_16x16x32_bf16 v[212:215], a[224:227], v[72:75], 0\n"
"v_max_f32 v131, v131, v135\n"
"v_max3_f32 v170, v170, v128, v129\n"
"v_mfma_f32_16x16x32_bf16 v[120:123], v[104:107], v[160:163], v[120:123]\n"
"s_waitcnt vmcnt(6)\n"
"ds_read_b128 v[72:75], v192 offset:39936\n"
"ds_write_b64 v203, a[148:149] offset:24576\n"
"v_mfma_f32_16x16x32_bf16 v[204:207], a[228:231], v[76:79], v[204:207]\n"
"s_waitcnt lgkmcnt(6)\n"
"v_max3_f32 v171, v171, v130, v131\n"
"v_max_f32 v136, v136, v140\n"
"v_mfma_f32_16x16x32_bf16 v[208:211], a[232:235], v[76:79], v[208:211]\n"
"ds_write_b128 v202, a[150:153] offset:30720\n"
"v_mfma_f32_16x16x32_bf16 v[212:215], a[236:239], v[76:79], v[212:215]\n"
"v_max_f32 v137, v137, v141\n"
"v_max_f32 v138, v138, v142\n"
"v_mfma_f32_16x16x32_bf16 v[112:115], v[108:111], v[162:165], v[112:115]\n"
"v_max_f32 v139, v139, v143\n"
"v_max3_f32 v170, v170, v136, v137\n"
"v_mfma_f32_16x16x32_bf16 v[204:207], a[240:243], v[68:71], v[204:207]\n"
"ds_write_b64 v203, a[154:155] offset:30720\n"
"v_mfma_f32_16x16x32_bf16 v[208:211], a[244:247], v[68:71], v[208:211]\n"
"s_waitcnt lgkmcnt(3)\n"
"v_max3_f32 v171, v171, v138, v139\n"
"v_cvt_pk_bf16_f32 v232, v232, v233\n"
"v_mfma_f32_16x16x32_bf16 v[212:215], a[248:251], v[68:71], v[212:215]\n"
"ds_read_b128 v[76:79], v192 offset:40960\n"
"ds_read_b128 v[68:71], v192 offset:41984\n"
"v_mfma_f32_16x16x32_bf16 v[204:207], a[252:255], v[72:75], v[204:207]\n"
"ds_read_b64_tr_b16 v[126:127], v183 offset:27136\n"
"v_max_f32 v120, v120, v112\n"
"v_mfma_f32_16x16x32_bf16 v[208:211], v[0:3], v[72:75], v[208:211]\n"
"ds_read_b64_tr_b16 v[132:133], v182 offset:27144\n"
"v_max_f32 v121, v121, v113\n"
"v_mfma_f32_16x16x32_bf16 v[212:215], v[4:7], v[72:75], v[212:215]\n"
"s_waitcnt vmcnt(4)\n"
"v_max_f32 v122, v122, v114\n"
"v_add3_u32 v201, v201, s50, -11\n"
"v_cmp_lt_u32 vcc, 64, v201\n"
"v_max_f32 v123, v123, v115\n"
"v_max3_f32 v170, v170, v120, v121\n"
"v_max3_f32 v171, v171, v122, v123\n"
"ds_read_b64_tr_b16 v[134:135], v183 offset:27144\n"
"s_nop 0\n"
"s_cbranch_vccnz .Lp10%=\n"
".Lr10%=:\n"
"buffer_load_dwordx4 a[162:165], v192, s[28:31], s15 offen sc1\n"
"ds_read_b64_tr_b16 v[140:141], v182 offset:27080\n"
"ds_read_b64_tr_b16 v[142:143], v183 offset:27080\n"
"ds_read_b64_tr_b16 v[112:113], v184 offset:128\n"
"v_max_f32 v170, v170, v171\n"
"v_mov_b32 v171, v170\n"
"v_cvt_pk_bf16_f32 v233, v234, v235\n"
"v_cvt_pk_bf16_f32 v234, v236, v237\n"
"v_permlane16_swap_b32 v170, v171\n"
"v_max_f32 v170, v170, v171\n"
"v_mov_b32 v171, v170\n"
"v_cvt_pk_bf16_f32 v235, v238, v239\n"
"v_cvt_pk_bf16_f32 v240, v240, v241\n"
"v_permlane32_swap_b32 v170, v171\n"
"v_max_f32 v170, v170, v171\n"
"v_mul_f32 v170, 0xbe553b95, v170\n"
"v_fmamk_f32 v116, v116, 0x3e553b95, v170\n"
"v_fmamk_f32 v117, v117, 0x3e553b95, v170\n"
"v_fmamk_f32 v118, v118, 0x3e553b95, v170\n"
"v_fmamk_f32 v119, v119, 0x3e553b95, v170\n"
"v_exp_f32 v116, v116\n"
"v_exp_f32 v117, v117\n"
"v_exp_f32 v118, v118\n"
"v_exp_f32 v119, v119\n"
"v_cvt_pk_bf16_f32 v82, v116, v117\n"
"v_cvt_pk_bf16_f32 v83, v118, v119\n"
"v_and_b32 v116, v82, v176\n"
"v_and_b32 v117, v83, v177\n"
"v_xor_b32 v118, v82, v116\n"
"v_xor_b32 v119, v83, v117\n"
"v_fmamk_f32 v128, v128, 0x3e553b95, v170\n"
"v_fmamk_f32 v129, v129, 0x3e553b95, v170\n"
"v_mfma_f32_16x16x32_bf16 v[248:251], v[248:251], v[116:119], 0\n"
"s_waitcnt lgkmcnt(6)\n"
"buffer_load_dwordx2 a[166:167], v193, s[28:31], s15 offen offset:1024 sc1\n"
"ds_read_b64_tr_b16 v[114:115], v185 offset:128\n"
"v_mfma_f32_16x16x32_bf16 v[244:247], v[244:247], v[116:119], 0\n"
"s_barrier\n"
"v_mfma_f32_16x16x32_bf16 v[252:255], v[252:255], v[116:119], 0\n"
"ds_read_b128 v[32:35], v192 offset:25600\n"
"ds_read_b128 v[36:39], v192 offset:26624\n"
"v_mfma_f32_16x16x32_bf16 v[204:207], v[8:11], v[76:79], v[204:207]\n"
"ds_read_b128 v[40:43], v192 offset:27648\n"
"v_fmamk_f32 v130, v130, 0x3e553b95, v170\n"
"v_mfma_f32_16x16x32_bf16 v[208:211], v[12:15], v[76:79], v[208:211]\n"
"ds_read_b128 v[44:47], v192 offset:28672\n"
"v_fmamk_f32 v131, v131, 0x3e553b95, v170\n"
"v_mfma_f32_16x16x32_bf16 v[212:215], v[16:19], v[76:79], v[212:215]\n"
"ds_read_b128 v[48:51], v192 offset:29696\n"
"v_cvt_pk_bf16_f32 v241, v242, v243\n"
"v_mfma_f32_16x16x32_bf16 v[204:207], v[20:23], v[68:71], v[204:207]\n"
"ds_read_b128 v[52:55], v192 offset:30720\n"
"v_fmamk_f32 v136, v136, 0x3e553b95, v170\n"
"v_mfma_f32_16x16x32_bf16 v[208:211], v[24:27], v[68:71], v[208:211]\n"
"ds_read_b64_tr_b16 v[98:99], v184 offset:136\n"
"v_fmamk_f32 v137, v137, 0x3e553b95, v170\n"
"v_mfma_f32_16x16x32_bf16 v[212:215], v[28:31], v[68:71], v[212:215]\n"
"ds_read_b128 v[68:71], v192 offset:24576\n"
"v_exp_f32 v128, v128\n"
"ds_read_b64_tr_b16 v[100:101], v185 offset:136\n"
"ds_read_b64_tr_b16 v[106:107], v184 offset:72\n"
"v_exp_f32 v129, v129\n"
"v_exp_f32 v130, v130\n"
"ds_read_b64_tr_b16 v[108:109], v185 offset:72\n"
"v_exp_f32 v131, v131\n"
"v_cvt_pk_bf16_f32 v84, v128, v129\n"
"v_cvt_pk_bf16_f32 v85, v130, v131\n"
"v_and_b32 v128, v84, v176\n"
"v_and_b32 v129, v85, v177\n"
"v_xor_b32 v130, v84, v128\n"
"v_xor_b32 v131, v85, v129\n"
"v_mfma_f32_16x16x32_bf16 v[82:85], v[172:175], v[82:85], 0\n"
"s_waitcnt lgkmcnt(13)\n"
"ds_write_b128 v196, v[232:235]\n"
"v_mfma_f32_16x16x32_bf16 v[248:251], v[124:127], v[128:131], v[248:251]\n"
"s_waitcnt lgkmcnt(6)\n"
"v_fmamk_f32 v138, v138, 0x3e553b95, v170\n"
"v_fmamk_f32 v139, v139, 0x3e553b95, v170\n"
"v_mfma_f32_16x16x32_bf16 v[244:247], v[132:135], v[128:131], v[244:247]\n"
"s_waitcnt lgkmcnt(4)\n"
"v_exp_f32 v136, v136\n"
"v_mfma_f32_16x16x32_bf16 v[252:255], v[140:143], v[128:131], v[252:255]\n"
"ds_write_b64 v197, v[240:241]\n"
"v_mfma_f32_16x16x32_bf16 v[216:219], a[216:219], v[68:71], 0\n"
"v_exp_f32 v137, v137\n"
"v_mfma_f32_16x16x32_bf16 v[220:223], a[220:223], v[68:71], 0\n"
"ds_read_b64_tr_b16 v[116:117], v184 offset:13632\n"
"ds_read_b64_tr_b16 v[118:119], v185 offset:13632\n"
"v_mfma_f32_16x16x32_bf16 v[224:227], a[224:227], v[68:71], 0\n"
"ds_read_b128 v[68:71], v192 offset:33792\n"
"v_cvt_pk_bf16_f32 v90, v136, v137\n"
"v_mfma_f32_16x16x32_bf16 v[216:219], a[228:231], v[32:35], v[216:219]\n"
"ds_read_b64_tr_b16 v[128:129], v184 offset:13640\n"
"v_and_b32 v136, v90, v176\n"
"v_mfma_f32_16x16x32_bf16 v[220:223], a[232:235], v[32:35], v[220:223]\n"
"ds_read_b64_tr_b16 v[130:131], v185 offset:13640\n"
"v_fmamk_f32 v120, v120, 0x3e553b95, v170\n"
"v_mfma_f32_16x16x32_bf16 v[224:227], a[236:239], v[32:35], v[224:227]\n"
"ds_read_b128 v[32:35], v192 offset:34816\n"
"v_fmamk_f32 v121, v121, 0x3e553b95, v170\n"
"v_mfma_f32_16x16x32_bf16 v[216:219], a[240:243], v[36:39], v[216:219]\n"
"ds_read_b64_tr_b16 v[124:125], v184 offset:13576\n"
"v_fmamk_f32 v122, v122, 0x3e553b95, v170\n"
"v_mfma_f32_16x16x32_bf16 v[220:223], a[244:247], v[36:39], v[220:223]\n"
"ds_read_b64_tr_b16 v[126:127], v185 offset:13576\n"
"v_fmamk_f32 v123, v123, 0x3e553b95, v170\n"
"v_mfma_f32_16x16x32_bf16 v[224:227], a[248:251], v[36:39], v[224:227]\n"
"s_waitcnt vmcnt(5)\n"
"ds_read_b128 v[36:39], v192 offset:35840\n"
"v_exp_f32 v138, v138\n"
"v_mfma_f32_16x16x32_bf16 v[216:219], a[252:255], v[40:43], v[216:219]\n"
"v_exp_f32 v139, v139\n"
"v_mfma_f32_16x16x32_bf16 v[220:223], v[0:3], v[40:43], v[220:223]\n"
"v_cvt_pk_bf16_f32 v91, v138, v139\n"
"v_and_b32 v137, v91, v177\n"
"v_mfma_f32_16x16x32_bf16 v[224:227], v[4:7], v[40:43], v[224:227]\n"
"v_xor_b32 v138, v90, v136\n"
"v_xor_b32 v139, v91, v137\n"
"v_mfma_f32_16x16x32_bf16 v[216:219], v[8:11], v[44:47], v[216:219]\n"
"s_waitcnt lgkmcnt(11)\n"
"v_exp_f32 v120, v120\n"
"v_mfma_f32_16x16x32_bf16 v[248:251], v[112:115], v[136:139], v[248:251]\n"
"v_exp_f32 v121, v121\n"
"v_mfma_f32_16x16x32_bf16 v[244:247], v[98:101], v[136:139], v[244:247]\n"
"v_exp_f32 v122, v122\n"
"v_mfma_f32_16x16x32_bf16 v[252:255], v[106:109], v[136:139], v[252:255]\n"
"v_exp_f32 v123, v123\n"
"v_mfma_f32_16x16x32_bf16 v[220:223], v[12:15], v[44:47], v[220:223]\n"
"v_cvt_pk_bf16_f32 v92, v120, v121\n"
"v_cvt_pk_bf16_f32 v93, v122, v123\n"
"v_mfma_f32_16x16x32_bf16 v[224:227], v[16:19], v[44:47], v[224:227]\n"
"v_and_b32 v120, v92, v176\n"
"v_and_b32 v121, v93, v177\n"
"v_mfma_f32_16x16x32_bf16 v[216:219], v[20:23], v[48:51], v[216:219]\n"
"v_xor_b32 v122, v92, v120\n"
"v_xor_b32 v123, v93, v121\n"
"v_mfma_f32_16x16x32_bf16 v[90:93], v[172:175], v[90:93], v[82:85]\n"
"s_waitcnt lgkmcnt(1)\n"
"ds_read_b128 v[72:75], v202 offset:49152\n"
"ds_read_b128 v[76:79], v202 offset:53248\n"
"v_mfma_f32_16x16x32_bf16 v[248:251], v[116:119], v[120:123], v[248:251]\n"
"v_cvt_pk_bf16_f32 v204, v204, v205\n"
"v_cvt_pk_bf16_f32 v205, v206, v207\n"
"v_mfma_f32_16x16x32_bf16 v[244:247], v[128:131], v[120:123], v[244:247]\n"
"v_cvt_pk_bf16_f32 v206, v208, v209\n"
"v_rcp_f32 v171, v90\n"
"v_mfma_f32_16x16x32_bf16 v[252:255], v[124:127], v[120:123], v[252:255]\n"
"v_cvt_pk_bf16_f32 v207, v210, v211\n"
"v_cvt_pk_bf16_f32 v212, v212, v213\n"
"v_mfma_f32_16x16x32_bf16 v[220:223], v[24:27], v[48:51], v[220:223]\n"
"ds_write_b128 v196, v[204:207] offset:12288\n"
"v_mfma_f32_16x16x32_bf16 v[224:227], v[28:31], v[48:51], v[224:227]\n"
"v_mul_f32 v248, v171, v248\n"
"v_mul_f32 v249, v171, v249\n"
"v_mfma_f32_16x16x32_bf16 v[232:235], a[216:219], v[52:55], 0\n"
"buffer_load_dwordx4 v[62:65], v192, s[28:31], s16 offen sc1\n"
"v_mul_f32 v250, v171, v250\n"
"v_mfma_f32_16x16x32_bf16 v[236:239], a[220:223], v[52:55], 0\n"
"s_waitcnt vmcnt(2) lgkmcnt(2)\n"
"ds_write_b128 v202, a[162:165] offset:36864\n"
"v_mfma_f32_16x16x32_bf16 v[240:243], a[224:227], v[52:55], 0\n"
"s_waitcnt lgkmcnt(2)\n"
"v_mul_f32 v251, v171, v251\n"
"buffer_load_dwordx2 v[66:67], v193, s[28:31], s16 offen offset:1024 sc1\n"
"v_cvt_pk_bf16_f32 v248, v248, v249\n"
"v_cvt_pk_bf16_f32 v249, v250, v251\n"
"v_mul_f32 v244, v171, v244\n"
"v_mul_f32 v245, v171, v245\n"
"v_mul_f32 v246, v171, v246\n"
"v_mul_f32 v247, v171, v247\n"
"v_cvt_pk_bf16_f32 v250, v244, v245\n"
"v_cvt_pk_bf16_f32 v251, v246, v247\n"
"v_mul_f32 v252, v171, v252\n"
"v_mul_f32 v253, v171, v253\n"
"v_mul_f32 v254, v171, v254\n"
"v_mul_f32 v255, v171, v255\n"
"v_cvt_pk_bf16_f32 v252, v252, v253\n"
"v_cvt_pk_bf16_f32 v213, v214, v215\n"
"v_cvt_pk_bf16_f32 v253, v254, v255\n"
"v_min_u32 v248, -2, v248\n"
"v_min_u32 v249, -2, v249\n"
"v_min_u32 v250, -2, v250\n"
"v_min_u32 v251, -2, v251\n"
"buffer_store_dwordx4 v[248:251], v192, s[28:31], s37 offen sc1\n"
"v_min_u32 v252, -2, v252\n"
"v_min_u32 v253, -2, v253\n"
"ds_write_b64 v197, v[212:213] offset:12288\n"
"buffer_store_dwordx2 v[252:253], v193, s[28:31], s37 offen offset:1024 sc1\n"
"s_add_u32 s37, s37, 6144\n"
"s_waitcnt vmcnt(4)\n"
"ds_write_b64 v203, a[166:167] offset:36864\n"
"v_cvt_pk_bf16_f32 v216, v216, v217\n"
"v_cvt_pk_bf16_f32 v217, v218, v219\n"
"v_cvt_pk_bf16_f32 v218, v220, v221\n"
"v_cvt_pk_bf16_f32 v219, v222, v223\n"
"v_cvt_pk_bf16_f32 v224, v224, v225\n"
"v_cvt_pk_bf16_f32 v225, v226, v227\n"
"ds_write_b128 v196, v[216:219] offset:43008\n"
"buffer_store_dwordx4 v[72:75], v194, s[24:27], s36 offen sc1\n"
"v_max3_u32 v200, v228, v229, v230\n"
"v_max3_u32 v200, v200, v231, v152\n"
"ds_read_b128 v[72:75], v192 offset:31744\n"
"ds_write_b64 v197, v[224:225] offset:43008\n"
"v_max_u32 v200, v200, v153\n"
"buffer_store_dwordx4 v[76:79], v195, s[24:27], s36 offen sc1\n"
"s_nop 1\n"
"ds_read_b128 v[76:79], v192 offset:32768\n"
"s_add_u32 s36, s36, s33\n"
"v_cmp_eq_u32 vcc, -1, v200\n"
"s_nop 4\n"
"s_cbranch_vccnz .Lp11%=\n"
".Lr11%=:\n"
"ds_write_b128 v202, v[228:231] offset:18432\n"
"s_waitcnt lgkmcnt(3)\n"
"v_mfma_f32_16x16x32_bf16 v[232:235], a[228:231], v[72:75], v[232:235]\n"
"ds_write_b64 v203, v[152:153] offset:18432\n"
"v_mfma_f32_16x16x32_bf16 v[236:239], a[232:235], v[72:75], v[236:239]\n"
"v_mfma_f32_16x16x32_bf16 v[240:243], a[236:239], v[72:75], v[240:243]\n"
"s_waitcnt lgkmcnt(2)\n"
"v_mfma_f32_16x16x32_bf16 v[232:235], a[240:243], v[76:79], v[232:235]\n"
"v_mfma_f32_16x16x32_bf16 v[236:239], a[244:247], v[76:79], v[236:239]\n"
"v_mfma_f32_16x16x32_bf16 v[240:243], a[248:251], v[76:79], v[240:243]\n"
"v_mfma_f32_16x16x32_bf16 v[232:235], a[252:255], v[68:71], v[232:235]\n"
"v_mfma_f32_16x16x32_bf16 v[236:239], v[0:3], v[68:71], v[236:239]\n"
"s_waitcnt lgkmcnt(0)\n"
"v_mfma_f32_16x16x32_bf16 v[240:243], v[4:7], v[68:71], v[240:243]\n"
"s_barrier\n"
"v_mfma_f32_16x16x32_bf16 v[232:235], v[8:11], v[32:35], v[232:235]\n"
"ds_read_b128 v[72:75], v192 offset:18432\n"
"ds_read_b128 v[76:79], v192 offset:19456\n"
"v_mfma_f32_16x16x32_bf16 v[236:239], v[12:15], v[32:35], v[236:239]\n"
"ds_read_b128 v[68:71], v192 offset:20480\n"
"v_mfma_f32_16x16x32_bf16 v[240:243], v[16:19], v[32:35], v[240:243]\n"
"ds_read_b128 v[32:35], v192 offset:21504\n"
"v_mfma_f32_16x16x32_bf16 v[232:235], v[20:23], v[36:39], v[232:235]\n"
"ds_read_b128 v[40:43], v202\n"
"v_mfma_f32_16x16x32_bf16 v[236:239], v[24:27], v[36:39], v[236:239]\n"
"ds_read_b128 v[44:47], v202 offset:4096\n"
"v_mfma_f32_16x16x32_bf16 v[240:243], v[28:31], v[36:39], v[240:243]\n"
"ds_read_b128 v[36:39], v192 offset:22528\n"
"s_nop 2\n"
"v_cvt_pk_bf16_f32 v232, v232, v233\n"
"v_cvt_pk_bf16_f32 v233, v234, v235\n"
"v_cvt_pk_bf16_f32 v234, v236, v237\n"
"v_cvt_pk_bf16_f32 v235, v238, v239\n"
"v_cvt_pk_bf16_f32 v240, v240, v241\n"
"v_cvt_pk_bf16_f32 v241, v242, v243\n"
"ds_write_b128 v196, v[232:235] offset:6144\n"
"s_waitcnt lgkmcnt(7)\n"
"v_mfma_f32_16x16x32_bf16 v[204:207], a[216:219], v[72:75], 0\n"
"s_waitcnt lgkmcnt(6)\n"
"v_mfma_f32_16x16x32_bf16 v[208:211], a[220:223], v[72:75], 0\n"
"ds_write_b64 v197, v[240:241] offset:6144\n"
"v_mfma_f32_16x16x32_bf16 v[212:215], a[224:227], v[72:75], 0\n"
"s_waitcnt lgkmcnt(3)\n"
"buffer_store_dwordx4 v[40:43], v194, s[24:27], s36 offen sc1\n"
"v_mfma_f32_16x16x32_bf16 v[204:207], a[228:231], v[76:79], v[204:207]\n"
"s_nop 0\n"
"ds_read_b128 v[40:43], v192 offset:23552\n"
"ds_read_b128 v[48:51], v202 offset:12288\n"
"v_mfma_f32_16x16x32_bf16 v[208:211], a[232:235], v[76:79], v[208:211]\n"
"ds_read_b128 v[52:55], v202 offset:16384\n"
"v_mfma_f32_16x16x32_bf16 v[212:215], a[236:239], v[76:79], v[212:215]\n"
"buffer_store_dwordx4 v[44:47], v195, s[24:27], s36 offen sc1\n"
"v_mfma_f32_16x16x32_bf16 v[204:207], a[240:243], v[68:71], v[204:207]\n"
"s_add_u32 s36, s36, s33\n"
"v_mfma_f32_16x16x32_bf16 v[208:211], a[244:247], v[68:71], v[208:211]\n"
"v_mfma_f32_16x16x32_bf16 v[212:215], a[248:251], v[68:71], v[212:215]\n"
"s_waitcnt vmcnt(7)\n"
"v_max3_u32 v200, v62, v63, v64\n"
"v_mfma_f32_16x16x32_bf16 v[204:207], a[252:255], v[32:35], v[204:207]\n"
"s_waitcnt lgkmcnt(5)\n"
"v_mfma_f32_16x16x32_bf16 v[208:211], v[0:3], v[32:35], v[208:211]\n"
"s_waitcnt vmcnt(6)\n"
"v_mfma_f32_16x16x32_bf16 v[212:215], v[4:7], v[32:35], v[212:215]\n"
"s_waitcnt lgkmcnt(1)\n"
"v_max3_u32 v200, v200, v65, v66\n"
"v_max_u32 v200, v200, v67\n"
"v_mfma_f32_16x16x32_bf16 v[204:207], v[8:11], v[36:39], v[204:207]\n"
"s_waitcnt lgkmcnt(0)\n"
"buffer_store_dwordx4 v[48:51], v194, s[24:27], s36 offen sc1\n"
"v_mfma_f32_16x16x32_bf16 v[208:211], v[12:15], v[36:39], v[208:211]\n"
"s_barrier\n"
"v_mfma_f32_16x16x32_bf16 v[212:215], v[16:19], v[36:39], v[212:215]\n"
"ds_read_b128 v[76:79], v192 offset:36864\n"
"ds_read_b128 v[68:71], v192 offset:37888\n"
"v_mfma_f32_16x16x32_bf16 v[204:207], v[20:23], v[40:43], v[204:207]\n"
"buffer_load_dwordx4 v[56:59], v192, s[28:31], s17 offen sc1\n"
"ds_read_b128 v[32:35], v192 offset:38912\n"
"v_mfma_f32_16x16x32_bf16 v[208:211], v[24:27], v[40:43], v[208:211]\n"
"ds_read_b128 v[36:39], v192 offset:39936\n"
"v_cmp_eq_u32 vcc, -1, v200\n"
"v_mfma_f32_16x16x32_bf16 v[212:215], v[28:31], v[40:43], v[212:215]\n"
"ds_read_b128 v[40:43], v192 offset:40960\n"
"s_nop 0\n"
"v_cvt_pk_bf16_f32 v204, v204, v205\n"
"v_cvt_pk_bf16_f32 v205, v206, v207\n"
"s_nop 0\n"
"v_cvt_pk_bf16_f32 v206, v208, v209\n"
"v_cvt_pk_bf16_f32 v207, v210, v211\n"
"buffer_load_dwordx2 v[60:61], v193, s[28:31], s17 offen offset:1024 sc1\n"
"ds_read_b128 v[44:47], v202 offset:43008\n"
"ds_write_b128 v196, v[204:207] offset:55296\n"
"v_cvt_pk_bf16_f32 v212, v212, v213\n"
"v_cvt_pk_bf16_f32 v213, v214, v215\n"
"s_cbranch_vccnz .Lp12%=\n"
".Lr12%=:\n"
"s_waitcnt lgkmcnt(6)\n"
"v_mfma_f32_16x16x32_bf16 v[216:219], a[216:219], v[76:79], 0\n"
"s_waitcnt lgkmcnt(5)\n"
"ds_write_b128 v202, v[62:65] offset:49152\n"
"v_mfma_f32_16x16x32_bf16 v[220:223], a[220:223], v[76:79], 0\n"
"buffer_store_dwordx4 v[52:55], v195, s[24:27], s36 offen sc1\n"
"v_mfma_f32_16x16x32_bf16 v[224:227], a[224:227], v[76:79], 0\n"
"s_add_u32 s36, s36, s33\n"
"ds_write_b64 v203, v[66:67] offset:49152\n"
"v_mfma_f32_16x16x32_bf16 v[216:219], a[228:231], v[68:71], v[216:219]\n"
"s_waitcnt lgkmcnt(6)\n"
"s_add_u32 s8, s8, s36\n"
"s_add_u32 s9, s9, s36\n"
"v_mfma_f32_16x16x32_bf16 v[220:223], a[232:235], v[68:71], v[220:223]\n"
"s_waitcnt lgkmcnt(3)\n"
"ds_write_b64 v197, v[212:213] offset:55296\n"
"v_mfma_f32_16x16x32_bf16 v[224:227], a[236:239], v[68:71], v[224:227]\n"
"s_add_u32 s10, s10, s36\n"
"buffer_store_dwordx4 v[44:47], v194, s[24:27], s8 offen sc1\n"
"v_mfma_f32_16x16x32_bf16 v[216:219], a[240:243], v[32:35], v[216:219]\n"
"s_add_u32 s11, s11, s36\n"
"ds_read_b128 v[44:47], v192 offset:41984\n"
"ds_read_b128 v[48:51], v202 offset:47104\n"
"v_mfma_f32_16x16x32_bf16 v[220:223], a[244:247], v[32:35], v[220:223]\n"
"s_add_u32 s20, s20, s36\n"
"ds_read_b128 v[52:55], v202 offset:6144\n"
"s_add_u32 s21, s21, s36\n"
"v_mfma_f32_16x16x32_bf16 v[224:227], a[248:251], v[32:35], v[224:227]\n"
"ds_read_b128 v[72:75], v202 offset:10240\n"
"v_mfma_f32_16x16x32_bf16 v[216:219], a[252:255], v[36:39], v[216:219]\n"
"v_mfma_f32_16x16x32_bf16 v[220:223], v[0:3], v[36:39], v[220:223]\n"
"v_mfma_f32_16x16x32_bf16 v[224:227], v[4:7], v[36:39], v[224:227]\n"
"s_waitcnt lgkmcnt(4)\n"
"s_barrier\n"
"v_mfma_f32_16x16x32_bf16 v[216:219], v[8:11], v[40:43], v[216:219]\n"
"ds_read_b128 v[76:79], v192 offset:50176\n"
"ds_read_b128 v[68:71], v192 offset:51200\n"
"v_mfma_f32_16x16x32_bf16 v[220:223], v[12:15], v[40:43], v[220:223]\n"
"s_waitcnt lgkmcnt(5)\n"
"ds_read_b128 v[32:35], v192 offset:52224\n"
"v_mfma_f32_16x16x32_bf16 v[224:227], v[16:19], v[40:43], v[224:227]\n"
"s_waitcnt lgkmcnt(5)\n"
"ds_read_b128 v[36:39], v192 offset:53248\n"
"v_mfma_f32_16x16x32_bf16 v[216:219], v[20:23], v[44:47], v[216:219]\n"
"s_waitcnt lgkmcnt(5)\n"
"ds_read_b128 v[40:43], v192 offset:54272\n"
"buffer_store_dwordx4 v[48:51], v195, s[24:27], s8 offen sc1\n"
"v_mfma_f32_16x16x32_bf16 v[220:223], v[24:27], v[44:47], v[220:223]\n"
"s_waitcnt lgkmcnt(5)\n"
"ds_read_b128 v[48:51], v202 offset:55296\n"
"v_mfma_f32_16x16x32_bf16 v[224:227], v[28:31], v[44:47], v[224:227]\n"
"s_nop 0\n"
"v_cvt_pk_bf16_f32 v216, v216, v217\n"
"v_cvt_pk_bf16_f32 v217, v218, v219\n"
"buffer_store_dwordx4 v[52:55], v194, s[24:27], s9 offen sc1\n"
"s_nop 0\n"
"v_cvt_pk_bf16_f32 v218, v220, v221\n"
"v_cvt_pk_bf16_f32 v219, v222, v223\n"
"s_nop 0\n"
"v_cvt_pk_bf16_f32 v224, v224, v225\n"
"v_cvt_pk_bf16_f32 v225, v226, v227\n"
"ds_write_b128 v196, v[216:219] offset:30720\n"
"buffer_store_dwordx4 v[72:75], v195, s[24:27], s9 offen sc1\n"
"s_nop 1\n"
"ds_read_b128 v[72:75], v192 offset:49152\n"
"ds_write_b64 v197, v[224:225] offset:30720\n"
"ds_read_b128 v[52:55], v202 offset:59392\n"
"s_waitcnt lgkmcnt(4)\n"
"buffer_store_dwordx4 v[48:51], v194, s[24:27], s10 offen sc1\n"
"s_waitcnt lgkmcnt(2)\n"
"v_mfma_f32_16x16x32_bf16 v[232:235], a[216:219], v[72:75], 0\n"
"v_mfma_f32_16x16x32_bf16 v[236:239], a[220:223], v[72:75], 0\n"
"s_waitcnt lgkmcnt(0)\n"
"buffer_store_dwordx4 v[52:55], v195, s[24:27], s10 offen sc1\n"
"v_mfma_f32_16x16x32_bf16 v[240:243], a[224:227], v[72:75], 0\n"
"v_mfma_f32_16x16x32_bf16 v[232:235], a[228:231], v[76:79], v[232:235]\n"
"v_mfma_f32_16x16x32_bf16 v[236:239], a[232:235], v[76:79], v[236:239]\n"
"v_mfma_f32_16x16x32_bf16 v[240:243], a[236:239], v[76:79], v[240:243]\n"
"v_mfma_f32_16x16x32_bf16 v[232:235], a[240:243], v[68:71], v[232:235]\n"
"v_mfma_f32_16x16x32_bf16 v[236:239], a[244:247], v[68:71], v[236:239]\n"
"v_mfma_f32_16x16x32_bf16 v[240:243], a[248:251], v[68:71], v[240:243]\n"
"v_mfma_f32_16x16x32_bf16 v[232:235], a[252:255], v[32:35], v[232:235]\n"
"v_mfma_f32_16x16x32_bf16 v[236:239], v[0:3], v[32:35], v[236:239]\n"
"s_waitcnt vmcnt(8)\n"
"v_max3_u32 v200, v56, v57, v58\n"
"v_mfma_f32_16x16x32_bf16 v[240:243], v[4:7], v[32:35], v[240:243]\n"
"v_mfma_f32_16x16x32_bf16 v[232:235], v[8:11], v[36:39], v[232:235]\n"
"v_mfma_f32_16x16x32_bf16 v[236:239], v[12:15], v[36:39], v[236:239]\n"
"v_mfma_f32_16x16x32_bf16 v[240:243], v[16:19], v[36:39], v[240:243]\n"
"s_waitcnt vmcnt(7)\n"
"v_max3_u32 v200, v200, v59, v60\n"
"v_mfma_f32_16x16x32_bf16 v[232:235], v[20:23], v[40:43], v[232:235]\n"
"v_max_u32 v200, v200, v61\n"
"v_cmp_eq_u32 vcc, -1, v200\n"
"v_mfma_f32_16x16x32_bf16 v[236:239], v[24:27], v[40:43], v[236:239]\n"
"v_mfma_f32_16x16x32_bf16 v[240:243], v[28:31], v[40:43], v[240:243]\n"
"s_nop 2\n"
"s_cbranch_vccnz .Lp13%=\n"
".Lr13%=:\n"
"ds_write_b128 v202, v[56:59]\n"
"v_cvt_pk_bf16_f32 v232, v232, v233\n"
"v_cvt_pk_bf16_f32 v233, v234, v235\n"
"v_cvt_pk_bf16_f32 v234, v236, v237\n"
"v_cvt_pk_bf16_f32 v235, v238, v239\n"
"ds_write_b64 v203, v[60:61]\n"
"v_cvt_pk_bf16_f32 v240, v240, v241\n"
"v_cvt_pk_bf16_f32 v241, v242, v243\n"
"ds_write_b128 v196, v[232:235] offset:24576\n"
"ds_write_b64 v197, v[240:241] offset:24576\n"
"s_waitcnt lgkmcnt(2)\n"
"s_barrier\n"
"ds_read_b128 v[52:55], v192\n"
"ds_read_b128 v[72:75], v192 offset:1024\n"
"ds_read_b128 v[76:79], v192 offset:2048\n"
"ds_read_b128 v[68:71], v192 offset:3072\n"
"ds_read_b128 v[32:35], v192 offset:4096\n"
"ds_read_b128 v[36:39], v192 offset:5120\n"
"ds_read_b128 v[44:47], v202 offset:30720\n"
"ds_read_b128 v[48:51], v202 offset:34816\n"
"s_waitcnt lgkmcnt(7)\n"
"v_mfma_f32_16x16x32_bf16 v[204:207], a[216:219], v[52:55], 0\n"
"s_waitcnt lgkmcnt(6)\n"
"v_mfma_f32_16x16x32_bf16 v[212:215], a[224:227], v[52:55], 0\n"
"v_mfma_f32_16x16x32_bf16 v[208:211], a[220:223], v[52:55], 0\n"
"v_mfma_f32_16x16x32_bf16 v[204:207], a[228:231], v[72:75], v[204:207]\n"
"s_waitcnt lgkmcnt(5)\n"
"v_mfma_f32_16x16x32_bf16 v[212:215], a[236:239], v[72:75], v[212:215]\n"
"v_mfma_f32_16x16x32_bf16 v[208:211], a[232:235], v[72:75], v[208:211]\n"
"s_waitcnt lgkmcnt(1)\n"
"buffer_store_dwordx4 v[44:47], v194, s[24:27], s11 offen sc1\n"
"v_mfma_f32_16x16x32_bf16 v[204:207], a[240:243], v[76:79], v[204:207]\n"
"s_waitcnt lgkmcnt(0)\n"
"v_mfma_f32_16x16x32_bf16 v[212:215], a[248:251], v[76:79], v[212:215]\n"
"v_mfma_f32_16x16x32_bf16 v[208:211], a[244:247], v[76:79], v[208:211]\n"
"buffer_store_dwordx4 v[48:51], v195, s[24:27], s11 offen sc1\n"
"v_mfma_f32_16x16x32_bf16 v[204:207], a[252:255], v[68:71], v[204:207]\n"
"v_mfma_f32_16x16x32_bf16 v[212:215], v[4:7], v[68:71], v[212:215]\n"
"v_mfma_f32_16x16x32_bf16 v[208:211], v[0:3], v[68:71], v[208:211]\n"
"v_mfma_f32_16x16x32_bf16 v[204:207], v[8:11], v[32:35], v[204:207]\n"
"v_mfma_f32_16x16x32_bf16 v[212:215], v[16:19], v[32:35], v[212:215]\n"
"v_mfma_f32_16x16x32_bf16 v[208:211], v[12:15], v[32:35], v[208:211]\n"
"v_mfma_f32_16x16x32_bf16 v[204:207], v[20:23], v[36:39], v[204:207]\n"
"v_mfma_f32_16x16x32_bf16 v[212:215], v[28:31], v[36:39], v[212:215]\n"
"v_mfma_f32_16x16x32_bf16 v[208:211], v[24:27], v[36:39], v[208:211]\n"
"s_nop 5\n"
"v_cvt_pk_bf16_f32 v204, v204, v205\n"
"v_cvt_pk_bf16_f32 v205, v206, v207\n"
"v_cvt_pk_bf16_f32 v212, v212, v213\n"
"v_cvt_pk_bf16_f32 v206, v208, v209\n"
"v_cvt_pk_bf16_f32 v207, v210, v211\n"
"v_cvt_pk_bf16_f32 v213, v214, v215\n"
"ds_write_b128 v196, v[204:207] offset:12288\n"
"ds_write_b64 v197, v[212:213] offset:12288\n"
"s_waitcnt lgkmcnt(0)\n"
"s_barrier\n"
"ds_read_b128 v[40:43], v202 offset:24576\n"
"ds_read_b128 v[44:47], v202 offset:28672\n"
"ds_read_b128 v[48:51], v202 offset:12288\n"
"ds_read_b128 v[52:55], v202 offset:16384\n"
"s_waitcnt lgkmcnt(3)\n"
"buffer_store_dwordx4 v[40:43], v194, s[24:27], s20 offen sc1\n"
"s_waitcnt lgkmcnt(0)\n"
"buffer_store_dwordx4 v[44:47], v195, s[24:27], s20 offen sc1\n"
"buffer_store_dwordx4 v[48:51], v194, s[24:27], s21 offen sc1\n"
"buffer_store_dwordx4 v[52:55], v195, s[24:27], s21 offen sc1\n"
"s_branch .Lend%=\n"
".Lp1%=:\n"
"s_sub_u32 s42, s42, 1\n"
"s_cbranch_scc1 .Lt1%=\n"
"s_sleep 1\n"
"buffer_load_dword v200, off, s[28:31], s39 sc1\n"
"s_waitcnt vmcnt(0)\n"
"v_add3_u32 v200, v200, s50, -2\n"
"v_cmp_lt_u32 vcc, 64, v200\n"
"s_nop 4\n"
"s_cbranch_vccnz .Lp1%=\n"
"s_branch .Lr1%=\n"
".Lt1%=:\n"
"s_mov_b32 s42, 0\n"
"s_branch .Lr1%=\n"
".Lp3%=:\n"
"s_sub_u32 s42, s42, 1\n"
"s_cbranch_scc1 .Lt3%=\n"
"s_sleep 1\n"
"buffer_load_dword v200, off, s[28:31], s39 sc1\n"
"s_waitcnt vmcnt(0)\n"
"v_add3_u32 v200, v200, s50, -4\n"
"v_cmp_lt_u32 vcc, 64, v200\n"
"s_nop 4\n"
"s_cbranch_vccnz .Lp3%=\n"
"s_branch .Lr3%=\n"
".Lt3%=:\n"
"s_mov_b32 s42, 0\n"
"s_branch .Lr3%=\n"
".Lp4%=:\n"
"s_sub_u32 s42, s42, 1\n"
"s_cbranch_scc1 .Lt4%=\n"
"s_sleep 1\n"
"buffer_load_dword v200, off, s[28:31], s39 sc1\n"
"s_waitcnt vmcnt(0)\n"
"v_add3_u32 v200, v200, s50, -5\n"
"v_cmp_lt_u32 vcc, 64, v200\n"
"s_nop 4\n"
"s_cbranch_vccnz .Lp4%=\n"
"s_branch .Lr4%=\n"
".Lt4%=:\n"
"s_mov_b32 s42, 0\n"
"s_branch .Lr4%=\n"
".Lp6%=:\n"
"s_sub_u32 s42, s42, 1\n"
"s_cbranch_scc1 .Lt6%=\n"
"s_sleep 1\n"
"buffer_load_dword v200, off, s[28:31], s39 sc1\n"
"s_waitcnt vmcnt(0)\n"
"v_add3_u32 v200, v200, s50, -7\n"
"v_cmp_lt_u32 vcc, 64, v200\n"
"s_nop 4\n"
"s_cbranch_vccnz .Lp6%=\n"
"s_branch .Lr6%=\n"
".Lt6%=:\n"
"s_mov_b32 s42, 0\n"
"s_branch .Lr6%=\n"
".Lp7%=:\n"
"s_sub_u32 s42, s42, 1\n"
"s_cbranch_scc1 .Lt7%=\n"
"s_sleep 1\n"
"buffer_load_dword v200, off, s[28:31], s39 sc1\n"
"s_waitcnt vmcnt(0)\n"
"v_add3_u32 v200, v200, s50, -8\n"
"v_cmp_lt_u32 vcc, 64, v200\n"
"s_nop 4\n"
"s_cbranch_vccnz .Lp7%=\n"
"s_branch .Lr7%=\n"
".Lt7%=:\n"
"s_mov_b32 s42, 0\n"
"s_branch .Lr7%=\n"
".Lp9%=:\n"
"s_sub_u32 s42, s42, 1\n"
"s_cbranch_scc1 .Lt9%=\n"
"s_sleep 1\n"
"buffer_load_dword v200, off, s[28:31], s39 sc1\n"
"s_waitcnt vmcnt(0)\n"
"v_add3_u32 v200, v200, s50, -10\n"
"v_cmp_lt_u32 vcc, 64, v200\n"
"s_nop 4\n"
"s_cbranch_vccnz .Lp9%=\n"
"s_branch .Lr9%=\n"
".Lt9%=:\n"
"s_mov_b32 s42, 0\n"
"s_branch .Lr9%=\n"
".Lp10%=:\n"
"s_sub_u32 s42, s42, 1\n"
"s_cbranch_scc1 .Lt10%=\n"
"s_sleep 1\n"
"buffer_load_dword v201, off, s[28:31], s39 sc1\n"
"s_waitcnt vmcnt(0)\n"
"v_add3_u32 v201, v201, s50, -11\n"
"v_cmp_lt_u32 vcc, 64, v201\n"
"s_nop 4\n"
"s_cbranch_vccnz .Lp10%=\n"
"s_branch .Lr10%=\n"
".Lt10%=:\n"
"s_mov_b32 s42, 0\n"
"s_branch .Lr10%=\n"
".Lp11%=:\n"
"s_sub_u32 s42, s42, 1\n"
"s_cbranch_scc1 .Lt11%=\n"
"buffer_load_dwordx4 v[228:231], v192, s[28:31], s14 offen sc1\n"
"buffer_load_dwordx2 v[152:153], v193, s[28:31], s14 offen offset:1024 sc1\n"
"s_waitcnt vmcnt(0)\n"
"v_max3_u32 v200, v228, v229, v230\n"
"v_max3_u32 v200, v200, v231, v152\n"
"v_max_u32 v200, v200, v153\n"
"v_cmp_eq_u32 vcc, -1, v200\n"
"s_nop 4\n"
"s_cbranch_vccnz .Lp11%=\n"
"s_branch .Lr11%=\n"
".Lt11%=:\n"
"s_mov_b32 s42, 0\n"
"s_branch .Lr11%=\n"
".Lp12%=:\n"
"s_sub_u32 s42, s42, 1\n"
"s_cbranch_scc1 .Lt12%=\n"
"buffer_load_dwordx4 v[62:65], v192, s[28:31], s16 offen sc1\n"
"buffer_load_dwordx2 v[66:67], v193, s[28:31], s16 offen offset:1024 sc1\n"
"s_waitcnt vmcnt(0)\n"
"v_max3_u32 v200, v62, v63, v64\n"
"v_max3_u32 v200, v200, v65, v66\n"
"v_max_u32 v200, v200, v67\n"
"v_cmp_eq_u32 vcc, -1, v200\n"
"s_nop 4\n"
"s_cbranch_vccnz .Lp12%=\n"
"s_branch .Lr12%=\n"
".Lt12%=:\n"
"s_mov_b32 s42, 0\n"
"s_branch .Lr12%=\n"
".Lp13%=:\n"
"s_sub_u32 s42, s42, 1\n"
"s_cbranch_scc1 .Lt13%=\n"
"buffer_load_dwordx4 v[56:59], v192, s[28:31], s17 offen sc1\n"
"buffer_load_dwordx2 v[60:61], v193, s[28:31], s17 offen offset:1024 sc1\n"
"s_waitcnt vmcnt(0)\n"
"v_max3_u32 v200, v56, v57, v58\n"
"v_max3_u32 v200, v200, v59, v60\n"
"v_max_u32 v200, v200, v61\n"
"v_cmp_eq_u32 vcc, -1, v200\n"
"s_nop 4\n"
"s_cbranch_vccnz .Lp13%=\n"
"s_branch .Lr13%=\n"
".Lt13%=:\n"
"s_mov_b32 s42, 0\n"
"s_branch .Lr13%=\n"
".Lend%=:\n"
      : "+{v31}"(tid), "+{s2}"(bid), "+{s[0:1]}"(ka)
      : "i"((__attribute__((address_space(3))) char*)smem)
      : "memory", "scc", "vcc", VCLOB, ACLOB, SCLOB);
}

extern "C" void kernel_launch(void* const* d_in, const int* in_sizes, int n_in,
                              void* d_out, int out_size, void* d_ws, size_t ws_size,
                              hipStream_t stream) {
  const uint16_t* x = (const uint16_t*)d_in[0];
  const uint16_t* wqkv = (const uint16_t*)d_in[1];
  const uint16_t* wo = (const uint16_t*)d_in[2];
  const uint16_t* rpb = (const uint16_t*)d_in[3];
  dinat_hs<<<256, 256, 0, stream>>>(x, wqkv, wo, rpb, (uint16_t*)d_out, (char*)d_ws + (2 << 20)
  );
}
```

```cpp
#include <hip/hip_runtime.h>
#include <hip/hip_bf16.h>
#include <stdint.h>

#define VCLOB "v0", "v1", "v2", "v3", "v4", "v5", "v6", "v7", "v8", "v9", "v10", "v11", "v12", "v13", "v14", "v15", "v16", "v17", "v18", "v19", "v20", "v21", "v22", "v23", "v24", "v25", "v26", "v27", "v28", "v29", "v30", "v32", "v33", "v34", "v35", "v36", "v37", "v38", "v39", "v40", "v41", "v42", "v43", "v44", "v45", "v46", "v47", "v48", "v49", "v50", "v51", "v52", "v53", "v54", "v55", "v56", "v57", "v58", "v59", "v60", "v61", "v62", "v63", "v64", "v65", "v66", "v67", "v68", "v69", "v70", "v71", "v72", "v73", "v74", "v75", "v76", "v77", "v78", "v79", "v80", "v81", "v82", "v83", "v84", "v85", "v86", "v87", "v88", "v89", "v90", "v91", "v92", "v93", "v94", "v95", "v96", "v97", "v98", "v99", "v100", "v101", "v102", "v103", "v104", "v105", "v106", "v107", "v108", "v109", "v110", "v111", "v112", "v113", "v114", "v115", "v116", "v117", "v118", "v119", "v120", "v121", "v122", "v123", "v124", "v125", "v126", "v127", "v128", "v129", "v130", "v131", "v132", "v133", "v134", "v135", "v136", "v137", "v138", "v139", "v140", "v141", "v142", "v143", "v144", "v145", "v146", "v147", "v148", "v149", "v150", "v151", "v152", "v153", "v154", "v155", "v156", "v157", "v158", "v159", "v160", "v161", "v162", "v163", "v164", "v165", "v166", "v167", "v168", "v169", "v170", "v171", "v172", "v173", "v174", "v175", "v176", "v177", "v178", "v179", "v180", "v181", "v182", "v183", "v184", "v185", "v186", "v187", "v188", "v189", "v190", "v191", "v192", "v193", "v194", "v195", "v196", "v197", "v198", "v199", "v200", "v201", "v202", "v203", "v204", "v205", "v206", "v207", "v208", "v209", "v210", "v211", "v212", "v213", "v214", "v215", "v216", "v217", "v218", "v219", "v220", "v221", "v222", "v223", "v224", "v225", "v226", "v227", "v228", "v229", "v230", "v231", "v232", "v233", "v234", "v235", "v236", "v237", "v238", "v239", "v240", "v241", "v242", "v243", "v244", "v245", "v246", "v247", "v248", "v249", "v250", "v251", "v252", "v253", "v254", "v255"
#define ACLOB "a0", "a1", "a2", "a3", "a4", "a5", "a6", "a7", "a8", "a9", "a10", "a11", "a12", "a13", "a14", "a15", "a16", "a17", "a18", "a19", "a20", "a21", "a22", "a23", "a24", "a25", "a26", "a27", "a28", "a29", "a30", "a31", "a32", "a33", "a34", "a35", "a36", "a37", "a38", "a39", "a40", "a41", "a42", "a43", "a44", "a45", "a46", "a47", "a48", "a49", "a50", "a51", "a52", "a53", "a54", "a55", "a56", "a57", "a58", "a59", "a60", "a61", "a62", "a63", "a64", "a65", "a66", "a67", "a68", "a69", "a70", "a71", "a72", "a73", "a74", "a75", "a76", "a77", "a78", "a79", "a80", "a81", "a82", "a83", "a84", "a85", "a86", "a87", "a88", "a89", "a90", "a91", "a92", "a93", "a94", "a95", "a96", "a97", "a98", "a99", "a100", "a101", "a102", "a103", "a104", "a105", "a106", "a107", "a108", "a109", "a110", "a111", "a112", "a113", "a114", "a115", "a116", "a117", "a118", "a119", "a120", "a121", "a122", "a123", "a124", "a125", "a126", "a127", "a128", "a129", "a130", "a131", "a132", "a133", "a134", "a135", "a136", "a137", "a138", "a139", "a140", "a141", "a142", "a143", "a144", "a145", "a146", "a147", "a148", "a149", "a150", "a151", "a152", "a153", "a154", "a155", "a156", "a157", "a158", "a159", "a160", "a161", "a162", "a163", "a164", "a165", "a166", "a167", "a168", "a169", "a170", "a171", "a172", "a173", "a174", "a175", "a176", "a177", "a178", "a179", "a180", "a181", "a182", "a183", "a184", "a185", "a186", "a187", "a188", "a189", "a190", "a191", "a192", "a193", "a194", "a195", "a196", "a197", "a198", "a199", "a200", "a201", "a202", "a203", "a204", "a205", "a206", "a207", "a208", "a209", "a210", "a211", "a212", "a213", "a214", "a215", "a216", "a217", "a218", "a219", "a220", "a221", "a222", "a223", "a224", "a225", "a226", "a227", "a228", "a229", "a230", "a231", "a232", "a233", "a234", "a235", "a236", "a237", "a238", "a239", "a240", "a241", "a242", "a243", "a244", "a245", "a246", "a247", "a248", "a249", "a250", "a251", "a252", "a253", "a254", "a255"
#define SCLOB "s3", "s4", "s5", "s6", "s7", "s8", "s9", "s10", "s11", "s12", "s13", "s14", "s15", "s16", "s17", "s18", "s19", "s20", "s21", "s22", "s23", "s24", "s25", "s26", "s27", "s28", "s29", "s30", "s31", "s32", "s33", "s34", "s35", "s36", "s37", "s38", "s39", "s40", "s41", "s42", "s43", "s44", "s45", "s46", "s47", "s48", "s49", "s50", "s51", "s52", "s53", "s54", "s55", "s56", "s57", "s58", "s59", "s60", "s61", "s62", "s63", "s64", "s65", "s66", "s67", "s68", "s69", "s70", "s71", "s72", "s73", "s74", "s75", "s76", "s77", "s78", "s79", "s80", "s81", "s82", "s83", "s84", "s85", "s86", "s87", "s88", "s89", "s90", "s91", "s92", "s93", "s94", "s95", "s96", "s97", "s98", "s99", "s100", "s101"
#define LDS_BYTES 163392

__global__ __launch_bounds__(256, 1) __attribute__((aligned(4096))) void dinat_hs(const uint16_t* __restrict__ x, const uint16_t* __restrict__ wqkv,
                                                   const uint16_t* __restrict__ wo, const uint16_t* __restrict__ rpb,
                                                   uint16_t* __restrict__ out, char* __restrict__ ws
                                                   ) {
  __shared__ __attribute__((aligned(16))) char smem[LDS_BYTES];
  unsigned tid = threadIdx.x, bid = blockIdx.x;
  auto ka = __builtin_amdgcn_kernarg_segment_ptr();
  asm volatile(
"s_load_dwordx2 s[8:9], s[0:1], 0\n"
"s_load_dwordx2 s[12:13], s[0:1], 8\n"
"s_load_dwordx2 s[16:17], s[0:1], 16\n"
"s_load_dwordx2 s[20:21], s[0:1], 24\n"
"s_load_dwordx2 s[24:25], s[0:1], 32\n"
"s_load_dwordx2 s[28:29], s[0:1], 40\n"
"s_getpc_b64 s[4:5]\n"
".Lpc%=:\n"
"s_bfe_u32 s53, s2, 0x20005\n"
"s_and_b32 s54, s2, 1\n"
"s_lshr_b32 s55, s2, 7\n"
"s_mul_i32 s59, s53, 27\n"
"v_lshrrev_b32 v113, 6, v31\n"
"s_max_u32 s59, s59, 1\n"
"s_mul_i32 s58, s55, 4816896\n"
"s_mul_i32 s71, s54, 14278656\n"
"s_nop 0\n"
"v_readfirstlane_b32 s51, v113\n"
"s_xnor_b32 s56, s54, s55\n"
"s_sub_u32 s59, s59, 1\n"
"s_add_u32 s58, s58, s71\n"
"v_and_b32 v112, 63, v31\n"
"s_and_b32 s56, s56, 1\n"
"s_mul_i32 s72, s59, 768\n"
"s_and_b32 s73, s2, 4\n"
"s_lshr_b32 s71, s2, 3\n"
"s_mul_i32 s57, s54, -344064\n"
"s_add_u32 s58, s58, s72\n"
"s_mul_i32 s73, s73, 21504\n"
"s_and_b32 s74, s2, 2\n"
"v_lshlrev_b32 v120, 4, v112\n"
"s_mul_i32 s85, s56, 3\n"
"s_lshl2_add_u32 s71, s71, s51\n"
"v_min_u32 v121, 2, v112\n"
"s_add_u32 s57, s57, 172032\n"
"s_add_u32 s58, s58, s73\n"
"s_mul_i32 s74, s74, 192\n"
"s_lshl_b32 s61, s51, 3\n"
"v_lshrrev_b32 v114, 3, v112\n"
"v_xor_b32 v115, v120, v112\n"
"s_sub_u32 s86, s85, 3\n"
"s_mul_i32 s72, s71, 384\n"
"v_lshlrev_b32 v121, 7, v121\n"
"s_bfe_u32 s52, s2, 0x20003\n"
"s_add_u32 s58, s58, s74\n"
"s_lshr_b32 s84, s2, 5\n"
"v_and_b32 v115, 0x70, v115\n"
"v_add_u32 v114, s61, v114\n"
"s_mul_i32 s86, s86, s57\n"
"v_add_u32 v121, s72, v121\n"
"s_add_u32 s84, s84, s52\n"
"v_xor_b32 v116, 64, v115\n"
"v_mul_u32_u24 v117, 0x300, v114\n"
"s_add_u32 s86, s86, s58\n"
"v_min_u32 v121, .Lend%=-.Lpc%=-4, v121\n"
"v_mul_u32_u24 v123, 0x2aab, v112\n"
"v_add_u32 v124, 64, v112\n"
"s_mul_i32 s84, s84, 2\n"
"v_add3_u32 v119, v117, s86, v116\n"
"s_mov_b32 m0, 163136\n"
"s_mul_i32 s73, s52, 338\n"
"global_load_lds_dword v121, s[4:5]\n"
"s_and_b32 s74, s51, 1\n"
"v_lshrrev_b32 v123, 16, v123\n"
"v_mul_u32_u24 v124, 0x2aab, v124\n"
"s_and_b32 s65, s51, 1\n"
"s_lshr_b32 s75, 0x9249200, s84\n"
"s_lshr_b32 s76, 0x12492480, s84\n"
"s_lshr_b32 s77, 0x4924900, s84\n"
"s_and_b32 s73, s73, -4\n"
"s_mul_i32 s74, s74, 88\n"
"v_lshlrev_b32 v126, 1, v123\n"
"v_lshrrev_b32 v124, 16, v124\n"
"s_lshr_b32 s66, s51, 1\n"
"s_mul_i32 s70, s52, 96\n"
"s_mul_i32 s86, s65, 384\n"
"s_mov_b32 s22, 1352\n"
"s_mov_b32 s23, 0x20000\n"
"s_add_u32 s73, s73, s74\n"
"v_lshlrev_b32 v122, 2, v112\n"
"s_and_b32 s60, s2, 7\n"
"s_lshr_b32 s84, s2, 5\n"
"v_and_b32 v126, 24, v126\n"
"v_lshlrev_b32 v127, 1, v124\n"
"s_add_u32 s86, s86, s70\n"
"s_mul_i32 s71, s66, 36864\n"
"s_movk_i32 s69, 1152\n"
"s_and_b32 s75, s75, 0x180\n"
"s_lshl3_add_u32 s60, s60, s84\n"
"v_and_or_b32 v126, v123, 3, v126\n"
"v_and_b32 v127, 24, v127\n"
"v_add_u32 v125, 128, v112\n"
"s_add_u32 s86, s86, s71\n"
"v_mad_u32_u24 v126, v126, s69, v120\n"
"v_mul_u32_u24 v27, 96, v123\n"
"v_and_or_b32 v127, v124, 3, v127\n"
"v_lshrrev_b32 v128, 2, v124\n"
"v_mul_u32_u24 v125, 0x2aab, v125\n"
"s_add_u32 s86, s86, 384\n"
"s_mul_i32 s65, s65, 3072\n"
"s_mul_i32 s72, s66, 6752\n"
"s_mul_i32 s84, s75, 576\n"
"s_mov_b32 s14, 221184\n"
"s_mov_b32 s15, 0x20000\n"
"v_sub_u32 v27, v126, v27\n"
"v_and_or_b32 v127, v128, 4, v127\n"
"v_lshrrev_b32 v125, 16, v125\n"
"s_add_u32 s72, s72, s65\n"
"s_add_u32 s84, s84, s86\n"
"v_mad_u32_u24 v127, v127, s69, v120\n"
"v_mul_u32_u24 v28, 96, v124\n"
"v_lshlrev_b32 v129, 1, v125\n"
"v_sub_u32 v28, v127, v28\n"
"v_and_b32 v129, 24, v129\n"
"v_add_u32 v28, 1024, v28\n"
"v_and_or_b32 v129, v125, 3, v129\n"
"v_mad_u32_u24 v129, v129, s69, v120\n"
"v_mul_u32_u24 v29, 96, v125\n"
"s_sub_u32 s64, 111, s59\n"
"v_sub_u32 v29, v129, v29\n"
"s_min_u32 s64, s64, 32\n"
"v_max_u32 v118, s85, v114\n"
"v_add_u32 v29, 6656, v29\n"
"s_mul_i32 s64, s64, 768\n"
"v_add_u32 v118, -3, v118\n"
"s_add_u32 s64, s64, s58\n"
"s_mul_i32 s50, s51, 6752\n"
"v_mul_i32_i24 v118, s57, v118\n"
"s_mov_b32 s10, 19267584\n"
"s_mov_b32 s11, 0x20000\n"
"s_add_u32 s50, s50, 27008\n"
"v_add3_u32 v118, v118, s64, v115\n"
"s_mul_i32 s34, s51, 12288\n"
"s_waitcnt lgkmcnt(0)\n"
"s_add_u32 m0, s74, 162768\n"
"s_lshl_b32 s74, s56, 3\n"
"buffer_load_dword v122, s[20:23], s73 offen lds\n"
"s_add_u32 m0, s72, 0\n"
"s_sub_u32 s74, s74, 2\n"
"buffer_load_dwordx4 v27, s[12:15], s84 offen lds\n"
"s_mul_i32 s74, s74, s57\n"
"s_add_u32 s74, s74, s58\n"
"s_add_u32 s34, s34, 81024\n"
"v_add3_u32 v130, v117, s74, v115\n"
"s_add_u32 s73, s74, s57\n"
"s_lshl2_add_u32 s85, s60, s52\n"
"v_add3_u32 v131, v117, s73, v116\n"
"s_and_b32 s76, s76, 0x180\n"
"s_add_u32 m0, s72, 1024\n"
"s_lshl2_add_u32 s39, s85, s51\n"
"s_mul_i32 s85, s76, 576\n"
"s_add_u32 s85, s85, s86\n"
"s_and_b32 s77, s77, 0x180\n"
"s_mul_i32 s71, s77, 576\n"
"buffer_load_dwordx4 v28, s[12:15], s84 offen lds\n"
"s_add_u32 s71, s71, s86\n"
"s_mul_i32 s74, s54, 172032\n"
"s_sub_u32 s36, s58, s74\n"
"s_add_u32 s32, s36, s75\n"
"v_add3_u32 v190, v117, s74, v115\n"
"s_sub_u32 s73, 172032, s74\n"
"v_add3_u32 v191, v117, s73, v116\n"
"s_add_u32 s0, s36, s76\n"
"s_add_u32 s1, s36, s77\n"
"s_add_u32 m0, s72, 2048\n"
"s_mul_i32 s35, s51, 6144\n"
"s_lshl_b32 s33, s57, 1\n"
"s_add_u32 s35, s35, 130176\n"
"v_and_b32 v134, 3, v112\n"
"v_lshrrev_b32 v137, 1, v112\n"
"buffer_load_dwordx4 v29, s[12:15], s84 offen lds\n"
"v_and_or_b32 v137, v137, 4, v134\n"
"v_bfe_u32 v136, v112, 2, 1\n"
"v_lshrrev_b32 v140, 1, v137\n"
"v_lshrrev_b32 v132, 4, v112\n"
"v_lshl_add_u32 v85, v136, 2, v140\n"
"v_xor_b32 v85, v85, v132\n"
"v_lshlrev_b32 v85, 4, v85\n"
"s_add_u32 m0, s50, 0\n"
"v_lshl_add_u32 v85, v137, 7, v85\n"
"v_mul_u32_u24 v25, 0xc00, v136\n"
"v_add_u32 v25, v25, v85\n"
"v_add_u32 v188, s34, v25\n"
"v_xad_u32 v189, v25, 64, s34\n"
"buffer_load_dwordx4 v118, s[8:11], s75 offen lds\n"
"v_xor_b32 v71, 64, v25\n"
"v_add_u32 v70, s35, v25\n"
"v_add_u32 v71, s35, v71\n"
"s_mul_i32 s3, s65, 12\n"
"s_add_u32 s70, s70, s3\n"
"s_mul_i32 s81, s77, 576\n"
"s_add_u32 s81, s81, s70\n"
"s_add_u32 m0, s50, 3072\n"
"s_sub_u32 s3, s76, s75\n"
"s_mul_i32 s3, s3, s66\n"
"s_add_u32 s3, s3, s75\n"
"s_mul_i32 s3, s3, 576\n"
"s_add_u32 s3, s3, s70\n"
"buffer_load_dwordx4 v119, s[8:11], s75 offen lds\n"
"v_lshrrev_b32 v84, 2, v112\n"
"v_mul_u32_u24 v84, 96, v84\n"
"v_lshl_add_u32 v23, v134, 3, v84\n"
"s_mov_b32 s42, 0x80000\n"
"v_add_u32 v72, 54016, v23\n"
"v_min_u32 v96, 0xdc, v31\n"
"v_mul_u32_u24 v97, 0xf1, v96\n"
"s_mul_i32 s86, s54, -52\n"
"v_lshrrev_b32 v97, 12, v97\n"
"s_add_u32 m0, s34, 6144\n"
"s_sub_u32 s86, s86, 8\n"
"v_lshlrev_b32 v30, 3, v96\n"
"v_mul_i32_i24 v97, s86, v97\n"
"v_lshl_add_u32 v96, v96, 1, v97\n"
"v_xor_b32 v69, 64, v25\n"
"buffer_load_dwordx4 v130, s[8:11], s75 offen lds\n"
"v_add_u32 v30, 160896, v30\n"
"v_add_u32 v68, s50, v25\n"
"v_add_u32 v69, s50, v69\n"
"v_add_u32 v142, s61, v137\n"
"v_bfe_u32 v86, v142, 1, 1\n"
"v_mul_u32_u24 v87, 43, v142\n"
"s_movk_i32 s68, 6752\n"
"s_add_u32 m0, s34, 9216\n"
"v_and_b32 v133, 48, v112\n"
"v_xor_b32 v86, v86, v132\n"
"v_lshrrev_b32 v87, 9, v87\n"
"s_movk_i32 s67, 192\n"
"v_lshlrev_b32 v86, 4, v86\n"
"buffer_load_dwordx4 v131, s[8:11], s75 offen lds\n"
"v_mul_u32_u24 v87, 80832, v87\n"
"v_mad_u32_u24 v21, v142, s68, v133\n"
"v_mad_u32_u24 v141, v142, s67, v86\n"
"v_sub_u32 v21, v21, v87\n"
"v_and_b32 v88, 1, v112\n"
"s_mul_i32 s48, s56, 13504\n"
"v_add_u32 v21, 6144, v21\n"
"s_add_u32 m0, s72, 13504\n"
"v_add_u32 v187, 74272, v141\n"
"v_add_u32 v198, 60768, v141\n"
"v_mad_u32_u24 v88, v88, s68, v133\n"
"s_mov_b32 s6, 0xf0f0f0f0\n"
"s_mov_b32 s7, 0xf0f0f0f0\n"
"buffer_load_dwordx4 v27, s[12:15], s85 offen lds\n"
"s_mov_b64 exec, s[6:7]\n"
"v_add_u32 v21, s48, v141\n"
"v_mov_b32 v187, v141\n"
"v_add_u32 v198, 60544, v88\n"
"s_mov_b64 exec, -1\n"
"s_lshl_b32 s86, s66, 10\n"
"v_add_u32 v192, 99456, v120\n"
"v_bfe_u32 v138, v112, 4, 1\n"
"v_lshrrev_b32 v139, 3, v112\n"
"s_add_u32 m0, s72, 14528\n"
"v_and_b32 v139, 4, v139\n"
"v_sub_u32 v94, v136, v138\n"
"s_add_u32 s62, s59, s61\n"
"s_mul_i32 s49, s56, 272\n"
"v_mul_i32_i24 v94, 0x88, v94\n"
"buffer_load_dwordx4 v28, s[12:15], s85 offen lds\n"
"v_sub_u32 v95, v137, v139\n"
"s_sub_u32 s63, 108, s62\n"
"v_lshl_add_u32 v94, v95, 3, v94\n"
"s_sub_u32 s62, 3, s62\n"
"v_add_u32 v150, 1, v139\n"
"v_add_u32 v151, 2, v139\n"
"v_add_u32 v152, 3, v139\n"
"s_add_u32 m0, s72, 15552\n"
"v_max_i32 v149, s62, v139\n"
"v_max_i32 v150, s62, v150\n"
"v_max_i32 v151, s62, v151\n"
"v_max_i32 v152, s62, v152\n"
"v_mov_b32 v153, 0x7f\n"
"buffer_load_dwordx4 v29, s[12:15], s85 offen lds\n"
"s_add_u32 s85, s49, 161064\n"
"v_add_u32 v20, s85, v94\n"
"s_lshl_b32 s85, s51, 10\n"
"v_min_i32 v149, s63, v149\n"
"v_min_i32 v150, s63, v150\n"
"v_min_i32 v151, s63, v151\n"
"v_min_i32 v152, s63, v152\n"
"s_add_u32 m0, s50, 1024\n"
"v_add_u32 v202, s85, v192\n"
"v_lshlrev_b32 v149, v149, v153\n"
"v_lshlrev_b32 v150, v150, v153\n"
"v_lshlrev_b32 v151, v151, v153\n"
"v_lshlrev_b32 v152, v152, v153\n"
"buffer_load_dwordx4 v118, s[8:11], s76 offen lds\n"
"s_sub_u32 s73, 3, s61\n"
"s_mov_b32 s85, 0xc0000007\n"
"s_cmp_eq_u32 s53, 0\n"
"s_cselect_b32 s85, 0xe0000000, s85\n"
"s_cmp_eq_u32 s53, 3\n"
"s_cselect_b32 s85, 7, s85\n"
"v_lshrrev_b32 v149, v137, v149\n"
"v_lshrrev_b32 v150, v137, v150\n"
"s_add_u32 m0, s50, 4096\n"
"v_lshrrev_b32 v151, v137, v151\n"
"v_lshrrev_b32 v152, v137, v152\n"
"s_max_i32 s73, s73, s62\n"
"s_mov_b32 s74, 0xffff\n"
"buffer_load_dwordx4 v119, s[8:11], s76 offen lds\n"
"v_bfe_i32 v98, v149, 0, 1\n"
"v_bfe_i32 v99, v150, 0, 1\n"
"v_bfe_i32 v100, v151, 0, 1\n"
"v_bfe_i32 v101, v152, 0, 1\n"
"v_bfe_i32 v102, v149, 8, 1\n"
"v_bfe_i32 v103, v150, 8, 1\n"
"v_bfe_i32 v56, v151, 8, 1\n"
"v_bfe_i32 v57, v152, 8, 1\n"
"v_max_i32 v58, s73, v137\n"
"v_bfi_b32 v0, s74, v98, v99\n"
"v_bfi_b32 v1, s74, v100, v101\n"
"v_bfi_b32 v2, s74, v102, v103\n"
"v_bfi_b32 v3, s74, v56, v57\n"
"s_mul_i32 s73, s51, 1536\n"
"v_and_b32 v250, 15, v112\n"
"s_lshl2_add_u32 s74, s60, s51\n"
"v_mul_u32_u24 v250, 0x180, v250\n"
"v_and_b32 v251, 7, v112\n"
"s_lshl2_add_u32 s40, s74, s52\n"
"s_mul_i32 s37, s74, 86016\n"
"v_add_u32 v250, 99456, v250\n"
"v_lshrrev_b32 v253, 5, v112\n"
"s_lshl_b32 s74, s51, 6\n"
"v_lshlrev_b32 v254, 3, v134\n"
"v_lshl_add_u32 v255, v23, 2, s74\n"
"v_sub_u32 v255, v255, v254\n"
"v_sub_u32 v255, v255, v254\n"
"v_add_u32 v236, 130176, v255\n"
"v_sub_u32 v84, v236, v254\n"
"s_lshl_b32 s84, s48, 2\n"
"v_mad_u32_u24 v186, v136, s68, v141\n"
"s_add_u32 s84, s84, 6752\n"
"v_add_u32 v22, s84, v186\n"
"s_sub_u32 s84, 28, s61\n"
"s_min_i32 s84, s84, s63\n"
"v_min_i32 v58, s84, v58\n"
"v_lshlrev_b32 v58, v58, -1\n"
"v_lshrrev_b32 v58, v139, v58\n"
"v_not_b32 v58, v58\n"
"v_and_b32 v59, 5, v58\n"
"v_sub_u32 v61, v136, v138\n"
"v_and_b32 v60, 10, v58\n"
"v_lshlrev_b32 v59, 1, v59\n"
"v_ashrrev_i32 v149, 31, v61\n"
"v_add_u32 v150, -1, v61\n"
"v_mov_b32 v160, 0x40de\n"
"v_mov_b32 v161, 0\n"
"v_lshlrev_b32 v63, 4, v134\n"
"v_mov_b64 v[156:157], 0\n"
"s_mov_b32 s6, 0x00f0000f\n"
"s_mov_b32 s7, 0xf0000f00\n"
"v_lshl_or_b32 v59, v60, 4, v59\n"
"v_not_b32 v149, v149\n"
"v_ashrrev_i32 v150, 31, v150\n"
"v_or_b32 v62, s56, v138\n"
"s_mov_b64 exec, s[6:7]\n"
"v_lshlrev_b64 v[156:157], v63, v[160:161]\n"
"s_mov_b64 exec, -1\n"
"v_or_b32 v59, 0x44, v59\n"
"v_and_b32 v12, v149, v0\n"
"v_and_b32 v16, v150, v0\n"
"v_and_b32 v13, v149, v1\n"
"v_and_b32 v17, v150, v1\n"
"v_and_b32 v14, v149, v2\n"
"v_and_b32 v18, v150, v2\n"
"v_and_b32 v15, v149, v3\n"
"v_and_b32 v19, v150, v3\n"
"v_sub_u32 v62, v136, v62\n"
"s_mov_b64 exec, 0\n"
"s_cmp_eq_u32 s51, 0\n"
"s_cbranch_scc1 .Lic0%=\n"
"s_cmp_eq_u32 s51, 1\n"
"s_cbranch_scc1 .Lic1%=\n"
"s_cmp_eq_u32 s51, 2\n"
"s_cbranch_scc1 .Lic2%=\n"
"s_branch .Lic3%=\n"
".Lib0%=:\n"
"s_mov_b64 exec, -1\n"
"v_mov_b32 v160, 0xffff\n"
"v_mov_b64 v[176:177], 0\n"
"v_lshl_or_b32 v66, v59, 8, v59\n"
"v_ashrrev_i32 v149, 31, v62\n"
"v_add_u32 v150, -1, v62\n"
"s_mov_b64 exec, s[6:7]\n"
"v_lshlrev_b64 v[176:177], v63, v[160:161]\n"
"s_mov_b64 exec, -1\n"
"s_add_u32 m0, s34, 7168\n"
"s_mov_b32 s30, 23068672\n"
"buffer_load_dwordx4 v130, s[8:11], s76 offen lds\n"
"s_mov_b32 s31, 0x20000\n"
"s_lshl_b32 s39, s39, 7\n"
"v_and_b32 v146, 8, v112\n"
"v_lshl_add_u32 v146, v136, 1, v146\n"
"v_add3_u32 v146, v146, s61, 1\n"
"v_lshrrev_b32 v145, 5, v112\n"
"v_max_u32 v144, 3, v142\n"
"v_mul_u32_u24 v145, s68, v145\n"
"v_bfe_u32 v92, v146, 1, 1\n"
"v_add_u32 v144, -3, v144\n"
"v_add_u32 v145, -768, v145\n"
"v_xor_b32 v92, v92, v134\n"
"s_add_u32 m0, s34, 10240\n"
"v_mov_b32 v143, s48\n"
"buffer_load_dwordx4 v131, s[8:11], s76 offen lds\n"
"v_bfe_u32 v90, v144, 1, 1\n"
"v_lshl_add_u32 v92, v92, 4, v145\n"
"v_mad_u32_u24 v143, v136, s68, v143\n"
"v_xor_b32 v90, v90, v132\n"
"v_cmp_eq_u32 s[46:47], 35, v146\n"
"v_mad_u32_u24 v184, v146, s67, v92\n"
"v_lshl_add_u32 v90, v90, 4, v143\n"
"v_add_u32 v146, 1, v146\n"
"v_mad_u32_u24 v178, v144, s67, v90\n"
"v_bfe_u32 v93, v146, 1, 1\n"
"v_add_u32 v144, 5, v142\n"
"s_add_u32 m0, s72, 60768\n"
"v_xor_b32 v93, v93, v134\n"
"buffer_load_dwordx4 v27, s[12:15], s71 offen lds\n"
"s_waitcnt vmcnt(12)\n"
"s_barrier\n"
"ds_read_b64_tr_b16 a[0:1], v23\n"
"ds_read_b64_tr_b16 a[2:3], v23 offset:1536\n"
"ds_read_b64_tr_b16 a[4:5], v23 offset:32\n"
"ds_read_b64_tr_b16 a[6:7], v23 offset:1568\n"
"ds_read_b64_tr_b16 a[8:9], v23 offset:64\n"
"ds_read_b64_tr_b16 a[10:11], v23 offset:1600\n"
"ds_read_b64_tr_b16 a[72:73], v23 offset:3072\n"
"s_add_u32 m0, s72, 61792\n"
"ds_read_b64_tr_b16 a[74:75], v23 offset:4608\n"
"buffer_load_dwordx4 v28, s[12:15], s71 offen lds\n"
"ds_read_b64_tr_b16 a[76:77], v23 offset:3104\n"
"v_min_u32 v144, 32, v144\n"
"ds_read_b64_tr_b16 a[78:79], v23 offset:4640\n"
"ds_read_b64_tr_b16 a[80:81], v23 offset:3136\n"
"ds_read_b64_tr_b16 a[82:83], v23 offset:4672\n"
"v_mul_u32_u24 v147, 13504, v138\n"
"ds_read_b64_tr_b16 a[12:13], v23 offset:6752\n"
"ds_read_b64_tr_b16 a[14:15], v23 offset:8288\n"
"ds_read_b64_tr_b16 a[16:17], v23 offset:6784\n"
"s_add_u32 m0, s72, 62816\n"
"ds_read_b64_tr_b16 a[18:19], v23 offset:8320\n"
"buffer_load_dwordx4 v29, s[12:15], s71 offen lds\n"
"s_waitcnt vmcnt(12)\n"
"ds_read_b128 a[144:147], v68\n"
"s_mul_i32 s72, s54, 312\n"
"s_and_b32 s71, s52, 1\n"
"ds_read_b128 a[148:151], v69\n"
"s_lshl1_add_u32 s72, s71, s72\n"
"s_add_u32 s72, s72, 162760\n"
"v_add_u32 v24, s72, v96\n"
"ds_read_b64_tr_b16 a[20:21], v23 offset:6816\n"
"ds_read_b64_tr_b16 a[22:23], v23 offset:8352\n"
"ds_read_b64_tr_b16 a[84:85], v23 offset:9824\n"
"s_add_u32 m0, s50, 2048\n"
"ds_read_b64_tr_b16 a[86:87], v23 offset:11360\n"
"buffer_load_dwordx4 v118, s[8:11], s77 offen lds\n"
"ds_read_b64_tr_b16 a[88:89], v23 offset:9856\n"
"s_and_b32 s71, s51, 1\n"
"ds_read_b64_tr_b16 a[90:91], v23 offset:11392\n"
"s_lshl3_add_u32 s86, s71, s86\n"
"ds_read_b64_tr_b16 a[92:93], v23 offset:9888\n"
"s_add_u32 s86, s86, 4096\n"
"ds_read_b64_tr_b16 a[94:95], v23 offset:11424\n"
"v_add_u32 v203, s86, v192\n"
"ds_read_u16 v240, v24 offset:6\n"
"ds_read_u16 v241, v24 offset:4\n"
"ds_read_u16 v242, v24 offset:2\n"
"v_lshl_add_u32 v93, v93, 4, v145\n"
"v_bfe_u32 v91, v144, 1, 1\n"
"v_add_u32 v147, s48, v147\n"
"v_mul_i32_i24 v148, -67520, v138\n"
"v_mad_u32_u24 v185, v146, s67, v93\n"
"v_bfe_u32 v135, v112, 2, 2\n"
"v_xor_b32 v91, v91, v132\n"
"v_add_u32 v148, 67520, v148\n"
"v_add_u32 v183, v185, v147\n"
"v_add_u32 v185, v185, v148\n"
"s_mov_b64 exec, s[46:47]\n"
"v_mad_u32_u24 v185, v138, s67, v185\n"
"s_mov_b64 exec, -1\n"
"v_lshl_add_u32 v91, v91, 4, v143\n"
"v_and_b32 v89, 1, v114\n"
"v_add_u32 v89, 29, v89\n"
"v_mul_i32_i24 v89, s57, v89\n"
"v_lshrrev_b32 v86, 2, v112\n"
"v_sub_u32 v87, 11, v112\n"
"v_add3_u32 v199, v89, s64, v116\n"
"v_mad_u32_u24 v179, v144, s67, v91\n"
"v_lshlrev_b32 v193, 3, v112\n"
"v_and_b32 v87, 0x80000000, v87\n"
"v_add_u32 v180, 40512, v178\n"
"v_add_u32 v181, 40512, v179\n"
"v_cmp_eq_u32 s[44:45], 32, v144\n"
"v_add_u32 v182, v184, v147\n"
"v_not_b32 v149, v149\n"
"v_mov_b32 v26, 0xf149f149\n"
"v_and_b32 v4, v149, v0\n"
"v_and_b32 v5, v149, v1\n"
"v_and_b32 v6, v149, v2\n"
"v_and_b32 v7, v149, v3\n"
"v_mov_b64 v[160:161], v[156:157]\n"
"v_ashrrev_i32 v150, 31, v150\n"
"v_and_b32 v8, v150, v0\n"
"v_and_b32 v9, v150, v1\n"
"v_and_b32 v10, v150, v2\n"
"v_and_b32 v11, v150, v3\n"
"v_add_u32 v184, v184, v148\n"
"v_mov_b32 v172, 0x3f803f80\n"
"v_mov_b32 v173, v172\n"
"v_mov_b64 v[174:175], v[172:173]\n"
"v_add_u32 v193, 99456, v193\n"
"v_mov_b64 v[164:165], v[156:157]\n"
"v_mov_b64 v[168:169], v[156:157]\n"
"s_waitcnt vmcnt(11) lgkmcnt(1)\n"
"v_mfma_f32_16x16x32_bf16 v[204:207], a[0:3], a[144:147], 0\n"
"s_add_u32 m0, s50, 5120\n"
"ds_read_b128 a[168:171], v188 offset:6144\n"
"buffer_load_dwordx4 v119, s[8:11], s77 offen lds\n"
"v_mfma_f32_16x16x32_bf16 v[208:211], a[4:7], a[144:147], 0\n"
"s_lshl_b32 s86, s51, 6\n"
"ds_read_b128 a[172:175], v189 offset:6144\n"
"ds_read_u16 v243, v24\n"
"v_mfma_f32_16x16x32_bf16 v[212:215], a[8:11], a[144:147], 0\n"
"s_add_u32 m0, s34, 8192\n"
"v_lshl_or_b32 v240, v241, 16, v240\n"
"v_add_u32 v81, s86, v112\n"
"v_mfma_f32_16x16x32_bf16 v[216:219], a[72:75], a[144:147], 0\n"
"s_lshl_b32 s71, s52, 3\n"
"v_add_u32 v154, s86, v112\n"
"v_add_u32 v81, 0x100, v81\n"
"v_mfma_f32_16x16x32_bf16 v[220:223], a[76:79], a[144:147], 0\n"
"s_lshl2_add_u32 s72, s60, s52\n"
"buffer_load_dwordx4 v130, s[8:11], s77 offen lds\n"
"v_mul_u32_u24 v155, 0xaab, v154\n"
"v_mfma_f32_16x16x32_bf16 v[224:227], a[80:83], a[144:147], 0\n"
"s_mul_i32 s38, s72, 86016\n"
"v_mul_u32_u24 v82, 0xaab, v81\n"
"v_lshrrev_b32 v155, 16, v155\n"
"v_mfma_f32_16x16x32_bf16 v[204:207], a[12:15], a[148:151], v[204:207]\n"
"s_add_u32 m0, s34, 11264\n"
"v_lshrrev_b32 v82, 16, v82\n"
"v_mul_i32_i24 v80, -24, v155\n"
"v_mfma_f32_16x16x32_bf16 v[208:211], a[16:19], a[148:151], v[208:211]\n"
"s_lshl_b32 s72, s51, 2\n"
"v_mul_i32_i24 v83, -24, v82\n"
"v_add_u32 v154, v154, v80\n"
"v_mfma_f32_16x16x32_bf16 v[212:215], a[20:23], a[148:151], v[212:215]\n"
"s_waitcnt lgkmcnt(2)\n"
"buffer_load_dwordx4 v131, s[8:11], s77 offen lds\n"
"v_add_u32 v81, v81, v83\n"
"v_mfma_f32_16x16x32_bf16 v[216:219], a[84:87], a[148:151], v[216:219]\n"
"s_waitcnt vmcnt(9) lgkmcnt(0)\n"
"v_and_b32 v80, 7, v155\n"
"v_and_b32 v83, 7, v82\n"
"v_mfma_f32_16x16x32_bf16 v[220:223], a[88:91], a[148:151], v[220:223]\n"
"s_add_u32 m0, s34, 0\n"
"ds_read_b128 a[152:155], v68 offset:1024\n"
"ds_read_b128 a[156:159], v69 offset:1024\n"
"v_mfma_f32_16x16x32_bf16 v[224:227], a[92:95], a[148:151], v[224:227]\n"
"s_add_u32 s38, s38, s73\n"
"v_lshl_or_b32 v241, v243, 16, v242\n"
"v_xor_b32 v154, v154, v80\n"
"v_mfma_f32_16x16x32_bf16 v[104:107], a[72:75], a[168:171], 0\n"
"s_lshl1_add_u32 s73, s51, 16\n"
"buffer_load_dwordx4 v190, s[8:11], s32 offen lds\n"
"ds_write_b64 v30, v[240:241]\n"
"v_mfma_f32_16x16x32_bf16 v[108:111], a[76:79], a[168:171], 0\n"
"s_and_b32 s86, s52, 1\n"
"v_xor_b32 v81, v81, v83\n"
"v_lshrrev_b32 v80, 1, v155\n"
"v_mfma_f32_16x16x32_bf16 v[228:231], a[80:83], a[168:171], 0\n"
"s_add_u32 m0, s34, 3072\n"
"v_lshrrev_b32 v83, 1, v82\n"
"v_and_b32 v80, 4, v80\n"
"v_mfma_f32_16x16x32_bf16 v[104:107], a[84:87], a[172:175], v[104:107]\n"
"s_lshl2_add_u32 s86, s86, s61\n"
"buffer_load_dwordx4 v191, s[8:11], s32 offen lds\n"
"v_and_b32 v83, 4, v83\n"
"v_mfma_f32_16x16x32_bf16 v[108:111], a[88:91], a[172:175], v[108:111]\n"
"s_add_u32 s32, s32, s33\n"
"v_and_or_b32 v80, v155, 3, v80\n"
"v_and_or_b32 v83, v82, 3, v83\n"
"v_mfma_f32_16x16x32_bf16 v[228:231], a[92:95], a[172:175], v[228:231]\n"
"s_waitcnt vmcnt(9)\n"
"v_add_u32 v80, s71, v80\n"
"s_add_u32 m0, s34, 1024\n"
"ds_read_b128 a[176:179], v188 offset:7168\n"
"ds_read_b128 a[180:183], v189 offset:7168\n"
"v_add_u32 v83, s71, v83\n"
"v_lshrrev_b32 v244, v80, s85\n"
"buffer_load_dwordx4 v190, s[8:11], s0 offen lds\n"
"v_lshrrev_b32 v245, 4, v155\n"
"v_bfe_u32 v246, v155, 2, 1\n"
"v_lshrrev_b32 v247, v83, s85\n"
"v_lshrrev_b32 v248, 4, v82\n"
"v_bfe_u32 v249, v82, 2, 1\n"
"v_or_b32 v244, v244, v245\n"
"v_xor_b32 v246, s54, v246\n"
"s_add_u32 m0, s34, 4096\n"
"v_or_b32 v247, v247, v248\n"
"v_xor_b32 v249, s54, v249\n"
"v_lshlrev_b32 v244, 31, v244\n"
"v_mul_u32_u24 v246, 172032, v246\n"
"v_mul_u32_u24 v80, 0x300, v80\n"
"buffer_load_dwordx4 v191, s[8:11], s0 offen lds\n"
"s_add_u32 s0, s0, s33\n"
"v_lshlrev_b32 v247, 31, v247\n"
"v_mul_u32_u24 v249, 172032, v249\n"
"v_mul_u32_u24 v83, 0x300, v83\n"
"v_add_u32 v252, s72, v132\n"
"v_add3_u32 v244, v244, v246, v80\n"
"v_add3_u32 v247, v247, v249, v83\n"
"s_add_u32 m0, s34, 2048\n"
"v_xor_b32 v252, v252, v251\n"
"v_lshl_add_u32 v194, v154, 4, v244\n"
"v_lshl_add_u32 v195, v81, 4, v247\n"
"v_mfma_f32_16x16x32_bf16 v[244:247], a[0:3], a[168:171], 0\n"
"s_mul_i32 s84, s52, 1536\n"
"buffer_load_dwordx4 v190, s[8:11], s1 offen lds\n"
"v_lshl_add_u32 v196, v252, 4, v250\n"
"v_mfma_f32_16x16x32_bf16 v[244:247], a[12:15], a[172:175], v[244:247]\n"
"s_add_u32 s37, s37, s84\n"
"s_mov_b64 exec, 0\n"
"s_cmp_eq_u32 s51, 0\n"
"s_cbranch_scc1 .Lic4%=\n"
"s_cmp_eq_u32 s51, 1\n"
"s_cbranch_scc1 .Lic5%=\n"
"s_cmp_eq_u32 s51, 2\n"
"s_cbranch_scc1 .Lic6%=\n"
"s_branch .Lic7%=\n"
".Lib1%=:\n"
"s_mov_b64 exec, -1\n"
"s_add_u32 m0, s34, 5120\n"
"s_barrier\n"
"buffer_load_dwordx4 v191, s[8:11], s1 offen lds\n"
"s_add_u32 s1, s1, s33\n"
"ds_read_b64_tr_b16 a[24:25], v23 offset:13504\n"
"ds_read_b64_tr_b16 a[26:27], v23 offset:15040\n"
"ds_read_b64_tr_b16 a[28:29], v23 offset:13536\n"
"ds_read_b64_tr_b16 a[30:31], v23 offset:15072\n"
"ds_read_b64_tr_b16 a[32:33], v23 offset:13568\n"
"ds_read_b64_tr_b16 a[34:35], v23 offset:15104\n"
"s_add_u32 m0, s35, 0\n"
"ds_read_b64_tr_b16 a[96:97], v23 offset:16576\n"
"ds_read_b64_tr_b16 a[98:99], v23 offset:18112\n"
"s_waitcnt vmcnt(10)\n"
"ds_read_b64_tr_b16 a[100:101], v23 offset:16608\n"
"s_barrier\n"
"buffer_load_dwordx4 v190, s[8:11], s32 offen lds\n"
"ds_read_b64_tr_b16 a[102:103], v23 offset:18144\n"
"ds_read_b64_tr_b16 a[104:105], v23 offset:16640\n"
"ds_read_b64_tr_b16 a[106:107], v23 offset:18176\n"
"ds_read_b64_tr_b16 a[36:37], v23 offset:20256\n"
"v_add_u32 v253, s73, v253\n"
"ds_read_b64_tr_b16 a[38:39], v23 offset:21792\n"
"ds_read_b64_tr_b16 a[40:41], v23 offset:20288\n"
"ds_read_b64_tr_b16 a[42:43], v23 offset:21824\n"
"v_lshl_add_u32 v250, v138, 3, v250\n"
"v_xor_b32 v253, v253, v251\n"
"v_lshl_add_u32 v197, v253, 4, v250\n"
"v_mfma_f32_16x16x32_bf16 v[248:251], a[4:7], a[168:171], 0\n"
"s_waitcnt vmcnt(9) lgkmcnt(14)\n"
"ds_read_b128 a[160:163], v68 offset:2048\n"
"ds_read_b128 a[164:167], v69 offset:2048\n"
"v_mfma_f32_16x16x32_bf16 v[204:207], a[24:27], a[152:155], v[204:207]\n"
"s_waitcnt lgkmcnt(14)\n"
"ds_read_b64_tr_b16 a[44:45], v23 offset:20320\n"
"v_add_u32 v68, 130176, v23\n"
"v_mfma_f32_16x16x32_bf16 v[208:211], a[28:31], a[152:155], v[208:211]\n"
"s_waitcnt lgkmcnt(13)\n"
"ds_read_b64_tr_b16 a[46:47], v23 offset:21856\n"
"ds_read_b64_tr_b16 a[108:109], v23 offset:23328\n"
"v_mfma_f32_16x16x32_bf16 v[212:215], a[32:35], a[152:155], v[212:215]\n"
"s_waitcnt lgkmcnt(13)\n"
"ds_read_b64_tr_b16 a[110:111], v23 offset:24864\n"
"ds_read_b64_tr_b16 a[112:113], v23 offset:23360\n"
"v_mfma_f32_16x16x32_bf16 v[216:219], a[96:99], a[152:155], v[216:219]\n"
"s_waitcnt lgkmcnt(13)\n"
"ds_read_b64_tr_b16 a[114:115], v23 offset:24896\n"
"ds_read_b64_tr_b16 a[116:117], v23 offset:23392\n"
"v_mfma_f32_16x16x32_bf16 v[220:223], a[100:103], a[152:155], v[220:223]\n"
"s_waitcnt lgkmcnt(13)\n"
"ds_read_b64_tr_b16 a[118:119], v23 offset:24928\n"
"ds_read_b64_tr_b16 a[48:49], v23 offset:60768\n"
"v_mfma_f32_16x16x32_bf16 v[224:227], a[104:107], a[152:155], v[224:227]\n"
"s_waitcnt lgkmcnt(13)\n"
"ds_read_b64_tr_b16 a[50:51], v23 offset:62304\n"
"ds_read_b64_tr_b16 a[52:53], v23 offset:60800\n"
"v_mfma_f32_16x16x32_bf16 v[204:207], a[36:39], a[156:159], v[204:207]\n"
"s_waitcnt vmcnt(7) lgkmcnt(13)\n"
"ds_read_b64_tr_b16 a[54:55], v23 offset:62336\n"
"ds_read_b64_tr_b16 a[56:57], v23 offset:60832\n"
"v_mfma_f32_16x16x32_bf16 v[208:211], a[40:43], a[156:159], v[208:211]\n"
"s_add_u32 m0, s35, 3072\n"
"ds_read_b128 a[184:187], v188 offset:8192\n"
"buffer_load_dwordx4 v191, s[8:11], s32 offen lds\n"
"v_mfma_f32_16x16x32_bf16 v[252:255], a[8:11], a[168:171], 0\n"
"s_add_u32 s32, s32, s33\n"
"ds_read_b128 a[188:191], v189 offset:8192\n"
"v_add_u32 v85, s86, v134\n"
"v_mfma_f32_16x16x32_bf16 v[248:251], a[16:19], a[172:175], v[248:251]\n"
"s_waitcnt lgkmcnt(13)\n"
"ds_read_b64_tr_b16 a[58:59], v23 offset:62368\n"
"ds_read_b64_tr_b16 a[120:121], v72 offset:9824\n"
"v_mfma_f32_16x16x32_bf16 v[212:215], a[44:47], a[156:159], v[212:215]\n"
"s_waitcnt lgkmcnt(13)\n"
"ds_read_b64_tr_b16 a[122:123], v72 offset:11360\n"
"ds_read_b64_tr_b16 a[124:125], v72 offset:9856\n"
"v_mfma_f32_16x16x32_bf16 v[216:219], a[108:111], a[156:159], v[216:219]\n"
"s_waitcnt lgkmcnt(13)\n"
"ds_read_b64_tr_b16 a[126:127], v72 offset:11392\n"
"ds_read_b64_tr_b16 a[128:129], v72 offset:9888\n"
"v_mfma_f32_16x16x32_bf16 v[220:223], a[112:115], a[156:159], v[220:223]\n"
"s_waitcnt lgkmcnt(13)\n"
"ds_read_b64_tr_b16 a[130:131], v72 offset:11424\n"
"ds_read_b64_tr_b16 a[60:61], v72 offset:13504\n"
"v_mfma_f32_16x16x32_bf16 v[224:227], a[116:119], a[156:159], v[224:227]\n"
"s_waitcnt lgkmcnt(13)\n"
"ds_read_b64_tr_b16 a[62:63], v72 offset:15040\n"
"ds_read_b64_tr_b16 a[64:65], v72 offset:13536\n"
"v_mfma_f32_16x16x32_bf16 v[204:207], a[48:51], a[160:163], v[204:207]\n"
"s_waitcnt lgkmcnt(13)\n"
"ds_read_b64_tr_b16 a[66:67], v72 offset:15072\n"
"ds_read_b64_tr_b16 a[68:69], v72 offset:13568\n"
"v_mfma_f32_16x16x32_bf16 v[208:211], a[52:55], a[160:163], v[208:211]\n"
"s_add_u32 m0, s35, 1024\n"
"ds_read_b64_tr_b16 a[70:71], v72 offset:15104\n"
"buffer_load_dwordx4 v190, s[8:11], s0 offen lds\n"
"v_mfma_f32_16x16x32_bf16 v[252:255], a[20:23], a[172:175], v[252:255]\n"
"s_lshl_b32 s84, s51, 5\n"
"ds_read_b64_tr_b16 a[132:133], v72 offset:16576\n"
"ds_read_b64_tr_b16 a[134:135], v72 offset:18112\n"
"v_mfma_f32_16x16x32_bf16 v[244:247], a[24:27], a[176:179], v[244:247]\n"
"s_waitcnt lgkmcnt(14)\n"
"ds_read_b64_tr_b16 a[136:137], v72 offset:16608\n"
"ds_read_b64_tr_b16 a[138:139], v72 offset:18144\n"
"v_mfma_f32_16x16x32_bf16 v[212:215], a[56:59], a[160:163], v[212:215]\n"
"s_waitcnt lgkmcnt(14)\n"
"ds_read_b64_tr_b16 a[140:141], v72 offset:16640\n"
"ds_read_b64_tr_b16 a[142:143], v72 offset:18176\n"
"v_mfma_f32_16x16x32_bf16 v[216:219], a[120:123], a[160:163], v[216:219]\n"
"s_waitcnt lgkmcnt(14)\n"
"v_mul_u32_u24 v85, 0x300, v85\n"
"v_lshl_add_u32 v85, v86, 7, v85\n"
"v_mfma_f32_16x16x32_bf16 v[220:223], a[124:127], a[160:163], v[220:223]\n"
"s_waitcnt lgkmcnt(12)\n"
"v_or_b32 v76, v87, v85\n"
"s_add_u32 s41, s39, 131072\n"
"v_mfma_f32_16x16x32_bf16 v[224:227], a[128:131], a[160:163], v[224:227]\n"
"s_waitcnt lgkmcnt(10)\n"
"s_add_u32 m0, s35, 4096\n"
"s_mul_i32 s20, s33, 4\n"
"v_mfma_f32_16x16x32_bf16 v[204:207], a[60:63], a[164:167], v[204:207]\n"
"s_waitcnt lgkmcnt(8)\n"
"buffer_load_dwordx4 v191, s[8:11], s0 offen lds\n"
"s_mul_i32 s21, s33, 5\n"
"v_mfma_f32_16x16x32_bf16 v[208:211], a[64:67], a[164:167], v[208:211]\n"
"s_waitcnt lgkmcnt(6)\n"
"s_add_u32 s0, s0, s33\n"
"v_mfma_f32_16x16x32_bf16 v[212:215], a[68:71], a[164:167], v[212:215]\n"
"s_waitcnt lgkmcnt(4)\n"
"v_cvt_pk_bf16_f32 v204, v204, v205\n"
"v_cvt_pk_bf16_f32 v205, v206, v207\n"
"v_mfma_f32_16x16x32_bf16 v[216:219], a[132:135], a[164:167], v[216:219]\n"
"s_waitcnt lgkmcnt(2)\n"
"v_cvt_pk_bf16_f32 v206, v208, v209\n"
"v_cvt_pk_bf16_f32 v207, v210, v211\n"
"v_mfma_f32_16x16x32_bf16 v[220:223], a[136:139], a[164:167], v[220:223]\n"
"s_waitcnt lgkmcnt(0)\n"
"v_cvt_pk_bf16_f32 v212, v212, v213\n"
"v_cvt_pk_bf16_f32 v213, v214, v215\n"
"v_mfma_f32_16x16x32_bf16 v[224:227], a[140:143], a[164:167], v[224:227]\n"
"s_add_u32 m0, s35, 2048\n"
"v_cvt_pk_bf16_f32 v216, v216, v217\n"
"v_cvt_pk_bf16_f32 v217, v218, v219\n"
"v_mfma_f32_16x16x32_bf16 v[248:251], a[28:31], a[176:179], v[248:251]\n"
"s_barrier\n"
"v_mfma_f32_16x16x32_bf16 v[252:255], a[32:35], a[176:179], v[252:255]\n"
"s_sub_u32 s84, 256, s84\n"
"buffer_load_dwordx4 v190, s[8:11], s1 offen lds\n"
"ds_write_b128 v21, v[204:207]\n"
"v_mfma_f32_16x16x32_bf16 v[104:107], a[96:99], a[176:179], v[104:107]\n"
"s_add_u32 m0, s35, 5120\n"
"v_cvt_pk_bf16_f32 v218, v220, v221\n"
"v_cvt_pk_bf16_f32 v219, v222, v223\n"
"v_mfma_f32_16x16x32_bf16 v[108:111], a[100:103], a[176:179], v[108:111]\n"
"s_add_u32 s37, s37, 949120\n"
"buffer_load_dwordx4 v191, s[8:11], s1 offen lds\n"
"ds_write_b128 v21, v[216:219] offset:128\n"
"v_mfma_f32_16x16x32_bf16 v[228:231], a[104:107], a[176:179], v[228:231]\n"
"s_add_u32 s1, s1, s33\n"
"v_cvt_pk_bf16_f32 v214, v224, v225\n"
"v_cvt_pk_bf16_f32 v215, v226, v227\n"
"v_mfma_f32_16x16x32_bf16 v[244:247], a[36:39], a[180:183], v[244:247]\n"
"s_lshr_b32 s71, s52, 1\n"
"v_add_u32 v235, s84, v84\n"
"v_mfma_f32_16x16x32_bf16 v[248:251], a[40:43], a[180:183], v[248:251]\n"
"s_add_u32 m0, s34, 6144\n"
"ds_write_b128 v21, v[212:215] offset:64\n"
"v_mfma_f32_16x16x32_bf16 v[252:255], a[44:47], a[180:183], v[252:255]\n"
"s_mul_i32 s71, s71, 172032\n"
"buffer_load_dwordx4 v190, s[8:11], s32 offen lds\n"
"v_mfma_f32_16x16x32_bf16 v[104:107], a[108:111], a[180:183], v[104:107]\n"
"s_waitcnt vmcnt(11)\n"
"s_mov_b32 s18, 73728\n"
"ds_read_b64 v[80:81], v20 offset:272\n"
"v_mfma_f32_16x16x32_bf16 v[108:111], a[112:115], a[180:183], v[108:111]\n"
"s_add_u32 m0, s34, 9216\n"
"ds_read_b128 v[32:35], v188\n"
"ds_read_b128 v[36:39], v189\n"
"v_mfma_f32_16x16x32_bf16 v[228:231], a[116:119], a[180:183], v[228:231]\n"
"s_mul_i32 s78, s33, 6\n"
"buffer_load_dwordx4 v191, s[8:11], s32 offen lds\n"
"ds_read_b64 v[86:87], v20 offset:336\n"
"v_mfma_f32_16x16x32_bf16 v[244:247], a[48:51], a[184:187], v[244:247]\n"
"s_add_u32 s32, s32, s33\n"
"ds_read_b64 v[88:89], v20 offset:544\n"
"ds_read_b64 v[94:95], v20 offset:608\n"
"v_mfma_f32_16x16x32_bf16 v[248:251], a[52:55], a[184:187], v[248:251]\n"
"s_add_u32 s71, s71, s36\n"
"ds_read_b64 v[96:97], v20 offset:816\n"
"ds_read_b64 v[102:103], v20 offset:880\n"
"v_mfma_f32_16x16x32_bf16 v[252:255], a[56:59], a[184:187], v[252:255]\n"
"s_add_u32 m0, s34, 7168\n"
"s_mul_i32 s79, s51, 3072\n"
"s_add_u32 s43, s79, 0\n"
"v_mfma_f32_16x16x32_bf16 v[104:107], a[120:123], a[184:187], v[104:107]\n"
"s_mul_i32 s80, s66, 12288\n"
"buffer_load_dwordx4 v190, s[8:11], s0 offen lds\n"
"v_mfma_f32_16x16x32_bf16 v[108:111], a[124:127], a[184:187], v[108:111]\n"
"s_waitcnt lgkmcnt(7)\n"
"s_mov_b32 s26, 19267584\n"
"v_mfma_f32_16x16x32_bf16 v[228:231], a[128:131], a[184:187], v[228:231]\n"
"s_waitcnt vmcnt(11)\n"
"v_bfi_b32 v80, v4, v80, v26\n"
"v_bfi_b32 v81, v5, v81, v26\n"
"v_mfma_f32_16x16x32_bf16 v[244:247], a[60:63], a[188:191], v[244:247]\n"
"s_waitcnt lgkmcnt(6)\n"
"ds_read_b128 v[40:43], v188 offset:1024\n"
"ds_read_b128 v[44:47], v189 offset:1024\n"
"v_mfma_f32_16x16x32_bf16 v[248:251], a[64:67], a[188:191], v[248:251]\n"
"s_waitcnt lgkmcnt(6)\n"
"s_add_u32 m0, s34, 10240\n"
"v_mfma_f32_16x16x32_bf16 v[252:255], a[68:71], a[188:191], v[252:255]\n"
"s_waitcnt lgkmcnt(4)\n"
"buffer_load_dwordx4 v191, s[8:11], s0 offen lds\n"
"v_cvt_pk_bf16_f32 v244, v244, v245\n"
"v_mfma_f32_16x16x32_bf16 v[104:107], a[132:135], a[188:191], v[104:107]\n"
"s_waitcnt lgkmcnt(2)\n"
"v_cvt_pk_bf16_f32 v245, v246, v247\n"
"v_cvt_pk_bf16_f32 v246, v248, v249\n"
"v_mfma_f32_16x16x32_bf16 v[108:111], a[136:139], a[188:191], v[108:111]\n"
"s_add_u32 m0, s34, 8192\n"
"v_cvt_pk_bf16_f32 v247, v250, v251\n"
"v_cvt_pk_bf16_f32 v252, v252, v253\n"
"v_mfma_f32_16x16x32_bf16 v[228:231], a[140:143], a[188:191], v[228:231]\n"
"s_add_u32 s0, s0, s33\n"
"buffer_load_dwordx4 v190, s[8:11], s1 offen lds\n"
"ds_write_b128 v22, v[244:247]\n"
"v_mfma_f32_16x16x32_bf16 v[204:207], a[0:3], v[32:35], 0\n"
"s_waitcnt vmcnt(11)\n"
"v_cvt_pk_bf16_f32 v253, v254, v255\n"
"v_cvt_pk_bf16_f32 v104, v104, v105\n"
"v_mfma_f32_16x16x32_bf16 v[208:211], a[4:7], v[32:35], 0\n"
"s_add_u32 m0, s34, 11264\n"
"v_cvt_pk_bf16_f32 v105, v106, v107\n"
"v_cvt_pk_bf16_f32 v106, v108, v109\n"
"v_mfma_f32_16x16x32_bf16 v[212:215], a[8:11], v[32:35], 0\n"
"s_mov_b32 s27, 0x20000\n"
"buffer_load_dwordx4 v191, s[8:11], s1 offen lds\n"
"ds_read_b128 v[48:51], v188 offset:2048\n"
"v_mfma_f32_16x16x32_bf16 v[216:219], a[72:75], v[32:35], 0\n"
"s_add_u32 s1, s1, s33\n"
"ds_read_b128 v[52:55], v189 offset:2048\n"
"v_cvt_pk_bf16_f32 v107, v110, v111\n"
"v_mfma_f32_16x16x32_bf16 v[220:223], a[76:79], v[32:35], 0\n"
"s_mov_b32 s19, 0x20000\n"
"ds_write_b128 v22, v[104:107] offset:128\n"
"v_mfma_f32_16x16x32_bf16 v[224:227], a[80:83], v[32:35], 0\n"
"s_add_u32 s78, s78, s71\n"
"v_cvt_pk_bf16_f32 v254, v228, v229\n"
"v_cvt_pk_bf16_f32 v255, v230, v231\n"
"v_mfma_f32_16x16x32_bf16 v[204:207], a[12:15], v[36:39], v[204:207]\n"
"s_waitcnt lgkmcnt(5)\n"
"v_bfi_b32 v86, v6, v86, v26\n"
"v_bfi_b32 v87, v7, v87, v26\n"
"v_mfma_f32_16x16x32_bf16 v[208:211], a[16:19], v[36:39], v[208:211]\n"
"s_add_u32 m0, s65, 154752\n"
"ds_write_b128 v22, v[252:255] offset:64\n"
"v_mfma_f32_16x16x32_bf16 v[212:215], a[20:23], v[36:39], v[212:215]\n"
"s_lshl_b32 s40, s40, 7\n"
"buffer_load_dwordx4 v27, s[12:15], s81 offen lds\n"
"v_bfi_b32 v88, v0, v88, v26\n"
"v_mfma_f32_16x16x32_bf16 v[216:219], a[84:87], v[36:39], v[216:219]\n"
"s_add_u32 s38, s38, 949120\n"
"v_bfi_b32 v89, v1, v89, v26\n"
"v_bfi_b32 v94, v2, v94, v26\n"
"v_mfma_f32_16x16x32_bf16 v[220:223], a[88:91], v[36:39], v[220:223]\n"
"s_sub_u32 s16, s16, 99456\n"
"s_subb_u32 s17, s17, 0\n"
"v_bfi_b32 v95, v3, v95, v26\n"
"v_mfma_f32_16x16x32_bf16 v[224:227], a[92:95], v[36:39], v[224:227]\n"
"s_mov_b32 s18, 173184\n"
"v_bfi_b32 v96, v0, v96, v26\n"
"v_bfi_b32 v97, v1, v97, v26\n"
"v_mfma_f32_16x16x32_bf16 v[204:207], a[24:27], v[40:43], v[204:207]\n"
"s_waitcnt lgkmcnt(5)\n"
"v_bfi_b32 v102, v2, v102, v26\n"
"v_bfi_b32 v103, v3, v103, v26\n"
"v_mfma_f32_16x16x32_bf16 v[208:211], a[28:31], v[40:43], v[208:211]\n"
"s_waitcnt vmcnt(11)\n"
"s_add_u32 m0, s65, 155776\n"
"v_mfma_f32_16x16x32_bf16 v[212:215], a[32:35], v[40:43], v[212:215]\n"
"s_add_u32 s80, s80, s79\n"
"buffer_load_dwordx4 v28, s[12:15], s81 offen lds\n"
"ds_read_b128 a[216:219], v70\n"
"v_mfma_f32_16x16x32_bf16 v[216:219], a[96:99], v[40:43], v[216:219]\n"
"ds_read_b128 a[220:223], v71\n"
"v_mfma_f32_16x16x32_bf16 v[220:223], a[100:103], v[40:43], v[220:223]\n"
"v_mfma_f32_16x16x32_bf16 v[224:227], a[104:107], v[40:43], v[224:227]\n"
"v_mfma_f32_16x16x32_bf16 v[204:207], a[36:39], v[44:47], v[204:207]\n"
"s_waitcnt lgkmcnt(5)\n"
"v_mfma_f32_16x16x32_bf16 v[208:211], a[40:43], v[44:47], v[208:211]\n"
"v_mfma_f32_16x16x32_bf16 v[212:215], a[44:47], v[44:47], v[212:215]\n"
"v_mfma_f32_16x16x32_bf16 v[216:219], a[108:111], v[44:47], v[216:219]\n"
"s_add_u32 m0, s65, 156800\n"
"v_mfma_f32_16x16x32_bf16 v[220:223], a[112:115], v[44:47], v[220:223]\n"
"s_waitcnt lgkmcnt(1)\n"
"buffer_load_dwordx4 v29, s[12:15], s81 offen lds\n"
"v_mfma_f32_16x16x32_bf16 v[224:227], a[116:119], v[44:47], v[224:227]\n"
"v_mfma_f32_16x16x32_bf16 v[204:207], a[48:51], v[48:51], v[204:207]\n"
"v_mfma_f32_16x16x32_bf16 v[208:211], a[52:55], v[48:51], v[208:211]\n"
"v_mfma_f32_16x16x32_bf16 v[212:215], a[56:59], v[48:51], v[212:215]\n"
"v_mfma_f32_16x16x32_bf16 v[216:219], a[120:123], v[48:51], v[216:219]\n"
"v_mfma_f32_16x16x32_bf16 v[220:223], a[124:127], v[48:51], v[220:223]\n"
"v_mfma_f32_16x16x32_bf16 v[224:227], a[128:131], v[48:51], v[224:227]\n"
"v_mfma_f32_16x16x32_bf16 v[204:207], a[60:63], v[52:55], v[204:207]\n"
"v_mfma_f32_16x16x32_bf16 v[208:211], a[64:67], v[52:55], v[208:211]\n"
"v_mfma_f32_16x16x32_bf16 v[212:215], a[68:71], v[52:55], v[212:215]\n"
"v_mfma_f32_16x16x32_bf16 v[216:219], a[132:135], v[52:55], v[216:219]\n"
"v_mfma_f32_16x16x32_bf16 v[220:223], a[136:139], v[52:55], v[220:223]\n"
"v_mfma_f32_16x16x32_bf16 v[224:227], a[140:143], v[52:55], v[224:227]\n"
"v_mfma_f32_16x16x32_bf16 v[244:247], a[0:3], a[216:219], 0\n"
"s_waitcnt lgkmcnt(0)\n"
"s_nop 0\n"
"v_cvt_pk_bf16_f32 v204, v204, v205\n"
"v_mfma_f32_16x16x32_bf16 v[248:251], a[4:7], a[216:219], 0\n"
"v_cvt_pk_bf16_f32 v205, v206, v207\n"
"v_cvt_pk_bf16_f32 v206, v208, v209\n"
"v_mfma_f32_16x16x32_bf16 v[252:255], a[8:11], a[216:219], 0\n"
"v_cvt_pk_bf16_f32 v207, v210, v211\n"
"v_cvt_pk_bf16_f32 v212, v212, v213\n"
"v_mfma_f32_16x16x32_bf16 v[104:107], a[72:75], a[216:219], 0\n"
"ds_write_b128 v186, v[204:207] offset:20256\n"
"v_mfma_f32_16x16x32_bf16 v[108:111], a[76:79], a[216:219], 0\n"
"v_cvt_pk_bf16_f32 v213, v214, v215\n"
"v_cvt_pk_bf16_f32 v216, v216, v217\n"
"v_mfma_f32_16x16x32_bf16 v[228:231], a[80:83], a[216:219], 0\n"
"s_waitcnt vmcnt(11)\n"
"v_cvt_pk_bf16_f32 v217, v218, v219\n"
"v_cvt_pk_bf16_f32 v218, v220, v221\n"
"v_mfma_f32_16x16x32_bf16 v[244:247], a[12:15], a[220:223], v[244:247]\n"
"ds_read_b128 a[224:227], v70 offset:1024\n"
"ds_read_b128 a[228:231], v71 offset:1024\n"
"v_mfma_f32_16x16x32_bf16 v[248:251], a[16:19], a[220:223], v[248:251]\n"
"v_cvt_pk_bf16_f32 v219, v222, v223\n"
"v_cvt_pk_bf16_f32 v214, v224, v225\n"
"v_mfma_f32_16x16x32_bf16 v[252:255], a[20:23], a[220:223], v[252:255]\n"
"ds_write_b128 v186, v[216:219] offset:20384\n"
"v_mfma_f32_16x16x32_bf16 v[104:107], a[84:87], a[220:223], v[104:107]\n"
"v_cvt_pk_bf16_f32 v215, v226, v227\n"
"v_mfma_f32_16x16x32_bf16 v[108:111], a[88:91], a[220:223], v[108:111]\n"
"ds_write_b128 v186, v[212:215] offset:20320\n"
"v_mfma_f32_16x16x32_bf16 v[228:231], a[92:95], a[220:223], v[228:231]\n"
"s_waitcnt vmcnt(9)\n"
"ds_read_b128 a[232:235], v70 offset:2048\n"
"ds_read_b128 a[236:239], v71 offset:2048\n"
"s_waitcnt lgkmcnt(5)\n"
"v_mfma_f32_16x16x32_bf16 v[244:247], a[24:27], a[224:227], v[244:247]\n"
"s_waitcnt lgkmcnt(4)\n"
"v_mfma_f32_16x16x32_bf16 v[248:251], a[28:31], a[224:227], v[248:251]\n"
"v_mfma_f32_16x16x32_bf16 v[252:255], a[32:35], a[224:227], v[252:255]\n"
"v_mfma_f32_16x16x32_bf16 v[104:107], a[96:99], a[224:227], v[104:107]\n"
"v_mfma_f32_16x16x32_bf16 v[108:111], a[100:103], a[224:227], v[108:111]\n"
"v_mfma_f32_16x16x32_bf16 v[228:231], a[104:107], a[224:227], v[228:231]\n"
"s_waitcnt vmcnt(7)\n"
"v_mfma_f32_16x16x32_bf16 v[244:247], a[36:39], a[228:231], v[244:247]\n"
"s_waitcnt lgkmcnt(1)\n"
"ds_read_b128 a[240:243], v188 offset:6144\n"
"ds_read_b128 a[244:247], v189 offset:6144\n"
"v_mfma_f32_16x16x32_bf16 v[248:251], a[40:43], a[228:231], v[248:251]\n"
"s_waitcnt lgkmcnt(2)\n"
"v_mfma_f32_16x16x32_bf16 v[252:255], a[44:47], a[228:231], v[252:255]\n"
"s_add_u32 m0, s35, 0\n"
"s_nop 0\n"
"buffer_load_dwordx4 v27, s[12:15], s3 offen lds\n"
"v_mfma_f32_16x16x32_bf16 v[104:107], a[108:111], a[228:231], v[104:107]\n"
"v_mfma_f32_16x16x32_bf16 v[108:111], a[112:115], a[228:231], v[108:111]\n"
"s_add_u32 m0, s35, 1024\n"
"v_mfma_f32_16x16x32_bf16 v[228:231], a[116:119], a[228:231], v[228:231]\n"
"buffer_load_dwordx4 v28, s[12:15], s3 offen lds\n"
"v_mfma_f32_16x16x32_bf16 v[244:247], a[48:51], a[232:235], v[244:247]\n"
"v_mfma_f32_16x16x32_bf16 v[248:251], a[52:55], a[232:235], v[248:251]\n"
"v_mfma_f32_16x16x32_bf16 v[252:255], a[56:59], a[232:235], v[252:255]\n"
"s_waitcnt lgkmcnt(1)\n"
"s_add_u32 m0, s35, 2048\n"
"v_mfma_f32_16x16x32_bf16 v[104:107], a[120:123], a[232:235], v[104:107]\n"
"s_waitcnt vmcnt(7)\n"
"buffer_load_dwordx4 v29, s[12:15], s3 offen lds\n"
"v_mfma_f32_16x16x32_bf16 v[108:111], a[124:127], a[232:235], v[108:111]\n"
"ds_read_b128 a[248:251], v188 offset:7168\n"
"ds_read_b128 a[252:255], v189 offset:7168\n"
"v_mfma_f32_16x16x32_bf16 v[228:231], a[128:131], a[232:235], v[228:231]\n"
"v_mfma_f32_16x16x32_bf16 v[244:247], a[60:63], a[236:239], v[244:247]\n"
"s_add_u32 m0, s34, 0\n"
"v_mfma_f32_16x16x32_bf16 v[248:251], a[64:67], a[236:239], v[248:251]\n"
"buffer_load_dwordx4 v190, s[8:11], s32 offen lds\n"
"v_mfma_f32_16x16x32_bf16 v[252:255], a[68:71], a[236:239], v[252:255]\n"
"v_mfma_f32_16x16x32_bf16 v[104:107], a[132:135], a[236:239], v[104:107]\n"
"v_mfma_f32_16x16x32_bf16 v[108:111], a[136:139], a[236:239], v[108:111]\n"
"s_add_u32 m0, s34, 3072\n"
"v_mfma_f32_16x16x32_bf16 v[228:231], a[140:143], a[236:239], v[228:231]\n"
"v_cvt_pk_bf16_f32 v244, v244, v245\n"
"v_cvt_pk_bf16_f32 v245, v246, v247\n"
"v_mfma_f32_16x16x32_bf16 v[120:123], a[0:3], a[240:243], 0\n"
"s_waitcnt lgkmcnt(2)\n"
"buffer_load_dwordx4 v191, s[8:11], s32 offen lds\n"
"v_cvt_pk_bf16_f32 v246, v248, v249\n"
"v_mfma_f32_16x16x32_bf16 v[124:127], a[4:7], a[240:243], 0\n"
"s_add_u32 s32, s32, s33\n"
"v_cvt_pk_bf16_f32 v247, v250, v251\n"
"v_cvt_pk_bf16_f32 v252, v252, v253\n"
"v_mfma_f32_16x16x32_bf16 v[128:131], a[8:11], a[240:243], 0\n"
"s_add_u32 m0, s34, 1024\n"
"ds_write_b128 v186, v[244:247] offset:33760\n"
"v_mfma_f32_16x16x32_bf16 v[132:135], a[72:75], a[240:243], 0\n"
"buffer_load_dwordx4 v190, s[8:11], s0 offen lds\n"
"v_cvt_pk_bf16_f32 v253, v254, v255\n"
"v_mfma_f32_16x16x32_bf16 v[136:139], a[76:79], a[240:243], 0\n"
"v_cvt_pk_bf16_f32 v104, v104, v105\n"
"v_cvt_pk_bf16_f32 v105, v106, v107\n"
"v_mfma_f32_16x16x32_bf16 v[140:143], a[80:83], a[240:243], 0\n"
"v_cvt_pk_bf16_f32 v106, v108, v109\n"
"v_cvt_pk_bf16_f32 v107, v110, v111\n"
"v_mfma_f32_16x16x32_bf16 v[120:123], a[12:15], a[244:247], v[120:123]\n"
"s_waitcnt lgkmcnt(2)\n"
"ds_write_b128 v186, v[104:107] offset:33888\n"
"v_mfma_f32_16x16x32_bf16 v[124:127], a[16:19], a[244:247], v[124:127]\n"
"s_waitcnt vmcnt(9)\n"
"v_cvt_pk_bf16_f32 v254, v228, v229\n"
"v_cvt_pk_bf16_f32 v255, v230, v231\n"
"v_mfma_f32_16x16x32_bf16 v[128:131], a[20:23], a[244:247], v[128:131]\n"
"s_add_u32 m0, s34, 4096\n"
"s_nop 0\n"
"buffer_load_dwordx4 v191, s[8:11], s0 offen lds\n"
"v_mfma_f32_16x16x32_bf16 v[132:135], a[84:87], a[244:247], v[132:135]\n"
"s_add_u32 s0, s0, s33\n"
"ds_read_b128 v[56:59], v188 offset:8192\n"
"ds_read_b128 v[60:63], v189 offset:8192\n"
"v_mfma_f32_16x16x32_bf16 v[136:139], a[88:91], a[244:247], v[136:139]\n"
"s_add_u32 m0, s34, 2048\n"
"ds_write_b128 v186, v[252:255] offset:33824\n"
"v_mfma_f32_16x16x32_bf16 v[140:143], a[92:95], a[244:247], v[140:143]\n"
"buffer_load_dwordx4 v190, s[8:11], s1 offen lds\n"
"v_mov_b64 v[252:253], 0\n"
"v_mfma_f32_16x16x32_bf16 v[120:123], a[24:27], a[248:251], v[120:123]\n"
"s_waitcnt lgkmcnt(5)\n"
"v_mov_b64 v[254:255], 0\n"
"v_mfma_f32_16x16x32_bf16 v[124:127], a[28:31], a[248:251], v[124:127]\n"
"ds_read_b64 v[104:105], v20 offset:1088\n"
"ds_read_b64 v[110:111], v20 offset:1152\n"
"v_mfma_f32_16x16x32_bf16 v[128:131], a[32:35], a[248:251], v[128:131]\n"
"s_add_u32 m0, s34, 5120\n"
"v_subrev_u32 v20, s49, v20\n"
"v_mfma_f32_16x16x32_bf16 v[132:135], a[96:99], a[248:251], v[132:135]\n"
"buffer_load_dwordx4 v191, s[8:11], s1 offen lds\n"
"s_add_u32 s1, s1, s33\n"
"v_mfma_f32_16x16x32_bf16 v[136:139], a[100:103], a[248:251], v[136:139]\n"
"v_mfma_f32_16x16x32_bf16 v[140:143], a[104:107], a[248:251], v[140:143]\n"
"s_add_u32 m0, s34, 6144\n"
"v_mfma_f32_16x16x32_bf16 v[120:123], a[36:39], a[252:255], v[120:123]\n"
"s_waitcnt lgkmcnt(4)\n"
"v_mfma_f32_16x16x32_bf16 v[124:127], a[40:43], a[252:255], v[124:127]\n"
"buffer_load_dwordx4 v190, s[8:11], s32 offen lds\n"
"v_mfma_f32_16x16x32_bf16 v[128:131], a[44:47], a[252:255], v[128:131]\n"
"s_waitcnt lgkmcnt(1)\n"
"v_mfma_f32_16x16x32_bf16 v[132:135], a[108:111], a[252:255], v[132:135]\n"
"s_waitcnt lgkmcnt(0)\n"
"v_bfi_b32 v104, v8, v104, v26\n"
"v_bfi_b32 v105, v9, v105, v26\n"
"v_mfma_f32_16x16x32_bf16 v[136:139], a[112:115], a[252:255], v[136:139]\n"
"v_bfi_b32 v110, v10, v110, v26\n"
"v_bfi_b32 v111, v11, v111, v26\n"
"v_mfma_f32_16x16x32_bf16 v[140:143], a[116:119], a[252:255], v[140:143]\n"
"v_mfma_f32_16x16x32_bf16 v[120:123], a[48:51], v[56:59], v[120:123]\n"
"v_mfma_f32_16x16x32_bf16 v[124:127], a[52:55], v[56:59], v[124:127]\n"
"v_mfma_f32_16x16x32_bf16 v[128:131], a[56:59], v[56:59], v[128:131]\n"
"s_add_u32 m0, s34, 9216\n"
"s_nop 0\n"
"buffer_load_dwordx4 v191, s[8:11], s32 offen lds\n"
"v_mfma_f32_16x16x32_bf16 v[132:135], a[120:123], v[56:59], v[132:135]\n"
"s_add_u32 s32, s32, s33\n"
"v_mfma_f32_16x16x32_bf16 v[136:139], a[124:127], v[56:59], v[136:139]\n"
"v_mfma_f32_16x16x32_bf16 v[140:143], a[128:131], v[56:59], v[140:143]\n"
"v_mfma_f32_16x16x32_bf16 v[120:123], a[60:63], v[60:63], v[120:123]\n"
"s_add_u32 m0, s34, 7168\n"
"s_nop 0\n"
"buffer_load_dwordx4 v190, s[8:11], s0 offen lds\n"
"v_mfma_f32_16x16x32_bf16 v[124:127], a[64:67], v[60:63], v[124:127]\n"
"v_mfma_f32_16x16x32_bf16 v[128:131], a[68:71], v[60:63], v[128:131]\n"
"v_mfma_f32_16x16x32_bf16 v[132:135], a[132:135], v[60:63], v[132:135]\n"
"v_mfma_f32_16x16x32_bf16 v[136:139], a[136:139], v[60:63], v[136:139]\n"
"s_add_u32 m0, s34, 10240\n"
"s_nop 0\n"
"buffer_load_dwordx4 v191, s[8:11], s0 offen lds\n"
"v_mfma_f32_16x16x32_bf16 v[140:143], a[140:143], v[60:63], v[140:143]\n"
"s_add_u32 s0, s0, s33\n"
"v_cvt_pk_bf16_f32 v120, v120, v121\n"
"v_cvt_pk_bf16_f32 v121, v122, v123\n"
"v_cvt_pk_bf16_f32 v122, v124, v125\n"
"v_cvt_pk_bf16_f32 v123, v126, v127\n"
"v_cvt_pk_bf16_f32 v128, v128, v129\n"
"v_cvt_pk_bf16_f32 v132, v132, v133\n"
"v_cvt_pk_bf16_f32 v129, v130, v131\n"
"v_cvt_pk_bf16_f32 v133, v134, v135\n"
"v_cvt_pk_bf16_f32 v134, v136, v137\n"
"v_cvt_pk_bf16_f32 v135, v138, v139\n"
"s_add_u32 m0, s34, 8192\n"
"v_cvt_pk_bf16_f32 v130, v140, v141\n"
"buffer_load_dwordx4 v190, s[8:11], s1 offen lds\n"
"v_cvt_pk_bf16_f32 v131, v142, v143\n"
"ds_write_b128 v186, v[120:123] offset:47264\n"
"s_add_u32 m0, s34, 11264\n"
"s_nop 0\n"
"buffer_load_dwordx4 v191, s[8:11], s1 offen lds\n"
"s_add_u32 s1, s1, s33\n"
"s_waitcnt vmcnt(12)\n"
"s_barrier\n"
"ds_read_b64_tr_b16 a[144:145], v68\n"
"ds_read_b64_tr_b16 a[146:147], v68 offset:1536\n"
"ds_read_b64_tr_b16 a[148:149], v68 offset:32\n"
"ds_read_b64_tr_b16 a[150:151], v68 offset:1568\n"
"ds_read_b64_tr_b16 a[152:153], v68 offset:64\n"
"ds_read_b64_tr_b16 a[154:155], v68 offset:1600\n"
"ds_read_b64_tr_b16 a[156:157], v68 offset:6144\n"
"ds_read_b64_tr_b16 a[158:159], v68 offset:7680\n"
"ds_read_b64_tr_b16 a[160:161], v68 offset:6176\n"
"ds_read_b64_tr_b16 a[162:163], v68 offset:7712\n"
"ds_read_b64_tr_b16 a[164:165], v68 offset:6208\n"
"ds_read_b64_tr_b16 a[166:167], v68 offset:7744\n"
"ds_read_b64_tr_b16 a[168:169], v68 offset:12288\n"
"ds_read_b64_tr_b16 a[170:171], v68 offset:13824\n"
"ds_read_b64_tr_b16 a[172:173], v68 offset:12320\n"
"ds_read_b64_tr_b16 a[174:175], v68 offset:13856\n"
"ds_read_b64_tr_b16 a[176:177], v68 offset:12352\n"
"ds_read_b64_tr_b16 a[178:179], v68 offset:13888\n"
"ds_read_b64_tr_b16 a[180:181], v68 offset:18432\n"
"ds_read_b64_tr_b16 a[182:183], v68 offset:19968\n"
"s_waitcnt lgkmcnt(15)\n"
"v_mfma_f32_16x16x32_bf16 v[204:207], a[144:147], v[32:35], 0\n"
"ds_read_b64_tr_b16 a[184:185], v68 offset:18464\n"
"ds_read_b64_tr_b16 a[186:187], v68 offset:20000\n"
"v_mfma_f32_16x16x32_bf16 v[208:211], a[148:151], v[32:35], 0\n"
"s_waitcnt lgkmcnt(15)\n"
"ds_read_b64_tr_b16 a[188:189], v68 offset:18496\n"
"ds_read_b64_tr_b16 a[190:191], v68 offset:20032\n"
"v_mfma_f32_16x16x32_bf16 v[212:215], a[152:155], v[32:35], 0\n"
"s_waitcnt lgkmcnt(15)\n"
"ds_read_b64_tr_b16 a[192:193], v68 offset:24576\n"
"ds_read_b64_tr_b16 a[194:195], v68 offset:26112\n"
"v_mfma_f32_16x16x32_bf16 v[204:207], a[156:159], v[36:39], v[204:207]\n"
"s_waitcnt lgkmcnt(15)\n"
"ds_read_b64_tr_b16 a[196:197], v68 offset:24608\n"
"ds_read_b64_tr_b16 a[198:199], v68 offset:26144\n"
"v_mfma_f32_16x16x32_bf16 v[208:211], a[160:163], v[36:39], v[208:211]\n"
"s_waitcnt lgkmcnt(15)\n"
"ds_read_b64_tr_b16 a[200:201], v68 offset:24640\n"
"ds_read_b64_tr_b16 a[202:203], v68 offset:26176\n"
"v_mfma_f32_16x16x32_bf16 v[212:215], a[164:167], v[36:39], v[212:215]\n"
"s_waitcnt lgkmcnt(15)\n"
"ds_read_b64_tr_b16 a[204:205], v68 offset:27648\n"
"ds_read_b64_tr_b16 a[206:207], v68 offset:29184\n"
"v_mfma_f32_16x16x32_bf16 v[204:207], a[168:171], v[40:43], v[204:207]\n"
"s_waitcnt lgkmcnt(15)\n"
"ds_read_b64_tr_b16 a[208:209], v68 offset:27680\n"
"ds_read_b64_tr_b16 a[210:211], v68 offset:29216\n"
"v_mfma_f32_16x16x32_bf16 v[208:211], a[172:175], v[40:43], v[208:211]\n"
"s_waitcnt lgkmcnt(15)\n"
"ds_read_b64_tr_b16 a[212:213], v68 offset:27712\n"
"ds_read_b64_tr_b16 a[214:215], v68 offset:29248\n"
"v_mfma_f32_16x16x32_bf16 v[212:215], a[176:179], v[40:43], v[212:215]\n"
"s_waitcnt lgkmcnt(15)\n"
"buffer_load_dword v200, off, s[28:31], s39 sc1\n"
"ds_write_b128 v186, v[128:131] offset:47328\n"
"v_mfma_f32_16x16x32_bf16 v[204:207], a[180:183], v[44:47], v[204:207]\n"
"s_waitcnt lgkmcnt(13)\n"
"v_mfma_f32_16x16x32_bf16 v[208:211], a[184:187], v[44:47], v[208:211]\n"
"s_waitcnt lgkmcnt(11)\n"
"ds_write_b128 v186, v[132:135] offset:47392\n"
"v_mfma_f32_16x16x32_bf16 v[212:215], a[188:191], v[44:47], v[212:215]\n"
"s_waitcnt lgkmcnt(8)\n"
"v_mfma_f32_16x16x32_bf16 v[204:207], a[192:195], v[48:51], v[204:207]\n"
"s_waitcnt lgkmcnt(6)\n"
"v_mfma_f32_16x16x32_bf16 v[208:211], a[196:199], v[48:51], v[208:211]\n"
"s_waitcnt lgkmcnt(4)\n"
"v_mfma_f32_16x16x32_bf16 v[212:215], a[200:203], v[48:51], v[212:215]\n"
"s_waitcnt lgkmcnt(2)\n"
"v_mfma_f32_16x16x32_bf16 v[204:207], a[204:207], v[52:55], v[204:207]\n"
"v_mfma_f32_16x16x32_bf16 v[208:211], a[208:211], v[52:55], v[208:211]\n"
"v_mfma_f32_16x16x32_bf16 v[212:215], a[212:215], v[52:55], v[212:215]\n"
"v_mfma_f32_16x16x32_bf16 v[216:219], a[144:147], a[216:219], 0\n"
"s_waitcnt lgkmcnt(0)\n"
"s_barrier\n"
"v_mfma_f32_16x16x32_bf16 v[216:219], a[156:159], a[220:223], v[216:219]\n"
"ds_read_b128 v[112:115], v178\n"
"ds_read_b128 v[120:123], v179\n"
"v_mfma_f32_16x16x32_bf16 v[220:223], a[148:151], a[216:219], 0\n"
"ds_read_b128 v[116:119], v178 offset:13504\n"
"v_cvt_pk_bf16_f32 v144, v204, v205\n"
"v_mfma_f32_16x16x32_bf16 v[216:219], a[168:171], a[224:227], v[216:219]\n"
"ds_read_b128 v[124:127], v179 offset:13504\n"
"v_cvt_pk_bf16_f32 v145, v206, v207\n"
"v_mfma_f32_16x16x32_bf16 v[220:223], a[160:163], a[220:223], v[220:223]\n"
"ds_read_b64 v[82:83], v178 offset:64\n"
"v_cvt_pk_bf16_f32 v146, v208, v209\n"
"v_mfma_f32_16x16x32_bf16 v[216:219], a[180:183], a[228:231], v[216:219]\n"
"ds_read_b64 v[84:85], v179 offset:64\n"
"v_cvt_pk_bf16_f32 v147, v210, v211\n"
"v_mfma_f32_16x16x32_bf16 v[220:223], a[172:175], a[224:227], v[220:223]\n"
"ds_read_b64 v[90:91], v178 offset:13568\n"
"v_cvt_pk_bf16_f32 v158, v212, v213\n"
"v_mfma_f32_16x16x32_bf16 v[216:219], a[192:195], a[232:235], v[216:219]\n"
"ds_read_b64 v[92:93], v179 offset:13568\n"
"v_cvt_pk_bf16_f32 v159, v214, v215\n"
"v_mfma_f32_16x16x32_bf16 v[220:223], a[184:187], a[228:231], v[220:223]\n"
"ds_read_b128 v[128:131], v178 offset:27008\n"
"v_mov_b64 v[68:69], -1\n"
"v_mfma_f32_16x16x32_bf16 v[216:219], a[204:207], a[236:239], v[216:219]\n"
"s_waitcnt lgkmcnt(8)\n"
"ds_read_b128 v[136:139], v179 offset:27008\n"
"v_mov_b64 v[70:71], -1\n"
"v_mfma_f32_16x16x32_bf16 v[112:115], v[112:115], v[144:147], 0\n"
"s_waitcnt lgkmcnt(8)\n"
"ds_read_b128 v[132:135], v180\n"
"v_mfma_f32_16x16x32_bf16 v[120:123], v[120:123], v[144:147], 0\n"
"s_waitcnt lgkmcnt(8)\n"
"ds_read_b128 v[140:143], v181\n"
"v_cvt_pk_bf16_f32 v148, v216, v217\n"
"v_mfma_f32_16x16x32_bf16 v[116:119], v[116:119], v[144:147], 0\n"
"s_waitcnt lgkmcnt(8)\n"
"ds_read_b64 v[98:99], v178 offset:27072\n"
".Lic0%=:\n"
"s_cbranch_execz .Lib0%=\n"
"v_cvt_pk_bf16_f32 v149, v218, v219\n"
"v_mfma_f32_16x16x32_bf16 v[124:127], v[124:127], v[144:147], 0\n"
"s_waitcnt lgkmcnt(8)\n"
"ds_read_b64 v[100:101], v179 offset:27072\n"
"v_subrev_u32 v178, s48, v178\n"
"v_mfma_f32_16x16x32_bf16 v[112:115], v[80:83], v[156:159], v[112:115]\n"
"s_waitcnt lgkmcnt(8)\n"
"ds_read_b64 v[106:107], v180 offset:64\n"
"v_subrev_u32 v179, s48, v179\n"
"v_mfma_f32_16x16x32_bf16 v[120:123], v[84:87], v[158:161], v[120:123]\n"
"s_waitcnt lgkmcnt(8)\n"
"ds_read_b64 v[108:109], v181 offset:64\n"
"v_subrev_u32 v180, s48, v180\n"
"v_mfma_f32_16x16x32_bf16 v[116:119], v[88:91], v[156:159], v[116:119]\n"
"s_waitcnt lgkmcnt(8)\n"
"ds_read_b64_tr_b16 v[244:245], v182 offset:128\n"
"v_subrev_u32 v181, s48, v181\n"
"v_mfma_f32_16x16x32_bf16 v[124:127], v[92:95], v[158:161], v[124:127]\n"
"s_waitcnt lgkmcnt(8)\n"
"ds_read_b64_tr_b16 v[246:247], v183 offset:128\n"
"v_max_f32 v112, v112, v120\n"
"v_mfma_f32_16x16x32_bf16 v[128:131], v[128:131], v[144:147], 0\n"
"s_waitcnt vmcnt(11) lgkmcnt(8)\n"
"ds_read_b64_tr_b16 v[248:249], v182 offset:896\n"
"v_max_f32 v113, v113, v121\n"
"v_mfma_f32_16x16x32_bf16 v[136:139], v[136:139], v[144:147], 0\n"
"s_waitcnt lgkmcnt(8)\n"
"ds_read_b128 v[32:35], v188\n"
"v_max_f32 v114, v114, v122\n"
"v_mfma_f32_16x16x32_bf16 v[132:135], v[132:135], v[144:147], 0\n"
"s_waitcnt lgkmcnt(8)\n"
"ds_read_b128 v[36:39], v189\n"
"v_max_f32 v115, v115, v123\n"
"v_mfma_f32_16x16x32_bf16 v[140:143], v[140:143], v[144:147], 0\n"
"s_waitcnt lgkmcnt(8)\n"
"ds_read_b64_tr_b16 v[250:251], v183 offset:896\n"
"v_max_f32 v116, v116, v124\n"
"v_mfma_f32_16x16x32_bf16 v[128:131], v[96:99], v[156:159], v[128:131]\n"
"s_waitcnt lgkmcnt(8)\n"
"ds_read_b64_tr_b16 v[120:121], v182 offset:136\n"
"v_max_f32 v117, v117, v125\n"
"v_mfma_f32_16x16x32_bf16 v[136:139], v[100:103], v[158:161], v[136:139]\n"
"s_waitcnt lgkmcnt(8)\n"
"ds_read_b64_tr_b16 v[122:123], v183 offset:136\n"
"v_max_f32 v118, v118, v126\n"
"v_mfma_f32_16x16x32_bf16 v[132:135], v[104:107], v[156:159], v[132:135]\n"
"s_waitcnt lgkmcnt(8)\n"
"ds_read_b64_tr_b16 v[124:125], v182 offset:904\n"
"v_max_f32 v119, v119, v127\n"
"v_mfma_f32_16x16x32_bf16 v[140:143], v[108:111], v[158:161], v[140:143]\n"
"ds_read_b64_tr_b16 v[126:127], v183 offset:904\n"
"v_max_f32 v170, v112, v113\n"
"v_mfma_f32_16x16x32_bf16 v[220:223], a[196:199], a[232:235], v[220:223]\n"
"v_max_f32 v171, v114, v115\n"
"v_max3_f32 v170, v170, v116, v117\n"
"v_mfma_f32_16x16x32_bf16 v[220:223], a[208:211], a[236:239], v[220:223]\n"
"v_max3_f32 v171, v171, v118, v119\n"
"v_max_f32 v128, v128, v136\n"
"v_mfma_f32_16x16x32_bf16 v[224:227], a[152:155], a[216:219], 0\n"
"s_waitcnt vmcnt(9) lgkmcnt(6)\n"
"v_max_f32 v129, v129, v137\n"
"v_max_f32 v130, v130, v138\n"
"v_mfma_f32_16x16x32_bf16 v[212:215], a[0:3], v[32:35], 0\n"
"s_waitcnt lgkmcnt(5)\n"
"ds_read_b128 v[40:43], v188 offset:1024\n"
"ds_read_b128 v[44:47], v189 offset:1024\n"
"v_mfma_f32_16x16x32_bf16 v[216:219], a[4:7], v[32:35], 0\n"
"s_add_u32 m0, s34, 0\n"
"ds_read_b64_tr_b16 v[136:137], v182 offset:72\n"
"buffer_load_dwordx4 v190, s[8:11], s32 offen lds\n"
"v_mfma_f32_16x16x32_bf16 v[212:215], a[12:15], v[36:39], v[212:215]\n"
"v_max_f32 v131, v131, v139\n"
"v_max3_f32 v170, v170, v128, v129\n"
"v_mfma_f32_16x16x32_bf16 v[216:219], a[16:19], v[36:39], v[216:219]\n"
"ds_read_b64_tr_b16 v[138:139], v183 offset:72\n"
"v_max3_f32 v171, v171, v130, v131\n"
"v_mfma_f32_16x16x32_bf16 v[224:227], a[164:167], a[220:223], v[224:227]\n"
"v_max_f32 v132, v132, v140\n"
"v_max_f32 v133, v133, v141\n"
"v_mfma_f32_16x16x32_bf16 v[228:231], a[144:147], a[240:243], 0\n"
"ds_read_b64_tr_b16 v[140:141], v182 offset:840\n"
"v_max_f32 v134, v134, v142\n"
"v_mfma_f32_16x16x32_bf16 v[224:227], a[176:179], a[224:227], v[224:227]\n"
"s_waitcnt vmcnt(8)\n"
"v_max_f32 v135, v135, v143\n"
"v_max3_f32 v170, v170, v132, v133\n"
"v_mfma_f32_16x16x32_bf16 v[228:231], a[156:159], a[244:247], v[228:231]\n"
"ds_read_b128 v[48:51], v188 offset:2048\n"
"ds_read_b128 v[52:55], v189 offset:2048\n"
"v_mfma_f32_16x16x32_bf16 v[204:207], a[148:151], a[240:243], 0\n"
"s_waitcnt lgkmcnt(6)\n"
"ds_read_b64_tr_b16 v[142:143], v183 offset:840\n"
"v_max3_f32 v171, v171, v134, v135\n"
"v_mfma_f32_16x16x32_bf16 v[212:215], a[24:27], v[40:43], v[212:215]\n"
"s_waitcnt lgkmcnt(6)\n"
"v_max_f32 v170, v170, v171\n"
"v_mov_b32 v171, v170\n"
"v_mfma_f32_16x16x32_bf16 v[216:219], a[28:31], v[40:43], v[216:219]\n"
"s_nop 0\n"
"v_permlane16_swap_b32 v170, v171\n"
"v_mfma_f32_16x16x32_bf16 v[212:215], a[36:39], v[44:47], v[212:215]\n"
"s_add_u32 m0, s34, 3072\n"
"v_max_f32 v170, v170, v171\n"
"v_mov_b32 v171, v170\n"
"v_mfma_f32_16x16x32_bf16 v[216:219], a[40:43], v[44:47], v[216:219]\n"
"buffer_load_dwordx4 v191, s[8:11], s32 offen lds\n"
"v_cvt_pk_bf16_f32 v150, v220, v221\n"
"v_mfma_f32_16x16x32_bf16 v[224:227], a[188:191], a[228:231], v[224:227]\n"
"s_add_u32 s32, s32, s33\n"
"v_cvt_pk_bf16_f32 v151, v222, v223\n"
"v_mov_b64 v[98:99], 0\n"
"v_mfma_f32_16x16x32_bf16 v[220:223], a[8:11], v[32:35], 0\n"
"v_permlane32_swap_b32 v170, v171\n"
"v_mfma_f32_16x16x32_bf16 v[220:223], a[20:23], v[36:39], v[220:223]\n"
"s_waitcnt lgkmcnt(2)\n"
"v_max_f32 v170, v170, v171\n"
"v_mul_f32 v170, 0xbe553b95, v170\n"
"v_mfma_f32_16x16x32_bf16 v[220:223], a[32:35], v[40:43], v[220:223]\n"
"v_fmamk_f32 v112, v112, 0x3e553b95, v170\n"
"v_fmamk_f32 v113, v113, 0x3e553b95, v170\n"
"v_mfma_f32_16x16x32_bf16 v[220:223], a[44:47], v[44:47], v[220:223]\n"
"v_fmamk_f32 v114, v114, 0x3e553b95, v170\n"
"v_fmamk_f32 v115, v115, 0x3e553b95, v170\n"
"v_mfma_f32_16x16x32_bf16 v[212:215], a[48:51], v[48:51], v[212:215]\n"
"s_waitcnt lgkmcnt(1)\n"
"v_fmamk_f32 v116, v116, 0x3e553b95, v170\n"
"v_fmamk_f32 v117, v117, 0x3e553b95, v170\n"
"v_mfma_f32_16x16x32_bf16 v[216:219], a[52:55], v[48:51], v[216:219]\n"
"v_fmamk_f32 v118, v118, 0x3e553b95, v170\n"
"v_fmamk_f32 v119, v119, 0x3e553b95, v170\n"
"v_mfma_f32_16x16x32_bf16 v[220:223], a[56:59], v[48:51], v[220:223]\n"
"v_exp_f32 v112, v112\n"
"v_mfma_f32_16x16x32_bf16 v[212:215], a[60:63], v[52:55], v[212:215]\n"
"v_exp_f32 v113, v113\n"
"v_mfma_f32_16x16x32_bf16 v[216:219], a[64:67], v[52:55], v[216:219]\n"
"s_add_u32 m0, s34, 1024\n"
"v_exp_f32 v114, v114\n"
"v_mfma_f32_16x16x32_bf16 v[220:223], a[68:71], v[52:55], v[220:223]\n"
"buffer_load_dwordx4 v190, s[8:11], s0 offen lds\n"
"v_mov_b64 v[100:101], 0\n"
"v_mfma_f32_16x16x32_bf16 v[228:231], a[168:171], a[248:251], v[228:231]\n"
"v_exp_f32 v115, v115\n"
"v_mfma_f32_16x16x32_bf16 v[204:207], a[160:163], a[244:247], v[204:207]\n"
"v_exp_f32 v116, v116\n"
"v_mfma_f32_16x16x32_bf16 v[224:227], a[200:203], a[232:235], v[224:227]\n"
"v_exp_f32 v117, v117\n"
"v_mfma_f32_16x16x32_bf16 v[228:231], a[180:183], a[252:255], v[228:231]\n"
"v_exp_f32 v118, v118\n"
"v_mfma_f32_16x16x32_bf16 v[204:207], a[172:175], a[248:251], v[204:207]\n"
"v_exp_f32 v119, v119\n"
"v_mfma_f32_16x16x32_bf16 v[224:227], a[212:215], a[236:239], v[224:227]\n"
"v_cvt_pk_bf16_f32 v82, v112, v114\n"
"v_cvt_pk_bf16_f32 v83, v113, v115\n"
"v_mfma_f32_16x16x32_bf16 v[228:231], a[192:195], v[56:59], v[228:231]\n"
"ds_read_b64_tr_b16 v[112:113], v182 offset:27136\n"
"ds_read_b64_tr_b16 v[114:115], v183 offset:27136\n"
"v_mfma_f32_16x16x32_bf16 v[204:207], a[184:187], a[252:255], v[204:207]\n"
"v_cvt_pk_bf16_f32 v84, v116, v118\n"
"v_cvt_pk_bf16_f32 v85, v117, v119\n"
"v_mfma_f32_16x16x32_bf16 v[228:231], a[204:207], v[60:63], v[228:231]\n"
"ds_read_b64_tr_b16 v[116:117], v182 offset:27904\n"
"ds_read_b64_tr_b16 v[118:119], v183 offset:27904\n"
"v_smfmac_f32_16x16x64_bf16 v[252:255], v[82:85], v[244:251], v66\n"
"s_waitcnt vmcnt(3)\n"
"ds_read_b64_tr_b16 v[244:245], v182 offset:27144\n"
"v_smfmac_f32_16x16x64_bf16 v[98:101], v[82:85], v[120:127], v66\n"
"s_add_u32 m0, s34, 4096\n"
"s_nop 0\n"
"buffer_load_dwordx4 v191, s[8:11], s0 offen lds\n"
"v_mfma_f32_16x16x32_bf16 v[204:207], a[196:199], v[56:59], v[204:207]\n"
"s_add_u32 s0, s0, s33\n"
"ds_read_b64_tr_b16 v[246:247], v183 offset:27144\n"
"ds_read_b64_tr_b16 v[248:249], v182 offset:27912\n"
"v_mfma_f32_16x16x32_bf16 v[204:207], a[208:211], v[60:63], v[204:207]\n"
"ds_read_b64_tr_b16 v[250:251], v183 offset:27912\n"
"v_mov_b64 v[106:107], 0\n"
"v_mfma_f32_16x16x32_bf16 v[208:211], a[152:155], a[240:243], 0\n"
"ds_read_b64_tr_b16 v[120:121], v182 offset:27080\n"
"v_mov_b64 v[108:109], 0\n"
"v_mfma_f32_16x16x32_bf16 v[208:211], a[164:167], a[244:247], v[208:211]\n"
"s_waitcnt lgkmcnt(9)\n"
"ds_read_b64_tr_b16 v[122:123], v183 offset:27080\n"
"v_fmamk_f32 v128, v128, 0x3e553b95, v170\n"
"v_smfmac_f32_16x16x64_bf16 v[106:109], v[82:85], v[136:143], v66\n"
"ds_read_b64_tr_b16 v[124:125], v182 offset:27848\n"
"v_mfma_f32_16x16x32_bf16 v[82:85], v[172:175], v[82:85], 0\n"
"ds_read_b64_tr_b16 v[126:127], v183 offset:27848\n"
"v_fmamk_f32 v129, v129, 0x3e553b95, v170\n"
"v_mfma_f32_16x16x32_bf16 v[208:211], a[176:179], a[248:251], v[208:211]\n"
"v_fmamk_f32 v130, v130, 0x3e553b95, v170\n"
"v_fmamk_f32 v131, v131, 0x3e553b95, v170\n"
"v_mfma_f32_16x16x32_bf16 v[208:211], a[188:191], a[252:255], v[208:211]\n"
"v_fmamk_f32 v132, v132, 0x3e553b95, v170\n"
"v_fmamk_f32 v133, v133, 0x3e553b95, v170\n"
"v_mfma_f32_16x16x32_bf16 v[208:211], a[200:203], v[56:59], v[208:211]\n"
"v_fmamk_f32 v134, v134, 0x3e553b95, v170\n"
"v_fmamk_f32 v135, v135, 0x3e553b95, v170\n"
"v_mfma_f32_16x16x32_bf16 v[208:211], a[212:215], v[60:63], v[208:211]\n"
"s_add_u32 m0, s34, 2048\n"
"v_exp_f32 v128, v128\n"
"buffer_load_dwordx4 v190, s[8:11], s1 offen lds\n"
"v_exp_f32 v129, v129\n"
"v_exp_f32 v130, v130\n"
"v_exp_f32 v131, v131\n"
"v_exp_f32 v132, v132\n"
"v_exp_f32 v133, v133\n"
"v_exp_f32 v134, v134\n"
"v_exp_f32 v135, v135\n"
"v_cvt_pk_bf16_f32 v90, v128, v130\n"
"v_cvt_pk_bf16_f32 v91, v129, v131\n"
"v_cvt_pk_bf16_f32 v92, v132, v134\n"
"v_cvt_pk_bf16_f32 v93, v133, v135\n"
"v_and_b32 v200, 0xffffff00, v200\n"
"v_add_u32 v200, 0x100, v200\n"
"v_sub_u32 v201, 0, v200\n"
"s_nop 0\n"
"v_readfirstlane_b32 s50, v201\n"
"v_cvt_pk_bf16_f32 v212, v212, v213\n"
"v_cvt_pk_bf16_f32 v213, v214, v215\n"
"v_cvt_pk_bf16_f32 v214, v216, v217\n"
"v_cvt_pk_bf16_f32 v215, v218, v219\n"
"v_mfma_f32_16x16x32_bf16 v[216:219], a[148:151], v[32:35], 0\n"
"s_waitcnt lgkmcnt(0)\n"
"s_mov_b64 exec, 1\n"
"buffer_store_dword v200, off, s[28:31], s41 sc1\n"
"s_mov_b64 exec, -1\n"
"v_smfmac_f32_16x16x64_bf16 v[252:255], v[90:93], v[112:119], v66\n"
"s_add_u32 s41, s40, 131072\n"
"ds_write_b128 v186, v[212:215] offset:60768\n"
"v_smfmac_f32_16x16x64_bf16 v[98:101], v[90:93], v[244:251], v66\n"
"s_add_u32 m0, s34, 5120\n"
"s_nop 0\n"
"buffer_load_dwordx4 v191, s[8:11], s1 offen lds\n"
"v_smfmac_f32_16x16x64_bf16 v[106:109], v[90:93], v[120:127], v66\n"
"s_add_u32 s1, s1, s33\n"
"v_cvt_pk_bf16_f32 v220, v220, v221\n"
"v_mfma_f32_16x16x32_bf16 v[90:93], v[172:175], v[90:93], v[82:85]\n"
"v_cvt_pk_bf16_f32 v252, v252, v253\n"
"v_cvt_pk_bf16_f32 v253, v254, v255\n"
"v_mfma_f32_16x16x32_bf16 v[212:215], a[152:155], v[32:35], 0\n"
"v_cvt_pk_bf16_f32 v98, v98, v99\n"
"v_cvt_pk_bf16_f32 v99, v100, v101\n"
"v_mfma_f32_16x16x32_bf16 v[216:219], a[160:163], v[36:39], v[216:219]\n"
"v_cvt_pk_bf16_f32 v106, v106, v107\n"
"v_cvt_pk_bf16_f32 v107, v108, v109\n"
"v_mfma_f32_16x16x32_bf16 v[212:215], a[164:167], v[36:39], v[212:215]\n"
"v_rcp_f32 v171, v90\n"
"v_mfma_f32_16x16x32_bf16 v[216:219], a[172:175], v[40:43], v[216:219]\n"
"v_cvt_pk_bf16_f32 v171, v171, v171\n"
"v_and_b32 v64, v171, v176\n"
"v_mfma_f32_16x16x32_bf16 v[212:215], a[176:179], v[40:43], v[212:215]\n"
"v_and_b32 v65, v171, v177\n"
"v_cvt_pk_bf16_f32 v221, v222, v223\n"
"v_mfma_f32_16x16x32_bf16 v[216:219], a[184:187], v[44:47], v[216:219]\n"
"v_cvt_pk_bf16_f32 v162, v224, v225\n"
"v_cvt_pk_bf16_f32 v163, v226, v227\n"
"v_mfma_f32_16x16x16_bf16 v[252:255], v[252:253], v[64:65], 0\n"
"v_cvt_pk_bf16_f32 v152, v228, v229\n"
"v_cvt_pk_bf16_f32 v153, v230, v231\n"
"v_mfma_f32_16x16x16_bf16 v[98:101], v[98:99], v[64:65], 0\n"
"v_cvt_pk_bf16_f32 v154, v204, v205\n"
"v_cvt_pk_bf16_f32 v155, v206, v207\n"
"v_mfma_f32_16x16x16_bf16 v[106:109], v[106:107], v[64:65], 0\n"
"ds_read_b128 v[136:139], v178 offset:13504\n"
"ds_read_b128 v[128:131], v179 offset:13504\n"
"v_mfma_f32_16x16x32_bf16 v[224:227], a[72:75], v[32:35], 0\n"
"v_cvt_pk_bf16_f32 v252, v252, v253\n"
"v_cvt_pk_bf16_f32 v253, v254, v255\n"
"v_mfma_f32_16x16x32_bf16 v[228:231], a[76:79], v[32:35], 0\n"
"v_cvt_pk_bf16_f32 v254, v98, v99\n"
"v_cvt_pk_bf16_f32 v255, v100, v101\n"
"v_mfma_f32_16x16x32_bf16 v[204:207], a[80:83], v[32:35], 0\n"
"buffer_store_dwordx4 v[252:255], v192, s[28:31], s37 offen sc1\n"
"v_mfma_f32_16x16x32_bf16 v[224:227], a[84:87], v[36:39], v[224:227]\n"
"v_cvt_pk_bf16_f32 v106, v106, v107\n"
"v_cvt_pk_bf16_f32 v107, v108, v109\n"
"v_mfma_f32_16x16x32_bf16 v[228:231], a[88:91], v[36:39], v[228:231]\n"
"ds_read_b128 v[140:143], v178 offset:27008\n"
"buffer_store_dwordx2 v[106:107], v193, s[28:31], s37 offen offset:1024 sc1\n"
"v_mfma_f32_16x16x32_bf16 v[204:207], a[92:95], v[36:39], v[204:207]\n"
"s_waitcnt lgkmcnt(2)\n"
"ds_read_b128 v[132:135], v179 offset:27008\n"
"ds_read_b64 v[80:81], v20 offset:272\n"
"v_mfma_f32_16x16x32_bf16 v[224:227], a[96:99], v[40:43], v[224:227]\n"
"s_waitcnt lgkmcnt(3)\n"
"ds_read_b64 v[86:87], v20 offset:336\n"
"ds_read_b64 v[88:89], v20 offset:544\n"
"v_mfma_f32_16x16x32_bf16 v[228:231], a[100:103], v[40:43], v[228:231]\n"
"s_add_u32 s37, s37, 6144\n"
"ds_read_b64 v[94:95], v20 offset:608\n"
"ds_read_b64 v[82:83], v178 offset:13568\n"
"v_mfma_f32_16x16x32_bf16 v[204:207], a[104:107], v[40:43], v[204:207]\n"
"s_add_u32 s4, s37, 61440\n"
"ds_read_b64 v[84:85], v179 offset:13568\n"
"v_cvt_pk_bf16_f32 v166, v208, v209\n"
"v_mfma_f32_16x16x32_bf16 v[224:227], a[108:111], v[44:47], v[224:227]\n"
"ds_read_b64 v[90:91], v178 offset:27072\n"
"v_cvt_pk_bf16_f32 v167, v210, v211\n"
"v_mfma_f32_16x16x32_bf16 v[228:231], a[112:115], v[44:47], v[228:231]\n"
"ds_read_b64 v[92:93], v179 offset:27072\n"
"v_subrev_u32 v182, s48, v182\n"
"v_mfma_f32_16x16x32_bf16 v[204:207], a[116:119], v[44:47], v[204:207]\n"
"s_waitcnt lgkmcnt(9)\n"
"ds_read_b128 v[112:115], v180\n"
"v_subrev_u32 v183, s48, v183\n"
"v_mfma_f32_16x16x32_bf16 v[224:227], a[120:123], v[48:51], v[224:227]\n"
"ds_read_b128 v[244:247], v181\n"
"v_mov_b64 v[252:253], 0\n"
"v_mfma_f32_16x16x32_bf16 v[228:231], a[124:127], v[48:51], v[228:231]\n"
"s_waitcnt lgkmcnt(9)\n"
"ds_read_b64 v[96:97], v20 offset:816\n"
"ds_read_b64 v[102:103], v20 offset:880\n"
"v_mfma_f32_16x16x32_bf16 v[204:207], a[128:131], v[48:51], v[204:207]\n"
"s_waitcnt lgkmcnt(9)\n"
"ds_read_b64 v[104:105], v20 offset:1088\n"
"ds_read_b64 v[110:111], v20 offset:1152\n"
"v_mfma_f32_16x16x32_bf16 v[224:227], a[132:135], v[52:55], v[224:227]\n"
"s_waitcnt lgkmcnt(10)\n"
"ds_read_b64 v[98:99], v180 offset:64\n"
"v_bfi_b32 v80, v4, v80, v26\n"
"v_mfma_f32_16x16x32_bf16 v[228:231], a[136:139], v[52:55], v[228:231]\n"
"ds_read_b64 v[100:101], v181 offset:64\n"
"v_bfi_b32 v81, v5, v81, v26\n"
"v_mfma_f32_16x16x32_bf16 v[204:207], a[140:143], v[52:55], v[204:207]\n"
"ds_read_b64_tr_b16 v[120:121], v182 offset:13632\n"
"v_cvt_pk_bf16_f32 v224, v224, v225\n"
"v_mfma_f32_16x16x32_bf16 v[208:211], a[144:147], v[32:35], 0\n"
"ds_read_b128 v[32:35], v188 offset:6144\n"
"v_cvt_pk_bf16_f32 v225, v226, v227\n"
"v_mfma_f32_16x16x32_bf16 v[208:211], a[156:159], v[36:39], v[208:211]\n"
"ds_read_b128 v[36:39], v189 offset:6144\n"
"v_cvt_pk_bf16_f32 v226, v228, v229\n"
"v_mfma_f32_16x16x32_bf16 v[136:139], v[136:139], v[148:151], 0\n"
"s_waitcnt lgkmcnt(10)\n"
"ds_read_b64_tr_b16 v[122:123], v183 offset:13632\n"
"v_cvt_pk_bf16_f32 v227, v230, v231\n"
"v_mfma_f32_16x16x32_bf16 v[128:131], v[128:131], v[148:151], 0\n"
"s_waitcnt lgkmcnt(9)\n"
"ds_write_b128 v186, v[224:227] offset:60896\n"
"v_mfma_f32_16x16x32_bf16 v[140:143], v[140:143], v[148:151], 0\n"
"s_waitcnt lgkmcnt(7)\n"
"v_cvt_pk_bf16_f32 v222, v204, v205\n"
"v_cvt_pk_bf16_f32 v223, v206, v207\n"
"v_mfma_f32_16x16x32_bf16 v[132:135], v[132:135], v[148:151], 0\n"
"v_bfi_b32 v86, v6, v86, v26\n"
"v_bfi_b32 v87, v7, v87, v26\n"
"v_mfma_f32_16x16x32_bf16 v[208:211], a[168:171], v[40:43], v[208:211]\n"
"ds_read_b128 v[40:43], v188 offset:7168\n"
"ds_write_b128 v186, v[220:223] offset:60832\n"
"v_mfma_f32_16x16x32_bf16 v[136:139], v[80:83], v[160:163], v[136:139]\n"
"s_waitcnt lgkmcnt(5)\n"
"v_bfi_b32 v88, v0, v88, v26\n"
"v_bfi_b32 v89, v1, v89, v26\n"
"v_mfma_f32_16x16x32_bf16 v[128:131], v[84:87], v[162:165], v[128:131]\n"
"s_waitcnt lgkmcnt(4)\n"
"v_bfi_b32 v94, v2, v94, v26\n"
"v_bfi_b32 v95, v3, v95, v26\n"
"v_mfma_f32_16x16x32_bf16 v[140:143], v[88:91], v[160:163], v[140:143]\n"
"s_add_u32 m0, s34, 6144\n"
"ds_read_b64_tr_b16 v[124:125], v182 offset:14400\n"
"buffer_load_dwordx4 v190, s[8:11], s32 offen lds\n"
"v_mfma_f32_16x16x32_bf16 v[132:135], v[92:95], v[162:165], v[132:135]\n"
"ds_read_b64_tr_b16 v[126:127], v183 offset:14400\n"
"v_bfi_b32 v96, v0, v96, v26\n"
"v_mfma_f32_16x16x32_bf16 v[112:115], v[112:115], v[148:151], 0\n"
"v_bfi_b32 v97, v1, v97, v26\n"
"v_bfi_b32 v102, v2, v102, v26\n"
"v_mfma_f32_16x16x32_bf16 v[244:247], v[244:247], v[148:151], 0\n"
"v_bfi_b32 v103, v3, v103, v26\n"
"v_bfi_b32 v104, v8, v104, v26\n"
"v_mfma_f32_16x16x32_bf16 v[112:115], v[96:99], v[160:163], v[112:115]\n"
"v_bfi_b32 v105, v9, v105, v26\n"
"v_bfi_b32 v110, v10, v110, v26\n"
"v_mfma_f32_16x16x32_bf16 v[244:247], v[100:103], v[162:165], v[244:247]\n"
"v_bfi_b32 v111, v11, v111, v26\n"
"v_max_f32 v136, v136, v128\n"
"v_mfma_f32_16x16x32_bf16 v[208:211], a[180:183], v[44:47], v[208:211]\n"
"s_waitcnt lgkmcnt(2)\n"
"v_max_f32 v137, v137, v129\n"
"v_max_f32 v138, v138, v130\n"
"v_mfma_f32_16x16x32_bf16 v[212:215], a[188:191], v[44:47], v[212:215]\n"
"s_barrier\n"
"v_mfma_f32_16x16x32_bf16 v[208:211], a[192:195], v[48:51], v[208:211]\n"
"ds_read_b128 v[44:47], v189 offset:7168\n"
"ds_read_b128 v[116:119], v180 offset:13504\n"
"v_mfma_f32_16x16x32_bf16 v[216:219], a[196:199], v[48:51], v[216:219]\n"
"ds_read_b128 v[248:251], v181 offset:13504\n"
"v_max_f32 v139, v139, v131\n"
"v_mfma_f32_16x16x32_bf16 v[212:215], a[200:203], v[48:51], v[212:215]\n"
"s_add_u32 m0, s79, 130176\n"
"ds_read_b128 v[48:51], v188 offset:8192\n"
"buffer_load_dwordx4 v192, s[16:19], s43 offen lds\n"
"v_mfma_f32_16x16x32_bf16 v[208:211], a[204:207], v[52:55], v[208:211]\n"
"ds_read_b64 v[106:107], v180 offset:13568\n"
"v_max_f32 v140, v140, v132\n"
"v_mfma_f32_16x16x32_bf16 v[216:219], a[208:211], v[52:55], v[216:219]\n"
"ds_read_b64 v[108:109], v181 offset:13568\n"
"v_max_f32 v141, v141, v133\n"
"v_mfma_f32_16x16x32_bf16 v[212:215], a[212:215], v[52:55], v[212:215]\n"
"ds_read_b128 v[52:55], v189 offset:8192\n"
"v_max_f32 v142, v142, v134\n"
"v_mfma_f32_16x16x32_bf16 v[228:231], a[0:3], v[32:35], 0\n"
"ds_read_b64_tr_b16 v[128:129], v182 offset:13640\n"
"v_max_f32 v143, v143, v135\n"
"v_mfma_f32_16x16x32_bf16 v[204:207], a[4:7], v[32:35], 0\n"
"ds_read_b64_tr_b16 v[130:131], v183 offset:13640\n"
"v_max_f32 v170, v136, v137\n"
"v_mfma_f32_16x16x32_bf16 v[220:223], a[8:11], v[32:35], 0\n"
"ds_read_b64_tr_b16 v[132:133], v182 offset:14408\n"
"v_max_f32 v171, v138, v139\n"
"v_mfma_f32_16x16x32_bf16 v[228:231], a[12:15], v[36:39], v[228:231]\n"
"s_waitcnt lgkmcnt(8)\n"
"ds_read_b64_tr_b16 v[134:135], v183 offset:14408\n"
"v_max3_f32 v170, v170, v140, v141\n"
"v_mfma_f32_16x16x32_bf16 v[116:119], v[116:119], v[148:151], 0\n"
"s_waitcnt lgkmcnt(8)\n"
"v_max3_f32 v171, v171, v142, v143\n"
"v_max_f32 v112, v112, v244\n"
"v_mfma_f32_16x16x32_bf16 v[248:251], v[248:251], v[148:151], 0\n"
"v_max_f32 v113, v113, v245\n"
"v_max_f32 v114, v114, v246\n"
"v_mfma_f32_16x16x32_bf16 v[204:207], a[16:19], v[36:39], v[204:207]\n"
"s_waitcnt lgkmcnt(6)\n"
"ds_read_b64_tr_b16 v[244:245], v182 offset:13576\n"
"v_max_f32 v115, v115, v247\n"
"v_mfma_f32_16x16x32_bf16 v[116:119], v[104:107], v[160:163], v[116:119]\n"
"s_waitcnt lgkmcnt(6)\n"
"ds_read_b64_tr_b16 v[246:247], v183 offset:13576\n"
"v_max3_f32 v170, v170, v112, v113\n"
"v_mfma_f32_16x16x32_bf16 v[248:251], v[108:111], v[162:165], v[248:251]\n"
"s_waitcnt lgkmcnt(6)\n"
"v_max3_f32 v171, v171, v114, v115\n"
"v_mov_b64 v[254:255], 0\n"
"v_mfma_f32_16x16x32_bf16 v[220:223], a[20:23], v[36:39], v[220:223]\n"
"s_add_u32 m0, s79, 130176\n"
"buffer_store_dwordx4 v[68:71], v192, s[28:31], s4 offen sc1\n"
"v_mfma_f32_16x16x32_bf16 v[228:231], a[24:27], v[40:43], v[228:231]\n"
"v_mov_b64 v[98:99], 0\n"
"v_max_f32 v116, v116, v248\n"
"v_mfma_f32_16x16x32_bf16 v[204:207], a[28:31], v[40:43], v[204:207]\n"
"v_max_f32 v117, v117, v249\n"
"v_max_f32 v118, v118, v250\n"
"v_mfma_f32_16x16x32_bf16 v[220:223], a[32:35], v[40:43], v[220:223]\n"
"buffer_load_dwordx4 v192, s[16:19], s43 offen offset:1024 lds\n"
"ds_read_b64_tr_b16 v[248:249], v182 offset:14344\n"
"v_mfma_f32_16x16x32_bf16 v[228:231], a[36:39], v[44:47], v[228:231]\n"
"v_max_f32 v119, v119, v251\n"
"v_max3_f32 v170, v170, v116, v117\n"
"v_mfma_f32_16x16x32_bf16 v[204:207], a[40:43], v[44:47], v[204:207]\n"
"ds_read_b64_tr_b16 v[250:251], v183 offset:14344\n"
"v_max3_f32 v171, v171, v118, v119\n"
"v_mfma_f32_16x16x32_bf16 v[220:223], a[44:47], v[44:47], v[220:223]\n"
"v_max_f32 v170, v170, v171\n"
"v_mov_b32 v171, v170\n"
"v_mfma_f32_16x16x32_bf16 v[228:231], a[48:51], v[48:51], v[228:231]\n"
"s_nop 0\n"
"v_permlane16_swap_b32 v170, v171\n"
"v_mfma_f32_16x16x32_bf16 v[204:207], a[52:55], v[48:51], v[204:207]\n"
"v_max_f32 v170, v170, v171\n"
"v_mov_b32 v171, v170\n"
"v_mfma_f32_16x16x32_bf16 v[220:223], a[56:59], v[48:51], v[220:223]\n"
"s_nop 0\n"
"v_permlane32_swap_b32 v170, v171\n"
"v_mfma_f32_16x16x32_bf16 v[228:231], a[60:63], v[52:55], v[228:231]\n"
"v_max_f32 v170, v170, v171\n"
"v_mul_f32 v170, 0xbe553b95, v170\n"
"v_mfma_f32_16x16x32_bf16 v[204:207], a[64:67], v[52:55], v[204:207]\n"
"v_fmamk_f32 v136, v136, 0x3e553b95, v170\n"
"v_fmamk_f32 v137, v137, 0x3e553b95, v170\n"
"v_mfma_f32_16x16x32_bf16 v[220:223], a[68:71], v[52:55], v[220:223]\n"
"v_fmamk_f32 v138, v138, 0x3e553b95, v170\n"
"v_fmamk_f32 v139, v139, 0x3e553b95, v170\n"
"v_mfma_f32_16x16x32_bf16 v[224:227], a[72:75], v[32:35], 0\n"
"s_add_u32 m0, s79, 130176\n"
"v_fmamk_f32 v140, v140, 0x3e553b95, v170\n"
"v_fmamk_f32 v141, v141, 0x3e553b95, v170\n"
"v_mfma_f32_16x16x32_bf16 v[224:227], a[84:87], v[36:39], v[224:227]\n"
"buffer_load_dwordx4 v192, s[16:19], s43 offen offset:2048 lds\n"
"v_fmamk_f32 v142, v142, 0x3e553b95, v170\n"
"v_mfma_f32_16x16x32_bf16 v[224:227], a[96:99], v[40:43], v[224:227]\n"
"s_add_u32 s43, s79, 18432\n"
"v_fmamk_f32 v143, v143, 0x3e553b95, v170\n"
"v_mov_b64 v[100:101], 0\n"
"v_mfma_f32_16x16x32_bf16 v[224:227], a[108:111], v[44:47], v[224:227]\n"
"v_exp_f32 v136, v136\n"
"v_mfma_f32_16x16x32_bf16 v[224:227], a[120:123], v[48:51], v[224:227]\n"
"v_exp_f32 v137, v137\n"
"v_mfma_f32_16x16x32_bf16 v[224:227], a[132:135], v[52:55], v[224:227]\n"
"v_exp_f32 v138, v138\n"
"v_exp_f32 v139, v139\n"
"v_exp_f32 v140, v140\n"
"v_exp_f32 v141, v141\n"
"v_exp_f32 v142, v142\n"
"v_exp_f32 v143, v143\n"
"v_cvt_pk_bf16_f32 v82, v136, v138\n"
"v_cvt_pk_bf16_f32 v83, v137, v139\n"
"v_cvt_pk_bf16_f32 v84, v140, v142\n"
"v_cvt_pk_bf16_f32 v85, v141, v143\n"
"v_mov_b64 v[106:107], 0\n"
"v_mov_b64 v[108:109], 0\n"
"v_smfmac_f32_16x16x64_bf16 v[252:255], v[82:85], v[120:127], v66\n"
"s_waitcnt lgkmcnt(0)\n"
"ds_read_b64_tr_b16 v[136:137], v182 offset:40640\n"
"v_smfmac_f32_16x16x64_bf16 v[98:101], v[82:85], v[128:135], v66\n"
"s_add_u32 m0, s34, 9216\n"
"s_nop 0\n"
"buffer_load_dwordx4 v191, s[8:11], s32 offen lds\n"
"v_smfmac_f32_16x16x64_bf16 v[106:109], v[82:85], v[244:251], v66\n"
"s_add_u32 s32, s32, s33\n"
"ds_read_b64_tr_b16 v[138:139], v183 offset:40640\n"
"v_mfma_f32_16x16x32_bf16 v[82:85], v[172:175], v[82:85], 0\n"
"ds_read_b64_tr_b16 v[140:141], v182 offset:41408\n"
"ds_read_b64_tr_b16 v[142:143], v183 offset:41408\n"
"v_fmamk_f32 v112, v112, 0x3e553b95, v170\n"
"ds_read_b64_tr_b16 v[120:121], v182 offset:40648\n"
"v_fmamk_f32 v113, v113, 0x3e553b95, v170\n"
"ds_read_b64_tr_b16 v[122:123], v183 offset:40648\n"
"v_fmamk_f32 v114, v114, 0x3e553b95, v170\n"
"ds_read_b64_tr_b16 v[124:125], v182 offset:41416\n"
"v_fmamk_f32 v115, v115, 0x3e553b95, v170\n"
"ds_read_b64_tr_b16 v[126:127], v183 offset:41416\n"
"v_fmamk_f32 v116, v116, 0x3e553b95, v170\n"
"ds_read_b64_tr_b16 v[128:129], v182 offset:40584\n"
"v_fmamk_f32 v117, v117, 0x3e553b95, v170\n"
"ds_read_b64_tr_b16 v[130:131], v183 offset:40584\n"
"v_fmamk_f32 v118, v118, 0x3e553b95, v170\n"
"ds_read_b64_tr_b16 v[132:133], v182 offset:41352\n"
"v_fmamk_f32 v119, v119, 0x3e553b95, v170\n"
"s_add_u32 m0, s34, 7168\n"
"ds_read_b64_tr_b16 v[134:135], v183 offset:41352\n"
"buffer_load_dwordx4 v190, s[8:11], s0 offen lds\n"
"v_exp_f32 v112, v112\n"
"v_exp_f32 v113, v113\n"
"v_exp_f32 v114, v114\n"
"v_exp_f32 v115, v115\n"
"v_exp_f32 v116, v116\n"
"v_exp_f32 v117, v117\n"
"v_exp_f32 v118, v118\n"
"v_exp_f32 v119, v119\n"
"v_cvt_pk_bf16_f32 v144, v208, v209\n"
"v_cvt_pk_bf16_f32 v145, v210, v211\n"
"v_mfma_f32_16x16x32_bf16 v[208:211], a[76:79], v[32:35], 0\n"
"v_cvt_pk_bf16_f32 v90, v112, v114\n"
"v_cvt_pk_bf16_f32 v91, v113, v115\n"
"v_mfma_f32_16x16x32_bf16 v[208:211], a[88:91], v[36:39], v[208:211]\n"
"v_cvt_pk_bf16_f32 v92, v116, v118\n"
"v_cvt_pk_bf16_f32 v93, v117, v119\n"
"v_mfma_f32_16x16x32_bf16 v[208:211], a[100:103], v[40:43], v[208:211]\n"
"s_waitcnt lgkmcnt(4)\n"
"v_cvt_pk_bf16_f32 v146, v216, v217\n"
"v_cvt_pk_bf16_f32 v147, v218, v219\n"
"v_smfmac_f32_16x16x64_bf16 v[252:255], v[90:93], v[136:143], v66\n"
"s_waitcnt lgkmcnt(0)\n"
"v_cvt_pk_bf16_f32 v158, v212, v213\n"
"v_smfmac_f32_16x16x64_bf16 v[98:101], v[90:93], v[120:127], v66\n"
"v_cvt_pk_bf16_f32 v159, v214, v215\n"
"v_smfmac_f32_16x16x64_bf16 v[106:109], v[90:93], v[128:135], v66\n"
"s_add_u32 m0, s34, 10240\n"
"s_nop 0\n"
"buffer_load_dwordx4 v191, s[8:11], s0 offen lds\n"
"v_mfma_f32_16x16x32_bf16 v[90:93], v[172:175], v[90:93], v[82:85]\n"
"s_add_u32 s0, s0, s33\n"
"v_cvt_pk_bf16_f32 v252, v252, v253\n"
"v_cvt_pk_bf16_f32 v253, v254, v255\n"
"v_mfma_f32_16x16x32_bf16 v[216:219], a[80:83], v[32:35], 0\n"
"v_cvt_pk_bf16_f32 v98, v98, v99\n"
"v_cvt_pk_bf16_f32 v99, v100, v101\n"
"v_mfma_f32_16x16x32_bf16 v[216:219], a[92:95], v[36:39], v[216:219]\n"
"v_cvt_pk_bf16_f32 v106, v106, v107\n"
"v_cvt_pk_bf16_f32 v107, v108, v109\n"
"v_mfma_f32_16x16x32_bf16 v[216:219], a[104:107], v[40:43], v[216:219]\n"
"v_rcp_f32 v171, v90\n"
"v_mfma_f32_16x16x32_bf16 v[208:211], a[112:115], v[44:47], v[208:211]\n"
"v_cvt_pk_bf16_f32 v171, v171, v171\n"
"v_and_b32 v64, v171, v176\n"
"v_mfma_f32_16x16x32_bf16 v[216:219], a[116:119], v[44:47], v[216:219]\n"
"v_and_b32 v65, v171, v177\n"
"v_cvt_pk_bf16_f32 v228, v228, v229\n"
"v_mfma_f32_16x16x32_bf16 v[208:211], a[124:127], v[48:51], v[208:211]\n"
"v_cvt_pk_bf16_f32 v229, v230, v231\n"
"v_cvt_pk_bf16_f32 v230, v204, v205\n"
"v_mfma_f32_16x16x16_bf16 v[252:255], v[252:253], v[64:65], 0\n"
"v_cvt_pk_bf16_f32 v231, v206, v207\n"
"v_cvt_pk_bf16_f32 v220, v220, v221\n"
"v_mfma_f32_16x16x16_bf16 v[98:101], v[98:99], v[64:65], 0\n"
"ds_write_b128 v187, v[228:231]\n"
"v_mfma_f32_16x16x16_bf16 v[106:109], v[106:107], v[64:65], 0\n"
"s_add_u32 m0, s34, 8192\n"
"v_cvt_pk_bf16_f32 v221, v222, v223\n"
"v_cvt_pk_bf16_f32 v224, v224, v225\n"
"v_mfma_f32_16x16x32_bf16 v[216:219], a[128:131], v[48:51], v[216:219]\n"
".Lic1%=:\n"
"s_cbranch_execz .Lib0%=\n"
"buffer_load_dwordx4 v190, s[8:11], s1 offen lds\n"
"v_cvt_pk_bf16_f32 v252, v252, v253\n"
"v_mfma_f32_16x16x32_bf16 v[208:211], a[136:139], v[52:55], v[208:211]\n"
"v_cvt_pk_bf16_f32 v253, v254, v255\n"
"v_cvt_pk_bf16_f32 v254, v98, v99\n"
"v_mfma_f32_16x16x32_bf16 v[216:219], a[140:143], v[52:55], v[216:219]\n"
"v_cvt_pk_bf16_f32 v255, v100, v101\n"
"v_cvt_pk_bf16_f32 v106, v106, v107\n"
"v_mfma_f32_16x16x32_bf16 v[212:215], a[144:147], v[32:35], 0\n"
"buffer_store_dwordx4 v[252:255], v192, s[28:31], s37 offen sc1\n"
"v_mfma_f32_16x16x32_bf16 v[204:207], a[148:151], v[32:35], 0\n"
"v_cvt_pk_bf16_f32 v107, v108, v109\n"
"v_cvt_pk_bf16_f32 v225, v226, v227\n"
"v_mfma_f32_16x16x32_bf16 v[228:231], a[152:155], v[32:35], 0\n"
"s_waitcnt vmcnt(17)\n"
"ds_read_b128 v[32:35], v188\n"
"v_cvt_pk_bf16_f32 v226, v208, v209\n"
"v_mfma_f32_16x16x32_bf16 v[212:215], a[156:159], v[36:39], v[212:215]\n"
"v_cvt_pk_bf16_f32 v227, v210, v211\n"
"v_cvt_pk_bf16_f32 v222, v216, v217\n"
"v_mfma_f32_16x16x32_bf16 v[204:207], a[160:163], v[36:39], v[204:207]\n"
"buffer_store_dwordx2 v[106:107], v193, s[28:31], s37 offen offset:1024 sc1\n"
"v_mfma_f32_16x16x32_bf16 v[228:231], a[164:167], v[36:39], v[228:231]\n"
"s_add_u32 s37, s37, 6144\n"
"ds_read_b128 v[36:39], v189\n"
"ds_write_b128 v187, v[224:227] offset:128\n"
"v_mfma_f32_16x16x32_bf16 v[212:215], a[168:171], v[40:43], v[212:215]\n"
"s_add_u32 m0, s34, 11264\n"
"v_cvt_pk_bf16_f32 v223, v218, v219\n"
"v_mov_b64 v[252:253], 0\n"
"v_mfma_f32_16x16x32_bf16 v[204:207], a[172:175], v[40:43], v[204:207]\n"
"s_add_u32 s4, s37, 55296\n"
"buffer_load_dwordx4 v191, s[8:11], s1 offen lds\n"
"ds_write_b128 v187, v[220:223] offset:64\n"
"v_mfma_f32_16x16x32_bf16 v[228:231], a[176:179], v[40:43], v[228:231]\n"
"s_waitcnt vmcnt(17) lgkmcnt(3)\n"
"v_mov_b64 v[254:255], 0\n"
"s_add_u32 s1, s1, s33\n"
"v_mfma_f32_16x16x32_bf16 v[212:215], a[180:183], v[44:47], v[212:215]\n"
"ds_read_b128 v[40:43], v188 offset:1024\n"
"v_mfma_f32_16x16x32_bf16 v[204:207], a[184:187], v[44:47], v[204:207]\n"
"ds_read_b128 v[244:247], v178 offset:27008\n"
"ds_read_b128 v[112:115], v179 offset:27008\n"
"v_mfma_f32_16x16x32_bf16 v[228:231], a[188:191], v[44:47], v[228:231]\n"
"ds_read_b128 v[44:47], v189 offset:1024\n"
"v_mfma_f32_16x16x32_bf16 v[212:215], a[192:195], v[48:51], v[212:215]\n"
"ds_read_b128 v[248:251], v180\n"
"v_mfma_f32_16x16x32_bf16 v[204:207], a[196:199], v[48:51], v[204:207]\n"
"ds_read_b128 v[116:119], v181\n"
"v_mfma_f32_16x16x32_bf16 v[228:231], a[200:203], v[48:51], v[228:231]\n"
"s_waitcnt vmcnt(14)\n"
"ds_read_b128 v[48:51], v188 offset:2048\n"
"v_mfma_f32_16x16x32_bf16 v[212:215], a[204:207], v[52:55], v[212:215]\n"
"ds_read_b64 v[80:81], v20 offset:272\n"
"ds_read_b64 v[86:87], v20 offset:336\n"
"v_mfma_f32_16x16x32_bf16 v[204:207], a[208:211], v[52:55], v[204:207]\n"
"s_waitcnt lgkmcnt(11)\n"
"ds_read_b64 v[82:83], v178 offset:27072\n"
"s_add_u32 m0, s34, 0\n"
"v_mfma_f32_16x16x32_bf16 v[228:231], a[212:215], v[52:55], v[228:231]\n"
"buffer_load_dwordx4 v190, s[8:11], s32 offen lds\n"
"ds_read_b128 v[52:55], v189 offset:2048\n"
"v_mfma_f32_16x16x32_bf16 v[208:211], a[0:3], v[32:35], 0\n"
"s_waitcnt lgkmcnt(9)\n"
"ds_read_b64 v[84:85], v179 offset:27072\n"
"v_cvt_pk_bf16_f32 v148, v212, v213\n"
"v_mfma_f32_16x16x32_bf16 v[216:219], a[4:7], v[32:35], 0\n"
"s_waitcnt lgkmcnt(9)\n"
"ds_read_b64 v[90:91], v180 offset:64\n"
"v_cvt_pk_bf16_f32 v149, v214, v215\n"
"v_mfma_f32_16x16x32_bf16 v[220:223], a[8:11], v[32:35], 0\n"
"ds_read_b64 v[92:93], v181 offset:64\n"
"v_cvt_pk_bf16_f32 v150, v204, v205\n"
"v_mfma_f32_16x16x32_bf16 v[208:211], a[12:15], v[36:39], v[208:211]\n"
"s_waitcnt lgkmcnt(9)\n"
"ds_read_b128 v[136:139], v180 offset:13504\n"
"v_cvt_pk_bf16_f32 v151, v206, v207\n"
"v_mfma_f32_16x16x32_bf16 v[216:219], a[16:19], v[36:39], v[216:219]\n"
"s_waitcnt lgkmcnt(9)\n"
"ds_read_b128 v[120:123], v181 offset:13504\n"
"v_cvt_pk_bf16_f32 v162, v228, v229\n"
"v_mfma_f32_16x16x32_bf16 v[220:223], a[20:23], v[36:39], v[220:223]\n"
"s_waitcnt lgkmcnt(8)\n"
"ds_read_b64 v[104:105], v20 offset:1088\n"
"ds_read_b64 v[110:111], v20 offset:1152\n"
"v_mfma_f32_16x16x32_bf16 v[208:211], a[24:27], v[40:43], v[208:211]\n"
"s_waitcnt lgkmcnt(9)\n"
"ds_read_b64 v[98:99], v180 offset:13568\n"
"v_cvt_pk_bf16_f32 v163, v230, v231\n"
"v_mfma_f32_16x16x32_bf16 v[216:219], a[28:31], v[40:43], v[216:219]\n"
"ds_read_b64 v[100:101], v181 offset:13568\n"
"v_bfi_b32 v80, v12, v80, v26\n"
"v_mfma_f32_16x16x32_bf16 v[220:223], a[32:35], v[40:43], v[220:223]\n"
"ds_read_b64_tr_b16 v[128:129], v182 offset:27136\n"
"v_bfi_b32 v81, v13, v81, v26\n"
"v_mfma_f32_16x16x32_bf16 v[208:211], a[36:39], v[44:47], v[208:211]\n"
"s_waitcnt lgkmcnt(10)\n"
"ds_read_b64_tr_b16 v[130:131], v183 offset:27136\n"
"v_bfi_b32 v86, v14, v86, v26\n"
"v_mfma_f32_16x16x32_bf16 v[216:219], a[40:43], v[44:47], v[216:219]\n"
"s_add_u32 m0, s34, 3072\n"
"ds_read_b64_tr_b16 v[132:133], v182 offset:27904\n"
"buffer_load_dwordx4 v191, s[8:11], s32 offen lds\n"
"v_mfma_f32_16x16x32_bf16 v[220:223], a[44:47], v[44:47], v[220:223]\n"
"s_add_u32 s32, s32, s33\n"
"ds_read_b64_tr_b16 v[134:135], v183 offset:27904\n"
"v_bfi_b32 v87, v15, v87, v26\n"
"v_mfma_f32_16x16x32_bf16 v[244:247], v[244:247], v[152:155], 0\n"
"s_waitcnt vmcnt(9) lgkmcnt(9)\n"
"s_mov_b64 exec, s[44:45]\n"
"v_add_u32 v179, 0xc0, v179\n"
"s_mov_b64 exec, -1\n"
"v_mfma_f32_16x16x32_bf16 v[112:115], v[112:115], v[152:155], 0\n"
"s_waitcnt lgkmcnt(7)\n"
"s_barrier\n"
"v_mfma_f32_16x16x32_bf16 v[248:251], v[248:251], v[152:155], 0\n"
"s_waitcnt lgkmcnt(6)\n"
"buffer_load_dword v201, off, s[28:31], s41 sc1\n"
"ds_read_b128 v[140:143], v180 offset:27008\n"
"v_mfma_f32_16x16x32_bf16 v[116:119], v[116:119], v[152:155], 0\n"
"s_add_u32 s41, s40, 131072\n"
"ds_read_b128 v[124:127], v181 offset:27008\n"
"ds_read_b64 v[106:107], v180 offset:27072\n"
"v_mfma_f32_16x16x32_bf16 v[208:211], a[48:51], v[48:51], v[208:211]\n"
"ds_read_b64 v[108:109], v181 offset:27072\n"
"v_bfi_b32 v104, v16, v104, v26\n"
"v_mfma_f32_16x16x32_bf16 v[244:247], v[80:83], v[164:167], v[244:247]\n"
"ds_read_b64_tr_b16 a[216:217], v236\n"
"v_bfi_b32 v105, v17, v105, v26\n"
"v_mfma_f32_16x16x32_bf16 v[112:115], v[84:87], v[166:169], v[112:115]\n"
"v_bfi_b32 v110, v18, v110, v26\n"
"v_bfi_b32 v111, v19, v111, v26\n"
"v_mfma_f32_16x16x32_bf16 v[248:251], v[88:91], v[164:167], v[248:251]\n"
"ds_read_b64_tr_b16 a[218:219], v236 offset:6144\n"
"s_add_u32 m0, s79, 142464\n"
"v_mfma_f32_16x16x32_bf16 v[116:119], v[92:95], v[166:169], v[116:119]\n"
"buffer_load_dwordx4 v192, s[16:19], s43 offen lds\n"
"v_mfma_f32_16x16x32_bf16 v[136:139], v[136:139], v[152:155], 0\n"
"s_waitcnt lgkmcnt(11)\n"
"v_max_f32 v244, v244, v112\n"
"v_max_f32 v245, v245, v113\n"
"v_mfma_f32_16x16x32_bf16 v[120:123], v[120:123], v[152:155], 0\n"
"s_waitcnt lgkmcnt(5)\n"
"ds_read_b64_tr_b16 v[112:113], v182 offset:27144\n"
"v_max_f32 v246, v246, v114\n"
"v_mfma_f32_16x16x32_bf16 v[140:143], v[140:143], v[152:155], 0\n"
"s_waitcnt lgkmcnt(5)\n"
"v_max_f32 v247, v247, v115\n"
"v_max_f32 v248, v248, v116\n"
"v_mfma_f32_16x16x32_bf16 v[124:127], v[124:127], v[152:155], 0\n"
"s_waitcnt lgkmcnt(4)\n"
"ds_read_b64_tr_b16 v[114:115], v183 offset:27144\n"
"v_max_f32 v249, v249, v117\n"
"v_mfma_f32_16x16x32_bf16 v[216:219], a[52:55], v[48:51], v[216:219]\n"
"s_waitcnt lgkmcnt(4)\n"
"ds_read_b64_tr_b16 v[116:117], v182 offset:27912\n"
"v_max_f32 v250, v250, v118\n"
"v_mfma_f32_16x16x32_bf16 v[136:139], v[96:99], v[164:167], v[136:139]\n"
"v_max_f32 v251, v251, v119\n"
"v_max_f32 v170, v244, v245\n"
"v_mfma_f32_16x16x32_bf16 v[120:123], v[100:103], v[166:169], v[120:123]\n"
"ds_read_b64_tr_b16 v[118:119], v183 offset:27912\n"
"v_max_f32 v171, v246, v247\n"
"v_mfma_f32_16x16x32_bf16 v[140:143], v[104:107], v[164:167], v[140:143]\n"
"v_max3_f32 v170, v170, v248, v249\n"
"v_max3_f32 v171, v171, v250, v251\n"
"v_mfma_f32_16x16x32_bf16 v[124:127], v[108:111], v[166:169], v[124:127]\n"
"v_mov_b64 v[98:99], 0\n"
"v_mov_b64 v[100:101], 0\n"
"v_mfma_f32_16x16x32_bf16 v[220:223], a[56:59], v[48:51], v[220:223]\n"
"s_add_u32 m0, s79, 142464\n"
"v_max_f32 v136, v136, v120\n"
"v_max_f32 v137, v137, v121\n"
"v_mfma_f32_16x16x32_bf16 v[208:211], a[60:63], v[52:55], v[208:211]\n"
"buffer_load_dwordx4 v192, s[16:19], s43 offen offset:1024 lds\n"
"ds_read_b64_tr_b16 v[120:121], v182 offset:27080\n"
"v_mfma_f32_16x16x32_bf16 v[216:219], a[64:67], v[52:55], v[216:219]\n"
"v_max_f32 v138, v138, v122\n"
"v_max_f32 v139, v139, v123\n"
"v_mfma_f32_16x16x32_bf16 v[220:223], a[68:71], v[52:55], v[220:223]\n"
"ds_read_b64_tr_b16 v[122:123], v183 offset:27080\n"
"v_max3_f32 v170, v170, v136, v137\n"
"v_mfma_f32_16x16x32_bf16 v[224:227], a[72:75], v[32:35], 0\n"
"buffer_store_dwordx2 v[68:69], v193, s[28:31], s4 offen offset:1024 sc1\n"
"v_mfma_f32_16x16x32_bf16 v[212:215], a[76:79], v[32:35], 0\n"
"v_max3_f32 v171, v171, v138, v139\n"
"v_max_f32 v140, v140, v124\n"
"v_mfma_f32_16x16x32_bf16 v[204:207], a[80:83], v[32:35], 0\n"
"v_max_f32 v141, v141, v125\n"
"v_max_f32 v142, v142, v126\n"
"v_mfma_f32_16x16x32_bf16 v[224:227], a[84:87], v[36:39], v[224:227]\n"
"ds_read_b64_tr_b16 v[124:125], v182 offset:27848\n"
"v_max_f32 v143, v143, v127\n"
"v_mfma_f32_16x16x32_bf16 v[212:215], a[88:91], v[36:39], v[212:215]\n"
"ds_read_b64_tr_b16 v[126:127], v183 offset:27848\n"
"v_max3_f32 v170, v170, v140, v141\n"
"v_mfma_f32_16x16x32_bf16 v[204:207], a[92:95], v[36:39], v[204:207]\n"
"v_max3_f32 v171, v171, v142, v143\n"
"v_max_f32 v170, v170, v171\n"
"v_mfma_f32_16x16x32_bf16 v[224:227], a[96:99], v[40:43], v[224:227]\n"
"v_mov_b32 v171, v170\n"
"v_cvt_pk_bf16_f32 v208, v208, v209\n"
"v_mfma_f32_16x16x32_bf16 v[212:215], a[100:103], v[40:43], v[212:215]\n"
"s_add_u32 m0, s79, 142464\n"
"v_permlane16_swap_b32 v170, v171\n"
"v_mfma_f32_16x16x32_bf16 v[204:207], a[104:107], v[40:43], v[204:207]\n"
"buffer_load_dwordx4 v192, s[16:19], s43 offen offset:2048 lds\n"
"v_max_f32 v170, v170, v171\n"
"v_mfma_f32_16x16x32_bf16 v[224:227], a[108:111], v[44:47], v[224:227]\n"
"s_add_u32 s43, s79, 36864\n"
"v_mov_b32 v171, v170\n"
"v_cvt_pk_bf16_f32 v209, v210, v211\n"
"v_mfma_f32_16x16x32_bf16 v[212:215], a[112:115], v[44:47], v[212:215]\n"
"v_permlane32_swap_b32 v170, v171\n"
"v_mfma_f32_16x16x32_bf16 v[204:207], a[116:119], v[44:47], v[204:207]\n"
"v_max_f32 v170, v170, v171\n"
"v_mul_f32 v170, 0xbe553b95, v170\n"
"v_mfma_f32_16x16x32_bf16 v[224:227], a[120:123], v[48:51], v[224:227]\n"
"v_fmamk_f32 v244, v244, 0x3e553b95, v170\n"
"v_fmamk_f32 v245, v245, 0x3e553b95, v170\n"
"v_mfma_f32_16x16x32_bf16 v[212:215], a[124:127], v[48:51], v[212:215]\n"
"v_fmamk_f32 v246, v246, 0x3e553b95, v170\n"
"v_fmamk_f32 v247, v247, 0x3e553b95, v170\n"
"v_mfma_f32_16x16x32_bf16 v[204:207], a[128:131], v[48:51], v[204:207]\n"
"v_fmamk_f32 v248, v248, 0x3e553b95, v170\n"
"v_fmamk_f32 v249, v249, 0x3e553b95, v170\n"
"v_mfma_f32_16x16x32_bf16 v[224:227], a[132:135], v[52:55], v[224:227]\n"
"v_fmamk_f32 v250, v250, 0x3e553b95, v170\n"
"v_fmamk_f32 v251, v251, 0x3e553b95, v170\n"
"v_mfma_f32_16x16x32_bf16 v[212:215], a[136:139], v[52:55], v[212:215]\n"
"v_exp_f32 v244, v244\n"
"v_mfma_f32_16x16x32_bf16 v[204:207], a[140:143], v[52:55], v[204:207]\n"
"v_exp_f32 v245, v245\n"
"v_mfma_f32_16x16x32_bf16 v[228:231], a[144:147], v[32:35], 0\n"
"s_add_u32 m0, s34, 1024\n"
"v_exp_f32 v246, v246\n"
"v_mfma_f32_16x16x32_bf16 v[228:231], a[156:159], v[36:39], v[228:231]\n"
"buffer_load_dwordx4 v190, s[8:11], s0 offen lds\n"
"v_cvt_pk_bf16_f32 v210, v216, v217\n"
"v_mfma_f32_16x16x32_bf16 v[228:231], a[168:171], v[40:43], v[228:231]\n"
"v_exp_f32 v247, v247\n"
"v_mfma_f32_16x16x32_bf16 v[228:231], a[180:183], v[44:47], v[228:231]\n"
"v_exp_f32 v248, v248\n"
"v_mfma_f32_16x16x32_bf16 v[228:231], a[192:195], v[48:51], v[228:231]\n"
"v_exp_f32 v249, v249\n"
"v_mfma_f32_16x16x32_bf16 v[228:231], a[204:207], v[52:55], v[228:231]\n"
"v_exp_f32 v250, v250\n"
"v_exp_f32 v251, v251\n"
"v_cvt_pk_bf16_f32 v82, v244, v246\n"
"v_cvt_pk_bf16_f32 v83, v245, v247\n"
"v_cvt_pk_bf16_f32 v84, v248, v250\n"
"v_cvt_pk_bf16_f32 v85, v249, v251\n"
"v_cvt_pk_bf16_f32 v211, v218, v219\n"
"v_mfma_f32_16x16x32_bf16 v[216:219], a[148:151], v[32:35], 0\n"
"s_waitcnt lgkmcnt(4)\n"
"ds_read_b64_tr_b16 v[244:245], v182 offset:54144\n"
"ds_read_b64_tr_b16 v[246:247], v183 offset:54144\n"
"v_smfmac_f32_16x16x64_bf16 v[252:255], v[82:85], v[128:135], v66\n"
"ds_read_b64_tr_b16 v[248:249], v182 offset:54912\n"
"v_smfmac_f32_16x16x64_bf16 v[98:101], v[82:85], v[112:119], v66\n"
"ds_read_b64_tr_b16 v[250:251], v183 offset:54912\n"
"v_mfma_f32_16x16x32_bf16 v[216:219], a[160:163], v[36:39], v[216:219]\n"
"s_add_u32 m0, s34, 4096\n"
"ds_read_b64_tr_b16 v[128:129], v182 offset:54152\n"
"buffer_load_dwordx4 v191, s[8:11], s0 offen lds\n"
"v_mfma_f32_16x16x32_bf16 v[216:219], a[172:175], v[40:43], v[216:219]\n"
"s_add_u32 s0, s0, s33\n"
"ds_read_b64_tr_b16 v[130:131], v183 offset:54152\n"
"v_cvt_pk_bf16_f32 v220, v220, v221\n"
"v_mfma_f32_16x16x32_bf16 v[216:219], a[184:187], v[44:47], v[216:219]\n"
"ds_read_b64_tr_b16 v[132:133], v182 offset:54920\n"
"v_cvt_pk_bf16_f32 v221, v222, v223\n"
"v_mfma_f32_16x16x32_bf16 v[216:219], a[196:199], v[48:51], v[216:219]\n"
"ds_read_b64_tr_b16 v[134:135], v183 offset:54920\n"
"v_mov_b64 v[106:107], 0\n"
"v_mfma_f32_16x16x32_bf16 v[216:219], a[208:211], v[52:55], v[216:219]\n"
"ds_read_b64_tr_b16 v[112:113], v182 offset:54088\n"
"v_mov_b64 v[108:109], 0\n"
"v_fmamk_f32 v136, v136, 0x3e553b95, v170\n"
"v_fmamk_f32 v137, v137, 0x3e553b95, v170\n"
"v_fmamk_f32 v138, v138, 0x3e553b95, v170\n"
"v_fmamk_f32 v139, v139, 0x3e553b95, v170\n"
"v_fmamk_f32 v140, v140, 0x3e553b95, v170\n"
"v_fmamk_f32 v141, v141, 0x3e553b95, v170\n"
"v_fmamk_f32 v142, v142, 0x3e553b95, v170\n"
"v_fmamk_f32 v143, v143, 0x3e553b95, v170\n"
"v_exp_f32 v136, v136\n"
"v_exp_f32 v137, v137\n"
"v_exp_f32 v138, v138\n"
"v_exp_f32 v139, v139\n"
"v_exp_f32 v140, v140\n"
"v_exp_f32 v141, v141\n"
"v_exp_f32 v142, v142\n"
"v_exp_f32 v143, v143\n"
"v_cvt_pk_bf16_f32 v90, v136, v138\n"
"v_cvt_pk_bf16_f32 v91, v137, v139\n"
"v_cvt_pk_bf16_f32 v92, v140, v142\n"
"v_cvt_pk_bf16_f32 v93, v141, v143\n"
"v_cvt_pk_bf16_f32 v224, v224, v225\n"
"v_cvt_pk_bf16_f32 v225, v226, v227\n"
"v_cvt_pk_bf16_f32 v226, v212, v213\n"
"v_cvt_pk_bf16_f32 v227, v214, v215\n"
"v_cvt_pk_bf16_f32 v222, v204, v205\n"
"v_cvt_pk_bf16_f32 v223, v206, v207\n"
"v_cvt_pk_bf16_f32 v152, v228, v229\n"
"v_cvt_pk_bf16_f32 v153, v230, v231\n"
"v_cvt_pk_bf16_f32 v154, v216, v217\n"
"v_cvt_pk_bf16_f32 v155, v218, v219\n"
"s_waitcnt lgkmcnt(1)\n"
"v_smfmac_f32_16x16x64_bf16 v[106:109], v[82:85], v[120:127], v66\n"
"s_add_u32 m0, s34, 2048\n"
"s_nop 0\n"
"buffer_load_dwordx4 v190, s[8:11], s1 offen lds\n"
"v_mfma_f32_16x16x32_bf16 v[82:85], v[172:175], v[82:85], 0\n"
"ds_read_b64_tr_b16 v[114:115], v183 offset:54088\n"
"ds_read_b64_tr_b16 v[116:117], v182 offset:54856\n"
"v_smfmac_f32_16x16x64_bf16 v[252:255], v[90:93], v[244:251], v66\n"
"ds_read_b64_tr_b16 v[118:119], v183 offset:54856\n"
"v_smfmac_f32_16x16x64_bf16 v[98:101], v[90:93], v[128:135], v66\n"
"ds_write_b128 v186, v[208:211] offset:6752\n"
"v_mfma_f32_16x16x32_bf16 v[208:211], a[152:155], v[32:35], 0\n"
"s_waitcnt vmcnt(16)\n"
"v_mfma_f32_16x16x32_bf16 v[208:211], a[164:167], v[36:39], v[208:211]\n"
"ds_read_b128 v[32:35], v188 offset:6144\n"
"v_mfma_f32_16x16x32_bf16 v[208:211], a[176:179], v[40:43], v[208:211]\n"
"s_waitcnt vmcnt(14)\n"
"ds_read_b128 v[36:39], v189 offset:6144\n"
"ds_read_b128 v[40:43], v188 offset:7168\n"
"v_mfma_f32_16x16x32_bf16 v[208:211], a[188:191], v[44:47], v[208:211]\n"
"ds_read_b128 v[44:47], v189 offset:7168\n"
"v_cvt_pk_bf16_f32 v252, v252, v253\n"
"v_mfma_f32_16x16x32_bf16 v[208:211], a[200:203], v[48:51], v[208:211]\n"
"s_waitcnt vmcnt(10)\n"
"ds_read_b64_tr_b16 a[220:221], v236 offset:8\n"
"v_cvt_pk_bf16_f32 v253, v254, v255\n"
"v_mfma_f32_16x16x32_bf16 v[208:211], a[212:215], v[52:55], v[208:211]\n"
"s_add_u32 m0, s34, 5120\n"
"v_cvt_pk_bf16_f32 v98, v98, v99\n"
"v_cvt_pk_bf16_f32 v99, v100, v101\n"
"buffer_load_dwordx4 v191, s[8:11], s1 offen lds\n"
"ds_read_b128 v[48:51], v188 offset:8192\n"
"s_waitcnt lgkmcnt(7)\n"
"v_smfmac_f32_16x16x64_bf16 v[106:109], v[90:93], v[112:119], v66\n"
"s_add_u32 s1, s1, s33\n"
"ds_read_b128 v[52:55], v189 offset:8192\n"
"v_mfma_f32_16x16x32_bf16 v[90:93], v[172:175], v[90:93], v[82:85]\n"
"ds_write_b128 v186, v[220:223] offset:6816\n"
"v_cvt_pk_bf16_f32 v166, v208, v209\n"
"v_cvt_pk_bf16_f32 v167, v210, v211\n"
"s_waitcnt lgkmcnt(7)\n"
"v_mfma_f32_16x16x32_bf16 v[212:215], a[0:3], v[32:35], 0\n"
"s_waitcnt lgkmcnt(6)\n"
"v_cvt_pk_bf16_f32 v106, v106, v107\n"
"v_cvt_pk_bf16_f32 v107, v108, v109\n"
"v_mfma_f32_16x16x32_bf16 v[204:207], a[4:7], v[32:35], 0\n"
"v_rcp_f32 v171, v90\n"
"v_mfma_f32_16x16x32_bf16 v[220:223], a[8:11], v[32:35], 0\n"
"ds_write_b128 v186, v[224:227] offset:6880\n"
"v_mfma_f32_16x16x32_bf16 v[212:215], a[12:15], v[36:39], v[212:215]\n"
"s_waitcnt lgkmcnt(6)\n"
"v_cvt_pk_bf16_f32 v171, v171, v171\n"
"v_and_b32 v64, v171, v176\n"
"v_mfma_f32_16x16x32_bf16 v[204:207], a[16:19], v[36:39], v[204:207]\n"
"v_and_b32 v65, v171, v177\n"
"s_add_u32 m0, s34, 6144\n"
"v_mfma_f32_16x16x32_bf16 v[220:223], a[20:23], v[36:39], v[220:223]\n"
"buffer_load_dwordx4 v190, s[8:11], s32 offen lds\n"
"ds_read_b64_tr_b16 a[222:223], v236 offset:6152\n"
"v_mfma_f32_16x16x16_bf16 v[252:255], v[252:253], v[64:65], 0\n"
"ds_read_b64_tr_b16 a[224:225], v235\n"
"v_mfma_f32_16x16x16_bf16 v[98:101], v[98:99], v[64:65], 0\n"
"v_mfma_f32_16x16x16_bf16 v[106:109], v[106:107], v[64:65], 0\n"
"ds_read_b64_tr_b16 a[226:227], v235 offset:6144\n"
"v_mfma_f32_16x16x32_bf16 v[212:215], a[24:27], v[40:43], v[212:215]\n"
"s_waitcnt lgkmcnt(8)\n"
"v_mfma_f32_16x16x32_bf16 v[204:207], a[28:31], v[40:43], v[204:207]\n"
"ds_read_b128 v[120:123], v180\n"
"v_cvt_pk_bf16_f32 v252, v252, v253\n"
"v_mfma_f32_16x16x32_bf16 v[220:223], a[32:35], v[40:43], v[220:223]\n"
"v_cvt_pk_bf16_f32 v253, v254, v255\n"
"v_cvt_pk_bf16_f32 v254, v98, v99\n"
"v_mfma_f32_16x16x32_bf16 v[212:215], a[36:39], v[44:47], v[212:215]\n"
"s_waitcnt lgkmcnt(7)\n"
"v_cvt_pk_bf16_f32 v255, v100, v101\n"
"v_cvt_pk_bf16_f32 v106, v106, v107\n"
"v_mfma_f32_16x16x32_bf16 v[204:207], a[40:43], v[44:47], v[204:207]\n"
"buffer_store_dwordx4 v[252:255], v192, s[28:31], s37 offen sc1\n"
"v_mfma_f32_16x16x32_bf16 v[220:223], a[44:47], v[44:47], v[220:223]\n"
"s_waitcnt vmcnt(10) lgkmcnt(6)\n"
"v_cvt_pk_bf16_f32 v107, v108, v109\n"
"v_mov_b64 v[252:253], 0\n"
"v_mfma_f32_16x16x32_bf16 v[212:215], a[48:51], v[48:51], v[212:215]\n"
"s_add_u32 m0, s34, 9216\n"
"v_readfirstlane_b32 s6, v201\n"
"v_mov_b64 v[254:255], 0\n"
"v_mfma_f32_16x16x32_bf16 v[204:207], a[52:55], v[48:51], v[204:207]\n"
"s_and_b32 s6, s6, 0xffffff00\n"
"buffer_load_dwordx4 v191, s[8:11], s32 offen lds\n"
"v_or_b32 v201, 2, s6\n"
"v_mfma_f32_16x16x32_bf16 v[220:223], a[56:59], v[48:51], v[220:223]\n"
"s_add_u32 s32, s32, s33\n"
"ds_read_b128 v[136:139], v181\n"
"ds_read_b128 v[124:127], v180 offset:13504\n"
"v_mfma_f32_16x16x32_bf16 v[212:215], a[60:63], v[52:55], v[212:215]\n"
"s_waitcnt lgkmcnt(2)\n"
"ds_read_b128 v[140:143], v181 offset:13504\n"
"v_mfma_f32_16x16x32_bf16 v[204:207], a[64:67], v[52:55], v[204:207]\n"
"ds_read_b64 v[82:83], v180 offset:64\n"
"s_mov_b64 exec, 1\n"
"buffer_store_dword v201, off, s[28:31], s40 sc1\n"
"s_mov_b64 exec, -1\n"
"v_mfma_f32_16x16x32_bf16 v[220:223], a[68:71], v[52:55], v[220:223]\n"
"ds_read_b64 v[84:85], v181 offset:64\n"
"ds_read_b64 v[90:91], v180 offset:13568\n"
"v_mfma_f32_16x16x32_bf16 v[120:123], v[120:123], v[144:147], 0\n"
"buffer_store_dwordx2 v[106:107], v193, s[28:31], s37 offen offset:1024 sc1\n"
"v_mfma_f32_16x16x32_bf16 v[224:227], a[72:75], v[32:35], 0\n"
"s_add_u32 s37, s37, 6144\n"
"ds_read_b64 v[92:93], v181 offset:13568\n"
"ds_read_b128 v[244:247], v180 offset:27008\n"
"v_mfma_f32_16x16x32_bf16 v[228:231], a[76:79], v[32:35], 0\n"
"s_add_u32 s4, s37, 55296\n"
"ds_read_b128 v[128:131], v181 offset:27008\n"
"v_cvt_pk_bf16_f32 v212, v212, v213\n"
"v_mfma_f32_16x16x32_bf16 v[216:219], a[80:83], v[32:35], 0\n"
"s_waitcnt lgkmcnt(8)\n"
"ds_read_b64 v[98:99], v180 offset:27072\n"
"v_cvt_pk_bf16_f32 v213, v214, v215\n"
"v_mfma_f32_16x16x32_bf16 v[136:139], v[136:139], v[144:147], 0\n"
"s_waitcnt lgkmcnt(8)\n"
"ds_read_b64 v[100:101], v181 offset:27072\n"
"v_cvt_pk_bf16_f32 v214, v204, v205\n"
"v_mfma_f32_16x16x32_bf16 v[124:127], v[124:127], v[144:147], 0\n"
"s_waitcnt lgkmcnt(8)\n"
"ds_read_b64_tr_b16 v[112:113], v182 offset:40640\n"
"v_cvt_pk_bf16_f32 v215, v206, v207\n"
"v_mfma_f32_16x16x32_bf16 v[140:143], v[140:143], v[144:147], 0\n"
"s_waitcnt lgkmcnt(8)\n"
"ds_read_b64_tr_b16 v[114:115], v183 offset:40640\n"
"v_cvt_pk_bf16_f32 v220, v220, v221\n"
"v_mfma_f32_16x16x32_bf16 v[120:123], v[80:83], v[156:159], v[120:123]\n"
"s_waitcnt lgkmcnt(8)\n"
"ds_read_b64_tr_b16 v[116:117], v182 offset:41408\n"
"v_cvt_pk_bf16_f32 v221, v222, v223\n"
"v_mfma_f32_16x16x32_bf16 v[136:139], v[84:87], v[158:161], v[136:139]\n"
"s_waitcnt lgkmcnt(8)\n"
"ds_read_b64_tr_b16 v[118:119], v183 offset:41408\n"
"s_add_u32 m0, s34, 7168\n"
"v_mfma_f32_16x16x32_bf16 v[124:127], v[88:91], v[156:159], v[124:127]\n"
"s_waitcnt lgkmcnt(8)\n"
"buffer_load_dwordx4 v190, s[8:11], s0 offen lds\n"
"v_mfma_f32_16x16x32_bf16 v[140:143], v[92:95], v[158:161], v[140:143]\n"
"s_waitcnt vmcnt(10) lgkmcnt(7)\n"
"v_max_f32 v120, v120, v136\n"
"v_max_f32 v121, v121, v137\n"
"v_mfma_f32_16x16x32_bf16 v[244:247], v[244:247], v[144:147], 0\n"
"s_waitcnt lgkmcnt(6)\n"
"ds_read_b64_tr_b16 v[136:137], v182 offset:40648\n"
"v_max_f32 v122, v122, v138\n"
"v_mfma_f32_16x16x32_bf16 v[128:131], v[128:131], v[144:147], 0\n"
"s_waitcnt lgkmcnt(6)\n"
"v_max_f32 v123, v123, v139\n"
"v_max_f32 v124, v124, v140\n"
"v_mfma_f32_16x16x32_bf16 v[244:247], v[96:99], v[156:159], v[244:247]\n"
"s_waitcnt lgkmcnt(5)\n"
"ds_read_b64_tr_b16 v[138:139], v183 offset:40648\n"
"v_max_f32 v125, v125, v141\n"
"v_mfma_f32_16x16x32_bf16 v[128:131], v[100:103], v[158:161], v[128:131]\n"
"s_barrier\n"
"v_mfma_f32_16x16x32_bf16 v[224:227], a[84:87], v[36:39], v[224:227]\n"
"ds_read_b128 v[248:251], v178\n"
"ds_read_b128 v[132:135], v179\n"
"v_mfma_f32_16x16x32_bf16 v[228:231], a[88:91], v[36:39], v[228:231]\n"
"ds_read_b64 v[106:107], v178 offset:64\n"
"v_max_f32 v126, v126, v142\n"
"v_mfma_f32_16x16x32_bf16 v[216:219], a[92:95], v[36:39], v[216:219]\n"
"ds_read_b64 v[108:109], v179 offset:64\n"
"v_max_f32 v127, v127, v143\n"
"v_mfma_f32_16x16x32_bf16 v[224:227], a[96:99], v[40:43], v[224:227]\n"
"ds_write_b128 v186, v[212:215] offset:20256\n"
"v_mfma_f32_16x16x32_bf16 v[228:231], a[100:103], v[40:43], v[228:231]\n"
"s_add_u32 m0, s79, 130176\n"
"v_max_f32 v170, v120, v121\n"
"v_max_f32 v171, v122, v123\n"
"v_mfma_f32_16x16x32_bf16 v[216:219], a[104:107], v[40:43], v[216:219]\n"
"buffer_load_dwordx4 v192, s[16:19], s43 offen lds\n"
"ds_read_b64_tr_b16 v[140:141], v182 offset:41416\n"
"v_mfma_f32_16x16x32_bf16 v[224:227], a[108:111], v[44:47], v[224:227]\n"
"ds_read_b64_tr_b16 v[142:143], v183 offset:41416\n"
"ds_read_b64_tr_b16 a[228:229], v236 offset:12288\n"
"v_mfma_f32_16x16x32_bf16 v[228:231], a[112:115], v[44:47], v[228:231]\n"
"s_waitcnt lgkmcnt(7)\n"
"v_max3_f32 v170, v170, v124, v125\n"
"v_max3_f32 v171, v171, v126, v127\n"
"v_mfma_f32_16x16x32_bf16 v[248:251], v[248:251], v[144:147], 0\n"
"s_waitcnt lgkmcnt(6)\n"
"ds_read_b64_tr_b16 a[230:231], v236 offset:18432\n"
"v_max_f32 v244, v244, v128\n"
"v_mfma_f32_16x16x32_bf16 v[132:135], v[132:135], v[144:147], 0\n"
"s_waitcnt lgkmcnt(6)\n"
"v_max_f32 v245, v245, v129\n"
"v_max_f32 v246, v246, v130\n"
"v_mfma_f32_16x16x32_bf16 v[248:251], v[104:107], v[156:159], v[248:251]\n"
"s_waitcnt lgkmcnt(5)\n"
"ds_read_b64_tr_b16 v[128:129], v182 offset:40584\n"
"v_max_f32 v247, v247, v131\n"
"v_mfma_f32_16x16x32_bf16 v[132:135], v[108:111], v[158:161], v[132:135]\n"
"ds_read_b64_tr_b16 v[130:131], v183 offset:40584\n"
"v_max3_f32 v170, v170, v244, v245\n"
"v_mfma_f32_16x16x32_bf16 v[216:219], a[116:119], v[44:47], v[216:219]\n"
"v_max3_f32 v171, v171, v246, v247\n"
"v_mov_b64 v[98:99], 0\n"
"v_mfma_f32_16x16x32_bf16 v[224:227], a[120:123], v[48:51], v[224:227]\n"
"ds_read_b64_tr_b16 a[232:233], v236 offset:12296\n"
"v_mov_b64 v[100:101], 0\n"
"v_mfma_f32_16x16x32_bf16 v[228:231], a[124:127], v[48:51], v[228:231]\n"
"v_max_f32 v248, v248, v132\n"
"v_max_f32 v249, v249, v133\n"
"v_mfma_f32_16x16x32_bf16 v[216:219], a[128:131], v[48:51], v[216:219]\n"
"s_add_u32 m0, s79, 130176\n"
"ds_read_b64_tr_b16 v[132:133], v182 offset:41352\n"
"buffer_load_dwordx4 v192, s[16:19], s43 offen offset:1024 lds\n"
"v_mfma_f32_16x16x32_bf16 v[224:227], a[132:135], v[52:55], v[224:227]\n"
"v_max_f32 v250, v250, v134\n"
"v_max_f32 v251, v251, v135\n"
"v_mfma_f32_16x16x32_bf16 v[228:231], a[136:139], v[52:55], v[228:231]\n"
"ds_read_b64_tr_b16 v[134:135], v183 offset:41352\n"
"v_max3_f32 v170, v170, v248, v249\n"
"v_mfma_f32_16x16x32_bf16 v[216:219], a[140:143], v[52:55], v[216:219]\n"
"v_max3_f32 v171, v171, v250, v251\n"
"v_max_f32 v170, v170, v171\n"
"v_mfma_f32_16x16x32_bf16 v[208:211], a[144:147], v[32:35], 0\n"
"v_mov_b32 v171, v170\n"
"v_mov_b64 v[106:107], 0\n"
"v_mfma_f32_16x16x32_bf16 v[204:207], a[148:151], v[32:35], 0\n"
"v_permlane16_swap_b32 v170, v171\n"
"v_mfma_f32_16x16x32_bf16 v[212:215], a[152:155], v[32:35], 0\n"
"ds_read_b128 v[32:35], v188\n"
"v_max_f32 v170, v170, v171\n"
"v_mfma_f32_16x16x32_bf16 v[208:211], a[156:159], v[36:39], v[208:211]\n"
"v_mov_b32 v171, v170\n"
"v_mov_b64 v[108:109], 0\n"
"v_mfma_f32_16x16x32_bf16 v[204:207], a[160:163], v[36:39], v[204:207]\n"
"v_permlane32_swap_b32 v170, v171\n"
"v_mfma_f32_16x16x32_bf16 v[212:215], a[164:167], v[36:39], v[212:215]\n"
"ds_read_b128 v[36:39], v189\n"
"v_max_f32 v170, v170, v171\n"
"v_mfma_f32_16x16x32_bf16 v[208:211], a[168:171], v[40:43], v[208:211]\n"
"s_add_u32 m0, s79, 130176\n"
"v_mul_f32 v170, 0xbe553b95, v170\n"
"v_fmamk_f32 v120, v120, 0x3e553b95, v170\n"
"v_mfma_f32_16x16x32_bf16 v[204:207], a[172:175], v[40:43], v[204:207]\n"
"buffer_load_dwordx4 v192, s[16:19], s43 offen offset:2048 lds\n"
"v_fmamk_f32 v121, v121, 0x3e553b95, v170\n"
"v_mfma_f32_16x16x32_bf16 v[212:215], a[176:179], v[40:43], v[212:215]\n"
"s_add_u32 s43, s79, 55296\n"
"v_fmamk_f32 v122, v122, 0x3e553b95, v170\n"
"v_fmamk_f32 v123, v123, 0x3e553b95, v170\n"
"v_mfma_f32_16x16x32_bf16 v[208:211], a[180:183], v[44:47], v[208:211]\n"
"v_fmamk_f32 v124, v124, 0x3e553b95, v170\n"
"v_fmamk_f32 v125, v125, 0x3e553b95, v170\n"
"v_mfma_f32_16x16x32_bf16 v[204:207], a[184:187], v[44:47], v[204:207]\n"
"v_fmamk_f32 v126, v126, 0x3e553b95, v170\n"
"v_fmamk_f32 v127, v127, 0x3e553b95, v170\n"
"v_mfma_f32_16x16x32_bf16 v[212:215], a[188:191], v[44:47], v[212:215]\n"
"s_waitcnt vmcnt(11)\n"
"buffer_store_dwordx4 v[68:71], v192, s[28:31], s4 offen sc1\n"
"v_mfma_f32_16x16x32_bf16 v[208:211], a[192:195], v[48:51], v[208:211]\n"
"ds_read_b128 v[40:43], v188 offset:1024\n"
"ds_read_b128 v[44:47], v189 offset:1024\n"
"v_mfma_f32_16x16x32_bf16 v[204:207], a[196:199], v[48:51], v[204:207]\n"
"v_exp_f32 v120, v120\n"
"v_mfma_f32_16x16x32_bf16 v[212:215], a[200:203], v[48:51], v[212:215]\n"
"v_exp_f32 v121, v121\n"
"v_mfma_f32_16x16x32_bf16 v[208:211], a[204:207], v[52:55], v[208:211]\n"
"v_exp_f32 v122, v122\n"
"v_mfma_f32_16x16x32_bf16 v[204:207], a[208:211], v[52:55], v[204:207]\n"
"s_waitcnt lgkmcnt(2)\n"
"v_exp_f32 v123, v123\n"
"v_mfma_f32_16x16x32_bf16 v[212:215], a[212:215], v[52:55], v[212:215]\n"
"s_add_u32 m0, s34, 10240\n"
"v_exp_f32 v124, v124\n"
"buffer_load_dwordx4 v191, s[8:11], s0 offen lds\n"
"s_add_u32 s0, s0, s33\n"
"v_exp_f32 v125, v125\n"
"v_exp_f32 v126, v126\n"
"v_exp_f32 v127, v127\n"
"v_cvt_pk_bf16_f32 v82, v120, v122\n"
"v_cvt_pk_bf16_f32 v83, v121, v123\n"
"v_cvt_pk_bf16_f32 v84, v124, v126\n"
"v_cvt_pk_bf16_f32 v85, v125, v127\n"
".Lic2%=:\n"
"s_cbranch_execz .Lib0%=\n"
"v_fmamk_f32 v244, v244, 0x3e553b95, v170\n"
"v_fmamk_f32 v245, v245, 0x3e553b95, v170\n"
"v_smfmac_f32_16x16x64_bf16 v[252:255], v[82:85], v[112:119], v66\n"
"ds_read_b64_tr_b16 v[120:121], v184 offset:128\n"
"v_smfmac_f32_16x16x64_bf16 v[98:101], v[82:85], v[136:143], v66\n"
"ds_read_b64_tr_b16 v[122:123], v185 offset:128\n"
"v_smfmac_f32_16x16x64_bf16 v[106:109], v[82:85], v[128:135], v66\n"
"ds_read_b64_tr_b16 v[124:125], v184 offset:896\n"
"v_mfma_f32_16x16x32_bf16 v[82:85], v[172:175], v[82:85], 0\n"
"ds_read_b64_tr_b16 v[126:127], v185 offset:896\n"
"ds_read_b64_tr_b16 v[112:113], v184 offset:136\n"
"v_fmamk_f32 v246, v246, 0x3e553b95, v170\n"
"ds_read_b64_tr_b16 v[114:115], v185 offset:136\n"
"v_fmamk_f32 v247, v247, 0x3e553b95, v170\n"
"ds_read_b64_tr_b16 v[116:117], v184 offset:904\n"
"s_add_u32 m0, s34, 8192\n"
"ds_read_b64_tr_b16 v[118:119], v185 offset:904\n"
"buffer_load_dwordx4 v190, s[8:11], s1 offen lds\n"
"v_fmamk_f32 v248, v248, 0x3e553b95, v170\n"
"v_fmamk_f32 v249, v249, 0x3e553b95, v170\n"
"ds_read_b64_tr_b16 v[136:137], v184 offset:72\n"
"v_fmamk_f32 v250, v250, 0x3e553b95, v170\n"
"ds_read_b64_tr_b16 v[138:139], v185 offset:72\n"
"v_fmamk_f32 v251, v251, 0x3e553b95, v170\n"
"ds_read_b64_tr_b16 v[140:141], v184 offset:840\n"
"v_exp_f32 v244, v244\n"
"ds_read_b64_tr_b16 v[142:143], v185 offset:840\n"
"v_exp_f32 v245, v245\n"
"v_exp_f32 v246, v246\n"
"v_exp_f32 v247, v247\n"
"v_exp_f32 v248, v248\n"
"v_exp_f32 v249, v249\n"
"v_exp_f32 v250, v250\n"
"v_exp_f32 v251, v251\n"
"s_waitcnt vmcnt(12)\n"
"ds_read_b128 v[48:51], v188 offset:2048\n"
"ds_read_b128 v[52:55], v189 offset:2048\n"
"v_cvt_pk_bf16_f32 v90, v244, v246\n"
"s_add_u32 m0, s34, 11264\n"
"v_cvt_pk_bf16_f32 v91, v245, v247\n"
"buffer_load_dwordx4 v191, s[8:11], s1 offen lds\n"
"s_add_u32 s1, s1, s33\n"
"v_cvt_pk_bf16_f32 v92, v248, v250\n"
"v_cvt_pk_bf16_f32 v93, v249, v251\n"
"v_cvt_pk_bf16_f32 v224, v224, v225\n"
"v_cvt_pk_bf16_f32 v225, v226, v227\n"
"v_cvt_pk_bf16_f32 v226, v228, v229\n"
"v_cvt_pk_bf16_f32 v227, v230, v231\n"
"v_mfma_f32_16x16x32_bf16 v[228:231], a[0:3], v[32:35], 0\n"
"s_waitcnt lgkmcnt(2)\n"
"ds_write_b128 v186, v[224:227] offset:20384\n"
"v_smfmac_f32_16x16x64_bf16 v[252:255], v[90:93], v[120:127], v66\n"
"v_cvt_pk_bf16_f32 v222, v216, v217\n"
"v_smfmac_f32_16x16x64_bf16 v[98:101], v[90:93], v[112:119], v66\n"
"ds_read_b64_tr_b16 a[234:235], v236 offset:18440\n"
"v_smfmac_f32_16x16x64_bf16 v[106:109], v[90:93], v[136:143], v66\n"
"ds_read_b64_tr_b16 a[236:237], v235 offset:12288\n"
"v_mfma_f32_16x16x32_bf16 v[90:93], v[172:175], v[90:93], v[82:85]\n"
"v_cvt_pk_bf16_f32 v223, v218, v219\n"
"v_cvt_pk_bf16_f32 v144, v208, v209\n"
"v_mfma_f32_16x16x32_bf16 v[216:219], a[4:7], v[32:35], 0\n"
"ds_write_b128 v186, v[220:223] offset:20320\n"
"v_mfma_f32_16x16x32_bf16 v[220:223], a[8:11], v[32:35], 0\n"
"s_waitcnt lgkmcnt(4)\n"
"v_cvt_pk_bf16_f32 v252, v252, v253\n"
"v_cvt_pk_bf16_f32 v253, v254, v255\n"
"v_mfma_f32_16x16x32_bf16 v[228:231], a[12:15], v[36:39], v[228:231]\n"
"s_add_u32 m0, s34, 0\n"
"v_rcp_f32 v171, v90\n"
"v_mfma_f32_16x16x32_bf16 v[216:219], a[16:19], v[36:39], v[216:219]\n"
"buffer_load_dwordx4 v190, s[8:11], s32 offen lds\n"
"v_cvt_pk_bf16_f32 v171, v171, v171\n"
"v_mfma_f32_16x16x32_bf16 v[220:223], a[20:23], v[36:39], v[220:223]\n"
"v_and_b32 v64, v171, v176\n"
"v_and_b32 v65, v171, v177\n"
"v_mfma_f32_16x16x32_bf16 v[228:231], a[24:27], v[40:43], v[228:231]\n"
"v_cvt_pk_bf16_f32 v98, v98, v99\n"
"v_cvt_pk_bf16_f32 v99, v100, v101\n"
"v_mfma_f32_16x16x16_bf16 v[252:255], v[252:253], v[64:65], 0\n"
"v_cvt_pk_bf16_f32 v106, v106, v107\n"
"v_cvt_pk_bf16_f32 v107, v108, v109\n"
"v_mfma_f32_16x16x16_bf16 v[98:101], v[98:99], v[64:65], 0\n"
"ds_read_b64_tr_b16 a[238:239], v235 offset:18432\n"
"v_cvt_pk_bf16_f32 v145, v210, v211\n"
"v_mfma_f32_16x16x16_bf16 v[106:109], v[106:107], v[64:65], 0\n"
"ds_read_b128 v[128:131], v180 offset:13504\n"
"buffer_load_dword v200, off, s[28:31], s39 sc1\n"
"v_mfma_f32_16x16x32_bf16 v[216:219], a[28:31], v[40:43], v[216:219]\n"
"v_cvt_pk_bf16_f32 v252, v252, v253\n"
"v_cvt_pk_bf16_f32 v253, v254, v255\n"
"v_mfma_f32_16x16x32_bf16 v[220:223], a[32:35], v[40:43], v[220:223]\n"
"v_cvt_pk_bf16_f32 v254, v98, v99\n"
"v_cvt_pk_bf16_f32 v255, v100, v101\n"
"v_mfma_f32_16x16x32_bf16 v[228:231], a[36:39], v[44:47], v[228:231]\n"
"buffer_store_dwordx4 v[252:255], v192, s[28:31], s37 offen sc1\n"
"v_mfma_f32_16x16x32_bf16 v[216:219], a[40:43], v[44:47], v[216:219]\n"
"s_add_u32 m0, s34, 3072\n"
"v_cvt_pk_bf16_f32 v106, v106, v107\n"
"v_cvt_pk_bf16_f32 v107, v108, v109\n"
"v_mfma_f32_16x16x32_bf16 v[220:223], a[44:47], v[44:47], v[220:223]\n"
"ds_read_b128 v[244:247], v181 offset:13504\n"
"buffer_load_dwordx4 v191, s[8:11], s32 offen lds\n"
"v_mfma_f32_16x16x32_bf16 v[228:231], a[48:51], v[48:51], v[228:231]\n"
"s_add_u32 s32, s32, s33\n"
"ds_read_b128 v[132:135], v180 offset:27008\n"
"ds_read_b128 v[248:251], v181 offset:27008\n"
"v_mfma_f32_16x16x32_bf16 v[216:219], a[52:55], v[48:51], v[216:219]\n"
"ds_read_b64 v[82:83], v180 offset:13568\n"
"v_cvt_pk_bf16_f32 v146, v204, v205\n"
"v_mfma_f32_16x16x32_bf16 v[220:223], a[56:59], v[48:51], v[220:223]\n"
"s_waitcnt lgkmcnt(4)\n"
"ds_read_b64 v[84:85], v181 offset:13568\n"
"v_cvt_pk_bf16_f32 v147, v206, v207\n"
"v_mfma_f32_16x16x32_bf16 v[128:131], v[128:131], v[148:151], 0\n"
"ds_read_b64 v[90:91], v180 offset:27072\n"
"buffer_store_dwordx2 v[106:107], v193, s[28:31], s37 offen offset:1024 sc1\n"
"v_mfma_f32_16x16x32_bf16 v[228:231], a[60:63], v[52:55], v[228:231]\n"
"s_add_u32 s37, s37, 6144\n"
"ds_read_b64 v[92:93], v181 offset:27072\n"
"ds_read_b128 v[120:123], v178\n"
"v_mfma_f32_16x16x32_bf16 v[216:219], a[64:67], v[52:55], v[216:219]\n"
"s_add_u32 s4, s37, 49152\n"
"ds_read_b128 v[112:115], v179\n"
"v_cvt_pk_bf16_f32 v158, v212, v213\n"
"v_mfma_f32_16x16x32_bf16 v[220:223], a[68:71], v[52:55], v[220:223]\n"
"s_waitcnt lgkmcnt(8)\n"
"ds_read_b64 v[98:99], v178 offset:64\n"
"v_cvt_pk_bf16_f32 v159, v214, v215\n"
"v_mfma_f32_16x16x32_bf16 v[244:247], v[244:247], v[148:151], 0\n"
"s_waitcnt lgkmcnt(8)\n"
"ds_read_b64 v[100:101], v179 offset:64\n"
"v_cvt_pk_bf16_f32 v228, v228, v229\n"
"v_mfma_f32_16x16x32_bf16 v[132:135], v[132:135], v[148:151], 0\n"
"s_waitcnt lgkmcnt(8)\n"
"ds_read_b64_tr_b16 v[136:137], v182 offset:54144\n"
"v_cvt_pk_bf16_f32 v229, v230, v231\n"
"v_mfma_f32_16x16x32_bf16 v[248:251], v[248:251], v[148:151], 0\n"
"s_waitcnt lgkmcnt(8)\n"
"ds_read_b64_tr_b16 v[138:139], v183 offset:54144\n"
"v_cvt_pk_bf16_f32 v230, v216, v217\n"
"v_mfma_f32_16x16x32_bf16 v[128:131], v[80:83], v[160:163], v[128:131]\n"
"s_waitcnt vmcnt(9) lgkmcnt(8)\n"
"ds_read_b64_tr_b16 v[140:141], v182 offset:54912\n"
"v_mov_b64 v[252:253], 0\n"
"v_mfma_f32_16x16x32_bf16 v[244:247], v[84:87], v[162:165], v[244:247]\n"
"s_waitcnt lgkmcnt(8)\n"
"ds_read_b64_tr_b16 v[142:143], v183 offset:54912\n"
"v_mov_b64 v[254:255], 0\n"
"v_mfma_f32_16x16x32_bf16 v[132:135], v[88:91], v[160:163], v[132:135]\n"
"s_waitcnt lgkmcnt(8)\n"
"v_cvt_pk_bf16_f32 v231, v218, v219\n"
"v_cvt_pk_bf16_f32 v220, v220, v221\n"
"v_mfma_f32_16x16x32_bf16 v[248:251], v[92:95], v[162:165], v[248:251]\n"
"s_waitcnt lgkmcnt(7)\n"
"v_max_f32 v128, v128, v244\n"
"v_max_f32 v129, v129, v245\n"
"v_mfma_f32_16x16x32_bf16 v[120:123], v[120:123], v[148:151], 0\n"
"s_waitcnt lgkmcnt(6)\n"
"ds_read_b64_tr_b16 v[244:245], v182 offset:54152\n"
"v_max_f32 v130, v130, v246\n"
"v_mfma_f32_16x16x32_bf16 v[112:115], v[112:115], v[148:151], 0\n"
"s_waitcnt lgkmcnt(6)\n"
"v_max_f32 v131, v131, v247\n"
"v_max_f32 v132, v132, v248\n"
"v_mfma_f32_16x16x32_bf16 v[120:123], v[96:99], v[160:163], v[120:123]\n"
"s_waitcnt lgkmcnt(5)\n"
"ds_read_b64_tr_b16 v[246:247], v183 offset:54152\n"
"v_max_f32 v133, v133, v249\n"
"v_mfma_f32_16x16x32_bf16 v[112:115], v[100:103], v[162:165], v[112:115]\n"
"s_add_u32 m0, s34, 1024\n"
"ds_read_b64_tr_b16 v[248:249], v182 offset:54920\n"
"buffer_load_dwordx4 v190, s[8:11], s0 offen lds\n"
"v_mfma_f32_16x16x32_bf16 v[224:227], a[72:75], v[32:35], 0\n"
"s_barrier\n"
"v_mfma_f32_16x16x32_bf16 v[208:211], a[76:79], v[32:35], 0\n"
"ds_read_b128 v[124:127], v178 offset:13504\n"
"ds_read_b128 v[116:119], v179 offset:13504\n"
"v_mfma_f32_16x16x32_bf16 v[204:207], a[80:83], v[32:35], 0\n"
"ds_read_b64 v[106:107], v178 offset:13568\n"
"v_max_f32 v134, v134, v250\n"
"v_mfma_f32_16x16x32_bf16 v[224:227], a[84:87], v[36:39], v[224:227]\n"
"ds_read_b64 v[108:109], v179 offset:13568\n"
"v_max_f32 v135, v135, v251\n"
"v_mfma_f32_16x16x32_bf16 v[208:211], a[88:91], v[36:39], v[208:211]\n"
"ds_read_b64_tr_b16 v[250:251], v183 offset:54920\n"
"v_max_f32 v170, v128, v129\n"
"v_mfma_f32_16x16x32_bf16 v[204:207], a[92:95], v[36:39], v[204:207]\n"
"ds_write_b128 v186, v[228:231] offset:33760\n"
"v_mfma_f32_16x16x32_bf16 v[224:227], a[96:99], v[40:43], v[224:227]\n"
"v_max_f32 v171, v130, v131\n"
"v_max3_f32 v170, v170, v132, v133\n"
"v_mfma_f32_16x16x32_bf16 v[208:211], a[100:103], v[40:43], v[208:211]\n"
"v_max3_f32 v171, v171, v134, v135\n"
"v_max_f32 v120, v120, v112\n"
"v_mfma_f32_16x16x32_bf16 v[204:207], a[104:107], v[40:43], v[204:207]\n"
"s_waitcnt lgkmcnt(5)\n"
"ds_read_b64_tr_b16 a[240:241], v236\n"
"v_max_f32 v121, v121, v113\n"
"v_mfma_f32_16x16x32_bf16 v[124:127], v[124:127], v[148:151], 0\n"
"s_waitcnt lgkmcnt(5)\n"
"ds_read_b64_tr_b16 v[112:113], v182 offset:54088\n"
"v_max_f32 v122, v122, v114\n"
"v_mfma_f32_16x16x32_bf16 v[116:119], v[116:119], v[148:151], 0\n"
"s_waitcnt lgkmcnt(5)\n"
"v_max_f32 v123, v123, v115\n"
"v_max3_f32 v170, v170, v120, v121\n"
"v_mfma_f32_16x16x32_bf16 v[124:127], v[104:107], v[160:163], v[124:127]\n"
"s_waitcnt lgkmcnt(4)\n"
"ds_read_b64_tr_b16 v[114:115], v183 offset:54088\n"
"v_max3_f32 v171, v171, v122, v123\n"
"v_mfma_f32_16x16x32_bf16 v[116:119], v[108:111], v[162:165], v[116:119]\n"
"s_add_u32 m0, s79, 142464\n"
"ds_read_b64_tr_b16 a[242:243], v236 offset:6144\n"
"buffer_load_dwordx4 v192, s[16:19], s43 offen lds\n"
"v_mfma_f32_16x16x32_bf16 v[224:227], a[108:111], v[44:47], v[224:227]\n"
"v_mov_b64 v[98:99], 0\n"
"v_mov_b64 v[100:101], 0\n"
"v_mfma_f32_16x16x32_bf16 v[208:211], a[112:115], v[44:47], v[208:211]\n"
"v_cvt_pk_bf16_f32 v221, v222, v223\n"
"v_max_f32 v124, v124, v116\n"
"v_mfma_f32_16x16x32_bf16 v[204:207], a[116:119], v[44:47], v[204:207]\n"
"v_max_f32 v125, v125, v117\n"
"v_max_f32 v126, v126, v118\n"
"v_mfma_f32_16x16x32_bf16 v[224:227], a[120:123], v[48:51], v[224:227]\n"
"ds_read_b64_tr_b16 v[116:117], v182 offset:54856\n"
"v_max_f32 v127, v127, v119\n"
"v_mfma_f32_16x16x32_bf16 v[208:211], a[124:127], v[48:51], v[208:211]\n"
"ds_read_b64_tr_b16 v[118:119], v183 offset:54856\n"
"v_max3_f32 v170, v170, v124, v125\n"
"v_mfma_f32_16x16x32_bf16 v[204:207], a[128:131], v[48:51], v[204:207]\n"
"v_max3_f32 v171, v171, v126, v127\n"
"v_max_f32 v170, v170, v171\n"
"v_mfma_f32_16x16x32_bf16 v[224:227], a[132:135], v[52:55], v[224:227]\n"
"v_mov_b32 v171, v170\n"
"s_mov_b64 exec, s[46:47]\n"
"v_add_u32 v183, 0xc0, v183\n"
"s_mov_b64 exec, -1\n"
"v_mfma_f32_16x16x32_bf16 v[208:211], a[136:139], v[52:55], v[208:211]\n"
"v_permlane16_swap_b32 v170, v171\n"
"v_mfma_f32_16x16x32_bf16 v[204:207], a[140:143], v[52:55], v[204:207]\n"
"v_max_f32 v170, v170, v171\n"
"v_mov_b32 v171, v170\n"
"v_mfma_f32_16x16x32_bf16 v[212:215], a[144:147], v[32:35], 0\n"
"s_add_u32 m0, s79, 142464\n"
"v_permlane32_swap_b32 v170, v171\n"
"v_mfma_f32_16x16x32_bf16 v[216:219], a[148:151], v[32:35], 0\n"
"buffer_load_dwordx4 v192, s[16:19], s43 offen offset:1024 lds\n"
"v_max_f32 v170, v170, v171\n"
"v_mfma_f32_16x16x32_bf16 v[228:231], a[152:155], v[32:35], 0\n"
"ds_read_b128 v[32:35], v188 offset:6144\n"
"v_mul_f32 v170, 0xbe553b95, v170\n"
"v_mfma_f32_16x16x32_bf16 v[212:215], a[156:159], v[36:39], v[212:215]\n"
"v_fmamk_f32 v128, v128, 0x3e553b95, v170\n"
"v_fmamk_f32 v129, v129, 0x3e553b95, v170\n"
"v_mfma_f32_16x16x32_bf16 v[216:219], a[160:163], v[36:39], v[216:219]\n"
"v_fmamk_f32 v130, v130, 0x3e553b95, v170\n"
"v_fmamk_f32 v131, v131, 0x3e553b95, v170\n"
"v_mfma_f32_16x16x32_bf16 v[228:231], a[164:167], v[36:39], v[228:231]\n"
"ds_read_b128 v[36:39], v189 offset:6144\n"
"v_fmamk_f32 v132, v132, 0x3e553b95, v170\n"
"v_mfma_f32_16x16x32_bf16 v[212:215], a[168:171], v[40:43], v[212:215]\n"
"v_fmamk_f32 v133, v133, 0x3e553b95, v170\n"
"v_fmamk_f32 v134, v134, 0x3e553b95, v170\n"
"v_mfma_f32_16x16x32_bf16 v[216:219], a[172:175], v[40:43], v[216:219]\n"
"v_fmamk_f32 v135, v135, 0x3e553b95, v170\n"
"v_mov_b64 v[106:107], 0\n"
"v_mfma_f32_16x16x32_bf16 v[228:231], a[176:179], v[40:43], v[228:231]\n"
"buffer_store_dwordx2 v[68:69], v193, s[28:31], s4 offen offset:1024 sc1\n"
"v_mfma_f32_16x16x32_bf16 v[212:215], a[180:183], v[44:47], v[212:215]\n"
"v_exp_f32 v128, v128\n"
"v_mfma_f32_16x16x32_bf16 v[216:219], a[184:187], v[44:47], v[216:219]\n"
"v_exp_f32 v129, v129\n"
"v_mfma_f32_16x16x32_bf16 v[228:231], a[188:191], v[44:47], v[228:231]\n"
"s_add_u32 m0, s79, 142464\n"
"v_exp_f32 v130, v130\n"
"v_mfma_f32_16x16x32_bf16 v[212:215], a[192:195], v[48:51], v[212:215]\n"
"buffer_load_dwordx4 v192, s[16:19], s43 offen offset:2048 lds\n"
"v_cvt_pk_bf16_f32 v82, v128, v130\n"
"v_mfma_f32_16x16x32_bf16 v[216:219], a[196:199], v[48:51], v[216:219]\n"
"s_waitcnt vmcnt(12)\n"
"v_exp_f32 v131, v131\n"
"v_mfma_f32_16x16x32_bf16 v[228:231], a[200:203], v[48:51], v[228:231]\n"
"s_add_u32 s43, s80, 12288\n"
"ds_read_b128 v[40:43], v188 offset:7168\n"
"ds_read_b128 v[44:47], v189 offset:7168\n"
"v_mfma_f32_16x16x32_bf16 v[212:215], a[204:207], v[52:55], v[212:215]\n"
"v_exp_f32 v132, v132\n"
"v_mfma_f32_16x16x32_bf16 v[216:219], a[208:211], v[52:55], v[216:219]\n"
"v_exp_f32 v133, v133\n"
"v_mfma_f32_16x16x32_bf16 v[228:231], a[212:215], v[52:55], v[228:231]\n"
"v_exp_f32 v134, v134\n"
"v_exp_f32 v135, v135\n"
"v_cvt_pk_bf16_f32 v83, v129, v131\n"
"v_cvt_pk_bf16_f32 v84, v132, v134\n"
"v_cvt_pk_bf16_f32 v85, v133, v135\n"
"v_mov_b64 v[108:109], 0\n"
"v_fmamk_f32 v120, v120, 0x3e553b95, v170\n"
"v_smfmac_f32_16x16x64_bf16 v[252:255], v[82:85], v[136:143], v66\n"
"s_waitcnt lgkmcnt(4)\n"
"ds_read_b64_tr_b16 v[128:129], v182 offset:128\n"
"v_smfmac_f32_16x16x64_bf16 v[98:101], v[82:85], v[244:251], v66\n"
"ds_read_b64_tr_b16 v[130:131], v183 offset:128\n"
"v_smfmac_f32_16x16x64_bf16 v[106:109], v[82:85], v[112:119], v66\n"
"s_waitcnt lgkmcnt(4)\n"
"ds_read_b64_tr_b16 v[132:133], v182 offset:896\n"
"v_mfma_f32_16x16x32_bf16 v[82:85], v[172:175], v[82:85], 0\n"
"s_add_u32 m0, s34, 4096\n"
"ds_read_b64_tr_b16 v[134:135], v183 offset:896\n"
"buffer_load_dwordx4 v191, s[8:11], s0 offen lds\n"
"s_add_u32 s0, s0, s33\n"
"ds_read_b64_tr_b16 v[136:137], v182 offset:136\n"
"ds_read_b64_tr_b16 v[138:139], v183 offset:136\n"
"v_fmamk_f32 v121, v121, 0x3e553b95, v170\n"
"v_fmamk_f32 v122, v122, 0x3e553b95, v170\n"
"ds_read_b64_tr_b16 v[140:141], v182 offset:904\n"
"v_fmamk_f32 v123, v123, 0x3e553b95, v170\n"
"ds_read_b64_tr_b16 v[142:143], v183 offset:904\n"
"v_fmamk_f32 v124, v124, 0x3e553b95, v170\n"
"ds_read_b64_tr_b16 v[244:245], v182 offset:72\n"
"v_fmamk_f32 v125, v125, 0x3e553b95, v170\n"
"ds_read_b64_tr_b16 v[246:247], v183 offset:72\n"
"v_fmamk_f32 v126, v126, 0x3e553b95, v170\n"
"ds_read_b64_tr_b16 v[248:249], v182 offset:840\n"
"v_fmamk_f32 v127, v127, 0x3e553b95, v170\n"
"ds_read_b64_tr_b16 v[250:251], v183 offset:840\n"
"v_exp_f32 v120, v120\n"
"v_exp_f32 v121, v121\n"
"v_exp_f32 v122, v122\n"
"v_exp_f32 v123, v123\n"
"v_exp_f32 v124, v124\n"
"s_add_u32 m0, s34, 2048\n"
"v_exp_f32 v125, v125\n"
"buffer_load_dwordx4 v190, s[8:11], s1 offen lds\n"
"s_waitcnt vmcnt(12)\n"
"ds_read_b128 v[48:51], v188 offset:8192\n"
"ds_read_b128 v[52:55], v189 offset:8192\n"
"v_exp_f32 v126, v126\n"
"v_exp_f32 v127, v127\n"
"v_cvt_pk_bf16_f32 v90, v120, v122\n"
"v_cvt_pk_bf16_f32 v91, v121, v123\n"
"v_cvt_pk_bf16_f32 v92, v124, v126\n"
"v_cvt_pk_bf16_f32 v93, v125, v127\n"
"v_cvt_pk_bf16_f32 v224, v224, v225\n"
"v_cvt_pk_bf16_f32 v225, v226, v227\n"
"v_cvt_pk_bf16_f32 v226, v208, v209\n"
"v_cvt_pk_bf16_f32 v227, v210, v211\n"
"v_mfma_f32_16x16x32_bf16 v[208:211], a[0:3], v[32:35], 0\n"
"s_waitcnt lgkmcnt(2)\n"
"ds_read_b64_tr_b16 a[244:245], v236 offset:8\n"
"v_cvt_pk_bf16_f32 v222, v204, v205\n"
"v_smfmac_f32_16x16x64_bf16 v[252:255], v[90:93], v[128:135], v66\n"
"ds_write_b128 v186, v[224:227] offset:33888\n"
"v_smfmac_f32_16x16x64_bf16 v[98:101], v[90:93], v[136:143], v66\n"
"v_cvt_pk_bf16_f32 v223, v206, v207\n"
"v_smfmac_f32_16x16x64_bf16 v[106:109], v[90:93], v[244:251], v66\n"
"ds_write_b128 v186, v[220:223] offset:33824\n"
"v_mfma_f32_16x16x32_bf16 v[90:93], v[172:175], v[90:93], v[82:85]\n"
"s_add_u32 m0, s34, 5120\n"
"v_cvt_pk_bf16_f32 v148, v212, v213\n"
"v_cvt_pk_bf16_f32 v252, v252, v253\n"
"v_mfma_f32_16x16x32_bf16 v[204:207], a[4:7], v[32:35], 0\n"
"s_waitcnt vmcnt(10)\n"
"buffer_load_dwordx4 v191, s[8:11], s1 offen lds\n"
"v_cvt_pk_bf16_f32 v253, v254, v255\n"
"v_mfma_f32_16x16x32_bf16 v[220:223], a[8:11], v[32:35], 0\n"
"s_add_u32 s1, s1, s33\n"
"v_add3_u32 v200, v200, s50, -2\n"
"v_cmp_lt_u32 vcc, 64, v200\n"
"v_mfma_f32_16x16x32_bf16 v[208:211], a[12:15], v[36:39], v[208:211]\n"
"v_rcp_f32 v171, v90\n"
"v_mfma_f32_16x16x32_bf16 v[204:207], a[16:19], v[36:39], v[204:207]\n"
"s_nop 1\n"
"s_cbranch_vccnz .Lp1%=\n"
".Lr1%=:\n"
"v_mfma_f32_16x16x32_bf16 v[220:223], a[20:23], v[36:39], v[220:223]\n"
"buffer_load_dword v201, off, s[28:31], s41 sc1\n"
"v_cvt_pk_bf16_f32 v171, v171, v171\n"
"v_mfma_f32_16x16x32_bf16 v[208:211], a[24:27], v[40:43], v[208:211]\n"
"buffer_load_dwordx4 v[56:59], v192, s[28:31], s38 offen sc1\n"
"v_and_b32 v64, v171, v176\n"
"v_mfma_f32_16x16x32_bf16 v[204:207], a[28:31], v[40:43], v[204:207]\n"
"v_and_b32 v65, v171, v177\n"
"v_cvt_pk_bf16_f32 v98, v98, v99\n"
"v_mfma_f32_16x16x32_bf16 v[220:223], a[32:35], v[40:43], v[220:223]\n"
"v_cvt_pk_bf16_f32 v99, v100, v101\n"
"v_cvt_pk_bf16_f32 v106, v106, v107\n"
"v_mfma_f32_16x16x16_bf16 v[252:255], v[252:253], v[64:65], 0\n"
"v_cvt_pk_bf16_f32 v107, v108, v109\n"
"v_cvt_pk_bf16_f32 v149, v214, v215\n"
"v_mfma_f32_16x16x16_bf16 v[98:101], v[98:99], v[64:65], 0\n"
"buffer_load_dwordx2 v[60:61], v193, s[28:31], s38 offen offset:1024 sc1\n"
"ds_read_b64_tr_b16 a[246:247], v236 offset:6152\n"
"v_mfma_f32_16x16x16_bf16 v[106:109], v[106:107], v[64:65], 0\n"
"s_add_u32 s38, s38, 6144\n"
"ds_read_b64_tr_b16 a[248:249], v235\n"
"v_cvt_pk_bf16_f32 v252, v252, v253\n"
"v_mfma_f32_16x16x32_bf16 v[208:211], a[36:39], v[44:47], v[208:211]\n"
"s_waitcnt lgkmcnt(6)\n"
"v_cvt_pk_bf16_f32 v253, v254, v255\n"
"v_cvt_pk_bf16_f32 v254, v98, v99\n"
"v_mfma_f32_16x16x32_bf16 v[204:207], a[40:43], v[44:47], v[204:207]\n"
"s_waitcnt lgkmcnt(5)\n"
"v_cvt_pk_bf16_f32 v255, v100, v101\n"
"v_cvt_pk_bf16_f32 v106, v106, v107\n"
"v_mfma_f32_16x16x32_bf16 v[220:223], a[44:47], v[44:47], v[220:223]\n"
"s_add_u32 m0, s34, 6144\n"
"buffer_store_dwordx4 v[252:255], v192, s[28:31], s37 offen sc1\n"
"v_mfma_f32_16x16x32_bf16 v[208:211], a[48:51], v[48:51], v[208:211]\n"
"v_cvt_pk_bf16_f32 v107, v108, v109\n"
"v_cvt_pk_bf16_f32 v150, v216, v217\n"
"v_mfma_f32_16x16x32_bf16 v[204:207], a[52:55], v[48:51], v[204:207]\n"
"ds_read_b64_tr_b16 a[250:251], v235 offset:6144\n"
"v_cvt_pk_bf16_f32 v151, v218, v219\n"
"v_mfma_f32_16x16x32_bf16 v[220:223], a[56:59], v[48:51], v[220:223]\n"
"buffer_load_dwordx4 v190, s[8:11], s32 offen lds\n"
"v_cvt_pk_bf16_f32 v162, v228, v229\n"
"v_mfma_f32_16x16x32_bf16 v[208:211], a[60:63], v[52:55], v[208:211]\n"
"v_cvt_pk_bf16_f32 v163, v230, v231\n"
"v_mov_b64 v[252:253], 0\n"
"v_mfma_f32_16x16x32_bf16 v[204:207], a[64:67], v[52:55], v[204:207]\n"
"ds_read_b128 v[112:115], v180 offset:27008\n"
"ds_read_b128 v[120:123], v181 offset:27008\n"
"v_mfma_f32_16x16x32_bf16 v[220:223], a[68:71], v[52:55], v[220:223]\n"
"ds_read_b128 v[116:119], v178\n"
"v_mov_b64 v[254:255], 0\n"
"v_mfma_f32_16x16x32_bf16 v[224:227], a[72:75], v[32:35], 0\n"
"buffer_store_dwordx2 v[106:107], v193, s[28:31], s37 offen offset:1024 sc1\n"
"v_mfma_f32_16x16x32_bf16 v[212:215], a[76:79], v[32:35], 0\n"
"s_add_u32 s37, s37, 6144\n"
"ds_read_b128 v[124:127], v179\n"
"ds_read_b64 v[82:83], v180 offset:27072\n"
"v_mfma_f32_16x16x32_bf16 v[216:219], a[80:83], v[32:35], 0\n"
"s_add_u32 s4, s37, 49152\n"
"ds_read_b64 v[84:85], v181 offset:27072\n"
"v_cvt_pk_bf16_f32 v208, v208, v209\n"
"v_mfma_f32_16x16x32_bf16 v[224:227], a[84:87], v[36:39], v[224:227]\n"
"ds_read_b64 v[90:91], v178 offset:64\n"
"v_cvt_pk_bf16_f32 v209, v210, v211\n"
"v_mfma_f32_16x16x32_bf16 v[212:215], a[88:91], v[36:39], v[212:215]\n"
"ds_read_b64 v[92:93], v179 offset:64\n"
"v_cvt_pk_bf16_f32 v210, v204, v205\n"
"v_mfma_f32_16x16x32_bf16 v[216:219], a[92:95], v[36:39], v[216:219]\n"
"s_waitcnt lgkmcnt(7)\n"
"ds_read_b128 v[128:131], v178 offset:13504\n"
"v_cvt_pk_bf16_f32 v211, v206, v207\n"
"v_mfma_f32_16x16x32_bf16 v[224:227], a[96:99], v[40:43], v[224:227]\n"
"s_waitcnt lgkmcnt(7)\n"
"ds_read_b128 v[136:139], v179 offset:13504\n"
"v_cvt_pk_bf16_f32 v220, v220, v221\n"
"v_mfma_f32_16x16x32_bf16 v[112:115], v[112:115], v[152:155], 0\n"
"s_waitcnt lgkmcnt(7)\n"
"ds_read_b64 v[98:99], v178 offset:13568\n"
"v_cvt_pk_bf16_f32 v221, v222, v223\n"
"v_mfma_f32_16x16x32_bf16 v[120:123], v[120:123], v[152:155], 0\n"
"s_add_u32 m0, s34, 9216\n"
"ds_read_b64 v[100:101], v179 offset:13568\n"
"buffer_load_dwordx4 v191, s[8:11], s32 offen lds\n"
"v_mfma_f32_16x16x32_bf16 v[116:119], v[116:119], v[152:155], 0\n"
"s_waitcnt lgkmcnt(8)\n"
"ds_read_b64_tr_b16 v[244:245], v184 offset:128\n"
"s_mov_b64 exec, s[44:45]\n"
"v_add_u32 v181, 0xc0, v181\n"
"s_mov_b64 exec, -1\n"
"v_mfma_f32_16x16x32_bf16 v[124:127], v[124:127], v[152:155], 0\n"
"s_waitcnt lgkmcnt(7)\n"
"ds_read_b64_tr_b16 v[246:247], v185 offset:128\n"
"ds_read_b64_tr_b16 v[248:249], v184 offset:896\n"
"v_mfma_f32_16x16x32_bf16 v[112:115], v[80:83], v[164:167], v[112:115]\n"
"s_waitcnt lgkmcnt(8)\n"
"ds_read_b64_tr_b16 v[250:251], v185 offset:896\n"
"s_add_u32 s32, s32, s33\n"
"v_mfma_f32_16x16x32_bf16 v[120:123], v[84:87], v[166:169], v[120:123]\n"
"s_waitcnt lgkmcnt(8)\n"
"v_mfma_f32_16x16x32_bf16 v[116:119], v[88:91], v[164:167], v[116:119]\n"
"s_waitcnt lgkmcnt(7)\n"
"v_mfma_f32_16x16x32_bf16 v[124:127], v[92:95], v[166:169], v[124:127]\n"
"s_waitcnt lgkmcnt(6)\n"
"v_mfma_f32_16x16x32_bf16 v[128:131], v[128:131], v[152:155], 0\n"
"s_waitcnt lgkmcnt(5)\n"
"s_nop 0\n"
"v_max_f32 v112, v112, v120\n"
"v_mfma_f32_16x16x32_bf16 v[136:139], v[136:139], v[152:155], 0\n"
"s_waitcnt lgkmcnt(4)\n"
"v_max_f32 v113, v113, v121\n"
"v_max_f32 v114, v114, v122\n"
"v_mfma_f32_16x16x32_bf16 v[128:131], v[96:99], v[164:167], v[128:131]\n"
"ds_read_b64_tr_b16 v[120:121], v184 offset:136\n"
"v_max_f32 v115, v115, v123\n"
"v_mfma_f32_16x16x32_bf16 v[136:139], v[100:103], v[166:169], v[136:139]\n"
"s_add_u32 m0, s34, 7168\n"
"ds_read_b64_tr_b16 v[122:123], v185 offset:136\n"
"buffer_load_dwordx4 v190, s[8:11], s0 offen lds\n"
"v_mfma_f32_16x16x32_bf16 v[212:215], a[100:103], v[40:43], v[212:215]\n"
"v_max_f32 v116, v116, v124\n"
"v_max_f32 v117, v117, v125\n"
"v_mfma_f32_16x16x32_bf16 v[216:219], a[104:107], v[40:43], v[216:219]\n"
"ds_read_b64_tr_b16 v[124:125], v184 offset:904\n"
"v_max_f32 v118, v118, v126\n"
"v_mfma_f32_16x16x32_bf16 v[224:227], a[108:111], v[44:47], v[224:227]\n"
"v_max_f32 v119, v119, v127\n"
"v_max_f32 v170, v112, v113\n"
"v_mfma_f32_16x16x32_bf16 v[212:215], a[112:115], v[44:47], v[212:215]\n"
"ds_read_b64_tr_b16 v[126:127], v185 offset:904\n"
"v_max_f32 v171, v114, v115\n"
"v_mfma_f32_16x16x32_bf16 v[216:219], a[116:119], v[44:47], v[216:219]\n"
"v_max3_f32 v170, v170, v116, v117\n"
"v_max3_f32 v171, v171, v118, v119\n"
"v_mfma_f32_16x16x32_bf16 v[224:227], a[120:123], v[48:51], v[224:227]\n"
"v_max_f32 v128, v128, v136\n"
"v_max_f32 v129, v129, v137\n"
"v_mfma_f32_16x16x32_bf16 v[212:215], a[124:127], v[48:51], v[212:215]\n"
"ds_read_b64_tr_b16 v[136:137], v184 offset:72\n"
"v_max_f32 v130, v130, v138\n"
"v_mfma_f32_16x16x32_bf16 v[216:219], a[128:131], v[48:51], v[216:219]\n"
"v_max_f32 v131, v131, v139\n"
"v_max3_f32 v170, v170, v128, v129\n"
"v_mfma_f32_16x16x32_bf16 v[224:227], a[132:135], v[52:55], v[224:227]\n"
"ds_read_b64_tr_b16 v[138:139], v185 offset:72\n"
"v_max3_f32 v171, v171, v130, v131\n"
"v_mfma_f32_16x16x32_bf16 v[212:215], a[136:139], v[52:55], v[212:215]\n"
"s_add_u32 m0, s34, 10240\n"
"v_mov_b64 v[98:99], 0\n"
"v_mov_b64 v[100:101], 0\n"
"v_mfma_f32_16x16x32_bf16 v[216:219], a[140:143], v[52:55], v[216:219]\n"
"buffer_load_dwordx4 v191, s[8:11], s0 offen lds\n"
"v_cvt_pk_bf16_f32 v224, v224, v225\n"
"v_mfma_f32_16x16x32_bf16 v[228:231], a[144:147], v[32:35], 0\n"
"s_add_u32 s0, s0, s33\n"
"v_cvt_pk_bf16_f32 v225, v226, v227\n"
"v_cvt_pk_bf16_f32 v226, v212, v213\n"
"v_mfma_f32_16x16x32_bf16 v[204:207], a[148:151], v[32:35], 0\n"
"v_cvt_pk_bf16_f32 v227, v214, v215\n"
"v_cvt_pk_bf16_f32 v222, v216, v217\n"
"v_mfma_f32_16x16x32_bf16 v[228:231], a[156:159], v[36:39], v[228:231]\n"
"v_cvt_pk_bf16_f32 v223, v218, v219\n"
"v_mfma_f32_16x16x32_bf16 v[204:207], a[160:163], v[36:39], v[204:207]\n"
"v_mfma_f32_16x16x32_bf16 v[228:231], a[168:171], v[40:43], v[228:231]\n"
"s_waitcnt vmcnt(12)\n"
"v_mfma_f32_16x16x32_bf16 v[204:207], a[172:175], v[40:43], v[204:207]\n"
"s_barrier\n"
"v_mfma_f32_16x16x32_bf16 v[228:231], a[180:183], v[44:47], v[228:231]\n"
"ds_read_b128 v[132:135], v178 offset:27008\n"
"ds_read_b128 v[140:143], v179 offset:27008\n"
"v_mfma_f32_16x16x32_bf16 v[204:207], a[184:187], v[44:47], v[204:207]\n"
"ds_read_b64 v[106:107], v178 offset:27072\n"
"v_mfma_f32_16x16x32_bf16 v[228:231], a[192:195], v[48:51], v[228:231]\n"
"ds_read_b64 v[108:109], v179 offset:27072\n"
"v_mfma_f32_16x16x32_bf16 v[204:207], a[196:199], v[48:51], v[204:207]\n"
"s_add_u32 m0, s79, 130176\n"
"ds_write_b128 v186, v[208:211] offset:47264\n"
"v_mfma_f32_16x16x32_bf16 v[208:211], a[152:155], v[32:35], 0\n"
"buffer_load_dwordx4 v192, s[16:19], s43 offen lds\n"
"v_mfma_f32_16x16x32_bf16 v[208:211], a[164:167], v[36:39], v[208:211]\n"
"ds_read_b128 v[32:35], v188\n"
"v_mfma_f32_16x16x32_bf16 v[208:211], a[176:179], v[40:43], v[208:211]\n"
"ds_read_b128 v[36:39], v189\n"
"ds_read_b64_tr_b16 a[252:253], v236 offset:12288\n"
"v_mfma_f32_16x16x32_bf16 v[208:211], a[188:191], v[44:47], v[208:211]\n"
"s_waitcnt lgkmcnt(7)\n"
"v_mfma_f32_16x16x32_bf16 v[132:135], v[132:135], v[152:155], 0\n"
"s_waitcnt lgkmcnt(6)\n"
"ds_read_b64_tr_b16 a[254:255], v236 offset:18432\n"
"buffer_store_dwordx4 v[68:71], v192, s[28:31], s4 offen sc1\n"
"v_mfma_f32_16x16x32_bf16 v[140:143], v[140:143], v[152:155], 0\n"
"s_waitcnt lgkmcnt(5)\n"
"ds_write_b128 v186, v[220:223] offset:47328\n"
"v_mfma_f32_16x16x32_bf16 v[132:135], v[104:107], v[164:167], v[132:135]\n"
"v_mov_b64 v[106:107], 0\n"
"v_mfma_f32_16x16x32_bf16 v[140:143], v[108:111], v[166:169], v[140:143]\n"
"s_waitcnt lgkmcnt(4)\n"
"v_mov_b64 v[108:109], 0\n"
"v_mfma_f32_16x16x32_bf16 v[208:211], a[200:203], v[48:51], v[208:211]\n"
"s_waitcnt lgkmcnt(3)\n"
"ds_read_b64_tr_b16 v[0:1], v236 offset:12296\n"
"s_add_u32 m0, s79, 130176\n"
"v_mfma_f32_16x16x32_bf16 v[228:231], a[204:207], v[52:55], v[228:231]\n"
"buffer_load_dwordx4 v192, s[16:19], s43 offen offset:1024 lds\n"
"v_max_f32 v132, v132, v140\n"
"v_mfma_f32_16x16x32_bf16 v[204:207], a[208:211], v[52:55], v[204:207]\n"
"v_max_f32 v133, v133, v141\n"
"v_max_f32 v134, v134, v142\n"
"v_mfma_f32_16x16x32_bf16 v[208:211], a[212:215], v[52:55], v[208:211]\n"
".Lic3%=:\n"
"s_cbranch_execz .Lib0%=\n"
"ds_read_b64_tr_b16 v[140:141], v184 offset:840\n"
"v_max_f32 v135, v135, v143\n"
"v_mfma_f32_16x16x32_bf16 v[212:215], a[0:3], v[32:35], 0\n"
"ds_read_b64_tr_b16 v[142:143], v185 offset:840\n"
"v_max3_f32 v170, v170, v132, v133\n"
"v_mfma_f32_16x16x32_bf16 v[216:219], a[4:7], v[32:35], 0\n"
"s_waitcnt vmcnt(14)\n"
"v_max3_f32 v171, v171, v134, v135\n"
"v_max_f32 v170, v170, v171\n"
"v_mfma_f32_16x16x32_bf16 v[220:223], a[8:11], v[32:35], 0\n"
"ds_read_b128 v[40:43], v188 offset:1024\n"
"ds_read_b128 v[44:47], v189 offset:1024\n"
"v_mfma_f32_16x16x32_bf16 v[212:215], a[12:15], v[36:39], v[212:215]\n"
"v_mov_b32 v171, v170\n"
"v_cvt_pk_bf16_f32 v152, v228, v229\n"
"v_mfma_f32_16x16x32_bf16 v[216:219], a[16:19], v[36:39], v[216:219]\n"
"v_permlane16_swap_b32 v170, v171\n"
"v_mfma_f32_16x16x32_bf16 v[220:223], a[20:23], v[36:39], v[220:223]\n"
"v_max_f32 v170, v170, v171\n"
"v_mov_b32 v171, v170\n"
"ds_write_b128 v186, v[224:227] offset:47392\n"
"v_mfma_f32_16x16x32_bf16 v[224:227], a[72:75], v[32:35], 0\n"
"s_add_u32 m0, s79, 130176\n"
"v_permlane32_swap_b32 v170, v171\n"
"v_mfma_f32_16x16x32_bf16 v[224:227], a[84:87], v[36:39], v[224:227]\n"
"buffer_load_dwordx4 v192, s[16:19], s43 offen offset:2048 lds\n"
"v_max_f32 v170, v170, v171\n"
"v_mul_f32 v170, 0xbe553b95, v170\n"
"v_fmamk_f32 v112, v112, 0x3e553b95, v170\n"
"v_fmamk_f32 v113, v113, 0x3e553b95, v170\n"
"v_fmamk_f32 v114, v114, 0x3e553b95, v170\n"
"v_fmamk_f32 v115, v115, 0x3e553b95, v170\n"
"v_fmamk_f32 v116, v116, 0x3e553b95, v170\n"
"v_fmamk_f32 v117, v117, 0x3e553b95, v170\n"
"v_fmamk_f32 v118, v118, 0x3e553b95, v170\n"
"v_fmamk_f32 v119, v119, 0x3e553b95, v170\n"
"v_exp_f32 v112, v112\n"
"v_exp_f32 v113, v113\n"
"v_exp_f32 v114, v114\n"
"v_exp_f32 v115, v115\n"
"v_exp_f32 v116, v116\n"
"v_exp_f32 v117, v117\n"
"v_exp_f32 v118, v118\n"
"v_exp_f32 v119, v119\n"
"v_cvt_pk_bf16_f32 v82, v112, v114\n"
"v_cvt_pk_bf16_f32 v83, v113, v115\n"
"v_cvt_pk_bf16_f32 v84, v116, v118\n"
"v_cvt_pk_bf16_f32 v85, v117, v119\n"
"v_fmamk_f32 v128, v128, 0x3e553b95, v170\n"
"v_fmamk_f32 v129, v129, 0x3e553b95, v170\n"
"v_smfmac_f32_16x16x64_bf16 v[252:255], v[82:85], v[244:251], v66\n"
"s_waitcnt lgkmcnt(2)\n"
"ds_read_b64_tr_b16 v[112:113], v182 offset:13632\n"
"v_smfmac_f32_16x16x64_bf16 v[98:101], v[82:85], v[120:127], v66\n"
"s_waitcnt lgkmcnt(2)\n"
"ds_read_b64_tr_b16 v[114:115], v183 offset:13632\n"
"v_smfmac_f32_16x16x64_bf16 v[106:109], v[82:85], v[136:143], v66\n"
"s_add_u32 m0, s34, 8192\n"
"s_nop 0\n"
"buffer_load_dwordx4 v190, s[8:11], s1 offen lds\n"
"v_mfma_f32_16x16x32_bf16 v[82:85], v[172:175], v[82:85], 0\n"
"s_add_u32 s43, s80, 49152\n"
"ds_read_b64_tr_b16 v[116:117], v182 offset:14400\n"
"ds_read_b64_tr_b16 v[118:119], v183 offset:14400\n"
"v_mfma_f32_16x16x32_bf16 v[212:215], a[24:27], v[40:43], v[212:215]\n"
"ds_read_b64_tr_b16 v[244:245], v182 offset:13640\n"
"v_fmamk_f32 v130, v130, 0x3e553b95, v170\n"
"v_mfma_f32_16x16x32_bf16 v[216:219], a[28:31], v[40:43], v[216:219]\n"
"ds_read_b64_tr_b16 v[246:247], v183 offset:13640\n"
"v_fmamk_f32 v131, v131, 0x3e553b95, v170\n"
"v_mfma_f32_16x16x32_bf16 v[220:223], a[32:35], v[40:43], v[220:223]\n"
"s_waitcnt vmcnt(12)\n"
"ds_read_b64_tr_b16 v[248:249], v182 offset:14408\n"
"v_fmamk_f32 v132, v132, 0x3e553b95, v170\n"
"v_mfma_f32_16x16x32_bf16 v[212:215], a[36:39], v[44:47], v[212:215]\n"
"v_readfirstlane_b32 s6, v201\n"
"v_fmamk_f32 v133, v133, 0x3e553b95, v170\n"
"v_mfma_f32_16x16x32_bf16 v[216:219], a[40:43], v[44:47], v[216:219]\n"
"s_and_b32 s6, s6, 0xffffff00\n"
"v_or_b32 v201, 4, s6\n"
"v_fmamk_f32 v134, v134, 0x3e553b95, v170\n"
"v_mfma_f32_16x16x32_bf16 v[220:223], a[44:47], v[44:47], v[220:223]\n"
"s_waitcnt vmcnt(11)\n"
"s_mov_b64 exec, 1\n"
"buffer_store_dword v201, off, s[28:31], s40 sc1\n"
"s_mov_b64 exec, -1\n"
"v_mfma_f32_16x16x32_bf16 v[224:227], a[96:99], v[40:43], v[224:227]\n"
"ds_read_b128 v[48:51], v188 offset:2048\n"
"ds_read_b128 v[52:55], v189 offset:2048\n"
"v_mfma_f32_16x16x32_bf16 v[224:227], a[108:111], v[44:47], v[224:227]\n"
"ds_read_b64_tr_b16 v[250:251], v183 offset:14408\n"
"s_add_u32 m0, s34, 11264\n"
"ds_read_b64_tr_b16 v[120:121], v182 offset:13576\n"
"buffer_load_dwordx4 v191, s[8:11], s1 offen lds\n"
"s_add_u32 s1, s1, s33\n"
"v_fmamk_f32 v135, v135, 0x3e553b95, v170\n"
"ds_read_b64_tr_b16 v[122:123], v183 offset:13576\n"
"v_exp_f32 v128, v128\n"
"ds_read_b64_tr_b16 v[124:125], v182 offset:14344\n"
"ds_read_b64_tr_b16 v[126:127], v183 offset:14344\n"
"v_exp_f32 v129, v129\n"
"v_exp_f32 v130, v130\n"
"v_exp_f32 v131, v131\n"
"v_exp_f32 v132, v132\n"
"v_exp_f32 v133, v133\n"
"v_exp_f32 v134, v134\n"
"v_cvt_pk_bf16_f32 v90, v128, v130\n"
"v_exp_f32 v135, v135\n"
"v_cvt_pk_bf16_f32 v91, v129, v131\n"
"v_cvt_pk_bf16_f32 v92, v132, v134\n"
"v_cvt_pk_bf16_f32 v93, v133, v135\n"
"v_cvt_pk_bf16_f32 v153, v230, v231\n"
"v_mfma_f32_16x16x32_bf16 v[228:231], a[76:79], v[32:35], 0\n"
"s_waitcnt lgkmcnt(4)\n"
"ds_read_b64_tr_b16 v[2:3], v236 offset:18440\n"
"v_cvt_pk_bf16_f32 v154, v204, v205\n"
"v_smfmac_f32_16x16x64_bf16 v[252:255], v[90:93], v[112:119], v66\n"
"ds_read_b64_tr_b16 v[4:5], v235 offset:12288\n"
"v_smfmac_f32_16x16x64_bf16 v[98:101], v[90:93], v[244:251], v66\n"
"s_waitcnt vmcnt(10)\n"
"v_cvt_pk_bf16_f32 v155, v206, v207\n"
"v_mfma_f32_16x16x32_bf16 v[212:215], a[48:51], v[48:51], v[212:215]\n"
"s_add_u32 m0, s34, 0\n"
"v_or_b32 v201, 5, s6\n"
"v_cvt_pk_bf16_f32 v166, v208, v209\n"
"v_mfma_f32_16x16x32_bf16 v[216:219], a[52:55], v[48:51], v[216:219]\n"
"s_waitcnt lgkmcnt(2)\n"
"buffer_load_dwordx4 v190, s[8:11], s32 offen lds\n"
"v_cvt_pk_bf16_f32 v252, v252, v253\n"
"v_smfmac_f32_16x16x64_bf16 v[106:109], v[90:93], v[120:127], v66\n"
"v_cvt_pk_bf16_f32 v253, v254, v255\n"
"v_mfma_f32_16x16x32_bf16 v[90:93], v[172:175], v[90:93], v[82:85]\n"
"v_cvt_pk_bf16_f32 v98, v98, v99\n"
"v_cvt_pk_bf16_f32 v99, v100, v101\n"
"v_mfma_f32_16x16x32_bf16 v[220:223], a[56:59], v[48:51], v[220:223]\n"
"s_mov_b64 exec, 1\n"
"buffer_store_dword v201, off, s[28:31], s40 sc1\n"
"s_mov_b64 exec, -1\n"
"v_mfma_f32_16x16x32_bf16 v[212:215], a[60:63], v[52:55], v[212:215]\n"
"v_cvt_pk_bf16_f32 v106, v106, v107\n"
"v_cvt_pk_bf16_f32 v107, v108, v109\n"
"v_mfma_f32_16x16x32_bf16 v[216:219], a[64:67], v[52:55], v[216:219]\n"
"v_rcp_f32 v171, v90\n"
"v_mfma_f32_16x16x32_bf16 v[220:223], a[68:71], v[52:55], v[220:223]\n"
"v_cvt_pk_bf16_f32 v171, v171, v171\n"
"v_and_b32 v64, v171, v176\n"
"v_mfma_f32_16x16x32_bf16 v[204:207], a[80:83], v[32:35], 0\n"
"v_and_b32 v65, v171, v177\n"
"v_cvt_pk_bf16_f32 v167, v210, v211\n"
"v_mfma_f32_16x16x32_bf16 v[228:231], a[88:91], v[36:39], v[228:231]\n"
"ds_read_b64_tr_b16 v[6:7], v235 offset:18432\n"
"v_cvt_pk_bf16_f32 v212, v212, v213\n"
"v_mfma_f32_16x16x16_bf16 v[252:255], v[252:253], v[64:65], 0\n"
"ds_read_b128 v[136:139], v178\n"
"v_cvt_pk_bf16_f32 v213, v214, v215\n"
"v_mfma_f32_16x16x16_bf16 v[98:101], v[98:99], v[64:65], 0\n"
"s_add_u32 m0, s34, 3072\n"
"ds_read_b128 v[128:131], v179\n"
"buffer_load_dwordx4 v191, s[8:11], s32 offen lds\n"
"v_mfma_f32_16x16x16_bf16 v[106:109], v[106:107], v[64:65], 0\n"
"s_add_u32 s32, s32, s33\n"
"v_cvt_pk_bf16_f32 v252, v252, v253\n"
"v_cvt_pk_bf16_f32 v253, v254, v255\n"
"v_mfma_f32_16x16x32_bf16 v[204:207], a[92:95], v[36:39], v[204:207]\n"
"v_cvt_pk_bf16_f32 v254, v98, v99\n"
"v_cvt_pk_bf16_f32 v255, v100, v101\n"
"v_mfma_f32_16x16x32_bf16 v[228:231], a[100:103], v[40:43], v[228:231]\n"
"ds_read_b64 v[82:83], v178 offset:64\n"
"buffer_store_dwordx4 v[252:255], v192, s[28:31], s37 offen sc1\n"
"v_mfma_f32_16x16x32_bf16 v[204:207], a[104:107], v[40:43], v[204:207]\n"
"v_cvt_pk_bf16_f32 v106, v106, v107\n"
"v_cvt_pk_bf16_f32 v107, v108, v109\n"
"v_mfma_f32_16x16x32_bf16 v[228:231], a[112:115], v[44:47], v[228:231]\n"
"ds_read_b64 v[84:85], v179 offset:64\n"
"ds_read_b128 v[140:143], v178 offset:13504\n"
"v_mfma_f32_16x16x32_bf16 v[204:207], a[116:119], v[44:47], v[204:207]\n"
"ds_read_b128 v[132:135], v179 offset:13504\n"
"buffer_store_dwordx2 v[106:107], v193, s[28:31], s37 offen offset:1024 sc1\n"
"v_mfma_f32_16x16x32_bf16 v[224:227], a[120:123], v[48:51], v[224:227]\n"
"s_waitcnt lgkmcnt(4)\n"
"ds_read_b64 v[90:91], v178 offset:13568\n"
"ds_read_b64 v[92:93], v179 offset:13568\n"
"v_mfma_f32_16x16x32_bf16 v[136:139], v[136:139], v[144:147], 0\n"
"s_add_u32 s37, s37, 6144\n"
"ds_read_b128 v[112:115], v178 offset:27008\n"
"v_cvt_pk_bf16_f32 v214, v216, v217\n"
"v_mfma_f32_16x16x32_bf16 v[128:131], v[128:131], v[144:147], 0\n"
"s_waitcnt lgkmcnt(6)\n"
"ds_write_b128 v202, v[56:59] offset:55296\n"
"v_mfma_f32_16x16x32_bf16 v[136:139], v[80:83], v[156:159], v[136:139]\n"
"s_add_u32 m0, s34, 1024\n"
"buffer_load_dwordx4 v[56:59], v192, s[28:31], s38 offen sc1\n"
"v_cvt_pk_bf16_f32 v215, v218, v219\n"
"v_mfma_f32_16x16x32_bf16 v[228:231], a[124:127], v[48:51], v[228:231]\n"
"s_waitcnt lgkmcnt(6)\n"
"v_cvt_pk_bf16_f32 v220, v220, v221\n"
"v_cvt_pk_bf16_f32 v221, v222, v223\n"
"v_mfma_f32_16x16x32_bf16 v[204:207], a[128:131], v[48:51], v[204:207]\n"
"s_waitcnt lgkmcnt(5)\n"
"ds_read_b128 v[244:247], v179 offset:27008\n"
"ds_read_b64 v[98:99], v178 offset:27072\n"
"v_mfma_f32_16x16x32_bf16 v[128:131], v[84:87], v[158:161], v[128:131]\n"
"s_waitcnt lgkmcnt(6)\n"
"buffer_load_dwordx4 v190, s[8:11], s0 offen lds\n"
"ds_read_b64 v[100:101], v179 offset:27072\n"
"v_mfma_f32_16x16x32_bf16 v[140:143], v[140:143], v[144:147], 0\n"
"s_waitcnt lgkmcnt(6)\n"
"ds_read_b64_tr_b16 v[120:121], v182 offset:128\n"
"v_mov_b64 v[252:253], 0\n"
"v_mfma_f32_16x16x32_bf16 v[132:135], v[132:135], v[144:147], 0\n"
"s_waitcnt lgkmcnt(6)\n"
"ds_read_b64_tr_b16 v[122:123], v183 offset:128\n"
"v_max_f32 v136, v136, v128\n"
"v_mfma_f32_16x16x32_bf16 v[140:143], v[88:91], v[156:159], v[140:143]\n"
"s_waitcnt lgkmcnt(6)\n"
"ds_read_b64_tr_b16 v[124:125], v182 offset:896\n"
"v_max_f32 v137, v137, v129\n"
"v_mfma_f32_16x16x32_bf16 v[132:135], v[92:95], v[158:161], v[132:135]\n"
"s_add_u32 s4, s37, 43008\n"
"ds_read_b64_tr_b16 v[126:127], v183 offset:896\n"
"v_max_f32 v138, v138, v130\n"
"v_mfma_f32_16x16x32_bf16 v[112:115], v[112:115], v[144:147], 0\n"
"ds_write_b64 v203, v[60:61] offset:55296\n"
"v_mfma_f32_16x16x32_bf16 v[224:227], a[132:135], v[52:55], v[224:227]\n"
"s_waitcnt lgkmcnt(7)\n"
"buffer_load_dwordx2 v[60:61], v193, s[28:31], s38 offen offset:1024 sc1\n"
"v_max_f32 v139, v139, v131\n"
"v_mfma_f32_16x16x32_bf16 v[228:231], a[136:139], v[52:55], v[228:231]\n"
"s_waitcnt lgkmcnt(6)\n"
"ds_read_b64_tr_b16 v[128:129], v182 offset:136\n"
"ds_read_b64_tr_b16 v[130:131], v183 offset:136\n"
"v_mfma_f32_16x16x32_bf16 v[244:247], v[244:247], v[144:147], 0\n"
"s_waitcnt lgkmcnt(7)\n"
"v_max_f32 v170, v136, v137\n"
"v_max_f32 v171, v138, v139\n"
"v_mfma_f32_16x16x32_bf16 v[112:115], v[96:99], v[156:159], v[112:115]\n"
"s_add_u32 s38, s38, 6144\n"
"v_max_f32 v140, v140, v132\n"
"v_max_f32 v141, v141, v133\n"
"v_mfma_f32_16x16x32_bf16 v[244:247], v[100:103], v[158:161], v[244:247]\n"
"s_add_u32 m0, s34, 4096\n"
"ds_read_b64_tr_b16 v[132:133], v182 offset:904\n"
"buffer_load_dwordx4 v191, s[8:11], s0 offen lds\n"
"v_mfma_f32_16x16x32_bf16 v[204:207], a[140:143], v[52:55], v[204:207]\n"
"s_add_u32 s0, s0, s33\n"
"v_max_f32 v142, v142, v134\n"
"v_max_f32 v143, v143, v135\n"
"v_mfma_f32_16x16x32_bf16 v[208:211], a[144:147], v[32:35], 0\n"
"ds_read_b64_tr_b16 v[134:135], v183 offset:904\n"
"v_max3_f32 v170, v170, v140, v141\n"
"v_mfma_f32_16x16x32_bf16 v[216:219], a[148:151], v[32:35], 0\n"
"v_max3_f32 v171, v171, v142, v143\n"
"v_max_f32 v112, v112, v244\n"
"v_mfma_f32_16x16x32_bf16 v[208:211], a[156:159], v[36:39], v[208:211]\n"
"v_cvt_pk_bf16_f32 v224, v224, v225\n"
"v_max_f32 v113, v113, v245\n"
"v_mfma_f32_16x16x32_bf16 v[216:219], a[160:163], v[36:39], v[216:219]\n"
"ds_read_b64_tr_b16 v[244:245], v182 offset:72\n"
"v_max_f32 v114, v114, v246\n"
"v_mfma_f32_16x16x32_bf16 v[208:211], a[168:171], v[40:43], v[208:211]\n"
"s_waitcnt vmcnt(12)\n"
"v_cvt_pk_bf16_f32 v225, v226, v227\n"
"v_max_f32 v115, v115, v247\n"
"v_mfma_f32_16x16x32_bf16 v[216:219], a[172:175], v[40:43], v[216:219]\n"
"s_barrier\n"
"v_mfma_f32_16x16x32_bf16 v[208:211], a[180:183], v[44:47], v[208:211]\n"
"ds_write_b128 v186, v[212:215] offset:60768\n"
"v_mfma_f32_16x16x32_bf16 v[212:215], a[152:155], v[32:35], 0\n"
"v_cvt_pk_bf16_f32 v226, v228, v229\n"
"v_max3_f32 v170, v170, v112, v113\n"
"v_mfma_f32_16x16x32_bf16 v[212:215], a[164:167], v[36:39], v[212:215]\n"
"s_add_u32 m0, s79, 142464\n"
"v_max3_f32 v171, v171, v114, v115\n"
"v_cvt_pk_bf16_f32 v227, v230, v231\n"
"v_mfma_f32_16x16x32_bf16 v[212:215], a[176:179], v[40:43], v[212:215]\n"
"buffer_load_dwordx4 v192, s[16:19], s43 offen lds\n"
"ds_read_b128 v[32:35], v188 offset:6144\n"
"v_mfma_f32_16x16x32_bf16 v[216:219], a[184:187], v[44:47], v[216:219]\n"
"ds_read_b128 v[36:39], v189 offset:6144\n"
"ds_read_b128 v[40:43], v188 offset:7168\n"
"v_mfma_f32_16x16x32_bf16 v[212:215], a[188:191], v[44:47], v[212:215]\n"
"ds_read_b128 v[44:47], v189 offset:7168\n"
"v_cvt_pk_bf16_f32 v222, v204, v205\n"
"v_mfma_f32_16x16x32_bf16 v[208:211], a[192:195], v[48:51], v[208:211]\n"
"ds_read_b128 v[116:119], v180\n"
"v_cvt_pk_bf16_f32 v223, v206, v207\n"
"v_mfma_f32_16x16x32_bf16 v[216:219], a[196:199], v[48:51], v[216:219]\n"
"ds_read_b128 v[248:251], v181\n"
"v_mov_b64 v[254:255], 0\n"
"v_mfma_f32_16x16x32_bf16 v[212:215], a[200:203], v[48:51], v[212:215]\n"
"ds_read_b64 v[106:107], v180 offset:64\n"
"v_mov_b64 v[98:99], 0\n"
"v_mfma_f32_16x16x32_bf16 v[208:211], a[204:207], v[52:55], v[208:211]\n"
"ds_read_b64 v[108:109], v181 offset:64\n"
"v_mov_b64 v[100:101], 0\n"
"v_mfma_f32_16x16x32_bf16 v[216:219], a[208:211], v[52:55], v[216:219]\n"
"ds_read_b64_tr_b16 v[8:9], v236\n"
"s_mov_b64 exec, s[46:47]\n"
"v_add_u32 v185, 0xc0, v185\n"
"s_mov_b64 exec, -1\n"
"v_mfma_f32_16x16x32_bf16 v[212:215], a[212:215], v[52:55], v[212:215]\n"
"s_waitcnt lgkmcnt(8)\n"
"ds_read_b64_tr_b16 v[10:11], v236 offset:6144\n"
"v_mfma_f32_16x16x32_bf16 v[228:231], a[0:3], v[32:35], 0\n"
"s_waitcnt lgkmcnt(8)\n"
"s_add_u32 m0, s79, 142464\n"
"v_mfma_f32_16x16x32_bf16 v[204:207], a[4:7], v[32:35], 0\n"
"buffer_load_dwordx4 v192, s[16:19], s43 offen offset:1024 lds\n"
"ds_write_b128 v186, v[220:223] offset:60832\n"
"v_mfma_f32_16x16x32_bf16 v[220:223], a[8:11], v[32:35], 0\n"
"s_waitcnt lgkmcnt(5)\n"
"v_mfma_f32_16x16x32_bf16 v[116:119], v[116:119], v[144:147], 0\n"
"s_waitcnt lgkmcnt(4)\n"
"ds_read_b64_tr_b16 v[246:247], v183 offset:72\n"
"v_mfma_f32_16x16x32_bf16 v[248:251], v[248:251], v[144:147], 0\n"
"s_waitcnt lgkmcnt(4)\n"
"ds_write_b128 v186, v[224:227] offset:60896\n"
"v_mfma_f32_16x16x32_bf16 v[116:119], v[104:107], v[156:159], v[116:119]\n"
"v_mov_b64 v[106:107], 0\n"
"v_cvt_pk_bf16_f32 v144, v208, v209\n"
"v_mfma_f32_16x16x32_bf16 v[248:251], v[108:111], v[158:161], v[248:251]\n"
"v_mov_b64 v[108:109], 0\n"
"v_cvt_pk_bf16_f32 v145, v210, v211\n"
"v_mfma_f32_16x16x32_bf16 v[228:231], a[12:15], v[36:39], v[228:231]\n"
"ds_read_b64_tr_b16 v[12:13], v236 offset:8\n"
"v_cvt_pk_bf16_f32 v146, v216, v217\n"
"v_mfma_f32_16x16x32_bf16 v[204:207], a[16:19], v[36:39], v[204:207]\n"
"ds_read_b64_tr_b16 v[14:15], v236 offset:6152\n"
"v_cvt_pk_bf16_f32 v147, v218, v219\n"
"v_mfma_f32_16x16x32_bf16 v[220:223], a[20:23], v[36:39], v[220:223]\n"
"s_add_u32 m0, s79, 142464\n"
"v_max_f32 v116, v116, v248\n"
"v_max_f32 v117, v117, v249\n"
"v_mfma_f32_16x16x32_bf16 v[228:231], a[24:27], v[40:43], v[228:231]\n"
"buffer_load_dwordx4 v192, s[16:19], s43 offen offset:2048 lds\n"
"ds_read_b64_tr_b16 v[248:249], v182 offset:840\n"
"v_mfma_f32_16x16x32_bf16 v[204:207], a[28:31], v[40:43], v[204:207]\n"
"v_max_f32 v118, v118, v250\n"
"v_max_f32 v119, v119, v251\n"
"v_mfma_f32_16x16x32_bf16 v[220:223], a[32:35], v[40:43], v[220:223]\n"
"ds_read_b64_tr_b16 v[250:251], v183 offset:840\n"
"v_max3_f32 v170, v170, v116, v117\n"
"v_mfma_f32_16x16x32_bf16 v[228:231], a[36:39], v[44:47], v[228:231]\n"
"v_max3_f32 v171, v171, v118, v119\n"
"v_max_f32 v170, v170, v171\n"
"v_mfma_f32_16x16x32_bf16 v[204:207], a[40:43], v[44:47], v[204:207]\n"
"ds_read_b64_tr_b16 v[16:17], v235\n"
"buffer_store_dwordx2 v[68:69], v193, s[28:31], s4 offen offset:1024 sc1\n"
"v_mfma_f32_16x16x32_bf16 v[220:223], a[44:47], v[44:47], v[220:223]\n"
"ds_read_b64_tr_b16 v[18:19], v235 offset:6144\n"
"v_mov_b32 v171, v170\n"
"v_mfma_f32_16x16x32_bf16 v[224:227], a[72:75], v[32:35], 0\n"
"s_nop 0\n"
"v_permlane16_swap_b32 v170, v171\n"
"v_mfma_f32_16x16x32_bf16 v[208:211], a[76:79], v[32:35], 0\n"
"v_max_f32 v170, v170, v171\n"
"v_mov_b32 v171, v170\n"
"v_mfma_f32_16x16x32_bf16 v[216:219], a[80:83], v[32:35], 0\n"
"s_nop 0\n"
"v_permlane32_swap_b32 v170, v171\n"
"v_mfma_f32_16x16x32_bf16 v[224:227], a[84:87], v[36:39], v[224:227]\n"
"buffer_load_dword v200, off, s[28:31], s39 sc1\n"
"v_max_f32 v170, v170, v171\n"
"v_mfma_f32_16x16x32_bf16 v[208:211], a[88:91], v[36:39], v[208:211]\n"
"s_add_u32 m0, s34, 2048\n"
"v_mul_f32 v170, 0xbe553b95, v170\n"
"v_fmamk_f32 v136, v136, 0x3e553b95, v170\n"
"v_mfma_f32_16x16x32_bf16 v[216:219], a[92:95], v[36:39], v[216:219]\n"
"buffer_load_dwordx4 v190, s[8:11], s1 offen lds\n"
"v_fmamk_f32 v137, v137, 0x3e553b95, v170\n"
"v_mfma_f32_16x16x32_bf16 v[224:227], a[96:99], v[40:43], v[224:227]\n"
"v_fmamk_f32 v138, v138, 0x3e553b95, v170\n"
"v_fmamk_f32 v139, v139, 0x3e553b95, v170\n"
"v_mfma_f32_16x16x32_bf16 v[208:211], a[100:103], v[40:43], v[208:211]\n"
"v_exp_f32 v136, v136\n"
"v_mfma_f32_16x16x32_bf16 v[216:219], a[104:107], v[40:43], v[216:219]\n"
"v_exp_f32 v137, v137\n"
"v_mfma_f32_16x16x32_bf16 v[224:227], a[108:111], v[44:47], v[224:227]\n"
"v_exp_f32 v138, v138\n"
"v_mfma_f32_16x16x32_bf16 v[208:211], a[112:115], v[44:47], v[208:211]\n"
"v_exp_f32 v139, v139\n"
"v_mfma_f32_16x16x32_bf16 v[216:219], a[116:119], v[44:47], v[216:219]\n"
"v_cvt_pk_bf16_f32 v82, v136, v138\n"
"v_cvt_pk_bf16_f32 v83, v137, v139\n"
"v_fmamk_f32 v140, v140, 0x3e553b95, v170\n"
"v_fmamk_f32 v141, v141, 0x3e553b95, v170\n"
"v_fmamk_f32 v142, v142, 0x3e553b95, v170\n"
"s_waitcnt vmcnt(15)\n"
"ds_read_b128 v[48:51], v188 offset:8192\n"
"ds_read_b128 v[52:55], v189 offset:8192\n"
"v_fmamk_f32 v143, v143, 0x3e553b95, v170\n"
"v_exp_f32 v140, v140\n"
"v_exp_f32 v141, v141\n"
"s_add_u32 m0, s34, 5120\n"
"v_exp_f32 v142, v142\n"
"buffer_load_dwordx4 v191, s[8:11], s1 offen lds\n"
"s_add_u32 s1, s1, s33\n"
"v_exp_f32 v143, v143\n"
"v_cvt_pk_bf16_f32 v84, v140, v142\n"
"v_cvt_pk_bf16_f32 v85, v141, v143\n"
"v_cvt_pk_bf16_f32 v158, v212, v213\n"
"v_fmamk_f32 v112, v112, 0x3e553b95, v170\n"
"v_smfmac_f32_16x16x64_bf16 v[252:255], v[82:85], v[120:127], v66\n"
"s_waitcnt lgkmcnt(4)\n"
"ds_read_b64_tr_b16 v[136:137], v182 offset:27136\n"
"v_smfmac_f32_16x16x64_bf16 v[98:101], v[82:85], v[128:135], v66\n"
"ds_read_b64_tr_b16 v[138:139], v183 offset:27136\n"
"v_smfmac_f32_16x16x64_bf16 v[106:109], v[82:85], v[244:251], v66\n"
"s_waitcnt lgkmcnt(3)\n"
"ds_read_b64_tr_b16 v[140:141], v182 offset:27904\n"
"v_mfma_f32_16x16x32_bf16 v[82:85], v[172:175], v[82:85], 0\n"
"ds_read_b64_tr_b16 v[142:143], v183 offset:27904\n"
"ds_read_b64_tr_b16 v[120:121], v182 offset:27144\n"
"v_mfma_f32_16x16x32_bf16 v[228:231], a[48:51], v[48:51], v[228:231]\n"
"s_waitcnt lgkmcnt(5)\n"
"ds_read_b64_tr_b16 v[122:123], v183 offset:27144\n"
"v_fmamk_f32 v113, v113, 0x3e553b95, v170\n"
"v_mfma_f32_16x16x32_bf16 v[204:207], a[52:55], v[48:51], v[204:207]\n"
"ds_read_b64_tr_b16 v[124:125], v182 offset:27912\n"
"v_cvt_pk_bf16_f32 v159, v214, v215\n"
"v_mfma_f32_16x16x32_bf16 v[220:223], a[56:59], v[48:51], v[220:223]\n"
"ds_read_b64_tr_b16 v[126:127], v183 offset:27912\n"
"v_fmamk_f32 v114, v114, 0x3e553b95, v170\n"
"v_mfma_f32_16x16x32_bf16 v[228:231], a[60:63], v[52:55], v[228:231]\n"
"ds_read_b64_tr_b16 v[128:129], v182 offset:27080\n"
"v_fmamk_f32 v115, v115, 0x3e553b95, v170\n"
"v_mfma_f32_16x16x32_bf16 v[204:207], a[64:67], v[52:55], v[204:207]\n"
"s_add_u32 m0, s34, 6144\n"
"ds_read_b64_tr_b16 v[130:131], v183 offset:27080\n"
"buffer_load_dwordx4 v190, s[8:11], s32 offen lds\n"
"v_mfma_f32_16x16x32_bf16 v[220:223], a[68:71], v[52:55], v[220:223]\n"
"ds_read_b64_tr_b16 v[132:133], v182 offset:27848\n"
"v_fmamk_f32 v116, v116, 0x3e553b95, v170\n"
"v_mfma_f32_16x16x32_bf16 v[224:227], a[120:123], v[48:51], v[224:227]\n"
"ds_read_b64_tr_b16 v[134:135], v183 offset:27848\n"
"v_fmamk_f32 v117, v117, 0x3e553b95, v170\n"
"v_mfma_f32_16x16x32_bf16 v[208:211], a[124:127], v[48:51], v[208:211]\n"
"v_exp_f32 v112, v112\n"
"v_mfma_f32_16x16x32_bf16 v[216:219], a[128:131], v[48:51], v[216:219]\n"
"v_exp_f32 v113, v113\n"
"v_mfma_f32_16x16x32_bf16 v[224:227], a[132:135], v[52:55], v[224:227]\n"
"ds_read_b128 v[244:247], v178 offset:13504\n"
"v_fmamk_f32 v118, v118, 0x3e553b95, v170\n"
"v_mfma_f32_16x16x32_bf16 v[208:211], a[136:139], v[52:55], v[208:211]\n"
"v_exp_f32 v114, v114\n"
"v_mfma_f32_16x16x32_bf16 v[216:219], a[140:143], v[52:55], v[216:219]\n"
"v_exp_f32 v115, v115\n"
"v_mfma_f32_16x16x32_bf16 v[212:215], a[144:147], v[32:35], 0\n"
"v_cvt_pk_bf16_f32 v90, v112, v114\n"
"v_cvt_pk_bf16_f32 v91, v113, v115\n"
"v_mfma_f32_16x16x32_bf16 v[212:215], a[156:159], v[36:39], v[212:215]\n"
"ds_read_b128 v[112:115], v179 offset:13504\n"
"v_fmamk_f32 v119, v119, 0x3e553b95, v170\n"
"v_mfma_f32_16x16x32_bf16 v[212:215], a[168:171], v[40:43], v[212:215]\n"
"s_add_u32 m0, s34, 9216\n"
"v_exp_f32 v116, v116\n"
"v_mfma_f32_16x16x32_bf16 v[212:215], a[180:183], v[44:47], v[212:215]\n"
"buffer_load_dwordx4 v191, s[8:11], s32 offen lds\n"
"ds_read_b128 v[248:251], v178 offset:27008\n"
"v_mfma_f32_16x16x32_bf16 v[212:215], a[192:195], v[48:51], v[212:215]\n"
"s_add_u32 s32, s32, s33\n"
"v_exp_f32 v117, v117\n"
"v_mfma_f32_16x16x32_bf16 v[212:215], a[204:207], v[52:55], v[212:215]\n"
"s_waitcnt lgkmcnt(2)\n"
"v_exp_f32 v118, v118\n"
"v_mfma_f32_16x16x32_bf16 v[244:247], v[244:247], v[148:151], 0\n"
"v_exp_f32 v119, v119\n"
"v_cvt_pk_bf16_f32 v92, v116, v118\n"
"v_cvt_pk_bf16_f32 v93, v117, v119\n"
"v_cvt_pk_bf16_f32 v228, v228, v229\n"
"v_cvt_pk_bf16_f32 v229, v230, v231\n"
"v_smfmac_f32_16x16x64_bf16 v[252:255], v[90:93], v[136:143], v66\n"
"ds_read_b128 v[116:119], v179 offset:27008\n"
"v_smfmac_f32_16x16x64_bf16 v[98:101], v[90:93], v[120:127], v66\n"
"s_waitcnt lgkmcnt(2)\n"
"ds_read_b128 v[136:139], v180\n"
"v_smfmac_f32_16x16x64_bf16 v[106:109], v[90:93], v[128:135], v66\n"
"ds_read_b128 v[120:123], v181\n"
"v_mfma_f32_16x16x32_bf16 v[90:93], v[172:175], v[90:93], v[82:85]\n"
"s_waitcnt lgkmcnt(3)\n"
"v_cvt_pk_bf16_f32 v252, v252, v253\n"
"v_cvt_pk_bf16_f32 v253, v254, v255\n"
"v_mfma_f32_16x16x32_bf16 v[112:115], v[112:115], v[148:151], 0\n"
"ds_read_b64 v[82:83], v178 offset:13568\n"
"ds_read_b64 v[84:85], v179 offset:13568\n"
"v_mfma_f32_16x16x32_bf16 v[248:251], v[248:251], v[148:151], 0\n"
"s_add_u32 m0, s34, 7168\n"
"v_rcp_f32 v171, v90\n"
"buffer_load_dwordx4 v190, s[8:11], s0 offen lds\n"
"ds_read_b64 v[90:91], v178 offset:27072\n"
"ds_read_b64 v[92:93], v179 offset:27072\n"
"v_cvt_pk_bf16_f32 v171, v171, v171\n"
"v_and_b32 v64, v171, v176\n"
"v_and_b32 v65, v171, v177\n"
"v_cvt_pk_bf16_f32 v98, v98, v99\n"
"v_cvt_pk_bf16_f32 v99, v100, v101\n"
"v_mfma_f32_16x16x16_bf16 v[252:255], v[252:253], v[64:65], 0\n"
"s_waitcnt lgkmcnt(5)\n"
"v_cvt_pk_bf16_f32 v106, v106, v107\n"
"v_cvt_pk_bf16_f32 v107, v108, v109\n"
"v_mfma_f32_16x16x16_bf16 v[98:101], v[98:99], v[64:65], 0\n"
"s_waitcnt lgkmcnt(4)\n"
"ds_read_b64_tr_b16 v[128:129], v182 offset:13632\n"
"ds_read_b64_tr_b16 v[130:131], v183 offset:13632\n"
"v_mfma_f32_16x16x16_bf16 v[106:109], v[106:107], v[64:65], 0\n"
"v_cvt_pk_bf16_f32 v252, v252, v253\n"
"v_cvt_pk_bf16_f32 v253, v254, v255\n"
"v_mfma_f32_16x16x32_bf16 v[116:119], v[116:119], v[148:151], 0\n"
"s_waitcnt lgkmcnt(5)\n"
"v_cvt_pk_bf16_f32 v254, v98, v99\n"
"v_cvt_pk_bf16_f32 v255, v100, v101\n"
"v_mfma_f32_16x16x32_bf16 v[244:247], v[80:83], v[160:163], v[244:247]\n"
"s_waitcnt lgkmcnt(4)\n"
"buffer_store_dwordx4 v[252:255], v192, s[28:31], s37 offen sc1\n"
"v_mfma_f32_16x16x32_bf16 v[112:115], v[84:87], v[162:165], v[112:115]\n"
"s_waitcnt lgkmcnt(3)\n"
"ds_read_b64 v[98:99], v180 offset:64\n"
"ds_read_b64 v[100:101], v181 offset:64\n"
"v_mfma_f32_16x16x32_bf16 v[248:251], v[88:91], v[160:163], v[248:251]\n"
"s_waitcnt lgkmcnt(4)\n"
"v_cvt_pk_bf16_f32 v106, v106, v107\n"
"v_cvt_pk_bf16_f32 v107, v108, v109\n"
"v_mfma_f32_16x16x32_bf16 v[116:119], v[92:95], v[162:165], v[116:119]\n"
"s_add_u32 m0, s34, 10240\n"
"buffer_store_dwordx2 v[106:107], v193, s[28:31], s37 offen offset:1024 sc1\n"
"v_mfma_f32_16x16x32_bf16 v[136:139], v[136:139], v[148:151], 0\n"
"s_add_u32 s37, s37, 6144\n"
"ds_read_b64_tr_b16 v[132:133], v182 offset:14400\n"
"v_cvt_pk_bf16_f32 v230, v204, v205\n"
"v_mfma_f32_16x16x32_bf16 v[120:123], v[120:123], v[148:151], 0\n"
"s_waitcnt vmcnt(9)\n"
"s_barrier\n"
"ds_read_b128 v[68:71], v192 offset:55296\n"
"buffer_load_dwordx4 v191, s[8:11], s0 offen lds\n"
"ds_read_b128 v[72:75], v192 offset:56320\n"
"ds_read_b128 v[76:79], v192 offset:57344\n"
"v_cvt_pk_bf16_f32 v231, v206, v207\n"
"v_mfma_f32_16x16x32_bf16 v[204:207], a[148:151], v[32:35], 0\n"
"s_add_u32 s0, s0, s33\n"
"ds_write_b128 v187, v[228:231]\n"
"v_mfma_f32_16x16x32_bf16 v[228:231], a[152:155], v[32:35], 0\n"
"s_waitcnt lgkmcnt(5)\n"
"v_cvt_pk_bf16_f32 v220, v220, v221\n"
"v_cvt_pk_bf16_f32 v221, v222, v223\n"
"v_mfma_f32_16x16x32_bf16 v[136:139], v[96:99], v[160:163], v[136:139]\n"
"v_max_f32 v244, v244, v112\n"
"v_max_f32 v245, v245, v113\n"
"v_mfma_f32_16x16x32_bf16 v[120:123], v[100:103], v[162:165], v[120:123]\n"
"ds_read_b128 v[32:35], v188\n"
"ds_write_b128 v202, v[56:59] offset:36864\n"
"v_mfma_f32_16x16x32_bf16 v[204:207], a[160:163], v[36:39], v[204:207]\n"
"s_waitcnt lgkmcnt(5)\n"
"v_max_f32 v246, v246, v114\n"
"v_max_f32 v247, v247, v115\n"
"v_mfma_f32_16x16x32_bf16 v[240:243], a[224:227], v[68:71], 0\n"
"s_waitcnt lgkmcnt(4)\n"
"v_max_f32 v170, v244, v245\n"
"v_max_f32 v171, v246, v247\n"
"v_mfma_f32_16x16x32_bf16 v[240:243], a[236:239], v[72:75], v[240:243]\n"
"s_waitcnt lgkmcnt(3)\n"
"ds_read_b128 v[140:143], v180 offset:13504\n"
"ds_read_b128 v[124:127], v181 offset:13504\n"
"v_mfma_f32_16x16x32_bf16 v[228:231], a[164:167], v[36:39], v[228:231]\n"
"s_add_u32 m0, s34, 8192\n"
"ds_read_b128 v[36:39], v189\n"
"buffer_load_dwordx4 v190, s[8:11], s1 offen lds\n"
"v_mfma_f32_16x16x32_bf16 v[204:207], a[172:175], v[40:43], v[204:207]\n"
"ds_read_b64 v[106:107], v180 offset:13568\n"
"v_max_f32 v248, v248, v116\n"
"v_mfma_f32_16x16x32_bf16 v[240:243], a[248:251], v[76:79], v[240:243]\n"
"ds_read_b64 v[108:109], v181 offset:13568\n"
"v_max_f32 v249, v249, v117\n"
"v_mfma_f32_16x16x32_bf16 v[228:231], a[176:179], v[40:43], v[228:231]\n"
"ds_read_b128 v[40:43], v188 offset:1024\n"
"v_max_f32 v250, v250, v118\n"
"v_mfma_f32_16x16x32_bf16 v[204:207], a[184:187], v[44:47], v[204:207]\n"
"ds_read_b64_tr_b16 v[20:21], v236 offset:12288\n"
"v_max_f32 v251, v251, v119\n"
"v_mfma_f32_16x16x32_bf16 v[228:231], a[188:191], v[44:47], v[228:231]\n"
"v_max3_f32 v170, v170, v248, v249\n"
"v_max3_f32 v171, v171, v250, v251\n"
"v_mfma_f32_16x16x32_bf16 v[204:207], a[196:199], v[48:51], v[204:207]\n"
"ds_read_b128 v[44:47], v189 offset:1024\n"
"v_max_f32 v136, v136, v120\n"
"v_mfma_f32_16x16x32_bf16 v[228:231], a[200:203], v[48:51], v[228:231]\n"
"s_waitcnt lgkmcnt(7)\n"
"ds_read_b64_tr_b16 v[22:23], v236 offset:18432\n"
"v_cvt_pk_bf16_f32 v224, v224, v225\n"
"v_mfma_f32_16x16x32_bf16 v[140:143], v[140:143], v[148:151], 0\n"
"s_waitcnt lgkmcnt(7)\n"
"v_max_f32 v137, v137, v121\n"
".Lic4%=:\n"
"s_cbranch_execz .Lib1%=\n"
"v_max_f32 v138, v138, v122\n"
"v_mfma_f32_16x16x32_bf16 v[124:127], v[124:127], v[148:151], 0\n"
"ds_read_b64_tr_b16 v[24:25], v236 offset:12296\n"
"v_cvt_pk_bf16_f32 v225, v226, v227\n"
"v_mfma_f32_16x16x32_bf16 v[204:207], a[208:211], v[52:55], v[204:207]\n"
"s_waitcnt lgkmcnt(6)\n"
"v_max_f32 v139, v139, v123\n"
"v_cvt_pk_bf16_f32 v226, v208, v209\n"
"v_mfma_f32_16x16x32_bf16 v[140:143], v[104:107], v[160:163], v[140:143]\n"
"s_waitcnt lgkmcnt(5)\n"
"ds_read_b64_tr_b16 v[26:27], v236 offset:18440\n"
"v_max3_f32 v170, v170, v136, v137\n"
"v_mfma_f32_16x16x32_bf16 v[124:127], v[108:111], v[162:165], v[124:127]\n"
"s_add_u32 m0, s34, 11264\n"
"v_max3_f32 v171, v171, v138, v139\n"
"v_cvt_pk_bf16_f32 v227, v210, v211\n"
"v_mfma_f32_16x16x32_bf16 v[236:239], a[220:223], v[68:71], 0\n"
"buffer_load_dwordx4 v191, s[8:11], s1 offen lds\n"
"ds_read_b64_tr_b16 v[134:135], v183 offset:14400\n"
"v_mfma_f32_16x16x32_bf16 v[236:239], a[232:235], v[72:75], v[236:239]\n"
"s_add_u32 s1, s1, s33\n"
"ds_read_b64_tr_b16 v[28:29], v235 offset:12288\n"
"v_max_f32 v140, v140, v124\n"
"v_mfma_f32_16x16x32_bf16 v[236:239], a[244:247], v[76:79], v[236:239]\n"
"v_cvt_pk_bf16_f32 v222, v216, v217\n"
"v_max_f32 v141, v141, v125\n"
"v_mfma_f32_16x16x32_bf16 v[228:231], a[212:215], v[52:55], v[228:231]\n"
"ds_read_b64_tr_b16 v[30:31], v235 offset:18432\n"
"v_max_f32 v142, v142, v126\n"
"v_mfma_f32_16x16x32_bf16 v[232:235], a[216:219], v[68:71], 0\n"
"v_cvt_pk_bf16_f32 v223, v218, v219\n"
"v_max_f32 v143, v143, v127\n"
"v_mfma_f32_16x16x32_bf16 v[232:235], a[228:231], v[72:75], v[232:235]\n"
"ds_read_b128 v[68:71], v192 offset:58368\n"
"v_max3_f32 v170, v170, v140, v141\n"
"v_mfma_f32_16x16x32_bf16 v[232:235], a[240:243], v[76:79], v[232:235]\n"
"s_waitcnt vmcnt(10)\n"
"ds_read_b128 v[72:75], v192 offset:59392\n"
"v_max3_f32 v171, v171, v142, v143\n"
"v_mfma_f32_16x16x32_bf16 v[208:211], a[0:3], v[32:35], 0\n"
"v_add3_u32 v200, v200, s50, -4\n"
"v_cmp_lt_u32 vcc, 64, v200\n"
"v_mfma_f32_16x16x32_bf16 v[216:219], a[4:7], v[32:35], 0\n"
"ds_read_b128 v[76:79], v192 offset:60416\n"
"ds_read_b64_tr_b16 v[112:113], v182 offset:13640\n"
"v_mfma_f32_16x16x32_bf16 v[208:211], a[12:15], v[36:39], v[208:211]\n"
"s_waitcnt lgkmcnt(12)\n"
"ds_read_b64_tr_b16 v[114:115], v183 offset:13640\n"
"v_mov_b64 v[252:253], 0\n"
"v_mfma_f32_16x16x32_bf16 v[216:219], a[16:19], v[36:39], v[216:219]\n"
"s_waitcnt lgkmcnt(11)\n"
"ds_read_b64_tr_b16 v[116:117], v182 offset:14408\n"
"v_mov_b64 v[254:255], 0\n"
"v_mfma_f32_16x16x32_bf16 v[208:211], a[24:27], v[40:43], v[208:211]\n"
"s_cbranch_vccnz .Lp3%=\n"
".Lr3%=:\n"
"v_mfma_f32_16x16x32_bf16 v[216:219], a[28:31], v[40:43], v[216:219]\n"
"s_add_u32 m0, s34, 0\n"
"buffer_load_dword v200, off, s[28:31], s39 sc1\n"
"ds_read_b64_tr_b16 v[118:119], v183 offset:14408\n"
"v_mfma_f32_16x16x32_bf16 v[208:211], a[36:39], v[44:47], v[208:211]\n"
"buffer_load_dwordx4 v190, s[8:11], s32 offen lds\n"
"ds_write_b64 v203, v[60:61] offset:36864\n"
"v_mfma_f32_16x16x32_bf16 v[216:219], a[40:43], v[44:47], v[216:219]\n"
"s_waitcnt lgkmcnt(7)\n"
"v_mov_b64 v[98:99], 0\n"
"v_mov_b64 v[100:101], 0\n"
"v_mfma_f32_16x16x32_bf16 v[232:235], a[252:255], v[68:71], v[232:235]\n"
"s_waitcnt lgkmcnt(6)\n"
"ds_write_b128 v187, v[220:223] offset:64\n"
"v_mfma_f32_16x16x32_bf16 v[236:239], v[0:3], v[68:71], v[236:239]\n"
"buffer_load_dwordx4 v[56:59], v192, s[28:31], s38 offen sc1\n"
"v_mov_b64 v[106:107], 0\n"
"v_mfma_f32_16x16x32_bf16 v[240:243], v[4:7], v[68:71], v[240:243]\n"
"v_mov_b64 v[108:109], 0\n"
"v_max_f32 v170, v170, v171\n"
"v_mfma_f32_16x16x32_bf16 v[232:235], v[8:11], v[72:75], v[232:235]\n"
"s_waitcnt lgkmcnt(6)\n"
"ds_read_b64_tr_b16 v[120:121], v182 offset:13576\n"
"ds_read_b64_tr_b16 v[122:123], v183 offset:13576\n"
"v_mfma_f32_16x16x32_bf16 v[240:243], v[16:19], v[72:75], v[240:243]\n"
"ds_read_b64_tr_b16 v[124:125], v182 offset:14344\n"
"buffer_load_dwordx2 v[60:61], v193, s[28:31], s38 offen offset:1024 sc1\n"
"v_mfma_f32_16x16x32_bf16 v[236:239], v[12:15], v[72:75], v[236:239]\n"
"s_add_u32 s38, s38, 6144\n"
"ds_read_b64_tr_b16 v[126:127], v183 offset:14344\n"
"v_mov_b32 v171, v170\n"
"v_mfma_f32_16x16x32_bf16 v[232:235], v[20:23], v[76:79], v[232:235]\n"
"ds_write_b128 v187, v[224:227] offset:128\n"
"v_mfma_f32_16x16x32_bf16 v[240:243], v[28:31], v[76:79], v[240:243]\n"
"s_add_u32 m0, s34, 3072\n"
"v_permlane16_swap_b32 v170, v171\n"
"v_mfma_f32_16x16x32_bf16 v[236:239], v[24:27], v[76:79], v[236:239]\n"
"buffer_load_dwordx4 v191, s[8:11], s32 offen lds\n"
"v_max_f32 v170, v170, v171\n"
"v_mfma_f32_16x16x32_bf16 v[220:223], a[8:11], v[32:35], 0\n"
"s_add_u32 s32, s32, s33\n"
"v_mov_b32 v171, v170\n"
"v_cvt_pk_bf16_f32 v232, v232, v233\n"
"v_mfma_f32_16x16x32_bf16 v[220:223], a[20:23], v[36:39], v[220:223]\n"
"v_permlane32_swap_b32 v170, v171\n"
"v_mfma_f32_16x16x32_bf16 v[220:223], a[32:35], v[40:43], v[220:223]\n"
"v_max_f32 v170, v170, v171\n"
"v_mul_f32 v170, 0xbe553b95, v170\n"
"v_mfma_f32_16x16x32_bf16 v[220:223], a[44:47], v[44:47], v[220:223]\n"
"v_fmamk_f32 v244, v244, 0x3e553b95, v170\n"
"v_fmamk_f32 v245, v245, 0x3e553b95, v170\n"
"v_mfma_f32_16x16x32_bf16 v[224:227], a[72:75], v[32:35], 0\n"
"s_waitcnt vmcnt(13)\n"
"v_fmamk_f32 v246, v246, 0x3e553b95, v170\n"
"v_fmamk_f32 v247, v247, 0x3e553b95, v170\n"
"v_mfma_f32_16x16x32_bf16 v[224:227], a[84:87], v[36:39], v[224:227]\n"
"ds_read_b128 v[48:51], v188 offset:2048\n"
"ds_read_b128 v[52:55], v189 offset:2048\n"
"v_mfma_f32_16x16x32_bf16 v[224:227], a[96:99], v[40:43], v[224:227]\n"
"v_exp_f32 v244, v244\n"
"v_mfma_f32_16x16x32_bf16 v[224:227], a[108:111], v[44:47], v[224:227]\n"
"v_exp_f32 v245, v245\n"
"v_exp_f32 v246, v246\n"
"v_exp_f32 v247, v247\n"
"s_add_u32 m0, s34, 1024\n"
"v_cvt_pk_bf16_f32 v82, v244, v246\n"
"buffer_load_dwordx4 v190, s[8:11], s0 offen lds\n"
"v_cvt_pk_bf16_f32 v83, v245, v247\n"
"v_fmamk_f32 v248, v248, 0x3e553b95, v170\n"
"v_fmamk_f32 v249, v249, 0x3e553b95, v170\n"
"v_fmamk_f32 v250, v250, 0x3e553b95, v170\n"
"v_fmamk_f32 v251, v251, 0x3e553b95, v170\n"
"v_exp_f32 v248, v248\n"
"v_exp_f32 v249, v249\n"
"v_exp_f32 v250, v250\n"
"v_exp_f32 v251, v251\n"
"v_cvt_pk_bf16_f32 v84, v248, v250\n"
"v_cvt_pk_bf16_f32 v85, v249, v251\n"
"v_cvt_pk_bf16_f32 v233, v234, v235\n"
"v_cvt_pk_bf16_f32 v240, v240, v241\n"
"v_smfmac_f32_16x16x64_bf16 v[252:255], v[82:85], v[128:135], v66\n"
"s_waitcnt lgkmcnt(1)\n"
"ds_read_b64_tr_b16 v[244:245], v182 offset:40640\n"
"v_smfmac_f32_16x16x64_bf16 v[98:101], v[82:85], v[112:119], v66\n"
"ds_read_b64_tr_b16 v[246:247], v183 offset:40640\n"
"v_smfmac_f32_16x16x64_bf16 v[106:109], v[82:85], v[120:127], v66\n"
"ds_read_b64_tr_b16 v[248:249], v182 offset:41408\n"
"v_mfma_f32_16x16x32_bf16 v[82:85], v[172:175], v[82:85], 0\n"
"ds_read_b64_tr_b16 v[250:251], v183 offset:41408\n"
"ds_read_b64_tr_b16 v[128:129], v182 offset:40648\n"
"v_mfma_f32_16x16x32_bf16 v[208:211], a[48:51], v[48:51], v[208:211]\n"
"s_waitcnt lgkmcnt(5)\n"
"ds_read_b64_tr_b16 v[130:131], v183 offset:40648\n"
"v_cvt_pk_bf16_f32 v234, v236, v237\n"
"v_mfma_f32_16x16x32_bf16 v[216:219], a[52:55], v[48:51], v[216:219]\n"
"s_add_u32 m0, s34, 4096\n"
"ds_read_b64_tr_b16 v[132:133], v182 offset:41416\n"
"buffer_load_dwordx4 v191, s[8:11], s0 offen lds\n"
"v_mfma_f32_16x16x32_bf16 v[220:223], a[56:59], v[48:51], v[220:223]\n"
"s_add_u32 s0, s0, s33\n"
"ds_read_b64_tr_b16 v[134:135], v183 offset:41416\n"
"v_cvt_pk_bf16_f32 v235, v238, v239\n"
"v_mfma_f32_16x16x32_bf16 v[208:211], a[60:63], v[52:55], v[208:211]\n"
"ds_write_b128 v196, v[232:235] offset:30720\n"
"v_mfma_f32_16x16x32_bf16 v[216:219], a[64:67], v[52:55], v[216:219]\n"
"v_cvt_pk_bf16_f32 v241, v242, v243\n"
"v_cvt_pk_bf16_f32 v148, v212, v213\n"
"v_mfma_f32_16x16x32_bf16 v[220:223], a[68:71], v[52:55], v[220:223]\n"
"v_cvt_pk_bf16_f32 v149, v214, v215\n"
"v_cvt_pk_bf16_f32 v150, v204, v205\n"
"v_mfma_f32_16x16x32_bf16 v[212:215], a[76:79], v[32:35], 0\n"
"ds_write_b64 v197, v[240:241] offset:30720\n"
"v_mfma_f32_16x16x32_bf16 v[212:215], a[88:91], v[36:39], v[212:215]\n"
"v_cvt_pk_bf16_f32 v151, v206, v207\n"
"v_cvt_pk_bf16_f32 v162, v228, v229\n"
"v_mfma_f32_16x16x32_bf16 v[204:207], a[80:83], v[32:35], 0\n"
"ds_read_b64_tr_b16 v[112:113], v182 offset:40584\n"
"ds_read_b64_tr_b16 v[114:115], v183 offset:40584\n"
"v_mfma_f32_16x16x32_bf16 v[204:207], a[92:95], v[36:39], v[204:207]\n"
"ds_read_b64_tr_b16 v[116:117], v182 offset:41352\n"
"v_fmamk_f32 v136, v136, 0x3e553b95, v170\n"
"v_mfma_f32_16x16x32_bf16 v[212:215], a[100:103], v[40:43], v[212:215]\n"
"s_add_u32 m0, s34, 2048\n"
"ds_read_b64_tr_b16 v[118:119], v183 offset:41352\n"
"buffer_load_dwordx4 v190, s[8:11], s1 offen lds\n"
"v_mfma_f32_16x16x32_bf16 v[204:207], a[104:107], v[40:43], v[204:207]\n"
"v_fmamk_f32 v137, v137, 0x3e553b95, v170\n"
"v_cvt_pk_bf16_f32 v163, v230, v231\n"
"v_mfma_f32_16x16x32_bf16 v[212:215], a[112:115], v[44:47], v[212:215]\n"
"v_fmamk_f32 v138, v138, 0x3e553b95, v170\n"
"v_fmamk_f32 v139, v139, 0x3e553b95, v170\n"
"v_mfma_f32_16x16x32_bf16 v[204:207], a[116:119], v[44:47], v[204:207]\n"
"s_waitcnt lgkmcnt(4)\n"
"v_exp_f32 v136, v136\n"
"v_mfma_f32_16x16x32_bf16 v[224:227], a[120:123], v[48:51], v[224:227]\n"
"s_barrier\n"
"v_mfma_f32_16x16x32_bf16 v[212:215], a[124:127], v[48:51], v[212:215]\n"
"ds_read_b128 v[76:79], v192 offset:36864\n"
"ds_read_b128 v[120:123], v178 offset:27008\n"
"v_mfma_f32_16x16x32_bf16 v[204:207], a[128:131], v[48:51], v[204:207]\n"
"v_exp_f32 v137, v137\n"
"v_mfma_f32_16x16x32_bf16 v[224:227], a[132:135], v[52:55], v[224:227]\n"
"v_exp_f32 v138, v138\n"
"v_mfma_f32_16x16x32_bf16 v[212:215], a[136:139], v[52:55], v[212:215]\n"
"v_exp_f32 v139, v139\n"
"v_mfma_f32_16x16x32_bf16 v[204:207], a[140:143], v[52:55], v[204:207]\n"
"v_cvt_pk_bf16_f32 v90, v136, v138\n"
"v_cvt_pk_bf16_f32 v91, v137, v139\n"
"v_mfma_f32_16x16x32_bf16 v[228:231], a[144:147], v[32:35], 0\n"
"s_add_u32 m0, s34, 5120\n"
"v_fmamk_f32 v140, v140, 0x3e553b95, v170\n"
"v_fmamk_f32 v141, v141, 0x3e553b95, v170\n"
"v_mfma_f32_16x16x32_bf16 v[228:231], a[156:159], v[36:39], v[228:231]\n"
"s_waitcnt vmcnt(11)\n"
"buffer_load_dwordx4 v191, s[8:11], s1 offen lds\n"
"ds_read_b128 v[136:139], v179 offset:27008\n"
"v_mfma_f32_16x16x32_bf16 v[228:231], a[168:171], v[40:43], v[228:231]\n"
"s_add_u32 s1, s1, s33\n"
"v_or_b32 v201, 7, s6\n"
"v_fmamk_f32 v142, v142, 0x3e553b95, v170\n"
"v_mfma_f32_16x16x32_bf16 v[228:231], a[180:183], v[44:47], v[228:231]\n"
"s_waitcnt lgkmcnt(2)\n"
"v_fmamk_f32 v143, v143, 0x3e553b95, v170\n"
"v_exp_f32 v140, v140\n"
"v_mfma_f32_16x16x32_bf16 v[232:235], a[216:219], v[76:79], 0\n"
"s_waitcnt lgkmcnt(1)\n"
"s_mov_b64 exec, 1\n"
"buffer_store_dword v201, off, s[28:31], s40 sc1\n"
"s_mov_b64 exec, -1\n"
"v_mfma_f32_16x16x32_bf16 v[120:123], v[120:123], v[152:155], 0\n"
"v_exp_f32 v141, v141\n"
"v_mfma_f32_16x16x32_bf16 v[236:239], a[220:223], v[76:79], 0\n"
"v_exp_f32 v142, v142\n"
"v_mfma_f32_16x16x32_bf16 v[240:243], a[224:227], v[76:79], 0\n"
"ds_read_b128 v[76:79], v192 offset:39936\n"
"v_cvt_pk_bf16_f32 v92, v140, v142\n"
"v_mfma_f32_16x16x32_bf16 v[228:231], a[192:195], v[48:51], v[228:231]\n"
"s_waitcnt lgkmcnt(1)\n"
"v_exp_f32 v143, v143\n"
"v_mfma_f32_16x16x32_bf16 v[136:139], v[136:139], v[152:155], 0\n"
"ds_read_b128 v[124:127], v180\n"
"v_cvt_pk_bf16_f32 v93, v141, v143\n"
"v_mfma_f32_16x16x32_bf16 v[228:231], a[204:207], v[52:55], v[228:231]\n"
"ds_read_b128 v[140:143], v181\n"
"ds_read_b128 v[68:71], v202 offset:30720\n"
"v_smfmac_f32_16x16x64_bf16 v[252:255], v[90:93], v[244:251], v66\n"
"ds_read_b128 v[244:247], v180 offset:13504\n"
"v_smfmac_f32_16x16x64_bf16 v[98:101], v[90:93], v[128:135], v66\n"
"ds_read_b128 v[128:131], v181 offset:13504\n"
"v_smfmac_f32_16x16x64_bf16 v[106:109], v[90:93], v[112:119], v66\n"
"ds_read_b128 v[248:251], v180 offset:27008\n"
"v_mfma_f32_16x16x32_bf16 v[90:93], v[172:175], v[90:93], v[82:85]\n"
"s_nop 1\n"
"v_cvt_pk_bf16_f32 v252, v252, v253\n"
"v_cvt_pk_bf16_f32 v253, v254, v255\n"
"v_cvt_pk_bf16_f32 v98, v98, v99\n"
"v_cvt_pk_bf16_f32 v99, v100, v101\n"
"v_cvt_pk_bf16_f32 v106, v106, v107\n"
"v_cvt_pk_bf16_f32 v107, v108, v109\n"
"v_rcp_f32 v171, v90\n"
"v_cvt_pk_bf16_f32 v208, v208, v209\n"
"v_cvt_pk_bf16_f32 v171, v171, v171\n"
"v_and_b32 v64, v171, v176\n"
"v_and_b32 v65, v171, v177\n"
"v_cvt_pk_bf16_f32 v209, v210, v211\n"
"v_cvt_pk_bf16_f32 v210, v216, v217\n"
"v_mfma_f32_16x16x16_bf16 v[252:255], v[252:253], v[64:65], 0\n"
"s_waitcnt lgkmcnt(3)\n"
"ds_read_b64 v[82:83], v178 offset:27072\n"
"ds_read_b64 v[84:85], v179 offset:27072\n"
"v_mfma_f32_16x16x16_bf16 v[98:101], v[98:99], v[64:65], 0\n"
"s_waitcnt lgkmcnt(4)\n"
"ds_read_b64 v[90:91], v180 offset:64\n"
"buffer_store_dwordx4 v[68:71], v194, s[24:27], s36 offen sc1\n"
"v_mfma_f32_16x16x16_bf16 v[106:109], v[106:107], v[64:65], 0\n"
"s_waitcnt lgkmcnt(3)\n"
"ds_read_b128 v[68:71], v192 offset:37888\n"
"ds_read_b64 v[92:93], v181 offset:64\n"
"v_mfma_f32_16x16x32_bf16 v[124:127], v[124:127], v[152:155], 0\n"
"ds_read_b128 v[132:135], v181 offset:27008\n"
"v_cvt_pk_bf16_f32 v252, v252, v253\n"
"v_mfma_f32_16x16x32_bf16 v[140:143], v[140:143], v[152:155], 0\n"
"v_cvt_pk_bf16_f32 v253, v254, v255\n"
"v_cvt_pk_bf16_f32 v254, v98, v99\n"
"v_mfma_f32_16x16x32_bf16 v[244:247], v[244:247], v[152:155], 0\n"
"ds_read_b64 v[98:99], v180 offset:13568\n"
"v_cvt_pk_bf16_f32 v255, v100, v101\n"
"v_mfma_f32_16x16x32_bf16 v[128:131], v[128:131], v[152:155], 0\n"
"buffer_store_dwordx4 v[252:255], v192, s[28:31], s37 offen sc1\n"
"v_mfma_f32_16x16x32_bf16 v[248:251], v[248:251], v[152:155], 0\n"
"s_waitcnt lgkmcnt(5)\n"
"ds_read_b64 v[100:101], v181 offset:13568\n"
"v_cvt_pk_bf16_f32 v106, v106, v107\n"
"v_mfma_f32_16x16x32_bf16 v[120:123], v[80:83], v[164:167], v[120:123]\n"
"s_waitcnt lgkmcnt(5)\n"
"v_cvt_pk_bf16_f32 v107, v108, v109\n"
"v_cvt_pk_bf16_f32 v211, v218, v219\n"
"v_mfma_f32_16x16x32_bf16 v[136:139], v[84:87], v[166:169], v[136:139]\n"
"s_waitcnt lgkmcnt(4)\n"
"buffer_store_dwordx2 v[106:107], v193, s[28:31], s37 offen offset:1024 sc1\n"
"v_mfma_f32_16x16x32_bf16 v[124:127], v[88:91], v[164:167], v[124:127]\n"
"s_waitcnt lgkmcnt(3)\n"
"ds_write_b128 v186, v[208:211] offset:6752\n"
"v_mfma_f32_16x16x32_bf16 v[140:143], v[92:95], v[166:169], v[140:143]\n"
"s_waitcnt lgkmcnt(2)\n"
"v_cvt_pk_bf16_f32 v220, v220, v221\n"
"v_cvt_pk_bf16_f32 v221, v222, v223\n"
"v_mfma_f32_16x16x32_bf16 v[244:247], v[96:99], v[164:167], v[244:247]\n"
"s_waitcnt vmcnt(12)\n"
"v_max_f32 v120, v120, v136\n"
"v_max_f32 v121, v121, v137\n"
"v_mfma_f32_16x16x32_bf16 v[132:135], v[132:135], v[152:155], 0\n"
"s_add_u32 s37, s37, 6144\n"
"v_add3_u32 v200, v200, s50, -5\n"
"v_cmp_lt_u32 vcc, 64, v200\n"
"v_mfma_f32_16x16x32_bf16 v[232:235], a[228:231], v[68:71], v[232:235]\n"
"s_waitcnt lgkmcnt(1)\n"
"ds_read_b64 v[106:107], v180 offset:27072\n"
"ds_read_b64 v[108:109], v181 offset:27072\n"
"v_mfma_f32_16x16x32_bf16 v[128:131], v[100:103], v[166:169], v[128:131]\n"
"s_cbranch_vccnz .Lp4%=\n"
".Lr4%=:\n"
"v_mfma_f32_16x16x32_bf16 v[216:219], a[148:151], v[32:35], 0\n"
"ds_read_b64_tr_b16 v[112:113], v182 offset:27136\n"
"ds_read_b64_tr_b16 v[114:115], v183 offset:27136\n"
"v_mfma_f32_16x16x32_bf16 v[208:211], a[152:155], v[32:35], 0\n"
"ds_read_b128 v[32:35], v188 offset:6144\n"
"v_max_f32 v122, v122, v138\n"
"v_mfma_f32_16x16x32_bf16 v[236:239], a[232:235], v[68:71], v[236:239]\n"
"ds_read_b128 v[72:75], v202 offset:34816\n"
"v_max_f32 v123, v123, v139\n"
"v_mfma_f32_16x16x32_bf16 v[216:219], a[160:163], v[36:39], v[216:219]\n"
"ds_read_b64_tr_b16 v[116:117], v182 offset:27904\n"
"v_max_f32 v170, v120, v121\n"
"v_mfma_f32_16x16x32_bf16 v[240:243], a[236:239], v[68:71], v[240:243]\n"
"ds_read_b128 v[68:71], v192 offset:40960\n"
"v_max_f32 v171, v122, v123\n"
"v_mfma_f32_16x16x32_bf16 v[208:211], a[164:167], v[36:39], v[208:211]\n"
"ds_read_b128 v[36:39], v189 offset:6144\n"
"v_max_f32 v124, v124, v140\n"
"v_mfma_f32_16x16x32_bf16 v[216:219], a[172:175], v[40:43], v[216:219]\n"
"s_waitcnt lgkmcnt(8)\n"
"ds_read_b64_tr_b16 v[118:119], v183 offset:27904\n"
"v_max_f32 v125, v125, v141\n"
"v_mfma_f32_16x16x32_bf16 v[248:251], v[104:107], v[164:167], v[248:251]\n"
"s_waitcnt lgkmcnt(8)\n"
"ds_read_b64_tr_b16 v[136:137], v182 offset:27144\n"
"v_max_f32 v126, v126, v142\n"
"v_mfma_f32_16x16x32_bf16 v[132:135], v[108:111], v[166:169], v[132:135]\n"
"ds_read_b64_tr_b16 v[138:139], v183 offset:27144\n"
"v_max_f32 v127, v127, v143\n"
"v_mfma_f32_16x16x32_bf16 v[208:211], a[176:179], v[40:43], v[208:211]\n"
"s_waitcnt vmcnt(10)\n"
"ds_read_b128 v[40:43], v188 offset:7168\n"
"v_max3_f32 v170, v170, v124, v125\n"
"v_mfma_f32_16x16x32_bf16 v[216:219], a[184:187], v[44:47], v[216:219]\n"
"s_waitcnt vmcnt(9)\n"
"ds_write_b128 v202, v[56:59] offset:43008\n"
"v_mfma_f32_16x16x32_bf16 v[208:211], a[188:191], v[44:47], v[208:211]\n"
"s_waitcnt lgkmcnt(8)\n"
"buffer_load_dwordx4 v[56:59], v192, s[28:31], s38 offen sc1\n"
"v_max3_f32 v171, v171, v126, v127\n"
"v_mfma_f32_16x16x32_bf16 v[216:219], a[196:199], v[48:51], v[216:219]\n"
"v_max_f32 v244, v244, v128\n"
"v_max_f32 v245, v245, v129\n"
"v_mfma_f32_16x16x32_bf16 v[208:211], a[200:203], v[48:51], v[208:211]\n"
"s_waitcnt lgkmcnt(5)\n"
"ds_read_b128 v[44:47], v189 offset:7168\n"
"ds_read_b128 v[48:51], v188 offset:8192\n"
"v_mfma_f32_16x16x32_bf16 v[216:219], a[208:211], v[52:55], v[216:219]\n"
"s_add_u32 m0, s34, 6144\n"
"buffer_store_dwordx4 v[72:75], v195, s[24:27], s36 offen sc1\n"
"v_mfma_f32_16x16x32_bf16 v[208:211], a[212:215], v[52:55], v[208:211]\n"
"s_add_u32 s36, s36, s33\n"
"ds_read_b128 v[72:75], v192 offset:38912\n"
"ds_read_b128 v[52:55], v189 offset:8192\n"
"ds_read_b64_tr_b16 v[140:141], v182 offset:27912\n"
"v_max_f32 v246, v246, v130\n"
"v_cvt_pk_bf16_f32 v224, v224, v225\n"
"v_max_f32 v247, v247, v131\n"
"v_max3_f32 v170, v170, v244, v245\n"
"v_cvt_pk_bf16_f32 v225, v226, v227\n"
"v_max3_f32 v171, v171, v246, v247\n"
"v_cvt_pk_bf16_f32 v226, v212, v213\n"
"v_max_f32 v248, v248, v132\n"
"buffer_load_dwordx4 v190, s[8:11], s32 offen lds\n"
"ds_read_b64_tr_b16 v[142:143], v183 offset:27912\n"
"v_max_f32 v249, v249, v133\n"
"v_cvt_pk_bf16_f32 v227, v214, v215\n"
"v_mfma_f32_16x16x32_bf16 v[212:215], a[0:3], v[32:35], 0\n"
"ds_write_b64 v203, v[60:61] offset:43008\n"
"v_mfma_f32_16x16x32_bf16 v[212:215], a[12:15], v[36:39], v[212:215]\n"
"s_waitcnt lgkmcnt(8)\n"
"v_max_f32 v250, v250, v134\n"
"v_cvt_pk_bf16_f32 v222, v204, v205\n"
"v_mfma_f32_16x16x32_bf16 v[212:215], a[24:27], v[40:43], v[212:215]\n"
"s_waitcnt lgkmcnt(6)\n"
"buffer_load_dwordx2 v[60:61], v193, s[28:31], s38 offen offset:1024 sc1\n"
"ds_read_b64_tr_b16 v[128:129], v182 offset:27080\n"
"v_mfma_f32_16x16x32_bf16 v[212:215], a[36:39], v[44:47], v[212:215]\n"
"s_waitcnt lgkmcnt(5)\n"
"ds_read_b64_tr_b16 v[130:131], v183 offset:27080\n"
"ds_read_b64_tr_b16 v[132:133], v182 offset:27848\n"
"v_mfma_f32_16x16x32_bf16 v[232:235], a[240:243], v[72:75], v[232:235]\n"
"s_add_u32 s38, s38, 6144\n"
"ds_write_b128 v186, v[224:227] offset:6880\n"
"v_mfma_f32_16x16x32_bf16 v[236:239], a[244:247], v[72:75], v[236:239]\n"
"v_max_f32 v251, v251, v135\n"
"v_max3_f32 v170, v170, v248, v249\n"
"v_mfma_f32_16x16x32_bf16 v[240:243], a[248:251], v[72:75], v[240:243]\n"
"v_cvt_pk_bf16_f32 v223, v206, v207\n"
"v_max3_f32 v171, v171, v250, v251\n"
"v_mfma_f32_16x16x32_bf16 v[232:235], a[252:255], v[76:79], v[232:235]\n"
"ds_read_b128 v[72:75], v192 offset:41984\n"
"ds_write_b128 v186, v[220:223] offset:6816\n"
"v_mfma_f32_16x16x32_bf16 v[240:243], v[4:7], v[76:79], v[240:243]\n"
"s_waitcnt lgkmcnt(9)\n"
"v_mov_b64 v[252:253], 0\n"
"v_mov_b64 v[254:255], 0\n"
"v_mfma_f32_16x16x32_bf16 v[236:239], v[0:3], v[76:79], v[236:239]\n"
"s_add_u32 m0, s34, 9216\n"
"v_mov_b64 v[98:99], 0\n"
"v_mov_b64 v[100:101], 0\n"
"v_mfma_f32_16x16x32_bf16 v[232:235], v[8:11], v[68:71], v[232:235]\n"
"buffer_load_dwordx4 v191, s[8:11], s32 offen lds\n"
"ds_read_b64_tr_b16 v[134:135], v183 offset:27848\n"
"v_mfma_f32_16x16x32_bf16 v[240:243], v[16:19], v[68:71], v[240:243]\n"
"s_add_u32 s32, s32, s33\n"
"v_mov_b64 v[106:107], 0\n"
"v_mov_b64 v[108:109], 0\n"
"v_mfma_f32_16x16x32_bf16 v[236:239], v[12:15], v[68:71], v[236:239]\n"
"v_max_f32 v170, v170, v171\n"
"v_mov_b32 v171, v170\n"
"v_mfma_f32_16x16x32_bf16 v[204:207], a[4:7], v[32:35], 0\n"
"s_nop 0\n"
"v_permlane16_swap_b32 v170, v171\n"
"v_mfma_f32_16x16x32_bf16 v[220:223], a[8:11], v[32:35], 0\n"
"s_waitcnt lgkmcnt(2)\n"
"v_max_f32 v170, v170, v171\n"
"v_mov_b32 v171, v170\n"
"v_mfma_f32_16x16x32_bf16 v[232:235], v[20:23], v[72:75], v[232:235]\n"
"s_nop 0\n"
"v_permlane32_swap_b32 v170, v171\n"
"v_mfma_f32_16x16x32_bf16 v[240:243], v[28:31], v[72:75], v[240:243]\n"
"v_max_f32 v170, v170, v171\n"
"v_mul_f32 v170, 0xbe553b95, v170\n"
"v_mfma_f32_16x16x32_bf16 v[236:239], v[24:27], v[72:75], v[236:239]\n"
"v_fmamk_f32 v120, v120, 0x3e553b95, v170\n"
"v_fmamk_f32 v121, v121, 0x3e553b95, v170\n"
"v_mfma_f32_16x16x32_bf16 v[204:207], a[16:19], v[36:39], v[204:207]\n"
"v_fmamk_f32 v122, v122, 0x3e553b95, v170\n"
"v_fmamk_f32 v123, v123, 0x3e553b95, v170\n"
"v_mfma_f32_16x16x32_bf16 v[220:223], a[20:23], v[36:39], v[220:223]\n"
"v_exp_f32 v120, v120\n"
"v_mfma_f32_16x16x32_bf16 v[204:207], a[28:31], v[40:43], v[204:207]\n"
"s_add_u32 m0, s34, 7168\n"
"v_exp_f32 v121, v121\n"
"v_mfma_f32_16x16x32_bf16 v[220:223], a[32:35], v[40:43], v[220:223]\n"
"buffer_load_dwordx4 v190, s[8:11], s0 offen lds\n"
"v_fmamk_f32 v124, v124, 0x3e553b95, v170\n"
"v_mfma_f32_16x16x32_bf16 v[204:207], a[40:43], v[44:47], v[204:207]\n"
"v_exp_f32 v122, v122\n"
"v_mfma_f32_16x16x32_bf16 v[220:223], a[44:47], v[44:47], v[220:223]\n"
"v_exp_f32 v123, v123\n"
"v_mfma_f32_16x16x32_bf16 v[212:215], a[48:51], v[48:51], v[212:215]\n"
"v_cvt_pk_bf16_f32 v82, v120, v122\n"
"v_cvt_pk_bf16_f32 v83, v121, v123\n"
"v_mfma_f32_16x16x32_bf16 v[204:207], a[52:55], v[48:51], v[204:207]\n"
"ds_read_b64_tr_b16 v[120:121], v182 offset:54144\n"
"ds_read_b64_tr_b16 v[122:123], v183 offset:54144\n"
"v_mfma_f32_16x16x32_bf16 v[220:223], a[56:59], v[48:51], v[220:223]\n"
"v_fmamk_f32 v125, v125, 0x3e553b95, v170\n"
"v_fmamk_f32 v126, v126, 0x3e553b95, v170\n"
"v_mfma_f32_16x16x32_bf16 v[212:215], a[60:63], v[52:55], v[212:215]\n"
"v_fmamk_f32 v127, v127, 0x3e553b95, v170\n"
"v_exp_f32 v124, v124\n"
"v_mfma_f32_16x16x32_bf16 v[204:207], a[64:67], v[52:55], v[204:207]\n"
"v_exp_f32 v125, v125\n"
"v_mfma_f32_16x16x32_bf16 v[220:223], a[68:71], v[52:55], v[220:223]\n"
"v_exp_f32 v126, v126\n"
"v_mfma_f32_16x16x32_bf16 v[224:227], a[72:75], v[32:35], 0\n"
"v_exp_f32 v127, v127\n"
"v_mfma_f32_16x16x32_bf16 v[224:227], a[84:87], v[36:39], v[224:227]\n"
"s_add_u32 m0, s34, 10240\n"
"v_cvt_pk_bf16_f32 v84, v124, v126\n"
"v_cvt_pk_bf16_f32 v85, v125, v127\n"
"v_mfma_f32_16x16x32_bf16 v[224:227], a[96:99], v[40:43], v[224:227]\n"
"s_waitcnt lgkmcnt(2)\n"
"buffer_load_dwordx4 v191, s[8:11], s0 offen lds\n"
"ds_read_b64_tr_b16 v[124:125], v182 offset:54912\n"
"v_smfmac_f32_16x16x64_bf16 v[252:255], v[82:85], v[112:119], v66\n"
"s_add_u32 s0, s0, s33\n"
"ds_read_b64_tr_b16 v[126:127], v183 offset:54912\n"
"v_smfmac_f32_16x16x64_bf16 v[98:101], v[82:85], v[136:143], v66\n"
"ds_read_b64_tr_b16 v[112:113], v182 offset:54152\n"
"v_smfmac_f32_16x16x64_bf16 v[106:109], v[82:85], v[128:135], v66\n"
"ds_read_b64_tr_b16 v[114:115], v183 offset:54152\n"
"v_mfma_f32_16x16x32_bf16 v[82:85], v[172:175], v[82:85], 0\n"
"ds_read_b64_tr_b16 v[116:117], v182 offset:54920\n"
"ds_read_b64_tr_b16 v[118:119], v183 offset:54920\n"
"v_mfma_f32_16x16x32_bf16 v[224:227], a[108:111], v[44:47], v[224:227]\n"
"buffer_load_dword v200, off, s[28:31], s39 sc1\n"
"ds_read_b64_tr_b16 v[136:137], v182 offset:54088\n"
"v_mfma_f32_16x16x32_bf16 v[224:227], a[120:123], v[48:51], v[224:227]\n"
"ds_read_b64_tr_b16 v[138:139], v183 offset:54088\n"
"v_cvt_pk_bf16_f32 v232, v232, v233\n"
"v_mfma_f32_16x16x32_bf16 v[224:227], a[132:135], v[52:55], v[224:227]\n"
"v_cvt_pk_bf16_f32 v233, v234, v235\n"
"v_cvt_pk_bf16_f32 v240, v240, v241\n"
"v_cvt_pk_bf16_f32 v234, v236, v237\n"
"v_cvt_pk_bf16_f32 v235, v238, v239\n"
"v_cvt_pk_bf16_f32 v241, v242, v243\n"
"v_cvt_pk_bf16_f32 v152, v228, v229\n"
"v_cvt_pk_bf16_f32 v153, v230, v231\n"
"v_mfma_f32_16x16x32_bf16 v[228:231], a[76:79], v[32:35], 0\n"
"ds_write_b128 v196, v[232:235] offset:55296\n"
"v_mfma_f32_16x16x32_bf16 v[228:231], a[88:91], v[36:39], v[228:231]\n"
"s_add_u32 m0, s34, 8192\n"
"v_cvt_pk_bf16_f32 v154, v216, v217\n"
"v_cvt_pk_bf16_f32 v155, v218, v219\n"
"v_mfma_f32_16x16x32_bf16 v[216:219], a[80:83], v[32:35], 0\n"
"buffer_load_dwordx4 v190, s[8:11], s1 offen lds\n"
"ds_write_b64 v197, v[240:241] offset:55296\n"
"v_mfma_f32_16x16x32_bf16 v[216:219], a[92:95], v[36:39], v[216:219]\n"
"v_fmamk_f32 v244, v244, 0x3e553b95, v170\n"
"v_cvt_pk_bf16_f32 v166, v208, v209\n"
"v_mfma_f32_16x16x32_bf16 v[228:231], a[100:103], v[40:43], v[228:231]\n"
"ds_read_b64_tr_b16 v[140:141], v182 offset:54856\n"
"ds_read_b64_tr_b16 v[142:143], v183 offset:54856\n"
"v_mfma_f32_16x16x32_bf16 v[216:219], a[104:107], v[40:43], v[216:219]\n"
"v_fmamk_f32 v245, v245, 0x3e553b95, v170\n"
"v_fmamk_f32 v246, v246, 0x3e553b95, v170\n"
"v_mfma_f32_16x16x32_bf16 v[228:231], a[112:115], v[44:47], v[228:231]\n"
"v_cvt_pk_bf16_f32 v167, v210, v211\n"
"v_fmamk_f32 v247, v247, 0x3e553b95, v170\n"
"v_mfma_f32_16x16x32_bf16 v[216:219], a[116:119], v[44:47], v[216:219]\n"
"v_exp_f32 v244, v244\n"
"v_mfma_f32_16x16x32_bf16 v[228:231], a[124:127], v[48:51], v[228:231]\n"
"v_exp_f32 v245, v245\n"
"v_mfma_f32_16x16x32_bf16 v[216:219], a[128:131], v[48:51], v[216:219]\n"
"s_waitcnt lgkmcnt(2)\n"
"v_exp_f32 v246, v246\n"
"v_mfma_f32_16x16x32_bf16 v[228:231], a[136:139], v[52:55], v[228:231]\n"
"s_barrier\n"
"v_mfma_f32_16x16x32_bf16 v[216:219], a[140:143], v[52:55], v[216:219]\n"
"ds_read_b128 v[72:75], v192 offset:43008\n"
"v_cvt_pk_bf16_f32 v90, v244, v246\n"
"v_mfma_f32_16x16x32_bf16 v[208:211], a[144:147], v[32:35], 0\n"
"s_add_u32 m0, s34, 11264\n"
"v_exp_f32 v247, v247\n"
"v_mfma_f32_16x16x32_bf16 v[208:211], a[156:159], v[36:39], v[208:211]\n"
"buffer_load_dwordx4 v191, s[8:11], s1 offen lds\n"
"v_cvt_pk_bf16_f32 v91, v245, v247\n"
"v_mfma_f32_16x16x32_bf16 v[208:211], a[168:171], v[40:43], v[208:211]\n"
"s_add_u32 s1, s1, s33\n"
"v_fmamk_f32 v248, v248, 0x3e553b95, v170\n"
"v_fmamk_f32 v249, v249, 0x3e553b95, v170\n"
"v_mfma_f32_16x16x32_bf16 v[208:211], a[180:183], v[44:47], v[208:211]\n"
"v_fmamk_f32 v250, v250, 0x3e553b95, v170\n"
"v_fmamk_f32 v251, v251, 0x3e553b95, v170\n"
"v_mfma_f32_16x16x32_bf16 v[208:211], a[192:195], v[48:51], v[208:211]\n"
"ds_read_b128 v[128:131], v180\n"
"ds_read_b128 v[244:247], v181\n"
"v_mfma_f32_16x16x32_bf16 v[208:211], a[204:207], v[52:55], v[208:211]\n"
"v_exp_f32 v248, v248\n"
"v_exp_f32 v249, v249\n"
"v_exp_f32 v250, v250\n"
"v_exp_f32 v251, v251\n"
"v_cvt_pk_bf16_f32 v92, v248, v250\n"
"v_cvt_pk_bf16_f32 v93, v249, v251\n"
"s_mov_b64 exec, s[44:45]\n"
"v_add_u32 v179, 0xc0, v179\n"
"s_mov_b64 exec, -1\n"
"v_smfmac_f32_16x16x64_bf16 v[252:255], v[90:93], v[120:127], v66\n"
"s_waitcnt vmcnt(10) lgkmcnt(2)\n"
"v_or_b32 v201, 8, s6\n"
"v_smfmac_f32_16x16x64_bf16 v[98:101], v[90:93], v[112:119], v66\n"
"s_mov_b64 exec, 1\n"
"buffer_store_dword v201, off, s[28:31], s40 sc1\n"
"s_mov_b64 exec, -1\n"
"v_smfmac_f32_16x16x64_bf16 v[106:109], v[90:93], v[136:143], v66\n"
"s_waitcnt lgkmcnt(1)\n"
"ds_read_b128 v[132:135], v180 offset:13504\n"
"v_mfma_f32_16x16x32_bf16 v[90:93], v[172:175], v[90:93], v[82:85]\n"
"s_waitcnt lgkmcnt(1)\n"
"ds_read_b128 v[248:251], v181 offset:13504\n"
"ds_read_b128 v[120:123], v180 offset:27008\n"
"v_mfma_f32_16x16x32_bf16 v[128:131], v[128:131], v[144:147], 0\n"
"ds_read_b64 v[82:83], v180 offset:64\n"
"v_cvt_pk_bf16_f32 v252, v252, v253\n"
"v_mfma_f32_16x16x32_bf16 v[244:247], v[244:247], v[144:147], 0\n"
".Lic5%=:\n"
"s_cbranch_execz .Lib1%=\n"
"ds_read_b64 v[84:85], v181 offset:64\n"
"v_cvt_pk_bf16_f32 v253, v254, v255\n"
"v_mfma_f32_16x16x32_bf16 v[232:235], a[216:219], v[72:75], 0\n"
"ds_read_b64 v[92:93], v181 offset:13568\n"
"v_cvt_pk_bf16_f32 v98, v98, v99\n"
"v_mfma_f32_16x16x32_bf16 v[236:239], a[220:223], v[72:75], 0\n"
"v_rcp_f32 v171, v90\n"
"v_mfma_f32_16x16x32_bf16 v[240:243], a[224:227], v[72:75], 0\n"
"ds_read_b128 v[72:75], v192 offset:46080\n"
"ds_read_b64 v[90:91], v180 offset:13568\n"
"v_cvt_pk_bf16_f32 v171, v171, v171\n"
"v_and_b32 v64, v171, v176\n"
"v_and_b32 v65, v171, v177\n"
"v_cvt_pk_bf16_f32 v99, v100, v101\n"
"v_cvt_pk_bf16_f32 v106, v106, v107\n"
"v_mfma_f32_16x16x16_bf16 v[252:255], v[252:253], v[64:65], 0\n"
"s_waitcnt lgkmcnt(7)\n"
"ds_read_b128 v[112:115], v181 offset:27008\n"
"v_cvt_pk_bf16_f32 v107, v108, v109\n"
"v_mfma_f32_16x16x16_bf16 v[98:101], v[98:99], v[64:65], 0\n"
"s_waitcnt lgkmcnt(7)\n"
"ds_read_b128 v[124:127], v178\n"
"ds_read_b128 v[116:119], v179\n"
"v_mfma_f32_16x16x16_bf16 v[106:109], v[106:107], v[64:65], 0\n"
"s_waitcnt lgkmcnt(8)\n"
"v_cvt_pk_bf16_f32 v252, v252, v253\n"
"v_cvt_pk_bf16_f32 v253, v254, v255\n"
"v_mfma_f32_16x16x32_bf16 v[132:135], v[132:135], v[144:147], 0\n"
"s_waitcnt lgkmcnt(7)\n"
"ds_read_b128 v[76:79], v202 offset:55296\n"
"v_cvt_pk_bf16_f32 v254, v98, v99\n"
"v_mfma_f32_16x16x32_bf16 v[128:131], v[80:83], v[156:159], v[128:131]\n"
"s_waitcnt lgkmcnt(7)\n"
"ds_read_b64 v[98:99], v180 offset:27072\n"
"v_cvt_pk_bf16_f32 v255, v100, v101\n"
"v_mfma_f32_16x16x32_bf16 v[244:247], v[84:87], v[158:161], v[244:247]\n"
"buffer_store_dwordx4 v[252:255], v192, s[28:31], s37 offen sc1\n"
"v_mfma_f32_16x16x32_bf16 v[248:251], v[248:251], v[144:147], 0\n"
"s_waitcnt lgkmcnt(5)\n"
"ds_read_b64 v[100:101], v181 offset:27072\n"
"v_cvt_pk_bf16_f32 v106, v106, v107\n"
"v_mfma_f32_16x16x32_bf16 v[132:135], v[88:91], v[156:159], v[132:135]\n"
"s_waitcnt vmcnt(11) lgkmcnt(5)\n"
"ds_write_b128 v202, v[56:59] offset:30720\n"
"v_mfma_f32_16x16x32_bf16 v[248:251], v[92:95], v[158:161], v[248:251]\n"
"s_waitcnt lgkmcnt(4)\n"
"buffer_load_dwordx4 v[56:59], v192, s[28:31], s38 offen sc1\n"
"v_cvt_pk_bf16_f32 v107, v108, v109\n"
"v_mfma_f32_16x16x32_bf16 v[120:123], v[120:123], v[144:147], 0\n"
"s_waitcnt lgkmcnt(3)\n"
"v_cvt_pk_bf16_f32 v212, v212, v213\n"
"v_cvt_pk_bf16_f32 v213, v214, v215\n"
"v_mfma_f32_16x16x32_bf16 v[112:115], v[112:115], v[144:147], 0\n"
"s_waitcnt lgkmcnt(2)\n"
"ds_read_b64 v[108:109], v179 offset:64\n"
"ds_read_b128 v[68:71], v202 offset:59392\n"
"v_mfma_f32_16x16x32_bf16 v[120:123], v[96:99], v[156:159], v[120:123]\n"
"buffer_store_dwordx2 v[106:107], v193, s[28:31], s37 offen offset:1024 sc1\n"
"v_mfma_f32_16x16x32_bf16 v[124:127], v[124:127], v[144:147], 0\n"
"s_add_u32 s37, s37, 6144\n"
"ds_read_b64 v[106:107], v178 offset:64\n"
"ds_read_b64_tr_b16 v[136:137], v182 offset:40640\n"
"v_mfma_f32_16x16x32_bf16 v[116:119], v[116:119], v[144:147], 0\n"
"s_waitcnt lgkmcnt(5)\n"
"ds_read_b64_tr_b16 v[138:139], v183 offset:40640\n"
"v_cvt_pk_bf16_f32 v214, v204, v205\n"
"v_mfma_f32_16x16x32_bf16 v[112:115], v[100:103], v[158:161], v[112:115]\n"
"v_cvt_pk_bf16_f32 v215, v206, v207\n"
"v_cvt_pk_bf16_f32 v220, v220, v221\n"
"v_mfma_f32_16x16x32_bf16 v[204:207], a[148:151], v[32:35], 0\n"
"ds_write_b128 v186, v[212:215] offset:20256\n"
"v_mfma_f32_16x16x32_bf16 v[212:215], a[152:155], v[32:35], 0\n"
"buffer_store_dwordx4 v[76:79], v194, s[24:27], s36 offen sc1\n"
"v_mfma_f32_16x16x32_bf16 v[204:207], a[160:163], v[36:39], v[204:207]\n"
"s_waitcnt lgkmcnt(4)\n"
"ds_read_b128 v[76:79], v192 offset:44032\n"
"ds_read_b128 v[32:35], v188\n"
"v_mfma_f32_16x16x32_bf16 v[116:119], v[108:111], v[158:161], v[116:119]\n"
"s_waitcnt vmcnt(11) lgkmcnt(5)\n"
"ds_read_b64_tr_b16 v[140:141], v182 offset:41408\n"
"v_cvt_pk_bf16_f32 v221, v222, v223\n"
"v_mfma_f32_16x16x32_bf16 v[124:127], v[104:107], v[156:159], v[124:127]\n"
"ds_read_b64_tr_b16 v[142:143], v183 offset:41408\n"
"buffer_store_dwordx4 v[68:71], v195, s[24:27], s36 offen sc1\n"
"v_mfma_f32_16x16x32_bf16 v[212:215], a[164:167], v[36:39], v[212:215]\n"
"s_add_u32 s36, s36, s33\n"
"ds_read_b128 v[68:71], v192 offset:45056\n"
"ds_read_b128 v[36:39], v189\n"
"v_mfma_f32_16x16x32_bf16 v[204:207], a[172:175], v[40:43], v[204:207]\n"
"ds_write_b64 v203, v[60:61] offset:30720\n"
"v_mfma_f32_16x16x32_bf16 v[212:215], a[176:179], v[40:43], v[212:215]\n"
"buffer_load_dwordx2 v[60:61], v193, s[28:31], s38 offen offset:1024 sc1\n"
"v_max_f32 v128, v128, v244\n"
"v_mfma_f32_16x16x32_bf16 v[204:207], a[184:187], v[44:47], v[204:207]\n"
"s_waitcnt lgkmcnt(6)\n"
"ds_read_b128 v[40:43], v188 offset:1024\n"
"v_max_f32 v129, v129, v245\n"
"v_mfma_f32_16x16x32_bf16 v[212:215], a[188:191], v[44:47], v[212:215]\n"
"s_add_u32 s38, s38, 6144\n"
"ds_read_b128 v[44:47], v189 offset:1024\n"
"ds_read_b64_tr_b16 v[244:245], v182 offset:40648\n"
"v_mfma_f32_16x16x32_bf16 v[232:235], a[228:231], v[76:79], v[232:235]\n"
"v_max_f32 v130, v130, v246\n"
"v_max_f32 v131, v131, v247\n"
"v_mfma_f32_16x16x32_bf16 v[236:239], a[232:235], v[76:79], v[236:239]\n"
"ds_read_b64_tr_b16 v[246:247], v183 offset:40648\n"
"v_max_f32 v170, v128, v129\n"
"v_mfma_f32_16x16x32_bf16 v[240:243], a[236:239], v[76:79], v[240:243]\n"
"s_waitcnt lgkmcnt(6)\n"
"ds_read_b128 v[76:79], v192 offset:47104\n"
"v_max_f32 v171, v130, v131\n"
"v_mfma_f32_16x16x32_bf16 v[204:207], a[196:199], v[48:51], v[204:207]\n"
"s_waitcnt lgkmcnt(6)\n"
"v_max_f32 v132, v132, v248\n"
"v_max_f32 v133, v133, v249\n"
"v_mfma_f32_16x16x32_bf16 v[232:235], a[240:243], v[68:71], v[232:235]\n"
"s_add_u32 m0, s34, 0\n"
"ds_read_b64_tr_b16 v[248:249], v182 offset:41416\n"
"buffer_load_dwordx4 v190, s[8:11], s32 offen lds\n"
"v_mfma_f32_16x16x32_bf16 v[236:239], a[244:247], v[68:71], v[236:239]\n"
"s_add_u32 s32, s32, s33\n"
"v_max_f32 v134, v134, v250\n"
"v_max_f32 v135, v135, v251\n"
"v_mfma_f32_16x16x32_bf16 v[240:243], a[248:251], v[68:71], v[240:243]\n"
"ds_read_b128 v[68:71], v192 offset:48128\n"
"ds_read_b64_tr_b16 v[250:251], v183 offset:41416\n"
"v_mfma_f32_16x16x32_bf16 v[232:235], a[252:255], v[72:75], v[232:235]\n"
"v_max3_f32 v170, v170, v132, v133\n"
"v_max3_f32 v171, v171, v134, v135\n"
"v_mfma_f32_16x16x32_bf16 v[240:243], v[4:7], v[72:75], v[240:243]\n"
"v_max_f32 v120, v120, v112\n"
"v_cvt_pk_bf16_f32 v224, v224, v225\n"
"v_mfma_f32_16x16x32_bf16 v[236:239], v[0:3], v[72:75], v[236:239]\n"
"v_max_f32 v121, v121, v113\n"
"v_max_f32 v122, v122, v114\n"
"v_mfma_f32_16x16x32_bf16 v[212:215], a[200:203], v[48:51], v[212:215]\n"
"s_waitcnt lgkmcnt(3)\n"
"ds_read_b128 v[48:51], v188 offset:2048\n"
"ds_read_b64_tr_b16 v[112:113], v182 offset:40584\n"
"v_mfma_f32_16x16x32_bf16 v[232:235], v[8:11], v[76:79], v[232:235]\n"
"v_cvt_pk_bf16_f32 v225, v226, v227\n"
"v_max_f32 v123, v123, v115\n"
"v_mfma_f32_16x16x32_bf16 v[240:243], v[16:19], v[76:79], v[240:243]\n"
"ds_read_b64_tr_b16 v[114:115], v183 offset:40584\n"
"v_cvt_pk_bf16_f32 v226, v228, v229\n"
"v_mfma_f32_16x16x32_bf16 v[236:239], v[12:15], v[76:79], v[236:239]\n"
"v_max3_f32 v170, v170, v120, v121\n"
"v_max3_f32 v171, v171, v122, v123\n"
"v_mfma_f32_16x16x32_bf16 v[204:207], a[208:211], v[52:55], v[204:207]\n"
"s_waitcnt lgkmcnt(4)\n"
"v_cvt_pk_bf16_f32 v227, v230, v231\n"
"v_max_f32 v124, v124, v116\n"
"v_mfma_f32_16x16x32_bf16 v[232:235], v[20:23], v[68:71], v[232:235]\n"
"s_add_u32 m0, s34, 3072\n"
"ds_write_b128 v186, v[224:227] offset:20384\n"
"v_mfma_f32_16x16x32_bf16 v[240:243], v[28:31], v[68:71], v[240:243]\n"
"buffer_load_dwordx4 v199, s[8:11], s75 offen lds\n"
"v_cvt_pk_bf16_f32 v222, v216, v217\n"
"v_mfma_f32_16x16x32_bf16 v[236:239], v[24:27], v[68:71], v[236:239]\n"
"v_max_f32 v125, v125, v117\n"
"v_max_f32 v126, v126, v118\n"
"v_mfma_f32_16x16x32_bf16 v[212:215], a[212:215], v[52:55], v[212:215]\n"
"ds_read_b128 v[52:55], v189 offset:2048\n"
"ds_read_b64_tr_b16 v[116:117], v182 offset:41352\n"
"v_mfma_f32_16x16x32_bf16 v[228:231], a[0:3], v[32:35], 0\n"
"v_cvt_pk_bf16_f32 v223, v218, v219\n"
"v_max_f32 v127, v127, v119\n"
"v_mfma_f32_16x16x32_bf16 v[216:219], a[4:7], v[32:35], 0\n"
"ds_write_b128 v186, v[220:223] offset:20320\n"
"v_mfma_f32_16x16x32_bf16 v[220:223], a[8:11], v[32:35], 0\n"
"v_max3_f32 v170, v170, v124, v125\n"
"v_max3_f32 v171, v171, v126, v127\n"
"v_mfma_f32_16x16x32_bf16 v[228:231], a[12:15], v[36:39], v[228:231]\n"
"v_mov_b64 v[252:253], 0\n"
"v_mov_b64 v[254:255], 0\n"
"v_mfma_f32_16x16x32_bf16 v[216:219], a[16:19], v[36:39], v[216:219]\n"
"ds_read_b64_tr_b16 v[118:119], v183 offset:41352\n"
"v_mov_b64 v[98:99], 0\n"
"v_mfma_f32_16x16x32_bf16 v[220:223], a[20:23], v[36:39], v[220:223]\n"
"v_mov_b64 v[100:101], 0\n"
"v_mov_b64 v[106:107], 0\n"
"v_mfma_f32_16x16x32_bf16 v[228:231], a[24:27], v[40:43], v[228:231]\n"
"s_waitcnt lgkmcnt(3)\n"
"v_mov_b64 v[108:109], 0\n"
"v_max_f32 v170, v170, v171\n"
"v_mfma_f32_16x16x32_bf16 v[216:219], a[28:31], v[40:43], v[216:219]\n"
"s_waitcnt vmcnt(11)\n"
"v_mov_b32 v171, v170\n"
"v_cvt_pk_bf16_f32 v232, v232, v233\n"
"v_mfma_f32_16x16x32_bf16 v[220:223], a[32:35], v[40:43], v[220:223]\n"
"s_add_u32 m0, s34, 1024\n"
"v_add3_u32 v200, v200, s50, -7\n"
"v_cmp_lt_u32 vcc, 64, v200\n"
"v_mfma_f32_16x16x32_bf16 v[228:231], a[36:39], v[44:47], v[228:231]\n"
"buffer_load_dwordx4 v190, s[8:11], s0 offen lds\n"
"v_permlane16_swap_b32 v170, v171\n"
"v_mfma_f32_16x16x32_bf16 v[216:219], a[40:43], v[44:47], v[216:219]\n"
"s_nop 0\n"
"s_cbranch_vccnz .Lp6%=\n"
".Lr6%=:\n"
"v_mfma_f32_16x16x32_bf16 v[220:223], a[44:47], v[44:47], v[220:223]\n"
"s_add_u32 s0, s0, s33\n"
"v_max_f32 v170, v170, v171\n"
"v_mov_b32 v171, v170\n"
"v_mfma_f32_16x16x32_bf16 v[228:231], a[48:51], v[48:51], v[228:231]\n"
"s_nop 0\n"
"v_permlane32_swap_b32 v170, v171\n"
"v_mfma_f32_16x16x32_bf16 v[216:219], a[52:55], v[48:51], v[216:219]\n"
"v_max_f32 v170, v170, v171\n"
"v_mul_f32 v170, 0xbe553b95, v170\n"
"v_mfma_f32_16x16x32_bf16 v[220:223], a[56:59], v[48:51], v[220:223]\n"
"v_fmamk_f32 v128, v128, 0x3e553b95, v170\n"
"v_fmamk_f32 v129, v129, 0x3e553b95, v170\n"
"v_mfma_f32_16x16x32_bf16 v[228:231], a[60:63], v[52:55], v[228:231]\n"
"v_fmamk_f32 v130, v130, 0x3e553b95, v170\n"
"v_fmamk_f32 v131, v131, 0x3e553b95, v170\n"
"v_mfma_f32_16x16x32_bf16 v[216:219], a[64:67], v[52:55], v[216:219]\n"
"v_exp_f32 v128, v128\n"
"v_mfma_f32_16x16x32_bf16 v[220:223], a[68:71], v[52:55], v[220:223]\n"
"v_exp_f32 v129, v129\n"
"v_mfma_f32_16x16x32_bf16 v[224:227], a[72:75], v[32:35], 0\n"
"v_exp_f32 v130, v130\n"
"v_mfma_f32_16x16x32_bf16 v[224:227], a[84:87], v[36:39], v[224:227]\n"
"s_add_u32 m0, s34, 4096\n"
"v_exp_f32 v131, v131\n"
"v_mfma_f32_16x16x32_bf16 v[224:227], a[96:99], v[40:43], v[224:227]\n"
"buffer_load_dwordx4 v199, s[8:11], s76 offen lds\n"
"v_cvt_pk_bf16_f32 v82, v128, v130\n"
"v_mfma_f32_16x16x32_bf16 v[224:227], a[108:111], v[44:47], v[224:227]\n"
"v_cvt_pk_bf16_f32 v83, v129, v131\n"
"v_fmamk_f32 v132, v132, 0x3e553b95, v170\n"
"v_mfma_f32_16x16x32_bf16 v[224:227], a[120:123], v[48:51], v[224:227]\n"
"ds_read_b64_tr_b16 v[128:129], v184 offset:128\n"
"ds_read_b64_tr_b16 v[130:131], v185 offset:128\n"
"v_mfma_f32_16x16x32_bf16 v[224:227], a[132:135], v[52:55], v[224:227]\n"
"v_fmamk_f32 v133, v133, 0x3e553b95, v170\n"
"v_fmamk_f32 v134, v134, 0x3e553b95, v170\n"
"v_fmamk_f32 v135, v135, 0x3e553b95, v170\n"
"v_exp_f32 v132, v132\n"
"v_exp_f32 v133, v133\n"
"v_exp_f32 v134, v134\n"
"v_exp_f32 v135, v135\n"
"v_cvt_pk_bf16_f32 v84, v132, v134\n"
"v_cvt_pk_bf16_f32 v85, v133, v135\n"
"v_cvt_pk_bf16_f32 v233, v234, v235\n"
"v_cvt_pk_bf16_f32 v240, v240, v241\n"
"v_smfmac_f32_16x16x64_bf16 v[252:255], v[82:85], v[136:143], v66\n"
"s_waitcnt lgkmcnt(2)\n"
"ds_read_b64_tr_b16 v[132:133], v184 offset:896\n"
"v_smfmac_f32_16x16x64_bf16 v[98:101], v[82:85], v[244:251], v66\n"
"ds_read_b64_tr_b16 v[134:135], v185 offset:896\n"
"v_smfmac_f32_16x16x64_bf16 v[106:109], v[82:85], v[112:119], v66\n"
"ds_read_b64_tr_b16 v[136:137], v184 offset:136\n"
"v_mfma_f32_16x16x32_bf16 v[82:85], v[172:175], v[82:85], 0\n"
"s_add_u32 m0, s34, 2048\n"
"ds_read_b64_tr_b16 v[138:139], v185 offset:136\n"
"buffer_load_dwordx4 v190, s[8:11], s1 offen lds\n"
"s_add_u32 s1, s1, s33\n"
"ds_read_b64_tr_b16 v[140:141], v184 offset:904\n"
"ds_read_b64_tr_b16 v[142:143], v185 offset:904\n"
"v_cvt_pk_bf16_f32 v234, v236, v237\n"
"v_cvt_pk_bf16_f32 v235, v238, v239\n"
"v_cvt_pk_bf16_f32 v241, v242, v243\n"
"v_cvt_pk_bf16_f32 v144, v208, v209\n"
"v_cvt_pk_bf16_f32 v145, v210, v211\n"
"v_mfma_f32_16x16x32_bf16 v[208:211], a[76:79], v[32:35], 0\n"
"ds_write_b128 v196, v[232:235] offset:49152\n"
"v_mfma_f32_16x16x32_bf16 v[208:211], a[88:91], v[36:39], v[208:211]\n"
"buffer_load_dword v200, off, s[28:31], s39 sc1\n"
"v_cvt_pk_bf16_f32 v146, v204, v205\n"
"v_mfma_f32_16x16x32_bf16 v[208:211], a[100:103], v[40:43], v[208:211]\n"
"v_cvt_pk_bf16_f32 v147, v206, v207\n"
"v_cvt_pk_bf16_f32 v158, v212, v213\n"
"v_mfma_f32_16x16x32_bf16 v[204:207], a[80:83], v[32:35], 0\n"
"ds_write_b64 v197, v[240:241] offset:49152\n"
"v_mfma_f32_16x16x32_bf16 v[204:207], a[92:95], v[36:39], v[204:207]\n"
"v_fmamk_f32 v120, v120, 0x3e553b95, v170\n"
"v_fmamk_f32 v121, v121, 0x3e553b95, v170\n"
"v_mfma_f32_16x16x32_bf16 v[204:207], a[104:107], v[40:43], v[204:207]\n"
"ds_read_b64_tr_b16 v[244:245], v184 offset:72\n"
"ds_read_b64_tr_b16 v[246:247], v185 offset:72\n"
"v_mfma_f32_16x16x32_bf16 v[208:211], a[112:115], v[44:47], v[208:211]\n"
"s_add_u32 m0, s34, 5120\n"
"ds_read_b64_tr_b16 v[248:249], v184 offset:840\n"
"buffer_load_dwordx4 v199, s[8:11], s77 offen lds\n"
"v_mfma_f32_16x16x32_bf16 v[204:207], a[116:119], v[44:47], v[204:207]\n"
"s_mul_i32 s8, s33, 0\n"
"ds_read_b64_tr_b16 v[250:251], v185 offset:840\n"
"v_cvt_pk_bf16_f32 v159, v214, v215\n"
"v_mfma_f32_16x16x32_bf16 v[208:211], a[124:127], v[48:51], v[208:211]\n"
"s_mul_i32 s9, s33, 1\n"
"v_fmamk_f32 v122, v122, 0x3e553b95, v170\n"
"v_fmamk_f32 v123, v123, 0x3e553b95, v170\n"
"v_mfma_f32_16x16x32_bf16 v[204:207], a[128:131], v[48:51], v[204:207]\n"
"s_mul_i32 s10, s33, 3\n"
"v_exp_f32 v120, v120\n"
"v_mfma_f32_16x16x32_bf16 v[208:211], a[136:139], v[52:55], v[208:211]\n"
"s_waitcnt lgkmcnt(4)\n"
"v_exp_f32 v121, v121\n"
"v_mfma_f32_16x16x32_bf16 v[204:207], a[140:143], v[52:55], v[204:207]\n"
"s_barrier\n"
"v_mfma_f32_16x16x32_bf16 v[212:215], a[144:147], v[32:35], 0\n"
"s_mul_i32 s11, s33, 2\n"
"ds_read_b128 v[68:71], v192 offset:30720\n"
"ds_read_b128 v[112:115], v180 offset:13504\n"
"v_mfma_f32_16x16x32_bf16 v[212:215], a[156:159], v[36:39], v[212:215]\n"
"v_exp_f32 v122, v122\n"
"v_mfma_f32_16x16x32_bf16 v[212:215], a[168:171], v[40:43], v[212:215]\n"
"v_exp_f32 v123, v123\n"
"v_mfma_f32_16x16x32_bf16 v[212:215], a[180:183], v[44:47], v[212:215]\n"
"v_cvt_pk_bf16_f32 v90, v120, v122\n"
"v_cvt_pk_bf16_f32 v91, v121, v123\n"
"v_mfma_f32_16x16x32_bf16 v[212:215], a[192:195], v[48:51], v[212:215]\n"
"v_fmamk_f32 v124, v124, 0x3e553b95, v170\n"
"v_fmamk_f32 v125, v125, 0x3e553b95, v170\n"
"v_mfma_f32_16x16x32_bf16 v[212:215], a[204:207], v[52:55], v[212:215]\n"
"v_fmamk_f32 v126, v126, 0x3e553b95, v170\n"
"v_fmamk_f32 v127, v127, 0x3e553b95, v170\n"
"v_exp_f32 v124, v124\n"
"v_exp_f32 v125, v125\n"
"v_exp_f32 v126, v126\n"
"v_exp_f32 v127, v127\n"
"v_cvt_pk_bf16_f32 v92, v124, v126\n"
"v_cvt_pk_bf16_f32 v93, v125, v127\n"
"v_cvt_pk_bf16_f32 v228, v228, v229\n"
"v_cvt_pk_bf16_f32 v229, v230, v231\n"
"v_smfmac_f32_16x16x64_bf16 v[252:255], v[90:93], v[128:135], v66\n"
"s_waitcnt vmcnt(11) lgkmcnt(0)\n"
"ds_read_b128 v[120:123], v181 offset:13504\n"
"v_smfmac_f32_16x16x64_bf16 v[98:101], v[90:93], v[136:143], v66\n"
"ds_read_b128 v[116:119], v180 offset:27008\n"
"v_smfmac_f32_16x16x64_bf16 v[106:109], v[90:93], v[244:251], v66\n"
"ds_read_b128 v[124:127], v181 offset:27008\n"
"v_mfma_f32_16x16x32_bf16 v[90:93], v[172:175], v[90:93], v[82:85]\n"
"ds_read_b128 v[128:131], v178\n"
"ds_read_b128 v[136:139], v179\n"
"v_mfma_f32_16x16x32_bf16 v[112:115], v[112:115], v[148:151], 0\n"
"ds_read_b64 v[82:83], v180 offset:13568\n"
"v_cvt_pk_bf16_f32 v252, v252, v253\n"
"v_mfma_f32_16x16x32_bf16 v[232:235], a[216:219], v[68:71], 0\n"
"ds_read_b64 v[84:85], v181 offset:13568\n"
"v_cvt_pk_bf16_f32 v253, v254, v255\n"
"v_mfma_f32_16x16x32_bf16 v[236:239], a[220:223], v[68:71], 0\n"
"ds_read_b64 v[92:93], v181 offset:27072\n"
"v_cvt_pk_bf16_f32 v98, v98, v99\n"
"v_mfma_f32_16x16x32_bf16 v[240:243], a[224:227], v[68:71], 0\n"
"ds_read_b128 v[68:71], v192 offset:33792\n"
"v_rcp_f32 v171, v90\n"
"v_cvt_pk_bf16_f32 v99, v100, v101\n"
"v_cvt_pk_bf16_f32 v171, v171, v171\n"
"v_and_b32 v64, v171, v176\n"
"v_and_b32 v65, v171, v177\n"
"v_cvt_pk_bf16_f32 v106, v106, v107\n"
"v_cvt_pk_bf16_f32 v107, v108, v109\n"
"v_mfma_f32_16x16x16_bf16 v[252:255], v[252:253], v[64:65], 0\n"
"s_waitcnt lgkmcnt(6)\n"
"ds_read_b64 v[90:91], v180 offset:27072\n"
"ds_write_b128 v202, v[56:59] offset:55296\n"
"v_mfma_f32_16x16x16_bf16 v[98:101], v[98:99], v[64:65], 0\n"
"s_waitcnt lgkmcnt(6)\n"
"v_cvt_pk_bf16_f32 v230, v216, v217\n"
"v_cvt_pk_bf16_f32 v231, v218, v219\n"
"v_mfma_f32_16x16x16_bf16 v[106:109], v[106:107], v[64:65], 0\n"
"s_waitcnt lgkmcnt(5)\n"
"v_cvt_pk_bf16_f32 v252, v252, v253\n"
"v_cvt_pk_bf16_f32 v253, v254, v255\n"
"v_mfma_f32_16x16x32_bf16 v[120:123], v[120:123], v[148:151], 0\n"
"s_waitcnt lgkmcnt(4)\n"
"ds_write_b128 v186, v[228:231] offset:33760\n"
"v_mfma_f32_16x16x32_bf16 v[112:115], v[80:83], v[160:163], v[112:115]\n"
"s_waitcnt vmcnt(7)\n"
"v_cvt_pk_bf16_f32 v254, v98, v99\n"
"v_cvt_pk_bf16_f32 v255, v100, v101\n"
"v_mfma_f32_16x16x32_bf16 v[120:123], v[84:87], v[162:165], v[120:123]\n"
"buffer_store_dwordx4 v[252:255], v192, s[28:31], s37 offen sc1\n"
"v_mfma_f32_16x16x32_bf16 v[116:119], v[116:119], v[148:151], 0\n"
"s_waitcnt lgkmcnt(2)\n"
"ds_read_b64 v[98:99], v178 offset:64\n"
"ds_read_b64 v[100:101], v179 offset:64\n"
"v_mfma_f32_16x16x32_bf16 v[124:127], v[124:127], v[148:151], 0\n"
"ds_read_b128 v[132:135], v178 offset:13504\n"
"v_cvt_pk_bf16_f32 v106, v106, v107\n"
"v_mfma_f32_16x16x32_bf16 v[116:119], v[88:91], v[160:163], v[116:119]\n"
"ds_read_b128 v[140:143], v179 offset:13504\n"
"v_cvt_pk_bf16_f32 v107, v108, v109\n"
"v_mfma_f32_16x16x32_bf16 v[124:127], v[92:95], v[162:165], v[124:127]\n"
"buffer_store_dwordx2 v[106:107], v193, s[28:31], s37 offen offset:1024 sc1\n"
"v_mfma_f32_16x16x32_bf16 v[128:131], v[128:131], v[148:151], 0\n"
"s_add_u32 s37, s37, 6144\n"
"ds_read_b64 v[106:107], v178 offset:13568\n"
"ds_read_b64 v[108:109], v179 offset:13568\n"
"v_mfma_f32_16x16x32_bf16 v[136:139], v[136:139], v[148:151], 0\n"
"ds_read_b128 v[72:75], v202 offset:49152\n"
"v_cvt_pk_bf16_f32 v220, v220, v221\n"
"v_mfma_f32_16x16x32_bf16 v[216:219], a[148:151], v[32:35], 0\n"
"ds_read_b64_tr_b16 v[244:245], v182 offset:54144\n"
"v_cvt_pk_bf16_f32 v221, v222, v223\n"
"v_mfma_f32_16x16x32_bf16 v[228:231], a[152:155], v[32:35], 0\n"
"s_waitcnt lgkmcnt(7)\n"
"ds_read_b128 v[32:35], v188 offset:6144\n"
"v_max_f32 v112, v112, v120\n"
"v_mfma_f32_16x16x32_bf16 v[216:219], a[160:163], v[36:39], v[216:219]\n"
"s_waitcnt lgkmcnt(7)\n"
"ds_read_b64_tr_b16 v[246:247], v183 offset:54144\n"
"v_max_f32 v113, v113, v121\n"
"v_mfma_f32_16x16x32_bf16 v[128:131], v[96:99], v[160:163], v[128:131]\n"
"s_waitcnt lgkmcnt(7)\n"
"ds_read_b64_tr_b16 v[248:249], v182 offset:54912\n"
"v_max_f32 v114, v114, v122\n"
"v_mfma_f32_16x16x32_bf16 v[136:139], v[100:103], v[162:165], v[136:139]\n"
"s_waitcnt lgkmcnt(7)\n"
"ds_read_b128 v[76:79], v202 offset:53248\n"
"v_max_f32 v115, v115, v123\n"
"v_mfma_f32_16x16x32_bf16 v[132:135], v[132:135], v[148:151], 0\n"
"ds_read_b64_tr_b16 v[250:251], v183 offset:54912\n"
"v_max_f32 v170, v112, v113\n"
"v_mfma_f32_16x16x32_bf16 v[140:143], v[140:143], v[148:151], 0\n"
"s_waitcnt lgkmcnt(8)\n"
"ds_read_b64_tr_b16 v[120:121], v182 offset:54152\n"
"v_max_f32 v171, v114, v115\n"
"v_mfma_f32_16x16x32_bf16 v[132:135], v[104:107], v[160:163], v[132:135]\n"
"s_waitcnt lgkmcnt(8)\n"
"ds_read_b64_tr_b16 v[122:123], v183 offset:54152\n"
"v_max_f32 v116, v116, v124\n"
"v_mfma_f32_16x16x32_bf16 v[140:143], v[108:111], v[162:165], v[140:143]\n"
"s_waitcnt lgkmcnt(8)\n"
"buffer_store_dwordx4 v[72:75], v194, s[24:27], s36 offen sc1\n"
"v_mfma_f32_16x16x32_bf16 v[228:231], a[164:167], v[36:39], v[228:231]\n"
"s_nop 0\n"
"ds_read_b128 v[72:75], v192 offset:31744\n"
"ds_read_b128 v[36:39], v189 offset:6144\n"
"v_mfma_f32_16x16x32_bf16 v[216:219], a[172:175], v[40:43], v[216:219]\n"
"ds_write_b64 v203, v[60:61] offset:55296\n"
"v_mfma_f32_16x16x32_bf16 v[228:231], a[176:179], v[40:43], v[228:231]\n"
"v_max_f32 v117, v117, v125\n"
"v_max_f32 v118, v118, v126\n"
"v_mfma_f32_16x16x32_bf16 v[216:219], a[184:187], v[44:47], v[216:219]\n"
"s_waitcnt lgkmcnt(6)\n"
"ds_read_b128 v[40:43], v188 offset:7168\n"
"buffer_store_dwordx4 v[76:79], v195, s[24:27], s36 offen sc1\n"
"v_mfma_f32_16x16x32_bf16 v[228:231], a[188:191], v[44:47], v[228:231]\n"
"s_add_u32 s36, s36, s33\n"
"ds_read_b128 v[76:79], v192 offset:32768\n"
"ds_read_b128 v[44:47], v189 offset:7168\n"
"v_mfma_f32_16x16x32_bf16 v[216:219], a[196:199], v[48:51], v[216:219]\n"
"ds_read_b64_tr_b16 v[124:125], v182 offset:54920\n"
"v_max_f32 v119, v119, v127\n"
"v_mfma_f32_16x16x32_bf16 v[228:231], a[200:203], v[48:51], v[228:231]\n"
"s_waitcnt lgkmcnt(6)\n"
"buffer_load_dwordx4 a[198:201], v192, s[28:31], s38 offen sc1\n"
"ds_read_b128 v[48:51], v188 offset:8192\n"
"v_mfma_f32_16x16x32_bf16 v[216:219], a[208:211], v[52:55], v[216:219]\n"
"ds_read_b64_tr_b16 v[126:127], v183 offset:54920\n"
"v_max3_f32 v170, v170, v116, v117\n"
"v_mfma_f32_16x16x32_bf16 v[232:235], a[228:231], v[72:75], v[232:235]\n"
"v_max3_f32 v171, v171, v118, v119\n"
"v_max_f32 v128, v128, v136\n"
"v_mfma_f32_16x16x32_bf16 v[236:239], a[232:235], v[72:75], v[236:239]\n"
"v_max_f32 v129, v129, v137\n"
"v_max_f32 v130, v130, v138\n"
"v_mfma_f32_16x16x32_bf16 v[240:243], a[236:239], v[72:75], v[240:243]\n"
"buffer_load_dwordx2 a[202:203], v193, s[28:31], s38 offen offset:1024 sc1\n"
"ds_read_b128 v[72:75], v192 offset:34816\n"
"v_mfma_f32_16x16x32_bf16 v[228:231], a[212:215], v[52:55], v[228:231]\n"
"s_add_u32 s38, s38, 6144\n"
"ds_read_b128 v[52:55], v189 offset:8192\n"
"ds_read_b64_tr_b16 v[136:137], v182 offset:54088\n"
"v_max_f32 v131, v131, v139\n"
"v_cvt_pk_bf16_f32 v224, v224, v225\n"
"v_max3_f32 v170, v170, v128, v129\n"
"v_max3_f32 v171, v171, v130, v131\n"
"v_cvt_pk_bf16_f32 v225, v226, v227\n"
"v_max_f32 v132, v132, v140\n"
"v_cvt_pk_bf16_f32 v226, v208, v209\n"
"v_max_f32 v133, v133, v141\n"
"v_max_f32 v134, v134, v142\n"
"v_cvt_pk_bf16_f32 v227, v210, v211\n"
"v_mfma_f32_16x16x32_bf16 v[208:211], a[0:3], v[32:35], 0\n"
"s_waitcnt lgkmcnt(7)\n"
"buffer_load_dwordx4 a[204:207], v192, s[28:31], s38 offen sc1\n"
"ds_read_b64_tr_b16 v[138:139], v183 offset:54088\n"
"v_mfma_f32_16x16x32_bf16 v[232:235], a[240:243], v[76:79], v[232:235]\n"
"ds_read_b64_tr_b16 v[140:141], v182 offset:54856\n"
"ds_write_b128 v186, v[224:227] offset:33888\n"
"v_mfma_f32_16x16x32_bf16 v[236:239], a[244:247], v[76:79], v[236:239]\n"
"v_max_f32 v135, v135, v143\n"
"v_cvt_pk_bf16_f32 v222, v204, v205\n"
"v_mfma_f32_16x16x32_bf16 v[240:243], a[248:251], v[76:79], v[240:243]\n"
"buffer_load_dwordx2 a[208:209], v193, s[28:31], s38 offen offset:1024 sc1\n"
"ds_read_b128 v[76:79], v192 offset:35840\n"
"v_mfma_f32_16x16x32_bf16 v[232:235], a[252:255], v[68:71], v[232:235]\n"
"s_waitcnt lgkmcnt(6)\n"
"ds_read_b64_tr_b16 v[142:143], v183 offset:54856\n"
"v_max3_f32 v170, v170, v132, v133\n"
"v_mfma_f32_16x16x32_bf16 v[240:243], v[4:7], v[68:71], v[240:243]\n"
"s_add_u32 s38, s38, 6144\n"
"v_max3_f32 v171, v171, v134, v135\n"
"v_cvt_pk_bf16_f32 v223, v206, v207\n"
"v_mfma_f32_16x16x32_bf16 v[236:239], v[0:3], v[68:71], v[236:239]\n"
"ds_write_b128 v186, v[220:223] offset:33824\n"
"v_mfma_f32_16x16x32_bf16 v[232:235], v[8:11], v[72:75], v[232:235]\n"
"s_mov_b64 exec, s[46:47]\n"
"v_add_u32 v183, 0xc0, v183\n"
"s_mov_b64 exec, -1\n"
"v_mfma_f32_16x16x32_bf16 v[240:243], v[16:19], v[72:75], v[240:243]\n"
"v_mov_b64 v[252:253], 0\n"
"v_mov_b64 v[254:255], 0\n"
"v_mfma_f32_16x16x32_bf16 v[236:239], v[12:15], v[72:75], v[236:239]\n"
"v_mov_b64 v[98:99], 0\n"
"v_mov_b64 v[100:101], 0\n"
"v_mfma_f32_16x16x32_bf16 v[204:207], a[4:7], v[32:35], 0\n"
"s_waitcnt lgkmcnt(2)\n"
"v_mov_b64 v[106:107], 0\n"
"v_mov_b64 v[108:109], 0\n"
"v_mfma_f32_16x16x32_bf16 v[232:235], v[20:23], v[76:79], v[232:235]\n"
"v_max_f32 v170, v170, v171\n"
"v_mov_b32 v171, v170\n"
"v_mfma_f32_16x16x32_bf16 v[240:243], v[28:31], v[76:79], v[240:243]\n"
"s_nop 0\n"
"v_permlane16_swap_b32 v170, v171\n"
"v_mfma_f32_16x16x32_bf16 v[236:239], v[24:27], v[76:79], v[236:239]\n"
"v_max_f32 v170, v170, v171\n"
"v_mov_b32 v171, v170\n"
"v_mfma_f32_16x16x32_bf16 v[220:223], a[8:11], v[32:35], 0\n"
"s_nop 0\n"
"v_permlane32_swap_b32 v170, v171\n"
"v_mfma_f32_16x16x32_bf16 v[208:211], a[12:15], v[36:39], v[208:211]\n"
"v_max_f32 v170, v170, v171\n"
"v_mul_f32 v170, 0xbe553b95, v170\n"
"v_mfma_f32_16x16x32_bf16 v[204:207], a[16:19], v[36:39], v[204:207]\n"
"v_fmamk_f32 v112, v112, 0x3e553b95, v170\n"
"v_fmamk_f32 v113, v113, 0x3e553b95, v170\n"
"v_mfma_f32_16x16x32_bf16 v[220:223], a[20:23], v[36:39], v[220:223]\n"
"v_fmamk_f32 v114, v114, 0x3e553b95, v170\n"
"v_fmamk_f32 v115, v115, 0x3e553b95, v170\n"
"v_mfma_f32_16x16x32_bf16 v[208:211], a[24:27], v[40:43], v[208:211]\n"
"v_exp_f32 v112, v112\n"
"v_mfma_f32_16x16x32_bf16 v[204:207], a[28:31], v[40:43], v[204:207]\n"
"v_exp_f32 v113, v113\n"
"v_mfma_f32_16x16x32_bf16 v[220:223], a[32:35], v[40:43], v[220:223]\n"
"v_exp_f32 v114, v114\n"
"v_mfma_f32_16x16x32_bf16 v[208:211], a[36:39], v[44:47], v[208:211]\n"
"v_exp_f32 v115, v115\n"
"v_mfma_f32_16x16x32_bf16 v[204:207], a[40:43], v[44:47], v[204:207]\n"
"v_cvt_pk_bf16_f32 v82, v112, v114\n"
"v_cvt_pk_bf16_f32 v83, v113, v115\n"
"v_mfma_f32_16x16x32_bf16 v[220:223], a[44:47], v[44:47], v[220:223]\n"
"s_waitcnt vmcnt(9)\n"
"ds_read_b64_tr_b16 v[112:113], v182 offset:128\n"
"v_add3_u32 v200, v200, s50, -8\n"
"v_mfma_f32_16x16x32_bf16 v[208:211], a[48:51], v[48:51], v[208:211]\n"
"v_cmp_lt_u32 vcc, 64, v200\n"
"v_fmamk_f32 v116, v116, 0x3e553b95, v170\n"
"v_mfma_f32_16x16x32_bf16 v[204:207], a[52:55], v[48:51], v[204:207]\n"
"ds_read_b64_tr_b16 v[114:115], v183 offset:128\n"
"v_fmamk_f32 v117, v117, 0x3e553b95, v170\n"
"v_mfma_f32_16x16x32_bf16 v[220:223], a[56:59], v[48:51], v[220:223]\n"
"s_cbranch_vccnz .Lp7%=\n"
".Lr7%=:\n"
"v_mfma_f32_16x16x32_bf16 v[208:211], a[60:63], v[52:55], v[208:211]\n"
"buffer_load_dwordx4 a[210:213], v192, s[28:31], s38 offen sc1\n"
"v_fmamk_f32 v118, v118, 0x3e553b95, v170\n"
"v_mfma_f32_16x16x32_bf16 v[204:207], a[64:67], v[52:55], v[204:207]\n"
"v_fmamk_f32 v119, v119, 0x3e553b95, v170\n"
"v_exp_f32 v116, v116\n"
"v_mfma_f32_16x16x32_bf16 v[220:223], a[68:71], v[52:55], v[220:223]\n"
"v_exp_f32 v117, v117\n"
"v_mfma_f32_16x16x32_bf16 v[224:227], a[72:75], v[32:35], 0\n"
"buffer_load_dwordx2 a[214:215], v193, s[28:31], s38 offen offset:1024 sc1\n"
"v_cvt_pk_bf16_f32 v232, v232, v233\n"
"v_mfma_f32_16x16x32_bf16 v[224:227], a[84:87], v[36:39], v[224:227]\n"
"s_add_u32 s38, s38, 6144\n"
"v_exp_f32 v118, v118\n"
"v_mfma_f32_16x16x32_bf16 v[224:227], a[96:99], v[40:43], v[224:227]\n"
"s_add_u32 s12, s38, 0\n"
"v_exp_f32 v119, v119\n"
"v_mfma_f32_16x16x32_bf16 v[224:227], a[108:111], v[44:47], v[224:227]\n"
"s_add_u32 s13, s38, 6144\n"
"v_cvt_pk_bf16_f32 v84, v116, v118\n"
"v_cvt_pk_bf16_f32 v85, v117, v119\n"
"v_mfma_f32_16x16x32_bf16 v[224:227], a[120:123], v[48:51], v[224:227]\n"
"s_waitcnt lgkmcnt(3)\n"
"ds_read_b64_tr_b16 v[116:117], v182 offset:896\n"
"ds_read_b64_tr_b16 v[118:119], v183 offset:896\n"
"v_smfmac_f32_16x16x64_bf16 v[252:255], v[82:85], v[244:251], v66\n"
"s_add_u32 s15, s38, 12288\n"
"ds_read_b64_tr_b16 v[244:245], v182 offset:136\n"
"v_smfmac_f32_16x16x64_bf16 v[98:101], v[82:85], v[120:127], v66\n"
"s_add_u32 s14, s38, 18432\n"
"ds_read_b64_tr_b16 v[246:247], v183 offset:136\n"
"v_smfmac_f32_16x16x64_bf16 v[106:109], v[82:85], v[136:143], v66\n"
"s_add_u32 s16, s38, 24576\n"
"ds_read_b64_tr_b16 v[248:249], v182 offset:904\n"
"v_mfma_f32_16x16x32_bf16 v[82:85], v[172:175], v[82:85], 0\n"
"s_add_u32 s17, s38, 30720\n"
".Lic6%=:\n"
"s_cbranch_execz .Lib1%=\n"
"ds_read_b64_tr_b16 v[250:251], v183 offset:904\n"
"v_cvt_pk_bf16_f32 v233, v234, v235\n"
"v_mfma_f32_16x16x32_bf16 v[224:227], a[132:135], v[52:55], v[224:227]\n"
"v_cvt_pk_bf16_f32 v240, v240, v241\n"
"v_cvt_pk_bf16_f32 v234, v236, v237\n"
"v_cvt_pk_bf16_f32 v235, v238, v239\n"
"v_cvt_pk_bf16_f32 v241, v242, v243\n"
"v_cvt_pk_bf16_f32 v148, v212, v213\n"
"v_cvt_pk_bf16_f32 v149, v214, v215\n"
"v_mfma_f32_16x16x32_bf16 v[212:215], a[76:79], v[32:35], 0\n"
"ds_write_b128 v196, v[232:235] offset:36864\n"
"v_mfma_f32_16x16x32_bf16 v[212:215], a[88:91], v[36:39], v[212:215]\n"
"v_cvt_pk_bf16_f32 v150, v216, v217\n"
"v_cvt_pk_bf16_f32 v151, v218, v219\n"
"v_mfma_f32_16x16x32_bf16 v[216:219], a[80:83], v[32:35], 0\n"
"s_waitcnt vmcnt(8)\n"
"v_cvt_pk_bf16_f32 v162, v228, v229\n"
"v_or_b32 v201, 10, s6\n"
"v_mfma_f32_16x16x32_bf16 v[216:219], a[92:95], v[36:39], v[216:219]\n"
"s_mov_b64 exec, 1\n"
"buffer_store_dword v201, off, s[28:31], s40 sc1\n"
"s_mov_b64 exec, -1\n"
"v_mfma_f32_16x16x32_bf16 v[212:215], a[100:103], v[40:43], v[212:215]\n"
"ds_write_b64 v197, v[240:241] offset:36864\n"
"v_mfma_f32_16x16x32_bf16 v[216:219], a[104:107], v[40:43], v[216:219]\n"
"v_fmamk_f32 v128, v128, 0x3e553b95, v170\n"
"v_fmamk_f32 v129, v129, 0x3e553b95, v170\n"
"v_mfma_f32_16x16x32_bf16 v[212:215], a[112:115], v[44:47], v[212:215]\n"
"ds_read_b128 v[32:35], v188\n"
"ds_read_b64_tr_b16 v[120:121], v182 offset:72\n"
"v_mfma_f32_16x16x32_bf16 v[216:219], a[116:119], v[44:47], v[216:219]\n"
"ds_read_b64_tr_b16 v[122:123], v183 offset:72\n"
"v_cvt_pk_bf16_f32 v163, v230, v231\n"
"v_mfma_f32_16x16x32_bf16 v[212:215], a[124:127], v[48:51], v[212:215]\n"
"ds_read_b64_tr_b16 v[124:125], v182 offset:840\n"
"v_fmamk_f32 v130, v130, 0x3e553b95, v170\n"
"v_mfma_f32_16x16x32_bf16 v[216:219], a[128:131], v[48:51], v[216:219]\n"
"ds_read_b128 v[36:39], v189\n"
"v_fmamk_f32 v131, v131, 0x3e553b95, v170\n"
"v_mfma_f32_16x16x32_bf16 v[212:215], a[136:139], v[52:55], v[212:215]\n"
"ds_read_b64_tr_b16 v[126:127], v183 offset:840\n"
"v_fmamk_f32 v132, v132, 0x3e553b95, v170\n"
"v_mfma_f32_16x16x32_bf16 v[216:219], a[140:143], v[52:55], v[216:219]\n"
"s_waitcnt lgkmcnt(6)\n"
"ds_read_b128 v[40:43], v188 offset:1024\n"
"v_exp_f32 v128, v128\n"
"v_exp_f32 v129, v129\n"
"v_exp_f32 v130, v130\n"
"v_exp_f32 v131, v131\n"
"v_cvt_pk_bf16_f32 v90, v128, v130\n"
"v_cvt_pk_bf16_f32 v91, v129, v131\n"
"v_fmamk_f32 v133, v133, 0x3e553b95, v170\n"
"v_fmamk_f32 v134, v134, 0x3e553b95, v170\n"
"v_fmamk_f32 v135, v135, 0x3e553b95, v170\n"
"v_exp_f32 v132, v132\n"
"v_exp_f32 v133, v133\n"
"v_exp_f32 v134, v134\n"
"v_exp_f32 v135, v135\n"
"v_cvt_pk_bf16_f32 v92, v132, v134\n"
"v_cvt_pk_bf16_f32 v93, v133, v135\n"
"v_cvt_pk_bf16_f32 v208, v208, v209\n"
"v_cvt_pk_bf16_f32 v209, v210, v211\n"
"v_smfmac_f32_16x16x64_bf16 v[252:255], v[90:93], v[112:119], v66\n"
"s_waitcnt lgkmcnt(6)\n"
"ds_read_b128 v[44:47], v189 offset:1024\n"
"v_smfmac_f32_16x16x64_bf16 v[98:101], v[90:93], v[244:251], v66\n"
"s_waitcnt lgkmcnt(2)\n"
"ds_read_b128 v[48:51], v188 offset:2048\n"
"v_smfmac_f32_16x16x64_bf16 v[106:109], v[90:93], v[120:127], v66\n"
"s_barrier\n"
"v_mfma_f32_16x16x32_bf16 v[90:93], v[172:175], v[90:93], v[82:85]\n"
"ds_read_b128 v[76:79], v192 offset:55296\n"
"ds_read_b128 v[52:55], v189 offset:2048\n"
"v_mfma_f32_16x16x32_bf16 v[228:231], a[0:3], v[32:35], 0\n"
"ds_read_b128 v[136:139], v180 offset:27008\n"
"v_cvt_pk_bf16_f32 v252, v252, v253\n"
"v_mfma_f32_16x16x32_bf16 v[228:231], a[12:15], v[36:39], v[228:231]\n"
"s_waitcnt lgkmcnt(5)\n"
"ds_read_b128 v[128:131], v181 offset:27008\n"
"v_cvt_pk_bf16_f32 v253, v254, v255\n"
"v_mfma_f32_16x16x32_bf16 v[228:231], a[24:27], v[40:43], v[228:231]\n"
"ds_read_b64 v[82:83], v180 offset:27072\n"
"v_rcp_f32 v171, v90\n"
"v_cvt_pk_bf16_f32 v98, v98, v99\n"
"v_cvt_pk_bf16_f32 v171, v171, v171\n"
"v_and_b32 v64, v171, v176\n"
"v_and_b32 v65, v171, v177\n"
"v_cvt_pk_bf16_f32 v99, v100, v101\n"
"v_cvt_pk_bf16_f32 v106, v106, v107\n"
"v_mfma_f32_16x16x16_bf16 v[252:255], v[252:253], v[64:65], 0\n"
"s_waitcnt lgkmcnt(6)\n"
"ds_read_b64 v[84:85], v181 offset:27072\n"
"ds_read_b128 v[140:143], v178\n"
"v_mfma_f32_16x16x16_bf16 v[98:101], v[98:99], v[64:65], 0\n"
"ds_read_b128 v[132:135], v179\n"
"v_cvt_pk_bf16_f32 v107, v108, v109\n"
"v_mfma_f32_16x16x32_bf16 v[228:231], a[36:39], v[44:47], v[228:231]\n"
"s_waitcnt lgkmcnt(7)\n"
"ds_read_b64 v[90:91], v178 offset:64\n"
"v_cvt_pk_bf16_f32 v252, v252, v253\n"
"v_mfma_f32_16x16x16_bf16 v[106:109], v[106:107], v[64:65], 0\n"
"ds_read_b64 v[92:93], v179 offset:64\n"
"v_cvt_pk_bf16_f32 v253, v254, v255\n"
"v_mfma_f32_16x16x32_bf16 v[232:235], a[216:219], v[76:79], 0\n"
"s_waitcnt lgkmcnt(7)\n"
"ds_read_b128 v[112:115], v178 offset:13504\n"
"v_cvt_pk_bf16_f32 v254, v98, v99\n"
"v_mfma_f32_16x16x32_bf16 v[136:139], v[136:139], v[152:155], 0\n"
"s_waitcnt lgkmcnt(7)\n"
"ds_read_b128 v[244:247], v179 offset:13504\n"
"v_cvt_pk_bf16_f32 v255, v100, v101\n"
"v_mfma_f32_16x16x32_bf16 v[128:131], v[128:131], v[152:155], 0\n"
"s_waitcnt lgkmcnt(7)\n"
"buffer_store_dwordx4 v[252:255], v192, s[28:31], s37 offen sc1\n"
"v_mfma_f32_16x16x32_bf16 v[136:139], v[80:83], v[164:167], v[136:139]\n"
"s_waitcnt lgkmcnt(6)\n"
"ds_read_b64 v[98:99], v178 offset:13568\n"
"ds_read_b64 v[100:101], v179 offset:13568\n"
"v_mfma_f32_16x16x32_bf16 v[128:131], v[84:87], v[166:169], v[128:131]\n"
"s_waitcnt lgkmcnt(7)\n"
"ds_read_b128 v[116:119], v178 offset:27008\n"
"v_cvt_pk_bf16_f32 v106, v106, v107\n"
"v_mfma_f32_16x16x32_bf16 v[140:143], v[140:143], v[152:155], 0\n"
"s_waitcnt lgkmcnt(7)\n"
"ds_read_b128 v[248:251], v179 offset:27008\n"
"v_cvt_pk_bf16_f32 v107, v108, v109\n"
"v_mfma_f32_16x16x32_bf16 v[132:135], v[132:135], v[152:155], 0\n"
"s_waitcnt lgkmcnt(7)\n"
"buffer_store_dwordx2 v[106:107], v193, s[28:31], s37 offen offset:1024 sc1\n"
"v_mfma_f32_16x16x32_bf16 v[140:143], v[88:91], v[164:167], v[140:143]\n"
"s_waitcnt lgkmcnt(6)\n"
"ds_read_b64 v[106:107], v178 offset:27072\n"
"ds_read_b64 v[108:109], v179 offset:27072\n"
"v_mfma_f32_16x16x32_bf16 v[132:135], v[92:95], v[166:169], v[132:135]\n"
"s_waitcnt vmcnt(8) lgkmcnt(7)\n"
"ds_read_b128 v[68:71], v202 offset:36864\n"
"v_cvt_pk_bf16_f32 v210, v204, v205\n"
"v_mfma_f32_16x16x32_bf16 v[112:115], v[112:115], v[152:155], 0\n"
"s_waitcnt lgkmcnt(7)\n"
"ds_write_b128 v202, a[198:201] offset:30720\n"
"v_mfma_f32_16x16x32_bf16 v[244:247], v[244:247], v[152:155], 0\n"
"s_waitcnt lgkmcnt(7)\n"
"v_cvt_pk_bf16_f32 v211, v206, v207\n"
"v_cvt_pk_bf16_f32 v220, v220, v221\n"
"v_mfma_f32_16x16x32_bf16 v[112:115], v[96:99], v[164:167], v[112:115]\n"
"s_waitcnt lgkmcnt(6)\n"
"v_cvt_pk_bf16_f32 v221, v222, v223\n"
"v_max_f32 v136, v136, v128\n"
"v_mfma_f32_16x16x32_bf16 v[244:247], v[100:103], v[166:169], v[244:247]\n"
"s_waitcnt vmcnt(7) lgkmcnt(5)\n"
"ds_write_b128 v186, v[208:211] offset:47264\n"
"v_mfma_f32_16x16x32_bf16 v[116:119], v[116:119], v[152:155], 0\n"
"s_waitcnt lgkmcnt(4)\n"
"v_max_f32 v137, v137, v129\n"
"v_max_f32 v138, v138, v130\n"
"v_mfma_f32_16x16x32_bf16 v[248:251], v[248:251], v[152:155], 0\n"
"s_waitcnt lgkmcnt(3)\n"
"v_max_f32 v139, v139, v131\n"
"v_max_f32 v170, v136, v137\n"
"v_mfma_f32_16x16x32_bf16 v[116:119], v[104:107], v[164:167], v[116:119]\n"
"s_waitcnt lgkmcnt(2)\n"
"ds_read_b64_tr_b16 v[120:121], v184 offset:128\n"
"ds_read_b64_tr_b16 v[122:123], v185 offset:128\n"
"v_mfma_f32_16x16x32_bf16 v[248:251], v[108:111], v[166:169], v[248:251]\n"
"s_add_u32 s37, s37, 6144\n"
"buffer_store_dwordx4 v[68:71], v194, s[24:27], s36 offen sc1\n"
"v_mfma_f32_16x16x32_bf16 v[236:239], a[220:223], v[76:79], 0\n"
"s_nop 0\n"
"ds_read_b128 v[68:71], v192 offset:56320\n"
"ds_read_b64_tr_b16 v[124:125], v184 offset:896\n"
"v_mfma_f32_16x16x32_bf16 v[240:243], a[224:227], v[76:79], 0\n"
"ds_read_b128 v[76:79], v192 offset:58368\n"
"v_max_f32 v171, v138, v139\n"
"v_mfma_f32_16x16x32_bf16 v[204:207], a[4:7], v[32:35], 0\n"
"ds_read_b128 v[72:75], v202 offset:40960\n"
"v_max_f32 v140, v140, v132\n"
"v_mfma_f32_16x16x32_bf16 v[208:211], a[8:11], v[32:35], 0\n"
"ds_read_b64_tr_b16 v[126:127], v185 offset:896\n"
"v_max_f32 v141, v141, v133\n"
"v_mfma_f32_16x16x32_bf16 v[204:207], a[16:19], v[36:39], v[204:207]\n"
"ds_read_b64_tr_b16 v[128:129], v184 offset:136\n"
"v_max_f32 v142, v142, v134\n"
"v_mfma_f32_16x16x32_bf16 v[208:211], a[20:23], v[36:39], v[208:211]\n"
"ds_read_b64_tr_b16 v[130:131], v185 offset:136\n"
"v_max_f32 v143, v143, v135\n"
"v_mfma_f32_16x16x32_bf16 v[204:207], a[28:31], v[40:43], v[204:207]\n"
"ds_read_b64_tr_b16 v[132:133], v184 offset:904\n"
"v_max3_f32 v170, v170, v140, v141\n"
"v_mfma_f32_16x16x32_bf16 v[208:211], a[32:35], v[40:43], v[208:211]\n"
"ds_read_b64_tr_b16 v[134:135], v185 offset:904\n"
"v_max3_f32 v171, v171, v142, v143\n"
"v_mfma_f32_16x16x32_bf16 v[204:207], a[40:43], v[44:47], v[204:207]\n"
"s_waitcnt lgkmcnt(8)\n"
"ds_write_b64 v203, a[202:203] offset:30720\n"
"v_mfma_f32_16x16x32_bf16 v[232:235], a[228:231], v[68:71], v[232:235]\n"
"v_max_f32 v112, v112, v244\n"
"v_max_f32 v113, v113, v245\n"
"v_mfma_f32_16x16x32_bf16 v[236:239], a[232:235], v[68:71], v[236:239]\n"
"s_waitcnt lgkmcnt(6)\n"
"ds_read_b64_tr_b16 v[244:245], v184 offset:72\n"
"v_max_f32 v114, v114, v246\n"
"v_mfma_f32_16x16x32_bf16 v[240:243], a[236:239], v[68:71], v[240:243]\n"
"ds_read_b128 v[68:71], v192 offset:59392\n"
"buffer_store_dwordx4 v[72:75], v195, s[24:27], s36 offen sc1\n"
"v_mfma_f32_16x16x32_bf16 v[208:211], a[44:47], v[44:47], v[208:211]\n"
"s_add_u32 s36, s36, s33\n"
"ds_read_b128 v[72:75], v192 offset:57344\n"
"v_max_f32 v115, v115, v247\n"
"v_mfma_f32_16x16x32_bf16 v[228:231], a[48:51], v[48:51], v[228:231]\n"
"ds_read_b64_tr_b16 v[246:247], v185 offset:72\n"
"v_max3_f32 v170, v170, v112, v113\n"
"v_mfma_f32_16x16x32_bf16 v[204:207], a[52:55], v[48:51], v[204:207]\n"
"v_max3_f32 v171, v171, v114, v115\n"
"v_max_f32 v116, v116, v248\n"
"v_mfma_f32_16x16x32_bf16 v[208:211], a[56:59], v[48:51], v[208:211]\n"
"v_max_f32 v117, v117, v249\n"
"v_max_f32 v118, v118, v250\n"
"v_mfma_f32_16x16x32_bf16 v[228:231], a[60:63], v[52:55], v[228:231]\n"
"ds_read_b64_tr_b16 v[248:249], v184 offset:840\n"
"v_max_f32 v119, v119, v251\n"
"v_mfma_f32_16x16x32_bf16 v[204:207], a[64:67], v[52:55], v[204:207]\n"
"ds_read_b64_tr_b16 v[250:251], v185 offset:840\n"
"v_max3_f32 v170, v170, v116, v117\n"
"v_mfma_f32_16x16x32_bf16 v[208:211], a[68:71], v[52:55], v[208:211]\n"
"v_max3_f32 v171, v171, v118, v119\n"
"v_mov_b64 v[252:253], 0\n"
"v_mov_b64 v[254:255], 0\n"
"v_mov_b64 v[98:99], 0\n"
"v_mov_b64 v[100:101], 0\n"
"v_mov_b64 v[106:107], 0\n"
"v_mov_b64 v[108:109], 0\n"
"v_max_f32 v170, v170, v171\n"
"v_mov_b32 v171, v170\n"
"v_cvt_pk_bf16_f32 v224, v224, v225\n"
"v_cvt_pk_bf16_f32 v225, v226, v227\n"
"v_permlane16_swap_b32 v170, v171\n"
"v_max_f32 v170, v170, v171\n"
"v_mov_b32 v171, v170\n"
"v_cvt_pk_bf16_f32 v226, v212, v213\n"
"v_cvt_pk_bf16_f32 v227, v214, v215\n"
"v_mfma_f32_16x16x32_bf16 v[212:215], a[72:75], v[32:35], 0\n"
"s_waitcnt lgkmcnt(3)\n"
"v_permlane32_swap_b32 v170, v171\n"
"v_mfma_f32_16x16x32_bf16 v[232:235], a[240:243], v[72:75], v[232:235]\n"
"ds_write_b128 v186, v[224:227] offset:47392\n"
"v_mfma_f32_16x16x32_bf16 v[236:239], a[244:247], v[72:75], v[236:239]\n"
"v_max_f32 v170, v170, v171\n"
"v_mul_f32 v170, 0xbe553b95, v170\n"
"v_mfma_f32_16x16x32_bf16 v[240:243], a[248:251], v[72:75], v[240:243]\n"
"v_fmamk_f32 v136, v136, 0x3e553b95, v170\n"
"v_fmamk_f32 v137, v137, 0x3e553b95, v170\n"
"v_mfma_f32_16x16x32_bf16 v[232:235], a[252:255], v[76:79], v[232:235]\n"
"ds_read_b128 v[72:75], v192 offset:60416\n"
"v_fmamk_f32 v138, v138, 0x3e553b95, v170\n"
"v_mfma_f32_16x16x32_bf16 v[240:243], v[4:7], v[76:79], v[240:243]\n"
"v_fmamk_f32 v139, v139, 0x3e553b95, v170\n"
"v_fmamk_f32 v140, v140, 0x3e553b95, v170\n"
"v_mfma_f32_16x16x32_bf16 v[236:239], v[0:3], v[76:79], v[236:239]\n"
"v_exp_f32 v136, v136\n"
"v_mfma_f32_16x16x32_bf16 v[232:235], v[8:11], v[68:71], v[232:235]\n"
"v_exp_f32 v137, v137\n"
"v_mfma_f32_16x16x32_bf16 v[240:243], v[16:19], v[68:71], v[240:243]\n"
"buffer_load_dword v200, off, s[28:31], s39 sc1\n"
"v_fmamk_f32 v141, v141, 0x3e553b95, v170\n"
"v_mfma_f32_16x16x32_bf16 v[236:239], v[12:15], v[68:71], v[236:239]\n"
"v_exp_f32 v138, v138\n"
"v_mfma_f32_16x16x32_bf16 v[212:215], a[84:87], v[36:39], v[212:215]\n"
"v_exp_f32 v139, v139\n"
"v_mfma_f32_16x16x32_bf16 v[212:215], a[96:99], v[40:43], v[212:215]\n"
"v_cvt_pk_bf16_f32 v82, v136, v138\n"
"v_cvt_pk_bf16_f32 v83, v137, v139\n"
"v_mfma_f32_16x16x32_bf16 v[212:215], a[108:111], v[44:47], v[212:215]\n"
"s_waitcnt lgkmcnt(0)\n"
"ds_read_b64_tr_b16 v[136:137], v182 offset:13632\n"
"ds_read_b64_tr_b16 v[138:139], v183 offset:13632\n"
"v_mfma_f32_16x16x32_bf16 v[232:235], v[20:23], v[72:75], v[232:235]\n"
"v_fmamk_f32 v142, v142, 0x3e553b95, v170\n"
"v_fmamk_f32 v143, v143, 0x3e553b95, v170\n"
"v_mfma_f32_16x16x32_bf16 v[240:243], v[28:31], v[72:75], v[240:243]\n"
"v_exp_f32 v140, v140\n"
"v_mfma_f32_16x16x32_bf16 v[236:239], v[24:27], v[72:75], v[236:239]\n"
"v_exp_f32 v141, v141\n"
"v_mfma_f32_16x16x32_bf16 v[212:215], a[120:123], v[48:51], v[212:215]\n"
"v_exp_f32 v142, v142\n"
"v_mfma_f32_16x16x32_bf16 v[212:215], a[132:135], v[52:55], v[212:215]\n"
"v_exp_f32 v143, v143\n"
"v_cvt_pk_bf16_f32 v84, v140, v142\n"
"v_cvt_pk_bf16_f32 v85, v141, v143\n"
"v_cvt_pk_bf16_f32 v222, v216, v217\n"
"v_cvt_pk_bf16_f32 v223, v218, v219\n"
"v_smfmac_f32_16x16x64_bf16 v[252:255], v[82:85], v[120:127], v66\n"
"ds_read_b64_tr_b16 v[140:141], v182 offset:14400\n"
"v_smfmac_f32_16x16x64_bf16 v[98:101], v[82:85], v[128:135], v66\n"
"ds_read_b64_tr_b16 v[142:143], v183 offset:14400\n"
"v_smfmac_f32_16x16x64_bf16 v[106:109], v[82:85], v[244:251], v66\n"
"ds_read_b64_tr_b16 v[120:121], v182 offset:13640\n"
"v_mfma_f32_16x16x32_bf16 v[82:85], v[172:175], v[82:85], 0\n"
"ds_read_b64_tr_b16 v[122:123], v183 offset:13640\n"
"ds_read_b64_tr_b16 v[124:125], v182 offset:14408\n"
"v_mfma_f32_16x16x32_bf16 v[216:219], a[76:79], v[32:35], 0\n"
"ds_write_b128 v186, v[220:223] offset:47328\n"
"v_mfma_f32_16x16x32_bf16 v[220:223], a[80:83], v[32:35], 0\n"
"v_cvt_pk_bf16_f32 v232, v232, v233\n"
"v_cvt_pk_bf16_f32 v233, v234, v235\n"
"v_mfma_f32_16x16x32_bf16 v[216:219], a[88:91], v[36:39], v[216:219]\n"
"v_cvt_pk_bf16_f32 v240, v240, v241\n"
"v_cvt_pk_bf16_f32 v234, v236, v237\n"
"v_mfma_f32_16x16x32_bf16 v[220:223], a[92:95], v[36:39], v[220:223]\n"
"ds_read_b64_tr_b16 v[126:127], v183 offset:14408\n"
"ds_read_b64_tr_b16 v[128:129], v182 offset:13576\n"
"v_mfma_f32_16x16x32_bf16 v[216:219], a[100:103], v[40:43], v[216:219]\n"
"ds_read_b64_tr_b16 v[130:131], v183 offset:13576\n"
"v_cvt_pk_bf16_f32 v235, v238, v239\n"
"v_mfma_f32_16x16x32_bf16 v[220:223], a[104:107], v[40:43], v[220:223]\n"
"ds_read_b64_tr_b16 v[132:133], v182 offset:14344\n"
"v_cvt_pk_bf16_f32 v241, v242, v243\n"
"v_mfma_f32_16x16x32_bf16 v[216:219], a[112:115], v[44:47], v[216:219]\n"
"ds_read_b64_tr_b16 v[134:135], v183 offset:14344\n"
"v_fmamk_f32 v112, v112, 0x3e553b95, v170\n"
"v_mfma_f32_16x16x32_bf16 v[220:223], a[116:119], v[44:47], v[220:223]\n"
"ds_write_b128 v196, v[232:235] offset:43008\n"
"v_mfma_f32_16x16x32_bf16 v[216:219], a[124:127], v[48:51], v[216:219]\n"
"v_fmamk_f32 v113, v113, 0x3e553b95, v170\n"
"v_fmamk_f32 v114, v114, 0x3e553b95, v170\n"
"v_mfma_f32_16x16x32_bf16 v[220:223], a[128:131], v[48:51], v[220:223]\n"
"v_fmamk_f32 v115, v115, 0x3e553b95, v170\n"
"v_fmamk_f32 v116, v116, 0x3e553b95, v170\n"
"v_mfma_f32_16x16x32_bf16 v[216:219], a[136:139], v[52:55], v[216:219]\n"
"ds_write_b64 v197, v[240:241] offset:43008\n"
"v_mfma_f32_16x16x32_bf16 v[220:223], a[140:143], v[52:55], v[220:223]\n"
"v_exp_f32 v112, v112\n"
"v_exp_f32 v113, v113\n"
"s_waitcnt vmcnt(3)\n"
"v_or_b32 v201, 11, s6\n"
"s_mov_b64 exec, 1\n"
"buffer_store_dword v201, off, s[28:31], s40 sc1\n"
"s_mov_b64 exec, -1\n"
"v_exp_f32 v114, v114\n"
"v_exp_f32 v115, v115\n"
"v_cvt_pk_bf16_f32 v90, v112, v114\n"
"v_cvt_pk_bf16_f32 v91, v113, v115\n"
"v_fmamk_f32 v117, v117, 0x3e553b95, v170\n"
"v_fmamk_f32 v118, v118, 0x3e553b95, v170\n"
"v_fmamk_f32 v119, v119, 0x3e553b95, v170\n"
"v_exp_f32 v116, v116\n"
"v_exp_f32 v117, v117\n"
"v_exp_f32 v118, v118\n"
"s_waitcnt lgkmcnt(0)\n"
"s_barrier\n"
"ds_read_b128 v[72:75], v192 offset:30720\n"
"v_exp_f32 v119, v119\n"
"v_cvt_pk_bf16_f32 v92, v116, v118\n"
"v_cvt_pk_bf16_f32 v93, v117, v119\n"
"s_mov_b64 exec, s[44:45]\n"
"v_add_u32 v181, 0xc0, v181\n"
"s_mov_b64 exec, -1\n"
"v_smfmac_f32_16x16x64_bf16 v[252:255], v[90:93], v[136:143], v66\n"
"ds_read_b128 v[244:247], v178\n"
"v_smfmac_f32_16x16x64_bf16 v[98:101], v[90:93], v[120:127], v66\n"
"ds_read_b128 v[112:115], v179\n"
"v_smfmac_f32_16x16x64_bf16 v[106:109], v[90:93], v[128:135], v66\n"
"ds_read_b128 v[248:251], v178 offset:13504\n"
"v_mfma_f32_16x16x32_bf16 v[90:93], v[172:175], v[90:93], v[82:85]\n"
"ds_read_b128 v[116:119], v179 offset:13504\n"
"ds_write_b128 v202, a[204:207] offset:55296\n"
"v_cvt_pk_bf16_f32 v252, v252, v253\n"
"v_cvt_pk_bf16_f32 v253, v254, v255\n"
"v_cvt_pk_bf16_f32 v98, v98, v99\n"
"v_cvt_pk_bf16_f32 v99, v100, v101\n"
"v_cvt_pk_bf16_f32 v106, v106, v107\n"
"v_cvt_pk_bf16_f32 v107, v108, v109\n"
"v_rcp_f32 v171, v90\n"
"v_cvt_pk_bf16_f32 v228, v228, v229\n"
"v_cvt_pk_bf16_f32 v171, v171, v171\n"
"v_and_b32 v64, v171, v176\n"
"v_and_b32 v65, v171, v177\n"
"v_cvt_pk_bf16_f32 v229, v230, v231\n"
"v_cvt_pk_bf16_f32 v230, v204, v205\n"
"v_mfma_f32_16x16x16_bf16 v[252:255], v[252:253], v[64:65], 0\n"
"s_waitcnt lgkmcnt(3)\n"
"ds_read_b64 v[82:83], v178 offset:64\n"
"ds_read_b64 v[84:85], v179 offset:64\n"
"v_mfma_f32_16x16x16_bf16 v[98:101], v[98:99], v[64:65], 0\n"
"s_waitcnt lgkmcnt(4)\n"
"ds_read_b64 v[90:91], v178 offset:13568\n"
"v_cvt_pk_bf16_f32 v231, v206, v207\n"
"v_mfma_f32_16x16x16_bf16 v[106:109], v[106:107], v[64:65], 0\n"
"s_waitcnt lgkmcnt(4)\n"
"ds_read_b64 v[92:93], v179 offset:13568\n"
"v_cvt_pk_bf16_f32 v252, v252, v253\n"
"v_mfma_f32_16x16x32_bf16 v[244:247], v[244:247], v[144:147], 0\n"
"ds_read_b128 v[136:139], v178 offset:27008\n"
"v_cvt_pk_bf16_f32 v253, v254, v255\n"
"v_mfma_f32_16x16x32_bf16 v[112:115], v[112:115], v[144:147], 0\n"
"ds_read_b128 v[120:123], v179 offset:27008\n"
"v_cvt_pk_bf16_f32 v254, v98, v99\n"
"v_mfma_f32_16x16x32_bf16 v[248:251], v[248:251], v[144:147], 0\n"
"ds_read_b64 v[98:99], v178 offset:27072\n"
"v_cvt_pk_bf16_f32 v255, v100, v101\n"
"v_mfma_f32_16x16x32_bf16 v[116:119], v[116:119], v[144:147], 0\n"
"ds_read_b64 v[100:101], v179 offset:27072\n"
"v_cvt_pk_bf16_f32 v106, v106, v107\n"
"v_mfma_f32_16x16x32_bf16 v[232:235], a[216:219], v[72:75], 0\n"
"ds_read_b128 v[140:143], v180\n"
"v_cvt_pk_bf16_f32 v107, v108, v109\n"
"v_mfma_f32_16x16x32_bf16 v[236:239], a[220:223], v[72:75], 0\n"
"s_waitcnt lgkmcnt(8)\n"
"ds_read_b128 v[124:127], v181\n"
"v_min_u32 v252, -2, v252\n"
"v_mfma_f32_16x16x32_bf16 v[244:247], v[80:83], v[156:159], v[244:247]\n"
"s_waitcnt lgkmcnt(8)\n"
"v_min_u32 v253, -2, v253\n"
"v_min_u32 v254, -2, v254\n"
"v_mfma_f32_16x16x32_bf16 v[112:115], v[84:87], v[158:161], v[112:115]\n"
"s_waitcnt lgkmcnt(7)\n"
"ds_read_b128 v[76:79], v202 offset:43008\n"
"ds_read_b64 v[108:109], v181 offset:64\n"
"v_mfma_f32_16x16x32_bf16 v[248:251], v[88:91], v[156:159], v[248:251]\n"
"s_waitcnt lgkmcnt(8)\n"
"ds_write_b128 v198, v[228:231]\n"
"v_mfma_f32_16x16x32_bf16 v[116:119], v[92:95], v[158:161], v[116:119]\n"
"s_waitcnt lgkmcnt(7)\n"
"v_min_u32 v255, -2, v255\n"
"v_min_u32 v106, -2, v106\n"
"v_mfma_f32_16x16x32_bf16 v[136:139], v[136:139], v[144:147], 0\n"
"s_waitcnt lgkmcnt(6)\n"
"buffer_store_dwordx4 v[252:255], v192, s[28:31], s37 offen sc1\n"
"v_mfma_f32_16x16x32_bf16 v[120:123], v[120:123], v[144:147], 0\n"
"s_waitcnt lgkmcnt(4)\n"
"v_min_u32 v107, -2, v107\n"
"v_cvt_pk_bf16_f32 v208, v208, v209\n"
"v_mfma_f32_16x16x32_bf16 v[136:139], v[96:99], v[156:159], v[136:139]\n"
"s_waitcnt lgkmcnt(3)\n"
"ds_write_b64 v203, a[208:209] offset:55296\n"
"v_mfma_f32_16x16x32_bf16 v[120:123], v[100:103], v[158:161], v[120:123]\n"
"s_waitcnt lgkmcnt(3)\n"
"buffer_store_dwordx2 v[106:107], v193, s[28:31], s37 offen offset:1024 sc1\n"
"v_mfma_f32_16x16x32_bf16 v[140:143], v[140:143], v[144:147], 0\n"
"s_add_u32 s37, s37, 6144\n"
"ds_read_b64 v[106:107], v180 offset:64\n"
"ds_read_b64_tr_b16 v[128:129], v182 offset:128\n"
"v_mfma_f32_16x16x32_bf16 v[124:127], v[124:127], v[144:147], 0\n"
"s_waitcnt lgkmcnt(4)\n"
"ds_read_b128 v[68:71], v202 offset:47104\n"
"v_cvt_pk_bf16_f32 v209, v210, v211\n"
"v_mfma_f32_16x16x32_bf16 v[124:127], v[108:111], v[158:161], v[124:127]\n"
"buffer_store_dwordx4 v[76:79], v194, s[24:27], s36 offen sc1\n"
"v_mfma_f32_16x16x32_bf16 v[240:243], a[224:227], v[72:75], 0\n"
"s_nop 0\n"
"ds_read_b128 v[76:79], v192 offset:31744\n"
"ds_read_b128 v[72:75], v192 offset:33792\n"
"ds_read_b64_tr_b16 v[130:131], v183 offset:128\n"
"v_max_f32 v244, v244, v112\n"
"v_max_f32 v245, v245, v113\n"
"v_max_f32 v246, v246, v114\n"
"v_max_f32 v247, v247, v115\n"
"v_max_f32 v170, v244, v245\n"
"v_max_f32 v171, v246, v247\n"
"v_max_f32 v248, v248, v116\n"
"v_max_f32 v249, v249, v117\n"
"v_max_f32 v250, v250, v118\n"
"v_max_f32 v251, v251, v119\n"
"v_max3_f32 v170, v170, v248, v249\n"
"v_max3_f32 v171, v171, v250, v251\n"
"v_max_f32 v136, v136, v120\n"
"v_max_f32 v137, v137, v121\n"
"v_max_f32 v138, v138, v122\n"
"v_max_f32 v139, v139, v123\n"
"v_max3_f32 v170, v170, v136, v137\n"
"v_max3_f32 v171, v171, v138, v139\n"
"v_mov_b64 v[252:253], 0\n"
"v_mov_b64 v[254:255], 0\n"
"v_mov_b64 v[98:99], 0\n"
"v_mov_b64 v[100:101], 0\n"
"v_mov_b64 v[108:109], 0\n"
"v_cvt_pk_bf16_f32 v212, v212, v213\n"
"v_cvt_pk_bf16_f32 v213, v214, v215\n"
"v_cvt_pk_bf16_f32 v214, v216, v217\n"
"v_cvt_pk_bf16_f32 v215, v218, v219\n"
"v_cvt_pk_bf16_f32 v210, v220, v221\n"
"v_cvt_pk_bf16_f32 v211, v222, v223\n"
"s_waitcnt vmcnt(4) lgkmcnt(2)\n"
"v_mfma_f32_16x16x32_bf16 v[140:143], v[104:107], v[156:159], v[140:143]\n"
"v_add3_u32 v200, v200, s50, -10\n"
"v_cmp_lt_u32 vcc, 64, v200\n"
"v_mfma_f32_16x16x32_bf16 v[232:235], a[228:231], v[76:79], v[232:235]\n"
"ds_read_b64_tr_b16 v[132:133], v182 offset:896\n"
"ds_read_b64_tr_b16 v[134:135], v183 offset:896\n"
"v_mfma_f32_16x16x32_bf16 v[236:239], a[232:235], v[76:79], v[236:239]\n"
"s_nop 0\n"
"s_cbranch_vccnz .Lp9%=\n"
".Lr9%=:\n"
"v_mfma_f32_16x16x32_bf16 v[240:243], a[236:239], v[76:79], v[240:243]\n"
"buffer_load_dwordx4 a[144:147], v192, s[28:31], s12 offen sc1\n"
"ds_read_b128 v[76:79], v192 offset:34816\n"
"ds_read_b64_tr_b16 v[112:113], v182 offset:136\n"
"ds_read_b64_tr_b16 v[114:115], v183 offset:136\n"
"v_max_f32 v140, v140, v124\n"
"v_max_f32 v141, v141, v125\n"
"v_max_f32 v142, v142, v126\n"
"ds_read_b64_tr_b16 v[116:117], v182 offset:904\n"
"v_max_f32 v143, v143, v127\n"
"ds_write_b128 v202, a[210:213] offset:36864\n"
"buffer_load_dwordx2 a[148:149], v193, s[28:31], s12 offen offset:1024 sc1\n"
"v_max3_f32 v170, v170, v140, v141\n"
"v_max3_f32 v171, v171, v142, v143\n"
"v_mov_b64 v[106:107], 0\n"
"v_max_f32 v170, v170, v171\n"
"v_mov_b32 v171, v170\n"
"ds_read_b64_tr_b16 v[118:119], v183 offset:904\n"
"ds_read_b64_tr_b16 v[120:121], v182 offset:72\n"
"ds_read_b64_tr_b16 v[122:123], v183 offset:72\n"
"v_permlane16_swap_b32 v170, v171\n"
"buffer_load_dwordx4 a[150:153], v192, s[28:31], s13 offen sc1\n"
"ds_read_b64_tr_b16 v[124:125], v182 offset:840\n"
"v_max_f32 v170, v170, v171\n"
"ds_read_b64_tr_b16 v[126:127], v183 offset:840\n"
"v_mov_b32 v171, v170\n"
"ds_write_b64 v203, a[214:215] offset:36864\n"
"s_nop 0\n"
"v_permlane32_swap_b32 v170, v171\n"
"buffer_load_dwordx2 a[154:155], v193, s[28:31], s13 offen offset:1024 sc1\n"
"v_max_f32 v170, v170, v171\n"
"v_mul_f32 v170, 0xbe553b95, v170\n"
"v_fmamk_f32 v244, v244, 0x3e553b95, v170\n"
"v_fmamk_f32 v245, v245, 0x3e553b95, v170\n"
"v_fmamk_f32 v246, v246, 0x3e553b95, v170\n"
"v_fmamk_f32 v247, v247, 0x3e553b95, v170\n"
"v_exp_f32 v244, v244\n"
"v_exp_f32 v245, v245\n"
"v_exp_f32 v246, v246\n"
"buffer_store_dwordx4 v[68:71], v195, s[24:27], s36 offen sc1\n"
"s_nop 1\n"
"ds_read_b128 v[68:71], v192 offset:32768\n"
"s_add_u32 s36, s36, s33\n"
"v_exp_f32 v247, v247\n"
"v_cvt_pk_bf16_f32 v82, v244, v246\n"
"v_cvt_pk_bf16_f32 v83, v245, v247\n"
"v_fmamk_f32 v248, v248, 0x3e553b95, v170\n"
"v_fmamk_f32 v249, v249, 0x3e553b95, v170\n"
"v_fmamk_f32 v250, v250, 0x3e553b95, v170\n"
"v_fmamk_f32 v251, v251, 0x3e553b95, v170\n"
"v_exp_f32 v248, v248\n"
"v_exp_f32 v249, v249\n"
"v_exp_f32 v250, v250\n"
"v_exp_f32 v251, v251\n"
"v_cvt_pk_bf16_f32 v84, v248, v250\n"
"v_cvt_pk_bf16_f32 v85, v249, v251\n"
"v_fmamk_f32 v136, v136, 0x3e553b95, v170\n"
"v_fmamk_f32 v137, v137, 0x3e553b95, v170\n"
"v_fmamk_f32 v138, v138, 0x3e553b95, v170\n"
"v_fmamk_f32 v139, v139, 0x3e553b95, v170\n"
"v_exp_f32 v136, v136\n"
"v_exp_f32 v137, v137\n"
"v_exp_f32 v138, v138\n"
"v_exp_f32 v139, v139\n"
"v_cvt_pk_bf16_f32 v90, v136, v138\n"
"v_cvt_pk_bf16_f32 v91, v137, v139\n"
"v_fmamk_f32 v140, v140, 0x3e553b95, v170\n"
"v_fmamk_f32 v141, v141, 0x3e553b95, v170\n"
"v_fmamk_f32 v142, v142, 0x3e553b95, v170\n"
"v_fmamk_f32 v143, v143, 0x3e553b95, v170\n"
"v_exp_f32 v140, v140\n"
"v_exp_f32 v141, v141\n"
"v_exp_f32 v142, v142\n"
"v_exp_f32 v143, v143\n"
"v_cvt_pk_bf16_f32 v92, v140, v142\n"
"v_cvt_pk_bf16_f32 v93, v141, v143\n"
"s_waitcnt lgkmcnt(0)\n"
"v_mfma_f32_16x16x32_bf16 v[232:235], a[240:243], v[68:71], v[232:235]\n"
"ds_read_b64_tr_b16 v[244:245], v182 offset:27136\n"
"buffer_load_dword v201, off, s[28:31], s39 sc1\n"
"v_mfma_f32_16x16x32_bf16 v[236:239], a[244:247], v[68:71], v[236:239]\n"
"ds_read_b64_tr_b16 v[246:247], v183 offset:27136\n"
"ds_read_b64_tr_b16 v[248:249], v182 offset:27904\n"
"v_mfma_f32_16x16x32_bf16 v[240:243], a[248:251], v[68:71], v[240:243]\n"
"ds_read_b128 v[68:71], v192 offset:35840\n"
"v_smfmac_f32_16x16x64_bf16 v[252:255], v[82:85], v[128:135], v66\n"
"ds_read_b64_tr_b16 v[250:251], v183 offset:27904\n"
"v_mfma_f32_16x16x32_bf16 v[232:235], a[252:255], v[72:75], v[232:235]\n"
"ds_read_b64_tr_b16 v[128:129], v182 offset:27144\n"
"v_smfmac_f32_16x16x64_bf16 v[98:101], v[82:85], v[112:119], v66\n"
"ds_read_b64_tr_b16 v[130:131], v183 offset:27144\n"
"v_mfma_f32_16x16x32_bf16 v[236:239], v[0:3], v[72:75], v[236:239]\n"
"ds_write_b128 v198, v[208:211] offset:64\n"
"v_mfma_f32_16x16x32_bf16 v[240:243], v[4:7], v[72:75], v[240:243]\n"
"v_smfmac_f32_16x16x64_bf16 v[106:109], v[82:85], v[120:127], v66\n"
"ds_read_b64_tr_b16 v[132:133], v182 offset:27912\n"
"v_mfma_f32_16x16x32_bf16 v[82:85], v[172:175], v[82:85], 0\n"
"ds_read_b64_tr_b16 v[134:135], v183 offset:27912\n"
"ds_read_b64_tr_b16 v[112:113], v182 offset:27080\n"
"v_mfma_f32_16x16x32_bf16 v[232:235], v[8:11], v[76:79], v[232:235]\n"
"s_waitcnt lgkmcnt(7)\n"
"ds_read_b64_tr_b16 v[114:115], v183 offset:27080\n"
"v_mfma_f32_16x16x32_bf16 v[236:239], v[12:15], v[76:79], v[236:239]\n"
"s_waitcnt lgkmcnt(7)\n"
"ds_read_b64_tr_b16 v[116:117], v182 offset:27848\n"
"v_mfma_f32_16x16x32_bf16 v[240:243], v[16:19], v[76:79], v[240:243]\n"
".Lic7%=:\n"
"s_cbranch_execz .Lib1%=\n"
"ds_write_b128 v198, v[212:215] offset:128\n"
"v_mfma_f32_16x16x32_bf16 v[232:235], v[20:23], v[68:71], v[232:235]\n"
"v_mfma_f32_16x16x32_bf16 v[236:239], v[24:27], v[68:71], v[236:239]\n"
"ds_read_b64_tr_b16 v[118:119], v183 offset:27848\n"
"v_smfmac_f32_16x16x64_bf16 v[252:255], v[90:93], v[244:251], v66\n"
"ds_read_b128 v[120:123], v178 offset:13504\n"
"v_mfma_f32_16x16x32_bf16 v[240:243], v[28:31], v[68:71], v[240:243]\n"
"s_waitcnt lgkmcnt(6)\n"
"ds_read_b128 v[136:139], v179 offset:13504\n"
"ds_read_b128 v[124:127], v178 offset:27008\n"
"v_smfmac_f32_16x16x64_bf16 v[98:101], v[90:93], v[128:135], v66\n"
"v_cvt_pk_bf16_f32 v232, v232, v233\n"
"v_cvt_pk_bf16_f32 v233, v234, v235\n"
"v_cvt_pk_bf16_f32 v234, v236, v237\n"
"v_cvt_pk_bf16_f32 v252, v252, v253\n"
"v_cvt_pk_bf16_f32 v253, v254, v255\n"
"v_cvt_pk_bf16_f32 v235, v238, v239\n"
"v_cvt_pk_bf16_f32 v240, v240, v241\n"
"v_cvt_pk_bf16_f32 v241, v242, v243\n"
"v_cvt_pk_bf16_f32 v98, v98, v99\n"
"v_cvt_pk_bf16_f32 v99, v100, v101\n"
"ds_write_b128 v196, v[232:235] offset:49152\n"
"s_waitcnt lgkmcnt(5)\n"
"s_barrier\n"
"s_waitcnt lgkmcnt(4)\n"
"v_smfmac_f32_16x16x64_bf16 v[106:109], v[90:93], v[112:119], v66\n"
"s_waitcnt lgkmcnt(3)\n"
"ds_read_b128 v[72:75], v192 offset:55296\n"
"v_mfma_f32_16x16x32_bf16 v[90:93], v[172:175], v[90:93], v[82:85]\n"
"s_waitcnt lgkmcnt(3)\n"
"ds_read_b128 v[76:79], v192 offset:56320\n"
"ds_read_b128 v[68:71], v192 offset:57344\n"
"v_mfma_f32_16x16x32_bf16 v[120:123], v[120:123], v[148:151], 0\n"
"s_waitcnt lgkmcnt(4)\n"
"ds_write_b64 v197, v[240:241] offset:49152\n"
"v_mfma_f32_16x16x32_bf16 v[136:139], v[136:139], v[148:151], 0\n"
"v_cvt_pk_bf16_f32 v106, v106, v107\n"
"v_cvt_pk_bf16_f32 v107, v108, v109\n"
"v_mfma_f32_16x16x32_bf16 v[124:127], v[124:127], v[148:151], 0\n"
"v_rcp_f32 v171, v90\n"
"ds_read_b64 v[82:83], v178 offset:13568\n"
"v_cvt_pk_bf16_f32 v171, v171, v171\n"
"v_and_b32 v64, v171, v176\n"
"v_and_b32 v65, v171, v177\n"
"s_nop 1\n"
"v_mfma_f32_16x16x16_bf16 v[252:255], v[252:253], v[64:65], 0\n"
"ds_read_b64 v[84:85], v179 offset:13568\n"
"ds_read_b128 v[140:143], v179 offset:27008\n"
"v_mfma_f32_16x16x16_bf16 v[98:101], v[98:99], v[64:65], 0\n"
"s_waitcnt lgkmcnt(6)\n"
"ds_read_b64 v[90:91], v178 offset:27072\n"
"v_mfma_f32_16x16x16_bf16 v[106:109], v[106:107], v[64:65], 0\n"
"ds_read_b64 v[92:93], v179 offset:27072\n"
"v_mfma_f32_16x16x32_bf16 v[232:235], a[216:219], v[72:75], 0\n"
"s_waitcnt lgkmcnt(7)\n"
"buffer_load_dwordx4 v[228:231], v192, s[28:31], s14 offen sc1\n"
"ds_read_b128 v[244:247], v180\n"
"v_mfma_f32_16x16x32_bf16 v[236:239], a[220:223], v[72:75], 0\n"
"ds_read_b128 v[128:131], v181\n"
"v_cvt_pk_bf16_f32 v252, v252, v253\n"
"v_mfma_f32_16x16x32_bf16 v[240:243], a[224:227], v[72:75], 0\n"
"ds_read_b128 v[72:75], v192 offset:58368\n"
"v_cvt_pk_bf16_f32 v253, v254, v255\n"
"v_mfma_f32_16x16x32_bf16 v[232:235], a[228:231], v[76:79], v[232:235]\n"
"s_waitcnt lgkmcnt(7)\n"
"buffer_load_dwordx2 v[152:153], v193, s[28:31], s14 offen offset:1024 sc1\n"
"ds_read_b128 v[248:251], v180 offset:13504\n"
"v_mfma_f32_16x16x32_bf16 v[120:123], v[80:83], v[160:163], v[120:123]\n"
"ds_read_b128 v[132:135], v181 offset:13504\n"
"v_cvt_pk_bf16_f32 v254, v98, v99\n"
"v_mfma_f32_16x16x32_bf16 v[236:239], a[232:235], v[76:79], v[236:239]\n"
"s_waitcnt lgkmcnt(8)\n"
"ds_read_b64 v[98:99], v180 offset:64\n"
"v_cvt_pk_bf16_f32 v255, v100, v101\n"
"v_mfma_f32_16x16x32_bf16 v[136:139], v[84:87], v[162:165], v[136:139]\n"
"s_waitcnt lgkmcnt(8)\n"
"ds_read_b64 v[100:101], v181 offset:64\n"
"v_cvt_pk_bf16_f32 v106, v106, v107\n"
"v_mfma_f32_16x16x32_bf16 v[240:243], a[236:239], v[76:79], v[240:243]\n"
"s_waitcnt lgkmcnt(8)\n"
"ds_read_b128 v[76:79], v192 offset:59392\n"
"v_cvt_pk_bf16_f32 v107, v108, v109\n"
"v_mfma_f32_16x16x32_bf16 v[232:235], a[240:243], v[68:71], v[232:235]\n"
"ds_read_b64 v[108:109], v181 offset:13568\n"
"v_min_u32 v252, -2, v252\n"
"v_mfma_f32_16x16x32_bf16 v[236:239], a[244:247], v[68:71], v[236:239]\n"
"s_waitcnt lgkmcnt(8)\n"
"v_min_u32 v253, -2, v253\n"
"v_min_u32 v254, -2, v254\n"
"v_mfma_f32_16x16x32_bf16 v[240:243], a[248:251], v[68:71], v[240:243]\n"
"s_waitcnt lgkmcnt(7)\n"
"ds_read_b128 v[68:71], v192 offset:60416\n"
"v_min_u32 v255, -2, v255\n"
"v_mfma_f32_16x16x32_bf16 v[140:143], v[140:143], v[148:151], 0\n"
"s_waitcnt lgkmcnt(7)\n"
"buffer_store_dwordx4 v[252:255], v192, s[28:31], s37 offen sc1\n"
"v_mfma_f32_16x16x32_bf16 v[124:127], v[88:91], v[160:163], v[124:127]\n"
"s_waitcnt lgkmcnt(5)\n"
"v_min_u32 v106, -2, v106\n"
"v_min_u32 v107, -2, v107\n"
"v_mfma_f32_16x16x32_bf16 v[140:143], v[92:95], v[162:165], v[140:143]\n"
"ds_read_b64_tr_b16 v[112:113], v182 offset:13632\n"
"buffer_store_dwordx2 v[106:107], v193, s[28:31], s37 offen offset:1024 sc1\n"
"v_mfma_f32_16x16x32_bf16 v[232:235], a[252:255], v[72:75], v[232:235]\n"
"s_waitcnt lgkmcnt(3)\n"
"ds_read_b64 v[106:107], v180 offset:13568\n"
"ds_read_b64_tr_b16 v[114:115], v183 offset:13632\n"
"v_mfma_f32_16x16x32_bf16 v[236:239], v[0:3], v[72:75], v[236:239]\n"
"s_add_u32 s37, s37, 6144\n"
"ds_read_b64_tr_b16 v[116:117], v182 offset:14400\n"
"v_max_f32 v120, v120, v136\n"
"v_mfma_f32_16x16x32_bf16 v[240:243], v[4:7], v[72:75], v[240:243]\n"
"ds_read_b128 v[72:75], v192 offset:36864\n"
"v_max_f32 v121, v121, v137\n"
"v_mfma_f32_16x16x32_bf16 v[244:247], v[244:247], v[148:151], 0\n"
"ds_read_b64_tr_b16 v[118:119], v183 offset:14400\n"
"v_max_f32 v122, v122, v138\n"
"v_mfma_f32_16x16x32_bf16 v[232:235], v[8:11], v[76:79], v[232:235]\n"
"s_waitcnt lgkmcnt(6)\n"
"ds_read_b64_tr_b16 v[136:137], v182 offset:13640\n"
"v_max_f32 v123, v123, v139\n"
"v_mfma_f32_16x16x32_bf16 v[236:239], v[12:15], v[76:79], v[236:239]\n"
"ds_read_b64_tr_b16 v[138:139], v183 offset:13640\n"
"v_max_f32 v170, v120, v121\n"
"v_mfma_f32_16x16x32_bf16 v[240:243], v[16:19], v[76:79], v[240:243]\n"
"ds_read_b128 v[76:79], v192 offset:37888\n"
"v_max_f32 v171, v122, v123\n"
"v_mfma_f32_16x16x32_bf16 v[128:131], v[128:131], v[148:151], 0\n"
"v_max_f32 v124, v124, v140\n"
"v_max_f32 v125, v125, v141\n"
"v_mfma_f32_16x16x32_bf16 v[232:235], v[20:23], v[68:71], v[232:235]\n"
"ds_read_b64_tr_b16 v[140:141], v182 offset:14408\n"
"v_max_f32 v126, v126, v142\n"
"v_mfma_f32_16x16x32_bf16 v[236:239], v[24:27], v[68:71], v[236:239]\n"
"v_max_f32 v127, v127, v143\n"
"v_max3_f32 v170, v170, v124, v125\n"
"v_mfma_f32_16x16x32_bf16 v[240:243], v[28:31], v[68:71], v[240:243]\n"
"ds_read_b128 v[68:71], v192 offset:38912\n"
"ds_read_b64_tr_b16 v[142:143], v183 offset:14408\n"
"v_mfma_f32_16x16x32_bf16 v[244:247], v[96:99], v[160:163], v[244:247]\n"
"s_waitcnt lgkmcnt(7)\n"
"v_max3_f32 v171, v171, v126, v127\n"
"v_mov_b64 v[252:253], 0\n"
"v_mfma_f32_16x16x32_bf16 v[128:131], v[100:103], v[162:165], v[128:131]\n"
"v_mov_b64 v[254:255], 0\n"
"v_mov_b64 v[98:99], 0\n"
"v_mfma_f32_16x16x32_bf16 v[248:251], v[248:251], v[148:151], 0\n"
"s_waitcnt vmcnt(9)\n"
"v_mov_b64 v[100:101], 0\n"
"v_cvt_pk_bf16_f32 v232, v232, v233\n"
"v_mfma_f32_16x16x32_bf16 v[204:207], a[216:219], v[72:75], 0\n"
"ds_write_b128 v202, a[144:147] offset:24576\n"
"v_mfma_f32_16x16x32_bf16 v[208:211], a[220:223], v[72:75], 0\n"
"s_waitcnt lgkmcnt(4)\n"
"v_max_f32 v244, v244, v128\n"
"v_max_f32 v245, v245, v129\n"
"v_mfma_f32_16x16x32_bf16 v[212:215], a[224:227], v[72:75], 0\n"
"v_max_f32 v246, v246, v130\n"
"v_max_f32 v247, v247, v131\n"
"v_mfma_f32_16x16x32_bf16 v[132:135], v[132:135], v[148:151], 0\n"
"s_waitcnt vmcnt(6)\n"
"ds_read_b128 v[72:75], v192 offset:39936\n"
"ds_write_b64 v203, a[148:149] offset:24576\n"
"v_mfma_f32_16x16x32_bf16 v[204:207], a[228:231], v[76:79], v[204:207]\n"
"s_waitcnt lgkmcnt(4)\n"
"v_max3_f32 v170, v170, v244, v245\n"
"v_max3_f32 v171, v171, v246, v247\n"
"v_mfma_f32_16x16x32_bf16 v[208:211], a[232:235], v[76:79], v[208:211]\n"
"ds_write_b128 v202, a[150:153] offset:30720\n"
"v_mfma_f32_16x16x32_bf16 v[212:215], a[236:239], v[76:79], v[212:215]\n"
"v_cvt_pk_bf16_f32 v233, v234, v235\n"
"v_cvt_pk_bf16_f32 v234, v236, v237\n"
"v_mfma_f32_16x16x32_bf16 v[248:251], v[104:107], v[160:163], v[248:251]\n"
"v_mov_b64 v[106:107], 0\n"
"v_cvt_pk_bf16_f32 v235, v238, v239\n"
"v_mfma_f32_16x16x32_bf16 v[204:207], a[240:243], v[68:71], v[204:207]\n"
"ds_write_b64 v203, a[154:155] offset:30720\n"
"v_mfma_f32_16x16x32_bf16 v[208:211], a[244:247], v[68:71], v[208:211]\n"
"s_waitcnt lgkmcnt(3)\n"
"v_cvt_pk_bf16_f32 v240, v240, v241\n"
"v_cvt_pk_bf16_f32 v241, v242, v243\n"
"v_mfma_f32_16x16x32_bf16 v[212:215], a[248:251], v[68:71], v[212:215]\n"
"ds_read_b128 v[76:79], v192 offset:40960\n"
"ds_read_b128 v[68:71], v192 offset:41984\n"
"v_mfma_f32_16x16x32_bf16 v[132:135], v[108:111], v[162:165], v[132:135]\n"
"ds_read_b64_tr_b16 v[128:129], v182 offset:13576\n"
"v_mov_b64 v[108:109], 0\n"
"v_mfma_f32_16x16x32_bf16 v[204:207], a[252:255], v[72:75], v[204:207]\n"
"ds_read_b64_tr_b16 v[130:131], v183 offset:13576\n"
"v_mfma_f32_16x16x32_bf16 v[208:211], v[0:3], v[72:75], v[208:211]\n"
"s_waitcnt vmcnt(4)\n"
"ds_write_b128 v196, v[232:235]\n"
"v_mfma_f32_16x16x32_bf16 v[212:215], v[4:7], v[72:75], v[212:215]\n"
"v_add3_u32 v201, v201, s50, -11\n"
"v_cmp_lt_u32 vcc, 64, v201\n"
"v_max_f32 v248, v248, v132\n"
"v_max_f32 v249, v249, v133\n"
"v_max_f32 v250, v250, v134\n"
"v_max_f32 v251, v251, v135\n"
"v_max3_f32 v170, v170, v248, v249\n"
"v_max3_f32 v171, v171, v250, v251\n"
"v_max_f32 v170, v170, v171\n"
"v_mov_b32 v171, v170\n"
"s_cbranch_vccnz .Lp10%=\n"
".Lr10%=:\n"
"buffer_load_dwordx4 a[162:165], v192, s[28:31], s15 offen sc1\n"
"ds_read_b64_tr_b16 v[132:133], v182 offset:14344\n"
"ds_read_b64_tr_b16 v[134:135], v183 offset:14344\n"
"ds_write_b64 v197, v[240:241]\n"
"v_permlane16_swap_b32 v170, v171\n"
"v_max_f32 v170, v170, v171\n"
"v_mov_b32 v171, v170\n"
"s_waitcnt lgkmcnt(6)\n"
"v_mfma_f32_16x16x32_bf16 v[204:207], v[8:11], v[76:79], v[204:207]\n"
"s_barrier\n"
"v_mfma_f32_16x16x32_bf16 v[208:211], v[12:15], v[76:79], v[208:211]\n"
"buffer_load_dwordx2 a[166:167], v193, s[28:31], s15 offen offset:1024 sc1\n"
"ds_read_b128 v[32:35], v192 offset:25600\n"
"v_mfma_f32_16x16x32_bf16 v[212:215], v[16:19], v[76:79], v[212:215]\n"
"ds_read_b128 v[36:39], v192 offset:26624\n"
"ds_read_b128 v[40:43], v192 offset:27648\n"
"v_mfma_f32_16x16x32_bf16 v[204:207], v[20:23], v[68:71], v[204:207]\n"
"ds_read_b128 v[44:47], v192 offset:28672\n"
"v_permlane32_swap_b32 v170, v171\n"
"v_mfma_f32_16x16x32_bf16 v[208:211], v[24:27], v[68:71], v[208:211]\n"
"ds_read_b128 v[48:51], v192 offset:29696\n"
"v_max_f32 v170, v170, v171\n"
"v_mfma_f32_16x16x32_bf16 v[212:215], v[28:31], v[68:71], v[212:215]\n"
"ds_read_b128 v[68:71], v192 offset:24576\n"
"v_mul_f32 v170, 0xbe553b95, v170\n"
"ds_read_b128 v[52:55], v192 offset:30720\n"
"v_fmamk_f32 v120, v120, 0x3e553b95, v170\n"
"v_fmamk_f32 v121, v121, 0x3e553b95, v170\n"
"v_fmamk_f32 v122, v122, 0x3e553b95, v170\n"
"v_fmamk_f32 v123, v123, 0x3e553b95, v170\n"
"v_exp_f32 v120, v120\n"
"v_exp_f32 v121, v121\n"
"v_exp_f32 v122, v122\n"
"v_exp_f32 v123, v123\n"
"v_cvt_pk_bf16_f32 v82, v120, v122\n"
"v_cvt_pk_bf16_f32 v83, v121, v123\n"
"v_fmamk_f32 v124, v124, 0x3e553b95, v170\n"
"v_fmamk_f32 v125, v125, 0x3e553b95, v170\n"
"v_fmamk_f32 v126, v126, 0x3e553b95, v170\n"
"v_fmamk_f32 v127, v127, 0x3e553b95, v170\n"
"v_exp_f32 v124, v124\n"
"v_exp_f32 v125, v125\n"
"v_exp_f32 v126, v126\n"
"v_exp_f32 v127, v127\n"
"v_cvt_pk_bf16_f32 v84, v124, v126\n"
"v_cvt_pk_bf16_f32 v85, v125, v127\n"
"v_fmamk_f32 v244, v244, 0x3e553b95, v170\n"
"v_fmamk_f32 v245, v245, 0x3e553b95, v170\n"
"v_smfmac_f32_16x16x64_bf16 v[252:255], v[82:85], v[112:119], v66\n"
"s_waitcnt lgkmcnt(0)\n"
"ds_read_b64_tr_b16 v[120:121], v182 offset:40640\n"
"v_smfmac_f32_16x16x64_bf16 v[98:101], v[82:85], v[136:143], v66\n"
"ds_read_b64_tr_b16 v[122:123], v183 offset:40640\n"
"v_smfmac_f32_16x16x64_bf16 v[106:109], v[82:85], v[128:135], v66\n"
"ds_read_b64_tr_b16 v[124:125], v182 offset:41408\n"
"v_mfma_f32_16x16x32_bf16 v[82:85], v[172:175], v[82:85], 0\n"
"ds_read_b64_tr_b16 v[126:127], v183 offset:41408\n"
"ds_read_b64_tr_b16 v[112:113], v182 offset:40648\n"
"v_mfma_f32_16x16x32_bf16 v[216:219], a[216:219], v[68:71], 0\n"
"ds_read_b64_tr_b16 v[114:115], v183 offset:40648\n"
"v_fmamk_f32 v246, v246, 0x3e553b95, v170\n"
"v_mfma_f32_16x16x32_bf16 v[220:223], a[220:223], v[68:71], 0\n"
"ds_read_b64_tr_b16 v[116:117], v182 offset:41416\n"
"v_fmamk_f32 v247, v247, 0x3e553b95, v170\n"
"v_mfma_f32_16x16x32_bf16 v[224:227], a[224:227], v[68:71], 0\n"
"ds_read_b128 v[68:71], v192 offset:33792\n"
"v_fmamk_f32 v248, v248, 0x3e553b95, v170\n"
"v_mfma_f32_16x16x32_bf16 v[216:219], a[228:231], v[32:35], v[216:219]\n"
"ds_read_b64_tr_b16 v[118:119], v183 offset:41416\n"
"v_fmamk_f32 v249, v249, 0x3e553b95, v170\n"
"v_mfma_f32_16x16x32_bf16 v[220:223], a[232:235], v[32:35], v[220:223]\n"
"ds_read_b64_tr_b16 v[136:137], v182 offset:40584\n"
"v_fmamk_f32 v250, v250, 0x3e553b95, v170\n"
"v_mfma_f32_16x16x32_bf16 v[224:227], a[236:239], v[32:35], v[224:227]\n"
"ds_read_b128 v[32:35], v192 offset:34816\n"
"v_fmamk_f32 v251, v251, 0x3e553b95, v170\n"
"v_mfma_f32_16x16x32_bf16 v[216:219], a[240:243], v[36:39], v[216:219]\n"
"ds_read_b64_tr_b16 v[138:139], v183 offset:40584\n"
"v_exp_f32 v244, v244\n"
"v_mfma_f32_16x16x32_bf16 v[220:223], a[244:247], v[36:39], v[220:223]\n"
"ds_read_b64_tr_b16 v[140:141], v182 offset:41352\n"
"v_exp_f32 v245, v245\n"
"v_mfma_f32_16x16x32_bf16 v[224:227], a[248:251], v[36:39], v[224:227]\n"
"s_waitcnt vmcnt(5)\n"
"ds_read_b128 v[36:39], v192 offset:35840\n"
"ds_read_b64_tr_b16 v[142:143], v183 offset:41352\n"
"v_mfma_f32_16x16x32_bf16 v[216:219], a[252:255], v[40:43], v[216:219]\n"
"v_exp_f32 v246, v246\n"
"v_mfma_f32_16x16x32_bf16 v[220:223], v[0:3], v[40:43], v[220:223]\n"
"v_exp_f32 v247, v247\n"
"v_mfma_f32_16x16x32_bf16 v[224:227], v[4:7], v[40:43], v[224:227]\n"
"v_cvt_pk_bf16_f32 v90, v244, v246\n"
"v_cvt_pk_bf16_f32 v91, v245, v247\n"
"v_mfma_f32_16x16x32_bf16 v[216:219], v[8:11], v[44:47], v[216:219]\n"
"v_exp_f32 v248, v248\n"
"v_mfma_f32_16x16x32_bf16 v[220:223], v[12:15], v[44:47], v[220:223]\n"
"v_exp_f32 v249, v249\n"
"v_mfma_f32_16x16x32_bf16 v[224:227], v[16:19], v[44:47], v[224:227]\n"
"v_exp_f32 v250, v250\n"
"v_mfma_f32_16x16x32_bf16 v[216:219], v[20:23], v[48:51], v[216:219]\n"
"v_exp_f32 v251, v251\n"
"v_mfma_f32_16x16x32_bf16 v[220:223], v[24:27], v[48:51], v[220:223]\n"
"v_cvt_pk_bf16_f32 v92, v248, v250\n"
"v_cvt_pk_bf16_f32 v93, v249, v251\n"
"v_mfma_f32_16x16x32_bf16 v[224:227], v[28:31], v[48:51], v[224:227]\n"
"s_waitcnt lgkmcnt(0)\n"
"ds_read_b128 v[72:75], v202 offset:49152\n"
"ds_read_b128 v[76:79], v202 offset:53248\n"
"v_smfmac_f32_16x16x64_bf16 v[252:255], v[90:93], v[120:127], v66\n"
"v_cvt_pk_bf16_f32 v204, v204, v205\n"
"v_smfmac_f32_16x16x64_bf16 v[98:101], v[90:93], v[112:119], v66\n"
"v_cvt_pk_bf16_f32 v205, v206, v207\n"
"v_smfmac_f32_16x16x64_bf16 v[106:109], v[90:93], v[136:143], v66\n"
"v_cvt_pk_bf16_f32 v206, v208, v209\n"
"v_mfma_f32_16x16x32_bf16 v[90:93], v[172:175], v[90:93], v[82:85]\n"
"v_cvt_pk_bf16_f32 v207, v210, v211\n"
"v_cvt_pk_bf16_f32 v212, v212, v213\n"
"v_mfma_f32_16x16x32_bf16 v[232:235], a[216:219], v[52:55], 0\n"
"ds_write_b128 v196, v[204:207] offset:12288\n"
"v_mfma_f32_16x16x32_bf16 v[236:239], a[220:223], v[52:55], 0\n"
"buffer_load_dwordx4 v[144:147], v192, s[28:31], s16 offen sc1\n"
"v_cvt_pk_bf16_f32 v252, v252, v253\n"
"v_mfma_f32_16x16x32_bf16 v[240:243], a[224:227], v[52:55], 0\n"
"s_waitcnt lgkmcnt(2)\n"
"v_rcp_f32 v171, v90\n"
"v_cvt_pk_bf16_f32 v253, v254, v255\n"
"v_cvt_pk_bf16_f32 v171, v171, v171\n"
"v_and_b32 v64, v171, v176\n"
"v_and_b32 v65, v171, v177\n"
"v_cvt_pk_bf16_f32 v98, v98, v99\n"
"v_cvt_pk_bf16_f32 v99, v100, v101\n"
"v_mfma_f32_16x16x16_bf16 v[252:255], v[252:253], v[64:65], 0\n"
"s_waitcnt lgkmcnt(1)\n"
"buffer_load_dwordx2 v[154:155], v193, s[28:31], s16 offen offset:1024 sc1\n"
"v_cvt_pk_bf16_f32 v106, v106, v107\n"
"v_mfma_f32_16x16x16_bf16 v[98:101], v[98:99], v[64:65], 0\n"
"v_cvt_pk_bf16_f32 v107, v108, v109\n"
"v_cvt_pk_bf16_f32 v213, v214, v215\n"
"v_cvt_pk_bf16_f32 v216, v216, v217\n"
"v_mfma_f32_16x16x16_bf16 v[106:109], v[106:107], v[64:65], 0\n"
"v_cvt_pk_bf16_f32 v252, v252, v253\n"
"v_cvt_pk_bf16_f32 v253, v254, v255\n"
"v_min_u32 v252, -2, v252\n"
"s_nop 0\n"
"v_cvt_pk_bf16_f32 v254, v98, v99\n"
"v_cvt_pk_bf16_f32 v255, v100, v101\n"
"v_min_u32 v253, -2, v253\n"
"v_min_u32 v254, -2, v254\n"
"v_cvt_pk_bf16_f32 v106, v106, v107\n"
"v_cvt_pk_bf16_f32 v107, v108, v109\n"
"v_min_u32 v255, -2, v255\n"
"buffer_store_dwordx4 v[252:255], v192, s[28:31], s37 offen sc1\n"
"s_waitcnt vmcnt(4)\n"
"ds_write_b128 v202, a[162:165] offset:36864\n"
"v_min_u32 v106, -2, v106\n"
"v_min_u32 v107, -2, v107\n"
"buffer_store_dwordx2 v[106:107], v193, s[28:31], s37 offen offset:1024 sc1\n"
"ds_write_b64 v197, v[212:213] offset:12288\n"
"s_add_u32 s37, s37, 6144\n"
"v_cvt_pk_bf16_f32 v217, v218, v219\n"
"v_cvt_pk_bf16_f32 v218, v220, v221\n"
"v_cvt_pk_bf16_f32 v219, v222, v223\n"
"s_waitcnt vmcnt(4)\n"
"ds_write_b64 v203, a[166:167] offset:36864\n"
"buffer_store_dwordx4 v[72:75], v194, s[24:27], s36 offen sc1\n"
"s_nop 1\n"
"ds_read_b128 v[72:75], v192 offset:31744\n"
"v_cvt_pk_bf16_f32 v224, v224, v225\n"
"v_cvt_pk_bf16_f32 v225, v226, v227\n"
"ds_write_b128 v196, v[216:219] offset:43008\n"
"v_max3_u32 v200, v228, v229, v230\n"
"buffer_store_dwordx4 v[76:79], v195, s[24:27], s36 offen sc1\n"
"s_nop 1\n"
"ds_read_b128 v[76:79], v192 offset:32768\n"
"s_add_u32 s36, s36, s33\n"
"ds_write_b64 v197, v[224:225] offset:43008\n"
"v_max3_u32 v200, v200, v231, v152\n"
"v_max_u32 v200, v200, v153\n"
"v_cmp_eq_u32 vcc, -1, v200\n"
"s_nop 4\n"
"s_cbranch_vccnz .Lp11%=\n"
".Lr11%=:\n"
"ds_write_b128 v202, v[228:231] offset:18432\n"
"s_waitcnt lgkmcnt(4)\n"
"v_mfma_f32_16x16x32_bf16 v[232:235], a[228:231], v[72:75], v[232:235]\n"
"v_mfma_f32_16x16x32_bf16 v[236:239], a[232:235], v[72:75], v[236:239]\n"
"ds_write_b64 v203, v[152:153] offset:18432\n"
"v_mfma_f32_16x16x32_bf16 v[240:243], a[236:239], v[72:75], v[240:243]\n"
"s_waitcnt lgkmcnt(3)\n"
"v_mfma_f32_16x16x32_bf16 v[232:235], a[240:243], v[76:79], v[232:235]\n"
"v_mfma_f32_16x16x32_bf16 v[236:239], a[244:247], v[76:79], v[236:239]\n"
"v_mfma_f32_16x16x32_bf16 v[240:243], a[248:251], v[76:79], v[240:243]\n"
"v_mfma_f32_16x16x32_bf16 v[232:235], a[252:255], v[68:71], v[232:235]\n"
"v_mfma_f32_16x16x32_bf16 v[236:239], v[0:3], v[68:71], v[236:239]\n"
"v_mfma_f32_16x16x32_bf16 v[240:243], v[4:7], v[68:71], v[240:243]\n"
"s_waitcnt lgkmcnt(0)\n"
"v_mfma_f32_16x16x32_bf16 v[232:235], v[8:11], v[32:35], v[232:235]\n"
"s_barrier\n"
"v_mfma_f32_16x16x32_bf16 v[236:239], v[12:15], v[32:35], v[236:239]\n"
"ds_read_b128 v[72:75], v192 offset:18432\n"
"ds_read_b128 v[76:79], v192 offset:19456\n"
"v_mfma_f32_16x16x32_bf16 v[240:243], v[16:19], v[32:35], v[240:243]\n"
"ds_read_b128 v[68:71], v192 offset:20480\n"
"v_mfma_f32_16x16x32_bf16 v[232:235], v[20:23], v[36:39], v[232:235]\n"
"ds_read_b128 v[32:35], v192 offset:21504\n"
"v_mfma_f32_16x16x32_bf16 v[236:239], v[24:27], v[36:39], v[236:239]\n"
"ds_read_b128 v[40:43], v202\n"
"v_mfma_f32_16x16x32_bf16 v[240:243], v[28:31], v[36:39], v[240:243]\n"
"ds_read_b128 v[36:39], v192 offset:22528\n"
"s_nop 2\n"
"v_cvt_pk_bf16_f32 v232, v232, v233\n"
"v_cvt_pk_bf16_f32 v233, v234, v235\n"
"v_cvt_pk_bf16_f32 v234, v236, v237\n"
"v_cvt_pk_bf16_f32 v235, v238, v239\n"
"v_cvt_pk_bf16_f32 v240, v240, v241\n"
"v_cvt_pk_bf16_f32 v241, v242, v243\n"
"ds_write_b128 v196, v[232:235] offset:6144\n"
"s_waitcnt lgkmcnt(6)\n"
"v_mfma_f32_16x16x32_bf16 v[204:207], a[216:219], v[72:75], 0\n"
"s_waitcnt lgkmcnt(5)\n"
"v_mfma_f32_16x16x32_bf16 v[208:211], a[220:223], v[72:75], 0\n"
"ds_write_b64 v197, v[240:241] offset:6144\n"
"v_mfma_f32_16x16x32_bf16 v[212:215], a[224:227], v[72:75], 0\n"
"s_waitcnt lgkmcnt(3)\n"
"v_mfma_f32_16x16x32_bf16 v[204:207], a[228:231], v[76:79], v[204:207]\n"
"ds_read_b128 v[44:47], v202 offset:4096\n"
"ds_read_b128 v[48:51], v202 offset:12288\n"
"v_mfma_f32_16x16x32_bf16 v[208:211], a[232:235], v[76:79], v[208:211]\n"
"ds_read_b128 v[52:55], v202 offset:16384\n"
"buffer_store_dwordx4 v[40:43], v194, s[24:27], s36 offen sc1\n"
"v_mfma_f32_16x16x32_bf16 v[212:215], a[236:239], v[76:79], v[212:215]\n"
"s_nop 0\n"
"ds_read_b128 v[40:43], v192 offset:23552\n"
"v_mfma_f32_16x16x32_bf16 v[204:207], a[240:243], v[68:71], v[204:207]\n"
"v_mfma_f32_16x16x32_bf16 v[208:211], a[244:247], v[68:71], v[208:211]\n"
"v_mfma_f32_16x16x32_bf16 v[212:215], a[248:251], v[68:71], v[212:215]\n"
"s_waitcnt vmcnt(6)\n"
"v_max3_u32 v200, v144, v145, v146\n"
"v_mfma_f32_16x16x32_bf16 v[204:207], a[252:255], v[32:35], v[204:207]\n"
"s_waitcnt lgkmcnt(6)\n"
"v_mfma_f32_16x16x32_bf16 v[208:211], v[0:3], v[32:35], v[208:211]\n"
"s_waitcnt lgkmcnt(3)\n"
"buffer_store_dwordx4 v[44:47], v195, s[24:27], s36 offen sc1\n"
"v_mfma_f32_16x16x32_bf16 v[212:215], v[4:7], v[32:35], v[212:215]\n"
"s_waitcnt vmcnt(6) lgkmcnt(1)\n"
"v_max3_u32 v200, v200, v147, v154\n"
"v_max_u32 v200, v200, v155\n"
"v_mfma_f32_16x16x32_bf16 v[204:207], v[8:11], v[36:39], v[204:207]\n"
"s_waitcnt lgkmcnt(0)\n"
"v_cmp_eq_u32 vcc, -1, v200\n"
"s_barrier\n"
"v_mfma_f32_16x16x32_bf16 v[208:211], v[12:15], v[36:39], v[208:211]\n"
"s_add_u32 s36, s36, s33\n"
"ds_read_b128 v[76:79], v192 offset:36864\n"
"ds_read_b128 v[68:71], v192 offset:37888\n"
"v_mfma_f32_16x16x32_bf16 v[212:215], v[16:19], v[36:39], v[212:215]\n"
"s_cbranch_vccnz .Lp12%=\n"
".Lr12%=:\n"
"v_mfma_f32_16x16x32_bf16 v[204:207], v[20:23], v[40:43], v[204:207]\n"
"ds_write_b128 v202, v[144:147] offset:49152\n"
"v_mfma_f32_16x16x32_bf16 v[208:211], v[24:27], v[40:43], v[208:211]\n"
"buffer_store_dwordx4 v[48:51], v194, s[24:27], s36 offen sc1\n"
"v_mfma_f32_16x16x32_bf16 v[212:215], v[28:31], v[40:43], v[212:215]\n"
"ds_write_b64 v203, v[154:155] offset:49152\n"
"s_nop 2\n"
"v_cvt_pk_bf16_f32 v204, v204, v205\n"
"v_cvt_pk_bf16_f32 v205, v206, v207\n"
"v_cvt_pk_bf16_f32 v206, v208, v209\n"
"v_cvt_pk_bf16_f32 v207, v210, v211\n"
"v_cvt_pk_bf16_f32 v212, v212, v213\n"
"v_cvt_pk_bf16_f32 v213, v214, v215\n"
"s_waitcnt lgkmcnt(3)\n"
"v_mfma_f32_16x16x32_bf16 v[216:219], a[216:219], v[76:79], 0\n"
"s_waitcnt lgkmcnt(2)\n"
"ds_read_b128 v[32:35], v192 offset:38912\n"
"ds_read_b128 v[36:39], v192 offset:39936\n"
"v_mfma_f32_16x16x32_bf16 v[220:223], a[220:223], v[76:79], 0\n"
"ds_read_b128 v[40:43], v192 offset:40960\n"
"buffer_store_dwordx4 v[52:55], v195, s[24:27], s36 offen sc1\n"
"v_mfma_f32_16x16x32_bf16 v[224:227], a[224:227], v[76:79], 0\n"
"s_add_u32 s36, s36, s33\n"
"ds_read_b128 v[44:47], v202 offset:43008\n"
"ds_write_b128 v196, v[204:207] offset:55296\n"
"v_mfma_f32_16x16x32_bf16 v[216:219], a[228:231], v[68:71], v[216:219]\n"
"s_add_u32 s8, s8, s36\n"
"s_add_u32 s9, s9, s36\n"
"s_add_u32 s10, s10, s36\n"
"v_mfma_f32_16x16x32_bf16 v[220:223], a[232:235], v[68:71], v[220:223]\n"
"s_add_u32 s11, s11, s36\n"
"s_add_u32 s20, s20, s36\n"
"ds_write_b64 v197, v[212:213] offset:55296\n"
"v_mfma_f32_16x16x32_bf16 v[224:227], a[236:239], v[68:71], v[224:227]\n"
"s_waitcnt lgkmcnt(5)\n"
"s_add_u32 s21, s21, s36\n"
"v_mfma_f32_16x16x32_bf16 v[216:219], a[240:243], v[32:35], v[216:219]\n"
"s_waitcnt lgkmcnt(4)\n"
"ds_read_b128 v[48:51], v202 offset:47104\n"
"ds_read_b128 v[52:55], v202 offset:6144\n"
"v_mfma_f32_16x16x32_bf16 v[220:223], a[244:247], v[32:35], v[220:223]\n"
"buffer_load_dwordx4 v[56:59], v192, s[28:31], s17 offen sc1\n"
"ds_read_b128 v[72:75], v202 offset:10240\n"
"v_mfma_f32_16x16x32_bf16 v[224:227], a[248:251], v[32:35], v[224:227]\n"
"s_waitcnt lgkmcnt(5)\n"
"v_mfma_f32_16x16x32_bf16 v[216:219], a[252:255], v[36:39], v[216:219]\n"
"v_mfma_f32_16x16x32_bf16 v[220:223], v[0:3], v[36:39], v[220:223]\n"
"buffer_load_dwordx2 v[60:61], v193, s[28:31], s17 offen offset:1024 sc1\n"
"v_mfma_f32_16x16x32_bf16 v[224:227], v[4:7], v[36:39], v[224:227]\n"
"s_waitcnt lgkmcnt(3)\n"
"v_mfma_f32_16x16x32_bf16 v[216:219], v[8:11], v[40:43], v[216:219]\n"
"s_barrier\n"
"v_mfma_f32_16x16x32_bf16 v[220:223], v[12:15], v[40:43], v[220:223]\n"
"ds_read_b128 v[76:79], v192 offset:50176\n"
"ds_read_b128 v[68:71], v192 offset:51200\n"
"v_mfma_f32_16x16x32_bf16 v[224:227], v[16:19], v[40:43], v[224:227]\n"
"s_waitcnt lgkmcnt(4)\n"
"ds_read_b128 v[32:35], v192 offset:52224\n"
"buffer_store_dwordx4 v[44:47], v194, s[24:27], s8 offen sc1\n"
"s_nop 1\n"
"ds_read_b128 v[44:47], v192 offset:41984\n"
"ds_read_b128 v[36:39], v192 offset:53248\n"
"ds_read_b128 v[40:43], v192 offset:54272\n"
"buffer_store_dwordx4 v[48:51], v195, s[24:27], s8 offen sc1\n"
"s_nop 1\n"
"ds_read_b128 v[48:51], v202 offset:55296\n"
"s_waitcnt lgkmcnt(7)\n"
"buffer_store_dwordx4 v[52:55], v194, s[24:27], s9 offen sc1\n"
"s_nop 1\n"
"ds_read_b128 v[52:55], v202 offset:59392\n"
"s_waitcnt lgkmcnt(4)\n"
"v_mfma_f32_16x16x32_bf16 v[216:219], v[20:23], v[44:47], v[216:219]\n"
"v_mfma_f32_16x16x32_bf16 v[220:223], v[24:27], v[44:47], v[220:223]\n"
"v_mfma_f32_16x16x32_bf16 v[224:227], v[28:31], v[44:47], v[224:227]\n"
"s_waitcnt lgkmcnt(1)\n"
"buffer_store_dwordx4 v[72:75], v195, s[24:27], s9 offen sc1\n"
"s_nop 1\n"
"ds_read_b128 v[72:75], v192 offset:49152\n"
"s_nop 0\n"
"v_cvt_pk_bf16_f32 v216, v216, v217\n"
"v_cvt_pk_bf16_f32 v217, v218, v219\n"
"v_cvt_pk_bf16_f32 v218, v220, v221\n"
"v_cvt_pk_bf16_f32 v219, v222, v223\n"
"v_cvt_pk_bf16_f32 v224, v224, v225\n"
"v_cvt_pk_bf16_f32 v225, v226, v227\n"
"ds_write_b128 v196, v[216:219] offset:30720\n"
"buffer_store_dwordx4 v[48:51], v194, s[24:27], s10 offen sc1\n"
"ds_write_b64 v197, v[224:225] offset:30720\n"
"s_waitcnt lgkmcnt(3)\n"
"buffer_store_dwordx4 v[52:55], v195, s[24:27], s10 offen sc1\n"
"s_waitcnt lgkmcnt(2)\n"
"v_mfma_f32_16x16x32_bf16 v[232:235], a[216:219], v[72:75], 0\n"
"v_mfma_f32_16x16x32_bf16 v[236:239], a[220:223], v[72:75], 0\n"
"v_mfma_f32_16x16x32_bf16 v[240:243], a[224:227], v[72:75], 0\n"
"v_mfma_f32_16x16x32_bf16 v[232:235], a[228:231], v[76:79], v[232:235]\n"
"v_mfma_f32_16x16x32_bf16 v[236:239], a[232:235], v[76:79], v[236:239]\n"
"v_mfma_f32_16x16x32_bf16 v[240:243], a[236:239], v[76:79], v[240:243]\n"
"v_mfma_f32_16x16x32_bf16 v[232:235], a[240:243], v[68:71], v[232:235]\n"
"v_mfma_f32_16x16x32_bf16 v[240:243], a[248:251], v[68:71], v[240:243]\n"
"v_mfma_f32_16x16x32_bf16 v[236:239], a[244:247], v[68:71], v[236:239]\n"
"v_mfma_f32_16x16x32_bf16 v[232:235], a[252:255], v[32:35], v[232:235]\n"
"v_mfma_f32_16x16x32_bf16 v[240:243], v[4:7], v[32:35], v[240:243]\n"
"v_mfma_f32_16x16x32_bf16 v[236:239], v[0:3], v[32:35], v[236:239]\n"
"v_mfma_f32_16x16x32_bf16 v[232:235], v[8:11], v[36:39], v[232:235]\n"
"v_mfma_f32_16x16x32_bf16 v[240:243], v[16:19], v[36:39], v[240:243]\n"
"v_mfma_f32_16x16x32_bf16 v[236:239], v[12:15], v[36:39], v[236:239]\n"
"v_mfma_f32_16x16x32_bf16 v[232:235], v[20:23], v[40:43], v[232:235]\n"
"v_mfma_f32_16x16x32_bf16 v[240:243], v[28:31], v[40:43], v[240:243]\n"
"v_mfma_f32_16x16x32_bf16 v[236:239], v[24:27], v[40:43], v[236:239]\n"
"s_nop 5\n"
"v_cvt_pk_bf16_f32 v232, v232, v233\n"
"v_cvt_pk_bf16_f32 v233, v234, v235\n"
"v_cvt_pk_bf16_f32 v240, v240, v241\n"
"v_cvt_pk_bf16_f32 v234, v236, v237\n"
"v_cvt_pk_bf16_f32 v235, v238, v239\n"
"v_cvt_pk_bf16_f32 v241, v242, v243\n"
"ds_write_b128 v196, v[232:235] offset:24576\n"
"ds_write_b64 v197, v[240:241] offset:24576\n"
"s_waitcnt vmcnt(7)\n"
"v_max3_u32 v200, v56, v57, v58\n"
"s_waitcnt vmcnt(6)\n"
"v_max3_u32 v200, v200, v59, v60\n"
"v_max_u32 v200, v200, v61\n"
"v_cmp_eq_u32 vcc, -1, v200\n"
"s_nop 4\n"
"s_cbranch_vccnz .Lp13%=\n"
".Lr13%=:\n"
"ds_write_b128 v202, v[56:59]\n"
"ds_write_b64 v203, v[60:61]\n"
"s_waitcnt lgkmcnt(0)\n"
"s_barrier\n"
"ds_read_b128 v[76:79], v192\n"
"ds_read_b128 v[68:71], v192 offset:1024\n"
"ds_read_b128 v[32:35], v192 offset:2048\n"
"ds_read_b128 v[36:39], v192 offset:3072\n"
"ds_read_b128 v[40:43], v192 offset:4096\n"
"ds_read_b128 v[44:47], v202 offset:30720\n"
"ds_read_b128 v[48:51], v202 offset:34816\n"
"ds_read_b128 v[52:55], v202 offset:24576\n"
"ds_read_b128 v[72:75], v202 offset:28672\n"
"s_waitcnt lgkmcnt(8)\n"
"v_mfma_f32_16x16x32_bf16 v[204:207], a[216:219], v[76:79], 0\n"
"s_waitcnt lgkmcnt(7)\n"
"v_mfma_f32_16x16x32_bf16 v[212:215], a[224:227], v[76:79], 0\n"
"v_mfma_f32_16x16x32_bf16 v[208:211], a[220:223], v[76:79], 0\n"
"v_mfma_f32_16x16x32_bf16 v[204:207], a[228:231], v[68:71], v[204:207]\n"
"s_waitcnt lgkmcnt(6)\n"
"v_mfma_f32_16x16x32_bf16 v[212:215], a[236:239], v[68:71], v[212:215]\n"
"s_waitcnt lgkmcnt(3)\n"
"buffer_store_dwordx4 v[44:47], v194, s[24:27], s11 offen sc1\n"
"v_mfma_f32_16x16x32_bf16 v[208:211], a[232:235], v[68:71], v[208:211]\n"
"s_waitcnt lgkmcnt(1)\n"
"ds_read_b128 v[44:47], v192 offset:5120\n"
"v_mfma_f32_16x16x32_bf16 v[204:207], a[240:243], v[32:35], v[204:207]\n"
"s_waitcnt lgkmcnt(1)\n"
"v_mfma_f32_16x16x32_bf16 v[212:215], a[248:251], v[32:35], v[212:215]\n"
"buffer_store_dwordx4 v[48:51], v195, s[24:27], s11 offen sc1\n"
"v_mfma_f32_16x16x32_bf16 v[208:211], a[244:247], v[32:35], v[208:211]\n"
"v_mfma_f32_16x16x32_bf16 v[204:207], a[252:255], v[36:39], v[204:207]\n"
"v_mfma_f32_16x16x32_bf16 v[212:215], v[4:7], v[36:39], v[212:215]\n"
"v_mfma_f32_16x16x32_bf16 v[208:211], v[0:3], v[36:39], v[208:211]\n"
"buffer_store_dwordx4 v[52:55], v194, s[24:27], s20 offen sc1\n"
"v_mfma_f32_16x16x32_bf16 v[204:207], v[8:11], v[40:43], v[204:207]\n"
"s_waitcnt lgkmcnt(0)\n"
"v_mfma_f32_16x16x32_bf16 v[212:215], v[16:19], v[40:43], v[212:215]\n"
"v_mfma_f32_16x16x32_bf16 v[208:211], v[12:15], v[40:43], v[208:211]\n"
"buffer_store_dwordx4 v[72:75], v195, s[24:27], s20 offen sc1\n"
"v_mfma_f32_16x16x32_bf16 v[204:207], v[20:23], v[44:47], v[204:207]\n"
"v_mfma_f32_16x16x32_bf16 v[212:215], v[28:31], v[44:47], v[212:215]\n"
"v_mfma_f32_16x16x32_bf16 v[208:211], v[24:27], v[44:47], v[208:211]\n"
"s_nop 5\n"
"v_cvt_pk_bf16_f32 v204, v204, v205\n"
"v_cvt_pk_bf16_f32 v205, v206, v207\n"
"v_cvt_pk_bf16_f32 v212, v212, v213\n"
"v_cvt_pk_bf16_f32 v206, v208, v209\n"
"v_cvt_pk_bf16_f32 v207, v210, v211\n"
"v_cvt_pk_bf16_f32 v213, v214, v215\n"
"ds_write_b128 v196, v[204:207] offset:12288\n"
"ds_write_b64 v197, v[212:213] offset:12288\n"
"s_waitcnt lgkmcnt(0)\n"
"s_barrier\n"
"ds_read_b128 v[48:51], v202 offset:12288\n"
"ds_read_b128 v[52:55], v202 offset:16384\n"
"s_waitcnt lgkmcnt(1)\n"
"buffer_store_dwordx4 v[48:51], v194, s[24:27], s21 offen sc1\n"
"s_waitcnt lgkmcnt(0)\n"
"buffer_store_dwordx4 v[52:55], v195, s[24:27], s21 offen sc1\n"
"s_branch .Lend%=\n"
".Lp1%=:\n"
"s_sub_u32 s42, s42, 1\n"
"s_cbranch_scc1 .Lt1%=\n"
"s_sleep 1\n"
"buffer_load_dword v200, off, s[28:31], s39 sc1\n"
"s_waitcnt vmcnt(0)\n"
"v_add3_u32 v200, v200, s50, -2\n"
"v_cmp_lt_u32 vcc, 64, v200\n"
"s_nop 4\n"
"s_cbranch_vccnz .Lp1%=\n"
"s_branch .Lr1%=\n"
".Lt1%=:\n"
"s_mov_b32 s42, 0\n"
"s_branch .Lr1%=\n"
".Lp3%=:\n"
"s_sub_u32 s42, s42, 1\n"
"s_cbranch_scc1 .Lt3%=\n"
"s_sleep 1\n"
"buffer_load_dword v200, off, s[28:31], s39 sc1\n"
"s_waitcnt vmcnt(0)\n"
"v_add3_u32 v200, v200, s50, -4\n"
"v_cmp_lt_u32 vcc, 64, v200\n"
"s_nop 4\n"
"s_cbranch_vccnz .Lp3%=\n"
"s_branch .Lr3%=\n"
".Lt3%=:\n"
"s_mov_b32 s42, 0\n"
"s_branch .Lr3%=\n"
".Lp4%=:\n"
"s_sub_u32 s42, s42, 1\n"
"s_cbranch_scc1 .Lt4%=\n"
"s_sleep 1\n"
"buffer_load_dword v200, off, s[28:31], s39 sc1\n"
"s_waitcnt vmcnt(0)\n"
"v_add3_u32 v200, v200, s50, -5\n"
"v_cmp_lt_u32 vcc, 64, v200\n"
"s_nop 4\n"
"s_cbranch_vccnz .Lp4%=\n"
"s_branch .Lr4%=\n"
".Lt4%=:\n"
"s_mov_b32 s42, 0\n"
"s_branch .Lr4%=\n"
".Lp6%=:\n"
"s_sub_u32 s42, s42, 1\n"
"s_cbranch_scc1 .Lt6%=\n"
"s_sleep 1\n"
"buffer_load_dword v200, off, s[28:31], s39 sc1\n"
"s_waitcnt vmcnt(0)\n"
"v_add3_u32 v200, v200, s50, -7\n"
"v_cmp_lt_u32 vcc, 64, v200\n"
"s_nop 4\n"
"s_cbranch_vccnz .Lp6%=\n"
"s_branch .Lr6%=\n"
".Lt6%=:\n"
"s_mov_b32 s42, 0\n"
"s_branch .Lr6%=\n"
".Lp7%=:\n"
"s_sub_u32 s42, s42, 1\n"
"s_cbranch_scc1 .Lt7%=\n"
"s_sleep 1\n"
"buffer_load_dword v200, off, s[28:31], s39 sc1\n"
"s_waitcnt vmcnt(0)\n"
"v_add3_u32 v200, v200, s50, -8\n"
"v_cmp_lt_u32 vcc, 64, v200\n"
"s_nop 4\n"
"s_cbranch_vccnz .Lp7%=\n"
"s_branch .Lr7%=\n"
".Lt7%=:\n"
"s_mov_b32 s42, 0\n"
"s_branch .Lr7%=\n"
".Lp9%=:\n"
"s_sub_u32 s42, s42, 1\n"
"s_cbranch_scc1 .Lt9%=\n"
"s_sleep 1\n"
"buffer_load_dword v200, off, s[28:31], s39 sc1\n"
"s_waitcnt vmcnt(0)\n"
"v_add3_u32 v200, v200, s50, -10\n"
"v_cmp_lt_u32 vcc, 64, v200\n"
"s_nop 4\n"
"s_cbranch_vccnz .Lp9%=\n"
"s_branch .Lr9%=\n"
".Lt9%=:\n"
"s_mov_b32 s42, 0\n"
"s_branch .Lr9%=\n"
".Lp10%=:\n"
"s_sub_u32 s42, s42, 1\n"
"s_cbranch_scc1 .Lt10%=\n"
"s_sleep 1\n"
"buffer_load_dword v201, off, s[28:31], s39 sc1\n"
"s_waitcnt vmcnt(0)\n"
"v_add3_u32 v201, v201, s50, -11\n"
"v_cmp_lt_u32 vcc, 64, v201\n"
"s_nop 4\n"
"s_cbranch_vccnz .Lp10%=\n"
"s_branch .Lr10%=\n"
".Lt10%=:\n"
"s_mov_b32 s42, 0\n"
"s_branch .Lr10%=\n"
".Lp11%=:\n"
"s_sub_u32 s42, s42, 1\n"
"s_cbranch_scc1 .Lt11%=\n"
"buffer_load_dwordx4 v[228:231], v192, s[28:31], s14 offen sc1\n"
"buffer_load_dwordx2 v[152:153], v193, s[28:31], s14 offen offset:1024 sc1\n"
"s_waitcnt vmcnt(0)\n"
"v_max3_u32 v200, v228, v229, v230\n"
"v_max3_u32 v200, v200, v231, v152\n"
"v_max_u32 v200, v200, v153\n"
"v_cmp_eq_u32 vcc, -1, v200\n"
"s_nop 4\n"
"s_cbranch_vccnz .Lp11%=\n"
"s_branch .Lr11%=\n"
".Lt11%=:\n"
"s_mov_b32 s42, 0\n"
"s_branch .Lr11%=\n"
".Lp12%=:\n"
"s_sub_u32 s42, s42, 1\n"
"s_cbranch_scc1 .Lt12%=\n"
"buffer_load_dwordx4 v[144:147], v192, s[28:31], s16 offen sc1\n"
"buffer_load_dwordx2 v[154:155], v193, s[28:31], s16 offen offset:1024 sc1\n"
"s_waitcnt vmcnt(0)\n"
"v_max3_u32 v200, v144, v145, v146\n"
"v_max3_u32 v200, v200, v147, v154\n"
"v_max_u32 v200, v200, v155\n"
"v_cmp_eq_u32 vcc, -1, v200\n"
"s_nop 4\n"
"s_cbranch_vccnz .Lp12%=\n"
"s_branch .Lr12%=\n"
".Lt12%=:\n"
"s_mov_b32 s42, 0\n"
"s_branch .Lr12%=\n"
".Lp13%=:\n"
"s_sub_u32 s42, s42, 1\n"
"s_cbranch_scc1 .Lt13%=\n"
"buffer_load_dwordx4 v[56:59], v192, s[28:31], s17 offen sc1\n"
"buffer_load_dwordx2 v[60:61], v193, s[28:31], s17 offen offset:1024 sc1\n"
"s_waitcnt vmcnt(0)\n"
"v_max3_u32 v200, v56, v57, v58\n"
"v_max3_u32 v200, v200, v59, v60\n"
"v_max_u32 v200, v200, v61\n"
"v_cmp_eq_u32 vcc, -1, v200\n"
"s_nop 4\n"
"s_cbranch_vccnz .Lp13%=\n"
"s_branch .Lr13%=\n"
".Lt13%=:\n"
"s_mov_b32 s42, 0\n"
"s_branch .Lr13%=\n"
".Lend%=:\n"
      : "+{v31}"(tid), "+{s2}"(bid), "+{s[0:1]}"(ka)
      : "i"((__attribute__((address_space(3))) char*)smem)
      : "memory", "scc", "vcc", VCLOB, ACLOB, SCLOB);
}

extern "C" void kernel_launch(void* const* d_in, const int* in_sizes, int n_in,
                              void* d_out, int out_size, void* d_ws, size_t ws_size,
                              hipStream_t stream) {
  const uint16_t* x = (const uint16_t*)d_in[0];
  const uint16_t* wqkv = (const uint16_t*)d_in[1];
  const uint16_t* wo = (const uint16_t*)d_in[2];
  const uint16_t* rpb = (const uint16_t*)d_in[3];
  dinat_hs<<<256, 256, 0, stream>>>(x, wqkv, wo, rpb, (uint16_t*)d_out, (char*)d_ws + (2 << 20)
  );
}
```

```cpp
#include <hip/hip_runtime.h>
#include <hip/hip_bf16.h>
#include <stdint.h>

#define VCLOB "v0", "v1", "v2", "v3", "v4", "v5", "v6", "v7", "v8", "v9", "v10", "v11", "v12", "v13", "v14", "v15", "v16", "v17", "v18", "v19", "v20", "v21", "v22", "v23", "v24", "v25", "v26", "v27", "v28", "v29", "v30", "v32", "v33", "v34", "v35", "v36", "v37", "v38", "v39", "v40", "v41", "v42", "v43", "v44", "v45", "v46", "v47", "v48", "v49", "v50", "v51", "v52", "v53", "v54", "v55", "v56", "v57", "v58", "v59", "v60", "v61", "v62", "v63", "v64", "v65", "v66", "v67", "v68", "v69", "v70", "v71", "v72", "v73", "v74", "v75", "v76", "v77", "v78", "v79", "v80", "v81", "v82", "v83", "v84", "v85", "v86", "v87", "v88", "v89", "v90", "v91", "v92", "v93", "v94", "v95", "v96", "v97", "v98", "v99", "v100", "v101", "v102", "v103", "v104", "v105", "v106", "v107", "v108", "v109", "v110", "v111", "v112", "v113", "v114", "v115", "v116", "v117", "v118", "v119", "v120", "v121", "v122", "v123", "v124", "v125", "v126", "v127", "v128", "v129", "v130", "v131", "v132", "v133", "v134", "v135", "v136", "v137", "v138", "v139", "v140", "v141", "v142", "v143", "v144", "v145", "v146", "v147", "v148", "v149", "v150", "v151", "v152", "v153", "v154", "v155", "v156", "v157", "v158", "v159", "v160", "v161", "v162", "v163", "v164", "v165", "v166", "v167", "v168", "v169", "v170", "v171", "v172", "v173", "v174", "v175", "v176", "v177", "v178", "v179", "v180", "v181", "v182", "v183", "v184", "v185", "v186", "v187", "v188", "v189", "v190", "v191", "v192", "v193", "v194", "v195", "v196", "v197", "v198", "v199", "v200", "v201", "v202", "v203", "v204", "v205", "v206", "v207", "v208", "v209", "v210", "v211", "v212", "v213", "v214", "v215", "v216", "v217", "v218", "v219", "v220", "v221", "v222", "v223", "v224", "v225", "v226", "v227", "v228", "v229", "v230", "v231", "v232", "v233", "v234", "v235", "v236", "v237", "v238", "v239", "v240", "v241", "v242", "v243", "v244", "v245", "v246", "v247", "v248", "v249", "v250", "v251", "v252", "v253", "v254", "v255"
#define ACLOB "a0", "a1", "a2", "a3", "a4", "a5", "a6", "a7", "a8", "a9", "a10", "a11", "a12", "a13", "a14", "a15", "a16", "a17", "a18", "a19", "a20", "a21", "a22", "a23", "a24", "a25", "a26", "a27", "a28", "a29", "a30", "a31", "a32", "a33", "a34", "a35", "a36", "a37", "a38", "a39", "a40", "a41", "a42", "a43", "a44", "a45", "a46", "a47", "a48", "a49", "a50", "a51", "a52", "a53", "a54", "a55", "a56", "a57", "a58", "a59", "a60", "a61", "a62", "a63", "a64", "a65", "a66", "a67", "a68", "a69", "a70", "a71", "a72", "a73", "a74", "a75", "a76", "a77", "a78", "a79", "a80", "a81", "a82", "a83", "a84", "a85", "a86", "a87", "a88", "a89", "a90", "a91", "a92", "a93", "a94", "a95", "a96", "a97", "a98", "a99", "a100", "a101", "a102", "a103", "a104", "a105", "a106", "a107", "a108", "a109", "a110", "a111", "a112", "a113", "a114", "a115", "a116", "a117", "a118", "a119", "a120", "a121", "a122", "a123", "a124", "a125", "a126", "a127", "a128", "a129", "a130", "a131", "a132", "a133", "a134", "a135", "a136", "a137", "a138", "a139", "a140", "a141", "a142", "a143", "a144", "a145", "a146", "a147", "a148", "a149", "a150", "a151", "a152", "a153", "a154", "a155", "a156", "a157", "a158", "a159", "a160", "a161", "a162", "a163", "a164", "a165", "a166", "a167", "a168", "a169", "a170", "a171", "a172", "a173", "a174", "a175", "a176", "a177", "a178", "a179", "a180", "a181", "a182", "a183", "a184", "a185", "a186", "a187", "a188", "a189", "a190", "a191", "a192", "a193", "a194", "a195", "a196", "a197", "a198", "a199", "a200", "a201", "a202", "a203", "a204", "a205", "a206", "a207", "a208", "a209", "a210", "a211", "a212", "a213", "a214", "a215", "a216", "a217", "a218", "a219", "a220", "a221", "a222", "a223", "a224", "a225", "a226", "a227", "a228", "a229", "a230", "a231", "a232", "a233", "a234", "a235", "a236", "a237", "a238", "a239", "a240", "a241", "a242", "a243", "a244", "a245", "a246", "a247", "a248", "a249", "a250", "a251", "a252", "a253", "a254", "a255"
#define SCLOB "s3", "s4", "s5", "s6", "s7", "s8", "s9", "s10", "s11", "s12", "s13", "s14", "s15", "s16", "s17", "s18", "s19", "s20", "s21", "s22", "s23", "s24", "s25", "s26", "s27", "s28", "s29", "s30", "s31", "s32", "s33", "s34", "s35", "s36", "s37", "s38", "s39", "s40", "s41", "s42", "s43", "s44", "s45", "s46", "s47", "s48", "s49", "s50", "s51", "s52", "s53", "s54", "s55", "s56", "s57", "s58", "s59", "s60", "s61", "s62", "s63", "s64", "s65", "s66", "s67", "s68", "s69", "s70", "s71", "s72", "s73", "s74", "s75", "s76", "s77", "s78", "s79", "s80", "s81", "s82", "s83", "s84", "s85", "s86", "s87", "s88", "s89", "s90", "s91", "s92", "s93", "s94", "s95", "s96", "s97", "s98", "s99", "s100", "s101"
#define LDS_BYTES 163392

__global__ __launch_bounds__(256, 1) __attribute__((aligned(4096))) void dinat_hs(const uint16_t* __restrict__ x, const uint16_t* __restrict__ wqkv,
                                                   const uint16_t* __restrict__ wo, const uint16_t* __restrict__ rpb,
                                                   uint16_t* __restrict__ out, char* __restrict__ ws
                                                   ) {
  __shared__ __attribute__((aligned(16))) char smem[LDS_BYTES];
  unsigned tid = threadIdx.x, bid = blockIdx.x;
  auto ka = __builtin_amdgcn_kernarg_segment_ptr();
  asm volatile(
"s_load_dwordx2 s[8:9], s[0:1], 0\n"
"s_load_dwordx2 s[12:13], s[0:1], 8\n"
"s_load_dwordx2 s[16:17], s[0:1], 16\n"
"s_load_dwordx2 s[20:21], s[0:1], 24\n"
"s_load_dwordx2 s[24:25], s[0:1], 32\n"
"s_load_dwordx2 s[28:29], s[0:1], 40\n"
"s_getpc_b64 s[4:5]\n"
".Lpc%=:\n"
"s_bfe_u32 s53, s2, 0x20005\n"
"s_and_b32 s54, s2, 1\n"
"s_lshr_b32 s55, s2, 7\n"
"s_mul_i32 s59, s53, 27\n"
"v_lshrrev_b32 v113, 6, v31\n"
"s_max_u32 s59, s59, 1\n"
"s_mul_i32 s58, s55, 4816896\n"
"s_mul_i32 s71, s54, 14278656\n"
"s_nop 0\n"
"v_readfirstlane_b32 s51, v113\n"
"s_sub_u32 s59, s59, 1\n"
"s_add_u32 s58, s58, s71\n"
"v_and_b32 v112, 63, v31\n"
"s_mul_i32 s72, s59, 768\n"
"s_lshr_b32 s71, s2, 3\n"
"s_add_u32 s58, s58, s72\n"
"s_and_b32 s73, s2, 4\n"
"s_lshl2_add_u32 s71, s71, s51\n"
"v_min_u32 v121, 2, v112\n"
"s_xnor_b32 s56, s54, s55\n"
"s_mul_i32 s73, s73, 21504\n"
"s_and_b32 s74, s2, 2\n"
"s_mul_i32 s72, s71, 384\n"
"v_lshlrev_b32 v121, 7, v121\n"
"s_and_b32 s56, s56, 1\n"
"s_add_u32 s58, s58, s73\n"
"s_mul_i32 s74, s74, 192\n"
"v_add_u32 v121, s72, v121\n"
"s_bfe_u32 s52, s2, 0x20003\n"
"s_mul_i32 s57, s54, -344064\n"
"s_add_u32 s58, s58, s74\n"
"v_lshlrev_b32 v120, 4, v112\n"
"s_mul_i32 s85, s56, 3\n"
"v_min_u32 v121, .Lend%=-.Lpc%=-4, v121\n"
"s_add_u32 s57, s57, 172032\n"
"s_lshl_b32 s61, s51, 3\n"
"v_lshrrev_b32 v114, 3, v112\n"
"v_xor_b32 v115, v120, v112\n"
"s_sub_u32 s86, s85, 3\n"
"s_mov_b32 m0, 163136\n"
"s_mul_i32 s73, s52, 338\n"
"global_load_lds_dword v121, s[4:5]\n"
"s_and_b32 s74, s51, 1\n"
"s_lshr_b32 s84, s2, 5\n"
"v_and_b32 v115, 0x70, v115\n"
"v_add_u32 v114, s61, v114\n"
"s_mul_i32 s86, s86, s57\n"
"s_and_b32 s73, s73, -4\n"
"s_mul_i32 s74, s74, 88\n"
"v_mul_u32_u24 v123, 0x2aab, v112\n"
"v_add_u32 v124, 64, v112\n"
"s_mov_b32 s22, 1352\n"
"s_mov_b32 s23, 0x20000\n"
"s_add_u32 s84, s84, s52\n"
"v_xor_b32 v116, 64, v115\n"
"v_mul_u32_u24 v117, 0x300, v114\n"
"s_add_u32 s86, s86, s58\n"
"v_max_u32 v118, s85, v114\n"
"s_add_u32 s73, s73, s74\n"
"v_lshlrev_b32 v122, 2, v112\n"
"v_lshrrev_b32 v123, 16, v123\n"
"v_mul_u32_u24 v124, 0x2aab, v124\n"
"s_and_b32 s65, s51, 1\n"
"s_mul_i32 s84, s84, 2\n"
"v_add3_u32 v119, v117, s86, v116\n"
"v_lshlrev_b32 v126, 1, v123\n"
"v_lshrrev_b32 v124, 16, v124\n"
"s_lshr_b32 s66, s51, 1\n"
"s_mul_i32 s70, s52, 96\n"
"s_mul_i32 s85, s65, 384\n"
"s_lshr_b32 s75, 0x9249200, s84\n"
"v_and_b32 v126, 24, v126\n"
"v_lshlrev_b32 v127, 1, v124\n"
"s_add_u32 s85, s85, s70\n"
"s_mul_i32 s86, s66, 36864\n"
"s_movk_i32 s69, 1152\n"
"s_and_b32 s75, s75, 0x180\n"
"v_and_or_b32 v126, v123, 3, v126\n"
"v_and_b32 v127, 24, v127\n"
"v_add_u32 v125, 128, v112\n"
"s_add_u32 s85, s85, s86\n"
"v_mad_u32_u24 v126, v126, s69, v120\n"
"v_mul_u32_u24 v27, 96, v123\n"
"v_and_or_b32 v127, v124, 3, v127\n"
"v_lshrrev_b32 v128, 2, v124\n"
"v_mul_u32_u24 v125, 0x2aab, v125\n"
"s_add_u32 s85, s85, 384\n"
"s_mul_i32 s65, s65, 3072\n"
"s_mul_i32 s71, s66, 6752\n"
"s_mov_b32 s14, 221184\n"
"s_mov_b32 s15, 0x20000\n"
"v_sub_u32 v27, v126, v27\n"
"v_and_or_b32 v127, v128, 4, v127\n"
"v_lshrrev_b32 v125, 16, v125\n"
"s_add_u32 s71, s71, s65\n"
"v_mad_u32_u24 v127, v127, s69, v120\n"
"v_mul_u32_u24 v28, 96, v124\n"
"v_lshlrev_b32 v129, 1, v125\n"
"v_sub_u32 v28, v127, v28\n"
"v_and_b32 v129, 24, v129\n"
"v_add_u32 v28, 1024, v28\n"
"v_and_or_b32 v129, v125, 3, v129\n"
"v_mad_u32_u24 v129, v129, s69, v120\n"
"v_mul_u32_u24 v29, 96, v125\n"
"s_sub_u32 s64, 111, s59\n"
"v_sub_u32 v29, v129, v29\n"
"s_min_u32 s64, s64, 32\n"
"v_add_u32 v29, 6656, v29\n"
"s_mul_i32 s64, s64, 768\n"
"v_add_u32 v118, -3, v118\n"
"s_add_u32 s64, s64, s58\n"
"s_mul_i32 s50, s51, 6752\n"
"v_mul_i32_i24 v118, s57, v118\n"
"s_mov_b32 s10, 19267584\n"
"s_mov_b32 s11, 0x20000\n"
"s_add_u32 s50, s50, 27008\n"
"v_add3_u32 v118, v118, s64, v115\n"
"s_lshl_b32 s72, s56, 3\n"
"s_sub_u32 s72, s72, 2\n"
"s_mul_i32 s72, s72, s57\n"
"s_mul_i32 s34, s51, 12288\n"
"s_add_u32 s72, s72, s58\n"
"s_add_u32 s34, s34, 81024\n"
"v_add3_u32 v130, v117, s72, v115\n"
"s_lshr_b32 s76, 0x12492480, s84\n"
"s_waitcnt lgkmcnt(0)\n"
"s_add_u32 m0, s74, 162768\n"
"s_lshr_b32 s77, 0x4924900, s84\n"
"buffer_load_dword v122, s[20:23], s73 offen lds\n"
"s_mul_i32 s73, s75, 576\n"
"s_add_u32 s73, s73, s85\n"
"s_add_u32 m0, s71, 0\n"
"s_add_u32 s74, s72, s57\n"
"buffer_load_dwordx4 v27, s[12:15], s73 offen lds\n"
"s_and_b32 s60, s2, 7\n"
"s_lshr_b32 s84, s2, 5\n"
"v_add3_u32 v131, v117, s74, v116\n"
"s_and_b32 s76, s76, 0x180\n"
"s_lshl3_add_u32 s60, s60, s84\n"
"s_mul_i32 s84, s76, 576\n"
"s_add_u32 m0, s71, 1024\n"
"s_add_u32 s84, s84, s85\n"
"s_and_b32 s77, s77, 0x180\n"
"s_mul_i32 s86, s77, 576\n"
"s_add_u32 s86, s86, s85\n"
"s_mul_i32 s72, s54, 172032\n"
"buffer_load_dwordx4 v28, s[12:15], s73 offen lds\n"
"s_sub_u32 s36, s58, s72\n"
"s_add_u32 s32, s36, s75\n"
"v_add3_u32 v190, v117, s72, v115\n"
"s_sub_u32 s74, 172032, s72\n"
"v_add3_u32 v191, v117, s74, v116\n"
"s_add_u32 s0, s36, s76\n"
"s_add_u32 s1, s36, s77\n"
"s_mul_i32 s35, s51, 6144\n"
"s_lshl_b32 s33, s57, 1\n"
"s_add_u32 m0, s71, 2048\n"
"s_add_u32 s35, s35, 130176\n"
"v_and_b32 v134, 3, v112\n"
"v_lshrrev_b32 v137, 1, v112\n"
"v_and_or_b32 v137, v137, 4, v134\n"
"v_bfe_u32 v136, v112, 2, 1\n"
"buffer_load_dwordx4 v29, s[12:15], s73 offen lds\n"
"v_lshrrev_b32 v140, 1, v137\n"
"v_lshrrev_b32 v132, 4, v112\n"
"v_lshl_add_u32 v85, v136, 2, v140\n"
"v_xor_b32 v85, v85, v132\n"
"v_lshlrev_b32 v85, 4, v85\n"
"v_lshl_add_u32 v85, v137, 7, v85\n"
"v_mul_u32_u24 v25, 0xc00, v136\n"
"s_add_u32 m0, s50, 0\n"
"v_add_u32 v25, v25, v85\n"
"v_add_u32 v188, s34, v25\n"
"v_xad_u32 v189, v25, 64, s34\n"
"v_xor_b32 v71, 64, v25\n"
"v_add_u32 v70, s35, v25\n"
"buffer_load_dwordx4 v118, s[8:11], s75 offen lds\n"
"v_add_u32 v71, s35, v71\n"
"s_mul_i32 s3, s65, 12\n"
"s_add_u32 s70, s70, s3\n"
"s_mul_i32 s81, s77, 576\n"
"s_add_u32 s81, s81, s70\n"
"s_sub_u32 s3, s76, s75\n"
"s_mul_i32 s3, s3, s66\n"
"s_add_u32 m0, s50, 3072\n"
"s_add_u32 s3, s3, s75\n"
"s_mul_i32 s3, s3, 576\n"
"s_add_u32 s3, s3, s70\n"
"v_lshrrev_b32 v84, 2, v112\n"
"v_mul_u32_u24 v84, 96, v84\n"
"buffer_load_dwordx4 v119, s[8:11], s75 offen lds\n"
"v_lshl_add_u32 v23, v134, 3, v84\n"
"s_mov_b32 s42, 0x80000\n"
"v_add_u32 v72, 54016, v23\n"
"v_min_u32 v96, 0xdc, v31\n"
"v_mul_u32_u24 v97, 0xf1, v96\n"
"s_mul_i32 s85, s54, -52\n"
"v_lshrrev_b32 v97, 12, v97\n"
"s_add_u32 m0, s34, 6144\n"
"s_sub_u32 s85, s85, 8\n"
"v_lshlrev_b32 v30, 3, v96\n"
"v_mul_i32_i24 v97, s85, v97\n"
"v_lshl_add_u32 v96, v96, 1, v97\n"
"v_xor_b32 v69, 64, v25\n"
"buffer_load_dwordx4 v130, s[8:11], s75 offen lds\n"
"v_add_u32 v30, 160896, v30\n"
"v_add_u32 v68, s50, v25\n"
"v_add_u32 v69, s50, v69\n"
"v_add_u32 v142, s61, v137\n"
"v_bfe_u32 v86, v142, 1, 1\n"
"v_mul_u32_u24 v87, 43, v142\n"
"s_movk_i32 s68, 6752\n"
"s_add_u32 m0, s34, 9216\n"
"v_and_b32 v133, 48, v112\n"
"v_xor_b32 v86, v86, v132\n"
"v_lshrrev_b32 v87, 9, v87\n"
"s_movk_i32 s67, 192\n"
"v_lshlrev_b32 v86, 4, v86\n"
"buffer_load_dwordx4 v131, s[8:11], s75 offen lds\n"
"v_mul_u32_u24 v87, 80832, v87\n"
"v_mad_u32_u24 v21, v142, s68, v133\n"
"v_mad_u32_u24 v141, v142, s67, v86\n"
"v_sub_u32 v21, v21, v87\n"
"v_and_b32 v88, 1, v112\n"
"s_mul_i32 s48, s56, 13504\n"
"v_add_u32 v21, 6144, v21\n"
"s_add_u32 m0, s71, 13504\n"
"v_add_u32 v187, 74272, v141\n"
"v_add_u32 v198, 60768, v141\n"
"v_mad_u32_u24 v88, v88, s68, v133\n"
"s_mov_b32 s6, 0xf0f0f0f0\n"
"s_mov_b32 s7, 0xf0f0f0f0\n"
"buffer_load_dwordx4 v27, s[12:15], s84 offen lds\n"
"s_mov_b64 exec, s[6:7]\n"
"v_add_u32 v21, s48, v141\n"
"v_mov_b32 v187, v141\n"
"v_add_u32 v198, 60544, v88\n"
"s_mov_b64 exec, -1\n"
"s_lshl_b32 s85, s66, 10\n"
"v_add_u32 v192, 99456, v120\n"
"v_bfe_u32 v138, v112, 4, 1\n"
"v_lshrrev_b32 v139, 3, v112\n"
"s_add_u32 m0, s71, 14528\n"
"v_and_b32 v139, 4, v139\n"
"v_sub_u32 v94, v136, v138\n"
"s_add_u32 s62, s59, s61\n"
"s_mul_i32 s49, s56, 272\n"
"v_mul_i32_i24 v94, 0x88, v94\n"
"buffer_load_dwordx4 v28, s[12:15], s84 offen lds\n"
"v_sub_u32 v95, v137, v139\n"
"s_sub_u32 s63, 108, s62\n"
"v_lshl_add_u32 v94, v95, 3, v94\n"
"s_sub_u32 s62, 3, s62\n"
"v_add_u32 v150, 1, v139\n"
"v_add_u32 v151, 2, v139\n"
"v_add_u32 v152, 3, v139\n"
"s_add_u32 m0, s71, 15552\n"
"v_max_i32 v149, s62, v139\n"
"v_max_i32 v150, s62, v150\n"
"v_max_i32 v151, s62, v151\n"
"v_max_i32 v152, s62, v152\n"
"v_mov_b32 v153, 0x7f\n"
"buffer_load_dwordx4 v29, s[12:15], s84 offen lds\n"
"s_add_u32 s84, s49, 161064\n"
"v_add_u32 v20, s84, v94\n"
"s_lshl_b32 s84, s51, 10\n"
"v_min_i32 v149, s63, v149\n"
"v_min_i32 v150, s63, v150\n"
"v_min_i32 v151, s63, v151\n"
"v_min_i32 v152, s63, v152\n"
"s_add_u32 m0, s50, 1024\n"
"v_add_u32 v202, s84, v192\n"
"v_lshlrev_b32 v149, v149, v153\n"
"v_lshlrev_b32 v150, v150, v153\n"
"v_lshlrev_b32 v151, v151, v153\n"
"v_lshlrev_b32 v152, v152, v153\n"
"buffer_load_dwordx4 v118, s[8:11], s76 offen lds\n"
"s_sub_u32 s74, 3, s61\n"
"s_mov_b32 s84, 0xc0000007\n"
"s_cmp_eq_u32 s53, 0\n"
"s_cselect_b32 s84, 0xe0000000, s84\n"
"s_cmp_eq_u32 s53, 3\n"
"s_cselect_b32 s84, 7, s84\n"
"v_lshrrev_b32 v149, v137, v149\n"
"s_add_u32 m0, s50, 4096\n"
"v_lshrrev_b32 v150, v137, v150\n"
"v_lshrrev_b32 v151, v137, v151\n"
"v_lshrrev_b32 v152, v137, v152\n"
"s_max_i32 s74, s74, s62\n"
"s_mov_b32 s72, 0xffff\n"
"buffer_load_dwordx4 v119, s[8:11], s76 offen lds\n"
"v_bfe_i32 v98, v149, 0, 1\n"
"v_bfe_i32 v99, v150, 0, 1\n"
"v_bfe_i32 v100, v151, 0, 1\n"
"v_bfe_i32 v101, v152, 0, 1\n"
"v_bfe_i32 v102, v149, 8, 1\n"
"v_bfe_i32 v103, v150, 8, 1\n"
"v_bfe_i32 v56, v151, 8, 1\n"
"v_bfe_i32 v57, v152, 8, 1\n"
"v_max_i32 v58, s74, v137\n"
"v_bfi_b32 v0, s72, v98, v99\n"
"v_bfi_b32 v1, s72, v100, v101\n"
"v_bfi_b32 v2, s72, v102, v103\n"
"v_bfi_b32 v3, s72, v56, v57\n"
"s_mul_i32 s74, s51, 1536\n"
"v_and_b32 v250, 15, v112\n"
"s_lshl2_add_u32 s72, s60, s51\n"
"v_mul_u32_u24 v250, 0x180, v250\n"
"v_and_b32 v251, 7, v112\n"
"s_mul_i32 s37, s72, 6144\n"
"v_add_u32 v250, 99456, v250\n"
"v_lshrrev_b32 v253, 5, v112\n"
"s_lshl_b32 s72, s51, 6\n"
"v_lshlrev_b32 v254, 3, v134\n"
"v_lshl_add_u32 v255, v23, 2, s72\n"
"v_sub_u32 v255, v255, v254\n"
"v_sub_u32 v255, v255, v254\n"
"v_add_u32 v236, 130176, v255\n"
"v_sub_u32 v84, v236, v254\n"
"s_lshl_b32 s73, s48, 2\n"
"v_mad_u32_u24 v186, v136, s68, v141\n"
"s_add_u32 s73, s73, 6752\n"
"v_add_u32 v22, s73, v186\n"
"s_sub_u32 s73, 28, s61\n"
"s_min_i32 s73, s73, s63\n"
"v_min_i32 v58, s73, v58\n"
"v_lshlrev_b32 v58, v58, -1\n"
"v_lshrrev_b32 v58, v139, v58\n"
"v_not_b32 v58, v58\n"
"v_and_b32 v59, 5, v58\n"
"v_sub_u32 v61, v136, v138\n"
"v_and_b32 v60, 10, v58\n"
"v_lshlrev_b32 v59, 1, v59\n"
"v_ashrrev_i32 v149, 31, v61\n"
"v_add_u32 v150, -1, v61\n"
"v_mov_b32 v160, 0x40de\n"
"v_mov_b32 v161, 0\n"
"v_lshlrev_b32 v63, 4, v134\n"
"v_mov_b64 v[156:157], 0\n"
"s_mov_b32 s6, 0x00f0000f\n"
"s_mov_b32 s7, 0xf0000f00\n"
"v_lshl_or_b32 v59, v60, 4, v59\n"
"v_not_b32 v149, v149\n"
"v_ashrrev_i32 v150, 31, v150\n"
"v_or_b32 v62, s56, v138\n"
"s_mov_b64 exec, s[6:7]\n"
"v_lshlrev_b64 v[156:157], v63, v[160:161]\n"
"s_mov_b64 exec, -1\n"
"v_or_b32 v59, 0x44, v59\n"
"v_and_b32 v12, v149, v0\n"
"v_and_b32 v16, v150, v0\n"
"v_and_b32 v13, v149, v1\n"
"v_and_b32 v17, v150, v1\n"
"v_and_b32 v14, v149, v2\n"
"v_and_b32 v18, v150, v2\n"
"v_and_b32 v15, v149, v3\n"
"v_and_b32 v19, v150, v3\n"
"v_sub_u32 v62, v136, v62\n"
"v_mov_b32 v160, 0xffff\n"
"s_mov_b64 exec, 0\n"
"s_cmp_eq_u32 s51, 0\n"
"s_cbranch_scc1 .Lic0%=\n"
"s_cmp_eq_u32 s51, 1\n"
"s_cbranch_scc1 .Lic1%=\n"
"s_cmp_eq_u32 s51, 2\n"
"s_cbranch_scc1 .Lic2%=\n"
"s_branch .Lic3%=\n"
".Lib0%=:\n"
"s_mov_b64 exec, -1\n"
"v_mov_b64 v[176:177], 0\n"
"v_lshl_or_b32 v66, v59, 8, v59\n"
"v_ashrrev_i32 v149, 31, v62\n"
"v_add_u32 v150, -1, v62\n"
"s_mov_b64 exec, s[6:7]\n"
"v_lshlrev_b64 v[176:177], v63, v[160:161]\n"
"s_mov_b64 exec, -1\n"
"s_add_u32 m0, s34, 7168\n"
"s_mov_b32 s30, 23724032\n"
"buffer_load_dwordx4 v130, s[8:11], s76 offen lds\n"
"s_mov_b32 s31, 0x20000\n"
"v_and_b32 v146, 8, v112\n"
"v_lshl_add_u32 v146, v136, 1, v146\n"
"v_add3_u32 v146, v146, s61, 1\n"
"v_lshrrev_b32 v145, 5, v112\n"
"v_max_u32 v144, 3, v142\n"
"v_mul_u32_u24 v145, s68, v145\n"
"v_bfe_u32 v92, v146, 1, 1\n"
"v_add_u32 v144, -3, v144\n"
"v_add_u32 v145, -768, v145\n"
"v_xor_b32 v92, v92, v134\n"
"v_mov_b32 v143, s48\n"
"v_bfe_u32 v90, v144, 1, 1\n"
"s_add_u32 m0, s34, 10240\n"
"v_lshl_add_u32 v92, v92, 4, v145\n"
"buffer_load_dwordx4 v131, s[8:11], s76 offen lds\n"
"v_mad_u32_u24 v143, v136, s68, v143\n"
"v_xor_b32 v90, v90, v132\n"
"v_cmp_eq_u32 s[46:47], 35, v146\n"
"v_mad_u32_u24 v184, v146, s67, v92\n"
"v_lshl_add_u32 v90, v90, 4, v143\n"
"v_add_u32 v146, 1, v146\n"
"v_mad_u32_u24 v178, v144, s67, v90\n"
"v_bfe_u32 v93, v146, 1, 1\n"
"v_add_u32 v144, 5, v142\n"
"v_xor_b32 v93, v93, v134\n"
"v_min_u32 v144, 32, v144\n"
"s_add_u32 m0, s71, 60768\n"
"v_mul_u32_u24 v147, 13504, v138\n"
"buffer_load_dwordx4 v27, s[12:15], s86 offen lds\n"
"s_waitcnt vmcnt(12)\n"
"s_barrier\n"
"ds_read_b64_tr_b16 a[0:1], v23\n"
"ds_read_b64_tr_b16 a[2:3], v23 offset:1536\n"
"ds_read_b64_tr_b16 a[4:5], v23 offset:32\n"
"ds_read_b64_tr_b16 a[6:7], v23 offset:1568\n"
"ds_read_b64_tr_b16 a[8:9], v23 offset:64\n"
"ds_read_b64_tr_b16 a[10:11], v23 offset:1600\n"
"ds_read_b64_tr_b16 a[72:73], v23 offset:3072\n"
"s_add_u32 m0, s71, 61792\n"
"ds_read_b64_tr_b16 a[74:75], v23 offset:4608\n"
"buffer_load_dwordx4 v28, s[12:15], s86 offen lds\n"
"ds_read_b64_tr_b16 a[76:77], v23 offset:3104\n"
"v_lshl_add_u32 v93, v93, 4, v145\n"
"ds_read_b64_tr_b16 a[78:79], v23 offset:4640\n"
"ds_read_b64_tr_b16 a[80:81], v23 offset:3136\n"
"ds_read_b64_tr_b16 a[82:83], v23 offset:4672\n"
"v_bfe_u32 v91, v144, 1, 1\n"
"ds_read_b64_tr_b16 a[12:13], v23 offset:6752\n"
"ds_read_b64_tr_b16 a[14:15], v23 offset:8288\n"
"ds_read_b64_tr_b16 a[16:17], v23 offset:6784\n"
"s_add_u32 m0, s71, 62816\n"
"ds_read_b64_tr_b16 a[18:19], v23 offset:8320\n"
"buffer_load_dwordx4 v29, s[12:15], s86 offen lds\n"
"s_waitcnt vmcnt(12)\n"
"ds_read_b128 a[144:147], v68\n"
"s_mul_i32 s71, s54, 312\n"
"s_and_b32 s86, s52, 1\n"
"ds_read_b128 a[148:151], v69\n"
"s_lshl1_add_u32 s71, s86, s71\n"
"s_add_u32 s71, s71, 162760\n"
"v_add_u32 v24, s71, v96\n"
"ds_read_b64_tr_b16 a[20:21], v23 offset:6816\n"
"ds_read_b64_tr_b16 a[22:23], v23 offset:8352\n"
"ds_read_b64_tr_b16 a[84:85], v23 offset:9824\n"
"s_add_u32 m0, s50, 2048\n"
"ds_read_b64_tr_b16 a[86:87], v23 offset:11360\n"
"buffer_load_dwordx4 v118, s[8:11], s77 offen lds\n"
"ds_read_b64_tr_b16 a[88:89], v23 offset:9856\n"
"s_and_b32 s86, s51, 1\n"
"ds_read_b64_tr_b16 a[90:91], v23 offset:11392\n"
"s_lshl3_add_u32 s85, s86, s85\n"
"ds_read_b64_tr_b16 a[92:93], v23 offset:9888\n"
"s_add_u32 s85, s85, 4096\n"
"ds_read_b64_tr_b16 a[94:95], v23 offset:11424\n"
"v_add_u32 v203, s85, v192\n"
"ds_read_u16 v240, v24 offset:6\n"
"ds_read_u16 v241, v24 offset:4\n"
"ds_read_u16 v242, v24 offset:2\n"
"v_add_u32 v147, s48, v147\n"
"v_mul_i32_i24 v148, -67520, v138\n"
"v_mad_u32_u24 v185, v146, s67, v93\n"
"v_bfe_u32 v135, v112, 2, 2\n"
"v_xor_b32 v91, v91, v132\n"
"v_add_u32 v148, 67520, v148\n"
"v_add_u32 v183, v185, v147\n"
"v_add_u32 v185, v185, v148\n"
"v_lshl_add_u32 v91, v91, 4, v143\n"
"s_mov_b64 exec, s[46:47]\n"
"v_mad_u32_u24 v185, v138, s67, v185\n"
"s_mov_b64 exec, -1\n"
"v_and_b32 v89, 1, v114\n"
"v_add_u32 v89, 29, v89\n"
"v_lshrrev_b32 v86, 2, v112\n"
"v_sub_u32 v87, 11, v112\n"
"v_mul_i32_i24 v89, s57, v89\n"
"v_mad_u32_u24 v179, v144, s67, v91\n"
"v_lshlrev_b32 v193, 3, v112\n"
"v_and_b32 v87, 0x80000000, v87\n"
"v_add3_u32 v199, v89, s64, v116\n"
"v_add_u32 v180, 40512, v178\n"
"v_add_u32 v181, 40512, v179\n"
"v_cmp_eq_u32 s[44:45], 32, v144\n"
"v_add_u32 v182, v184, v147\n"
"v_not_b32 v149, v149\n"
"v_mov_b32 v26, 0xf149f149\n"
"v_and_b32 v4, v149, v0\n"
"v_and_b32 v5, v149, v1\n"
"v_and_b32 v6, v149, v2\n"
"v_and_b32 v7, v149, v3\n"
"v_mov_b64 v[160:161], v[156:157]\n"
"v_ashrrev_i32 v150, 31, v150\n"
"v_and_b32 v8, v150, v0\n"
"v_and_b32 v9, v150, v1\n"
"v_and_b32 v10, v150, v2\n"
"v_and_b32 v11, v150, v3\n"
"v_add_u32 v184, v184, v148\n"
"v_mov_b32 v172, 0x3f803f80\n"
"v_mov_b32 v173, v172\n"
"v_mov_b64 v[174:175], v[172:173]\n"
"v_add_u32 v193, 99456, v193\n"
"v_mov_b64 v[164:165], v[156:157]\n"
"v_mov_b64 v[168:169], v[156:157]\n"
"s_waitcnt vmcnt(11) lgkmcnt(1)\n"
"v_mfma_f32_16x16x32_bf16 v[204:207], a[0:3], a[144:147], 0\n"
"s_add_u32 m0, s50, 5120\n"
"ds_read_b128 a[168:171], v188 offset:6144\n"
"buffer_load_dwordx4 v119, s[8:11], s77 offen lds\n"
"v_mfma_f32_16x16x32_bf16 v[208:211], a[4:7], a[144:147], 0\n"
"s_lshl_b32 s85, s51, 6\n"
"ds_read_b128 a[172:175], v189 offset:6144\n"
"ds_read_u16 v243, v24\n"
"v_mfma_f32_16x16x32_bf16 v[212:215], a[8:11], a[144:147], 0\n"
"s_add_u32 m0, s34, 8192\n"
"v_lshl_or_b32 v240, v241, 16, v240\n"
"v_add_u32 v81, s85, v112\n"
"v_mfma_f32_16x16x32_bf16 v[216:219], a[72:75], a[144:147], 0\n"
"s_lshl_b32 s86, s52, 3\n"
"v_add_u32 v154, s85, v112\n"
"v_add_u32 v81, 0x100, v81\n"
"v_mfma_f32_16x16x32_bf16 v[220:223], a[76:79], a[144:147], 0\n"
"s_lshl2_add_u32 s71, s60, s52\n"
"buffer_load_dwordx4 v130, s[8:11], s77 offen lds\n"
"v_mul_u32_u24 v155, 0xaab, v154\n"
"v_mfma_f32_16x16x32_bf16 v[224:227], a[80:83], a[144:147], 0\n"
"s_mul_i32 s38, s71, 6144\n"
"v_mul_u32_u24 v82, 0xaab, v81\n"
"v_lshrrev_b32 v155, 16, v155\n"
"v_mfma_f32_16x16x32_bf16 v[204:207], a[12:15], a[148:151], v[204:207]\n"
"s_add_u32 m0, s34, 11264\n"
"v_lshrrev_b32 v82, 16, v82\n"
"v_mul_i32_i24 v80, -24, v155\n"
"v_mfma_f32_16x16x32_bf16 v[208:211], a[16:19], a[148:151], v[208:211]\n"
"s_lshl_b32 s71, s51, 2\n"
"v_mul_i32_i24 v83, -24, v82\n"
"v_add_u32 v154, v154, v80\n"
"v_mfma_f32_16x16x32_bf16 v[212:215], a[20:23], a[148:151], v[212:215]\n"
"s_waitcnt lgkmcnt(2)\n"
"buffer_load_dwordx4 v131, s[8:11], s77 offen lds\n"
"v_add_u32 v81, v81, v83\n"
"v_mfma_f32_16x16x32_bf16 v[216:219], a[84:87], a[148:151], v[216:219]\n"
"s_waitcnt lgkmcnt(0)\n"
"v_and_b32 v80, 7, v155\n"
"v_and_b32 v83, 7, v82\n"
"v_mfma_f32_16x16x32_bf16 v[220:223], a[88:91], a[148:151], v[220:223]\n"
"s_waitcnt vmcnt(9)\n"
"v_lshl_or_b32 v241, v243, 16, v242\n"
"v_xor_b32 v154, v154, v80\n"
"v_mfma_f32_16x16x32_bf16 v[224:227], a[92:95], a[148:151], v[224:227]\n"
"s_add_u32 m0, s34, 0\n"
"ds_read_b128 a[152:155], v68 offset:1024\n"
"buffer_load_dwordx4 v190, s[8:11], s32 offen lds\n"
"v_mfma_f32_16x16x32_bf16 v[104:107], a[72:75], a[168:171], 0\n"
"s_add_u32 s38, s38, s74\n"
"ds_read_b128 a[156:159], v69 offset:1024\n"
"ds_write_b64 v30, v[240:241]\n"
"v_mfma_f32_16x16x32_bf16 v[108:111], a[76:79], a[168:171], 0\n"
"s_lshl1_add_u32 s74, s51, 16\n"
"v_xor_b32 v81, v81, v83\n"
"v_lshrrev_b32 v80, 1, v155\n"
"v_mfma_f32_16x16x32_bf16 v[228:231], a[80:83], a[168:171], 0\n"
"s_add_u32 m0, s34, 3072\n"
"v_lshrrev_b32 v83, 1, v82\n"
"v_and_b32 v80, 4, v80\n"
"v_mfma_f32_16x16x32_bf16 v[104:107], a[84:87], a[172:175], v[104:107]\n"
"s_add_u32 s38, s38, 31616\n"
"buffer_load_dwordx4 v191, s[8:11], s32 offen lds\n"
"v_and_b32 v83, 4, v83\n"
"v_mfma_f32_16x16x32_bf16 v[108:111], a[88:91], a[172:175], v[108:111]\n"
"s_add_u32 s32, s32, s33\n"
"v_and_or_b32 v80, v155, 3, v80\n"
"v_and_or_b32 v83, v82, 3, v83\n"
"v_mfma_f32_16x16x32_bf16 v[228:231], a[92:95], a[172:175], v[228:231]\n"
"s_waitcnt vmcnt(9)\n"
"v_add_u32 v80, s86, v80\n"
"s_add_u32 m0, s34, 1024\n"
"v_add_u32 v83, s86, v83\n"
"ds_read_b128 a[176:179], v188 offset:7168\n"
"ds_read_b128 a[180:183], v189 offset:7168\n"
"buffer_load_dwordx4 v190, s[8:11], s0 offen lds\n"
"v_lshrrev_b32 v244, v80, s84\n"
"v_lshrrev_b32 v245, 4, v155\n"
"v_bfe_u32 v246, v155, 2, 1\n"
"v_lshrrev_b32 v247, v83, s84\n"
"v_lshrrev_b32 v248, 4, v82\n"
"v_bfe_u32 v249, v82, 2, 1\n"
"v_or_b32 v244, v244, v245\n"
"v_xor_b32 v246, s54, v246\n"
"v_or_b32 v247, v247, v248\n"
"s_add_u32 m0, s34, 4096\n"
"v_xor_b32 v249, s54, v249\n"
"v_lshlrev_b32 v244, 31, v244\n"
"v_mul_u32_u24 v246, 172032, v246\n"
"v_mul_u32_u24 v80, 0x300, v80\n"
"v_lshlrev_b32 v247, 31, v247\n"
"buffer_load_dwordx4 v191, s[8:11], s0 offen lds\n"
"s_add_u32 s0, s0, s33\n"
"v_mul_u32_u24 v249, 172032, v249\n"
"v_mul_u32_u24 v83, 0x300, v83\n"
"v_add_u32 v252, s71, v132\n"
"v_add3_u32 v244, v244, v246, v80\n"
"v_add3_u32 v247, v247, v249, v83\n"
"v_xor_b32 v252, v252, v251\n"
"s_add_u32 m0, s34, 2048\n"
"v_lshl_add_u32 v194, v154, 4, v244\n"
"v_lshl_add_u32 v195, v81, 4, v247\n"
"v_mfma_f32_16x16x32_bf16 v[244:247], a[0:3], a[168:171], 0\n"
"s_add_u32 s39, s38, 22119552\n"
"v_lshl_add_u32 v196, v252, 4, v250\n"
"v_add_u32 v253, s74, v253\n"
"v_mfma_f32_16x16x32_bf16 v[244:247], a[12:15], a[172:175], v[244:247]\n"
"s_and_b32 s85, s52, 1\n"
"buffer_load_dwordx4 v190, s[8:11], s1 offen lds\n"
"v_lshl_add_u32 v250, v138, 3, v250\n"
"v_xor_b32 v253, v253, v251\n"
"s_mov_b64 exec, 0\n"
"s_cmp_eq_u32 s51, 0\n"
"s_cbranch_scc1 .Lic4%=\n"
"s_cmp_eq_u32 s51, 1\n"
"s_cbranch_scc1 .Lic5%=\n"
"s_cmp_eq_u32 s51, 2\n"
"s_cbranch_scc1 .Lic6%=\n"
"s_branch .Lic7%=\n"
".Lib1%=:\n"
"s_mov_b64 exec, -1\n"
"s_add_u32 m0, s34, 5120\n"
"s_barrier\n"
"buffer_load_dwordx4 v191, s[8:11], s1 offen lds\n"
"s_add_u32 s1, s1, s33\n"
"ds_read_b64_tr_b16 a[24:25], v23 offset:13504\n"
"ds_read_b64_tr_b16 a[26:27], v23 offset:15040\n"
"ds_read_b64_tr_b16 a[28:29], v23 offset:13536\n"
"ds_read_b64_tr_b16 a[30:31], v23 offset:15072\n"
"s_add_u32 m0, s35, 0\n"
"ds_read_b64_tr_b16 a[32:33], v23 offset:13568\n"
"ds_read_b64_tr_b16 a[34:35], v23 offset:15104\n"
"ds_read_b64_tr_b16 a[96:97], v23 offset:16576\n"
"ds_read_b64_tr_b16 a[98:99], v23 offset:18112\n"
"s_waitcnt vmcnt(10)\n"
"buffer_load_dwordx4 v190, s[8:11], s32 offen lds\n"
"ds_read_b64_tr_b16 a[100:101], v23 offset:16608\n"
"ds_read_b64_tr_b16 a[102:103], v23 offset:18144\n"
"s_barrier\n"
"ds_read_b64_tr_b16 a[104:105], v23 offset:16640\n"
"ds_read_b64_tr_b16 a[106:107], v23 offset:18176\n"
"ds_read_b64_tr_b16 a[36:37], v23 offset:20256\n"
"v_lshl_add_u32 v197, v253, 4, v250\n"
"v_mfma_f32_16x16x32_bf16 v[248:251], a[4:7], a[168:171], 0\n"
"s_add_u32 m0, s35, 3072\n"
"ds_read_b64_tr_b16 a[38:39], v23 offset:21792\n"
"ds_read_b64_tr_b16 a[40:41], v23 offset:20288\n"
"v_mfma_f32_16x16x32_bf16 v[252:255], a[8:11], a[168:171], 0\n"
"s_waitcnt vmcnt(9)\n"
"buffer_load_dwordx4 v191, s[8:11], s32 offen lds\n"
"ds_read_b64_tr_b16 a[42:43], v23 offset:21824\n"
"v_mfma_f32_16x16x32_bf16 v[248:251], a[16:19], a[172:175], v[248:251]\n"
"s_waitcnt lgkmcnt(14)\n"
"ds_read_b128 a[160:163], v68 offset:2048\n"
"ds_read_b128 a[164:167], v69 offset:2048\n"
"v_mfma_f32_16x16x32_bf16 v[204:207], a[24:27], a[152:155], v[204:207]\n"
"s_waitcnt lgkmcnt(14)\n"
"ds_read_b64_tr_b16 a[44:45], v23 offset:20320\n"
"v_add_u32 v68, 130176, v23\n"
"v_mfma_f32_16x16x32_bf16 v[208:211], a[28:31], a[152:155], v[208:211]\n"
"s_waitcnt lgkmcnt(13)\n"
"ds_read_b64_tr_b16 a[46:47], v23 offset:21856\n"
"ds_read_b64_tr_b16 a[108:109], v23 offset:23328\n"
"v_mfma_f32_16x16x32_bf16 v[212:215], a[32:35], a[152:155], v[212:215]\n"
"s_waitcnt lgkmcnt(13)\n"
"ds_read_b64_tr_b16 a[110:111], v23 offset:24864\n"
"ds_read_b64_tr_b16 a[112:113], v23 offset:23360\n"
"v_mfma_f32_16x16x32_bf16 v[216:219], a[96:99], a[152:155], v[216:219]\n"
"s_waitcnt lgkmcnt(13)\n"
"ds_read_b64_tr_b16 a[114:115], v23 offset:24896\n"
"ds_read_b64_tr_b16 a[116:117], v23 offset:23392\n"
"v_mfma_f32_16x16x32_bf16 v[220:223], a[100:103], a[152:155], v[220:223]\n"
"s_waitcnt lgkmcnt(13)\n"
"ds_read_b64_tr_b16 a[118:119], v23 offset:24928\n"
"ds_read_b64_tr_b16 a[48:49], v23 offset:60768\n"
"v_mfma_f32_16x16x32_bf16 v[224:227], a[104:107], a[152:155], v[224:227]\n"
"s_waitcnt lgkmcnt(13)\n"
"ds_read_b64_tr_b16 a[50:51], v23 offset:62304\n"
"ds_read_b64_tr_b16 a[52:53], v23 offset:60800\n"
"v_mfma_f32_16x16x32_bf16 v[204:207], a[36:39], a[156:159], v[204:207]\n"
"s_waitcnt vmcnt(8) lgkmcnt(13)\n"
"ds_read_b64_tr_b16 a[54:55], v23 offset:62336\n"
"ds_read_b64_tr_b16 a[56:57], v23 offset:60832\n"
"v_mfma_f32_16x16x32_bf16 v[208:211], a[40:43], a[156:159], v[208:211]\n"
"s_add_u32 m0, s35, 1024\n"
"ds_read_b128 a[184:187], v188 offset:8192\n"
"buffer_load_dwordx4 v190, s[8:11], s0 offen lds\n"
"v_mfma_f32_16x16x32_bf16 v[252:255], a[20:23], a[172:175], v[252:255]\n"
"s_add_u32 s32, s32, s33\n"
"ds_read_b128 a[188:191], v189 offset:8192\n"
"s_mul_i32 s20, s33, 4\n"
"v_mfma_f32_16x16x32_bf16 v[244:247], a[24:27], a[176:179], v[244:247]\n"
"s_waitcnt lgkmcnt(13)\n"
"ds_read_b64_tr_b16 a[58:59], v23 offset:62368\n"
"ds_read_b64_tr_b16 a[120:121], v72 offset:9824\n"
"v_mfma_f32_16x16x32_bf16 v[212:215], a[44:47], a[156:159], v[212:215]\n"
"s_waitcnt lgkmcnt(13)\n"
"ds_read_b64_tr_b16 a[122:123], v72 offset:11360\n"
"ds_read_b64_tr_b16 a[124:125], v72 offset:9856\n"
"v_mfma_f32_16x16x32_bf16 v[216:219], a[108:111], a[156:159], v[216:219]\n"
"s_waitcnt lgkmcnt(13)\n"
"ds_read_b64_tr_b16 a[126:127], v72 offset:11392\n"
"ds_read_b64_tr_b16 a[128:129], v72 offset:9888\n"
"v_mfma_f32_16x16x32_bf16 v[220:223], a[112:115], a[156:159], v[220:223]\n"
"s_waitcnt lgkmcnt(13)\n"
"ds_read_b64_tr_b16 a[130:131], v72 offset:11424\n"
"ds_read_b64_tr_b16 a[60:61], v72 offset:13504\n"
"v_mfma_f32_16x16x32_bf16 v[224:227], a[116:119], a[156:159], v[224:227]\n"
"s_waitcnt lgkmcnt(13)\n"
"ds_read_b64_tr_b16 a[62:63], v72 offset:15040\n"
"ds_read_b64_tr_b16 a[64:65], v72 offset:13536\n"
"v_mfma_f32_16x16x32_bf16 v[204:207], a[48:51], a[160:163], v[204:207]\n"
"s_waitcnt lgkmcnt(13)\n"
"ds_read_b64_tr_b16 a[66:67], v72 offset:15072\n"
"ds_read_b64_tr_b16 a[68:69], v72 offset:13568\n"
"v_mfma_f32_16x16x32_bf16 v[208:211], a[52:55], a[160:163], v[208:211]\n"
"s_add_u32 m0, s35, 4096\n"
"ds_read_b64_tr_b16 a[70:71], v72 offset:15104\n"
"buffer_load_dwordx4 v191, s[8:11], s0 offen lds\n"
"v_mfma_f32_16x16x32_bf16 v[248:251], a[28:31], a[176:179], v[248:251]\n"
"s_add_u32 s0, s0, s33\n"
"ds_read_b64_tr_b16 a[132:133], v72 offset:16576\n"
"ds_read_b64_tr_b16 a[134:135], v72 offset:18112\n"
"v_mfma_f32_16x16x32_bf16 v[252:255], a[32:35], a[176:179], v[252:255]\n"
"s_waitcnt lgkmcnt(14)\n"
"ds_read_b64_tr_b16 a[136:137], v72 offset:16608\n"
"ds_read_b64_tr_b16 a[138:139], v72 offset:18144\n"
"v_mfma_f32_16x16x32_bf16 v[212:215], a[56:59], a[160:163], v[212:215]\n"
"s_waitcnt lgkmcnt(14)\n"
"ds_read_b64_tr_b16 a[140:141], v72 offset:16640\n"
"ds_read_b64_tr_b16 a[142:143], v72 offset:18176\n"
"v_mfma_f32_16x16x32_bf16 v[216:219], a[120:123], a[160:163], v[216:219]\n"
"s_waitcnt lgkmcnt(14)\n"
"s_add_u32 m0, s35, 2048\n"
"s_mul_i32 s21, s33, 5\n"
"v_mfma_f32_16x16x32_bf16 v[220:223], a[124:127], a[160:163], v[220:223]\n"
"s_waitcnt lgkmcnt(12)\n"
"buffer_load_dwordx4 v190, s[8:11], s1 offen lds\n"
"v_mfma_f32_16x16x32_bf16 v[224:227], a[128:131], a[160:163], v[224:227]\n"
"s_waitcnt lgkmcnt(10)\n"
"s_lshl2_add_u32 s85, s85, s61\n"
"v_add_u32 v85, s85, v134\n"
"v_mfma_f32_16x16x32_bf16 v[204:207], a[60:63], a[164:167], v[204:207]\n"
"s_waitcnt lgkmcnt(8)\n"
"v_mul_u32_u24 v85, 0x300, v85\n"
"v_lshl_add_u32 v85, v86, 7, v85\n"
"v_mfma_f32_16x16x32_bf16 v[208:211], a[64:67], a[164:167], v[208:211]\n"
"s_waitcnt lgkmcnt(6)\n"
"v_or_b32 v76, v87, v85\n"
"v_mfma_f32_16x16x32_bf16 v[212:215], a[68:71], a[164:167], v[212:215]\n"
"s_waitcnt lgkmcnt(4)\n"
"v_cvt_pk_bf16_f32 v204, v204, v205\n"
"v_cvt_pk_bf16_f32 v205, v206, v207\n"
"v_mfma_f32_16x16x32_bf16 v[216:219], a[132:135], a[164:167], v[216:219]\n"
"s_waitcnt lgkmcnt(2)\n"
"v_cvt_pk_bf16_f32 v206, v208, v209\n"
"v_cvt_pk_bf16_f32 v207, v210, v211\n"
"v_mfma_f32_16x16x32_bf16 v[220:223], a[136:139], a[164:167], v[220:223]\n"
"s_waitcnt lgkmcnt(0)\n"
"v_cvt_pk_bf16_f32 v212, v212, v213\n"
"v_cvt_pk_bf16_f32 v213, v214, v215\n"
"v_mfma_f32_16x16x32_bf16 v[224:227], a[140:143], a[164:167], v[224:227]\n"
"s_add_u32 m0, s35, 5120\n"
"v_cvt_pk_bf16_f32 v216, v216, v217\n"
"v_cvt_pk_bf16_f32 v217, v218, v219\n"
"v_mfma_f32_16x16x32_bf16 v[104:107], a[96:99], a[176:179], v[104:107]\n"
"s_barrier\n"
"v_mfma_f32_16x16x32_bf16 v[108:111], a[100:103], a[176:179], v[108:111]\n"
"s_mul_i32 s73, s52, 1536\n"
"buffer_load_dwordx4 v191, s[8:11], s1 offen lds\n"
"ds_write_b128 v21, v[204:207]\n"
"v_mfma_f32_16x16x32_bf16 v[228:231], a[104:107], a[176:179], v[228:231]\n"
"s_add_u32 s1, s1, s33\n"
"v_cvt_pk_bf16_f32 v218, v220, v221\n"
"v_cvt_pk_bf16_f32 v219, v222, v223\n"
"v_mfma_f32_16x16x32_bf16 v[244:247], a[36:39], a[180:183], v[244:247]\n"
"s_add_u32 s37, s37, s73\n"
"v_cvt_pk_bf16_f32 v214, v224, v225\n"
"v_cvt_pk_bf16_f32 v215, v226, v227\n"
"v_mfma_f32_16x16x32_bf16 v[248:251], a[40:43], a[180:183], v[248:251]\n"
"s_lshl_b32 s73, s51, 5\n"
"ds_write_b128 v21, v[212:215] offset:64\n"
"v_mfma_f32_16x16x32_bf16 v[252:255], a[44:47], a[180:183], v[252:255]\n"
"s_sub_u32 s73, 256, s73\n"
"v_add_u32 v235, s73, v84\n"
"v_mfma_f32_16x16x32_bf16 v[104:107], a[108:111], a[180:183], v[104:107]\n"
"s_add_u32 m0, s34, 6144\n"
"s_add_u32 s37, s37, 31616\n"
"buffer_load_dwordx4 v190, s[8:11], s32 offen lds\n"
"v_mfma_f32_16x16x32_bf16 v[108:111], a[112:115], a[180:183], v[108:111]\n"
"s_add_u32 s40, s37, 22119552\n"
"ds_write_b128 v21, v[216:219] offset:128\n"
"v_mfma_f32_16x16x32_bf16 v[228:231], a[116:119], a[180:183], v[228:231]\n"
"s_lshr_b32 s86, s52, 1\n"
"s_mul_i32 s86, s86, 172032\n"
"v_mfma_f32_16x16x32_bf16 v[244:247], a[48:51], a[184:187], v[244:247]\n"
"s_waitcnt vmcnt(11)\n"
"s_add_u32 m0, s34, 9216\n"
"ds_read_b128 v[32:35], v188\n"
"v_mfma_f32_16x16x32_bf16 v[248:251], a[52:55], a[184:187], v[248:251]\n"
"s_add_u32 s86, s86, s36\n"
"buffer_load_dwordx4 v191, s[8:11], s32 offen lds\n"
"ds_read_b128 v[36:39], v189\n"
"v_mfma_f32_16x16x32_bf16 v[252:255], a[56:59], a[184:187], v[252:255]\n"
"s_add_u32 s32, s32, s33\n"
"ds_read_b64 v[80:81], v20 offset:272\n"
"ds_read_b64 v[86:87], v20 offset:336\n"
"v_mfma_f32_16x16x32_bf16 v[104:107], a[120:123], a[184:187], v[104:107]\n"
"s_mul_i32 s78, s33, 6\n"
"ds_read_b64 v[88:89], v20 offset:544\n"
"ds_read_b64 v[94:95], v20 offset:608\n"
"v_mfma_f32_16x16x32_bf16 v[108:111], a[124:127], a[184:187], v[108:111]\n"
"s_add_u32 m0, s34, 7168\n"
"ds_read_b64 v[96:97], v20 offset:816\n"
"buffer_load_dwordx4 v190, s[8:11], s0 offen lds\n"
"v_mfma_f32_16x16x32_bf16 v[228:231], a[128:131], a[184:187], v[228:231]\n"
"s_mov_b32 s18, 73728\n"
"ds_read_b64 v[102:103], v20 offset:880\n"
"v_mfma_f32_16x16x32_bf16 v[244:247], a[60:63], a[188:191], v[244:247]\n"
"s_mul_i32 s79, s51, 3072\n"
"s_mul_i32 s80, s66, 12288\n"
"s_add_u32 s43, s79, 0\n"
"v_mfma_f32_16x16x32_bf16 v[248:251], a[64:67], a[188:191], v[248:251]\n"
"s_mov_b32 s26, 19267584\n"
"s_mov_b32 s27, 0x20000\n"
"v_mfma_f32_16x16x32_bf16 v[252:255], a[68:71], a[188:191], v[252:255]\n"
"s_waitcnt vmcnt(11) lgkmcnt(7)\n"
"ds_read_b128 v[40:43], v188 offset:1024\n"
"ds_read_b128 v[44:47], v189 offset:1024\n"
"v_mfma_f32_16x16x32_bf16 v[104:107], a[132:135], a[188:191], v[104:107]\n"
"s_waitcnt lgkmcnt(7)\n"
"v_cvt_pk_bf16_f32 v244, v244, v245\n"
"v_cvt_pk_bf16_f32 v245, v246, v247\n"
"v_mfma_f32_16x16x32_bf16 v[108:111], a[136:139], a[188:191], v[108:111]\n"
"s_waitcnt lgkmcnt(5)\n"
"v_cvt_pk_bf16_f32 v246, v248, v249\n"
"v_cvt_pk_bf16_f32 v247, v250, v251\n"
"v_mfma_f32_16x16x32_bf16 v[228:231], a[140:143], a[188:191], v[228:231]\n"
"s_waitcnt lgkmcnt(3)\n"
"ds_write_b128 v22, v[244:247]\n"
"v_mfma_f32_16x16x32_bf16 v[204:207], a[0:3], v[32:35], 0\n"
"s_waitcnt lgkmcnt(3)\n"
"v_cvt_pk_bf16_f32 v252, v252, v253\n"
"v_cvt_pk_bf16_f32 v253, v254, v255\n"
"v_mfma_f32_16x16x32_bf16 v[208:211], a[4:7], v[32:35], 0\n"
"s_add_u32 m0, s34, 10240\n"
"v_cvt_pk_bf16_f32 v104, v104, v105\n"
"v_cvt_pk_bf16_f32 v105, v106, v107\n"
"v_mfma_f32_16x16x32_bf16 v[212:215], a[8:11], v[32:35], 0\n"
"s_mov_b32 s19, 0x20000\n"
"buffer_load_dwordx4 v191, s[8:11], s0 offen lds\n"
"v_cvt_pk_bf16_f32 v106, v108, v109\n"
"v_mfma_f32_16x16x32_bf16 v[216:219], a[72:75], v[32:35], 0\n"
"s_waitcnt vmcnt(10)\n"
"v_cvt_pk_bf16_f32 v107, v110, v111\n"
"v_cvt_pk_bf16_f32 v254, v228, v229\n"
"v_mfma_f32_16x16x32_bf16 v[220:223], a[76:79], v[32:35], 0\n"
"s_add_u32 m0, s34, 8192\n"
"ds_read_b128 v[48:51], v188 offset:2048\n"
"ds_read_b128 v[52:55], v189 offset:2048\n"
"v_mfma_f32_16x16x32_bf16 v[224:227], a[80:83], v[32:35], 0\n"
"s_add_u32 s0, s0, s33\n"
"buffer_load_dwordx4 v190, s[8:11], s1 offen lds\n"
"ds_write_b128 v22, v[104:107] offset:128\n"
"v_mfma_f32_16x16x32_bf16 v[204:207], a[12:15], v[36:39], v[204:207]\n"
"s_waitcnt lgkmcnt(5)\n"
"v_cvt_pk_bf16_f32 v255, v230, v231\n"
"v_bfi_b32 v80, v4, v80, v26\n"
"v_mfma_f32_16x16x32_bf16 v[208:211], a[16:19], v[36:39], v[208:211]\n"
"s_add_u32 m0, s34, 11264\n"
"v_bfi_b32 v81, v5, v81, v26\n"
"v_bfi_b32 v86, v6, v86, v26\n"
"v_mfma_f32_16x16x32_bf16 v[212:215], a[20:23], v[36:39], v[212:215]\n"
"s_add_u32 s78, s78, s86\n"
"buffer_load_dwordx4 v191, s[8:11], s1 offen lds\n"
"ds_write_b128 v22, v[252:255] offset:64\n"
"v_mfma_f32_16x16x32_bf16 v[216:219], a[84:87], v[36:39], v[216:219]\n"
"s_add_u32 s1, s1, s33\n"
"v_bfi_b32 v87, v7, v87, v26\n"
"v_bfi_b32 v88, v0, v88, v26\n"
"v_mfma_f32_16x16x32_bf16 v[220:223], a[88:91], v[36:39], v[220:223]\n"
"s_waitcnt vmcnt(10)\n"
"v_bfi_b32 v89, v1, v89, v26\n"
"v_bfi_b32 v94, v2, v94, v26\n"
"v_mfma_f32_16x16x32_bf16 v[224:227], a[92:95], v[36:39], v[224:227]\n"
"s_add_u32 m0, s65, 154752\n"
"ds_read_b128 a[216:219], v70\n"
"buffer_load_dwordx4 v27, s[12:15], s81 offen lds\n"
"v_mfma_f32_16x16x32_bf16 v[204:207], a[24:27], v[40:43], v[204:207]\n"
"s_waitcnt lgkmcnt(6)\n"
"ds_read_b128 a[220:223], v71\n"
"v_bfi_b32 v95, v3, v95, v26\n"
"v_mfma_f32_16x16x32_bf16 v[208:211], a[28:31], v[40:43], v[208:211]\n"
"s_sub_u32 s16, s16, 99456\n"
"s_subb_u32 s17, s17, 0\n"
"v_bfi_b32 v96, v0, v96, v26\n"
"v_mfma_f32_16x16x32_bf16 v[212:215], a[32:35], v[40:43], v[212:215]\n"
"s_add_u32 m0, s65, 155776\n"
"v_bfi_b32 v97, v1, v97, v26\n"
"v_bfi_b32 v102, v2, v102, v26\n"
"v_mfma_f32_16x16x32_bf16 v[216:219], a[96:99], v[40:43], v[216:219]\n"
"s_mov_b32 s18, 173184\n"
"buffer_load_dwordx4 v28, s[12:15], s81 offen lds\n"
"v_bfi_b32 v103, v3, v103, v26\n"
"v_mfma_f32_16x16x32_bf16 v[220:223], a[100:103], v[40:43], v[220:223]\n"
"s_add_u32 s80, s80, s79\n"
"v_mfma_f32_16x16x32_bf16 v[224:227], a[104:107], v[40:43], v[224:227]\n"
"v_mfma_f32_16x16x32_bf16 v[204:207], a[36:39], v[44:47], v[204:207]\n"
"s_waitcnt lgkmcnt(5)\n"
"s_add_u32 m0, s65, 156800\n"
"v_mfma_f32_16x16x32_bf16 v[208:211], a[40:43], v[44:47], v[208:211]\n"
"s_waitcnt lgkmcnt(1)\n"
"buffer_load_dwordx4 v29, s[12:15], s81 offen lds\n"
"v_mfma_f32_16x16x32_bf16 v[212:215], a[44:47], v[44:47], v[212:215]\n"
"v_mfma_f32_16x16x32_bf16 v[216:219], a[108:111], v[44:47], v[216:219]\n"
"v_mfma_f32_16x16x32_bf16 v[220:223], a[112:115], v[44:47], v[220:223]\n"
"v_mfma_f32_16x16x32_bf16 v[224:227], a[116:119], v[44:47], v[224:227]\n"
"v_mfma_f32_16x16x32_bf16 v[204:207], a[48:51], v[48:51], v[204:207]\n"
"v_mfma_f32_16x16x32_bf16 v[208:211], a[52:55], v[48:51], v[208:211]\n"
"v_mfma_f32_16x16x32_bf16 v[212:215], a[56:59], v[48:51], v[212:215]\n"
"v_mfma_f32_16x16x32_bf16 v[216:219], a[120:123], v[48:51], v[216:219]\n"
"v_mfma_f32_16x16x32_bf16 v[220:223], a[124:127], v[48:51], v[220:223]\n"
"v_mfma_f32_16x16x32_bf16 v[224:227], a[128:131], v[48:51], v[224:227]\n"
"v_mfma_f32_16x16x32_bf16 v[204:207], a[60:63], v[52:55], v[204:207]\n"
"v_mfma_f32_16x16x32_bf16 v[208:211], a[64:67], v[52:55], v[208:211]\n"
"v_mfma_f32_16x16x32_bf16 v[212:215], a[68:71], v[52:55], v[212:215]\n"
"v_mfma_f32_16x16x32_bf16 v[216:219], a[132:135], v[52:55], v[216:219]\n"
"v_mfma_f32_16x16x32_bf16 v[220:223], a[136:139], v[52:55], v[220:223]\n"
"v_mfma_f32_16x16x32_bf16 v[224:227], a[140:143], v[52:55], v[224:227]\n"
"v_mfma_f32_16x16x32_bf16 v[244:247], a[0:3], a[216:219], 0\n"
"s_waitcnt lgkmcnt(0)\n"
"s_nop 0\n"
"v_cvt_pk_bf16_f32 v204, v204, v205\n"
"v_mfma_f32_16x16x32_bf16 v[248:251], a[4:7], a[216:219], 0\n"
"v_cvt_pk_bf16_f32 v205, v206, v207\n"
"v_cvt_pk_bf16_f32 v206, v208, v209\n"
"v_mfma_f32_16x16x32_bf16 v[252:255], a[8:11], a[216:219], 0\n"
"v_cvt_pk_bf16_f32 v207, v210, v211\n"
"v_cvt_pk_bf16_f32 v212, v212, v213\n"
"v_mfma_f32_16x16x32_bf16 v[104:107], a[72:75], a[216:219], 0\n"
"ds_write_b128 v186, v[204:207] offset:20256\n"
"v_mfma_f32_16x16x32_bf16 v[108:111], a[76:79], a[216:219], 0\n"
"v_cvt_pk_bf16_f32 v213, v214, v215\n"
"v_cvt_pk_bf16_f32 v216, v216, v217\n"
"v_mfma_f32_16x16x32_bf16 v[228:231], a[80:83], a[216:219], 0\n"
"v_cvt_pk_bf16_f32 v217, v218, v219\n"
"v_cvt_pk_bf16_f32 v218, v220, v221\n"
"v_mfma_f32_16x16x32_bf16 v[244:247], a[12:15], a[220:223], v[244:247]\n"
"v_cvt_pk_bf16_f32 v219, v222, v223\n"
"v_cvt_pk_bf16_f32 v214, v224, v225\n"
"v_mfma_f32_16x16x32_bf16 v[248:251], a[16:19], a[220:223], v[248:251]\n"
"s_waitcnt vmcnt(11)\n"
"ds_read_b128 a[224:227], v70 offset:1024\n"
"ds_read_b128 a[228:231], v71 offset:1024\n"
"v_mfma_f32_16x16x32_bf16 v[252:255], a[20:23], a[220:223], v[252:255]\n"
"ds_write_b128 v186, v[216:219] offset:20384\n"
"v_mfma_f32_16x16x32_bf16 v[104:107], a[84:87], a[220:223], v[104:107]\n"
"v_cvt_pk_bf16_f32 v215, v226, v227\n"
"v_mfma_f32_16x16x32_bf16 v[108:111], a[88:91], a[220:223], v[108:111]\n"
"ds_write_b128 v186, v[212:215] offset:20320\n"
"v_mfma_f32_16x16x32_bf16 v[228:231], a[92:95], a[220:223], v[228:231]\n"
"s_waitcnt vmcnt(9) lgkmcnt(3)\n"
"v_mfma_f32_16x16x32_bf16 v[244:247], a[24:27], a[224:227], v[244:247]\n"
"s_waitcnt lgkmcnt(2)\n"
"ds_read_b128 a[232:235], v70 offset:2048\n"
"ds_read_b128 a[236:239], v71 offset:2048\n"
"v_mfma_f32_16x16x32_bf16 v[248:251], a[28:31], a[224:227], v[248:251]\n"
"v_mfma_f32_16x16x32_bf16 v[252:255], a[32:35], a[224:227], v[252:255]\n"
"v_mfma_f32_16x16x32_bf16 v[104:107], a[96:99], a[224:227], v[104:107]\n"
"v_mfma_f32_16x16x32_bf16 v[108:111], a[100:103], a[224:227], v[108:111]\n"
"v_mfma_f32_16x16x32_bf16 v[228:231], a[104:107], a[224:227], v[228:231]\n"
"v_mfma_f32_16x16x32_bf16 v[244:247], a[36:39], a[228:231], v[244:247]\n"
"v_mfma_f32_16x16x32_bf16 v[248:251], a[40:43], a[228:231], v[248:251]\n"
"s_waitcnt vmcnt(7)\n"
"ds_read_b128 a[240:243], v188 offset:6144\n"
"v_mfma_f32_16x16x32_bf16 v[252:255], a[44:47], a[228:231], v[252:255]\n"
"s_waitcnt lgkmcnt(2)\n"
"ds_read_b128 a[244:247], v189 offset:6144\n"
"v_mfma_f32_16x16x32_bf16 v[104:107], a[108:111], a[228:231], v[104:107]\n"
"s_waitcnt lgkmcnt(2)\n"
"s_add_u32 m0, s35, 0\n"
"v_mfma_f32_16x16x32_bf16 v[108:111], a[112:115], a[228:231], v[108:111]\n"
"buffer_load_dwordx4 v27, s[12:15], s3 offen lds\n"
"v_mfma_f32_16x16x32_bf16 v[228:231], a[116:119], a[228:231], v[228:231]\n"
"v_mfma_f32_16x16x32_bf16 v[244:247], a[48:51], a[232:235], v[244:247]\n"
"s_add_u32 m0, s35, 1024\n"
"v_mfma_f32_16x16x32_bf16 v[248:251], a[52:55], a[232:235], v[248:251]\n"
"buffer_load_dwordx4 v28, s[12:15], s3 offen lds\n"
"v_mfma_f32_16x16x32_bf16 v[252:255], a[56:59], a[232:235], v[252:255]\n"
"v_mfma_f32_16x16x32_bf16 v[104:107], a[120:123], a[232:235], v[104:107]\n"
"s_waitcnt lgkmcnt(1)\n"
"v_mfma_f32_16x16x32_bf16 v[108:111], a[124:127], a[232:235], v[108:111]\n"
"s_waitcnt vmcnt(7)\n"
"s_add_u32 m0, s35, 2048\n"
"v_mfma_f32_16x16x32_bf16 v[228:231], a[128:131], a[232:235], v[228:231]\n"
"buffer_load_dwordx4 v29, s[12:15], s3 offen lds\n"
"ds_read_b128 a[248:251], v188 offset:7168\n"
"v_mfma_f32_16x16x32_bf16 v[244:247], a[60:63], a[236:239], v[244:247]\n"
"ds_read_b128 a[252:255], v189 offset:7168\n"
"v_mfma_f32_16x16x32_bf16 v[248:251], a[64:67], a[236:239], v[248:251]\n"
"v_mfma_f32_16x16x32_bf16 v[252:255], a[68:71], a[236:239], v[252:255]\n"
"s_add_u32 m0, s34, 0\n"
"v_mfma_f32_16x16x32_bf16 v[104:107], a[132:135], a[236:239], v[104:107]\n"
"buffer_load_dwordx4 v190, s[8:11], s32 offen lds\n"
"v_mfma_f32_16x16x32_bf16 v[108:111], a[136:139], a[236:239], v[108:111]\n"
"s_nop 0\n"
"v_cvt_pk_bf16_f32 v244, v244, v245\n"
"v_mfma_f32_16x16x32_bf16 v[228:231], a[140:143], a[236:239], v[228:231]\n"
"v_cvt_pk_bf16_f32 v245, v246, v247\n"
"v_cvt_pk_bf16_f32 v246, v248, v249\n"
"v_mfma_f32_16x16x32_bf16 v[120:123], a[0:3], a[240:243], 0\n"
"s_waitcnt lgkmcnt(2)\n"
"v_cvt_pk_bf16_f32 v247, v250, v251\n"
"v_cvt_pk_bf16_f32 v252, v252, v253\n"
"v_mfma_f32_16x16x32_bf16 v[124:127], a[4:7], a[240:243], 0\n"
"s_waitcnt vmcnt(7)\n"
"ds_write_b128 v186, v[244:247] offset:33760\n"
"v_mfma_f32_16x16x32_bf16 v[128:131], a[8:11], a[240:243], 0\n"
"s_add_u32 m0, s34, 3072\n"
"v_cvt_pk_bf16_f32 v253, v254, v255\n"
"v_cvt_pk_bf16_f32 v104, v104, v105\n"
"v_mfma_f32_16x16x32_bf16 v[132:135], a[72:75], a[240:243], 0\n"
"buffer_load_dwordx4 v191, s[8:11], s32 offen lds\n"
"ds_read_b128 v[56:59], v188 offset:8192\n"
"v_mfma_f32_16x16x32_bf16 v[136:139], a[76:79], a[240:243], 0\n"
"s_add_u32 s32, s32, s33\n"
"ds_read_b128 v[60:63], v189 offset:8192\n"
"v_cvt_pk_bf16_f32 v105, v106, v107\n"
"v_mfma_f32_16x16x32_bf16 v[140:143], a[80:83], a[240:243], 0\n"
"s_add_u32 m0, s34, 1024\n"
"v_cvt_pk_bf16_f32 v106, v108, v109\n"
"v_cvt_pk_bf16_f32 v107, v110, v111\n"
"v_mfma_f32_16x16x32_bf16 v[120:123], a[12:15], a[244:247], v[120:123]\n"
"s_waitcnt lgkmcnt(4)\n"
"buffer_load_dwordx4 v190, s[8:11], s0 offen lds\n"
"ds_write_b128 v186, v[104:107] offset:33888\n"
"v_mfma_f32_16x16x32_bf16 v[124:127], a[16:19], a[244:247], v[124:127]\n"
"v_cvt_pk_bf16_f32 v254, v228, v229\n"
"v_cvt_pk_bf16_f32 v255, v230, v231\n"
"v_mfma_f32_16x16x32_bf16 v[128:131], a[20:23], a[244:247], v[128:131]\n"
"s_add_u32 m0, s34, 4096\n"
"ds_write_b128 v186, v[252:255] offset:33824\n"
"v_mfma_f32_16x16x32_bf16 v[132:135], a[84:87], a[244:247], v[132:135]\n"
"buffer_load_dwordx4 v191, s[8:11], s0 offen lds\n"
"v_mov_b64 v[252:253], 0\n"
"v_mfma_f32_16x16x32_bf16 v[136:139], a[88:91], a[244:247], v[136:139]\n"
"s_add_u32 s0, s0, s33\n"
"v_mov_b64 v[254:255], 0\n"
"v_mfma_f32_16x16x32_bf16 v[140:143], a[92:95], a[244:247], v[140:143]\n"
"s_add_u32 m0, s34, 2048\n"
"ds_read_b64 v[104:105], v20 offset:1088\n"
"ds_read_b64 v[110:111], v20 offset:1152\n"
"v_mfma_f32_16x16x32_bf16 v[120:123], a[24:27], a[248:251], v[120:123]\n"
"s_waitcnt lgkmcnt(7)\n"
"buffer_load_dwordx4 v190, s[8:11], s1 offen lds\n"
"v_subrev_u32 v20, s49, v20\n"
"v_mfma_f32_16x16x32_bf16 v[124:127], a[28:31], a[248:251], v[124:127]\n"
"v_mfma_f32_16x16x32_bf16 v[128:131], a[32:35], a[248:251], v[128:131]\n"
"v_mfma_f32_16x16x32_bf16 v[132:135], a[96:99], a[248:251], v[132:135]\n"
"s_add_u32 m0, s34, 5120\n"
"v_mfma_f32_16x16x32_bf16 v[136:139], a[100:103], a[248:251], v[136:139]\n"
"buffer_load_dwordx4 v191, s[8:11], s1 offen lds\n"
"s_add_u32 s1, s1, s33\n"
"v_mfma_f32_16x16x32_bf16 v[140:143], a[104:107], a[248:251], v[140:143]\n"
"v_mfma_f32_16x16x32_bf16 v[120:123], a[36:39], a[252:255], v[120:123]\n"
"s_waitcnt lgkmcnt(1)\n"
"v_mfma_f32_16x16x32_bf16 v[124:127], a[40:43], a[252:255], v[124:127]\n"
"s_waitcnt lgkmcnt(0)\n"
"v_bfi_b32 v104, v8, v104, v26\n"
"v_bfi_b32 v105, v9, v105, v26\n"
"v_mfma_f32_16x16x32_bf16 v[128:131], a[44:47], a[252:255], v[128:131]\n"
"s_add_u32 m0, s34, 6144\n"
"v_bfi_b32 v110, v10, v110, v26\n"
"v_bfi_b32 v111, v11, v111, v26\n"
"v_mfma_f32_16x16x32_bf16 v[132:135], a[108:111], a[252:255], v[132:135]\n"
"buffer_load_dwordx4 v190, s[8:11], s32 offen lds\n"
"v_mfma_f32_16x16x32_bf16 v[136:139], a[112:115], a[252:255], v[136:139]\n"
"v_mfma_f32_16x16x32_bf16 v[140:143], a[116:119], a[252:255], v[140:143]\n"
"v_mfma_f32_16x16x32_bf16 v[120:123], a[48:51], v[56:59], v[120:123]\n"
"v_mfma_f32_16x16x32_bf16 v[124:127], a[52:55], v[56:59], v[124:127]\n"
"v_mfma_f32_16x16x32_bf16 v[128:131], a[56:59], v[56:59], v[128:131]\n"
"v_mfma_f32_16x16x32_bf16 v[132:135], a[120:123], v[56:59], v[132:135]\n"
"s_add_u32 m0, s34, 9216\n"
"s_nop 0\n"
"buffer_load_dwordx4 v191, s[8:11], s32 offen lds\n"
"v_mfma_f32_16x16x32_bf16 v[136:139], a[124:127], v[56:59], v[136:139]\n"
"s_add_u32 s32, s32, s33\n"
"v_mfma_f32_16x16x32_bf16 v[140:143], a[128:131], v[56:59], v[140:143]\n"
"v_mfma_f32_16x16x32_bf16 v[120:123], a[60:63], v[60:63], v[120:123]\n"
"v_mfma_f32_16x16x32_bf16 v[124:127], a[64:67], v[60:63], v[124:127]\n"
"s_add_u32 m0, s34, 7168\n"
"s_nop 0\n"
"buffer_load_dwordx4 v190, s[8:11], s0 offen lds\n"
"v_mfma_f32_16x16x32_bf16 v[128:131], a[68:71], v[60:63], v[128:131]\n"
"v_mfma_f32_16x16x32_bf16 v[132:135], a[132:135], v[60:63], v[132:135]\n"
"v_mfma_f32_16x16x32_bf16 v[136:139], a[136:139], v[60:63], v[136:139]\n"
"v_mfma_f32_16x16x32_bf16 v[140:143], a[140:143], v[60:63], v[140:143]\n"
"s_add_u32 m0, s34, 10240\n"
"v_cvt_pk_bf16_f32 v120, v120, v121\n"
"buffer_load_dwordx4 v191, s[8:11], s0 offen lds\n"
"s_add_u32 s0, s0, s33\n"
"v_cvt_pk_bf16_f32 v121, v122, v123\n"
"v_cvt_pk_bf16_f32 v122, v124, v125\n"
"v_cvt_pk_bf16_f32 v123, v126, v127\n"
"v_cvt_pk_bf16_f32 v128, v128, v129\n"
"v_cvt_pk_bf16_f32 v132, v132, v133\n"
"v_cvt_pk_bf16_f32 v129, v130, v131\n"
"v_cvt_pk_bf16_f32 v133, v134, v135\n"
"v_cvt_pk_bf16_f32 v134, v136, v137\n"
"v_cvt_pk_bf16_f32 v135, v138, v139\n"
"s_add_u32 m0, s34, 8192\n"
"v_cvt_pk_bf16_f32 v130, v140, v141\n"
"buffer_load_dwordx4 v190, s[8:11], s1 offen lds\n"
"v_cvt_pk_bf16_f32 v131, v142, v143\n"
"ds_write_b128 v186, v[120:123] offset:47264\n"
"s_add_u32 m0, s34, 11264\n"
"s_nop 0\n"
"buffer_load_dwordx4 v191, s[8:11], s1 offen lds\n"
"s_add_u32 s1, s1, s33\n"
"s_waitcnt vmcnt(12)\n"
"s_barrier\n"
"ds_read_b64_tr_b16 a[144:145], v68\n"
"ds_read_b64_tr_b16 a[146:147], v68 offset:1536\n"
"ds_read_b64_tr_b16 a[148:149], v68 offset:32\n"
"ds_read_b64_tr_b16 a[150:151], v68 offset:1568\n"
"ds_read_b64_tr_b16 a[152:153], v68 offset:64\n"
"ds_read_b64_tr_b16 a[154:155], v68 offset:1600\n"
"ds_read_b64_tr_b16 a[156:157], v68 offset:6144\n"
"ds_read_b64_tr_b16 a[158:159], v68 offset:7680\n"
"ds_read_b64_tr_b16 a[160:161], v68 offset:6176\n"
"ds_read_b64_tr_b16 a[162:163], v68 offset:7712\n"
"ds_read_b64_tr_b16 a[164:165], v68 offset:6208\n"
"ds_read_b64_tr_b16 a[166:167], v68 offset:7744\n"
"ds_read_b64_tr_b16 a[168:169], v68 offset:12288\n"
"ds_read_b64_tr_b16 a[170:171], v68 offset:13824\n"
"ds_read_b64_tr_b16 a[172:173], v68 offset:12320\n"
"ds_read_b64_tr_b16 a[174:175], v68 offset:13856\n"
"ds_read_b64_tr_b16 a[176:177], v68 offset:12352\n"
"ds_read_b64_tr_b16 a[178:179], v68 offset:13888\n"
"ds_read_b64_tr_b16 a[180:181], v68 offset:18432\n"
"ds_read_b64_tr_b16 a[182:183], v68 offset:19968\n"
"s_waitcnt lgkmcnt(15)\n"
"v_mfma_f32_16x16x32_bf16 v[204:207], a[144:147], v[32:35], 0\n"
"ds_read_b64_tr_b16 a[184:185], v68 offset:18464\n"
"ds_read_b64_tr_b16 a[186:187], v68 offset:20000\n"
"v_mfma_f32_16x16x32_bf16 v[208:211], a[148:151], v[32:35], 0\n"
"s_waitcnt lgkmcnt(15)\n"
"ds_read_b64_tr_b16 a[188:189], v68 offset:18496\n"
"ds_read_b64_tr_b16 a[190:191], v68 offset:20032\n"
"v_mfma_f32_16x16x32_bf16 v[212:215], a[152:155], v[32:35], 0\n"
"s_waitcnt lgkmcnt(15)\n"
"ds_read_b64_tr_b16 a[192:193], v68 offset:24576\n"
"ds_read_b64_tr_b16 a[194:195], v68 offset:26112\n"
"v_mfma_f32_16x16x32_bf16 v[204:207], a[156:159], v[36:39], v[204:207]\n"
"s_waitcnt lgkmcnt(15)\n"
"ds_read_b64_tr_b16 a[196:197], v68 offset:24608\n"
"ds_read_b64_tr_b16 a[198:199], v68 offset:26144\n"
"v_mfma_f32_16x16x32_bf16 v[208:211], a[160:163], v[36:39], v[208:211]\n"
"s_waitcnt lgkmcnt(15)\n"
"ds_read_b64_tr_b16 a[200:201], v68 offset:24640\n"
"ds_read_b64_tr_b16 a[202:203], v68 offset:26176\n"
"v_mfma_f32_16x16x32_bf16 v[212:215], a[164:167], v[36:39], v[212:215]\n"
"s_waitcnt lgkmcnt(15)\n"
"ds_read_b64_tr_b16 a[204:205], v68 offset:27648\n"
"ds_read_b64_tr_b16 a[206:207], v68 offset:29184\n"
"v_mfma_f32_16x16x32_bf16 v[204:207], a[168:171], v[40:43], v[204:207]\n"
"s_waitcnt lgkmcnt(15)\n"
"ds_read_b64_tr_b16 a[208:209], v68 offset:27680\n"
"ds_read_b64_tr_b16 a[210:211], v68 offset:29216\n"
"v_mfma_f32_16x16x32_bf16 v[208:211], a[172:175], v[40:43], v[208:211]\n"
"s_waitcnt lgkmcnt(15)\n"
"ds_read_b64_tr_b16 a[212:213], v68 offset:27712\n"
"ds_read_b64_tr_b16 a[214:215], v68 offset:29248\n"
"v_mfma_f32_16x16x32_bf16 v[212:215], a[176:179], v[40:43], v[212:215]\n"
"s_waitcnt lgkmcnt(15)\n"
"buffer_load_dword v200, off, s[28:31], s39 sc1\n"
"ds_write_b128 v186, v[128:131] offset:47328\n"
"v_mfma_f32_16x16x32_bf16 v[204:207], a[180:183], v[44:47], v[204:207]\n"
"s_waitcnt lgkmcnt(13)\n"
"v_mfma_f32_16x16x32_bf16 v[208:211], a[184:187], v[44:47], v[208:211]\n"
"s_waitcnt lgkmcnt(11)\n"
"ds_write_b128 v186, v[132:135] offset:47392\n"
"v_mfma_f32_16x16x32_bf16 v[212:215], a[188:191], v[44:47], v[212:215]\n"
"s_waitcnt lgkmcnt(8)\n"
"v_mfma_f32_16x16x32_bf16 v[204:207], a[192:195], v[48:51], v[204:207]\n"
"s_waitcnt lgkmcnt(6)\n"
"v_mfma_f32_16x16x32_bf16 v[208:211], a[196:199], v[48:51], v[208:211]\n"
"s_waitcnt lgkmcnt(4)\n"
"v_mfma_f32_16x16x32_bf16 v[212:215], a[200:203], v[48:51], v[212:215]\n"
"s_waitcnt lgkmcnt(2)\n"
"v_mfma_f32_16x16x32_bf16 v[204:207], a[204:207], v[52:55], v[204:207]\n"
"v_mfma_f32_16x16x32_bf16 v[208:211], a[208:211], v[52:55], v[208:211]\n"
"v_mfma_f32_16x16x32_bf16 v[212:215], a[212:215], v[52:55], v[212:215]\n"
"v_mfma_f32_16x16x32_bf16 v[216:219], a[144:147], a[216:219], 0\n"
"s_waitcnt lgkmcnt(0)\n"
"s_barrier\n"
"v_mfma_f32_16x16x32_bf16 v[216:219], a[156:159], a[220:223], v[216:219]\n"
"ds_read_b128 v[112:115], v178\n"
"ds_read_b128 v[120:123], v179\n"
"v_mfma_f32_16x16x32_bf16 v[220:223], a[148:151], a[216:219], 0\n"
"ds_read_b128 v[116:119], v178 offset:13504\n"
"v_cvt_pk_bf16_f32 v144, v204, v205\n"
"v_mfma_f32_16x16x32_bf16 v[216:219], a[168:171], a[224:227], v[216:219]\n"
"ds_read_b128 v[124:127], v179 offset:13504\n"
"v_cvt_pk_bf16_f32 v145, v206, v207\n"
"v_mfma_f32_16x16x32_bf16 v[220:223], a[160:163], a[220:223], v[220:223]\n"
"ds_read_b128 v[128:131], v178 offset:27008\n"
"v_cvt_pk_bf16_f32 v146, v208, v209\n"
"v_mfma_f32_16x16x32_bf16 v[216:219], a[180:183], a[228:231], v[216:219]\n"
"ds_read_b128 v[136:139], v179 offset:27008\n"
"v_cvt_pk_bf16_f32 v147, v210, v211\n"
"v_mfma_f32_16x16x32_bf16 v[220:223], a[172:175], a[224:227], v[220:223]\n"
"ds_read_b64 v[82:83], v178 offset:64\n"
"v_cvt_pk_bf16_f32 v158, v212, v213\n"
"v_mfma_f32_16x16x32_bf16 v[216:219], a[192:195], a[232:235], v[216:219]\n"
"ds_read_b64 v[84:85], v179 offset:64\n"
"v_cvt_pk_bf16_f32 v159, v214, v215\n"
"v_mfma_f32_16x16x32_bf16 v[220:223], a[184:187], a[228:231], v[220:223]\n"
"ds_read_b64 v[90:91], v178 offset:13568\n"
"v_mov_b64 v[68:69], -1\n"
"v_mfma_f32_16x16x32_bf16 v[216:219], a[204:207], a[236:239], v[216:219]\n"
"s_waitcnt lgkmcnt(8)\n"
"ds_read_b64 v[92:93], v179 offset:13568\n"
"v_mov_b64 v[70:71], -1\n"
"v_mfma_f32_16x16x32_bf16 v[112:115], v[112:115], v[144:147], 0\n"
"s_waitcnt lgkmcnt(8)\n"
"ds_read_b128 v[132:135], v180\n"
"v_mfma_f32_16x16x32_bf16 v[120:123], v[120:123], v[144:147], 0\n"
"s_waitcnt lgkmcnt(8)\n"
"ds_read_b128 v[140:143], v181\n"
"v_cvt_pk_bf16_f32 v148, v216, v217\n"
"v_mfma_f32_16x16x32_bf16 v[116:119], v[116:119], v[144:147], 0\n"
"s_waitcnt lgkmcnt(8)\n"
"ds_read_b64 v[98:99], v178 offset:27072\n"
"v_cvt_pk_bf16_f32 v149, v218, v219\n"
"v_mfma_f32_16x16x32_bf16 v[124:127], v[124:127], v[144:147], 0\n"
"s_waitcnt lgkmcnt(8)\n"
"ds_read_b64 v[100:101], v179 offset:27072\n"
".Lic0%=:\n"
"s_cbranch_execz .Lib0%=\n"
"v_subrev_u32 v178, s48, v178\n"
"v_mfma_f32_16x16x32_bf16 v[128:131], v[128:131], v[144:147], 0\n"
"s_waitcnt lgkmcnt(8)\n"
"ds_read_b64 v[106:107], v180 offset:64\n"
"v_subrev_u32 v179, s48, v179\n"
"v_mfma_f32_16x16x32_bf16 v[136:139], v[136:139], v[144:147], 0\n"
"s_waitcnt lgkmcnt(8)\n"
"ds_read_b64 v[108:109], v181 offset:64\n"
"v_subrev_u32 v180, s48, v180\n"
"v_mfma_f32_16x16x32_bf16 v[112:115], v[80:83], v[156:159], v[112:115]\n"
"s_waitcnt lgkmcnt(8)\n"
"ds_read_b64_tr_b16 v[244:245], v182 offset:128\n"
"v_subrev_u32 v181, s48, v181\n"
"v_mfma_f32_16x16x32_bf16 v[120:123], v[84:87], v[158:161], v[120:123]\n"
"s_waitcnt lgkmcnt(8)\n"
"ds_read_b64_tr_b16 v[246:247], v183 offset:128\n"
"v_mfma_f32_16x16x32_bf16 v[116:119], v[88:91], v[156:159], v[116:119]\n"
"s_waitcnt lgkmcnt(8)\n"
"ds_read_b64_tr_b16 v[248:249], v182 offset:896\n"
"v_mfma_f32_16x16x32_bf16 v[124:127], v[92:95], v[158:161], v[124:127]\n"
"s_waitcnt lgkmcnt(8)\n"
"ds_read_b64_tr_b16 v[250:251], v183 offset:896\n"
"v_max_f32 v112, v112, v120\n"
"v_mfma_f32_16x16x32_bf16 v[132:135], v[132:135], v[144:147], 0\n"
"s_waitcnt vmcnt(11) lgkmcnt(8)\n"
"v_max_f32 v113, v113, v121\n"
"v_max_f32 v114, v114, v122\n"
"v_mfma_f32_16x16x32_bf16 v[140:143], v[140:143], v[144:147], 0\n"
"s_waitcnt lgkmcnt(7)\n"
"ds_read_b128 v[32:35], v188\n"
"ds_read_b128 v[36:39], v189\n"
"v_mfma_f32_16x16x32_bf16 v[128:131], v[96:99], v[156:159], v[128:131]\n"
"s_waitcnt lgkmcnt(8)\n"
"ds_read_b64_tr_b16 v[120:121], v182 offset:136\n"
"v_max_f32 v115, v115, v123\n"
"v_mfma_f32_16x16x32_bf16 v[136:139], v[100:103], v[158:161], v[136:139]\n"
"s_waitcnt lgkmcnt(8)\n"
"ds_read_b64_tr_b16 v[122:123], v183 offset:136\n"
"v_max_f32 v116, v116, v124\n"
"v_mfma_f32_16x16x32_bf16 v[132:135], v[104:107], v[156:159], v[132:135]\n"
"s_waitcnt lgkmcnt(8)\n"
"v_max_f32 v117, v117, v125\n"
"v_max_f32 v118, v118, v126\n"
"v_mfma_f32_16x16x32_bf16 v[140:143], v[108:111], v[158:161], v[140:143]\n"
"ds_read_b64_tr_b16 v[124:125], v182 offset:904\n"
"v_max_f32 v119, v119, v127\n"
"v_mfma_f32_16x16x32_bf16 v[220:223], a[196:199], a[232:235], v[220:223]\n"
"ds_read_b64_tr_b16 v[126:127], v183 offset:904\n"
"v_max3_f32 v170, v112, v113, v114\n"
"v_mfma_f32_16x16x32_bf16 v[220:223], a[208:211], a[236:239], v[220:223]\n"
"v_max3_f32 v170, v170, v115, v116\n"
"v_max3_f32 v170, v170, v117, v118\n"
"v_mfma_f32_16x16x32_bf16 v[224:227], a[152:155], a[216:219], 0\n"
"s_waitcnt vmcnt(9)\n"
"v_max_f32 v128, v128, v136\n"
"v_max_f32 v129, v129, v137\n"
"v_mfma_f32_16x16x32_bf16 v[224:227], a[164:167], a[220:223], v[224:227]\n"
"s_waitcnt lgkmcnt(5)\n"
"ds_read_b128 v[40:43], v188 offset:1024\n"
"ds_read_b128 v[44:47], v189 offset:1024\n"
"v_mfma_f32_16x16x32_bf16 v[212:215], a[0:3], v[32:35], 0\n"
"s_waitcnt lgkmcnt(6)\n"
"ds_read_b64_tr_b16 v[136:137], v182 offset:72\n"
"v_max_f32 v130, v130, v138\n"
"v_mfma_f32_16x16x32_bf16 v[216:219], a[4:7], v[32:35], 0\n"
"s_add_u32 m0, s34, 0\n"
"v_max3_f32 v170, v170, v119, v128\n"
"v_max_f32 v131, v131, v139\n"
"v_mfma_f32_16x16x32_bf16 v[212:215], a[12:15], v[36:39], v[212:215]\n"
"buffer_load_dwordx4 v190, s[8:11], s32 offen lds\n"
"ds_read_b64_tr_b16 v[138:139], v183 offset:72\n"
"v_mfma_f32_16x16x32_bf16 v[216:219], a[16:19], v[36:39], v[216:219]\n"
"v_max3_f32 v170, v170, v129, v130\n"
"v_max_f32 v132, v132, v140\n"
"v_mfma_f32_16x16x32_bf16 v[228:231], a[144:147], a[240:243], 0\n"
"v_max_f32 v133, v133, v141\n"
"v_max_f32 v134, v134, v142\n"
"v_mfma_f32_16x16x32_bf16 v[224:227], a[176:179], a[224:227], v[224:227]\n"
"s_waitcnt vmcnt(8)\n"
"ds_read_b64_tr_b16 v[140:141], v182 offset:840\n"
"v_max3_f32 v170, v170, v131, v132\n"
"v_mfma_f32_16x16x32_bf16 v[228:231], a[156:159], a[244:247], v[228:231]\n"
"ds_read_b128 v[48:51], v188 offset:2048\n"
"ds_read_b128 v[52:55], v189 offset:2048\n"
"v_mfma_f32_16x16x32_bf16 v[204:207], a[148:151], a[240:243], 0\n"
"s_waitcnt lgkmcnt(6)\n"
"v_max_f32 v135, v135, v143\n"
"v_max3_f32 v170, v170, v133, v134\n"
"v_mfma_f32_16x16x32_bf16 v[212:215], a[24:27], v[40:43], v[212:215]\n"
"s_waitcnt lgkmcnt(5)\n"
"ds_read_b64_tr_b16 v[142:143], v183 offset:840\n"
"v_max_f32 v171, v170, v135\n"
"v_mfma_f32_16x16x32_bf16 v[216:219], a[28:31], v[40:43], v[216:219]\n"
"v_max_f32 v170, v170, v135\n"
"v_cvt_pk_bf16_f32 v150, v220, v221\n"
"v_mfma_f32_16x16x32_bf16 v[212:215], a[36:39], v[44:47], v[212:215]\n"
"v_permlane16_swap_b32 v170, v171\n"
"v_mfma_f32_16x16x32_bf16 v[216:219], a[40:43], v[44:47], v[216:219]\n"
"v_max_f32 v170, v170, v171\n"
"v_mov_b32 v171, v170\n"
"v_mfma_f32_16x16x32_bf16 v[224:227], a[188:191], a[228:231], v[224:227]\n"
"s_add_u32 m0, s34, 3072\n"
"v_cvt_pk_bf16_f32 v151, v222, v223\n"
"v_mov_b64 v[98:99], 0\n"
"v_mfma_f32_16x16x32_bf16 v[220:223], a[8:11], v[32:35], 0\n"
"buffer_load_dwordx4 v191, s[8:11], s32 offen lds\n"
"v_mov_b64 v[100:101], 0\n"
"v_mfma_f32_16x16x32_bf16 v[220:223], a[20:23], v[36:39], v[220:223]\n"
"s_waitcnt lgkmcnt(2)\n"
"v_permlane32_swap_b32 v170, v171\n"
"v_mfma_f32_16x16x32_bf16 v[220:223], a[32:35], v[40:43], v[220:223]\n"
"s_add_u32 s32, s32, s33\n"
"v_max_f32 v170, v170, v171\n"
"v_mul_f32 v170, 0xbe553b95, v170\n"
"v_mfma_f32_16x16x32_bf16 v[220:223], a[44:47], v[44:47], v[220:223]\n"
"v_fmamk_f32 v112, v112, 0x3e553b95, v170\n"
"v_fmamk_f32 v113, v113, 0x3e553b95, v170\n"
"v_mfma_f32_16x16x32_bf16 v[212:215], a[48:51], v[48:51], v[212:215]\n"
"s_waitcnt lgkmcnt(1)\n"
"v_fmamk_f32 v114, v114, 0x3e553b95, v170\n"
"v_fmamk_f32 v115, v115, 0x3e553b95, v170\n"
"v_mfma_f32_16x16x32_bf16 v[216:219], a[52:55], v[48:51], v[216:219]\n"
"v_fmamk_f32 v116, v116, 0x3e553b95, v170\n"
"v_fmamk_f32 v117, v117, 0x3e553b95, v170\n"
"v_mfma_f32_16x16x32_bf16 v[220:223], a[56:59], v[48:51], v[220:223]\n"
"v_fmamk_f32 v118, v118, 0x3e553b95, v170\n"
"v_fmamk_f32 v119, v119, 0x3e553b95, v170\n"
"v_mfma_f32_16x16x32_bf16 v[212:215], a[60:63], v[52:55], v[212:215]\n"
"v_exp_f32 v112, v112\n"
"v_mfma_f32_16x16x32_bf16 v[216:219], a[64:67], v[52:55], v[216:219]\n"
"v_exp_f32 v113, v113\n"
"v_mfma_f32_16x16x32_bf16 v[220:223], a[68:71], v[52:55], v[220:223]\n"
"v_exp_f32 v114, v114\n"
"v_mfma_f32_16x16x32_bf16 v[228:231], a[168:171], a[248:251], v[228:231]\n"
"s_add_u32 m0, s34, 1024\n"
"v_exp_f32 v115, v115\n"
"v_mfma_f32_16x16x32_bf16 v[204:207], a[160:163], a[244:247], v[204:207]\n"
"buffer_load_dwordx4 v190, s[8:11], s0 offen lds\n"
"v_cvt_pk_bf16_f32 v82, v112, v114\n"
"v_mfma_f32_16x16x32_bf16 v[224:227], a[200:203], a[232:235], v[224:227]\n"
"v_exp_f32 v116, v116\n"
"v_mfma_f32_16x16x32_bf16 v[228:231], a[180:183], a[252:255], v[228:231]\n"
"v_exp_f32 v117, v117\n"
"v_mfma_f32_16x16x32_bf16 v[204:207], a[172:175], a[248:251], v[204:207]\n"
"v_exp_f32 v118, v118\n"
"v_mfma_f32_16x16x32_bf16 v[224:227], a[212:215], a[236:239], v[224:227]\n"
"v_exp_f32 v119, v119\n"
"v_mfma_f32_16x16x32_bf16 v[228:231], a[192:195], v[56:59], v[228:231]\n"
"v_cvt_pk_bf16_f32 v83, v113, v115\n"
"v_cvt_pk_bf16_f32 v84, v116, v118\n"
"v_mfma_f32_16x16x32_bf16 v[204:207], a[184:187], a[252:255], v[204:207]\n"
"ds_read_b64_tr_b16 v[112:113], v182 offset:27136\n"
"ds_read_b64_tr_b16 v[114:115], v183 offset:27136\n"
"v_mfma_f32_16x16x32_bf16 v[228:231], a[204:207], v[60:63], v[228:231]\n"
"v_cvt_pk_bf16_f32 v85, v117, v119\n"
"v_mov_b64 v[106:107], 0\n"
"v_mfma_f32_16x16x32_bf16 v[204:207], a[196:199], v[56:59], v[204:207]\n"
"s_waitcnt vmcnt(3)\n"
"ds_read_b64_tr_b16 v[116:117], v182 offset:27904\n"
"ds_read_b64_tr_b16 v[118:119], v183 offset:27904\n"
"v_smfmac_f32_16x16x64_bf16 v[252:255], v[82:85], v[244:251], v66\n"
"ds_read_b64_tr_b16 v[244:245], v182 offset:27144\n"
"v_smfmac_f32_16x16x64_bf16 v[98:101], v[82:85], v[120:127], v66\n"
"s_add_u32 m0, s34, 4096\n"
"s_nop 0\n"
"buffer_load_dwordx4 v191, s[8:11], s0 offen lds\n"
"v_mfma_f32_16x16x32_bf16 v[204:207], a[208:211], v[60:63], v[204:207]\n"
"s_add_u32 s0, s0, s33\n"
"ds_read_b64_tr_b16 v[246:247], v183 offset:27144\n"
"ds_read_b64_tr_b16 v[248:249], v182 offset:27912\n"
"v_mfma_f32_16x16x32_bf16 v[208:211], a[152:155], a[240:243], 0\n"
"ds_read_b64_tr_b16 v[250:251], v183 offset:27912\n"
"v_mov_b64 v[108:109], 0\n"
"v_mfma_f32_16x16x32_bf16 v[208:211], a[164:167], a[244:247], v[208:211]\n"
"s_waitcnt lgkmcnt(8)\n"
"ds_read_b64_tr_b16 v[120:121], v182 offset:27080\n"
"v_fmamk_f32 v128, v128, 0x3e553b95, v170\n"
"v_smfmac_f32_16x16x64_bf16 v[106:109], v[82:85], v[136:143], v66\n"
"ds_read_b64_tr_b16 v[122:123], v183 offset:27080\n"
"v_mfma_f32_16x16x32_bf16 v[82:85], v[172:175], v[82:85], 0\n"
"ds_read_b64_tr_b16 v[124:125], v182 offset:27848\n"
"v_fmamk_f32 v129, v129, 0x3e553b95, v170\n"
"v_mfma_f32_16x16x32_bf16 v[208:211], a[176:179], a[248:251], v[208:211]\n"
"ds_read_b64_tr_b16 v[126:127], v183 offset:27848\n"
"v_fmamk_f32 v130, v130, 0x3e553b95, v170\n"
"v_mfma_f32_16x16x32_bf16 v[208:211], a[188:191], a[252:255], v[208:211]\n"
"v_fmamk_f32 v131, v131, 0x3e553b95, v170\n"
"v_fmamk_f32 v132, v132, 0x3e553b95, v170\n"
"v_mfma_f32_16x16x32_bf16 v[208:211], a[200:203], v[56:59], v[208:211]\n"
"v_fmamk_f32 v133, v133, 0x3e553b95, v170\n"
"v_fmamk_f32 v134, v134, 0x3e553b95, v170\n"
"v_mfma_f32_16x16x32_bf16 v[208:211], a[212:215], v[60:63], v[208:211]\n"
"v_fmamk_f32 v135, v135, 0x3e553b95, v170\n"
"v_exp_f32 v128, v128\n"
"s_add_u32 m0, s34, 2048\n"
"v_exp_f32 v129, v129\n"
"buffer_load_dwordx4 v190, s[8:11], s1 offen lds\n"
"v_exp_f32 v130, v130\n"
"v_exp_f32 v131, v131\n"
"v_exp_f32 v132, v132\n"
"v_exp_f32 v133, v133\n"
"v_exp_f32 v134, v134\n"
"v_exp_f32 v135, v135\n"
"v_cvt_pk_bf16_f32 v90, v128, v130\n"
"v_cvt_pk_bf16_f32 v91, v129, v131\n"
"v_cvt_pk_bf16_f32 v92, v132, v134\n"
"v_cvt_pk_bf16_f32 v93, v133, v135\n"
"v_and_b32 v200, 0xffffff00, v200\n"
"v_add_u32 v200, 0x100, v200\n"
"v_sub_u32 v201, 0, v200\n"
"s_nop 0\n"
"v_readfirstlane_b32 s50, v201\n"
"v_cvt_pk_bf16_f32 v212, v212, v213\n"
"v_cvt_pk_bf16_f32 v213, v214, v215\n"
"v_cvt_pk_bf16_f32 v214, v216, v217\n"
"v_cvt_pk_bf16_f32 v215, v218, v219\n"
"v_mfma_f32_16x16x32_bf16 v[216:219], a[148:151], v[32:35], 0\n"
"s_waitcnt lgkmcnt(0)\n"
"s_mov_b64 exec, 1\n"
"buffer_store_dword v200, off, s[28:31], s39 offset:128 sc1\n"
"s_mov_b64 exec, -1\n"
"v_smfmac_f32_16x16x64_bf16 v[252:255], v[90:93], v[112:119], v66\n"
"ds_write_b128 v186, v[212:215] offset:60768\n"
"v_smfmac_f32_16x16x64_bf16 v[98:101], v[90:93], v[244:251], v66\n"
"s_add_u32 m0, s34, 5120\n"
"s_nop 0\n"
"buffer_load_dwordx4 v191, s[8:11], s1 offen lds\n"
"v_smfmac_f32_16x16x64_bf16 v[106:109], v[90:93], v[120:127], v66\n"
"s_add_u32 s1, s1, s33\n"
"v_cvt_pk_bf16_f32 v220, v220, v221\n"
"v_mfma_f32_16x16x32_bf16 v[90:93], v[172:175], v[90:93], v[82:85]\n"
"v_cvt_pk_bf16_f32 v252, v252, v253\n"
"v_cvt_pk_bf16_f32 v253, v254, v255\n"
"v_mfma_f32_16x16x32_bf16 v[212:215], a[152:155], v[32:35], 0\n"
"v_cvt_pk_bf16_f32 v98, v98, v99\n"
"v_cvt_pk_bf16_f32 v99, v100, v101\n"
"v_mfma_f32_16x16x32_bf16 v[216:219], a[160:163], v[36:39], v[216:219]\n"
"v_cvt_pk_bf16_f32 v106, v106, v107\n"
"v_cvt_pk_bf16_f32 v107, v108, v109\n"
"v_mfma_f32_16x16x32_bf16 v[212:215], a[164:167], v[36:39], v[212:215]\n"
"v_rcp_f32 v171, v90\n"
"v_mfma_f32_16x16x32_bf16 v[216:219], a[172:175], v[40:43], v[216:219]\n"
"v_cvt_pk_bf16_f32 v171, v171, v171\n"
"v_and_b32 v64, v171, v176\n"
"v_mfma_f32_16x16x32_bf16 v[212:215], a[176:179], v[40:43], v[212:215]\n"
"v_and_b32 v65, v171, v177\n"
"v_cvt_pk_bf16_f32 v221, v222, v223\n"
"v_mfma_f32_16x16x32_bf16 v[216:219], a[184:187], v[44:47], v[216:219]\n"
"v_cvt_pk_bf16_f32 v162, v224, v225\n"
"v_cvt_pk_bf16_f32 v163, v226, v227\n"
"v_mfma_f32_16x16x16_bf16 v[252:255], v[252:253], v[64:65], 0\n"
"v_cvt_pk_bf16_f32 v152, v228, v229\n"
"v_cvt_pk_bf16_f32 v153, v230, v231\n"
"v_mfma_f32_16x16x16_bf16 v[98:101], v[98:99], v[64:65], 0\n"
"v_cvt_pk_bf16_f32 v154, v204, v205\n"
"v_cvt_pk_bf16_f32 v155, v206, v207\n"
"v_mfma_f32_16x16x16_bf16 v[106:109], v[106:107], v[64:65], 0\n"
"ds_read_b128 v[136:139], v178 offset:13504\n"
"ds_read_b128 v[128:131], v179 offset:13504\n"
"v_mfma_f32_16x16x32_bf16 v[224:227], a[72:75], v[32:35], 0\n"
"v_cvt_pk_bf16_f32 v252, v252, v253\n"
"v_cvt_pk_bf16_f32 v253, v254, v255\n"
"v_mfma_f32_16x16x32_bf16 v[228:231], a[76:79], v[32:35], 0\n"
"v_cvt_pk_bf16_f32 v254, v98, v99\n"
"v_cvt_pk_bf16_f32 v255, v100, v101\n"
"v_mfma_f32_16x16x32_bf16 v[204:207], a[80:83], v[32:35], 0\n"
"buffer_store_dwordx4 v[252:255], v192, s[28:31], s37 offen sc1\n"
"v_mfma_f32_16x16x32_bf16 v[224:227], a[84:87], v[36:39], v[224:227]\n"
"v_cvt_pk_bf16_f32 v106, v106, v107\n"
"v_cvt_pk_bf16_f32 v107, v108, v109\n"
"v_mfma_f32_16x16x32_bf16 v[228:231], a[88:91], v[36:39], v[228:231]\n"
"ds_read_b128 v[140:143], v178 offset:27008\n"
"buffer_store_dwordx2 v[106:107], v193, s[28:31], s37 offen offset:1024 sc1\n"
"v_mfma_f32_16x16x32_bf16 v[204:207], a[92:95], v[36:39], v[204:207]\n"
"s_waitcnt lgkmcnt(2)\n"
"ds_read_b128 v[132:135], v179 offset:27008\n"
"ds_read_b64 v[80:81], v20 offset:272\n"
"v_mfma_f32_16x16x32_bf16 v[224:227], a[96:99], v[40:43], v[224:227]\n"
"s_waitcnt lgkmcnt(3)\n"
"ds_read_b64 v[86:87], v20 offset:336\n"
"ds_read_b64 v[88:89], v20 offset:544\n"
"v_mfma_f32_16x16x32_bf16 v[228:231], a[100:103], v[40:43], v[228:231]\n"
"s_add_u32 s37, s37, 1572864\n"
"ds_read_b64 v[94:95], v20 offset:608\n"
"ds_read_b128 v[112:115], v180\n"
"v_mfma_f32_16x16x32_bf16 v[204:207], a[104:107], v[40:43], v[204:207]\n"
"s_add_u32 s4, s37, 15728640\n"
"ds_read_b128 v[244:247], v181\n"
"v_cvt_pk_bf16_f32 v166, v208, v209\n"
"v_mfma_f32_16x16x32_bf16 v[224:227], a[108:111], v[44:47], v[224:227]\n"
"ds_read_b64 v[96:97], v20 offset:816\n"
"ds_read_b64 v[102:103], v20 offset:880\n"
"v_mfma_f32_16x16x32_bf16 v[228:231], a[112:115], v[44:47], v[228:231]\n"
"ds_read_b64 v[82:83], v178 offset:13568\n"
"v_cvt_pk_bf16_f32 v167, v210, v211\n"
"v_mfma_f32_16x16x32_bf16 v[204:207], a[116:119], v[44:47], v[204:207]\n"
"s_waitcnt lgkmcnt(10)\n"
"ds_read_b64 v[84:85], v179 offset:13568\n"
"v_subrev_u32 v182, s48, v182\n"
"v_mfma_f32_16x16x32_bf16 v[224:227], a[120:123], v[48:51], v[224:227]\n"
"ds_read_b64 v[90:91], v178 offset:27072\n"
"v_subrev_u32 v183, s48, v183\n"
"v_mfma_f32_16x16x32_bf16 v[228:231], a[124:127], v[48:51], v[228:231]\n"
"s_waitcnt lgkmcnt(10)\n"
"ds_read_b64 v[92:93], v179 offset:27072\n"
"v_mov_b64 v[252:253], 0\n"
"v_mfma_f32_16x16x32_bf16 v[204:207], a[128:131], v[48:51], v[204:207]\n"
"s_waitcnt lgkmcnt(9)\n"
"ds_read_b64 v[104:105], v20 offset:1088\n"
"ds_read_b64 v[110:111], v20 offset:1152\n"
"v_mfma_f32_16x16x32_bf16 v[224:227], a[132:135], v[52:55], v[224:227]\n"
"s_waitcnt lgkmcnt(10)\n"
"ds_read_b64 v[98:99], v180 offset:64\n"
"v_bfi_b32 v80, v4, v80, v26\n"
"v_mfma_f32_16x16x32_bf16 v[228:231], a[136:139], v[52:55], v[228:231]\n"
"s_waitcnt lgkmcnt(10)\n"
"ds_read_b64 v[100:101], v181 offset:64\n"
"v_bfi_b32 v81, v5, v81, v26\n"
"v_mfma_f32_16x16x32_bf16 v[204:207], a[140:143], v[52:55], v[204:207]\n"
"s_waitcnt lgkmcnt(9)\n"
"ds_read_b64_tr_b16 v[120:121], v182 offset:13632\n"
"v_cvt_pk_bf16_f32 v224, v224, v225\n"
"v_mfma_f32_16x16x32_bf16 v[208:211], a[144:147], v[32:35], 0\n"
"s_waitcnt lgkmcnt(9)\n"
"ds_read_b128 v[32:35], v188 offset:6144\n"
"v_cvt_pk_bf16_f32 v225, v226, v227\n"
"v_mfma_f32_16x16x32_bf16 v[208:211], a[156:159], v[36:39], v[208:211]\n"
"ds_read_b128 v[36:39], v189 offset:6144\n"
"v_cvt_pk_bf16_f32 v226, v228, v229\n"
"v_mfma_f32_16x16x32_bf16 v[136:139], v[136:139], v[148:151], 0\n"
"s_waitcnt lgkmcnt(10)\n"
"ds_read_b64_tr_b16 v[122:123], v183 offset:13632\n"
"v_cvt_pk_bf16_f32 v227, v230, v231\n"
"v_mfma_f32_16x16x32_bf16 v[128:131], v[128:131], v[148:151], 0\n"
"ds_write_b128 v186, v[224:227] offset:60896\n"
"v_mfma_f32_16x16x32_bf16 v[140:143], v[140:143], v[148:151], 0\n"
"s_waitcnt lgkmcnt(7)\n"
"v_cvt_pk_bf16_f32 v222, v204, v205\n"
"v_cvt_pk_bf16_f32 v223, v206, v207\n"
"v_mfma_f32_16x16x32_bf16 v[132:135], v[132:135], v[148:151], 0\n"
"v_bfi_b32 v86, v6, v86, v26\n"
"v_bfi_b32 v87, v7, v87, v26\n"
"v_mfma_f32_16x16x32_bf16 v[112:115], v[112:115], v[148:151], 0\n"
"ds_write_b128 v186, v[220:223] offset:60832\n"
"v_mfma_f32_16x16x32_bf16 v[244:247], v[244:247], v[148:151], 0\n"
"s_waitcnt lgkmcnt(4)\n"
"v_bfi_b32 v88, v0, v88, v26\n"
"v_bfi_b32 v89, v1, v89, v26\n"
"v_mfma_f32_16x16x32_bf16 v[208:211], a[168:171], v[40:43], v[208:211]\n"
"s_waitcnt lgkmcnt(3)\n"
"v_bfi_b32 v94, v2, v94, v26\n"
"v_bfi_b32 v95, v3, v95, v26\n"
"v_mfma_f32_16x16x32_bf16 v[136:139], v[80:83], v[160:163], v[136:139]\n"
"s_add_u32 m0, s34, 6144\n"
"ds_read_b128 v[40:43], v188 offset:7168\n"
"buffer_load_dwordx4 v190, s[8:11], s32 offen lds\n"
"v_mfma_f32_16x16x32_bf16 v[128:131], v[84:87], v[162:165], v[128:131]\n"
"ds_read_b64_tr_b16 v[124:125], v182 offset:14400\n"
"ds_read_b64_tr_b16 v[126:127], v183 offset:14400\n"
"v_mfma_f32_16x16x32_bf16 v[140:143], v[88:91], v[160:163], v[140:143]\n"
"v_bfi_b32 v96, v0, v96, v26\n"
"v_bfi_b32 v97, v1, v97, v26\n"
"v_mfma_f32_16x16x32_bf16 v[132:135], v[92:95], v[162:165], v[132:135]\n"
"v_bfi_b32 v102, v2, v102, v26\n"
"v_bfi_b32 v103, v3, v103, v26\n"
"v_mfma_f32_16x16x32_bf16 v[112:115], v[96:99], v[160:163], v[112:115]\n"
"v_bfi_b32 v104, v8, v104, v26\n"
"v_bfi_b32 v105, v9, v105, v26\n"
"v_mfma_f32_16x16x32_bf16 v[244:247], v[100:103], v[162:165], v[244:247]\n"
"s_waitcnt lgkmcnt(3)\n"
"v_bfi_b32 v110, v10, v110, v26\n"
"v_bfi_b32 v111, v11, v111, v26\n"
"v_mfma_f32_16x16x32_bf16 v[208:211], a[180:183], v[44:47], v[208:211]\n"
"s_barrier\n"
"v_mfma_f32_16x16x32_bf16 v[212:215], a[188:191], v[44:47], v[212:215]\n"
"ds_read_b128 v[44:47], v189 offset:7168\n"
"ds_read_b128 v[116:119], v180 offset:13504\n"
"v_mfma_f32_16x16x32_bf16 v[208:211], a[192:195], v[48:51], v[208:211]\n"
"ds_read_b128 v[248:251], v181 offset:13504\n"
"v_max_f32 v136, v136, v128\n"
"v_mfma_f32_16x16x32_bf16 v[216:219], a[196:199], v[48:51], v[216:219]\n"
"ds_read_b64 v[106:107], v180 offset:13568\n"
"v_max_f32 v137, v137, v129\n"
"v_mfma_f32_16x16x32_bf16 v[212:215], a[200:203], v[48:51], v[212:215]\n"
"s_add_u32 m0, s79, 130176\n"
"ds_read_b128 v[48:51], v188 offset:8192\n"
"buffer_load_dwordx4 v192, s[16:19], s43 offen lds\n"
"v_mfma_f32_16x16x32_bf16 v[208:211], a[204:207], v[52:55], v[208:211]\n"
"ds_read_b64 v[108:109], v181 offset:13568\n"
"v_max_f32 v138, v138, v130\n"
"v_mfma_f32_16x16x32_bf16 v[216:219], a[208:211], v[52:55], v[216:219]\n"
"ds_read_b64_tr_b16 v[128:129], v182 offset:13640\n"
"v_max_f32 v139, v139, v131\n"
"v_mfma_f32_16x16x32_bf16 v[212:215], a[212:215], v[52:55], v[212:215]\n"
"ds_read_b128 v[52:55], v189 offset:8192\n"
"v_max_f32 v140, v140, v132\n"
"v_mfma_f32_16x16x32_bf16 v[228:231], a[0:3], v[32:35], 0\n"
"ds_read_b64_tr_b16 v[130:131], v183 offset:13640\n"
"v_max_f32 v141, v141, v133\n"
"v_mfma_f32_16x16x32_bf16 v[204:207], a[4:7], v[32:35], 0\n"
"ds_read_b64_tr_b16 v[132:133], v182 offset:14408\n"
"v_max_f32 v142, v142, v134\n"
"v_mfma_f32_16x16x32_bf16 v[220:223], a[8:11], v[32:35], 0\n"
"s_waitcnt lgkmcnt(8)\n"
"v_max_f32 v143, v143, v135\n"
"v_max3_f32 v170, v136, v137, v138\n"
"v_mfma_f32_16x16x32_bf16 v[116:119], v[116:119], v[148:151], 0\n"
"s_waitcnt lgkmcnt(7)\n"
"ds_read_b64_tr_b16 v[134:135], v183 offset:14408\n"
"v_max3_f32 v170, v170, v139, v140\n"
"v_mfma_f32_16x16x32_bf16 v[248:251], v[248:251], v[148:151], 0\n"
"s_waitcnt lgkmcnt(7)\n"
"v_max3_f32 v170, v170, v141, v142\n"
"v_max_f32 v112, v112, v244\n"
"v_mfma_f32_16x16x32_bf16 v[116:119], v[104:107], v[160:163], v[116:119]\n"
"v_max_f32 v113, v113, v245\n"
"v_max_f32 v114, v114, v246\n"
"v_mfma_f32_16x16x32_bf16 v[228:231], a[12:15], v[36:39], v[228:231]\n"
"s_waitcnt lgkmcnt(5)\n"
"ds_read_b64_tr_b16 v[244:245], v182 offset:13576\n"
"v_max3_f32 v170, v170, v143, v112\n"
"v_mfma_f32_16x16x32_bf16 v[248:251], v[108:111], v[162:165], v[248:251]\n"
"s_add_u32 m0, s79, 130176\n"
"v_max_f32 v115, v115, v247\n"
"v_max3_f32 v170, v170, v113, v114\n"
"v_mfma_f32_16x16x32_bf16 v[204:207], a[16:19], v[36:39], v[204:207]\n"
"buffer_load_dwordx4 v192, s[16:19], s43 offen offset:1024 lds\n"
"ds_read_b64_tr_b16 v[246:247], v183 offset:13576\n"
"v_mfma_f32_16x16x32_bf16 v[220:223], a[20:23], v[36:39], v[220:223]\n"
"v_mov_b64 v[254:255], 0\n"
"v_max_f32 v116, v116, v248\n"
"v_mfma_f32_16x16x32_bf16 v[228:231], a[24:27], v[40:43], v[228:231]\n"
"v_max_f32 v117, v117, v249\n"
"v_max_f32 v118, v118, v250\n"
"v_mfma_f32_16x16x32_bf16 v[204:207], a[28:31], v[40:43], v[204:207]\n"
"buffer_store_dwordx4 v[68:71], v192, s[28:31], s4 offen sc1\n"
"v_mfma_f32_16x16x32_bf16 v[220:223], a[32:35], v[40:43], v[220:223]\n"
"ds_read_b64_tr_b16 v[248:249], v182 offset:14344\n"
"v_max3_f32 v170, v170, v115, v116\n"
"v_mfma_f32_16x16x32_bf16 v[228:231], a[36:39], v[44:47], v[228:231]\n"
"v_max_f32 v119, v119, v251\n"
"v_max3_f32 v170, v170, v117, v118\n"
"v_mfma_f32_16x16x32_bf16 v[204:207], a[40:43], v[44:47], v[204:207]\n"
"ds_read_b64_tr_b16 v[250:251], v183 offset:14344\n"
"v_max_f32 v171, v170, v119\n"
"v_mfma_f32_16x16x32_bf16 v[220:223], a[44:47], v[44:47], v[220:223]\n"
"v_max_f32 v170, v170, v119\n"
"v_mov_b64 v[98:99], 0\n"
"v_mfma_f32_16x16x32_bf16 v[228:231], a[48:51], v[48:51], v[228:231]\n"
"s_waitcnt lgkmcnt(7)\n"
"v_permlane16_swap_b32 v170, v171\n"
"v_mfma_f32_16x16x32_bf16 v[204:207], a[52:55], v[48:51], v[204:207]\n"
"s_add_u32 m0, s79, 130176\n"
"v_max_f32 v170, v170, v171\n"
"v_mov_b32 v171, v170\n"
"v_mfma_f32_16x16x32_bf16 v[220:223], a[56:59], v[48:51], v[220:223]\n"
"buffer_load_dwordx4 v192, s[16:19], s43 offen offset:2048 lds\n"
"v_mov_b64 v[100:101], 0\n"
"v_mfma_f32_16x16x32_bf16 v[228:231], a[60:63], v[52:55], v[228:231]\n"
"s_add_u32 s43, s79, 18432\n"
"v_permlane32_swap_b32 v170, v171\n"
"v_mfma_f32_16x16x32_bf16 v[204:207], a[64:67], v[52:55], v[204:207]\n"
"v_max_f32 v170, v170, v171\n"
"v_mul_f32 v170, 0xbe553b95, v170\n"
"v_mfma_f32_16x16x32_bf16 v[220:223], a[68:71], v[52:55], v[220:223]\n"
"v_fmamk_f32 v136, v136, 0x3e553b95, v170\n"
"v_fmamk_f32 v137, v137, 0x3e553b95, v170\n"
"v_mfma_f32_16x16x32_bf16 v[224:227], a[72:75], v[32:35], 0\n"
"v_fmamk_f32 v138, v138, 0x3e553b95, v170\n"
"v_fmamk_f32 v139, v139, 0x3e553b95, v170\n"
"v_mfma_f32_16x16x32_bf16 v[224:227], a[84:87], v[36:39], v[224:227]\n"
"v_fmamk_f32 v140, v140, 0x3e553b95, v170\n"
"v_fmamk_f32 v141, v141, 0x3e553b95, v170\n"
"v_mfma_f32_16x16x32_bf16 v[224:227], a[96:99], v[40:43], v[224:227]\n"
"v_fmamk_f32 v142, v142, 0x3e553b95, v170\n"
"v_fmamk_f32 v143, v143, 0x3e553b95, v170\n"
"v_mfma_f32_16x16x32_bf16 v[224:227], a[108:111], v[44:47], v[224:227]\n"
"v_exp_f32 v136, v136\n"
"v_mfma_f32_16x16x32_bf16 v[224:227], a[120:123], v[48:51], v[224:227]\n"
"v_exp_f32 v137, v137\n"
"v_mfma_f32_16x16x32_bf16 v[224:227], a[132:135], v[52:55], v[224:227]\n"
"v_exp_f32 v138, v138\n"
"s_add_u32 m0, s34, 9216\n"
"v_exp_f32 v139, v139\n"
"buffer_load_dwordx4 v191, s[8:11], s32 offen lds\n"
"s_add_u32 s32, s32, s33\n"
"v_exp_f32 v140, v140\n"
"v_exp_f32 v141, v141\n"
"v_exp_f32 v142, v142\n"
"v_exp_f32 v143, v143\n"
"v_cvt_pk_bf16_f32 v82, v136, v138\n"
"v_cvt_pk_bf16_f32 v83, v137, v139\n"
"v_cvt_pk_bf16_f32 v84, v140, v142\n"
"v_cvt_pk_bf16_f32 v85, v141, v143\n"
"v_mov_b64 v[106:107], 0\n"
"v_mov_b64 v[108:109], 0\n"
"v_smfmac_f32_16x16x64_bf16 v[252:255], v[82:85], v[120:127], v66\n"
"s_waitcnt lgkmcnt(0)\n"
"ds_read_b64_tr_b16 v[136:137], v182 offset:40640\n"
"v_smfmac_f32_16x16x64_bf16 v[98:101], v[82:85], v[128:135], v66\n"
"ds_read_b64_tr_b16 v[138:139], v183 offset:40640\n"
"v_smfmac_f32_16x16x64_bf16 v[106:109], v[82:85], v[244:251], v66\n"
"ds_read_b64_tr_b16 v[140:141], v182 offset:41408\n"
"v_mfma_f32_16x16x32_bf16 v[82:85], v[172:175], v[82:85], 0\n"
"ds_read_b64_tr_b16 v[142:143], v183 offset:41408\n"
"ds_read_b64_tr_b16 v[120:121], v182 offset:40648\n"
"v_fmamk_f32 v112, v112, 0x3e553b95, v170\n"
"ds_read_b64_tr_b16 v[122:123], v183 offset:40648\n"
"v_fmamk_f32 v113, v113, 0x3e553b95, v170\n"
"ds_read_b64_tr_b16 v[124:125], v182 offset:41416\n"
"s_add_u32 m0, s34, 7168\n"
"ds_read_b64_tr_b16 v[126:127], v183 offset:41416\n"
"buffer_load_dwordx4 v190, s[8:11], s0 offen lds\n"
"v_fmamk_f32 v114, v114, 0x3e553b95, v170\n"
"v_fmamk_f32 v115, v115, 0x3e553b95, v170\n"
"ds_read_b64_tr_b16 v[128:129], v182 offset:40584\n"
"v_fmamk_f32 v116, v116, 0x3e553b95, v170\n"
"ds_read_b64_tr_b16 v[130:131], v183 offset:40584\n"
"v_fmamk_f32 v117, v117, 0x3e553b95, v170\n"
"ds_read_b64_tr_b16 v[132:133], v182 offset:41352\n"
"v_fmamk_f32 v118, v118, 0x3e553b95, v170\n"
"ds_read_b64_tr_b16 v[134:135], v183 offset:41352\n"
"v_fmamk_f32 v119, v119, 0x3e553b95, v170\n"
"v_exp_f32 v112, v112\n"
"v_exp_f32 v113, v113\n"
"v_exp_f32 v114, v114\n"
"v_exp_f32 v115, v115\n"
"v_exp_f32 v116, v116\n"
"v_exp_f32 v117, v117\n"
"v_exp_f32 v118, v118\n"
"v_exp_f32 v119, v119\n"
"v_cvt_pk_bf16_f32 v144, v208, v209\n"
"v_cvt_pk_bf16_f32 v145, v210, v211\n"
"v_mfma_f32_16x16x32_bf16 v[208:211], a[76:79], v[32:35], 0\n"
"s_add_u32 m0, s34, 10240\n"
"v_cvt_pk_bf16_f32 v90, v112, v114\n"
"v_cvt_pk_bf16_f32 v91, v113, v115\n"
"v_mfma_f32_16x16x32_bf16 v[208:211], a[88:91], v[36:39], v[208:211]\n"
"buffer_load_dwordx4 v191, s[8:11], s0 offen lds\n"
"v_cvt_pk_bf16_f32 v92, v116, v118\n"
"v_mfma_f32_16x16x32_bf16 v[208:211], a[100:103], v[40:43], v[208:211]\n"
"s_add_u32 s0, s0, s33\n"
"v_cvt_pk_bf16_f32 v93, v117, v119\n"
"v_cvt_pk_bf16_f32 v146, v216, v217\n"
"v_mfma_f32_16x16x32_bf16 v[208:211], a[112:115], v[44:47], v[208:211]\n"
"s_waitcnt lgkmcnt(0)\n"
"v_cvt_pk_bf16_f32 v147, v218, v219\n"
"v_cvt_pk_bf16_f32 v158, v212, v213\n"
"v_smfmac_f32_16x16x64_bf16 v[252:255], v[90:93], v[136:143], v66\n"
"v_cvt_pk_bf16_f32 v159, v214, v215\n"
"v_smfmac_f32_16x16x64_bf16 v[98:101], v[90:93], v[120:127], v66\n"
"v_cvt_pk_bf16_f32 v228, v228, v229\n"
"v_smfmac_f32_16x16x64_bf16 v[106:109], v[90:93], v[128:135], v66\n"
"v_cvt_pk_bf16_f32 v229, v230, v231\n"
"v_mfma_f32_16x16x32_bf16 v[90:93], v[172:175], v[90:93], v[82:85]\n"
"v_cvt_pk_bf16_f32 v230, v204, v205\n"
"v_cvt_pk_bf16_f32 v231, v206, v207\n"
"v_mfma_f32_16x16x32_bf16 v[216:219], a[80:83], v[32:35], 0\n"
"v_cvt_pk_bf16_f32 v252, v252, v253\n"
"v_cvt_pk_bf16_f32 v253, v254, v255\n"
"v_mfma_f32_16x16x32_bf16 v[216:219], a[92:95], v[36:39], v[216:219]\n"
"v_cvt_pk_bf16_f32 v98, v98, v99\n"
"v_cvt_pk_bf16_f32 v99, v100, v101\n"
"v_mfma_f32_16x16x32_bf16 v[216:219], a[104:107], v[40:43], v[216:219]\n"
"v_rcp_f32 v171, v90\n"
"v_mfma_f32_16x16x32_bf16 v[216:219], a[116:119], v[44:47], v[216:219]\n"
"s_add_u32 m0, s34, 8192\n"
"v_cvt_pk_bf16_f32 v171, v171, v171\n"
"v_and_b32 v64, v171, v176\n"
"v_mfma_f32_16x16x32_bf16 v[208:211], a[124:127], v[48:51], v[208:211]\n"
"buffer_load_dwordx4 v190, s[8:11], s1 offen lds\n"
"v_and_b32 v65, v171, v177\n"
"v_mfma_f32_16x16x32_bf16 v[216:219], a[128:131], v[48:51], v[216:219]\n"
"v_cvt_pk_bf16_f32 v106, v106, v107\n"
"v_cvt_pk_bf16_f32 v107, v108, v109\n"
"v_mfma_f32_16x16x16_bf16 v[252:255], v[252:253], v[64:65], 0\n"
"ds_write_b128 v187, v[228:231]\n"
"v_mfma_f32_16x16x16_bf16 v[98:101], v[98:99], v[64:65], 0\n"
"v_cvt_pk_bf16_f32 v220, v220, v221\n"
"v_cvt_pk_bf16_f32 v221, v222, v223\n"
"v_mfma_f32_16x16x16_bf16 v[106:109], v[106:107], v[64:65], 0\n"
"v_cvt_pk_bf16_f32 v224, v224, v225\n"
"v_cvt_pk_bf16_f32 v225, v226, v227\n"
"v_mfma_f32_16x16x32_bf16 v[208:211], a[136:139], v[52:55], v[208:211]\n"
"v_cvt_pk_bf16_f32 v252, v252, v253\n"
"v_cvt_pk_bf16_f32 v253, v254, v255\n"
"v_mfma_f32_16x16x32_bf16 v[216:219], a[140:143], v[52:55], v[216:219]\n"
".Lic1%=:\n"
"s_cbranch_execz .Lib0%=\n"
"v_cvt_pk_bf16_f32 v254, v98, v99\n"
"v_cvt_pk_bf16_f32 v255, v100, v101\n"
"v_mfma_f32_16x16x32_bf16 v[212:215], a[144:147], v[32:35], 0\n"
"buffer_store_dwordx4 v[252:255], v192, s[28:31], s37 offen sc1\n"
"v_mfma_f32_16x16x32_bf16 v[204:207], a[148:151], v[32:35], 0\n"
"v_cvt_pk_bf16_f32 v106, v106, v107\n"
"v_cvt_pk_bf16_f32 v107, v108, v109\n"
"v_mfma_f32_16x16x32_bf16 v[228:231], a[152:155], v[32:35], 0\n"
"s_waitcnt vmcnt(17)\n"
"ds_read_b128 v[32:35], v188\n"
"buffer_store_dwordx2 v[106:107], v193, s[28:31], s37 offen offset:1024 sc1\n"
"v_mfma_f32_16x16x32_bf16 v[212:215], a[156:159], v[36:39], v[212:215]\n"
"s_add_u32 s37, s37, 1572864\n"
"v_cvt_pk_bf16_f32 v226, v208, v209\n"
"v_cvt_pk_bf16_f32 v227, v210, v211\n"
"v_mfma_f32_16x16x32_bf16 v[204:207], a[160:163], v[36:39], v[204:207]\n"
"s_add_u32 s4, s37, 14155776\n"
"ds_write_b128 v187, v[224:227] offset:128\n"
"v_mfma_f32_16x16x32_bf16 v[228:231], a[164:167], v[36:39], v[228:231]\n"
"s_add_u32 m0, s34, 11264\n"
"v_cvt_pk_bf16_f32 v222, v216, v217\n"
"v_cvt_pk_bf16_f32 v223, v218, v219\n"
"v_mfma_f32_16x16x32_bf16 v[212:215], a[168:171], v[40:43], v[212:215]\n"
"buffer_load_dwordx4 v191, s[8:11], s1 offen lds\n"
"ds_read_b128 v[36:39], v189\n"
"v_mfma_f32_16x16x32_bf16 v[204:207], a[172:175], v[40:43], v[204:207]\n"
"s_add_u32 s1, s1, s33\n"
"ds_write_b128 v187, v[220:223] offset:64\n"
"v_mfma_f32_16x16x32_bf16 v[228:231], a[176:179], v[40:43], v[228:231]\n"
"s_waitcnt vmcnt(17) lgkmcnt(3)\n"
"v_mov_b64 v[252:253], 0\n"
"v_mov_b64 v[254:255], 0\n"
"v_mfma_f32_16x16x32_bf16 v[212:215], a[180:183], v[44:47], v[212:215]\n"
"ds_read_b128 v[40:43], v188 offset:1024\n"
"v_mfma_f32_16x16x32_bf16 v[204:207], a[184:187], v[44:47], v[204:207]\n"
"ds_read_b128 v[244:247], v178 offset:27008\n"
"ds_read_b128 v[112:115], v179 offset:27008\n"
"v_mfma_f32_16x16x32_bf16 v[228:231], a[188:191], v[44:47], v[228:231]\n"
"ds_read_b128 v[44:47], v189 offset:1024\n"
"v_mfma_f32_16x16x32_bf16 v[212:215], a[192:195], v[48:51], v[212:215]\n"
"ds_read_b128 v[248:251], v180\n"
"v_mfma_f32_16x16x32_bf16 v[204:207], a[196:199], v[48:51], v[204:207]\n"
"ds_read_b128 v[116:119], v181\n"
"v_mfma_f32_16x16x32_bf16 v[228:231], a[200:203], v[48:51], v[228:231]\n"
"s_waitcnt vmcnt(14)\n"
"ds_read_b128 v[48:51], v188 offset:2048\n"
"v_mfma_f32_16x16x32_bf16 v[212:215], a[204:207], v[52:55], v[212:215]\n"
"s_waitcnt lgkmcnt(8)\n"
"ds_read_b64 v[80:81], v20 offset:272\n"
"ds_read_b64 v[86:87], v20 offset:336\n"
"v_mfma_f32_16x16x32_bf16 v[204:207], a[208:211], v[52:55], v[204:207]\n"
"s_add_u32 m0, s34, 0\n"
"ds_read_b128 v[136:139], v180 offset:13504\n"
"buffer_load_dwordx4 v190, s[8:11], s32 offen lds\n"
"v_mfma_f32_16x16x32_bf16 v[228:231], a[212:215], v[52:55], v[228:231]\n"
"ds_read_b128 v[52:55], v189 offset:2048\n"
"v_cvt_pk_bf16_f32 v148, v212, v213\n"
"v_mfma_f32_16x16x32_bf16 v[208:211], a[0:3], v[32:35], 0\n"
"s_waitcnt lgkmcnt(9)\n"
"ds_read_b128 v[120:123], v181 offset:13504\n"
"v_cvt_pk_bf16_f32 v149, v214, v215\n"
"v_mfma_f32_16x16x32_bf16 v[216:219], a[4:7], v[32:35], 0\n"
"s_waitcnt lgkmcnt(9)\n"
"ds_read_b64 v[82:83], v178 offset:27072\n"
"v_cvt_pk_bf16_f32 v150, v204, v205\n"
"v_mfma_f32_16x16x32_bf16 v[220:223], a[8:11], v[32:35], 0\n"
"ds_read_b64 v[84:85], v179 offset:27072\n"
"v_cvt_pk_bf16_f32 v151, v206, v207\n"
"v_mfma_f32_16x16x32_bf16 v[208:211], a[12:15], v[36:39], v[208:211]\n"
"s_waitcnt lgkmcnt(9)\n"
"ds_read_b64 v[90:91], v180 offset:64\n"
"v_cvt_pk_bf16_f32 v162, v228, v229\n"
"v_mfma_f32_16x16x32_bf16 v[216:219], a[16:19], v[36:39], v[216:219]\n"
"s_waitcnt lgkmcnt(9)\n"
"ds_read_b64 v[92:93], v181 offset:64\n"
"v_cvt_pk_bf16_f32 v163, v230, v231\n"
"v_mfma_f32_16x16x32_bf16 v[220:223], a[20:23], v[36:39], v[220:223]\n"
"s_waitcnt lgkmcnt(8)\n"
"ds_read_b64 v[104:105], v20 offset:1088\n"
"ds_read_b64 v[110:111], v20 offset:1152\n"
"v_mfma_f32_16x16x32_bf16 v[208:211], a[24:27], v[40:43], v[208:211]\n"
"s_waitcnt lgkmcnt(9)\n"
"ds_read_b64 v[98:99], v180 offset:13568\n"
"v_bfi_b32 v80, v12, v80, v26\n"
"v_mfma_f32_16x16x32_bf16 v[216:219], a[28:31], v[40:43], v[216:219]\n"
"s_waitcnt lgkmcnt(9)\n"
"ds_read_b64 v[100:101], v181 offset:13568\n"
"v_bfi_b32 v81, v13, v81, v26\n"
"v_mfma_f32_16x16x32_bf16 v[220:223], a[32:35], v[40:43], v[220:223]\n"
"s_waitcnt lgkmcnt(9)\n"
"ds_read_b64_tr_b16 v[128:129], v182 offset:27136\n"
"v_bfi_b32 v86, v14, v86, v26\n"
"v_mfma_f32_16x16x32_bf16 v[208:211], a[36:39], v[44:47], v[208:211]\n"
"s_waitcnt lgkmcnt(9)\n"
"ds_read_b64_tr_b16 v[130:131], v183 offset:27136\n"
"v_bfi_b32 v87, v15, v87, v26\n"
"v_mfma_f32_16x16x32_bf16 v[216:219], a[40:43], v[44:47], v[216:219]\n"
"s_add_u32 m0, s34, 3072\n"
"ds_read_b64_tr_b16 v[132:133], v182 offset:27904\n"
"buffer_load_dwordx4 v191, s[8:11], s32 offen lds\n"
"v_mfma_f32_16x16x32_bf16 v[220:223], a[44:47], v[44:47], v[220:223]\n"
"s_add_u32 s32, s32, s33\n"
"ds_read_b64_tr_b16 v[134:135], v183 offset:27904\n"
"s_mov_b64 exec, s[44:45]\n"
"v_add_u32 v179, 0xc0, v179\n"
"s_mov_b64 exec, -1\n"
"v_mfma_f32_16x16x32_bf16 v[244:247], v[244:247], v[152:155], 0\n"
"s_waitcnt lgkmcnt(11)\n"
"v_mfma_f32_16x16x32_bf16 v[112:115], v[112:115], v[152:155], 0\n"
"s_waitcnt lgkmcnt(6)\n"
"v_bfi_b32 v104, v16, v104, v26\n"
"v_mfma_f32_16x16x32_bf16 v[248:251], v[248:251], v[152:155], 0\n"
"s_waitcnt vmcnt(9)\n"
"v_bfi_b32 v105, v17, v105, v26\n"
"v_bfi_b32 v110, v18, v110, v26\n"
"v_mfma_f32_16x16x32_bf16 v[116:119], v[116:119], v[152:155], 0\n"
"s_barrier\n"
"v_mfma_f32_16x16x32_bf16 v[208:211], a[48:51], v[48:51], v[208:211]\n"
"buffer_load_dword v201, off, s[28:31], s40 offset:128 sc1\n"
"ds_read_b128 v[140:143], v180 offset:27008\n"
"v_mfma_f32_16x16x32_bf16 v[136:139], v[136:139], v[152:155], 0\n"
"s_waitcnt lgkmcnt(6)\n"
"ds_read_b128 v[124:127], v181 offset:27008\n"
"ds_read_b64 v[106:107], v180 offset:27072\n"
"v_mfma_f32_16x16x32_bf16 v[120:123], v[120:123], v[152:155], 0\n"
"s_waitcnt lgkmcnt(7)\n"
"ds_read_b64 v[108:109], v181 offset:27072\n"
"v_bfi_b32 v111, v19, v111, v26\n"
"v_mfma_f32_16x16x32_bf16 v[244:247], v[80:83], v[164:167], v[244:247]\n"
"ds_read_b64_tr_b16 a[216:217], v236\n"
"s_add_u32 m0, s79, 142464\n"
"v_mfma_f32_16x16x32_bf16 v[112:115], v[84:87], v[166:169], v[112:115]\n"
"buffer_load_dwordx4 v192, s[16:19], s43 offen lds\n"
"v_mfma_f32_16x16x32_bf16 v[248:251], v[88:91], v[164:167], v[248:251]\n"
"ds_read_b64_tr_b16 a[218:219], v236 offset:6144\n"
"v_mfma_f32_16x16x32_bf16 v[116:119], v[92:95], v[166:169], v[116:119]\n"
"v_mfma_f32_16x16x32_bf16 v[216:219], a[52:55], v[48:51], v[216:219]\n"
"ds_read_b64_tr_b16 a[220:221], v236 offset:8\n"
"v_mfma_f32_16x16x32_bf16 v[136:139], v[96:99], v[164:167], v[136:139]\n"
"s_waitcnt lgkmcnt(6)\n"
"v_max_f32 v244, v244, v112\n"
"v_max_f32 v245, v245, v113\n"
"v_mfma_f32_16x16x32_bf16 v[140:143], v[140:143], v[152:155], 0\n"
"s_waitcnt lgkmcnt(5)\n"
"ds_read_b64_tr_b16 v[112:113], v182 offset:27144\n"
"v_max_f32 v246, v246, v114\n"
"v_mfma_f32_16x16x32_bf16 v[124:127], v[124:127], v[152:155], 0\n"
"s_waitcnt lgkmcnt(5)\n"
"v_max_f32 v247, v247, v115\n"
"v_max_f32 v248, v248, v116\n"
"v_mfma_f32_16x16x32_bf16 v[120:123], v[100:103], v[166:169], v[120:123]\n"
"s_waitcnt lgkmcnt(4)\n"
"ds_read_b64_tr_b16 v[114:115], v183 offset:27144\n"
"v_max_f32 v249, v249, v117\n"
"v_mfma_f32_16x16x32_bf16 v[140:143], v[104:107], v[164:167], v[140:143]\n"
"buffer_store_dwordx2 v[68:69], v193, s[28:31], s4 offen offset:1024 sc1\n"
"v_mfma_f32_16x16x32_bf16 v[124:127], v[108:111], v[166:169], v[124:127]\n"
"ds_read_b64_tr_b16 v[116:117], v182 offset:27912\n"
"v_max_f32 v250, v250, v118\n"
"v_mfma_f32_16x16x32_bf16 v[220:223], a[56:59], v[48:51], v[220:223]\n"
"s_add_u32 m0, s79, 142464\n"
"v_max_f32 v251, v251, v119\n"
"v_max3_f32 v170, v244, v245, v246\n"
"v_mfma_f32_16x16x32_bf16 v[208:211], a[60:63], v[52:55], v[208:211]\n"
"buffer_load_dwordx4 v192, s[16:19], s43 offen offset:1024 lds\n"
"ds_read_b64_tr_b16 v[118:119], v183 offset:27912\n"
"v_mfma_f32_16x16x32_bf16 v[216:219], a[64:67], v[52:55], v[216:219]\n"
"v_max3_f32 v170, v170, v247, v248\n"
"v_max3_f32 v170, v170, v249, v250\n"
"v_mfma_f32_16x16x32_bf16 v[220:223], a[68:71], v[52:55], v[220:223]\n"
"v_max_f32 v136, v136, v120\n"
"v_max_f32 v137, v137, v121\n"
"v_mfma_f32_16x16x32_bf16 v[224:227], a[72:75], v[32:35], 0\n"
"ds_read_b64_tr_b16 v[120:121], v182 offset:27080\n"
"v_max_f32 v138, v138, v122\n"
"v_mfma_f32_16x16x32_bf16 v[212:215], a[76:79], v[32:35], 0\n"
"v_max3_f32 v170, v170, v251, v136\n"
"v_max_f32 v139, v139, v123\n"
"v_mfma_f32_16x16x32_bf16 v[204:207], a[80:83], v[32:35], 0\n"
"ds_read_b64_tr_b16 v[122:123], v183 offset:27080\n"
"v_max3_f32 v170, v170, v137, v138\n"
"v_mfma_f32_16x16x32_bf16 v[224:227], a[84:87], v[36:39], v[224:227]\n"
"v_max_f32 v140, v140, v124\n"
"v_max_f32 v141, v141, v125\n"
"v_mfma_f32_16x16x32_bf16 v[212:215], a[88:91], v[36:39], v[212:215]\n"
"ds_read_b64_tr_b16 v[124:125], v182 offset:27848\n"
"v_max_f32 v142, v142, v126\n"
"v_mfma_f32_16x16x32_bf16 v[204:207], a[92:95], v[36:39], v[204:207]\n"
"v_max3_f32 v170, v170, v139, v140\n"
"v_max_f32 v143, v143, v127\n"
"v_mfma_f32_16x16x32_bf16 v[224:227], a[96:99], v[40:43], v[224:227]\n"
"ds_read_b64_tr_b16 v[126:127], v183 offset:27848\n"
"v_max3_f32 v170, v170, v141, v142\n"
"v_mfma_f32_16x16x32_bf16 v[212:215], a[100:103], v[40:43], v[212:215]\n"
"s_add_u32 m0, s79, 142464\n"
"v_max_f32 v171, v170, v143\n"
"v_max_f32 v170, v170, v143\n"
"v_mfma_f32_16x16x32_bf16 v[204:207], a[104:107], v[40:43], v[204:207]\n"
"buffer_load_dwordx4 v192, s[16:19], s43 offen offset:2048 lds\n"
"v_cvt_pk_bf16_f32 v208, v208, v209\n"
"v_mfma_f32_16x16x32_bf16 v[224:227], a[108:111], v[44:47], v[224:227]\n"
"s_add_u32 s43, s79, 36864\n"
"v_permlane16_swap_b32 v170, v171\n"
"v_mfma_f32_16x16x32_bf16 v[212:215], a[112:115], v[44:47], v[212:215]\n"
"v_max_f32 v170, v170, v171\n"
"v_mov_b32 v171, v170\n"
"v_mfma_f32_16x16x32_bf16 v[204:207], a[116:119], v[44:47], v[204:207]\n"
"s_nop 0\n"
"v_permlane32_swap_b32 v170, v171\n"
"v_mfma_f32_16x16x32_bf16 v[224:227], a[120:123], v[48:51], v[224:227]\n"
"v_max_f32 v170, v170, v171\n"
"v_mul_f32 v170, 0xbe553b95, v170\n"
"v_mfma_f32_16x16x32_bf16 v[212:215], a[124:127], v[48:51], v[212:215]\n"
"v_cvt_pk_bf16_f32 v209, v210, v211\n"
"v_fmamk_f32 v244, v244, 0x3e553b95, v170\n"
"v_mfma_f32_16x16x32_bf16 v[204:207], a[128:131], v[48:51], v[204:207]\n"
"v_fmamk_f32 v245, v245, 0x3e553b95, v170\n"
"v_fmamk_f32 v246, v246, 0x3e553b95, v170\n"
"v_mfma_f32_16x16x32_bf16 v[224:227], a[132:135], v[52:55], v[224:227]\n"
"v_fmamk_f32 v247, v247, 0x3e553b95, v170\n"
"v_fmamk_f32 v248, v248, 0x3e553b95, v170\n"
"v_mfma_f32_16x16x32_bf16 v[212:215], a[136:139], v[52:55], v[212:215]\n"
"v_fmamk_f32 v249, v249, 0x3e553b95, v170\n"
"v_fmamk_f32 v250, v250, 0x3e553b95, v170\n"
"v_mfma_f32_16x16x32_bf16 v[204:207], a[140:143], v[52:55], v[204:207]\n"
"v_fmamk_f32 v251, v251, 0x3e553b95, v170\n"
"v_cvt_pk_bf16_f32 v210, v216, v217\n"
"v_mfma_f32_16x16x32_bf16 v[228:231], a[144:147], v[32:35], 0\n"
"s_add_u32 m0, s34, 1024\n"
"v_exp_f32 v244, v244\n"
"v_mfma_f32_16x16x32_bf16 v[228:231], a[156:159], v[36:39], v[228:231]\n"
"buffer_load_dwordx4 v190, s[8:11], s0 offen lds\n"
"v_mov_b64 v[98:99], 0\n"
"v_mfma_f32_16x16x32_bf16 v[228:231], a[168:171], v[40:43], v[228:231]\n"
"v_exp_f32 v245, v245\n"
"v_mfma_f32_16x16x32_bf16 v[228:231], a[180:183], v[44:47], v[228:231]\n"
"v_exp_f32 v246, v246\n"
"v_mfma_f32_16x16x32_bf16 v[228:231], a[192:195], v[48:51], v[228:231]\n"
"v_exp_f32 v247, v247\n"
"v_mfma_f32_16x16x32_bf16 v[228:231], a[204:207], v[52:55], v[228:231]\n"
"v_exp_f32 v248, v248\n"
"v_exp_f32 v249, v249\n"
"v_exp_f32 v250, v250\n"
"v_exp_f32 v251, v251\n"
"v_mov_b64 v[100:101], 0\n"
"v_cvt_pk_bf16_f32 v82, v244, v246\n"
"v_cvt_pk_bf16_f32 v83, v245, v247\n"
"v_cvt_pk_bf16_f32 v84, v248, v250\n"
"v_cvt_pk_bf16_f32 v85, v249, v251\n"
"v_cvt_pk_bf16_f32 v211, v218, v219\n"
"v_mfma_f32_16x16x32_bf16 v[216:219], a[148:151], v[32:35], 0\n"
"s_waitcnt lgkmcnt(4)\n"
"ds_read_b64_tr_b16 v[244:245], v182 offset:54144\n"
"ds_read_b64_tr_b16 v[246:247], v183 offset:54144\n"
"v_smfmac_f32_16x16x64_bf16 v[252:255], v[82:85], v[128:135], v66\n"
"ds_read_b64_tr_b16 v[248:249], v182 offset:54912\n"
"v_smfmac_f32_16x16x64_bf16 v[98:101], v[82:85], v[112:119], v66\n"
"s_add_u32 m0, s34, 4096\n"
"s_nop 0\n"
"buffer_load_dwordx4 v191, s[8:11], s0 offen lds\n"
"v_mfma_f32_16x16x32_bf16 v[216:219], a[160:163], v[36:39], v[216:219]\n"
"s_add_u32 s0, s0, s33\n"
"ds_read_b64_tr_b16 v[250:251], v183 offset:54912\n"
"ds_read_b64_tr_b16 v[128:129], v182 offset:54152\n"
"v_mfma_f32_16x16x32_bf16 v[216:219], a[172:175], v[40:43], v[216:219]\n"
"ds_read_b64_tr_b16 v[130:131], v183 offset:54152\n"
"v_cvt_pk_bf16_f32 v220, v220, v221\n"
"v_mfma_f32_16x16x32_bf16 v[216:219], a[184:187], v[44:47], v[216:219]\n"
"ds_read_b64_tr_b16 v[132:133], v182 offset:54920\n"
"v_cvt_pk_bf16_f32 v221, v222, v223\n"
"v_mfma_f32_16x16x32_bf16 v[216:219], a[196:199], v[48:51], v[216:219]\n"
"ds_read_b64_tr_b16 v[134:135], v183 offset:54920\n"
"v_mov_b64 v[106:107], 0\n"
"v_mfma_f32_16x16x32_bf16 v[216:219], a[208:211], v[52:55], v[216:219]\n"
"ds_read_b64_tr_b16 v[112:113], v182 offset:54088\n"
"v_mov_b64 v[108:109], 0\n"
"v_fmamk_f32 v136, v136, 0x3e553b95, v170\n"
"v_fmamk_f32 v137, v137, 0x3e553b95, v170\n"
"v_fmamk_f32 v138, v138, 0x3e553b95, v170\n"
"v_fmamk_f32 v139, v139, 0x3e553b95, v170\n"
"v_fmamk_f32 v140, v140, 0x3e553b95, v170\n"
"v_fmamk_f32 v141, v141, 0x3e553b95, v170\n"
"v_fmamk_f32 v142, v142, 0x3e553b95, v170\n"
"v_fmamk_f32 v143, v143, 0x3e553b95, v170\n"
"v_exp_f32 v136, v136\n"
"v_exp_f32 v137, v137\n"
"v_exp_f32 v138, v138\n"
"v_exp_f32 v139, v139\n"
"v_exp_f32 v140, v140\n"
"v_exp_f32 v141, v141\n"
"v_exp_f32 v142, v142\n"
"v_exp_f32 v143, v143\n"
"v_cvt_pk_bf16_f32 v90, v136, v138\n"
"v_cvt_pk_bf16_f32 v91, v137, v139\n"
"v_cvt_pk_bf16_f32 v92, v140, v142\n"
"v_cvt_pk_bf16_f32 v93, v141, v143\n"
"v_cvt_pk_bf16_f32 v224, v224, v225\n"
"v_cvt_pk_bf16_f32 v225, v226, v227\n"
"v_cvt_pk_bf16_f32 v226, v212, v213\n"
"v_cvt_pk_bf16_f32 v227, v214, v215\n"
"v_cvt_pk_bf16_f32 v222, v204, v205\n"
"v_cvt_pk_bf16_f32 v223, v206, v207\n"
"v_cvt_pk_bf16_f32 v152, v228, v229\n"
"v_cvt_pk_bf16_f32 v153, v230, v231\n"
"v_cvt_pk_bf16_f32 v154, v216, v217\n"
"v_cvt_pk_bf16_f32 v155, v218, v219\n"
"s_waitcnt lgkmcnt(1)\n"
"v_smfmac_f32_16x16x64_bf16 v[106:109], v[82:85], v[120:127], v66\n"
"s_add_u32 m0, s34, 2048\n"
"s_nop 0\n"
"buffer_load_dwordx4 v190, s[8:11], s1 offen lds\n"
"v_mfma_f32_16x16x32_bf16 v[82:85], v[172:175], v[82:85], 0\n"
"ds_read_b64_tr_b16 v[114:115], v183 offset:54088\n"
"ds_read_b64_tr_b16 v[116:117], v182 offset:54856\n"
"v_smfmac_f32_16x16x64_bf16 v[252:255], v[90:93], v[244:251], v66\n"
"ds_read_b64_tr_b16 v[118:119], v183 offset:54856\n"
"v_smfmac_f32_16x16x64_bf16 v[98:101], v[90:93], v[128:135], v66\n"
"ds_write_b128 v186, v[208:211] offset:6752\n"
"v_mfma_f32_16x16x32_bf16 v[208:211], a[152:155], v[32:35], 0\n"
"s_waitcnt vmcnt(16)\n"
"v_mfma_f32_16x16x32_bf16 v[208:211], a[164:167], v[36:39], v[208:211]\n"
"ds_read_b128 v[32:35], v188 offset:6144\n"
"v_mfma_f32_16x16x32_bf16 v[208:211], a[176:179], v[40:43], v[208:211]\n"
"s_waitcnt vmcnt(14)\n"
"ds_read_b128 v[36:39], v189 offset:6144\n"
"ds_read_b128 v[40:43], v188 offset:7168\n"
"v_mfma_f32_16x16x32_bf16 v[208:211], a[188:191], v[44:47], v[208:211]\n"
"ds_read_b128 v[44:47], v189 offset:7168\n"
"v_cvt_pk_bf16_f32 v252, v252, v253\n"
"v_mfma_f32_16x16x32_bf16 v[208:211], a[200:203], v[48:51], v[208:211]\n"
"s_waitcnt vmcnt(10)\n"
"ds_read_b128 v[48:51], v188 offset:8192\n"
"v_cvt_pk_bf16_f32 v253, v254, v255\n"
"v_mfma_f32_16x16x32_bf16 v[208:211], a[212:215], v[52:55], v[208:211]\n"
"s_add_u32 m0, s34, 5120\n"
"ds_read_b128 v[52:55], v189 offset:8192\n"
"buffer_load_dwordx4 v191, s[8:11], s1 offen lds\n"
"v_cvt_pk_bf16_f32 v98, v98, v99\n"
"v_cvt_pk_bf16_f32 v99, v100, v101\n"
"s_waitcnt lgkmcnt(7)\n"
"v_smfmac_f32_16x16x64_bf16 v[106:109], v[90:93], v[112:119], v66\n"
"s_add_u32 s1, s1, s33\n"
"ds_write_b128 v186, v[220:223] offset:6816\n"
"v_mfma_f32_16x16x32_bf16 v[90:93], v[172:175], v[90:93], v[82:85]\n"
"s_waitcnt lgkmcnt(6)\n"
"v_cvt_pk_bf16_f32 v166, v208, v209\n"
"v_cvt_pk_bf16_f32 v167, v210, v211\n"
"v_mfma_f32_16x16x32_bf16 v[212:215], a[0:3], v[32:35], 0\n"
"s_waitcnt lgkmcnt(5)\n"
"v_cvt_pk_bf16_f32 v106, v106, v107\n"
"v_cvt_pk_bf16_f32 v107, v108, v109\n"
"v_mfma_f32_16x16x32_bf16 v[204:207], a[4:7], v[32:35], 0\n"
"v_rcp_f32 v171, v90\n"
"v_mfma_f32_16x16x32_bf16 v[220:223], a[8:11], v[32:35], 0\n"
"ds_write_b128 v186, v[224:227] offset:6880\n"
"v_mfma_f32_16x16x32_bf16 v[212:215], a[12:15], v[36:39], v[212:215]\n"
"s_waitcnt lgkmcnt(5)\n"
"v_cvt_pk_bf16_f32 v171, v171, v171\n"
"v_and_b32 v64, v171, v176\n"
"v_mfma_f32_16x16x32_bf16 v[204:207], a[16:19], v[36:39], v[204:207]\n"
"v_and_b32 v65, v171, v177\n"
"s_add_u32 m0, s34, 6144\n"
"v_mfma_f32_16x16x32_bf16 v[220:223], a[20:23], v[36:39], v[220:223]\n"
"buffer_load_dwordx4 v190, s[8:11], s32 offen lds\n"
"ds_read_b64_tr_b16 a[222:223], v236 offset:6152\n"
"v_mfma_f32_16x16x16_bf16 v[252:255], v[252:253], v[64:65], 0\n"
"ds_read_b64_tr_b16 a[224:225], v235\n"
"v_mfma_f32_16x16x16_bf16 v[98:101], v[98:99], v[64:65], 0\n"
"v_mfma_f32_16x16x16_bf16 v[106:109], v[106:107], v[64:65], 0\n"
"ds_read_b64_tr_b16 a[226:227], v235 offset:6144\n"
"v_mfma_f32_16x16x32_bf16 v[212:215], a[24:27], v[40:43], v[212:215]\n"
"s_waitcnt lgkmcnt(7)\n"
"v_mfma_f32_16x16x32_bf16 v[204:207], a[28:31], v[40:43], v[204:207]\n"
"ds_read_b128 v[120:123], v180\n"
"v_cvt_pk_bf16_f32 v252, v252, v253\n"
"v_mfma_f32_16x16x32_bf16 v[220:223], a[32:35], v[40:43], v[220:223]\n"
"v_cvt_pk_bf16_f32 v253, v254, v255\n"
"v_cvt_pk_bf16_f32 v254, v98, v99\n"
"v_mfma_f32_16x16x32_bf16 v[212:215], a[36:39], v[44:47], v[212:215]\n"
"s_waitcnt lgkmcnt(7)\n"
"v_cvt_pk_bf16_f32 v255, v100, v101\n"
"v_cvt_pk_bf16_f32 v106, v106, v107\n"
"v_mfma_f32_16x16x32_bf16 v[204:207], a[40:43], v[44:47], v[204:207]\n"
"buffer_store_dwordx4 v[252:255], v192, s[28:31], s37 offen sc1\n"
"v_mfma_f32_16x16x32_bf16 v[220:223], a[44:47], v[44:47], v[220:223]\n"
"s_waitcnt vmcnt(10) lgkmcnt(6)\n"
"v_cvt_pk_bf16_f32 v107, v108, v109\n"
"v_mov_b64 v[252:253], 0\n"
"v_mfma_f32_16x16x32_bf16 v[212:215], a[48:51], v[48:51], v[212:215]\n"
"s_add_u32 m0, s34, 9216\n"
"v_readfirstlane_b32 s6, v201\n"
"v_mov_b64 v[254:255], 0\n"
"v_mfma_f32_16x16x32_bf16 v[204:207], a[52:55], v[48:51], v[204:207]\n"
"s_and_b32 s6, s6, 0xffffff00\n"
"buffer_load_dwordx4 v191, s[8:11], s32 offen lds\n"
"v_or_b32 v201, 2, s6\n"
"v_mfma_f32_16x16x32_bf16 v[220:223], a[56:59], v[48:51], v[220:223]\n"
"s_add_u32 s32, s32, s33\n"
"ds_read_b128 v[136:139], v181\n"
"ds_read_b128 v[124:127], v180 offset:13504\n"
"v_mfma_f32_16x16x32_bf16 v[212:215], a[60:63], v[52:55], v[212:215]\n"
"s_waitcnt lgkmcnt(2)\n"
"ds_read_b128 v[140:143], v181 offset:13504\n"
"v_mfma_f32_16x16x32_bf16 v[204:207], a[64:67], v[52:55], v[204:207]\n"
"ds_read_b128 v[244:247], v180 offset:27008\n"
"s_mov_b64 exec, 1\n"
"buffer_store_dword v201, off, s[28:31], s40 sc1\n"
"s_mov_b64 exec, -1\n"
"v_mfma_f32_16x16x32_bf16 v[220:223], a[68:71], v[52:55], v[220:223]\n"
"ds_read_b128 v[128:131], v181 offset:27008\n"
"ds_read_b64 v[82:83], v180 offset:64\n"
"v_mfma_f32_16x16x32_bf16 v[120:123], v[120:123], v[144:147], 0\n"
"buffer_store_dwordx2 v[106:107], v193, s[28:31], s37 offen offset:1024 sc1\n"
"v_mfma_f32_16x16x32_bf16 v[224:227], a[72:75], v[32:35], 0\n"
"s_add_u32 s37, s37, 1572864\n"
"ds_read_b64 v[84:85], v181 offset:64\n"
"ds_read_b64 v[90:91], v180 offset:13568\n"
"v_mfma_f32_16x16x32_bf16 v[228:231], a[76:79], v[32:35], 0\n"
"s_add_u32 s4, s37, 14155776\n"
"ds_read_b64 v[92:93], v181 offset:13568\n"
"v_cvt_pk_bf16_f32 v212, v212, v213\n"
"v_mfma_f32_16x16x32_bf16 v[216:219], a[80:83], v[32:35], 0\n"
"s_waitcnt lgkmcnt(8)\n"
"ds_read_b64 v[98:99], v180 offset:27072\n"
"v_cvt_pk_bf16_f32 v213, v214, v215\n"
"v_mfma_f32_16x16x32_bf16 v[136:139], v[136:139], v[144:147], 0\n"
"s_waitcnt vmcnt(9) lgkmcnt(8)\n"
"ds_read_b64 v[100:101], v181 offset:27072\n"
"v_cvt_pk_bf16_f32 v214, v204, v205\n"
"v_mfma_f32_16x16x32_bf16 v[124:127], v[124:127], v[144:147], 0\n"
"s_waitcnt lgkmcnt(8)\n"
"ds_read_b64_tr_b16 v[112:113], v182 offset:40640\n"
"v_cvt_pk_bf16_f32 v215, v206, v207\n"
"v_mfma_f32_16x16x32_bf16 v[140:143], v[140:143], v[144:147], 0\n"
"s_waitcnt lgkmcnt(8)\n"
"ds_read_b64_tr_b16 v[114:115], v183 offset:40640\n"
"v_cvt_pk_bf16_f32 v220, v220, v221\n"
"v_mfma_f32_16x16x32_bf16 v[244:247], v[244:247], v[144:147], 0\n"
"s_waitcnt lgkmcnt(8)\n"
"ds_read_b64_tr_b16 v[116:117], v182 offset:41408\n"
"v_cvt_pk_bf16_f32 v221, v222, v223\n"
"v_mfma_f32_16x16x32_bf16 v[128:131], v[128:131], v[144:147], 0\n"
"s_waitcnt lgkmcnt(8)\n"
"ds_read_b64_tr_b16 v[118:119], v183 offset:41408\n"
"s_add_u32 m0, s34, 7168\n"
"v_mfma_f32_16x16x32_bf16 v[120:123], v[80:83], v[156:159], v[120:123]\n"
"s_waitcnt lgkmcnt(8)\n"
"buffer_load_dwordx4 v190, s[8:11], s0 offen lds\n"
"s_barrier\n"
"v_mfma_f32_16x16x32_bf16 v[136:139], v[84:87], v[158:161], v[136:139]\n"
"s_waitcnt lgkmcnt(6)\n"
"ds_read_b128 v[248:251], v178\n"
"ds_read_b128 v[132:135], v179\n"
"v_mfma_f32_16x16x32_bf16 v[124:127], v[88:91], v[156:159], v[124:127]\n"
"s_waitcnt lgkmcnt(7)\n"
"ds_read_b64 v[106:107], v178 offset:64\n"
"v_mfma_f32_16x16x32_bf16 v[140:143], v[92:95], v[158:161], v[140:143]\n"
"s_waitcnt lgkmcnt(7)\n"
"ds_read_b64 v[108:109], v179 offset:64\n"
"v_max_f32 v120, v120, v136\n"
"v_mfma_f32_16x16x32_bf16 v[244:247], v[96:99], v[156:159], v[244:247]\n"
"ds_write_b128 v186, v[212:215] offset:20256\n"
"v_mfma_f32_16x16x32_bf16 v[128:131], v[100:103], v[158:161], v[128:131]\n"
"v_max_f32 v121, v121, v137\n"
"v_max_f32 v122, v122, v138\n"
"v_mfma_f32_16x16x32_bf16 v[224:227], a[84:87], v[36:39], v[224:227]\n"
"v_max_f32 v123, v123, v139\n"
"v_max_f32 v124, v124, v140\n"
"v_mfma_f32_16x16x32_bf16 v[228:231], a[88:91], v[36:39], v[228:231]\n"
"ds_read_b64_tr_b16 v[136:137], v182 offset:40648\n"
"ds_read_b64_tr_b16 v[138:139], v183 offset:40648\n"
"v_mfma_f32_16x16x32_bf16 v[216:219], a[92:95], v[36:39], v[216:219]\n"
"s_waitcnt lgkmcnt(6)\n"
"ds_read_b64_tr_b16 a[228:229], v236 offset:12288\n"
"v_max_f32 v125, v125, v141\n"
"v_mfma_f32_16x16x32_bf16 v[248:251], v[248:251], v[144:147], 0\n"
"s_waitcnt lgkmcnt(6)\n"
"v_max_f32 v126, v126, v142\n"
"v_max_f32 v127, v127, v143\n"
"v_mfma_f32_16x16x32_bf16 v[132:135], v[132:135], v[144:147], 0\n"
"s_waitcnt lgkmcnt(5)\n"
"ds_read_b64_tr_b16 v[140:141], v182 offset:41416\n"
"v_max3_f32 v170, v120, v121, v122\n"
"v_mfma_f32_16x16x32_bf16 v[248:251], v[104:107], v[156:159], v[248:251]\n"
"s_waitcnt lgkmcnt(5)\n"
"ds_read_b64_tr_b16 v[142:143], v183 offset:41416\n"
"v_max3_f32 v170, v170, v123, v124\n"
"v_mfma_f32_16x16x32_bf16 v[132:135], v[108:111], v[158:161], v[132:135]\n"
"s_add_u32 m0, s79, 130176\n"
"ds_read_b64_tr_b16 a[230:231], v236 offset:18432\n"
"buffer_load_dwordx4 v192, s[16:19], s43 offen lds\n"
"v_mfma_f32_16x16x32_bf16 v[224:227], a[96:99], v[40:43], v[224:227]\n"
"v_max3_f32 v170, v170, v125, v126\n"
"v_max_f32 v244, v244, v128\n"
"v_mfma_f32_16x16x32_bf16 v[228:231], a[100:103], v[40:43], v[228:231]\n"
"v_max_f32 v245, v245, v129\n"
"v_max_f32 v246, v246, v130\n"
"v_mfma_f32_16x16x32_bf16 v[216:219], a[104:107], v[40:43], v[216:219]\n"
"ds_read_b64_tr_b16 v[128:129], v182 offset:40584\n"
"v_max3_f32 v170, v170, v127, v244\n"
"v_mfma_f32_16x16x32_bf16 v[224:227], a[108:111], v[44:47], v[224:227]\n"
"v_max_f32 v247, v247, v131\n"
"v_max3_f32 v170, v170, v245, v246\n"
"v_mfma_f32_16x16x32_bf16 v[228:231], a[112:115], v[44:47], v[228:231]\n"
"ds_read_b64_tr_b16 v[130:131], v183 offset:40584\n"
"v_max_f32 v248, v248, v132\n"
"v_mfma_f32_16x16x32_bf16 v[216:219], a[116:119], v[44:47], v[216:219]\n"
"v_max_f32 v249, v249, v133\n"
"v_max_f32 v250, v250, v134\n"
"v_mfma_f32_16x16x32_bf16 v[224:227], a[120:123], v[48:51], v[224:227]\n"
"ds_read_b64_tr_b16 v[132:133], v182 offset:41352\n"
"v_max3_f32 v170, v170, v247, v248\n"
"v_mfma_f32_16x16x32_bf16 v[228:231], a[124:127], v[48:51], v[228:231]\n"
"v_max_f32 v251, v251, v135\n"
"v_max3_f32 v170, v170, v249, v250\n"
"v_mfma_f32_16x16x32_bf16 v[216:219], a[128:131], v[48:51], v[216:219]\n"
"ds_read_b64_tr_b16 v[134:135], v183 offset:41352\n"
"v_max_f32 v171, v170, v251\n"
"v_mfma_f32_16x16x32_bf16 v[224:227], a[132:135], v[52:55], v[224:227]\n"
"s_add_u32 m0, s79, 130176\n"
"v_max_f32 v170, v170, v251\n"
"v_mov_b64 v[98:99], 0\n"
"v_mfma_f32_16x16x32_bf16 v[228:231], a[136:139], v[52:55], v[228:231]\n"
"buffer_load_dwordx4 v192, s[16:19], s43 offen offset:1024 lds\n"
"ds_read_b64_tr_b16 a[232:233], v236 offset:12296\n"
"v_mfma_f32_16x16x32_bf16 v[216:219], a[140:143], v[52:55], v[216:219]\n"
"v_permlane16_swap_b32 v170, v171\n"
"v_mfma_f32_16x16x32_bf16 v[208:211], a[144:147], v[32:35], 0\n"
"v_max_f32 v170, v170, v171\n"
"v_mov_b32 v171, v170\n"
"v_mfma_f32_16x16x32_bf16 v[204:207], a[148:151], v[32:35], 0\n"
"s_nop 0\n"
"v_permlane32_swap_b32 v170, v171\n"
"v_mfma_f32_16x16x32_bf16 v[212:215], a[152:155], v[32:35], 0\n"
"ds_read_b128 v[32:35], v188\n"
"v_max_f32 v170, v170, v171\n"
"v_mfma_f32_16x16x32_bf16 v[208:211], a[156:159], v[36:39], v[208:211]\n"
"v_mul_f32 v170, 0xbe553b95, v170\n"
"v_fmamk_f32 v120, v120, 0x3e553b95, v170\n"
"v_mfma_f32_16x16x32_bf16 v[204:207], a[160:163], v[36:39], v[204:207]\n"
"v_fmamk_f32 v121, v121, 0x3e553b95, v170\n"
"v_fmamk_f32 v122, v122, 0x3e553b95, v170\n"
"v_mfma_f32_16x16x32_bf16 v[212:215], a[164:167], v[36:39], v[212:215]\n"
"ds_read_b128 v[36:39], v189\n"
"v_fmamk_f32 v123, v123, 0x3e553b95, v170\n"
"v_mfma_f32_16x16x32_bf16 v[208:211], a[168:171], v[40:43], v[208:211]\n"
"v_fmamk_f32 v124, v124, 0x3e553b95, v170\n"
"v_fmamk_f32 v125, v125, 0x3e553b95, v170\n"
"v_mfma_f32_16x16x32_bf16 v[204:207], a[172:175], v[40:43], v[204:207]\n"
"v_fmamk_f32 v126, v126, 0x3e553b95, v170\n"
"v_fmamk_f32 v127, v127, 0x3e553b95, v170\n"
"v_mfma_f32_16x16x32_bf16 v[212:215], a[176:179], v[40:43], v[212:215]\n"
"s_add_u32 m0, s79, 130176\n"
"v_exp_f32 v120, v120\n"
"v_mfma_f32_16x16x32_bf16 v[208:211], a[180:183], v[44:47], v[208:211]\n"
"buffer_load_dwordx4 v192, s[16:19], s43 offen offset:2048 lds\n"
"v_mov_b64 v[100:101], 0\n"
"v_mfma_f32_16x16x32_bf16 v[204:207], a[184:187], v[44:47], v[204:207]\n"
"s_add_u32 s43, s79, 55296\n"
"v_exp_f32 v121, v121\n"
"v_mfma_f32_16x16x32_bf16 v[212:215], a[188:191], v[44:47], v[212:215]\n"
"s_waitcnt vmcnt(11)\n"
"ds_read_b128 v[40:43], v188 offset:1024\n"
"ds_read_b128 v[44:47], v189 offset:1024\n"
"v_mfma_f32_16x16x32_bf16 v[208:211], a[192:195], v[48:51], v[208:211]\n"
"buffer_store_dwordx4 v[68:71], v192, s[28:31], s4 offen sc1\n"
"v_mfma_f32_16x16x32_bf16 v[204:207], a[196:199], v[48:51], v[204:207]\n"
"v_exp_f32 v122, v122\n"
"v_mfma_f32_16x16x32_bf16 v[212:215], a[200:203], v[48:51], v[212:215]\n"
"v_exp_f32 v123, v123\n"
"v_mfma_f32_16x16x32_bf16 v[208:211], a[204:207], v[52:55], v[208:211]\n"
"v_exp_f32 v124, v124\n"
"v_mfma_f32_16x16x32_bf16 v[204:207], a[208:211], v[52:55], v[204:207]\n"
"v_exp_f32 v125, v125\n"
"v_mfma_f32_16x16x32_bf16 v[212:215], a[212:215], v[52:55], v[212:215]\n"
"v_exp_f32 v126, v126\n"
"v_exp_f32 v127, v127\n"
"s_add_u32 m0, s34, 10240\n"
"v_cvt_pk_bf16_f32 v82, v120, v122\n"
"buffer_load_dwordx4 v191, s[8:11], s0 offen lds\n"
"s_add_u32 s0, s0, s33\n"
"v_cvt_pk_bf16_f32 v83, v121, v123\n"
"v_cvt_pk_bf16_f32 v84, v124, v126\n"
"v_cvt_pk_bf16_f32 v85, v125, v127\n"
"v_mov_b64 v[106:107], 0\n"
"v_mov_b64 v[108:109], 0\n"
"v_smfmac_f32_16x16x64_bf16 v[252:255], v[82:85], v[112:119], v66\n"
"s_waitcnt lgkmcnt(5)\n"
"ds_read_b64_tr_b16 v[120:121], v184 offset:128\n"
"v_smfmac_f32_16x16x64_bf16 v[98:101], v[82:85], v[136:143], v66\n"
"ds_read_b64_tr_b16 v[122:123], v185 offset:128\n"
".Lic2%=:\n"
"s_cbranch_execz .Lib0%=\n"
"v_smfmac_f32_16x16x64_bf16 v[106:109], v[82:85], v[128:135], v66\n"
"ds_read_b64_tr_b16 v[124:125], v184 offset:896\n"
"v_mfma_f32_16x16x32_bf16 v[82:85], v[172:175], v[82:85], 0\n"
"ds_read_b64_tr_b16 v[126:127], v185 offset:896\n"
"ds_read_b64_tr_b16 v[112:113], v184 offset:136\n"
"v_fmamk_f32 v244, v244, 0x3e553b95, v170\n"
"ds_read_b64_tr_b16 v[114:115], v185 offset:136\n"
"v_fmamk_f32 v245, v245, 0x3e553b95, v170\n"
"ds_read_b64_tr_b16 v[116:117], v184 offset:904\n"
"v_fmamk_f32 v246, v246, 0x3e553b95, v170\n"
"ds_read_b64_tr_b16 v[118:119], v185 offset:904\n"
"v_fmamk_f32 v247, v247, 0x3e553b95, v170\n"
"ds_read_b64_tr_b16 v[136:137], v184 offset:72\n"
"s_add_u32 m0, s34, 8192\n"
"ds_read_b64_tr_b16 v[138:139], v185 offset:72\n"
"buffer_load_dwordx4 v190, s[8:11], s1 offen lds\n"
"v_fmamk_f32 v248, v248, 0x3e553b95, v170\n"
"v_fmamk_f32 v249, v249, 0x3e553b95, v170\n"
"ds_read_b64_tr_b16 v[140:141], v184 offset:840\n"
"v_fmamk_f32 v250, v250, 0x3e553b95, v170\n"
"ds_read_b64_tr_b16 v[142:143], v185 offset:840\n"
"v_fmamk_f32 v251, v251, 0x3e553b95, v170\n"
"v_exp_f32 v244, v244\n"
"v_exp_f32 v245, v245\n"
"v_exp_f32 v246, v246\n"
"v_exp_f32 v247, v247\n"
"v_exp_f32 v248, v248\n"
"v_exp_f32 v249, v249\n"
"v_exp_f32 v250, v250\n"
"v_exp_f32 v251, v251\n"
"s_waitcnt vmcnt(12)\n"
"ds_read_b128 v[48:51], v188 offset:2048\n"
"ds_read_b128 v[52:55], v189 offset:2048\n"
"v_cvt_pk_bf16_f32 v90, v244, v246\n"
"v_cvt_pk_bf16_f32 v91, v245, v247\n"
"v_cvt_pk_bf16_f32 v92, v248, v250\n"
"v_cvt_pk_bf16_f32 v93, v249, v251\n"
"v_cvt_pk_bf16_f32 v224, v224, v225\n"
"v_cvt_pk_bf16_f32 v225, v226, v227\n"
"v_cvt_pk_bf16_f32 v226, v228, v229\n"
"v_cvt_pk_bf16_f32 v227, v230, v231\n"
"v_cvt_pk_bf16_f32 v222, v216, v217\n"
"v_cvt_pk_bf16_f32 v223, v218, v219\n"
"v_cvt_pk_bf16_f32 v144, v208, v209\n"
"v_cvt_pk_bf16_f32 v145, v210, v211\n"
"v_cvt_pk_bf16_f32 v146, v204, v205\n"
"v_cvt_pk_bf16_f32 v147, v206, v207\n"
"v_cvt_pk_bf16_f32 v158, v212, v213\n"
"v_cvt_pk_bf16_f32 v159, v214, v215\n"
"s_waitcnt lgkmcnt(2)\n"
"v_smfmac_f32_16x16x64_bf16 v[252:255], v[90:93], v[120:127], v66\n"
"s_add_u32 m0, s34, 11264\n"
"s_nop 0\n"
"buffer_load_dwordx4 v191, s[8:11], s1 offen lds\n"
"v_smfmac_f32_16x16x64_bf16 v[98:101], v[90:93], v[112:119], v66\n"
"s_add_u32 s1, s1, s33\n"
"ds_write_b128 v186, v[220:223] offset:20320\n"
"v_smfmac_f32_16x16x64_bf16 v[106:109], v[90:93], v[136:143], v66\n"
"v_mfma_f32_16x16x32_bf16 v[90:93], v[172:175], v[90:93], v[82:85]\n"
"v_cvt_pk_bf16_f32 v252, v252, v253\n"
"v_mfma_f32_16x16x32_bf16 v[228:231], a[0:3], v[32:35], 0\n"
"ds_write_b128 v186, v[224:227] offset:20384\n"
"v_mfma_f32_16x16x32_bf16 v[216:219], a[4:7], v[32:35], 0\n"
"v_cvt_pk_bf16_f32 v253, v254, v255\n"
"v_cvt_pk_bf16_f32 v98, v98, v99\n"
"v_mfma_f32_16x16x32_bf16 v[220:223], a[8:11], v[32:35], 0\n"
"s_nop 0\n"
"v_rcp_f32 v171, v90\n"
"v_mfma_f32_16x16x32_bf16 v[228:231], a[12:15], v[36:39], v[228:231]\n"
"ds_read_b64_tr_b16 a[234:235], v236 offset:18440\n"
"v_cvt_pk_bf16_f32 v171, v171, v171\n"
"v_mfma_f32_16x16x32_bf16 v[216:219], a[16:19], v[36:39], v[216:219]\n"
"v_and_b32 v64, v171, v176\n"
"v_and_b32 v65, v171, v177\n"
"v_mfma_f32_16x16x32_bf16 v[220:223], a[20:23], v[36:39], v[220:223]\n"
"s_waitcnt lgkmcnt(3)\n"
"v_cvt_pk_bf16_f32 v99, v100, v101\n"
"v_cvt_pk_bf16_f32 v106, v106, v107\n"
"v_mfma_f32_16x16x16_bf16 v[252:255], v[252:253], v[64:65], 0\n"
"s_add_u32 m0, s34, 0\n"
"v_cvt_pk_bf16_f32 v107, v108, v109\n"
"v_mfma_f32_16x16x16_bf16 v[98:101], v[98:99], v[64:65], 0\n"
"buffer_load_dwordx4 v190, s[8:11], s32 offen lds\n"
"ds_read_b64_tr_b16 a[236:237], v235 offset:12288\n"
"v_mfma_f32_16x16x16_bf16 v[106:109], v[106:107], v[64:65], 0\n"
"ds_read_b64_tr_b16 a[238:239], v235 offset:18432\n"
"v_mfma_f32_16x16x32_bf16 v[228:231], a[24:27], v[40:43], v[228:231]\n"
"v_cvt_pk_bf16_f32 v252, v252, v253\n"
"v_cvt_pk_bf16_f32 v253, v254, v255\n"
"v_mfma_f32_16x16x32_bf16 v[216:219], a[28:31], v[40:43], v[216:219]\n"
"buffer_load_dword v200, off, s[28:31], s39 sc1\n"
"v_cvt_pk_bf16_f32 v254, v98, v99\n"
"v_mfma_f32_16x16x32_bf16 v[220:223], a[32:35], v[40:43], v[220:223]\n"
"v_cvt_pk_bf16_f32 v255, v100, v101\n"
"v_cvt_pk_bf16_f32 v106, v106, v107\n"
"v_mfma_f32_16x16x32_bf16 v[228:231], a[36:39], v[44:47], v[228:231]\n"
"buffer_store_dwordx4 v[252:255], v192, s[28:31], s37 offen sc1\n"
"v_mfma_f32_16x16x32_bf16 v[216:219], a[40:43], v[44:47], v[216:219]\n"
"v_cvt_pk_bf16_f32 v107, v108, v109\n"
"v_mov_b64 v[252:253], 0\n"
"v_mfma_f32_16x16x32_bf16 v[220:223], a[44:47], v[44:47], v[220:223]\n"
"ds_read_b128 v[128:131], v180 offset:13504\n"
"ds_read_b128 v[244:247], v181 offset:13504\n"
"v_mfma_f32_16x16x32_bf16 v[228:231], a[48:51], v[48:51], v[228:231]\n"
"buffer_store_dwordx2 v[106:107], v193, s[28:31], s37 offen offset:1024 sc1\n"
"v_mfma_f32_16x16x32_bf16 v[216:219], a[52:55], v[48:51], v[216:219]\n"
"s_add_u32 s37, s37, 1572864\n"
"ds_read_b128 v[132:135], v180 offset:27008\n"
"ds_read_b128 v[248:251], v181 offset:27008\n"
"v_mfma_f32_16x16x32_bf16 v[220:223], a[56:59], v[48:51], v[220:223]\n"
"s_add_u32 s4, s37, 12582912\n"
"ds_read_b128 v[120:123], v178\n"
"v_mov_b64 v[254:255], 0\n"
"v_mfma_f32_16x16x32_bf16 v[228:231], a[60:63], v[52:55], v[228:231]\n"
"s_add_u32 m0, s34, 3072\n"
"ds_read_b128 v[112:115], v179\n"
"v_mfma_f32_16x16x32_bf16 v[216:219], a[64:67], v[52:55], v[216:219]\n"
"buffer_load_dwordx4 v191, s[8:11], s32 offen lds\n"
"ds_read_b64 v[82:83], v180 offset:13568\n"
"v_mfma_f32_16x16x32_bf16 v[220:223], a[68:71], v[52:55], v[220:223]\n"
"s_add_u32 s32, s32, s33\n"
"ds_read_b64 v[84:85], v181 offset:13568\n"
"v_cvt_pk_bf16_f32 v228, v228, v229\n"
"v_mfma_f32_16x16x32_bf16 v[224:227], a[72:75], v[32:35], 0\n"
"s_waitcnt lgkmcnt(7)\n"
"ds_read_b64 v[90:91], v180 offset:27072\n"
"v_cvt_pk_bf16_f32 v229, v230, v231\n"
"v_mfma_f32_16x16x32_bf16 v[208:211], a[76:79], v[32:35], 0\n"
"s_waitcnt lgkmcnt(7)\n"
"ds_read_b64 v[92:93], v181 offset:27072\n"
"v_cvt_pk_bf16_f32 v230, v216, v217\n"
"v_mfma_f32_16x16x32_bf16 v[128:131], v[128:131], v[148:151], 0\n"
"ds_read_b64 v[98:99], v178 offset:64\n"
"v_cvt_pk_bf16_f32 v231, v218, v219\n"
"v_mfma_f32_16x16x32_bf16 v[244:247], v[244:247], v[148:151], 0\n"
"s_waitcnt vmcnt(9) lgkmcnt(8)\n"
"ds_read_b64 v[100:101], v179 offset:64\n"
"v_cvt_pk_bf16_f32 v220, v220, v221\n"
"v_mfma_f32_16x16x32_bf16 v[132:135], v[132:135], v[148:151], 0\n"
"s_waitcnt lgkmcnt(8)\n"
"ds_read_b64_tr_b16 v[136:137], v182 offset:54144\n"
"v_cvt_pk_bf16_f32 v221, v222, v223\n"
"v_mfma_f32_16x16x32_bf16 v[248:251], v[248:251], v[148:151], 0\n"
"s_waitcnt lgkmcnt(8)\n"
"ds_read_b64_tr_b16 v[138:139], v183 offset:54144\n"
"s_barrier\n"
"v_mfma_f32_16x16x32_bf16 v[120:123], v[120:123], v[148:151], 0\n"
"s_waitcnt lgkmcnt(7)\n"
"ds_read_b128 v[124:127], v178 offset:13504\n"
"ds_read_b128 v[116:119], v179 offset:13504\n"
"v_mfma_f32_16x16x32_bf16 v[112:115], v[112:115], v[148:151], 0\n"
"s_waitcnt lgkmcnt(8)\n"
"ds_read_b64 v[106:107], v178 offset:13568\n"
"s_add_u32 m0, s79, 142464\n"
"v_mfma_f32_16x16x32_bf16 v[128:131], v[80:83], v[160:163], v[128:131]\n"
"s_waitcnt lgkmcnt(8)\n"
"buffer_load_dwordx4 v192, s[16:19], s43 offen lds\n"
"ds_read_b64 v[108:109], v179 offset:13568\n"
"v_mfma_f32_16x16x32_bf16 v[244:247], v[84:87], v[162:165], v[244:247]\n"
"s_waitcnt lgkmcnt(8)\n"
"ds_read_b64_tr_b16 v[140:141], v182 offset:54912\n"
"v_mfma_f32_16x16x32_bf16 v[132:135], v[88:91], v[160:163], v[132:135]\n"
"s_waitcnt lgkmcnt(8)\n"
"ds_read_b64_tr_b16 v[142:143], v183 offset:54912\n"
"v_mfma_f32_16x16x32_bf16 v[248:251], v[92:95], v[162:165], v[248:251]\n"
"s_waitcnt lgkmcnt(8)\n"
"ds_write_b128 v186, v[228:231] offset:33760\n"
"v_mfma_f32_16x16x32_bf16 v[120:123], v[96:99], v[160:163], v[120:123]\n"
"v_max_f32 v128, v128, v244\n"
"v_max_f32 v129, v129, v245\n"
"v_mfma_f32_16x16x32_bf16 v[112:115], v[100:103], v[162:165], v[112:115]\n"
"s_waitcnt lgkmcnt(6)\n"
"v_max_f32 v130, v130, v246\n"
"v_max_f32 v131, v131, v247\n"
"v_mfma_f32_16x16x32_bf16 v[204:207], a[80:83], v[32:35], 0\n"
"s_waitcnt lgkmcnt(5)\n"
"ds_read_b64_tr_b16 v[244:245], v182 offset:54152\n"
"ds_read_b64_tr_b16 v[246:247], v183 offset:54152\n"
"v_mfma_f32_16x16x32_bf16 v[124:127], v[124:127], v[148:151], 0\n"
"s_waitcnt lgkmcnt(6)\n"
"ds_read_b64_tr_b16 a[240:241], v236\n"
"v_max_f32 v132, v132, v248\n"
"v_mfma_f32_16x16x32_bf16 v[116:119], v[116:119], v[148:151], 0\n"
"s_waitcnt lgkmcnt(6)\n"
"v_max_f32 v133, v133, v249\n"
"v_max_f32 v134, v134, v250\n"
"v_mfma_f32_16x16x32_bf16 v[124:127], v[104:107], v[160:163], v[124:127]\n"
"s_add_u32 m0, s79, 142464\n"
"ds_read_b64_tr_b16 v[248:249], v182 offset:54920\n"
"buffer_load_dwordx4 v192, s[16:19], s43 offen offset:1024 lds\n"
"v_mfma_f32_16x16x32_bf16 v[116:119], v[108:111], v[162:165], v[116:119]\n"
"v_max_f32 v135, v135, v251\n"
"v_max3_f32 v170, v128, v129, v130\n"
"v_mfma_f32_16x16x32_bf16 v[224:227], a[84:87], v[36:39], v[224:227]\n"
"ds_read_b64_tr_b16 v[250:251], v183 offset:54920\n"
"v_max3_f32 v170, v170, v131, v132\n"
"v_mfma_f32_16x16x32_bf16 v[208:211], a[88:91], v[36:39], v[208:211]\n"
"v_max3_f32 v170, v170, v133, v134\n"
"v_max_f32 v120, v120, v112\n"
"v_mfma_f32_16x16x32_bf16 v[204:207], a[92:95], v[36:39], v[204:207]\n"
"v_max_f32 v121, v121, v113\n"
"v_max_f32 v122, v122, v114\n"
"v_mfma_f32_16x16x32_bf16 v[224:227], a[96:99], v[40:43], v[224:227]\n"
"ds_read_b64_tr_b16 v[112:113], v182 offset:54088\n"
"v_max3_f32 v170, v170, v135, v120\n"
"v_mfma_f32_16x16x32_bf16 v[208:211], a[100:103], v[40:43], v[208:211]\n"
"v_max_f32 v123, v123, v115\n"
"v_max3_f32 v170, v170, v121, v122\n"
"v_mfma_f32_16x16x32_bf16 v[204:207], a[104:107], v[40:43], v[204:207]\n"
"ds_read_b64_tr_b16 v[114:115], v183 offset:54088\n"
"v_max_f32 v124, v124, v116\n"
"v_mfma_f32_16x16x32_bf16 v[224:227], a[108:111], v[44:47], v[224:227]\n"
"v_max_f32 v125, v125, v117\n"
"v_max_f32 v126, v126, v118\n"
"v_mfma_f32_16x16x32_bf16 v[208:211], a[112:115], v[44:47], v[208:211]\n"
"ds_read_b64_tr_b16 v[116:117], v182 offset:54856\n"
"v_max3_f32 v170, v170, v123, v124\n"
"v_mfma_f32_16x16x32_bf16 v[204:207], a[116:119], v[44:47], v[204:207]\n"
"s_add_u32 m0, s79, 142464\n"
"v_max_f32 v127, v127, v119\n"
"v_max3_f32 v170, v170, v125, v126\n"
"v_mfma_f32_16x16x32_bf16 v[224:227], a[120:123], v[48:51], v[224:227]\n"
"buffer_load_dwordx4 v192, s[16:19], s43 offen offset:2048 lds\n"
"ds_read_b64_tr_b16 v[118:119], v183 offset:54856\n"
"v_mfma_f32_16x16x32_bf16 v[208:211], a[124:127], v[48:51], v[208:211]\n"
"s_add_u32 s43, s80, 12288\n"
"v_max_f32 v171, v170, v127\n"
"v_max_f32 v170, v170, v127\n"
"v_mfma_f32_16x16x32_bf16 v[204:207], a[128:131], v[48:51], v[204:207]\n"
"s_nop 0\n"
"v_permlane16_swap_b32 v170, v171\n"
"v_mfma_f32_16x16x32_bf16 v[224:227], a[132:135], v[52:55], v[224:227]\n"
"v_max_f32 v170, v170, v171\n"
"v_mov_b32 v171, v170\n"
"v_mfma_f32_16x16x32_bf16 v[208:211], a[136:139], v[52:55], v[208:211]\n"
"s_nop 0\n"
"v_permlane32_swap_b32 v170, v171\n"
"v_mfma_f32_16x16x32_bf16 v[204:207], a[140:143], v[52:55], v[204:207]\n"
"v_max_f32 v170, v170, v171\n"
"v_mul_f32 v170, 0xbe553b95, v170\n"
"v_mfma_f32_16x16x32_bf16 v[212:215], a[144:147], v[32:35], 0\n"
"v_fmamk_f32 v128, v128, 0x3e553b95, v170\n"
"v_fmamk_f32 v129, v129, 0x3e553b95, v170\n"
"v_mfma_f32_16x16x32_bf16 v[216:219], a[148:151], v[32:35], 0\n"
"v_fmamk_f32 v130, v130, 0x3e553b95, v170\n"
"v_fmamk_f32 v131, v131, 0x3e553b95, v170\n"
"v_mfma_f32_16x16x32_bf16 v[228:231], a[152:155], v[32:35], 0\n"
"ds_read_b128 v[32:35], v188 offset:6144\n"
"v_fmamk_f32 v132, v132, 0x3e553b95, v170\n"
"v_mfma_f32_16x16x32_bf16 v[212:215], a[156:159], v[36:39], v[212:215]\n"
"v_fmamk_f32 v133, v133, 0x3e553b95, v170\n"
"v_fmamk_f32 v134, v134, 0x3e553b95, v170\n"
"v_mfma_f32_16x16x32_bf16 v[216:219], a[160:163], v[36:39], v[216:219]\n"
"s_add_u32 m0, s34, 1024\n"
"v_fmamk_f32 v135, v135, 0x3e553b95, v170\n"
"v_mov_b64 v[98:99], 0\n"
"v_mfma_f32_16x16x32_bf16 v[228:231], a[164:167], v[36:39], v[228:231]\n"
"buffer_load_dwordx4 v190, s[8:11], s0 offen lds\n"
"ds_read_b128 v[36:39], v189 offset:6144\n"
"v_mfma_f32_16x16x32_bf16 v[212:215], a[168:171], v[40:43], v[212:215]\n"
"ds_read_b64_tr_b16 a[242:243], v236 offset:6144\n"
"v_mov_b64 v[100:101], 0\n"
"v_mfma_f32_16x16x32_bf16 v[216:219], a[172:175], v[40:43], v[216:219]\n"
"v_exp_f32 v128, v128\n"
"v_mfma_f32_16x16x32_bf16 v[228:231], a[176:179], v[40:43], v[228:231]\n"
"buffer_store_dwordx2 v[68:69], v193, s[28:31], s4 offen offset:1024 sc1\n"
"v_mfma_f32_16x16x32_bf16 v[212:215], a[180:183], v[44:47], v[212:215]\n"
"v_exp_f32 v129, v129\n"
"v_mfma_f32_16x16x32_bf16 v[216:219], a[184:187], v[44:47], v[216:219]\n"
"v_exp_f32 v130, v130\n"
"v_mfma_f32_16x16x32_bf16 v[228:231], a[188:191], v[44:47], v[228:231]\n"
"v_exp_f32 v131, v131\n"
"v_mfma_f32_16x16x32_bf16 v[212:215], a[192:195], v[48:51], v[212:215]\n"
"v_exp_f32 v132, v132\n"
"v_mfma_f32_16x16x32_bf16 v[216:219], a[196:199], v[48:51], v[216:219]\n"
"v_exp_f32 v133, v133\n"
"v_mfma_f32_16x16x32_bf16 v[228:231], a[200:203], v[48:51], v[228:231]\n"
"s_waitcnt vmcnt(12)\n"
"ds_read_b128 v[40:43], v188 offset:7168\n"
"ds_read_b128 v[44:47], v189 offset:7168\n"
"v_mfma_f32_16x16x32_bf16 v[212:215], a[204:207], v[52:55], v[212:215]\n"
"s_waitcnt lgkmcnt(3)\n"
"v_exp_f32 v134, v134\n"
"v_mfma_f32_16x16x32_bf16 v[216:219], a[208:211], v[52:55], v[216:219]\n"
"s_add_u32 m0, s34, 4096\n"
"v_exp_f32 v135, v135\n"
"v_mfma_f32_16x16x32_bf16 v[228:231], a[212:215], v[52:55], v[228:231]\n"
"buffer_load_dwordx4 v191, s[8:11], s0 offen lds\n"
"s_add_u32 s0, s0, s33\n"
"v_cvt_pk_bf16_f32 v82, v128, v130\n"
"v_cvt_pk_bf16_f32 v83, v129, v131\n"
"v_cvt_pk_bf16_f32 v84, v132, v134\n"
"v_cvt_pk_bf16_f32 v85, v133, v135\n"
"s_mov_b64 exec, s[46:47]\n"
"v_add_u32 v183, 0xc0, v183\n"
"s_mov_b64 exec, -1\n"
"v_smfmac_f32_16x16x64_bf16 v[252:255], v[82:85], v[136:143], v66\n"
"ds_read_b64_tr_b16 v[128:129], v182 offset:128\n"
"v_smfmac_f32_16x16x64_bf16 v[98:101], v[82:85], v[244:251], v66\n"
"ds_read_b64_tr_b16 v[130:131], v183 offset:128\n"
"ds_read_b64_tr_b16 v[132:133], v182 offset:896\n"
"ds_read_b64_tr_b16 v[134:135], v183 offset:896\n"
"v_mov_b64 v[106:107], 0\n"
"v_mov_b64 v[108:109], 0\n"
"v_fmamk_f32 v120, v120, 0x3e553b95, v170\n"
"v_fmamk_f32 v121, v121, 0x3e553b95, v170\n"
"v_smfmac_f32_16x16x64_bf16 v[106:109], v[82:85], v[112:119], v66\n"
"ds_read_b64_tr_b16 v[136:137], v182 offset:136\n"
"v_mfma_f32_16x16x32_bf16 v[82:85], v[172:175], v[82:85], 0\n"
"ds_read_b64_tr_b16 v[138:139], v183 offset:136\n"
"ds_read_b64_tr_b16 v[140:141], v182 offset:904\n"
"v_fmamk_f32 v122, v122, 0x3e553b95, v170\n"
"v_fmamk_f32 v123, v123, 0x3e553b95, v170\n"
"v_fmamk_f32 v124, v124, 0x3e553b95, v170\n"
"ds_read_b64_tr_b16 v[142:143], v183 offset:904\n"
"v_fmamk_f32 v125, v125, 0x3e553b95, v170\n"
"s_add_u32 m0, s34, 2048\n"
"ds_read_b64_tr_b16 v[244:245], v182 offset:72\n"
"buffer_load_dwordx4 v190, s[8:11], s1 offen lds\n"
"ds_read_b64_tr_b16 v[246:247], v183 offset:72\n"
"v_fmamk_f32 v126, v126, 0x3e553b95, v170\n"
"v_fmamk_f32 v127, v127, 0x3e553b95, v170\n"
"v_exp_f32 v120, v120\n"
"ds_read_b64_tr_b16 v[248:249], v182 offset:840\n"
"ds_read_b64_tr_b16 v[250:251], v183 offset:840\n"
"v_exp_f32 v121, v121\n"
"v_exp_f32 v122, v122\n"
"v_exp_f32 v123, v123\n"
"v_exp_f32 v124, v124\n"
"v_exp_f32 v125, v125\n"
"v_exp_f32 v126, v126\n"
"v_exp_f32 v127, v127\n"
"v_cvt_pk_bf16_f32 v90, v120, v122\n"
"v_cvt_pk_bf16_f32 v91, v121, v123\n"
"v_cvt_pk_bf16_f32 v92, v124, v126\n"
"v_cvt_pk_bf16_f32 v93, v125, v127\n"
"v_cvt_pk_bf16_f32 v224, v224, v225\n"
"v_cvt_pk_bf16_f32 v225, v226, v227\n"
"v_cvt_pk_bf16_f32 v226, v208, v209\n"
"v_cvt_pk_bf16_f32 v227, v210, v211\n"
"v_mfma_f32_16x16x32_bf16 v[208:211], a[0:3], v[32:35], 0\n"
"s_waitcnt lgkmcnt(4)\n"
"ds_read_b64_tr_b16 a[244:245], v236 offset:8\n"
"v_cvt_pk_bf16_f32 v222, v204, v205\n"
"v_smfmac_f32_16x16x64_bf16 v[252:255], v[90:93], v[128:135], v66\n"
"s_waitcnt vmcnt(12)\n"
"ds_read_b128 v[48:51], v188 offset:8192\n"
"v_smfmac_f32_16x16x64_bf16 v[98:101], v[90:93], v[136:143], v66\n"
"s_add_u32 m0, s34, 5120\n"
"s_nop 0\n"
"buffer_load_dwordx4 v191, s[8:11], s1 offen lds\n"
"v_mfma_f32_16x16x32_bf16 v[208:211], a[12:15], v[36:39], v[208:211]\n"
"s_waitcnt lgkmcnt(2)\n"
"ds_read_b128 v[52:55], v189 offset:8192\n"
"ds_write_b128 v186, v[224:227] offset:33888\n"
"v_smfmac_f32_16x16x64_bf16 v[106:109], v[90:93], v[244:251], v66\n"
"s_add_u32 s1, s1, s33\n"
"v_cvt_pk_bf16_f32 v252, v252, v253\n"
"v_mfma_f32_16x16x32_bf16 v[90:93], v[172:175], v[90:93], v[82:85]\n"
"v_cvt_pk_bf16_f32 v253, v254, v255\n"
"v_cvt_pk_bf16_f32 v98, v98, v99\n"
"v_mfma_f32_16x16x32_bf16 v[208:211], a[24:27], v[40:43], v[208:211]\n"
"v_cvt_pk_bf16_f32 v99, v100, v101\n"
"v_cvt_pk_bf16_f32 v223, v206, v207\n"
"v_mfma_f32_16x16x32_bf16 v[204:207], a[4:7], v[32:35], 0\n"
"ds_write_b128 v186, v[220:223] offset:33824\n"
"v_mfma_f32_16x16x32_bf16 v[220:223], a[8:11], v[32:35], 0\n"
"v_rcp_f32 v171, v90\n"
"v_mfma_f32_16x16x32_bf16 v[204:207], a[16:19], v[36:39], v[204:207]\n"
"v_cvt_pk_bf16_f32 v171, v171, v171\n"
"v_and_b32 v64, v171, v176\n"
"v_mfma_f32_16x16x32_bf16 v[220:223], a[20:23], v[36:39], v[220:223]\n"
"v_and_b32 v65, v171, v177\n"
"v_cvt_pk_bf16_f32 v106, v106, v107\n"
"v_mfma_f32_16x16x32_bf16 v[204:207], a[28:31], v[40:43], v[204:207]\n"
"s_waitcnt lgkmcnt(2)\n"
"v_cvt_pk_bf16_f32 v107, v108, v109\n"
"v_cvt_pk_bf16_f32 v148, v212, v213\n"
"v_mfma_f32_16x16x16_bf16 v[252:255], v[252:253], v[64:65], 0\n"
"s_waitcnt vmcnt(11)\n"
"v_add3_u32 v200, v200, s50, -2\n"
"v_cmp_lt_u32 vcc, 64, v200\n"
"v_mfma_f32_16x16x16_bf16 v[98:101], v[98:99], v[64:65], 0\n"
"s_add_u32 m0, s34, 6144\n"
"ds_read_b64_tr_b16 a[246:247], v236 offset:6152\n"
"buffer_load_dwordx4 v190, s[8:11], s32 offen lds\n"
"v_mfma_f32_16x16x16_bf16 v[106:109], v[106:107], v[64:65], 0\n"
"s_cbranch_vccnz .Lp1%=\n"
".Lr1%=:\n"
"v_mfma_f32_16x16x32_bf16 v[220:223], a[32:35], v[40:43], v[220:223]\n"
"v_cvt_pk_bf16_f32 v252, v252, v253\n"
"v_cvt_pk_bf16_f32 v253, v254, v255\n"
"v_mfma_f32_16x16x32_bf16 v[208:211], a[36:39], v[44:47], v[208:211]\n"
"v_cvt_pk_bf16_f32 v254, v98, v99\n"
"v_cvt_pk_bf16_f32 v255, v100, v101\n"
"v_mfma_f32_16x16x32_bf16 v[204:207], a[40:43], v[44:47], v[204:207]\n"
"buffer_load_dword v201, off, s[28:31], s40 offset:128 sc1\n"
"v_cvt_pk_bf16_f32 v106, v106, v107\n"
"v_mfma_f32_16x16x32_bf16 v[220:223], a[44:47], v[44:47], v[220:223]\n"
"buffer_store_dwordx4 v[252:255], v192, s[28:31], s37 offen sc1\n"
"v_mfma_f32_16x16x32_bf16 v[208:211], a[48:51], v[48:51], v[208:211]\n"
"v_cvt_pk_bf16_f32 v107, v108, v109\n"
"v_cvt_pk_bf16_f32 v149, v214, v215\n"
"v_mfma_f32_16x16x32_bf16 v[204:207], a[52:55], v[48:51], v[204:207]\n"
"ds_read_b64_tr_b16 a[248:249], v235\n"
"v_cvt_pk_bf16_f32 v150, v216, v217\n"
"v_mfma_f32_16x16x32_bf16 v[220:223], a[56:59], v[48:51], v[220:223]\n"
"buffer_store_dwordx2 v[106:107], v193, s[28:31], s37 offen offset:1024 sc1\n"
"v_mfma_f32_16x16x32_bf16 v[208:211], a[60:63], v[52:55], v[208:211]\n"
"s_add_u32 s37, s37, 1572864\n"
"ds_read_b64_tr_b16 a[250:251], v235 offset:6144\n"
"v_cvt_pk_bf16_f32 v151, v218, v219\n"
"v_mfma_f32_16x16x32_bf16 v[204:207], a[64:67], v[52:55], v[204:207]\n"
"s_add_u32 s4, s37, 12582912\n"
"v_cvt_pk_bf16_f32 v162, v228, v229\n"
"v_cvt_pk_bf16_f32 v163, v230, v231\n"
"v_mfma_f32_16x16x32_bf16 v[220:223], a[68:71], v[52:55], v[220:223]\n"
"s_add_u32 m0, s34, 9216\n"
"ds_read_b128 v[112:115], v180 offset:27008\n"
"ds_read_b128 v[120:123], v181 offset:27008\n"
"v_mfma_f32_16x16x32_bf16 v[224:227], a[72:75], v[32:35], 0\n"
"buffer_load_dwordx4 v191, s[8:11], s32 offen lds\n"
"ds_read_b128 v[116:119], v178\n"
"v_mfma_f32_16x16x32_bf16 v[212:215], a[76:79], v[32:35], 0\n"
"s_add_u32 s32, s32, s33\n"
"ds_read_b128 v[124:127], v179\n"
"v_cvt_pk_bf16_f32 v208, v208, v209\n"
"v_mfma_f32_16x16x32_bf16 v[216:219], a[80:83], v[32:35], 0\n"
"ds_read_b64 v[82:83], v180 offset:27072\n"
"v_cvt_pk_bf16_f32 v209, v210, v211\n"
"v_mfma_f32_16x16x32_bf16 v[224:227], a[84:87], v[36:39], v[224:227]\n"
"ds_read_b128 v[128:131], v178 offset:13504\n"
"v_cvt_pk_bf16_f32 v210, v204, v205\n"
"v_mfma_f32_16x16x32_bf16 v[212:215], a[88:91], v[36:39], v[212:215]\n"
"buffer_load_dwordx4 v[56:59], v192, s[28:31], s38 offen sc1\n"
"ds_read_b128 v[136:139], v179 offset:13504\n"
"v_mfma_f32_16x16x32_bf16 v[216:219], a[92:95], v[36:39], v[216:219]\n"
"s_waitcnt vmcnt(11)\n"
"ds_read_b64 v[84:85], v181 offset:27072\n"
"v_cvt_pk_bf16_f32 v211, v206, v207\n"
"v_mfma_f32_16x16x32_bf16 v[224:227], a[96:99], v[40:43], v[224:227]\n"
"s_waitcnt lgkmcnt(8)\n"
"ds_read_b64 v[90:91], v178 offset:64\n"
"v_cvt_pk_bf16_f32 v220, v220, v221\n"
"v_mfma_f32_16x16x32_bf16 v[212:215], a[100:103], v[40:43], v[212:215]\n"
"s_waitcnt lgkmcnt(8)\n"
"ds_read_b64 v[92:93], v179 offset:64\n"
"v_cvt_pk_bf16_f32 v221, v222, v223\n"
"v_mfma_f32_16x16x32_bf16 v[112:115], v[112:115], v[152:155], 0\n"
"s_waitcnt lgkmcnt(8)\n"
"buffer_load_dwordx2 v[60:61], v193, s[28:31], s38 offen offset:1024 sc1\n"
"ds_read_b64 v[98:99], v178 offset:13568\n"
"v_mfma_f32_16x16x32_bf16 v[120:123], v[120:123], v[152:155], 0\n"
"s_waitcnt lgkmcnt(8)\n"
"ds_read_b64 v[100:101], v179 offset:13568\n"
"v_mov_b64 v[252:253], 0\n"
"v_mfma_f32_16x16x32_bf16 v[116:119], v[116:119], v[152:155], 0\n"
"s_waitcnt lgkmcnt(8)\n"
"ds_read_b64_tr_b16 v[244:245], v184 offset:128\n"
"v_mov_b64 v[254:255], 0\n"
"v_mfma_f32_16x16x32_bf16 v[124:127], v[124:127], v[152:155], 0\n"
"s_waitcnt lgkmcnt(8)\n"
"ds_read_b64_tr_b16 v[246:247], v185 offset:128\n"
"s_mov_b64 exec, s[44:45]\n"
"v_add_u32 v181, 0xc0, v181\n"
"s_mov_b64 exec, -1\n"
"v_mfma_f32_16x16x32_bf16 v[112:115], v[80:83], v[164:167], v[112:115]\n"
"s_waitcnt lgkmcnt(7)\n"
"ds_read_b64_tr_b16 v[248:249], v184 offset:896\n"
"ds_read_b64_tr_b16 v[250:251], v185 offset:896\n"
"v_mfma_f32_16x16x32_bf16 v[128:131], v[128:131], v[152:155], 0\n"
"s_waitcnt lgkmcnt(8)\n"
"s_add_u32 m0, s34, 7168\n"
"s_barrier\n"
"v_mfma_f32_16x16x32_bf16 v[136:139], v[136:139], v[152:155], 0\n"
"s_waitcnt lgkmcnt(7)\n"
"buffer_load_dwordx4 v190, s[8:11], s0 offen lds\n"
"ds_read_b128 v[132:135], v178 offset:27008\n"
"v_mfma_f32_16x16x32_bf16 v[120:123], v[84:87], v[166:169], v[120:123]\n"
"s_waitcnt lgkmcnt(7)\n"
"ds_read_b128 v[140:143], v179 offset:27008\n"
"ds_read_b64 v[106:107], v178 offset:27072\n"
"v_mfma_f32_16x16x32_bf16 v[116:119], v[88:91], v[164:167], v[116:119]\n"
"s_waitcnt lgkmcnt(8)\n"
"ds_read_b64 v[108:109], v179 offset:27072\n"
"s_add_u32 s38, s38, 1572864\n"
"v_mfma_f32_16x16x32_bf16 v[124:127], v[92:95], v[166:169], v[124:127]\n"
"s_waitcnt lgkmcnt(8)\n"
"ds_write_b128 v186, v[208:211] offset:47264\n"
"v_mfma_f32_16x16x32_bf16 v[128:131], v[96:99], v[164:167], v[128:131]\n"
"v_max_f32 v112, v112, v120\n"
"v_max_f32 v113, v113, v121\n"
"v_mfma_f32_16x16x32_bf16 v[136:139], v[100:103], v[166:169], v[136:139]\n"
"v_max_f32 v114, v114, v122\n"
"v_max_f32 v115, v115, v123\n"
"v_mfma_f32_16x16x32_bf16 v[216:219], a[104:107], v[40:43], v[216:219]\n"
"ds_read_b64_tr_b16 v[120:121], v184 offset:136\n"
"ds_read_b64_tr_b16 v[122:123], v185 offset:136\n"
"v_mfma_f32_16x16x32_bf16 v[224:227], a[108:111], v[44:47], v[224:227]\n"
"s_waitcnt lgkmcnt(6)\n"
"ds_read_b64_tr_b16 a[252:253], v236 offset:12288\n"
"v_max_f32 v116, v116, v124\n"
"v_mfma_f32_16x16x32_bf16 v[132:135], v[132:135], v[152:155], 0\n"
"s_waitcnt lgkmcnt(6)\n"
"v_max_f32 v117, v117, v125\n"
"v_max_f32 v118, v118, v126\n"
"v_mfma_f32_16x16x32_bf16 v[140:143], v[140:143], v[152:155], 0\n"
"s_waitcnt lgkmcnt(5)\n"
"ds_read_b64_tr_b16 v[124:125], v184 offset:904\n"
"v_max_f32 v119, v119, v127\n"
"v_mfma_f32_16x16x32_bf16 v[132:135], v[104:107], v[164:167], v[132:135]\n"
"s_waitcnt lgkmcnt(5)\n"
"ds_read_b64_tr_b16 v[126:127], v185 offset:904\n"
"v_max3_f32 v170, v112, v113, v114\n"
"v_mfma_f32_16x16x32_bf16 v[140:143], v[108:111], v[166:169], v[140:143]\n"
"s_add_u32 m0, s79, 130176\n"
"ds_read_b64_tr_b16 a[254:255], v236 offset:18432\n"
"buffer_load_dwordx4 v192, s[16:19], s43 offen lds\n"
"v_mfma_f32_16x16x32_bf16 v[212:215], a[112:115], v[44:47], v[212:215]\n"
"v_max3_f32 v170, v170, v115, v116\n"
"v_max3_f32 v170, v170, v117, v118\n"
"v_mfma_f32_16x16x32_bf16 v[216:219], a[116:119], v[44:47], v[216:219]\n"
"v_max_f32 v128, v128, v136\n"
"v_max_f32 v129, v129, v137\n"
"v_mfma_f32_16x16x32_bf16 v[224:227], a[120:123], v[48:51], v[224:227]\n"
"ds_read_b64_tr_b16 v[136:137], v184 offset:72\n"
"v_max_f32 v130, v130, v138\n"
"v_mfma_f32_16x16x32_bf16 v[212:215], a[124:127], v[48:51], v[212:215]\n"
"v_max3_f32 v170, v170, v119, v128\n"
"v_max_f32 v131, v131, v139\n"
"v_mfma_f32_16x16x32_bf16 v[216:219], a[128:131], v[48:51], v[216:219]\n"
"ds_read_b64_tr_b16 v[138:139], v185 offset:72\n"
"v_max3_f32 v170, v170, v129, v130\n"
"v_mfma_f32_16x16x32_bf16 v[224:227], a[132:135], v[52:55], v[224:227]\n"
"v_max_f32 v132, v132, v140\n"
"v_max_f32 v133, v133, v141\n"
"v_mfma_f32_16x16x32_bf16 v[212:215], a[136:139], v[52:55], v[212:215]\n"
"ds_read_b64_tr_b16 v[140:141], v184 offset:840\n"
"v_max_f32 v134, v134, v142\n"
"v_mfma_f32_16x16x32_bf16 v[216:219], a[140:143], v[52:55], v[216:219]\n"
"v_max3_f32 v170, v170, v131, v132\n"
"v_max_f32 v135, v135, v143\n"
"v_mfma_f32_16x16x32_bf16 v[228:231], a[144:147], v[32:35], 0\n"
"ds_read_b64_tr_b16 v[142:143], v185 offset:840\n"
"v_max3_f32 v170, v170, v133, v134\n"
"v_mfma_f32_16x16x32_bf16 v[204:207], a[148:151], v[32:35], 0\n"
"s_add_u32 m0, s79, 130176\n"
"v_max_f32 v171, v170, v135\n"
"v_max_f32 v170, v170, v135\n"
"v_mfma_f32_16x16x32_bf16 v[208:211], a[152:155], v[32:35], 0\n"
"buffer_load_dwordx4 v192, s[16:19], s43 offen offset:1024 lds\n"
"ds_read_b128 v[32:35], v188\n"
"v_mfma_f32_16x16x32_bf16 v[228:231], a[156:159], v[36:39], v[228:231]\n"
"v_permlane16_swap_b32 v170, v171\n"
"v_mfma_f32_16x16x32_bf16 v[204:207], a[160:163], v[36:39], v[204:207]\n"
"v_max_f32 v170, v170, v171\n"
"v_mov_b32 v171, v170\n"
"v_mfma_f32_16x16x32_bf16 v[208:211], a[164:167], v[36:39], v[208:211]\n"
"ds_read_b128 v[36:39], v189\n"
"ds_read_b64_tr_b16 v[0:1], v236 offset:12296\n"
"v_mfma_f32_16x16x32_bf16 v[228:231], a[168:171], v[40:43], v[228:231]\n"
"buffer_store_dwordx4 v[68:71], v192, s[28:31], s4 offen sc1\n"
"v_mfma_f32_16x16x32_bf16 v[204:207], a[172:175], v[40:43], v[204:207]\n"
"v_permlane32_swap_b32 v170, v171\n"
"v_mfma_f32_16x16x32_bf16 v[208:211], a[176:179], v[40:43], v[208:211]\n"
"v_max_f32 v170, v170, v171\n"
"v_mul_f32 v170, 0xbe553b95, v170\n"
"v_mfma_f32_16x16x32_bf16 v[228:231], a[180:183], v[44:47], v[228:231]\n"
"v_fmamk_f32 v112, v112, 0x3e553b95, v170\n"
"v_fmamk_f32 v113, v113, 0x3e553b95, v170\n"
"v_mfma_f32_16x16x32_bf16 v[204:207], a[184:187], v[44:47], v[204:207]\n"
"v_fmamk_f32 v114, v114, 0x3e553b95, v170\n"
"v_fmamk_f32 v115, v115, 0x3e553b95, v170\n"
"v_mfma_f32_16x16x32_bf16 v[208:211], a[188:191], v[44:47], v[208:211]\n"
"s_add_u32 m0, s79, 130176\n"
"v_fmamk_f32 v116, v116, 0x3e553b95, v170\n"
"v_fmamk_f32 v117, v117, 0x3e553b95, v170\n"
"v_mfma_f32_16x16x32_bf16 v[228:231], a[192:195], v[48:51], v[228:231]\n"
"buffer_load_dwordx4 v192, s[16:19], s43 offen offset:2048 lds\n"
"v_fmamk_f32 v118, v118, 0x3e553b95, v170\n"
"v_mfma_f32_16x16x32_bf16 v[204:207], a[196:199], v[48:51], v[204:207]\n"
"s_add_u32 s43, s80, 49152\n"
"v_fmamk_f32 v119, v119, 0x3e553b95, v170\n"
"v_mov_b64 v[98:99], 0\n"
"v_mfma_f32_16x16x32_bf16 v[208:211], a[200:203], v[48:51], v[208:211]\n"
".Lic3%=:\n"
"s_cbranch_execz .Lib0%=\n"
"v_exp_f32 v112, v112\n"
"v_mfma_f32_16x16x32_bf16 v[228:231], a[204:207], v[52:55], v[228:231]\n"
"v_exp_f32 v113, v113\n"
"v_mfma_f32_16x16x32_bf16 v[204:207], a[208:211], v[52:55], v[204:207]\n"
"v_exp_f32 v114, v114\n"
"v_mfma_f32_16x16x32_bf16 v[208:211], a[212:215], v[52:55], v[208:211]\n"
"s_waitcnt vmcnt(14)\n"
"ds_read_b128 v[40:43], v188 offset:1024\n"
"ds_read_b128 v[44:47], v189 offset:1024\n"
"v_exp_f32 v115, v115\n"
"v_exp_f32 v116, v116\n"
"v_exp_f32 v117, v117\n"
"v_exp_f32 v118, v118\n"
"v_exp_f32 v119, v119\n"
"v_mov_b64 v[100:101], 0\n"
"v_cvt_pk_bf16_f32 v82, v112, v114\n"
"v_cvt_pk_bf16_f32 v83, v113, v115\n"
"v_cvt_pk_bf16_f32 v84, v116, v118\n"
"v_cvt_pk_bf16_f32 v85, v117, v119\n"
"v_mov_b64 v[106:107], 0\n"
"v_mov_b64 v[108:109], 0\n"
"v_smfmac_f32_16x16x64_bf16 v[252:255], v[82:85], v[244:251], v66\n"
"s_waitcnt lgkmcnt(3)\n"
"ds_read_b64_tr_b16 v[112:113], v182 offset:13632\n"
"v_smfmac_f32_16x16x64_bf16 v[98:101], v[82:85], v[120:127], v66\n"
"s_add_u32 m0, s34, 10240\n"
"s_nop 0\n"
"buffer_load_dwordx4 v191, s[8:11], s0 offen lds\n"
"v_smfmac_f32_16x16x64_bf16 v[106:109], v[82:85], v[136:143], v66\n"
"s_add_u32 s0, s0, s33\n"
"ds_read_b64_tr_b16 v[114:115], v183 offset:13632\n"
"v_mfma_f32_16x16x32_bf16 v[82:85], v[172:175], v[82:85], 0\n"
"ds_read_b64_tr_b16 v[116:117], v182 offset:14400\n"
"ds_read_b64_tr_b16 v[118:119], v183 offset:14400\n"
"v_fmamk_f32 v128, v128, 0x3e553b95, v170\n"
"ds_read_b64_tr_b16 v[244:245], v182 offset:13640\n"
"v_fmamk_f32 v129, v129, 0x3e553b95, v170\n"
"ds_read_b64_tr_b16 v[246:247], v183 offset:13640\n"
"v_fmamk_f32 v130, v130, 0x3e553b95, v170\n"
"ds_read_b64_tr_b16 v[248:249], v182 offset:14408\n"
"v_fmamk_f32 v131, v131, 0x3e553b95, v170\n"
"ds_read_b64_tr_b16 v[250:251], v183 offset:14408\n"
"v_fmamk_f32 v132, v132, 0x3e553b95, v170\n"
"ds_read_b64_tr_b16 v[120:121], v182 offset:13576\n"
"v_fmamk_f32 v133, v133, 0x3e553b95, v170\n"
"ds_read_b64_tr_b16 v[122:123], v183 offset:13576\n"
"v_fmamk_f32 v134, v134, 0x3e553b95, v170\n"
"ds_read_b64_tr_b16 v[124:125], v182 offset:14344\n"
"v_fmamk_f32 v135, v135, 0x3e553b95, v170\n"
"s_add_u32 m0, s34, 8192\n"
"ds_read_b64_tr_b16 v[126:127], v183 offset:14344\n"
"buffer_load_dwordx4 v190, s[8:11], s1 offen lds\n"
"v_exp_f32 v128, v128\n"
"v_exp_f32 v129, v129\n"
"v_exp_f32 v130, v130\n"
"v_exp_f32 v131, v131\n"
"v_exp_f32 v132, v132\n"
"v_exp_f32 v133, v133\n"
"v_exp_f32 v134, v134\n"
"v_exp_f32 v135, v135\n"
"s_waitcnt vmcnt(14)\n"
"ds_read_b128 v[48:51], v188 offset:2048\n"
"ds_read_b128 v[52:55], v189 offset:2048\n"
"v_cvt_pk_bf16_f32 v90, v128, v130\n"
"v_cvt_pk_bf16_f32 v91, v129, v131\n"
"v_cvt_pk_bf16_f32 v92, v132, v134\n"
"v_cvt_pk_bf16_f32 v93, v133, v135\n"
"v_cvt_pk_bf16_f32 v224, v224, v225\n"
"v_cvt_pk_bf16_f32 v225, v226, v227\n"
"v_cvt_pk_bf16_f32 v226, v212, v213\n"
"v_cvt_pk_bf16_f32 v227, v214, v215\n"
"v_mfma_f32_16x16x32_bf16 v[212:215], a[0:3], v[32:35], 0\n"
"s_waitcnt lgkmcnt(6)\n"
"ds_write_b128 v186, v[224:227] offset:47392\n"
"v_smfmac_f32_16x16x64_bf16 v[252:255], v[90:93], v[112:119], v66\n"
"s_waitcnt lgkmcnt(3)\n"
"v_cvt_pk_bf16_f32 v222, v216, v217\n"
"v_smfmac_f32_16x16x64_bf16 v[98:101], v[90:93], v[244:251], v66\n"
"s_add_u32 m0, s34, 11264\n"
"s_nop 0\n"
"buffer_load_dwordx4 v191, s[8:11], s1 offen lds\n"
"v_smfmac_f32_16x16x64_bf16 v[106:109], v[90:93], v[120:127], v66\n"
"s_add_u32 s1, s1, s33\n"
"ds_read_b64_tr_b16 v[2:3], v236 offset:18440\n"
"v_mfma_f32_16x16x32_bf16 v[90:93], v[172:175], v[90:93], v[82:85]\n"
"v_cvt_pk_bf16_f32 v252, v252, v253\n"
"v_cvt_pk_bf16_f32 v253, v254, v255\n"
"v_mfma_f32_16x16x32_bf16 v[212:215], a[12:15], v[36:39], v[212:215]\n"
"v_cvt_pk_bf16_f32 v98, v98, v99\n"
"v_cvt_pk_bf16_f32 v99, v100, v101\n"
"v_mfma_f32_16x16x32_bf16 v[212:215], a[24:27], v[40:43], v[212:215]\n"
"v_cvt_pk_bf16_f32 v106, v106, v107\n"
"v_cvt_pk_bf16_f32 v107, v108, v109\n"
"v_mfma_f32_16x16x32_bf16 v[212:215], a[36:39], v[44:47], v[212:215]\n"
"s_waitcnt lgkmcnt(3)\n"
"v_rcp_f32 v171, v90\n"
"v_mfma_f32_16x16x32_bf16 v[212:215], a[48:51], v[48:51], v[212:215]\n"
"s_waitcnt vmcnt(13) lgkmcnt(2)\n"
"v_cvt_pk_bf16_f32 v171, v171, v171\n"
"v_and_b32 v64, v171, v176\n"
"v_mfma_f32_16x16x32_bf16 v[212:215], a[60:63], v[52:55], v[212:215]\n"
"v_readfirstlane_b32 s6, v201\n"
"v_and_b32 v65, v171, v177\n"
"v_mfma_f32_16x16x32_bf16 v[224:227], a[72:75], v[32:35], 0\n"
"s_and_b32 s6, s6, 0xffffff00\n"
"v_or_b32 v201, 4, s6\n"
"v_cvt_pk_bf16_f32 v223, v218, v219\n"
"v_mfma_f32_16x16x16_bf16 v[252:255], v[252:253], v[64:65], 0\n"
"s_mov_b64 exec, 1\n"
"buffer_store_dword v201, off, s[28:31], s40 sc1\n"
"s_mov_b64 exec, -1\n"
"v_mfma_f32_16x16x16_bf16 v[98:101], v[98:99], v[64:65], 0\n"
"s_waitcnt vmcnt(12)\n"
"v_or_b32 v201, 5, s6\n"
"v_cvt_pk_bf16_f32 v152, v228, v229\n"
"v_mfma_f32_16x16x16_bf16 v[106:109], v[106:107], v[64:65], 0\n"
"s_add_u32 m0, s34, 0\n"
"s_mov_b64 exec, 1\n"
"buffer_store_dword v201, off, s[28:31], s40 sc1\n"
"s_mov_b64 exec, -1\n"
"v_mfma_f32_16x16x32_bf16 v[216:219], a[4:7], v[32:35], 0\n"
"buffer_load_dwordx4 v190, s[8:11], s32 offen lds\n"
"ds_write_b128 v186, v[220:223] offset:47328\n"
"v_mfma_f32_16x16x32_bf16 v[220:223], a[8:11], v[32:35], 0\n"
"v_cvt_pk_bf16_f32 v252, v252, v253\n"
"v_cvt_pk_bf16_f32 v253, v254, v255\n"
"v_mfma_f32_16x16x32_bf16 v[216:219], a[16:19], v[36:39], v[216:219]\n"
"v_cvt_pk_bf16_f32 v254, v98, v99\n"
"v_cvt_pk_bf16_f32 v255, v100, v101\n"
"v_mfma_f32_16x16x32_bf16 v[220:223], a[20:23], v[36:39], v[220:223]\n"
"buffer_store_dwordx4 v[252:255], v192, s[28:31], s37 offen sc1\n"
"v_mfma_f32_16x16x32_bf16 v[216:219], a[28:31], v[40:43], v[216:219]\n"
"s_waitcnt vmcnt(13)\n"
"v_cvt_pk_bf16_f32 v106, v106, v107\n"
"v_cvt_pk_bf16_f32 v107, v108, v109\n"
"v_mfma_f32_16x16x32_bf16 v[220:223], a[32:35], v[40:43], v[220:223]\n"
"ds_read_b64_tr_b16 v[4:5], v235 offset:12288\n"
"v_cvt_pk_bf16_f32 v153, v230, v231\n"
"v_mfma_f32_16x16x32_bf16 v[216:219], a[40:43], v[44:47], v[216:219]\n"
"s_waitcnt vmcnt(12)\n"
"ds_read_b64_tr_b16 v[6:7], v235 offset:18432\n"
"buffer_store_dwordx2 v[106:107], v193, s[28:31], s37 offen offset:1024 sc1\n"
"v_mfma_f32_16x16x32_bf16 v[220:223], a[44:47], v[44:47], v[220:223]\n"
"s_add_u32 s37, s37, 1572864\n"
"v_cvt_pk_bf16_f32 v154, v204, v205\n"
"v_cvt_pk_bf16_f32 v155, v206, v207\n"
"v_mfma_f32_16x16x32_bf16 v[216:219], a[52:55], v[48:51], v[216:219]\n"
"s_add_u32 s4, s37, 11010048\n"
"v_cvt_pk_bf16_f32 v166, v208, v209\n"
"v_cvt_pk_bf16_f32 v167, v210, v211\n"
"v_mfma_f32_16x16x32_bf16 v[220:223], a[56:59], v[48:51], v[220:223]\n"
"s_add_u32 m0, s34, 3072\n"
"ds_read_b128 v[136:139], v178\n"
"ds_read_b128 v[128:131], v179\n"
"v_mfma_f32_16x16x32_bf16 v[216:219], a[64:67], v[52:55], v[216:219]\n"
"buffer_load_dwordx4 v191, s[8:11], s32 offen lds\n"
"ds_read_b64 v[82:83], v178 offset:64\n"
"v_mfma_f32_16x16x32_bf16 v[220:223], a[68:71], v[52:55], v[220:223]\n"
"s_add_u32 s32, s32, s33\n"
"ds_read_b64 v[84:85], v179 offset:64\n"
"v_cvt_pk_bf16_f32 v212, v212, v213\n"
"v_mfma_f32_16x16x32_bf16 v[228:231], a[76:79], v[32:35], 0\n"
"ds_read_b128 v[140:143], v178 offset:13504\n"
"v_cvt_pk_bf16_f32 v213, v214, v215\n"
"v_mfma_f32_16x16x32_bf16 v[204:207], a[80:83], v[32:35], 0\n"
"ds_read_b128 v[132:135], v179 offset:13504\n"
"v_cvt_pk_bf16_f32 v214, v216, v217\n"
"v_mfma_f32_16x16x32_bf16 v[224:227], a[84:87], v[36:39], v[224:227]\n"
"ds_read_b64 v[90:91], v178 offset:13568\n"
"v_cvt_pk_bf16_f32 v215, v218, v219\n"
"v_mfma_f32_16x16x32_bf16 v[228:231], a[88:91], v[36:39], v[228:231]\n"
"ds_read_b64 v[92:93], v179 offset:13568\n"
"v_cvt_pk_bf16_f32 v220, v220, v221\n"
"v_mfma_f32_16x16x32_bf16 v[204:207], a[92:95], v[36:39], v[204:207]\n"
"ds_read_b128 v[112:115], v178 offset:27008\n"
"v_cvt_pk_bf16_f32 v221, v222, v223\n"
"v_mfma_f32_16x16x32_bf16 v[224:227], a[96:99], v[40:43], v[224:227]\n"
"s_waitcnt lgkmcnt(8)\n"
"ds_write_b128 v202, v[56:59] offset:55296\n"
"v_mfma_f32_16x16x32_bf16 v[136:139], v[136:139], v[144:147], 0\n"
"s_waitcnt lgkmcnt(7)\n"
"buffer_load_dwordx4 v[56:59], v192, s[28:31], s38 offen sc1\n"
"v_mov_b64 v[252:253], 0\n"
"v_mfma_f32_16x16x32_bf16 v[128:131], v[128:131], v[144:147], 0\n"
"s_waitcnt lgkmcnt(6)\n"
"v_mov_b64 v[254:255], 0\n"
"s_mov_b64 exec, s[46:47]\n"
"v_add_u32 v185, 0xc0, v185\n"
"s_mov_b64 exec, -1\n"
"v_mfma_f32_16x16x32_bf16 v[136:139], v[80:83], v[156:159], v[136:139]\n"
"s_waitcnt lgkmcnt(4)\n"
"ds_read_b128 v[244:247], v179 offset:27008\n"
"ds_read_b64 v[98:99], v178 offset:27072\n"
"v_mfma_f32_16x16x32_bf16 v[128:131], v[84:87], v[158:161], v[128:131]\n"
"s_add_u32 m0, s34, 1024\n"
"ds_read_b64 v[100:101], v179 offset:27072\n"
"buffer_load_dwordx4 v190, s[8:11], s0 offen lds\n"
"v_mfma_f32_16x16x32_bf16 v[140:143], v[140:143], v[144:147], 0\n"
"s_waitcnt lgkmcnt(6)\n"
"ds_read_b64_tr_b16 v[120:121], v182 offset:128\n"
"v_mfma_f32_16x16x32_bf16 v[132:135], v[132:135], v[144:147], 0\n"
"s_waitcnt lgkmcnt(5)\n"
"ds_read_b64_tr_b16 v[122:123], v183 offset:128\n"
"v_max_f32 v136, v136, v128\n"
"v_mfma_f32_16x16x32_bf16 v[140:143], v[88:91], v[156:159], v[140:143]\n"
"ds_read_b64_tr_b16 v[124:125], v182 offset:896\n"
"v_max_f32 v137, v137, v129\n"
"v_mfma_f32_16x16x32_bf16 v[132:135], v[92:95], v[158:161], v[132:135]\n"
"ds_read_b64_tr_b16 v[126:127], v183 offset:896\n"
"v_max_f32 v138, v138, v130\n"
"v_mfma_f32_16x16x32_bf16 v[112:115], v[112:115], v[144:147], 0\n"
"ds_write_b64 v203, v[60:61] offset:55296\n"
"v_mfma_f32_16x16x32_bf16 v[228:231], a[100:103], v[40:43], v[228:231]\n"
"s_waitcnt lgkmcnt(7)\n"
"buffer_load_dwordx2 v[60:61], v193, s[28:31], s38 offen offset:1024 sc1\n"
"v_max_f32 v139, v139, v131\n"
"v_mfma_f32_16x16x32_bf16 v[204:207], a[104:107], v[40:43], v[204:207]\n"
"s_waitcnt lgkmcnt(6)\n"
"ds_read_b64_tr_b16 v[128:129], v182 offset:136\n"
"ds_read_b64_tr_b16 v[130:131], v183 offset:136\n"
"v_mfma_f32_16x16x32_bf16 v[244:247], v[244:247], v[144:147], 0\n"
"s_waitcnt lgkmcnt(7)\n"
"v_max3_f32 v170, v136, v137, v138\n"
"v_max_f32 v140, v140, v132\n"
"v_mfma_f32_16x16x32_bf16 v[112:115], v[96:99], v[156:159], v[112:115]\n"
"s_add_u32 s38, s38, 1572864\n"
"v_max_f32 v141, v141, v133\n"
"v_max_f32 v142, v142, v134\n"
"v_mfma_f32_16x16x32_bf16 v[244:247], v[100:103], v[158:161], v[244:247]\n"
"s_add_u32 m0, s34, 4096\n"
"ds_read_b64_tr_b16 v[132:133], v182 offset:904\n"
"buffer_load_dwordx4 v191, s[8:11], s0 offen lds\n"
"v_mfma_f32_16x16x32_bf16 v[224:227], a[108:111], v[44:47], v[224:227]\n"
"s_add_u32 s0, s0, s33\n"
"v_max_f32 v143, v143, v135\n"
"v_max3_f32 v170, v170, v139, v140\n"
"v_mfma_f32_16x16x32_bf16 v[228:231], a[112:115], v[44:47], v[228:231]\n"
"ds_read_b64_tr_b16 v[134:135], v183 offset:904\n"
"v_max3_f32 v170, v170, v141, v142\n"
"v_mfma_f32_16x16x32_bf16 v[204:207], a[116:119], v[44:47], v[204:207]\n"
"v_max_f32 v112, v112, v244\n"
"v_max_f32 v113, v113, v245\n"
"v_mfma_f32_16x16x32_bf16 v[224:227], a[120:123], v[48:51], v[224:227]\n"
"ds_read_b64_tr_b16 v[244:245], v182 offset:72\n"
"v_max_f32 v114, v114, v246\n"
"v_mfma_f32_16x16x32_bf16 v[228:231], a[124:127], v[48:51], v[228:231]\n"
"v_max_f32 v115, v115, v247\n"
"v_max3_f32 v170, v170, v143, v112\n"
"v_mfma_f32_16x16x32_bf16 v[204:207], a[128:131], v[48:51], v[204:207]\n"
"ds_read_b64_tr_b16 v[246:247], v183 offset:72\n"
"v_max3_f32 v170, v170, v113, v114\n"
"v_mfma_f32_16x16x32_bf16 v[224:227], a[132:135], v[52:55], v[224:227]\n"
"s_waitcnt vmcnt(13)\n"
"v_mov_b64 v[98:99], 0\n"
"v_mov_b64 v[100:101], 0\n"
"v_mfma_f32_16x16x32_bf16 v[228:231], a[136:139], v[52:55], v[228:231]\n"
"s_barrier\n"
"v_mfma_f32_16x16x32_bf16 v[204:207], a[140:143], v[52:55], v[204:207]\n"
"ds_write_b128 v186, v[212:215] offset:60768\n"
"v_mfma_f32_16x16x32_bf16 v[208:211], a[144:147], v[32:35], 0\n"
"s_add_u32 m0, s79, 142464\n"
"v_cvt_pk_bf16_f32 v224, v224, v225\n"
"v_cvt_pk_bf16_f32 v225, v226, v227\n"
"v_mfma_f32_16x16x32_bf16 v[216:219], a[148:151], v[32:35], 0\n"
"buffer_load_dwordx4 v192, s[16:19], s43 offen lds\n"
"ds_read_b128 v[116:119], v180\n"
"v_mfma_f32_16x16x32_bf16 v[212:215], a[152:155], v[32:35], 0\n"
"ds_read_b128 v[32:35], v188 offset:6144\n"
"ds_read_b128 v[248:251], v181\n"
"v_mfma_f32_16x16x32_bf16 v[208:211], a[156:159], v[36:39], v[208:211]\n"
"ds_read_b64 v[106:107], v180 offset:64\n"
"v_cvt_pk_bf16_f32 v226, v228, v229\n"
"v_mfma_f32_16x16x32_bf16 v[216:219], a[160:163], v[36:39], v[216:219]\n"
"ds_read_b64 v[108:109], v181 offset:64\n"
"v_cvt_pk_bf16_f32 v227, v230, v231\n"
"v_mfma_f32_16x16x32_bf16 v[212:215], a[164:167], v[36:39], v[212:215]\n"
"ds_read_b128 v[36:39], v189 offset:6144\n"
"v_cvt_pk_bf16_f32 v222, v204, v205\n"
"v_mfma_f32_16x16x32_bf16 v[208:211], a[168:171], v[40:43], v[208:211]\n"
"ds_read_b64_tr_b16 v[8:9], v236\n"
"v_cvt_pk_bf16_f32 v223, v206, v207\n"
"v_mfma_f32_16x16x32_bf16 v[216:219], a[172:175], v[40:43], v[216:219]\n"
"v_mfma_f32_16x16x32_bf16 v[212:215], a[176:179], v[40:43], v[212:215]\n"
"ds_read_b64_tr_b16 v[10:11], v236 offset:6144\n"
"buffer_store_dwordx2 v[68:69], v193, s[28:31], s4 offen offset:1024 sc1\n"
"v_mfma_f32_16x16x32_bf16 v[208:211], a[180:183], v[44:47], v[208:211]\n"
"s_waitcnt lgkmcnt(6)\n"
"ds_write_b128 v186, v[220:223] offset:60832\n"
"v_mfma_f32_16x16x32_bf16 v[116:119], v[116:119], v[144:147], 0\n"
"s_waitcnt lgkmcnt(5)\n"
"s_add_u32 m0, s79, 142464\n"
"v_mfma_f32_16x16x32_bf16 v[248:251], v[248:251], v[144:147], 0\n"
"s_waitcnt lgkmcnt(4)\n"
"buffer_load_dwordx4 v192, s[16:19], s43 offen offset:1024 lds\n"
"ds_write_b128 v186, v[224:227] offset:60896\n"
"v_mfma_f32_16x16x32_bf16 v[116:119], v[104:107], v[156:159], v[116:119]\n"
"v_mov_b64 v[106:107], 0\n"
"v_mfma_f32_16x16x32_bf16 v[248:251], v[108:111], v[158:161], v[248:251]\n"
"v_mov_b64 v[108:109], 0\n"
"v_mfma_f32_16x16x32_bf16 v[216:219], a[184:187], v[44:47], v[216:219]\n"
"ds_read_b64_tr_b16 v[12:13], v236 offset:8\n"
"v_mfma_f32_16x16x32_bf16 v[212:215], a[188:191], v[44:47], v[212:215]\n"
"ds_read_b64_tr_b16 v[14:15], v236 offset:6152\n"
"v_mfma_f32_16x16x32_bf16 v[208:211], a[192:195], v[48:51], v[208:211]\n"
"v_mfma_f32_16x16x32_bf16 v[216:219], a[196:199], v[48:51], v[216:219]\n"
"ds_read_b64_tr_b16 v[16:17], v235\n"
"v_max_f32 v116, v116, v248\n"
"v_mfma_f32_16x16x32_bf16 v[212:215], a[200:203], v[48:51], v[212:215]\n"
"s_waitcnt vmcnt(15)\n"
"v_max_f32 v117, v117, v249\n"
"v_max_f32 v118, v118, v250\n"
"v_mfma_f32_16x16x32_bf16 v[208:211], a[204:207], v[52:55], v[208:211]\n"
"ds_read_b128 v[40:43], v188 offset:7168\n"
"v_max_f32 v119, v119, v251\n"
"v_mfma_f32_16x16x32_bf16 v[216:219], a[208:211], v[52:55], v[216:219]\n"
"s_waitcnt lgkmcnt(8)\n"
"ds_read_b128 v[44:47], v189 offset:7168\n"
"v_max3_f32 v170, v170, v115, v116\n"
"v_mfma_f32_16x16x32_bf16 v[212:215], a[212:215], v[52:55], v[212:215]\n"
"s_add_u32 m0, s79, 142464\n"
"ds_read_b64_tr_b16 v[248:249], v182 offset:840\n"
"buffer_load_dwordx4 v192, s[16:19], s43 offen offset:2048 lds\n"
"v_mfma_f32_16x16x32_bf16 v[228:231], a[0:3], v[32:35], 0\n"
"ds_read_b64_tr_b16 v[250:251], v183 offset:840\n"
"v_max3_f32 v170, v170, v117, v118\n"
"v_mfma_f32_16x16x32_bf16 v[204:207], a[4:7], v[32:35], 0\n"
"ds_read_b64_tr_b16 v[18:19], v235 offset:6144\n"
"v_max_f32 v171, v170, v119\n"
"v_mfma_f32_16x16x32_bf16 v[220:223], a[8:11], v[32:35], 0\n"
"v_max_f32 v170, v170, v119\n"
"v_cvt_pk_bf16_f32 v144, v208, v209\n"
"v_mfma_f32_16x16x32_bf16 v[228:231], a[12:15], v[36:39], v[228:231]\n"
"v_permlane16_swap_b32 v170, v171\n"
"v_mfma_f32_16x16x32_bf16 v[204:207], a[16:19], v[36:39], v[204:207]\n"
"v_max_f32 v170, v170, v171\n"
"v_mov_b32 v171, v170\n"
"v_mfma_f32_16x16x32_bf16 v[220:223], a[20:23], v[36:39], v[220:223]\n"
"s_nop 0\n"
"v_permlane32_swap_b32 v170, v171\n"
"v_mfma_f32_16x16x32_bf16 v[224:227], a[72:75], v[32:35], 0\n"
"s_waitcnt lgkmcnt(4)\n"
"v_max_f32 v170, v170, v171\n"
"v_mul_f32 v170, 0xbe553b95, v170\n"
"v_mfma_f32_16x16x32_bf16 v[228:231], a[24:27], v[40:43], v[228:231]\n"
"s_waitcnt lgkmcnt(3)\n"
"v_fmamk_f32 v136, v136, 0x3e553b95, v170\n"
"v_fmamk_f32 v137, v137, 0x3e553b95, v170\n"
"v_mfma_f32_16x16x32_bf16 v[204:207], a[28:31], v[40:43], v[204:207]\n"
"v_fmamk_f32 v138, v138, 0x3e553b95, v170\n"
"v_fmamk_f32 v139, v139, 0x3e553b95, v170\n"
"v_mfma_f32_16x16x32_bf16 v[220:223], a[32:35], v[40:43], v[220:223]\n"
"s_add_u32 m0, s34, 2048\n"
"v_exp_f32 v136, v136\n"
"v_mfma_f32_16x16x32_bf16 v[228:231], a[36:39], v[44:47], v[228:231]\n"
"buffer_load_dwordx4 v190, s[8:11], s1 offen lds\n"
"v_fmamk_f32 v140, v140, 0x3e553b95, v170\n"
"v_mfma_f32_16x16x32_bf16 v[204:207], a[40:43], v[44:47], v[204:207]\n"
"v_exp_f32 v137, v137\n"
"v_mfma_f32_16x16x32_bf16 v[220:223], a[44:47], v[44:47], v[220:223]\n"
"v_exp_f32 v138, v138\n"
"v_mfma_f32_16x16x32_bf16 v[224:227], a[84:87], v[36:39], v[224:227]\n"
"v_exp_f32 v139, v139\n"
"v_mfma_f32_16x16x32_bf16 v[224:227], a[96:99], v[40:43], v[224:227]\n"
"v_cvt_pk_bf16_f32 v82, v136, v138\n"
"v_cvt_pk_bf16_f32 v83, v137, v139\n"
"v_mfma_f32_16x16x32_bf16 v[224:227], a[108:111], v[44:47], v[224:227]\n"
"v_fmamk_f32 v141, v141, 0x3e553b95, v170\n"
"v_fmamk_f32 v142, v142, 0x3e553b95, v170\n"
"v_fmamk_f32 v143, v143, 0x3e553b95, v170\n"
"v_exp_f32 v140, v140\n"
"v_exp_f32 v141, v141\n"
"v_exp_f32 v142, v142\n"
"s_waitcnt vmcnt(15)\n"
"ds_read_b128 v[48:51], v188 offset:8192\n"
"ds_read_b128 v[52:55], v189 offset:8192\n"
"v_exp_f32 v143, v143\n"
"v_cvt_pk_bf16_f32 v84, v140, v142\n"
"v_cvt_pk_bf16_f32 v85, v141, v143\n"
"v_cvt_pk_bf16_f32 v145, v210, v211\n"
"v_mfma_f32_16x16x32_bf16 v[208:211], a[76:79], v[32:35], 0\n"
"s_waitcnt lgkmcnt(3)\n"
"buffer_load_dword v200, off, s[28:31], s39 sc1\n"
"ds_read_b64_tr_b16 v[136:137], v182 offset:27136\n"
"v_smfmac_f32_16x16x64_bf16 v[252:255], v[82:85], v[120:127], v66\n"
"s_add_u32 m0, s34, 5120\n"
"s_nop 0\n"
"buffer_load_dwordx4 v191, s[8:11], s1 offen lds\n"
"v_smfmac_f32_16x16x64_bf16 v[98:101], v[82:85], v[128:135], v66\n"
"s_add_u32 s1, s1, s33\n"
"ds_read_b64_tr_b16 v[138:139], v183 offset:27136\n"
"v_smfmac_f32_16x16x64_bf16 v[106:109], v[82:85], v[244:251], v66\n"
"ds_read_b64_tr_b16 v[140:141], v182 offset:27904\n"
"v_mfma_f32_16x16x32_bf16 v[82:85], v[172:175], v[82:85], 0\n"
"ds_read_b64_tr_b16 v[142:143], v183 offset:27904\n"
"ds_read_b64_tr_b16 v[120:121], v182 offset:27144\n"
"v_mfma_f32_16x16x32_bf16 v[208:211], a[88:91], v[36:39], v[208:211]\n"
"s_waitcnt lgkmcnt(6)\n"
"ds_read_b64_tr_b16 v[122:123], v183 offset:27144\n"
"v_cvt_pk_bf16_f32 v146, v216, v217\n"
"v_mfma_f32_16x16x32_bf16 v[208:211], a[100:103], v[40:43], v[208:211]\n"
"ds_read_b64_tr_b16 v[124:125], v182 offset:27912\n"
"v_cvt_pk_bf16_f32 v147, v218, v219\n"
"v_mfma_f32_16x16x32_bf16 v[228:231], a[48:51], v[48:51], v[228:231]\n"
"s_waitcnt lgkmcnt(7)\n"
"ds_read_b64_tr_b16 v[126:127], v183 offset:27912\n"
"v_fmamk_f32 v112, v112, 0x3e553b95, v170\n"
"v_mfma_f32_16x16x32_bf16 v[204:207], a[52:55], v[48:51], v[204:207]\n"
"ds_read_b64_tr_b16 v[128:129], v182 offset:27080\n"
"v_cvt_pk_bf16_f32 v158, v212, v213\n"
"v_mfma_f32_16x16x32_bf16 v[220:223], a[56:59], v[48:51], v[220:223]\n"
"ds_read_b64_tr_b16 v[130:131], v183 offset:27080\n"
"v_fmamk_f32 v113, v113, 0x3e553b95, v170\n"
"v_mfma_f32_16x16x32_bf16 v[228:231], a[60:63], v[52:55], v[228:231]\n"
"ds_read_b64_tr_b16 v[132:133], v182 offset:27848\n"
"v_cvt_pk_bf16_f32 v159, v214, v215\n"
"v_mfma_f32_16x16x32_bf16 v[204:207], a[64:67], v[52:55], v[204:207]\n"
"s_add_u32 m0, s34, 6144\n"
"ds_read_b64_tr_b16 v[134:135], v183 offset:27848\n"
"buffer_load_dwordx4 v190, s[8:11], s32 offen lds\n"
"v_mfma_f32_16x16x32_bf16 v[220:223], a[68:71], v[52:55], v[220:223]\n"
"v_fmamk_f32 v114, v114, 0x3e553b95, v170\n"
"v_fmamk_f32 v115, v115, 0x3e553b95, v170\n"
"v_mfma_f32_16x16x32_bf16 v[216:219], a[80:83], v[32:35], 0\n"
"v_exp_f32 v112, v112\n"
"v_mfma_f32_16x16x32_bf16 v[216:219], a[92:95], v[36:39], v[216:219]\n"
"v_exp_f32 v113, v113\n"
"v_mfma_f32_16x16x32_bf16 v[216:219], a[104:107], v[40:43], v[216:219]\n"
"ds_read_b128 v[244:247], v178 offset:13504\n"
"v_fmamk_f32 v116, v116, 0x3e553b95, v170\n"
"v_mfma_f32_16x16x32_bf16 v[208:211], a[112:115], v[44:47], v[208:211]\n"
"v_exp_f32 v114, v114\n"
"v_mfma_f32_16x16x32_bf16 v[216:219], a[116:119], v[44:47], v[216:219]\n"
"v_exp_f32 v115, v115\n"
"v_mfma_f32_16x16x32_bf16 v[224:227], a[120:123], v[48:51], v[224:227]\n"
"v_cvt_pk_bf16_f32 v90, v112, v114\n"
"v_cvt_pk_bf16_f32 v91, v113, v115\n"
"v_mfma_f32_16x16x32_bf16 v[208:211], a[124:127], v[48:51], v[208:211]\n"
"ds_read_b128 v[112:115], v179 offset:13504\n"
"v_fmamk_f32 v117, v117, 0x3e553b95, v170\n"
"v_mfma_f32_16x16x32_bf16 v[216:219], a[128:131], v[48:51], v[216:219]\n"
"v_fmamk_f32 v118, v118, 0x3e553b95, v170\n"
"v_fmamk_f32 v119, v119, 0x3e553b95, v170\n"
"v_mfma_f32_16x16x32_bf16 v[224:227], a[132:135], v[52:55], v[224:227]\n"
"s_add_u32 m0, s34, 9216\n"
"v_exp_f32 v116, v116\n"
"v_mfma_f32_16x16x32_bf16 v[208:211], a[136:139], v[52:55], v[208:211]\n"
"buffer_load_dwordx4 v191, s[8:11], s32 offen lds\n"
"ds_read_b128 v[248:251], v178 offset:27008\n"
"v_mfma_f32_16x16x32_bf16 v[216:219], a[140:143], v[52:55], v[216:219]\n"
"s_waitcnt lgkmcnt(2)\n"
"v_exp_f32 v117, v117\n"
"v_mfma_f32_16x16x32_bf16 v[244:247], v[244:247], v[148:151], 0\n"
"s_add_u32 s32, s32, s33\n"
"v_exp_f32 v118, v118\n"
"v_mfma_f32_16x16x32_bf16 v[212:215], a[144:147], v[32:35], 0\n"
"v_exp_f32 v119, v119\n"
"v_mfma_f32_16x16x32_bf16 v[212:215], a[156:159], v[36:39], v[212:215]\n"
"v_cvt_pk_bf16_f32 v92, v116, v118\n"
"v_cvt_pk_bf16_f32 v93, v117, v119\n"
"v_mfma_f32_16x16x32_bf16 v[212:215], a[168:171], v[40:43], v[212:215]\n"
"s_waitcnt lgkmcnt(1)\n"
"ds_read_b128 v[116:119], v179 offset:27008\n"
"v_cvt_pk_bf16_f32 v228, v228, v229\n"
"v_smfmac_f32_16x16x64_bf16 v[252:255], v[90:93], v[136:143], v66\n"
"ds_read_b128 v[136:139], v180\n"
"v_smfmac_f32_16x16x64_bf16 v[98:101], v[90:93], v[120:127], v66\n"
"ds_read_b128 v[120:123], v181\n"
"v_smfmac_f32_16x16x64_bf16 v[106:109], v[90:93], v[128:135], v66\n"
"s_waitcnt lgkmcnt(3)\n"
"ds_read_b64_tr_b16 v[128:129], v182 offset:13632\n"
"v_mfma_f32_16x16x32_bf16 v[90:93], v[172:175], v[90:93], v[82:85]\n"
"v_cvt_pk_bf16_f32 v229, v230, v231\n"
"v_cvt_pk_bf16_f32 v252, v252, v253\n"
"v_mfma_f32_16x16x32_bf16 v[112:115], v[112:115], v[148:151], 0\n"
"s_add_u32 m0, s34, 7168\n"
"ds_read_b64 v[82:83], v178 offset:13568\n"
"buffer_load_dwordx4 v190, s[8:11], s0 offen lds\n"
"v_mfma_f32_16x16x32_bf16 v[248:251], v[248:251], v[148:151], 0\n"
"ds_read_b64 v[84:85], v179 offset:13568\n"
"ds_read_b64 v[92:93], v179 offset:27072\n"
"v_mfma_f32_16x16x32_bf16 v[212:215], a[180:183], v[44:47], v[212:215]\n"
"v_rcp_f32 v171, v90\n"
"v_mfma_f32_16x16x32_bf16 v[212:215], a[192:195], v[48:51], v[212:215]\n"
"s_waitcnt lgkmcnt(6)\n"
"ds_read_b64 v[90:91], v178 offset:27072\n"
"v_cvt_pk_bf16_f32 v171, v171, v171\n"
"v_mfma_f32_16x16x32_bf16 v[116:119], v[116:119], v[148:151], 0\n"
"s_waitcnt lgkmcnt(6)\n"
"v_and_b32 v64, v171, v176\n"
"v_and_b32 v65, v171, v177\n"
"v_mfma_f32_16x16x32_bf16 v[136:139], v[136:139], v[148:151], 0\n"
"s_waitcnt lgkmcnt(5)\n"
"v_cvt_pk_bf16_f32 v253, v254, v255\n"
"v_cvt_pk_bf16_f32 v98, v98, v99\n"
"v_mfma_f32_16x16x32_bf16 v[120:123], v[120:123], v[148:151], 0\n"
"v_cvt_pk_bf16_f32 v99, v100, v101\n"
"v_cvt_pk_bf16_f32 v106, v106, v107\n"
"v_mfma_f32_16x16x16_bf16 v[252:255], v[252:253], v[64:65], 0\n"
"v_cvt_pk_bf16_f32 v107, v108, v109\n"
"v_cvt_pk_bf16_f32 v230, v204, v205\n"
"v_mfma_f32_16x16x16_bf16 v[98:101], v[98:99], v[64:65], 0\n"
"s_waitcnt lgkmcnt(3)\n"
"ds_read_b64_tr_b16 v[130:131], v183 offset:13632\n"
"ds_read_b64_tr_b16 v[132:133], v182 offset:14400\n"
"v_mfma_f32_16x16x16_bf16 v[106:109], v[106:107], v[64:65], 0\n"
"s_waitcnt lgkmcnt(4)\n"
"v_cvt_pk_bf16_f32 v252, v252, v253\n"
"v_cvt_pk_bf16_f32 v253, v254, v255\n"
"v_mfma_f32_16x16x32_bf16 v[244:247], v[80:83], v[160:163], v[244:247]\n"
"s_waitcnt lgkmcnt(3)\n"
"v_cvt_pk_bf16_f32 v254, v98, v99\n"
"v_cvt_pk_bf16_f32 v255, v100, v101\n"
"v_mfma_f32_16x16x32_bf16 v[112:115], v[84:87], v[162:165], v[112:115]\n"
"s_waitcnt lgkmcnt(2)\n"
"buffer_store_dwordx4 v[252:255], v192, s[28:31], s37 offen sc1\n"
"v_mfma_f32_16x16x32_bf16 v[248:251], v[88:91], v[160:163], v[248:251]\n"
"s_add_u32 m0, s34, 10240\n"
"ds_read_b64 v[98:99], v180 offset:64\n"
"ds_read_b64 v[100:101], v181 offset:64\n"
"v_mfma_f32_16x16x32_bf16 v[116:119], v[92:95], v[162:165], v[116:119]\n"
"v_cvt_pk_bf16_f32 v106, v106, v107\n"
"v_cvt_pk_bf16_f32 v107, v108, v109\n"
"v_mfma_f32_16x16x32_bf16 v[212:215], a[204:207], v[52:55], v[212:215]\n"
"buffer_load_dwordx4 v191, s[8:11], s0 offen lds\n"
"s_add_u32 s0, s0, s33\n"
"v_cvt_pk_bf16_f32 v231, v206, v207\n"
"v_mfma_f32_16x16x32_bf16 v[204:207], a[148:151], v[32:35], 0\n"
"ds_read_b64_tr_b16 v[134:135], v183 offset:14400\n"
"v_cvt_pk_bf16_f32 v220, v220, v221\n"
"v_mfma_f32_16x16x32_bf16 v[204:207], a[160:163], v[36:39], v[204:207]\n"
"v_cvt_pk_bf16_f32 v221, v222, v223\n"
"v_max_f32 v244, v244, v112\n"
"v_mfma_f32_16x16x32_bf16 v[204:207], a[172:175], v[40:43], v[204:207]\n"
"buffer_store_dwordx2 v[106:107], v193, s[28:31], s37 offen offset:1024 sc1\n"
"v_mfma_f32_16x16x32_bf16 v[204:207], a[184:187], v[44:47], v[204:207]\n"
"s_add_u32 s37, s37, 1572864\n"
"v_max_f32 v245, v245, v113\n"
"v_max_f32 v246, v246, v114\n"
"v_mfma_f32_16x16x32_bf16 v[204:207], a[196:199], v[48:51], v[204:207]\n"
"s_waitcnt lgkmcnt(2)\n"
"ds_read_b64_tr_b16 v[112:113], v182 offset:13640\n"
"v_max_f32 v247, v247, v115\n"
"v_mfma_f32_16x16x32_bf16 v[136:139], v[96:99], v[160:163], v[136:139]\n"
"s_waitcnt lgkmcnt(2)\n"
"ds_read_b64_tr_b16 v[114:115], v183 offset:13640\n"
"v_max3_f32 v170, v244, v245, v246\n"
"v_mfma_f32_16x16x32_bf16 v[120:123], v[100:103], v[162:165], v[120:123]\n"
"v_max_f32 v248, v248, v116\n"
"v_max_f32 v249, v249, v117\n"
"v_mfma_f32_16x16x32_bf16 v[204:207], a[208:211], v[52:55], v[204:207]\n"
"v_max_f32 v250, v250, v118\n"
"v_max_f32 v251, v251, v119\n"
"v_max3_f32 v170, v170, v247, v248\n"
"v_max3_f32 v170, v170, v249, v250\n"
"s_add_u32 m0, s34, 8192\n"
"v_max_f32 v136, v136, v120\n"
"buffer_load_dwordx4 v190, s[8:11], s1 offen lds\n"
"s_waitcnt vmcnt(10)\n"
"s_barrier\n"
"ds_read_b128 v[68:71], v192 offset:55296\n"
"ds_read_b128 v[72:75], v192 offset:56320\n"
"ds_read_b128 v[76:79], v192 offset:57344\n"
"v_max_f32 v137, v137, v121\n"
"v_cvt_pk_bf16_f32 v224, v224, v225\n"
"v_max_f32 v138, v138, v122\n"
"ds_write_b128 v187, v[228:231]\n"
"v_mfma_f32_16x16x32_bf16 v[228:231], a[152:155], v[32:35], 0\n"
"v_max_f32 v139, v139, v123\n"
"v_cvt_pk_bf16_f32 v225, v226, v227\n"
"v_mfma_f32_16x16x32_bf16 v[228:231], a[164:167], v[36:39], v[228:231]\n"
"v_max3_f32 v170, v170, v251, v136\n"
"v_cvt_pk_bf16_f32 v226, v208, v209\n"
"v_mfma_f32_16x16x32_bf16 v[228:231], a[176:179], v[40:43], v[228:231]\n"
"ds_read_b128 v[32:35], v188\n"
"ds_read_b128 v[36:39], v189\n"
"v_mfma_f32_16x16x32_bf16 v[228:231], a[188:191], v[44:47], v[228:231]\n"
"ds_read_b128 v[40:43], v188 offset:1024\n"
"v_max3_f32 v170, v170, v137, v138\n"
"v_mfma_f32_16x16x32_bf16 v[228:231], a[200:203], v[48:51], v[228:231]\n"
"s_waitcnt lgkmcnt(6)\n"
"ds_read_b128 v[44:47], v189 offset:1024\n"
"v_cvt_pk_bf16_f32 v227, v210, v211\n"
"v_mfma_f32_16x16x32_bf16 v[240:243], a[224:227], v[68:71], 0\n"
"s_waitcnt lgkmcnt(6)\n"
"ds_write_b128 v202, v[56:59] offset:36864\n"
"v_mfma_f32_16x16x32_bf16 v[240:243], a[236:239], v[72:75], v[240:243]\n"
"s_waitcnt lgkmcnt(6)\n"
".Lic4%=:\n"
"s_cbranch_execz .Lib1%=\n"
"v_cvt_pk_bf16_f32 v222, v216, v217\n"
"v_cvt_pk_bf16_f32 v223, v218, v219\n"
"v_mfma_f32_16x16x32_bf16 v[240:243], a[248:251], v[76:79], v[240:243]\n"
"s_add_u32 m0, s34, 11264\n"
"v_mov_b64 v[252:253], 0\n"
"v_mov_b64 v[254:255], 0\n"
"v_mfma_f32_16x16x32_bf16 v[228:231], a[212:215], v[52:55], v[228:231]\n"
"buffer_load_dwordx4 v191, s[8:11], s1 offen lds\n"
"ds_read_b128 v[140:143], v180 offset:13504\n"
"ds_read_b128 v[124:127], v181 offset:13504\n"
"ds_read_b64 v[106:107], v180 offset:13568\n"
"v_mov_b64 v[98:99], 0\n"
"v_mov_b64 v[100:101], 0\n"
"s_waitcnt lgkmcnt(7)\n"
"v_mfma_f32_16x16x32_bf16 v[208:211], a[0:3], v[32:35], 0\n"
"s_waitcnt lgkmcnt(6)\n"
"ds_read_b64 v[108:109], v181 offset:13568\n"
"s_add_u32 s1, s1, s33\n"
"v_mfma_f32_16x16x32_bf16 v[216:219], a[4:7], v[32:35], 0\n"
"ds_read_b64_tr_b16 v[20:21], v236 offset:12288\n"
"v_mfma_f32_16x16x32_bf16 v[208:211], a[12:15], v[36:39], v[208:211]\n"
"s_waitcnt lgkmcnt(7)\n"
"v_mfma_f32_16x16x32_bf16 v[216:219], a[16:19], v[36:39], v[216:219]\n"
"ds_read_b64_tr_b16 v[22:23], v236 offset:18432\n"
"v_mfma_f32_16x16x32_bf16 v[208:211], a[24:27], v[40:43], v[208:211]\n"
"s_waitcnt lgkmcnt(7)\n"
"v_mfma_f32_16x16x32_bf16 v[216:219], a[28:31], v[40:43], v[216:219]\n"
"ds_read_b64_tr_b16 v[24:25], v236 offset:12296\n"
"v_mfma_f32_16x16x32_bf16 v[208:211], a[36:39], v[44:47], v[208:211]\n"
"s_waitcnt lgkmcnt(6)\n"
"v_mfma_f32_16x16x32_bf16 v[140:143], v[140:143], v[148:151], 0\n"
"s_waitcnt lgkmcnt(5)\n"
"ds_read_b64_tr_b16 v[26:27], v236 offset:18440\n"
"s_add_u32 m0, s34, 0\n"
"v_mfma_f32_16x16x32_bf16 v[124:127], v[124:127], v[148:151], 0\n"
"s_waitcnt lgkmcnt(5)\n"
"buffer_load_dwordx4 v190, s[8:11], s32 offen lds\n"
"v_cvt_pk_bf16_f32 v148, v212, v213\n"
"v_mfma_f32_16x16x32_bf16 v[140:143], v[104:107], v[160:163], v[140:143]\n"
"s_waitcnt lgkmcnt(4)\n"
"ds_read_b64_tr_b16 v[28:29], v235 offset:12288\n"
"v_mov_b64 v[106:107], 0\n"
"v_mfma_f32_16x16x32_bf16 v[124:127], v[108:111], v[162:165], v[124:127]\n"
"v_mov_b64 v[108:109], 0\n"
"v_cvt_pk_bf16_f32 v149, v214, v215\n"
"v_mfma_f32_16x16x32_bf16 v[236:239], a[220:223], v[68:71], 0\n"
"ds_read_b64_tr_b16 v[30:31], v235 offset:18432\n"
"v_cvt_pk_bf16_f32 v150, v204, v205\n"
"v_mfma_f32_16x16x32_bf16 v[232:235], a[216:219], v[68:71], 0\n"
"v_cvt_pk_bf16_f32 v151, v206, v207\n"
"v_cvt_pk_bf16_f32 v162, v228, v229\n"
"v_mfma_f32_16x16x32_bf16 v[232:235], a[228:231], v[72:75], v[232:235]\n"
"ds_read_b128 v[68:71], v192 offset:58368\n"
"v_max_f32 v140, v140, v124\n"
"v_mfma_f32_16x16x32_bf16 v[236:239], a[232:235], v[72:75], v[236:239]\n"
"ds_read_b128 v[72:75], v192 offset:59392\n"
"v_max_f32 v141, v141, v125\n"
"v_mfma_f32_16x16x32_bf16 v[232:235], a[240:243], v[76:79], v[232:235]\n"
"ds_read_b64_tr_b16 v[116:117], v182 offset:14408\n"
"v_max_f32 v142, v142, v126\n"
"v_mfma_f32_16x16x32_bf16 v[236:239], a[244:247], v[76:79], v[236:239]\n"
"s_waitcnt vmcnt(10)\n"
"ds_read_b128 v[76:79], v192 offset:60416\n"
"v_max_f32 v143, v143, v127\n"
"v_mfma_f32_16x16x32_bf16 v[216:219], a[40:43], v[44:47], v[216:219]\n"
"v_add3_u32 v200, v200, s50, -4\n"
"v_cmp_lt_u32 vcc, 64, v200\n"
"v_mfma_f32_16x16x32_bf16 v[212:215], a[76:79], v[32:35], 0\n"
"s_add_u32 m0, s34, 3072\n"
"ds_read_b64_tr_b16 v[118:119], v183 offset:14408\n"
"buffer_load_dwordx4 v191, s[8:11], s32 offen lds\n"
"v_mfma_f32_16x16x32_bf16 v[204:207], a[80:83], v[32:35], 0\n"
"s_cbranch_vccnz .Lp3%=\n"
".Lr3%=:\n"
"v_mfma_f32_16x16x32_bf16 v[212:215], a[88:91], v[36:39], v[212:215]\n"
"s_add_u32 s32, s32, s33\n"
"ds_write_b64 v203, v[60:61] offset:36864\n"
"v_mfma_f32_16x16x32_bf16 v[204:207], a[92:95], v[36:39], v[204:207]\n"
"s_waitcnt lgkmcnt(5)\n"
"buffer_load_dword v200, off, s[28:31], s39 sc1\n"
"v_max3_f32 v170, v170, v139, v140\n"
"v_mfma_f32_16x16x32_bf16 v[212:215], a[100:103], v[40:43], v[212:215]\n"
"ds_write_b128 v187, v[220:223] offset:64\n"
"v_mfma_f32_16x16x32_bf16 v[232:235], a[252:255], v[68:71], v[232:235]\n"
"s_waitcnt lgkmcnt(5)\n"
"buffer_load_dwordx4 v[56:59], v192, s[28:31], s38 offen sc1\n"
"v_max3_f32 v170, v170, v141, v142\n"
"v_mfma_f32_16x16x32_bf16 v[236:239], v[0:3], v[68:71], v[236:239]\n"
"v_max_f32 v171, v170, v143\n"
"v_max_f32 v170, v170, v143\n"
"v_mfma_f32_16x16x32_bf16 v[240:243], v[4:7], v[68:71], v[240:243]\n"
"ds_read_b64_tr_b16 v[120:121], v182 offset:13576\n"
"ds_read_b64_tr_b16 v[122:123], v183 offset:13576\n"
"v_mfma_f32_16x16x32_bf16 v[232:235], v[8:11], v[72:75], v[232:235]\n"
"s_waitcnt lgkmcnt(5)\n"
"buffer_load_dwordx2 v[60:61], v193, s[28:31], s38 offen offset:1024 sc1\n"
"ds_read_b64_tr_b16 v[124:125], v182 offset:14344\n"
"v_mfma_f32_16x16x32_bf16 v[240:243], v[16:19], v[72:75], v[240:243]\n"
"s_waitcnt vmcnt(13)\n"
"ds_read_b128 v[48:51], v188 offset:2048\n"
"v_permlane16_swap_b32 v170, v171\n"
"v_mfma_f32_16x16x32_bf16 v[236:239], v[12:15], v[72:75], v[236:239]\n"
"s_add_u32 s38, s38, 1572864\n"
"ds_read_b128 v[52:55], v189 offset:2048\n"
"ds_read_b64_tr_b16 v[126:127], v183 offset:14344\n"
"v_mfma_f32_16x16x32_bf16 v[232:235], v[20:23], v[76:79], v[232:235]\n"
"s_add_u32 m0, s34, 1024\n"
"v_max_f32 v170, v170, v171\n"
"v_mov_b32 v171, v170\n"
"v_mfma_f32_16x16x32_bf16 v[240:243], v[28:31], v[76:79], v[240:243]\n"
"buffer_load_dwordx4 v190, s[8:11], s0 offen lds\n"
"ds_write_b128 v187, v[224:227] offset:128\n"
"v_mfma_f32_16x16x32_bf16 v[236:239], v[24:27], v[76:79], v[236:239]\n"
"v_permlane32_swap_b32 v170, v171\n"
"v_mfma_f32_16x16x32_bf16 v[220:223], a[8:11], v[32:35], 0\n"
"v_max_f32 v170, v170, v171\n"
"v_mul_f32 v170, 0xbe553b95, v170\n"
"v_mfma_f32_16x16x32_bf16 v[220:223], a[20:23], v[36:39], v[220:223]\n"
"v_fmamk_f32 v244, v244, 0x3e553b95, v170\n"
"v_fmamk_f32 v245, v245, 0x3e553b95, v170\n"
"v_mfma_f32_16x16x32_bf16 v[220:223], a[32:35], v[40:43], v[220:223]\n"
"s_waitcnt lgkmcnt(3)\n"
"v_fmamk_f32 v246, v246, 0x3e553b95, v170\n"
"v_fmamk_f32 v247, v247, 0x3e553b95, v170\n"
"v_mfma_f32_16x16x32_bf16 v[220:223], a[44:47], v[44:47], v[220:223]\n"
"v_exp_f32 v244, v244\n"
"v_mfma_f32_16x16x32_bf16 v[208:211], a[48:51], v[48:51], v[208:211]\n"
"s_waitcnt lgkmcnt(2)\n"
"v_exp_f32 v245, v245\n"
"v_mfma_f32_16x16x32_bf16 v[216:219], a[52:55], v[48:51], v[216:219]\n"
"v_exp_f32 v246, v246\n"
"v_mfma_f32_16x16x32_bf16 v[220:223], a[56:59], v[48:51], v[220:223]\n"
"v_exp_f32 v247, v247\n"
"v_mfma_f32_16x16x32_bf16 v[208:211], a[60:63], v[52:55], v[208:211]\n"
"s_add_u32 m0, s34, 4096\n"
"v_cvt_pk_bf16_f32 v82, v244, v246\n"
"v_cvt_pk_bf16_f32 v83, v245, v247\n"
"v_mfma_f32_16x16x32_bf16 v[216:219], a[64:67], v[52:55], v[216:219]\n"
"buffer_load_dwordx4 v191, s[8:11], s0 offen lds\n"
"ds_read_b64_tr_b16 v[244:245], v182 offset:40640\n"
"v_mfma_f32_16x16x32_bf16 v[220:223], a[68:71], v[52:55], v[220:223]\n"
"s_add_u32 s0, s0, s33\n"
"ds_read_b64_tr_b16 v[246:247], v183 offset:40640\n"
"v_fmamk_f32 v248, v248, 0x3e553b95, v170\n"
"v_mfma_f32_16x16x32_bf16 v[224:227], a[72:75], v[32:35], 0\n"
"v_fmamk_f32 v249, v249, 0x3e553b95, v170\n"
"v_fmamk_f32 v250, v250, 0x3e553b95, v170\n"
"v_mfma_f32_16x16x32_bf16 v[224:227], a[84:87], v[36:39], v[224:227]\n"
"v_fmamk_f32 v251, v251, 0x3e553b95, v170\n"
"v_exp_f32 v248, v248\n"
"v_mfma_f32_16x16x32_bf16 v[224:227], a[96:99], v[40:43], v[224:227]\n"
"v_exp_f32 v249, v249\n"
"v_mfma_f32_16x16x32_bf16 v[204:207], a[104:107], v[40:43], v[204:207]\n"
"v_exp_f32 v250, v250\n"
"v_mfma_f32_16x16x32_bf16 v[224:227], a[108:111], v[44:47], v[224:227]\n"
"v_exp_f32 v251, v251\n"
"v_mfma_f32_16x16x32_bf16 v[212:215], a[112:115], v[44:47], v[212:215]\n"
"v_cvt_pk_bf16_f32 v84, v248, v250\n"
"v_cvt_pk_bf16_f32 v85, v249, v251\n"
"v_mfma_f32_16x16x32_bf16 v[204:207], a[116:119], v[44:47], v[204:207]\n"
"s_waitcnt lgkmcnt(3)\n"
"ds_read_b64_tr_b16 v[248:249], v182 offset:41408\n"
"ds_read_b64_tr_b16 v[250:251], v183 offset:41408\n"
"v_smfmac_f32_16x16x64_bf16 v[252:255], v[82:85], v[128:135], v66\n"
"ds_read_b64_tr_b16 v[128:129], v182 offset:40648\n"
"v_smfmac_f32_16x16x64_bf16 v[98:101], v[82:85], v[112:119], v66\n"
"s_add_u32 m0, s34, 2048\n"
"s_nop 0\n"
"buffer_load_dwordx4 v190, s[8:11], s1 offen lds\n"
"v_smfmac_f32_16x16x64_bf16 v[106:109], v[82:85], v[120:127], v66\n"
"ds_read_b64_tr_b16 v[130:131], v183 offset:40648\n"
"v_mfma_f32_16x16x32_bf16 v[82:85], v[172:175], v[82:85], 0\n"
"ds_read_b64_tr_b16 v[132:133], v182 offset:41416\n"
"ds_read_b64_tr_b16 v[134:135], v183 offset:41416\n"
"v_mfma_f32_16x16x32_bf16 v[224:227], a[120:123], v[48:51], v[224:227]\n"
"ds_read_b64_tr_b16 v[112:113], v182 offset:40584\n"
"v_cvt_pk_bf16_f32 v232, v232, v233\n"
"v_mfma_f32_16x16x32_bf16 v[212:215], a[124:127], v[48:51], v[212:215]\n"
"ds_read_b64_tr_b16 v[114:115], v183 offset:40584\n"
"v_cvt_pk_bf16_f32 v233, v234, v235\n"
"v_mfma_f32_16x16x32_bf16 v[204:207], a[128:131], v[48:51], v[204:207]\n"
"ds_read_b64_tr_b16 v[116:117], v182 offset:41352\n"
"v_cvt_pk_bf16_f32 v240, v240, v241\n"
"v_mfma_f32_16x16x32_bf16 v[224:227], a[132:135], v[52:55], v[224:227]\n"
"ds_read_b64_tr_b16 v[118:119], v183 offset:41352\n"
"v_cvt_pk_bf16_f32 v234, v236, v237\n"
"v_mfma_f32_16x16x32_bf16 v[212:215], a[136:139], v[52:55], v[212:215]\n"
"v_cvt_pk_bf16_f32 v235, v238, v239\n"
"v_cvt_pk_bf16_f32 v241, v242, v243\n"
"v_mfma_f32_16x16x32_bf16 v[204:207], a[140:143], v[52:55], v[204:207]\n"
"ds_write_b128 v196, v[232:235] offset:30720\n"
"v_fmamk_f32 v136, v136, 0x3e553b95, v170\n"
"v_fmamk_f32 v137, v137, 0x3e553b95, v170\n"
"s_add_u32 m0, s34, 5120\n"
"v_cvt_pk_bf16_f32 v163, v230, v231\n"
"v_mfma_f32_16x16x32_bf16 v[228:231], a[144:147], v[32:35], 0\n"
"buffer_load_dwordx4 v191, s[8:11], s1 offen lds\n"
"ds_write_b64 v197, v[240:241] offset:30720\n"
"v_mfma_f32_16x16x32_bf16 v[228:231], a[156:159], v[36:39], v[228:231]\n"
"s_add_u32 s1, s1, s33\n"
"v_fmamk_f32 v138, v138, 0x3e553b95, v170\n"
"v_fmamk_f32 v139, v139, 0x3e553b95, v170\n"
"v_mfma_f32_16x16x32_bf16 v[228:231], a[168:171], v[40:43], v[228:231]\n"
"v_exp_f32 v136, v136\n"
"v_mfma_f32_16x16x32_bf16 v[228:231], a[180:183], v[44:47], v[228:231]\n"
"v_exp_f32 v137, v137\n"
"v_mfma_f32_16x16x32_bf16 v[228:231], a[192:195], v[48:51], v[228:231]\n"
"v_exp_f32 v138, v138\n"
"v_mfma_f32_16x16x32_bf16 v[228:231], a[204:207], v[52:55], v[228:231]\n"
"v_exp_f32 v139, v139\n"
"v_cvt_pk_bf16_f32 v90, v136, v138\n"
"v_cvt_pk_bf16_f32 v91, v137, v139\n"
"v_fmamk_f32 v140, v140, 0x3e553b95, v170\n"
"v_fmamk_f32 v141, v141, 0x3e553b95, v170\n"
"v_fmamk_f32 v142, v142, 0x3e553b95, v170\n"
"v_fmamk_f32 v143, v143, 0x3e553b95, v170\n"
"v_exp_f32 v140, v140\n"
"v_exp_f32 v141, v141\n"
"s_waitcnt lgkmcnt(0)\n"
"s_barrier\n"
"ds_read_b128 v[76:79], v192 offset:36864\n"
"v_exp_f32 v142, v142\n"
"s_waitcnt vmcnt(11)\n"
"v_or_b32 v201, 7, s6\n"
"s_mov_b64 exec, 1\n"
"buffer_store_dword v201, off, s[28:31], s40 sc1\n"
"s_mov_b64 exec, -1\n"
"v_exp_f32 v143, v143\n"
"v_cvt_pk_bf16_f32 v92, v140, v142\n"
"v_cvt_pk_bf16_f32 v93, v141, v143\n"
"v_cvt_pk_bf16_f32 v208, v208, v209\n"
"v_cvt_pk_bf16_f32 v209, v210, v211\n"
"v_smfmac_f32_16x16x64_bf16 v[252:255], v[90:93], v[244:251], v66\n"
"ds_read_b128 v[120:123], v178 offset:27008\n"
"v_smfmac_f32_16x16x64_bf16 v[98:101], v[90:93], v[128:135], v66\n"
"ds_read_b128 v[136:139], v179 offset:27008\n"
"v_smfmac_f32_16x16x64_bf16 v[106:109], v[90:93], v[112:119], v66\n"
"ds_read_b128 v[124:127], v180\n"
"v_mfma_f32_16x16x32_bf16 v[90:93], v[172:175], v[90:93], v[82:85]\n"
"s_waitcnt lgkmcnt(3)\n"
"ds_read_b128 v[140:143], v181\n"
"ds_read_b128 v[244:247], v180 offset:13504\n"
"v_mfma_f32_16x16x32_bf16 v[232:235], a[216:219], v[76:79], 0\n"
"ds_read_b64 v[82:83], v178 offset:27072\n"
"v_cvt_pk_bf16_f32 v252, v252, v253\n"
"v_mfma_f32_16x16x32_bf16 v[236:239], a[220:223], v[76:79], 0\n"
"ds_read_b64 v[84:85], v179 offset:27072\n"
"v_cvt_pk_bf16_f32 v253, v254, v255\n"
"v_mfma_f32_16x16x32_bf16 v[240:243], a[224:227], v[76:79], 0\n"
"ds_read_b128 v[76:79], v192 offset:39936\n"
"v_rcp_f32 v171, v90\n"
"v_cvt_pk_bf16_f32 v98, v98, v99\n"
"v_cvt_pk_bf16_f32 v171, v171, v171\n"
"v_and_b32 v64, v171, v176\n"
"v_and_b32 v65, v171, v177\n"
"v_cvt_pk_bf16_f32 v99, v100, v101\n"
"v_cvt_pk_bf16_f32 v106, v106, v107\n"
"v_mfma_f32_16x16x16_bf16 v[252:255], v[252:253], v[64:65], 0\n"
"s_waitcnt lgkmcnt(6)\n"
"ds_read_b64 v[90:91], v180 offset:64\n"
"ds_read_b64 v[92:93], v181 offset:64\n"
"v_mfma_f32_16x16x16_bf16 v[98:101], v[98:99], v[64:65], 0\n"
"s_waitcnt lgkmcnt(7)\n"
"ds_read_b128 v[128:131], v181 offset:13504\n"
"v_cvt_pk_bf16_f32 v107, v108, v109\n"
"v_mfma_f32_16x16x32_bf16 v[120:123], v[120:123], v[152:155], 0\n"
"s_waitcnt lgkmcnt(7)\n"
"ds_read_b128 v[248:251], v180 offset:27008\n"
"v_cvt_pk_bf16_f32 v252, v252, v253\n"
"v_mfma_f32_16x16x16_bf16 v[106:109], v[106:107], v[64:65], 0\n"
"s_waitcnt lgkmcnt(7)\n"
"ds_read_b128 v[132:135], v181 offset:27008\n"
"v_cvt_pk_bf16_f32 v253, v254, v255\n"
"v_mfma_f32_16x16x32_bf16 v[136:139], v[136:139], v[152:155], 0\n"
"s_waitcnt lgkmcnt(7)\n"
"ds_read_b128 v[68:71], v202 offset:30720\n"
"v_cvt_pk_bf16_f32 v254, v98, v99\n"
"v_mfma_f32_16x16x32_bf16 v[120:123], v[80:83], v[164:167], v[120:123]\n"
"s_waitcnt lgkmcnt(7)\n"
"ds_read_b64 v[98:99], v180 offset:13568\n"
"v_cvt_pk_bf16_f32 v255, v100, v101\n"
"v_mfma_f32_16x16x32_bf16 v[136:139], v[84:87], v[166:169], v[136:139]\n"
"buffer_store_dwordx4 v[252:255], v192, s[28:31], s37 offen sc1\n"
"v_mfma_f32_16x16x32_bf16 v[124:127], v[124:127], v[152:155], 0\n"
"s_waitcnt lgkmcnt(6)\n"
"ds_read_b64 v[100:101], v181 offset:13568\n"
"v_cvt_pk_bf16_f32 v106, v106, v107\n"
"v_mfma_f32_16x16x32_bf16 v[140:143], v[140:143], v[152:155], 0\n"
"s_waitcnt lgkmcnt(6)\n"
"v_cvt_pk_bf16_f32 v107, v108, v109\n"
"v_cvt_pk_bf16_f32 v210, v216, v217\n"
"v_mfma_f32_16x16x32_bf16 v[124:127], v[88:91], v[164:167], v[124:127]\n"
"s_waitcnt lgkmcnt(5)\n"
"buffer_store_dwordx2 v[106:107], v193, s[28:31], s37 offen offset:1024 sc1\n"
"v_mfma_f32_16x16x32_bf16 v[140:143], v[92:95], v[166:169], v[140:143]\n"
"s_waitcnt lgkmcnt(4)\n"
"ds_read_b64 v[106:107], v180 offset:27072\n"
"ds_read_b64 v[108:109], v181 offset:27072\n"
"v_mfma_f32_16x16x32_bf16 v[244:247], v[244:247], v[152:155], 0\n"
"s_waitcnt lgkmcnt(5)\n"
"ds_read_b64_tr_b16 v[112:113], v182 offset:27136\n"
"v_cvt_pk_bf16_f32 v211, v218, v219\n"
"v_mfma_f32_16x16x32_bf16 v[128:131], v[128:131], v[152:155], 0\n"
"s_waitcnt lgkmcnt(5)\n"
"ds_write_b128 v186, v[208:211] offset:6752\n"
"v_mfma_f32_16x16x32_bf16 v[248:251], v[248:251], v[152:155], 0\n"
"s_waitcnt lgkmcnt(5)\n"
"buffer_store_dwordx4 v[68:71], v194, s[24:27], s36 offen sc1\n"
"v_mfma_f32_16x16x32_bf16 v[244:247], v[96:99], v[164:167], v[244:247]\n"
"s_waitcnt lgkmcnt(4)\n"
"ds_read_b128 v[68:71], v192 offset:37888\n"
"ds_read_b64_tr_b16 v[114:115], v183 offset:27136\n"
"v_mfma_f32_16x16x32_bf16 v[128:131], v[100:103], v[166:169], v[128:131]\n"
"s_add_u32 s37, s37, 1572864\n"
"ds_read_b128 v[72:75], v202 offset:34816\n"
"v_cvt_pk_bf16_f32 v220, v220, v221\n"
"v_mfma_f32_16x16x32_bf16 v[132:135], v[132:135], v[152:155], 0\n"
"s_waitcnt lgkmcnt(6)\n"
"ds_read_b64_tr_b16 v[116:117], v182 offset:27904\n"
"v_cvt_pk_bf16_f32 v221, v222, v223\n"
"v_mfma_f32_16x16x32_bf16 v[248:251], v[104:107], v[164:167], v[248:251]\n"
"s_waitcnt lgkmcnt(6)\n"
"ds_read_b64_tr_b16 v[118:119], v183 offset:27904\n"
"v_max_f32 v120, v120, v136\n"
"v_mfma_f32_16x16x32_bf16 v[132:135], v[108:111], v[166:169], v[132:135]\n"
"v_max_f32 v121, v121, v137\n"
"v_max_f32 v122, v122, v138\n"
"v_mfma_f32_16x16x32_bf16 v[216:219], a[148:151], v[32:35], 0\n"
"ds_read_b64_tr_b16 v[136:137], v182 offset:27144\n"
"v_max_f32 v123, v123, v139\n"
"v_mfma_f32_16x16x32_bf16 v[208:211], a[152:155], v[32:35], 0\n"
"ds_read_b128 v[32:35], v188 offset:6144\n"
"ds_read_b64_tr_b16 v[138:139], v183 offset:27144\n"
"v_mfma_f32_16x16x32_bf16 v[216:219], a[160:163], v[36:39], v[216:219]\n"
"v_max3_f32 v170, v120, v121, v122\n"
"v_max_f32 v124, v124, v140\n"
"v_mfma_f32_16x16x32_bf16 v[208:211], a[164:167], v[36:39], v[208:211]\n"
"s_waitcnt lgkmcnt(7)\n"
"ds_read_b128 v[36:39], v189 offset:6144\n"
"v_max_f32 v125, v125, v141\n"
"v_mfma_f32_16x16x32_bf16 v[232:235], a[228:231], v[68:71], v[232:235]\n"
"ds_read_b64_tr_b16 v[140:141], v182 offset:27912\n"
"v_max_f32 v126, v126, v142\n"
"v_mfma_f32_16x16x32_bf16 v[236:239], a[232:235], v[68:71], v[236:239]\n"
"s_waitcnt lgkmcnt(7)\n"
"buffer_store_dwordx4 v[72:75], v195, s[24:27], s36 offen sc1\n"
"v_mfma_f32_16x16x32_bf16 v[240:243], a[236:239], v[68:71], v[240:243]\n"
"s_add_u32 s36, s36, s33\n"
"ds_read_b128 v[72:75], v192 offset:38912\n"
"ds_read_b128 v[68:71], v192 offset:40960\n"
"v_mfma_f32_16x16x32_bf16 v[216:219], a[172:175], v[40:43], v[216:219]\n"
"v_max_f32 v127, v127, v143\n"
"v_max3_f32 v170, v170, v123, v124\n"
"v_mfma_f32_16x16x32_bf16 v[208:211], a[176:179], v[40:43], v[208:211]\n"
"ds_read_b128 v[40:43], v188 offset:7168\n"
"ds_read_b64_tr_b16 v[142:143], v183 offset:27912\n"
"v_mfma_f32_16x16x32_bf16 v[216:219], a[184:187], v[44:47], v[216:219]\n"
"s_waitcnt vmcnt(10)\n"
"v_max3_f32 v170, v170, v125, v126\n"
"v_max_f32 v244, v244, v128\n"
"v_mfma_f32_16x16x32_bf16 v[208:211], a[188:191], v[44:47], v[208:211]\n"
"s_waitcnt lgkmcnt(5)\n"
"v_add3_u32 v200, v200, s50, -5\n"
"v_cmp_lt_u32 vcc, 64, v200\n"
"v_mfma_f32_16x16x32_bf16 v[216:219], a[196:199], v[48:51], v[216:219]\n"
"s_add_u32 m0, s34, 6144\n"
"ds_read_b128 v[44:47], v189 offset:7168\n"
"buffer_load_dwordx4 v190, s[8:11], s32 offen lds\n"
"v_mfma_f32_16x16x32_bf16 v[208:211], a[200:203], v[48:51], v[208:211]\n"
"s_waitcnt vmcnt(10)\n"
"ds_read_b128 v[48:51], v188 offset:8192\n"
"ds_write_b128 v202, v[56:59] offset:43008\n"
"v_mfma_f32_16x16x32_bf16 v[216:219], a[208:211], v[52:55], v[216:219]\n"
"s_waitcnt lgkmcnt(6)\n"
"v_max_f32 v245, v245, v129\n"
"v_max_f32 v246, v246, v130\n"
"v_mfma_f32_16x16x32_bf16 v[232:235], a[240:243], v[72:75], v[232:235]\n"
"s_cbranch_vccnz .Lp4%=\n"
".Lr4%=:\n"
"v_mfma_f32_16x16x32_bf16 v[236:239], a[244:247], v[72:75], v[236:239]\n"
"buffer_load_dwordx4 v[56:59], v192, s[28:31], s38 offen sc1\n"
"ds_write_b64 v203, v[60:61] offset:43008\n"
"v_mfma_f32_16x16x32_bf16 v[240:243], a[248:251], v[72:75], v[240:243]\n"
"v_max_f32 v247, v247, v131\n"
"v_cvt_pk_bf16_f32 v224, v224, v225\n"
"v_mfma_f32_16x16x32_bf16 v[232:235], a[252:255], v[76:79], v[232:235]\n"
"s_waitcnt lgkmcnt(6)\n"
"ds_read_b128 v[72:75], v192 offset:41984\n"
"ds_read_b64_tr_b16 v[128:129], v182 offset:27080\n"
"v_mfma_f32_16x16x32_bf16 v[240:243], v[4:7], v[76:79], v[240:243]\n"
"buffer_load_dwordx2 v[60:61], v193, s[28:31], s38 offen offset:1024 sc1\n"
"ds_read_b64_tr_b16 v[130:131], v183 offset:27080\n"
"v_mfma_f32_16x16x32_bf16 v[236:239], v[0:3], v[76:79], v[236:239]\n"
"s_add_u32 s38, s38, 1572864\n"
"v_max3_f32 v170, v170, v127, v244\n"
"v_max3_f32 v170, v170, v245, v246\n"
"v_mfma_f32_16x16x32_bf16 v[232:235], v[8:11], v[68:71], v[232:235]\n"
"v_cvt_pk_bf16_f32 v225, v226, v227\n"
"v_max_f32 v248, v248, v132\n"
"v_mfma_f32_16x16x32_bf16 v[240:243], v[16:19], v[68:71], v[240:243]\n"
"s_waitcnt lgkmcnt(6)\n"
"v_cvt_pk_bf16_f32 v226, v212, v213\n"
"v_max_f32 v249, v249, v133\n"
"v_mfma_f32_16x16x32_bf16 v[236:239], v[12:15], v[68:71], v[236:239]\n"
"s_add_u32 m0, s34, 9216\n"
"ds_read_b64_tr_b16 v[132:133], v182 offset:27848\n"
"buffer_load_dwordx4 v191, s[8:11], s32 offen lds\n"
"v_mfma_f32_16x16x32_bf16 v[208:211], a[212:215], v[52:55], v[208:211]\n"
"s_add_u32 s32, s32, s33\n"
"ds_read_b128 v[52:55], v189 offset:8192\n"
"v_cvt_pk_bf16_f32 v227, v214, v215\n"
"v_mfma_f32_16x16x32_bf16 v[212:215], a[0:3], v[32:35], 0\n"
"ds_write_b128 v186, v[224:227] offset:6880\n"
"v_mfma_f32_16x16x32_bf16 v[212:215], a[12:15], v[36:39], v[212:215]\n"
"s_waitcnt lgkmcnt(5)\n"
"v_max_f32 v250, v250, v134\n"
"v_max_f32 v251, v251, v135\n"
"v_mfma_f32_16x16x32_bf16 v[232:235], v[20:23], v[72:75], v[232:235]\n"
"v_cvt_pk_bf16_f32 v222, v204, v205\n"
"v_max3_f32 v170, v170, v247, v248\n"
"v_mfma_f32_16x16x32_bf16 v[240:243], v[28:31], v[72:75], v[240:243]\n"
"ds_read_b64_tr_b16 v[134:135], v183 offset:27848\n"
"v_cvt_pk_bf16_f32 v223, v206, v207\n"
"v_mfma_f32_16x16x32_bf16 v[236:239], v[24:27], v[72:75], v[236:239]\n"
"ds_write_b128 v186, v[220:223] offset:6816\n"
"v_mfma_f32_16x16x32_bf16 v[204:207], a[4:7], v[32:35], 0\n"
"v_max3_f32 v170, v170, v249, v250\n"
"v_mov_b64 v[252:253], 0\n"
"v_mfma_f32_16x16x32_bf16 v[220:223], a[8:11], v[32:35], 0\n"
"v_mov_b64 v[254:255], 0\n"
"v_mov_b64 v[98:99], 0\n"
"v_mfma_f32_16x16x32_bf16 v[204:207], a[16:19], v[36:39], v[204:207]\n"
"s_waitcnt lgkmcnt(3)\n"
"v_mov_b64 v[100:101], 0\n"
"v_mov_b64 v[106:107], 0\n"
"v_mfma_f32_16x16x32_bf16 v[220:223], a[20:23], v[36:39], v[220:223]\n"
"s_add_u32 m0, s34, 7168\n"
"v_mov_b64 v[108:109], 0\n"
"v_max_f32 v171, v170, v251\n"
"v_mfma_f32_16x16x32_bf16 v[212:215], a[24:27], v[40:43], v[212:215]\n"
"buffer_load_dwordx4 v190, s[8:11], s0 offen lds\n"
"v_max_f32 v170, v170, v251\n"
"v_mfma_f32_16x16x32_bf16 v[204:207], a[28:31], v[40:43], v[204:207]\n"
"s_nop 0\n"
"v_permlane16_swap_b32 v170, v171\n"
"v_mfma_f32_16x16x32_bf16 v[220:223], a[32:35], v[40:43], v[220:223]\n"
"v_max_f32 v170, v170, v171\n"
"v_mov_b32 v171, v170\n"
"v_mfma_f32_16x16x32_bf16 v[212:215], a[36:39], v[44:47], v[212:215]\n"
"s_nop 0\n"
"v_permlane32_swap_b32 v170, v171\n"
"v_mfma_f32_16x16x32_bf16 v[204:207], a[40:43], v[44:47], v[204:207]\n"
"v_max_f32 v170, v170, v171\n"
"v_mul_f32 v170, 0xbe553b95, v170\n"
"v_mfma_f32_16x16x32_bf16 v[220:223], a[44:47], v[44:47], v[220:223]\n"
"v_fmamk_f32 v120, v120, 0x3e553b95, v170\n"
"v_fmamk_f32 v121, v121, 0x3e553b95, v170\n"
"v_mfma_f32_16x16x32_bf16 v[212:215], a[48:51], v[48:51], v[212:215]\n"
"v_fmamk_f32 v122, v122, 0x3e553b95, v170\n"
"v_fmamk_f32 v123, v123, 0x3e553b95, v170\n"
"v_mfma_f32_16x16x32_bf16 v[204:207], a[52:55], v[48:51], v[204:207]\n"
"v_exp_f32 v120, v120\n"
"v_mfma_f32_16x16x32_bf16 v[220:223], a[56:59], v[48:51], v[220:223]\n"
"v_exp_f32 v121, v121\n"
"v_mfma_f32_16x16x32_bf16 v[212:215], a[60:63], v[52:55], v[212:215]\n"
"v_exp_f32 v122, v122\n"
"v_mfma_f32_16x16x32_bf16 v[204:207], a[64:67], v[52:55], v[204:207]\n"
"s_add_u32 m0, s34, 10240\n"
"v_exp_f32 v123, v123\n"
"v_mfma_f32_16x16x32_bf16 v[220:223], a[68:71], v[52:55], v[220:223]\n"
"buffer_load_dwordx4 v191, s[8:11], s0 offen lds\n"
"v_cvt_pk_bf16_f32 v82, v120, v122\n"
"v_mfma_f32_16x16x32_bf16 v[224:227], a[72:75], v[32:35], 0\n"
"s_add_u32 s0, s0, s33\n"
"v_cvt_pk_bf16_f32 v83, v121, v123\n"
"v_fmamk_f32 v124, v124, 0x3e553b95, v170\n"
"v_mfma_f32_16x16x32_bf16 v[224:227], a[84:87], v[36:39], v[224:227]\n"
"ds_read_b64_tr_b16 v[120:121], v182 offset:54144\n"
"ds_read_b64_tr_b16 v[122:123], v183 offset:54144\n"
"v_mfma_f32_16x16x32_bf16 v[224:227], a[96:99], v[40:43], v[224:227]\n"
"v_fmamk_f32 v125, v125, 0x3e553b95, v170\n"
"v_fmamk_f32 v126, v126, 0x3e553b95, v170\n"
"v_mfma_f32_16x16x32_bf16 v[224:227], a[108:111], v[44:47], v[224:227]\n"
"v_fmamk_f32 v127, v127, 0x3e553b95, v170\n"
"v_exp_f32 v124, v124\n"
"v_mfma_f32_16x16x32_bf16 v[224:227], a[120:123], v[48:51], v[224:227]\n"
"v_exp_f32 v125, v125\n"
"v_mfma_f32_16x16x32_bf16 v[224:227], a[132:135], v[52:55], v[224:227]\n"
"v_exp_f32 v126, v126\n"
"v_exp_f32 v127, v127\n"
"v_cvt_pk_bf16_f32 v84, v124, v126\n"
"v_cvt_pk_bf16_f32 v85, v125, v127\n"
"v_cvt_pk_bf16_f32 v232, v232, v233\n"
"v_cvt_pk_bf16_f32 v233, v234, v235\n"
"v_smfmac_f32_16x16x64_bf16 v[252:255], v[82:85], v[112:119], v66\n"
"s_waitcnt lgkmcnt(3)\n"
"ds_read_b64_tr_b16 v[124:125], v182 offset:54912\n"
"v_smfmac_f32_16x16x64_bf16 v[98:101], v[82:85], v[136:143], v66\n"
"s_add_u32 m0, s34, 8192\n"
"s_nop 0\n"
"buffer_load_dwordx4 v190, s[8:11], s1 offen lds\n"
"v_smfmac_f32_16x16x64_bf16 v[106:109], v[82:85], v[128:135], v66\n"
"ds_read_b64_tr_b16 v[126:127], v183 offset:54912\n"
"v_mfma_f32_16x16x32_bf16 v[82:85], v[172:175], v[82:85], 0\n"
"ds_read_b64_tr_b16 v[112:113], v182 offset:54152\n"
"ds_read_b64_tr_b16 v[114:115], v183 offset:54152\n"
"v_cvt_pk_bf16_f32 v240, v240, v241\n"
"v_cvt_pk_bf16_f32 v234, v236, v237\n"
"v_cvt_pk_bf16_f32 v235, v238, v239\n"
"buffer_load_dword v200, off, s[28:31], s39 sc1\n"
"ds_read_b64_tr_b16 v[116:117], v182 offset:54920\n"
"ds_read_b64_tr_b16 v[118:119], v183 offset:54920\n"
"v_cvt_pk_bf16_f32 v241, v242, v243\n"
"v_cvt_pk_bf16_f32 v152, v228, v229\n"
"v_cvt_pk_bf16_f32 v153, v230, v231\n"
"v_mfma_f32_16x16x32_bf16 v[228:231], a[76:79], v[32:35], 0\n"
"ds_read_b64_tr_b16 v[136:137], v182 offset:54088\n"
"ds_write_b128 v196, v[232:235] offset:55296\n"
"v_mfma_f32_16x16x32_bf16 v[228:231], a[88:91], v[36:39], v[228:231]\n"
"v_cvt_pk_bf16_f32 v154, v216, v217\n"
"v_cvt_pk_bf16_f32 v155, v218, v219\n"
"v_mfma_f32_16x16x32_bf16 v[216:219], a[80:83], v[32:35], 0\n"
"v_fmamk_f32 v244, v244, 0x3e553b95, v170\n"
"v_fmamk_f32 v245, v245, 0x3e553b95, v170\n"
"v_mfma_f32_16x16x32_bf16 v[216:219], a[92:95], v[36:39], v[216:219]\n"
"ds_write_b64 v197, v[240:241] offset:55296\n"
"v_mfma_f32_16x16x32_bf16 v[228:231], a[100:103], v[40:43], v[228:231]\n"
"s_add_u32 m0, s34, 11264\n"
"v_cvt_pk_bf16_f32 v166, v208, v209\n"
"v_fmamk_f32 v246, v246, 0x3e553b95, v170\n"
"v_mfma_f32_16x16x32_bf16 v[216:219], a[104:107], v[40:43], v[216:219]\n"
"buffer_load_dwordx4 v191, s[8:11], s1 offen lds\n"
"ds_read_b64_tr_b16 v[138:139], v183 offset:54088\n"
"v_mfma_f32_16x16x32_bf16 v[228:231], a[112:115], v[44:47], v[228:231]\n"
"s_add_u32 s1, s1, s33\n"
"ds_read_b64_tr_b16 v[140:141], v182 offset:54856\n"
"ds_read_b64_tr_b16 v[142:143], v183 offset:54856\n"
"v_mfma_f32_16x16x32_bf16 v[216:219], a[116:119], v[44:47], v[216:219]\n"
"v_cvt_pk_bf16_f32 v167, v210, v211\n"
"v_fmamk_f32 v247, v247, 0x3e553b95, v170\n"
"v_mfma_f32_16x16x32_bf16 v[228:231], a[124:127], v[48:51], v[228:231]\n"
"v_exp_f32 v244, v244\n"
"v_mfma_f32_16x16x32_bf16 v[216:219], a[128:131], v[48:51], v[216:219]\n"
"v_exp_f32 v245, v245\n"
"v_mfma_f32_16x16x32_bf16 v[228:231], a[136:139], v[52:55], v[228:231]\n"
"s_waitcnt lgkmcnt(3)\n"
"v_exp_f32 v246, v246\n"
"v_mfma_f32_16x16x32_bf16 v[216:219], a[140:143], v[52:55], v[216:219]\n"
"s_barrier\n"
"v_mfma_f32_16x16x32_bf16 v[208:211], a[144:147], v[32:35], 0\n"
"ds_read_b128 v[72:75], v192 offset:43008\n"
"v_cvt_pk_bf16_f32 v90, v244, v246\n"
"v_mfma_f32_16x16x32_bf16 v[208:211], a[156:159], v[36:39], v[208:211]\n"
"v_exp_f32 v247, v247\n"
"v_mfma_f32_16x16x32_bf16 v[208:211], a[168:171], v[40:43], v[208:211]\n"
"v_cvt_pk_bf16_f32 v91, v245, v247\n"
"v_fmamk_f32 v248, v248, 0x3e553b95, v170\n"
"v_mfma_f32_16x16x32_bf16 v[208:211], a[180:183], v[44:47], v[208:211]\n"
"v_fmamk_f32 v249, v249, 0x3e553b95, v170\n"
"v_fmamk_f32 v250, v250, 0x3e553b95, v170\n"
"v_mfma_f32_16x16x32_bf16 v[208:211], a[192:195], v[48:51], v[208:211]\n"
"v_fmamk_f32 v251, v251, 0x3e553b95, v170\n"
"v_exp_f32 v248, v248\n"
"v_mfma_f32_16x16x32_bf16 v[208:211], a[204:207], v[52:55], v[208:211]\n"
"s_waitcnt vmcnt(11)\n"
"v_exp_f32 v249, v249\n"
"v_or_b32 v201, 8, s6\n"
"v_exp_f32 v250, v250\n"
"v_exp_f32 v251, v251\n"
"v_cvt_pk_bf16_f32 v92, v248, v250\n"
"v_cvt_pk_bf16_f32 v93, v249, v251\n"
"s_mov_b64 exec, s[44:45]\n"
"v_add_u32 v179, 0xc0, v179\n"
"s_mov_b64 exec, -1\n"
"v_smfmac_f32_16x16x64_bf16 v[252:255], v[90:93], v[120:127], v66\n"
"s_waitcnt lgkmcnt(0)\n"
"s_mov_b64 exec, 1\n"
"buffer_store_dword v201, off, s[28:31], s40 sc1\n"
"s_mov_b64 exec, -1\n"
"v_smfmac_f32_16x16x64_bf16 v[98:101], v[90:93], v[112:119], v66\n"
"ds_read_b128 v[128:131], v180\n"
"v_smfmac_f32_16x16x64_bf16 v[106:109], v[90:93], v[136:143], v66\n"
"ds_read_b128 v[244:247], v181\n"
"v_mfma_f32_16x16x32_bf16 v[90:93], v[172:175], v[90:93], v[82:85]\n"
"ds_read_b128 v[132:135], v180 offset:13504\n"
"ds_read_b128 v[248:251], v181 offset:13504\n"
"v_mfma_f32_16x16x32_bf16 v[232:235], a[216:219], v[72:75], 0\n"
".Lic5%=:\n"
"s_cbranch_execz .Lib1%=\n"
"ds_read_b64 v[82:83], v180 offset:64\n"
"v_cvt_pk_bf16_f32 v252, v252, v253\n"
"v_mfma_f32_16x16x32_bf16 v[236:239], a[220:223], v[72:75], 0\n"
"ds_read_b64 v[84:85], v181 offset:64\n"
"v_cvt_pk_bf16_f32 v253, v254, v255\n"
"v_mfma_f32_16x16x32_bf16 v[240:243], a[224:227], v[72:75], 0\n"
"ds_read_b128 v[72:75], v192 offset:46080\n"
"v_rcp_f32 v171, v90\n"
"v_cvt_pk_bf16_f32 v98, v98, v99\n"
"v_cvt_pk_bf16_f32 v171, v171, v171\n"
"v_and_b32 v64, v171, v176\n"
"v_and_b32 v65, v171, v177\n"
"v_cvt_pk_bf16_f32 v99, v100, v101\n"
"v_cvt_pk_bf16_f32 v106, v106, v107\n"
"v_mfma_f32_16x16x16_bf16 v[252:255], v[252:253], v[64:65], 0\n"
"s_waitcnt lgkmcnt(6)\n"
"ds_read_b64 v[90:91], v180 offset:13568\n"
"ds_read_b64 v[92:93], v181 offset:13568\n"
"v_mfma_f32_16x16x16_bf16 v[98:101], v[98:99], v[64:65], 0\n"
"s_waitcnt lgkmcnt(7)\n"
"ds_read_b128 v[120:123], v180 offset:27008\n"
"v_cvt_pk_bf16_f32 v107, v108, v109\n"
"v_mfma_f32_16x16x32_bf16 v[128:131], v[128:131], v[144:147], 0\n"
"s_waitcnt lgkmcnt(7)\n"
"ds_read_b128 v[112:115], v181 offset:27008\n"
"v_cvt_pk_bf16_f32 v252, v252, v253\n"
"v_mfma_f32_16x16x16_bf16 v[106:109], v[106:107], v[64:65], 0\n"
"s_waitcnt lgkmcnt(7)\n"
"ds_read_b128 v[124:127], v178\n"
"v_cvt_pk_bf16_f32 v253, v254, v255\n"
"v_mfma_f32_16x16x32_bf16 v[244:247], v[244:247], v[144:147], 0\n"
"s_waitcnt lgkmcnt(7)\n"
"ds_read_b128 v[116:119], v179\n"
"v_cvt_pk_bf16_f32 v254, v98, v99\n"
"v_mfma_f32_16x16x32_bf16 v[128:131], v[80:83], v[156:159], v[128:131]\n"
"s_waitcnt lgkmcnt(7)\n"
"ds_read_b64 v[98:99], v180 offset:27072\n"
"v_cvt_pk_bf16_f32 v255, v100, v101\n"
"v_mfma_f32_16x16x32_bf16 v[244:247], v[84:87], v[158:161], v[244:247]\n"
"buffer_store_dwordx4 v[252:255], v192, s[28:31], s37 offen sc1\n"
"v_mfma_f32_16x16x32_bf16 v[132:135], v[132:135], v[144:147], 0\n"
"s_waitcnt lgkmcnt(6)\n"
"ds_read_b64 v[100:101], v181 offset:27072\n"
"ds_read_b128 v[76:79], v202 offset:55296\n"
"v_mfma_f32_16x16x32_bf16 v[248:251], v[248:251], v[144:147], 0\n"
"s_waitcnt lgkmcnt(7)\n"
"v_cvt_pk_bf16_f32 v106, v106, v107\n"
"v_cvt_pk_bf16_f32 v107, v108, v109\n"
"v_mfma_f32_16x16x32_bf16 v[132:135], v[88:91], v[156:159], v[132:135]\n"
"s_waitcnt lgkmcnt(6)\n"
"buffer_store_dwordx2 v[106:107], v193, s[28:31], s37 offen offset:1024 sc1\n"
"v_mfma_f32_16x16x32_bf16 v[248:251], v[92:95], v[158:161], v[248:251]\n"
"s_waitcnt lgkmcnt(5)\n"
"ds_read_b64 v[106:107], v178 offset:64\n"
"ds_read_b64 v[108:109], v179 offset:64\n"
"v_mfma_f32_16x16x32_bf16 v[120:123], v[120:123], v[144:147], 0\n"
"s_waitcnt lgkmcnt(6)\n"
"v_cvt_pk_bf16_f32 v212, v212, v213\n"
"v_cvt_pk_bf16_f32 v213, v214, v215\n"
"v_mfma_f32_16x16x32_bf16 v[112:115], v[112:115], v[144:147], 0\n"
"s_waitcnt lgkmcnt(5)\n"
"ds_read_b128 v[68:71], v202 offset:59392\n"
"ds_read_b64_tr_b16 v[136:137], v182 offset:40640\n"
"v_mfma_f32_16x16x32_bf16 v[124:127], v[124:127], v[144:147], 0\n"
"s_waitcnt lgkmcnt(6)\n"
"ds_read_b64_tr_b16 v[138:139], v183 offset:40640\n"
"v_cvt_pk_bf16_f32 v214, v204, v205\n"
"v_mfma_f32_16x16x32_bf16 v[120:123], v[96:99], v[156:159], v[120:123]\n"
"s_waitcnt lgkmcnt(6)\n"
"ds_read_b64_tr_b16 v[140:141], v182 offset:41408\n"
"v_cvt_pk_bf16_f32 v215, v206, v207\n"
"v_mfma_f32_16x16x32_bf16 v[116:119], v[116:119], v[144:147], 0\n"
"s_waitcnt lgkmcnt(6)\n"
"ds_write_b128 v186, v[212:215] offset:20256\n"
"v_mfma_f32_16x16x32_bf16 v[112:115], v[100:103], v[158:161], v[112:115]\n"
"s_waitcnt vmcnt(10)\n"
"buffer_store_dwordx4 v[76:79], v194, s[24:27], s36 offen sc1\n"
"v_mfma_f32_16x16x32_bf16 v[204:207], a[148:151], v[32:35], 0\n"
"s_waitcnt vmcnt(10) lgkmcnt(5)\n"
"ds_read_b128 v[76:79], v192 offset:44032\n"
"ds_write_b128 v202, v[56:59] offset:30720\n"
"v_mfma_f32_16x16x32_bf16 v[124:127], v[104:107], v[156:159], v[124:127]\n"
"s_waitcnt lgkmcnt(6)\n"
"buffer_load_dwordx4 v[56:59], v192, s[28:31], s38 offen sc1\n"
"v_cvt_pk_bf16_f32 v220, v220, v221\n"
"v_mfma_f32_16x16x32_bf16 v[116:119], v[108:111], v[158:161], v[116:119]\n"
"s_add_u32 s37, s37, 1572864\n"
"v_cvt_pk_bf16_f32 v221, v222, v223\n"
"v_max_f32 v128, v128, v244\n"
"v_mfma_f32_16x16x32_bf16 v[212:215], a[152:155], v[32:35], 0\n"
"ds_read_b128 v[32:35], v188\n"
"ds_read_b64_tr_b16 v[142:143], v183 offset:41408\n"
"v_mfma_f32_16x16x32_bf16 v[204:207], a[160:163], v[36:39], v[204:207]\n"
"ds_write_b64 v203, v[60:61] offset:30720\n"
"v_mfma_f32_16x16x32_bf16 v[212:215], a[164:167], v[36:39], v[212:215]\n"
"buffer_load_dwordx2 v[60:61], v193, s[28:31], s38 offen offset:1024 sc1\n"
"v_max_f32 v129, v129, v245\n"
"v_mfma_f32_16x16x32_bf16 v[204:207], a[172:175], v[40:43], v[204:207]\n"
"s_waitcnt lgkmcnt(4)\n"
"ds_read_b128 v[36:39], v189\n"
"ds_read_b64_tr_b16 v[244:245], v182 offset:40648\n"
"v_mfma_f32_16x16x32_bf16 v[212:215], a[176:179], v[40:43], v[212:215]\n"
"s_add_u32 s38, s38, 1572864\n"
"ds_read_b128 v[40:43], v188 offset:1024\n"
"v_max_f32 v130, v130, v246\n"
"v_mfma_f32_16x16x32_bf16 v[232:235], a[228:231], v[76:79], v[232:235]\n"
"v_max_f32 v131, v131, v247\n"
"v_max3_f32 v170, v128, v129, v130\n"
"v_mfma_f32_16x16x32_bf16 v[236:239], a[232:235], v[76:79], v[236:239]\n"
"buffer_store_dwordx4 v[68:71], v195, s[24:27], s36 offen sc1\n"
"v_mfma_f32_16x16x32_bf16 v[240:243], a[236:239], v[76:79], v[240:243]\n"
"s_add_u32 s36, s36, s33\n"
"ds_read_b128 v[68:71], v192 offset:45056\n"
"ds_read_b128 v[76:79], v192 offset:47104\n"
"v_mfma_f32_16x16x32_bf16 v[204:207], a[184:187], v[44:47], v[204:207]\n"
"ds_read_b64_tr_b16 v[246:247], v183 offset:40648\n"
"v_max_f32 v132, v132, v248\n"
"v_mfma_f32_16x16x32_bf16 v[212:215], a[188:191], v[44:47], v[212:215]\n"
"ds_read_b128 v[44:47], v189 offset:1024\n"
"v_max_f32 v133, v133, v249\n"
"v_mfma_f32_16x16x32_bf16 v[204:207], a[196:199], v[48:51], v[204:207]\n"
"s_waitcnt lgkmcnt(6)\n"
"ds_read_b64_tr_b16 v[248:249], v182 offset:41416\n"
"v_max_f32 v134, v134, v250\n"
"v_mfma_f32_16x16x32_bf16 v[212:215], a[200:203], v[48:51], v[212:215]\n"
"s_add_u32 m0, s34, 0\n"
"ds_read_b128 v[48:51], v188 offset:2048\n"
"buffer_load_dwordx4 v190, s[8:11], s32 offen lds\n"
"v_mfma_f32_16x16x32_bf16 v[204:207], a[208:211], v[52:55], v[204:207]\n"
"s_add_u32 s32, s32, s33\n"
"v_max_f32 v135, v135, v251\n"
"v_max3_f32 v170, v170, v131, v132\n"
"v_mfma_f32_16x16x32_bf16 v[212:215], a[212:215], v[52:55], v[212:215]\n"
"ds_read_b128 v[52:55], v189 offset:2048\n"
"ds_read_b64_tr_b16 v[250:251], v183 offset:41416\n"
"v_max3_f32 v170, v170, v133, v134\n"
"v_max_f32 v120, v120, v112\n"
"v_max_f32 v121, v121, v113\n"
"v_cvt_pk_bf16_f32 v224, v224, v225\n"
"v_max_f32 v122, v122, v114\n"
"v_max_f32 v123, v123, v115\n"
"v_cvt_pk_bf16_f32 v225, v226, v227\n"
"v_max3_f32 v170, v170, v135, v120\n"
"v_cvt_pk_bf16_f32 v226, v228, v229\n"
"v_max3_f32 v170, v170, v121, v122\n"
"v_cvt_pk_bf16_f32 v227, v230, v231\n"
"v_mfma_f32_16x16x32_bf16 v[228:231], a[0:3], v[32:35], 0\n"
"s_waitcnt lgkmcnt(7)\n"
"ds_read_b64_tr_b16 v[112:113], v182 offset:40584\n"
"ds_read_b64_tr_b16 v[114:115], v183 offset:40584\n"
"v_mfma_f32_16x16x32_bf16 v[232:235], a[240:243], v[68:71], v[232:235]\n"
"ds_write_b128 v186, v[224:227] offset:20384\n"
"v_mfma_f32_16x16x32_bf16 v[236:239], a[244:247], v[68:71], v[236:239]\n"
"v_max_f32 v124, v124, v116\n"
"v_max_f32 v125, v125, v117\n"
"v_mfma_f32_16x16x32_bf16 v[240:243], a[248:251], v[68:71], v[240:243]\n"
"v_cvt_pk_bf16_f32 v222, v216, v217\n"
"v_max_f32 v126, v126, v118\n"
"v_mfma_f32_16x16x32_bf16 v[232:235], a[252:255], v[72:75], v[232:235]\n"
"s_waitcnt lgkmcnt(4)\n"
"ds_read_b128 v[68:71], v192 offset:48128\n"
"ds_read_b64_tr_b16 v[116:117], v182 offset:41352\n"
"v_mfma_f32_16x16x32_bf16 v[240:243], v[4:7], v[72:75], v[240:243]\n"
"s_add_u32 m0, s34, 3072\n"
"v_cvt_pk_bf16_f32 v223, v218, v219\n"
"v_max_f32 v127, v127, v119\n"
"v_mfma_f32_16x16x32_bf16 v[236:239], v[0:3], v[72:75], v[236:239]\n"
"buffer_load_dwordx4 v199, s[8:11], s75 offen lds\n"
"ds_write_b128 v186, v[220:223] offset:20320\n"
"v_mfma_f32_16x16x32_bf16 v[232:235], v[8:11], v[76:79], v[232:235]\n"
"v_max3_f32 v170, v170, v123, v124\n"
"v_max3_f32 v170, v170, v125, v126\n"
"v_mfma_f32_16x16x32_bf16 v[240:243], v[16:19], v[76:79], v[240:243]\n"
"v_mov_b64 v[252:253], 0\n"
"v_mov_b64 v[254:255], 0\n"
"v_mfma_f32_16x16x32_bf16 v[236:239], v[12:15], v[76:79], v[236:239]\n"
"ds_read_b64_tr_b16 v[118:119], v183 offset:41352\n"
"v_mov_b64 v[98:99], 0\n"
"v_mfma_f32_16x16x32_bf16 v[216:219], a[4:7], v[32:35], 0\n"
"v_mov_b64 v[100:101], 0\n"
"v_mov_b64 v[106:107], 0\n"
"v_mfma_f32_16x16x32_bf16 v[220:223], a[8:11], v[32:35], 0\n"
"s_waitcnt lgkmcnt(3)\n"
"v_mov_b64 v[108:109], 0\n"
"v_max_f32 v171, v170, v127\n"
"v_mfma_f32_16x16x32_bf16 v[232:235], v[20:23], v[68:71], v[232:235]\n"
"v_max_f32 v170, v170, v127\n"
"v_cvt_pk_bf16_f32 v144, v208, v209\n"
"v_mfma_f32_16x16x32_bf16 v[240:243], v[28:31], v[68:71], v[240:243]\n"
"v_permlane16_swap_b32 v170, v171\n"
"v_mfma_f32_16x16x32_bf16 v[236:239], v[24:27], v[68:71], v[236:239]\n"
"v_max_f32 v170, v170, v171\n"
"v_mov_b32 v171, v170\n"
"v_mfma_f32_16x16x32_bf16 v[228:231], a[12:15], v[36:39], v[228:231]\n"
"s_add_u32 m0, s34, 1024\n"
"v_permlane32_swap_b32 v170, v171\n"
"v_mfma_f32_16x16x32_bf16 v[216:219], a[16:19], v[36:39], v[216:219]\n"
"buffer_load_dwordx4 v190, s[8:11], s0 offen lds\n"
"v_max_f32 v170, v170, v171\n"
"v_mfma_f32_16x16x32_bf16 v[220:223], a[20:23], v[36:39], v[220:223]\n"
"s_add_u32 s0, s0, s33\n"
"v_mul_f32 v170, 0xbe553b95, v170\n"
"v_fmamk_f32 v128, v128, 0x3e553b95, v170\n"
"v_mfma_f32_16x16x32_bf16 v[228:231], a[24:27], v[40:43], v[228:231]\n"
"v_fmamk_f32 v129, v129, 0x3e553b95, v170\n"
"v_fmamk_f32 v130, v130, 0x3e553b95, v170\n"
"v_mfma_f32_16x16x32_bf16 v[216:219], a[28:31], v[40:43], v[216:219]\n"
"v_fmamk_f32 v131, v131, 0x3e553b95, v170\n"
"v_fmamk_f32 v132, v132, 0x3e553b95, v170\n"
"v_mfma_f32_16x16x32_bf16 v[220:223], a[32:35], v[40:43], v[220:223]\n"
"s_waitcnt vmcnt(11)\n"
"v_add3_u32 v200, v200, s50, -7\n"
"v_cmp_lt_u32 vcc, 64, v200\n"
"v_mfma_f32_16x16x32_bf16 v[228:231], a[36:39], v[44:47], v[228:231]\n"
"v_exp_f32 v128, v128\n"
"v_mfma_f32_16x16x32_bf16 v[216:219], a[40:43], v[44:47], v[216:219]\n"
"s_nop 1\n"
"s_cbranch_vccnz .Lp6%=\n"
".Lr6%=:\n"
"v_mfma_f32_16x16x32_bf16 v[220:223], a[44:47], v[44:47], v[220:223]\n"
"v_exp_f32 v129, v129\n"
"v_mfma_f32_16x16x32_bf16 v[228:231], a[48:51], v[48:51], v[228:231]\n"
"v_exp_f32 v130, v130\n"
"v_mfma_f32_16x16x32_bf16 v[216:219], a[52:55], v[48:51], v[216:219]\n"
"v_exp_f32 v131, v131\n"
"v_mfma_f32_16x16x32_bf16 v[220:223], a[56:59], v[48:51], v[220:223]\n"
"s_add_u32 m0, s34, 4096\n"
"v_cvt_pk_bf16_f32 v82, v128, v130\n"
"v_cvt_pk_bf16_f32 v83, v129, v131\n"
"v_mfma_f32_16x16x32_bf16 v[228:231], a[60:63], v[52:55], v[228:231]\n"
"buffer_load_dwordx4 v199, s[8:11], s76 offen lds\n"
"ds_read_b64_tr_b16 v[128:129], v184 offset:128\n"
"v_mfma_f32_16x16x32_bf16 v[216:219], a[64:67], v[52:55], v[216:219]\n"
"ds_read_b64_tr_b16 v[130:131], v185 offset:128\n"
"v_fmamk_f32 v133, v133, 0x3e553b95, v170\n"
"v_mfma_f32_16x16x32_bf16 v[220:223], a[68:71], v[52:55], v[220:223]\n"
"v_fmamk_f32 v134, v134, 0x3e553b95, v170\n"
"v_fmamk_f32 v135, v135, 0x3e553b95, v170\n"
"v_mfma_f32_16x16x32_bf16 v[224:227], a[72:75], v[32:35], 0\n"
"v_exp_f32 v132, v132\n"
"v_mfma_f32_16x16x32_bf16 v[224:227], a[84:87], v[36:39], v[224:227]\n"
"v_exp_f32 v133, v133\n"
"v_mfma_f32_16x16x32_bf16 v[224:227], a[96:99], v[40:43], v[224:227]\n"
"v_exp_f32 v134, v134\n"
"v_mfma_f32_16x16x32_bf16 v[224:227], a[108:111], v[44:47], v[224:227]\n"
"v_exp_f32 v135, v135\n"
"v_mfma_f32_16x16x32_bf16 v[224:227], a[120:123], v[48:51], v[224:227]\n"
"v_cvt_pk_bf16_f32 v84, v132, v134\n"
"v_cvt_pk_bf16_f32 v85, v133, v135\n"
"v_mfma_f32_16x16x32_bf16 v[224:227], a[132:135], v[52:55], v[224:227]\n"
"s_waitcnt lgkmcnt(2)\n"
"ds_read_b64_tr_b16 v[132:133], v184 offset:896\n"
"ds_read_b64_tr_b16 v[134:135], v185 offset:896\n"
"v_smfmac_f32_16x16x64_bf16 v[252:255], v[82:85], v[136:143], v66\n"
"ds_read_b64_tr_b16 v[136:137], v184 offset:136\n"
"v_smfmac_f32_16x16x64_bf16 v[98:101], v[82:85], v[244:251], v66\n"
"s_add_u32 m0, s34, 2048\n"
"s_nop 0\n"
"buffer_load_dwordx4 v190, s[8:11], s1 offen lds\n"
"v_smfmac_f32_16x16x64_bf16 v[106:109], v[82:85], v[112:119], v66\n"
"s_add_u32 s1, s1, s33\n"
"ds_read_b64_tr_b16 v[138:139], v185 offset:136\n"
"v_mfma_f32_16x16x32_bf16 v[82:85], v[172:175], v[82:85], 0\n"
"ds_read_b64_tr_b16 v[140:141], v184 offset:904\n"
"ds_read_b64_tr_b16 v[142:143], v185 offset:904\n"
"v_cvt_pk_bf16_f32 v232, v232, v233\n"
"v_cvt_pk_bf16_f32 v233, v234, v235\n"
"v_cvt_pk_bf16_f32 v240, v240, v241\n"
"v_cvt_pk_bf16_f32 v234, v236, v237\n"
"v_cvt_pk_bf16_f32 v235, v238, v239\n"
"v_cvt_pk_bf16_f32 v241, v242, v243\n"
"v_cvt_pk_bf16_f32 v145, v210, v211\n"
"v_mfma_f32_16x16x32_bf16 v[208:211], a[76:79], v[32:35], 0\n"
"buffer_load_dword v200, off, s[28:31], s39 sc1\n"
"ds_write_b128 v196, v[232:235] offset:49152\n"
"v_mfma_f32_16x16x32_bf16 v[208:211], a[88:91], v[36:39], v[208:211]\n"
"v_cvt_pk_bf16_f32 v146, v204, v205\n"
"v_cvt_pk_bf16_f32 v147, v206, v207\n"
"v_mfma_f32_16x16x32_bf16 v[204:207], a[80:83], v[32:35], 0\n"
"v_fmamk_f32 v120, v120, 0x3e553b95, v170\n"
"v_cvt_pk_bf16_f32 v158, v212, v213\n"
"v_mfma_f32_16x16x32_bf16 v[204:207], a[92:95], v[36:39], v[204:207]\n"
"ds_write_b64 v197, v[240:241] offset:49152\n"
"v_mfma_f32_16x16x32_bf16 v[208:211], a[100:103], v[40:43], v[208:211]\n"
"s_add_u32 m0, s34, 5120\n"
"v_fmamk_f32 v121, v121, 0x3e553b95, v170\n"
"v_cvt_pk_bf16_f32 v159, v214, v215\n"
"v_mfma_f32_16x16x32_bf16 v[204:207], a[104:107], v[40:43], v[204:207]\n"
"buffer_load_dwordx4 v199, s[8:11], s77 offen lds\n"
"ds_read_b64_tr_b16 v[244:245], v184 offset:72\n"
"v_mfma_f32_16x16x32_bf16 v[208:211], a[112:115], v[44:47], v[208:211]\n"
"s_mul_i32 s8, s33, 0\n"
"ds_read_b64_tr_b16 v[246:247], v185 offset:72\n"
"ds_read_b64_tr_b16 v[248:249], v184 offset:840\n"
"v_mfma_f32_16x16x32_bf16 v[204:207], a[116:119], v[44:47], v[204:207]\n"
"s_mul_i32 s9, s33, 1\n"
"ds_read_b64_tr_b16 v[250:251], v185 offset:840\n"
"v_fmamk_f32 v122, v122, 0x3e553b95, v170\n"
"v_mfma_f32_16x16x32_bf16 v[208:211], a[124:127], v[48:51], v[208:211]\n"
"s_mul_i32 s10, s33, 3\n"
"v_fmamk_f32 v123, v123, 0x3e553b95, v170\n"
"v_fmamk_f32 v124, v124, 0x3e553b95, v170\n"
"v_mfma_f32_16x16x32_bf16 v[204:207], a[128:131], v[48:51], v[204:207]\n"
"s_mul_i32 s11, s33, 2\n"
"v_exp_f32 v120, v120\n"
"v_mfma_f32_16x16x32_bf16 v[208:211], a[136:139], v[52:55], v[208:211]\n"
"s_waitcnt lgkmcnt(4)\n"
"v_exp_f32 v121, v121\n"
"v_mfma_f32_16x16x32_bf16 v[204:207], a[140:143], v[52:55], v[204:207]\n"
"s_barrier\n"
"v_mfma_f32_16x16x32_bf16 v[212:215], a[144:147], v[32:35], 0\n"
"ds_read_b128 v[68:71], v192 offset:30720\n"
"ds_read_b128 v[112:115], v180 offset:13504\n"
"v_mfma_f32_16x16x32_bf16 v[212:215], a[156:159], v[36:39], v[212:215]\n"
"v_exp_f32 v122, v122\n"
"v_mfma_f32_16x16x32_bf16 v[212:215], a[168:171], v[40:43], v[212:215]\n"
"v_exp_f32 v123, v123\n"
"v_mfma_f32_16x16x32_bf16 v[212:215], a[180:183], v[44:47], v[212:215]\n"
"v_cvt_pk_bf16_f32 v90, v120, v122\n"
"v_cvt_pk_bf16_f32 v91, v121, v123\n"
"v_mfma_f32_16x16x32_bf16 v[212:215], a[192:195], v[48:51], v[212:215]\n"
"v_fmamk_f32 v125, v125, 0x3e553b95, v170\n"
"v_fmamk_f32 v126, v126, 0x3e553b95, v170\n"
"v_mfma_f32_16x16x32_bf16 v[212:215], a[204:207], v[52:55], v[212:215]\n"
"v_fmamk_f32 v127, v127, 0x3e553b95, v170\n"
"v_exp_f32 v124, v124\n"
"v_exp_f32 v125, v125\n"
"v_exp_f32 v126, v126\n"
"v_exp_f32 v127, v127\n"
"v_cvt_pk_bf16_f32 v92, v124, v126\n"
"v_cvt_pk_bf16_f32 v93, v125, v127\n"
"v_cvt_pk_bf16_f32 v228, v228, v229\n"
"v_cvt_pk_bf16_f32 v229, v230, v231\n"
"v_smfmac_f32_16x16x64_bf16 v[252:255], v[90:93], v[128:135], v66\n"
"s_waitcnt lgkmcnt(0)\n"
"ds_read_b128 v[120:123], v181 offset:13504\n"
"v_smfmac_f32_16x16x64_bf16 v[98:101], v[90:93], v[136:143], v66\n"
"ds_read_b128 v[116:119], v180 offset:27008\n"
"v_smfmac_f32_16x16x64_bf16 v[106:109], v[90:93], v[244:251], v66\n"
"ds_read_b128 v[124:127], v181 offset:27008\n"
"v_mfma_f32_16x16x32_bf16 v[90:93], v[172:175], v[90:93], v[82:85]\n"
"ds_read_b128 v[128:131], v178\n"
"ds_read_b128 v[136:139], v179\n"
"v_mfma_f32_16x16x32_bf16 v[112:115], v[112:115], v[148:151], 0\n"
"ds_read_b64 v[82:83], v180 offset:13568\n"
"v_cvt_pk_bf16_f32 v252, v252, v253\n"
"v_mfma_f32_16x16x32_bf16 v[232:235], a[216:219], v[68:71], 0\n"
"ds_read_b64 v[84:85], v181 offset:13568\n"
"v_cvt_pk_bf16_f32 v253, v254, v255\n"
"v_mfma_f32_16x16x32_bf16 v[236:239], a[220:223], v[68:71], 0\n"
"ds_read_b64 v[92:93], v181 offset:27072\n"
"v_cvt_pk_bf16_f32 v98, v98, v99\n"
"v_mfma_f32_16x16x32_bf16 v[240:243], a[224:227], v[68:71], 0\n"
"ds_read_b128 v[68:71], v192 offset:33792\n"
"v_rcp_f32 v171, v90\n"
"v_cvt_pk_bf16_f32 v99, v100, v101\n"
"v_cvt_pk_bf16_f32 v171, v171, v171\n"
"v_and_b32 v64, v171, v176\n"
"v_and_b32 v65, v171, v177\n"
"v_cvt_pk_bf16_f32 v106, v106, v107\n"
"v_cvt_pk_bf16_f32 v107, v108, v109\n"
"v_mfma_f32_16x16x16_bf16 v[252:255], v[252:253], v[64:65], 0\n"
"s_waitcnt vmcnt(9) lgkmcnt(6)\n"
"ds_read_b64 v[90:91], v180 offset:27072\n"
"ds_write_b128 v202, v[56:59] offset:55296\n"
"v_mfma_f32_16x16x16_bf16 v[98:101], v[98:99], v[64:65], 0\n"
"s_waitcnt lgkmcnt(6)\n"
"v_cvt_pk_bf16_f32 v230, v216, v217\n"
"v_cvt_pk_bf16_f32 v231, v218, v219\n"
"v_mfma_f32_16x16x16_bf16 v[106:109], v[106:107], v[64:65], 0\n"
"s_waitcnt vmcnt(8) lgkmcnt(5)\n"
"v_cvt_pk_bf16_f32 v252, v252, v253\n"
"v_cvt_pk_bf16_f32 v253, v254, v255\n"
"v_mfma_f32_16x16x32_bf16 v[120:123], v[120:123], v[148:151], 0\n"
"s_waitcnt lgkmcnt(4)\n"
"ds_write_b128 v186, v[228:231] offset:33760\n"
"v_mfma_f32_16x16x32_bf16 v[112:115], v[80:83], v[160:163], v[112:115]\n"
"v_cvt_pk_bf16_f32 v254, v98, v99\n"
"v_cvt_pk_bf16_f32 v255, v100, v101\n"
"v_mfma_f32_16x16x32_bf16 v[120:123], v[84:87], v[162:165], v[120:123]\n"
"buffer_store_dwordx4 v[252:255], v192, s[28:31], s37 offen sc1\n"
"v_mfma_f32_16x16x32_bf16 v[116:119], v[116:119], v[148:151], 0\n"
"s_waitcnt lgkmcnt(2)\n"
"ds_read_b64 v[98:99], v178 offset:64\n"
"ds_read_b64 v[100:101], v179 offset:64\n"
"v_mfma_f32_16x16x32_bf16 v[124:127], v[124:127], v[148:151], 0\n"
"ds_read_b128 v[132:135], v178 offset:13504\n"
"v_cvt_pk_bf16_f32 v106, v106, v107\n"
"v_mfma_f32_16x16x32_bf16 v[116:119], v[88:91], v[160:163], v[116:119]\n"
"ds_read_b128 v[140:143], v179 offset:13504\n"
"v_cvt_pk_bf16_f32 v107, v108, v109\n"
"v_mfma_f32_16x16x32_bf16 v[124:127], v[92:95], v[162:165], v[124:127]\n"
"buffer_store_dwordx2 v[106:107], v193, s[28:31], s37 offen offset:1024 sc1\n"
"v_mfma_f32_16x16x32_bf16 v[128:131], v[128:131], v[148:151], 0\n"
"s_add_u32 s37, s37, 1572864\n"
"ds_read_b64 v[106:107], v178 offset:13568\n"
"ds_read_b64 v[108:109], v179 offset:13568\n"
"v_mfma_f32_16x16x32_bf16 v[136:139], v[136:139], v[148:151], 0\n"
"ds_read_b128 v[72:75], v202 offset:49152\n"
"v_cvt_pk_bf16_f32 v220, v220, v221\n"
"v_mfma_f32_16x16x32_bf16 v[216:219], a[148:151], v[32:35], 0\n"
"ds_read_b64_tr_b16 v[244:245], v182 offset:54144\n"
"v_cvt_pk_bf16_f32 v221, v222, v223\n"
"v_mfma_f32_16x16x32_bf16 v[228:231], a[152:155], v[32:35], 0\n"
"s_waitcnt lgkmcnt(7)\n"
"ds_read_b128 v[32:35], v188 offset:6144\n"
"v_max_f32 v112, v112, v120\n"
"v_mfma_f32_16x16x32_bf16 v[216:219], a[160:163], v[36:39], v[216:219]\n"
"s_waitcnt lgkmcnt(7)\n"
"ds_read_b64_tr_b16 v[246:247], v183 offset:54144\n"
"v_max_f32 v113, v113, v121\n"
"v_mfma_f32_16x16x32_bf16 v[128:131], v[96:99], v[160:163], v[128:131]\n"
"s_waitcnt lgkmcnt(7)\n"
"ds_read_b64_tr_b16 v[248:249], v182 offset:54912\n"
"v_max_f32 v114, v114, v122\n"
"v_mfma_f32_16x16x32_bf16 v[136:139], v[100:103], v[162:165], v[136:139]\n"
"s_waitcnt lgkmcnt(7)\n"
"ds_read_b128 v[76:79], v202 offset:53248\n"
"v_max_f32 v115, v115, v123\n"
"v_mfma_f32_16x16x32_bf16 v[132:135], v[132:135], v[148:151], 0\n"
"ds_read_b64_tr_b16 v[250:251], v183 offset:54912\n"
"v_max3_f32 v170, v112, v113, v114\n"
"v_mfma_f32_16x16x32_bf16 v[140:143], v[140:143], v[148:151], 0\n"
"s_waitcnt lgkmcnt(8)\n"
"ds_read_b64_tr_b16 v[120:121], v182 offset:54152\n"
"v_max_f32 v116, v116, v124\n"
"v_mfma_f32_16x16x32_bf16 v[132:135], v[104:107], v[160:163], v[132:135]\n"
"s_waitcnt lgkmcnt(8)\n"
"ds_read_b64_tr_b16 v[122:123], v183 offset:54152\n"
"v_max_f32 v117, v117, v125\n"
"v_mfma_f32_16x16x32_bf16 v[140:143], v[108:111], v[162:165], v[140:143]\n"
"s_waitcnt lgkmcnt(8)\n"
"buffer_store_dwordx4 v[72:75], v194, s[24:27], s36 offen sc1\n"
"v_mfma_f32_16x16x32_bf16 v[228:231], a[164:167], v[36:39], v[228:231]\n"
"s_nop 0\n"
"ds_read_b128 v[72:75], v192 offset:31744\n"
"ds_read_b128 v[36:39], v189 offset:6144\n"
"v_mfma_f32_16x16x32_bf16 v[216:219], a[172:175], v[40:43], v[216:219]\n"
"ds_read_b64_tr_b16 v[124:125], v182 offset:54920\n"
"v_max_f32 v118, v118, v126\n"
"v_mfma_f32_16x16x32_bf16 v[228:231], a[176:179], v[40:43], v[228:231]\n"
"ds_read_b128 v[40:43], v188 offset:7168\n"
"v_max_f32 v119, v119, v127\n"
"v_mfma_f32_16x16x32_bf16 v[216:219], a[184:187], v[44:47], v[216:219]\n"
"s_waitcnt lgkmcnt(7)\n"
"ds_read_b64_tr_b16 v[126:127], v183 offset:54920\n"
"buffer_store_dwordx4 v[76:79], v195, s[24:27], s36 offen sc1\n"
"v_mfma_f32_16x16x32_bf16 v[228:231], a[188:191], v[44:47], v[228:231]\n"
"s_add_u32 s36, s36, s33\n"
"ds_read_b128 v[76:79], v192 offset:32768\n"
"ds_read_b128 v[44:47], v189 offset:7168\n"
"v_mfma_f32_16x16x32_bf16 v[216:219], a[196:199], v[48:51], v[216:219]\n"
"ds_write_b64 v203, v[60:61] offset:55296\n"
"v_mfma_f32_16x16x32_bf16 v[228:231], a[200:203], v[48:51], v[228:231]\n"
"s_waitcnt lgkmcnt(7)\n"
"buffer_load_dwordx4 a[198:201], v192, s[28:31], s38 offen sc1\n"
"v_max3_f32 v170, v170, v115, v116\n"
"v_mfma_f32_16x16x32_bf16 v[216:219], a[208:211], v[52:55], v[216:219]\n"
"ds_read_b128 v[48:51], v188 offset:8192\n"
"v_max3_f32 v170, v170, v117, v118\n"
"v_mfma_f32_16x16x32_bf16 v[232:235], a[228:231], v[72:75], v[232:235]\n"
"v_max_f32 v128, v128, v136\n"
"v_max_f32 v129, v129, v137\n"
"v_mfma_f32_16x16x32_bf16 v[236:239], a[232:235], v[72:75], v[236:239]\n"
"ds_read_b64_tr_b16 v[136:137], v182 offset:54088\n"
"v_max_f32 v130, v130, v138\n"
"v_mfma_f32_16x16x32_bf16 v[240:243], a[236:239], v[72:75], v[240:243]\n"
"buffer_load_dwordx2 a[202:203], v193, s[28:31], s38 offen offset:1024 sc1\n"
"ds_read_b128 v[72:75], v192 offset:34816\n"
"v_mfma_f32_16x16x32_bf16 v[228:231], a[212:215], v[52:55], v[228:231]\n"
"s_waitcnt lgkmcnt(5)\n"
"ds_read_b128 v[52:55], v189 offset:8192\n"
"v_max_f32 v131, v131, v139\n"
"v_max3_f32 v170, v170, v119, v128\n"
"v_mfma_f32_16x16x32_bf16 v[232:235], a[240:243], v[76:79], v[232:235]\n"
"s_add_u32 s38, s38, 1572864\n"
"ds_read_b64_tr_b16 v[138:139], v183 offset:54088\n"
"v_cvt_pk_bf16_f32 v224, v224, v225\n"
"v_mfma_f32_16x16x32_bf16 v[236:239], a[244:247], v[76:79], v[236:239]\n"
"v_max3_f32 v170, v170, v129, v130\n"
"v_max_f32 v132, v132, v140\n"
"v_mfma_f32_16x16x32_bf16 v[240:243], a[248:251], v[76:79], v[240:243]\n"
"buffer_load_dwordx4 a[204:207], v192, s[28:31], s38 offen sc1\n"
"ds_read_b128 v[76:79], v192 offset:35840\n"
"v_mfma_f32_16x16x32_bf16 v[232:235], a[252:255], v[68:71], v[232:235]\n"
"v_cvt_pk_bf16_f32 v225, v226, v227\n"
"v_max_f32 v133, v133, v141\n"
"v_mfma_f32_16x16x32_bf16 v[240:243], v[4:7], v[68:71], v[240:243]\n"
"ds_read_b64_tr_b16 v[140:141], v182 offset:54856\n"
"v_cvt_pk_bf16_f32 v226, v208, v209\n"
"v_mfma_f32_16x16x32_bf16 v[236:239], v[0:3], v[68:71], v[236:239]\n"
"v_max_f32 v134, v134, v142\n"
"v_cvt_pk_bf16_f32 v227, v210, v211\n"
"v_mfma_f32_16x16x32_bf16 v[208:211], a[0:3], v[32:35], 0\n"
"s_waitcnt lgkmcnt(4)\n"
"buffer_load_dwordx2 a[208:209], v193, s[28:31], s38 offen offset:1024 sc1\n"
"ds_write_b128 v186, v[224:227] offset:33888\n"
"v_mfma_f32_16x16x32_bf16 v[232:235], v[8:11], v[72:75], v[232:235]\n"
"s_add_u32 s38, s38, 1572864\n"
"v_max_f32 v135, v135, v143\n"
"v_max3_f32 v170, v170, v131, v132\n"
"v_mfma_f32_16x16x32_bf16 v[240:243], v[16:19], v[72:75], v[240:243]\n"
"v_cvt_pk_bf16_f32 v222, v204, v205\n"
"v_max3_f32 v170, v170, v133, v134\n"
"v_mfma_f32_16x16x32_bf16 v[236:239], v[12:15], v[72:75], v[236:239]\n"
"s_waitcnt lgkmcnt(2)\n"
"ds_read_b64_tr_b16 v[142:143], v183 offset:54856\n"
"v_cvt_pk_bf16_f32 v223, v206, v207\n"
"v_mfma_f32_16x16x32_bf16 v[232:235], v[20:23], v[76:79], v[232:235]\n"
"ds_write_b128 v186, v[220:223] offset:33824\n"
"v_mfma_f32_16x16x32_bf16 v[240:243], v[28:31], v[76:79], v[240:243]\n"
"s_mov_b64 exec, s[46:47]\n"
"v_add_u32 v183, 0xc0, v183\n"
"s_mov_b64 exec, -1\n"
"v_mfma_f32_16x16x32_bf16 v[236:239], v[24:27], v[76:79], v[236:239]\n"
"v_mov_b64 v[252:253], 0\n"
"v_mov_b64 v[254:255], 0\n"
"v_mfma_f32_16x16x32_bf16 v[204:207], a[4:7], v[32:35], 0\n"
"v_mov_b64 v[98:99], 0\n"
"v_mov_b64 v[100:101], 0\n"
"v_mfma_f32_16x16x32_bf16 v[220:223], a[8:11], v[32:35], 0\n"
"v_mov_b64 v[106:107], 0\n"
"v_mov_b64 v[108:109], 0\n"
"v_mfma_f32_16x16x32_bf16 v[208:211], a[12:15], v[36:39], v[208:211]\n"
"v_max_f32 v171, v170, v135\n"
"v_max_f32 v170, v170, v135\n"
"v_mfma_f32_16x16x32_bf16 v[204:207], a[16:19], v[36:39], v[204:207]\n"
"s_nop 0\n"
"v_permlane16_swap_b32 v170, v171\n"
"v_mfma_f32_16x16x32_bf16 v[220:223], a[20:23], v[36:39], v[220:223]\n"
"v_max_f32 v170, v170, v171\n"
"v_mov_b32 v171, v170\n"
"v_mfma_f32_16x16x32_bf16 v[208:211], a[24:27], v[40:43], v[208:211]\n"
"s_nop 0\n"
"v_permlane32_swap_b32 v170, v171\n"
"v_mfma_f32_16x16x32_bf16 v[204:207], a[28:31], v[40:43], v[204:207]\n"
"v_max_f32 v170, v170, v171\n"
"v_mul_f32 v170, 0xbe553b95, v170\n"
"v_mfma_f32_16x16x32_bf16 v[220:223], a[32:35], v[40:43], v[220:223]\n"
"v_fmamk_f32 v112, v112, 0x3e553b95, v170\n"
"v_fmamk_f32 v113, v113, 0x3e553b95, v170\n"
"v_mfma_f32_16x16x32_bf16 v[208:211], a[36:39], v[44:47], v[208:211]\n"
"v_fmamk_f32 v114, v114, 0x3e553b95, v170\n"
"v_fmamk_f32 v115, v115, 0x3e553b95, v170\n"
"v_mfma_f32_16x16x32_bf16 v[204:207], a[40:43], v[44:47], v[204:207]\n"
"v_exp_f32 v112, v112\n"
"v_mfma_f32_16x16x32_bf16 v[220:223], a[44:47], v[44:47], v[220:223]\n"
"s_waitcnt vmcnt(9)\n"
"v_exp_f32 v113, v113\n"
"v_mfma_f32_16x16x32_bf16 v[208:211], a[48:51], v[48:51], v[208:211]\n"
"v_add3_u32 v200, v200, s50, -8\n"
"v_cmp_lt_u32 vcc, 64, v200\n"
"v_mfma_f32_16x16x32_bf16 v[204:207], a[52:55], v[48:51], v[204:207]\n"
"v_exp_f32 v114, v114\n"
"v_mfma_f32_16x16x32_bf16 v[220:223], a[56:59], v[48:51], v[220:223]\n"
"s_nop 1\n"
"s_cbranch_vccnz .Lp7%=\n"
".Lr7%=:\n"
"v_mfma_f32_16x16x32_bf16 v[208:211], a[60:63], v[52:55], v[208:211]\n"
"buffer_load_dwordx4 a[210:213], v192, s[28:31], s38 offen sc1\n"
"v_cvt_pk_bf16_f32 v82, v112, v114\n"
"v_mfma_f32_16x16x32_bf16 v[204:207], a[64:67], v[52:55], v[204:207]\n"
"v_exp_f32 v115, v115\n"
"v_mfma_f32_16x16x32_bf16 v[220:223], a[68:71], v[52:55], v[220:223]\n"
"v_cvt_pk_bf16_f32 v83, v113, v115\n"
"v_fmamk_f32 v116, v116, 0x3e553b95, v170\n"
"v_mfma_f32_16x16x32_bf16 v[224:227], a[72:75], v[32:35], 0\n"
"buffer_load_dwordx2 a[214:215], v193, s[28:31], s38 offen offset:1024 sc1\n"
"ds_read_b64_tr_b16 v[112:113], v182 offset:128\n"
"v_mfma_f32_16x16x32_bf16 v[224:227], a[84:87], v[36:39], v[224:227]\n"
"s_add_u32 s38, s38, 1572864\n"
"ds_read_b64_tr_b16 v[114:115], v183 offset:128\n"
"v_fmamk_f32 v117, v117, 0x3e553b95, v170\n"
"v_mfma_f32_16x16x32_bf16 v[224:227], a[96:99], v[40:43], v[224:227]\n"
"s_add_u32 s12, s38, 0\n"
"v_fmamk_f32 v118, v118, 0x3e553b95, v170\n"
"v_fmamk_f32 v119, v119, 0x3e553b95, v170\n"
"v_mfma_f32_16x16x32_bf16 v[224:227], a[108:111], v[44:47], v[224:227]\n"
"s_add_u32 s13, s38, 1572864\n"
"v_exp_f32 v116, v116\n"
"v_mfma_f32_16x16x32_bf16 v[224:227], a[120:123], v[48:51], v[224:227]\n"
"s_add_u32 s15, s38, 3145728\n"
"v_exp_f32 v117, v117\n"
"v_mfma_f32_16x16x32_bf16 v[224:227], a[132:135], v[52:55], v[224:227]\n"
"s_add_u32 s14, s38, 4718592\n"
"v_exp_f32 v118, v118\n"
"v_exp_f32 v119, v119\n"
"v_cvt_pk_bf16_f32 v84, v116, v118\n"
"v_cvt_pk_bf16_f32 v85, v117, v119\n"
"v_cvt_pk_bf16_f32 v232, v232, v233\n"
"v_cvt_pk_bf16_f32 v233, v234, v235\n"
"v_smfmac_f32_16x16x64_bf16 v[252:255], v[82:85], v[244:251], v66\n"
"s_waitcnt lgkmcnt(3)\n"
"ds_read_b64_tr_b16 v[116:117], v182 offset:896\n"
"v_smfmac_f32_16x16x64_bf16 v[98:101], v[82:85], v[120:127], v66\n"
"s_add_u32 s16, s38, 6291456\n"
"ds_read_b64_tr_b16 v[118:119], v183 offset:896\n"
".Lic6%=:\n"
"s_cbranch_execz .Lib1%=\n"
"v_smfmac_f32_16x16x64_bf16 v[106:109], v[82:85], v[136:143], v66\n"
"s_add_u32 s17, s38, 7864320\n"
"ds_read_b64_tr_b16 v[244:245], v182 offset:136\n"
"v_mfma_f32_16x16x32_bf16 v[82:85], v[172:175], v[82:85], 0\n"
"ds_read_b64_tr_b16 v[246:247], v183 offset:136\n"
"ds_read_b64_tr_b16 v[248:249], v182 offset:904\n"
"v_cvt_pk_bf16_f32 v240, v240, v241\n"
"v_cvt_pk_bf16_f32 v234, v236, v237\n"
"v_cvt_pk_bf16_f32 v235, v238, v239\n"
"v_cvt_pk_bf16_f32 v241, v242, v243\n"
"v_cvt_pk_bf16_f32 v148, v212, v213\n"
"ds_read_b64_tr_b16 v[250:251], v183 offset:904\n"
"ds_write_b128 v196, v[232:235] offset:36864\n"
"v_cvt_pk_bf16_f32 v149, v214, v215\n"
"v_mfma_f32_16x16x32_bf16 v[212:215], a[76:79], v[32:35], 0\n"
"v_cvt_pk_bf16_f32 v150, v216, v217\n"
"v_cvt_pk_bf16_f32 v151, v218, v219\n"
"v_mfma_f32_16x16x32_bf16 v[216:219], a[80:83], v[32:35], 0\n"
"s_waitcnt vmcnt(8)\n"
"ds_write_b64 v197, v[240:241] offset:36864\n"
"v_mfma_f32_16x16x32_bf16 v[212:215], a[88:91], v[36:39], v[212:215]\n"
"v_or_b32 v201, 10, s6\n"
"v_fmamk_f32 v128, v128, 0x3e553b95, v170\n"
"v_mfma_f32_16x16x32_bf16 v[216:219], a[92:95], v[36:39], v[216:219]\n"
"s_mov_b64 exec, 1\n"
"buffer_store_dword v201, off, s[28:31], s40 sc1\n"
"s_mov_b64 exec, -1\n"
"v_mfma_f32_16x16x32_bf16 v[212:215], a[100:103], v[40:43], v[212:215]\n"
"ds_read_b64_tr_b16 v[120:121], v182 offset:72\n"
"ds_read_b128 v[32:35], v188\n"
"v_mfma_f32_16x16x32_bf16 v[216:219], a[104:107], v[40:43], v[216:219]\n"
"ds_read_b64_tr_b16 v[122:123], v183 offset:72\n"
"v_cvt_pk_bf16_f32 v162, v228, v229\n"
"v_mfma_f32_16x16x32_bf16 v[212:215], a[112:115], v[44:47], v[212:215]\n"
"ds_read_b64_tr_b16 v[124:125], v182 offset:840\n"
"v_fmamk_f32 v129, v129, 0x3e553b95, v170\n"
"v_mfma_f32_16x16x32_bf16 v[216:219], a[116:119], v[44:47], v[216:219]\n"
"ds_read_b128 v[36:39], v189\n"
"v_cvt_pk_bf16_f32 v163, v230, v231\n"
"v_mfma_f32_16x16x32_bf16 v[212:215], a[124:127], v[48:51], v[212:215]\n"
"s_waitcnt lgkmcnt(5)\n"
"ds_read_b64_tr_b16 v[126:127], v183 offset:840\n"
"v_fmamk_f32 v130, v130, 0x3e553b95, v170\n"
"v_mfma_f32_16x16x32_bf16 v[216:219], a[128:131], v[48:51], v[216:219]\n"
"s_barrier\n"
"v_mfma_f32_16x16x32_bf16 v[212:215], a[136:139], v[52:55], v[212:215]\n"
"ds_read_b128 v[76:79], v192 offset:55296\n"
"ds_read_b128 v[40:43], v188 offset:1024\n"
"v_mfma_f32_16x16x32_bf16 v[216:219], a[140:143], v[52:55], v[216:219]\n"
"ds_read_b128 v[44:47], v189 offset:1024\n"
"v_fmamk_f32 v131, v131, 0x3e553b95, v170\n"
"v_exp_f32 v128, v128\n"
"v_exp_f32 v129, v129\n"
"v_fmamk_f32 v132, v132, 0x3e553b95, v170\n"
"v_fmamk_f32 v133, v133, 0x3e553b95, v170\n"
"v_exp_f32 v130, v130\n"
"v_exp_f32 v131, v131\n"
"v_cvt_pk_bf16_f32 v90, v128, v130\n"
"v_cvt_pk_bf16_f32 v91, v129, v131\n"
"v_fmamk_f32 v134, v134, 0x3e553b95, v170\n"
"v_fmamk_f32 v135, v135, 0x3e553b95, v170\n"
"v_exp_f32 v132, v132\n"
"v_exp_f32 v133, v133\n"
"v_exp_f32 v134, v134\n"
"v_exp_f32 v135, v135\n"
"v_cvt_pk_bf16_f32 v92, v132, v134\n"
"v_cvt_pk_bf16_f32 v93, v133, v135\n"
"v_cvt_pk_bf16_f32 v208, v208, v209\n"
"v_cvt_pk_bf16_f32 v209, v210, v211\n"
"v_smfmac_f32_16x16x64_bf16 v[252:255], v[90:93], v[112:119], v66\n"
"s_waitcnt lgkmcnt(2)\n"
"ds_read_b128 v[48:51], v188 offset:2048\n"
"v_smfmac_f32_16x16x64_bf16 v[98:101], v[90:93], v[244:251], v66\n"
"ds_read_b128 v[52:55], v189 offset:2048\n"
"v_smfmac_f32_16x16x64_bf16 v[106:109], v[90:93], v[120:127], v66\n"
"ds_read_b128 v[136:139], v180 offset:27008\n"
"v_mfma_f32_16x16x32_bf16 v[90:93], v[172:175], v[90:93], v[82:85]\n"
"ds_read_b128 v[128:131], v181 offset:27008\n"
"ds_read_b128 v[140:143], v178\n"
"v_mfma_f32_16x16x32_bf16 v[232:235], a[216:219], v[76:79], 0\n"
"ds_read_b64 v[82:83], v180 offset:27072\n"
"v_cvt_pk_bf16_f32 v252, v252, v253\n"
"v_mfma_f32_16x16x32_bf16 v[236:239], a[220:223], v[76:79], 0\n"
"ds_read_b64 v[84:85], v181 offset:27072\n"
"v_cvt_pk_bf16_f32 v253, v254, v255\n"
"v_mfma_f32_16x16x32_bf16 v[240:243], a[224:227], v[76:79], 0\n"
"ds_read_b128 v[76:79], v192 offset:58368\n"
"v_cvt_pk_bf16_f32 v98, v98, v99\n"
"v_mfma_f32_16x16x32_bf16 v[228:231], a[0:3], v[32:35], 0\n"
"ds_read_b128 v[132:135], v179\n"
"v_cvt_pk_bf16_f32 v99, v100, v101\n"
"v_mfma_f32_16x16x32_bf16 v[228:231], a[12:15], v[36:39], v[228:231]\n"
"s_waitcnt lgkmcnt(10)\n"
"ds_read_b64 v[92:93], v179 offset:64\n"
"v_cvt_pk_bf16_f32 v106, v106, v107\n"
"v_mfma_f32_16x16x32_bf16 v[228:231], a[24:27], v[40:43], v[228:231]\n"
"s_waitcnt lgkmcnt(10)\n"
"ds_read_b128 v[112:115], v178 offset:13504\n"
"v_cvt_pk_bf16_f32 v107, v108, v109\n"
"v_mfma_f32_16x16x32_bf16 v[228:231], a[36:39], v[44:47], v[228:231]\n"
"s_waitcnt lgkmcnt(8)\n"
"v_rcp_f32 v171, v90\n"
"v_mfma_f32_16x16x32_bf16 v[136:139], v[136:139], v[152:155], 0\n"
"s_waitcnt lgkmcnt(7)\n"
"ds_read_b64 v[90:91], v178 offset:64\n"
"ds_read_b128 v[244:247], v179 offset:13504\n"
"v_mfma_f32_16x16x32_bf16 v[128:131], v[128:131], v[152:155], 0\n"
"s_waitcnt lgkmcnt(8)\n"
"v_cvt_pk_bf16_f32 v171, v171, v171\n"
"v_and_b32 v64, v171, v176\n"
"v_mfma_f32_16x16x32_bf16 v[140:143], v[140:143], v[152:155], 0\n"
"s_waitcnt lgkmcnt(7)\n"
"v_and_b32 v65, v171, v177\n"
"v_cvt_pk_bf16_f32 v210, v204, v205\n"
"v_mfma_f32_16x16x32_bf16 v[136:139], v[80:83], v[164:167], v[136:139]\n"
"s_waitcnt lgkmcnt(6)\n"
"ds_read_b128 v[116:119], v178 offset:27008\n"
"ds_read_b128 v[248:251], v179 offset:27008\n"
"v_mfma_f32_16x16x16_bf16 v[252:255], v[252:253], v[64:65], 0\n"
"ds_read_b128 v[68:71], v202 offset:36864\n"
"v_cvt_pk_bf16_f32 v211, v206, v207\n"
"v_mfma_f32_16x16x16_bf16 v[98:101], v[98:99], v[64:65], 0\n"
"s_waitcnt lgkmcnt(7)\n"
"ds_write_b128 v186, v[208:211] offset:47264\n"
"v_mfma_f32_16x16x16_bf16 v[106:109], v[106:107], v[64:65], 0\n"
"s_waitcnt lgkmcnt(6)\n"
"v_cvt_pk_bf16_f32 v220, v220, v221\n"
"v_cvt_pk_bf16_f32 v252, v252, v253\n"
"v_mfma_f32_16x16x32_bf16 v[128:131], v[84:87], v[166:169], v[128:131]\n"
"s_waitcnt lgkmcnt(5)\n"
"v_cvt_pk_bf16_f32 v253, v254, v255\n"
"v_cvt_pk_bf16_f32 v254, v98, v99\n"
"v_mfma_f32_16x16x32_bf16 v[132:135], v[132:135], v[152:155], 0\n"
"s_waitcnt lgkmcnt(4)\n"
"ds_read_b64 v[98:99], v178 offset:13568\n"
"v_cvt_pk_bf16_f32 v255, v100, v101\n"
"v_mfma_f32_16x16x32_bf16 v[140:143], v[88:91], v[164:167], v[140:143]\n"
"s_waitcnt vmcnt(6)\n"
"buffer_store_dwordx4 v[252:255], v192, s[28:31], s37 offen sc1\n"
"v_mfma_f32_16x16x32_bf16 v[132:135], v[92:95], v[166:169], v[132:135]\n"
"s_waitcnt lgkmcnt(3)\n"
"ds_read_b64 v[100:101], v179 offset:13568\n"
"ds_write_b128 v202, a[198:201] offset:30720\n"
"v_mfma_f32_16x16x32_bf16 v[112:115], v[112:115], v[152:155], 0\n"
"s_waitcnt vmcnt(6) lgkmcnt(4)\n"
"v_cvt_pk_bf16_f32 v106, v106, v107\n"
"v_cvt_pk_bf16_f32 v107, v108, v109\n"
"v_mfma_f32_16x16x32_bf16 v[244:247], v[244:247], v[152:155], 0\n"
"buffer_store_dwordx2 v[106:107], v193, s[28:31], s37 offen offset:1024 sc1\n"
"v_mfma_f32_16x16x32_bf16 v[116:119], v[116:119], v[152:155], 0\n"
"s_add_u32 s37, s37, 1572864\n"
"ds_read_b64 v[106:107], v178 offset:27072\n"
"ds_read_b64 v[108:109], v179 offset:27072\n"
"v_mfma_f32_16x16x32_bf16 v[248:251], v[248:251], v[152:155], 0\n"
"s_waitcnt lgkmcnt(4)\n"
"ds_read_b64_tr_b16 v[120:121], v184 offset:128\n"
"v_cvt_pk_bf16_f32 v221, v222, v223\n"
"v_mfma_f32_16x16x32_bf16 v[112:115], v[96:99], v[164:167], v[112:115]\n"
"buffer_store_dwordx4 v[68:71], v194, s[24:27], s36 offen sc1\n"
"v_mfma_f32_16x16x32_bf16 v[204:207], a[4:7], v[32:35], 0\n"
"s_waitcnt lgkmcnt(4)\n"
"ds_read_b128 v[68:71], v192 offset:56320\n"
"ds_read_b64_tr_b16 v[122:123], v185 offset:128\n"
"v_mfma_f32_16x16x32_bf16 v[244:247], v[100:103], v[166:169], v[244:247]\n"
"ds_read_b64_tr_b16 v[124:125], v184 offset:896\n"
"v_max_f32 v136, v136, v128\n"
"v_mfma_f32_16x16x32_bf16 v[208:211], a[8:11], v[32:35], 0\n"
"ds_read_b128 v[72:75], v202 offset:40960\n"
"v_max_f32 v137, v137, v129\n"
"v_mfma_f32_16x16x32_bf16 v[204:207], a[16:19], v[36:39], v[204:207]\n"
"ds_read_b64_tr_b16 v[126:127], v185 offset:896\n"
"v_max_f32 v138, v138, v130\n"
"v_mfma_f32_16x16x32_bf16 v[208:211], a[20:23], v[36:39], v[208:211]\n"
"s_waitcnt lgkmcnt(7)\n"
"ds_read_b64_tr_b16 v[128:129], v184 offset:136\n"
"v_max_f32 v139, v139, v131\n"
"v_mfma_f32_16x16x32_bf16 v[116:119], v[104:107], v[164:167], v[116:119]\n"
"s_waitcnt lgkmcnt(7)\n"
"ds_read_b64_tr_b16 v[130:131], v185 offset:136\n"
"v_max3_f32 v170, v136, v137, v138\n"
"v_mfma_f32_16x16x32_bf16 v[248:251], v[108:111], v[166:169], v[248:251]\n"
"ds_write_b64 v203, a[202:203] offset:30720\n"
"v_mfma_f32_16x16x32_bf16 v[204:207], a[28:31], v[40:43], v[204:207]\n"
"s_waitcnt lgkmcnt(7)\n"
"v_max_f32 v140, v140, v132\n"
"v_max_f32 v141, v141, v133\n"
"v_mfma_f32_16x16x32_bf16 v[208:211], a[32:35], v[40:43], v[208:211]\n"
"ds_read_b64_tr_b16 v[132:133], v184 offset:904\n"
"v_max_f32 v142, v142, v134\n"
"v_mfma_f32_16x16x32_bf16 v[232:235], a[228:231], v[68:71], v[232:235]\n"
"v_max_f32 v143, v143, v135\n"
"v_max3_f32 v170, v170, v139, v140\n"
"v_mfma_f32_16x16x32_bf16 v[236:239], a[232:235], v[68:71], v[236:239]\n"
"s_waitcnt lgkmcnt(5)\n"
"ds_read_b64_tr_b16 v[134:135], v185 offset:904\n"
"v_max3_f32 v170, v170, v141, v142\n"
"v_mfma_f32_16x16x32_bf16 v[240:243], a[236:239], v[68:71], v[240:243]\n"
"ds_read_b128 v[68:71], v192 offset:59392\n"
"buffer_store_dwordx4 v[72:75], v195, s[24:27], s36 offen sc1\n"
"v_mfma_f32_16x16x32_bf16 v[204:207], a[40:43], v[44:47], v[204:207]\n"
"s_add_u32 s36, s36, s33\n"
"ds_read_b128 v[72:75], v192 offset:57344\n"
"v_max_f32 v112, v112, v244\n"
"v_mfma_f32_16x16x32_bf16 v[208:211], a[44:47], v[44:47], v[208:211]\n"
"v_max_f32 v113, v113, v245\n"
"v_max_f32 v114, v114, v246\n"
"v_mfma_f32_16x16x32_bf16 v[228:231], a[48:51], v[48:51], v[228:231]\n"
"ds_read_b64_tr_b16 v[244:245], v184 offset:72\n"
"v_max_f32 v115, v115, v247\n"
"v_mfma_f32_16x16x32_bf16 v[204:207], a[52:55], v[48:51], v[204:207]\n"
"ds_read_b64_tr_b16 v[246:247], v185 offset:72\n"
"v_max3_f32 v170, v170, v143, v112\n"
"v_mfma_f32_16x16x32_bf16 v[208:211], a[56:59], v[48:51], v[208:211]\n"
"v_max3_f32 v170, v170, v113, v114\n"
"v_max_f32 v116, v116, v248\n"
"v_mfma_f32_16x16x32_bf16 v[228:231], a[60:63], v[52:55], v[228:231]\n"
"v_max_f32 v117, v117, v249\n"
"v_max_f32 v118, v118, v250\n"
"v_mfma_f32_16x16x32_bf16 v[204:207], a[64:67], v[52:55], v[204:207]\n"
"ds_read_b64_tr_b16 v[248:249], v184 offset:840\n"
"v_max_f32 v119, v119, v251\n"
"v_mfma_f32_16x16x32_bf16 v[208:211], a[68:71], v[52:55], v[208:211]\n"
"ds_read_b64_tr_b16 v[250:251], v185 offset:840\n"
"v_max3_f32 v170, v170, v115, v116\n"
"v_max3_f32 v170, v170, v117, v118\n"
"v_mov_b64 v[252:253], 0\n"
"v_mov_b64 v[254:255], 0\n"
"v_mov_b64 v[98:99], 0\n"
"v_mov_b64 v[100:101], 0\n"
"v_mov_b64 v[106:107], 0\n"
"v_mov_b64 v[108:109], 0\n"
"v_max_f32 v171, v170, v119\n"
"v_max_f32 v170, v170, v119\n"
"v_cvt_pk_bf16_f32 v224, v224, v225\n"
"v_cvt_pk_bf16_f32 v225, v226, v227\n"
"v_permlane16_swap_b32 v170, v171\n"
"v_max_f32 v170, v170, v171\n"
"v_mov_b32 v171, v170\n"
"v_cvt_pk_bf16_f32 v226, v212, v213\n"
"v_cvt_pk_bf16_f32 v227, v214, v215\n"
"v_mfma_f32_16x16x32_bf16 v[212:215], a[72:75], v[32:35], 0\n"
"s_waitcnt lgkmcnt(4)\n"
"v_permlane32_swap_b32 v170, v171\n"
"v_mfma_f32_16x16x32_bf16 v[232:235], a[240:243], v[72:75], v[232:235]\n"
"v_max_f32 v170, v170, v171\n"
"v_mul_f32 v170, 0xbe553b95, v170\n"
"v_mfma_f32_16x16x32_bf16 v[236:239], a[244:247], v[72:75], v[236:239]\n"
"ds_write_b128 v186, v[224:227] offset:47392\n"
"v_mfma_f32_16x16x32_bf16 v[240:243], a[248:251], v[72:75], v[240:243]\n"
"v_fmamk_f32 v136, v136, 0x3e553b95, v170\n"
"v_fmamk_f32 v137, v137, 0x3e553b95, v170\n"
"v_mfma_f32_16x16x32_bf16 v[232:235], a[252:255], v[76:79], v[232:235]\n"
"v_fmamk_f32 v138, v138, 0x3e553b95, v170\n"
"v_fmamk_f32 v139, v139, 0x3e553b95, v170\n"
"v_mfma_f32_16x16x32_bf16 v[240:243], v[4:7], v[76:79], v[240:243]\n"
"ds_read_b128 v[72:75], v192 offset:60416\n"
"v_fmamk_f32 v140, v140, 0x3e553b95, v170\n"
"v_mfma_f32_16x16x32_bf16 v[236:239], v[0:3], v[76:79], v[236:239]\n"
"v_exp_f32 v136, v136\n"
"v_mfma_f32_16x16x32_bf16 v[232:235], v[8:11], v[68:71], v[232:235]\n"
"v_exp_f32 v137, v137\n"
"v_mfma_f32_16x16x32_bf16 v[240:243], v[16:19], v[68:71], v[240:243]\n"
"v_exp_f32 v138, v138\n"
"v_mfma_f32_16x16x32_bf16 v[236:239], v[12:15], v[68:71], v[236:239]\n"
"buffer_load_dword v200, off, s[28:31], s39 sc1\n"
"v_cvt_pk_bf16_f32 v82, v136, v138\n"
"v_mfma_f32_16x16x32_bf16 v[212:215], a[84:87], v[36:39], v[212:215]\n"
"v_exp_f32 v139, v139\n"
"v_mfma_f32_16x16x32_bf16 v[212:215], a[96:99], v[40:43], v[212:215]\n"
"v_cvt_pk_bf16_f32 v83, v137, v139\n"
"v_fmamk_f32 v141, v141, 0x3e553b95, v170\n"
"v_mfma_f32_16x16x32_bf16 v[212:215], a[108:111], v[44:47], v[212:215]\n"
"ds_read_b64_tr_b16 v[136:137], v182 offset:13632\n"
"ds_read_b64_tr_b16 v[138:139], v183 offset:13632\n"
"v_mfma_f32_16x16x32_bf16 v[212:215], a[120:123], v[48:51], v[212:215]\n"
"s_waitcnt lgkmcnt(2)\n"
"v_fmamk_f32 v142, v142, 0x3e553b95, v170\n"
"v_fmamk_f32 v143, v143, 0x3e553b95, v170\n"
"v_mfma_f32_16x16x32_bf16 v[232:235], v[20:23], v[72:75], v[232:235]\n"
"v_exp_f32 v140, v140\n"
"v_mfma_f32_16x16x32_bf16 v[240:243], v[28:31], v[72:75], v[240:243]\n"
"v_exp_f32 v141, v141\n"
"v_mfma_f32_16x16x32_bf16 v[236:239], v[24:27], v[72:75], v[236:239]\n"
"v_exp_f32 v142, v142\n"
"v_mfma_f32_16x16x32_bf16 v[212:215], a[132:135], v[52:55], v[212:215]\n"
"v_exp_f32 v143, v143\n"
"v_cvt_pk_bf16_f32 v84, v140, v142\n"
"v_cvt_pk_bf16_f32 v85, v141, v143\n"
"v_cvt_pk_bf16_f32 v222, v216, v217\n"
"v_cvt_pk_bf16_f32 v223, v218, v219\n"
"v_smfmac_f32_16x16x64_bf16 v[252:255], v[82:85], v[120:127], v66\n"
"ds_read_b64_tr_b16 v[140:141], v182 offset:14400\n"
"v_smfmac_f32_16x16x64_bf16 v[98:101], v[82:85], v[128:135], v66\n"
"ds_read_b64_tr_b16 v[142:143], v183 offset:14400\n"
"v_smfmac_f32_16x16x64_bf16 v[106:109], v[82:85], v[244:251], v66\n"
"ds_read_b64_tr_b16 v[120:121], v182 offset:13640\n"
"v_mfma_f32_16x16x32_bf16 v[82:85], v[172:175], v[82:85], 0\n"
"ds_read_b64_tr_b16 v[122:123], v183 offset:13640\n"
"ds_read_b64_tr_b16 v[124:125], v182 offset:14408\n"
"v_mfma_f32_16x16x32_bf16 v[216:219], a[76:79], v[32:35], 0\n"
"ds_write_b128 v186, v[220:223] offset:47328\n"
"v_mfma_f32_16x16x32_bf16 v[220:223], a[80:83], v[32:35], 0\n"
"v_cvt_pk_bf16_f32 v232, v232, v233\n"
"v_cvt_pk_bf16_f32 v233, v234, v235\n"
"v_mfma_f32_16x16x32_bf16 v[216:219], a[88:91], v[36:39], v[216:219]\n"
"v_cvt_pk_bf16_f32 v240, v240, v241\n"
"v_cvt_pk_bf16_f32 v234, v236, v237\n"
"v_mfma_f32_16x16x32_bf16 v[220:223], a[92:95], v[36:39], v[220:223]\n"
"ds_read_b64_tr_b16 v[126:127], v183 offset:14408\n"
"ds_read_b64_tr_b16 v[128:129], v182 offset:13576\n"
"v_mfma_f32_16x16x32_bf16 v[216:219], a[100:103], v[40:43], v[216:219]\n"
"ds_read_b64_tr_b16 v[130:131], v183 offset:13576\n"
"v_cvt_pk_bf16_f32 v235, v238, v239\n"
"v_mfma_f32_16x16x32_bf16 v[220:223], a[104:107], v[40:43], v[220:223]\n"
"ds_read_b64_tr_b16 v[132:133], v182 offset:14344\n"
"v_cvt_pk_bf16_f32 v241, v242, v243\n"
"v_mfma_f32_16x16x32_bf16 v[216:219], a[112:115], v[44:47], v[216:219]\n"
"ds_read_b64_tr_b16 v[134:135], v183 offset:14344\n"
"v_fmamk_f32 v112, v112, 0x3e553b95, v170\n"
"v_mfma_f32_16x16x32_bf16 v[220:223], a[116:119], v[44:47], v[220:223]\n"
"ds_write_b128 v196, v[232:235] offset:43008\n"
"v_mfma_f32_16x16x32_bf16 v[216:219], a[124:127], v[48:51], v[216:219]\n"
"v_fmamk_f32 v113, v113, 0x3e553b95, v170\n"
"v_fmamk_f32 v114, v114, 0x3e553b95, v170\n"
"v_mfma_f32_16x16x32_bf16 v[220:223], a[128:131], v[48:51], v[220:223]\n"
"v_fmamk_f32 v115, v115, 0x3e553b95, v170\n"
"v_fmamk_f32 v116, v116, 0x3e553b95, v170\n"
"v_mfma_f32_16x16x32_bf16 v[216:219], a[136:139], v[52:55], v[216:219]\n"
"ds_write_b64 v197, v[240:241] offset:43008\n"
"v_mfma_f32_16x16x32_bf16 v[220:223], a[140:143], v[52:55], v[220:223]\n"
"v_exp_f32 v112, v112\n"
"v_exp_f32 v113, v113\n"
"v_exp_f32 v114, v114\n"
"v_exp_f32 v115, v115\n"
"v_cvt_pk_bf16_f32 v90, v112, v114\n"
"v_cvt_pk_bf16_f32 v91, v113, v115\n"
"v_fmamk_f32 v117, v117, 0x3e553b95, v170\n"
"v_fmamk_f32 v118, v118, 0x3e553b95, v170\n"
"v_fmamk_f32 v119, v119, 0x3e553b95, v170\n"
"v_exp_f32 v116, v116\n"
"v_exp_f32 v117, v117\n"
"v_exp_f32 v118, v118\n"
"v_exp_f32 v119, v119\n"
"v_cvt_pk_bf16_f32 v92, v116, v118\n"
"v_cvt_pk_bf16_f32 v93, v117, v119\n"
"s_mov_b64 exec, s[44:45]\n"
"v_add_u32 v181, 0xc0, v181\n"
"s_mov_b64 exec, -1\n"
"v_cvt_pk_bf16_f32 v228, v228, v229\n"
"v_cvt_pk_bf16_f32 v229, v230, v231\n"
"v_cvt_pk_bf16_f32 v230, v204, v205\n"
"v_cvt_pk_bf16_f32 v231, v206, v207\n"
"v_cvt_pk_bf16_f32 v208, v208, v209\n"
"v_cvt_pk_bf16_f32 v209, v210, v211\n"
"v_cvt_pk_bf16_f32 v212, v212, v213\n"
"v_cvt_pk_bf16_f32 v213, v214, v215\n"
"v_cvt_pk_bf16_f32 v214, v216, v217\n"
"v_cvt_pk_bf16_f32 v215, v218, v219\n"
"v_cvt_pk_bf16_f32 v210, v220, v221\n"
"v_cvt_pk_bf16_f32 v211, v222, v223\n"
"s_waitcnt vmcnt(3) lgkmcnt(0)\n"
"v_smfmac_f32_16x16x64_bf16 v[252:255], v[90:93], v[136:143], v66\n"
"s_barrier\n"
"v_smfmac_f32_16x16x64_bf16 v[98:101], v[90:93], v[120:127], v66\n"
"v_or_b32 v201, 11, s6\n"
"v_smfmac_f32_16x16x64_bf16 v[106:109], v[90:93], v[128:135], v66\n"
"s_mov_b64 exec, 1\n"
"buffer_store_dword v201, off, s[28:31], s40 sc1\n"
"s_mov_b64 exec, -1\n"
"v_mfma_f32_16x16x32_bf16 v[90:93], v[172:175], v[90:93], v[82:85]\n"
"ds_read_b128 v[72:75], v192 offset:30720\n"
"v_cvt_pk_bf16_f32 v252, v252, v253\n"
"v_cvt_pk_bf16_f32 v253, v254, v255\n"
"v_cvt_pk_bf16_f32 v98, v98, v99\n"
"v_cvt_pk_bf16_f32 v99, v100, v101\n"
"v_cvt_pk_bf16_f32 v106, v106, v107\n"
"v_cvt_pk_bf16_f32 v107, v108, v109\n"
"ds_read_b128 v[244:247], v178\n"
"v_rcp_f32 v171, v90\n"
"ds_read_b128 v[112:115], v179\n"
"v_cvt_pk_bf16_f32 v171, v171, v171\n"
"v_and_b32 v64, v171, v176\n"
"v_and_b32 v65, v171, v177\n"
"s_nop 1\n"
"v_mfma_f32_16x16x16_bf16 v[252:255], v[252:253], v[64:65], 0\n"
"ds_read_b64 v[82:83], v178 offset:64\n"
"ds_read_b64 v[84:85], v179 offset:64\n"
"v_mfma_f32_16x16x16_bf16 v[98:101], v[98:99], v[64:65], 0\n"
"ds_read_b128 v[248:251], v178 offset:13504\n"
"v_mfma_f32_16x16x16_bf16 v[106:109], v[106:107], v[64:65], 0\n"
"s_waitcnt lgkmcnt(5)\n"
"ds_read_b128 v[116:119], v179 offset:13504\n"
"v_mfma_f32_16x16x32_bf16 v[232:235], a[216:219], v[72:75], 0\n"
"ds_read_b64 v[90:91], v178 offset:13568\n"
"v_cvt_pk_bf16_f32 v252, v252, v253\n"
"v_mfma_f32_16x16x32_bf16 v[236:239], a[220:223], v[72:75], 0\n"
"s_waitcnt lgkmcnt(6)\n"
"ds_write_b128 v202, a[204:207] offset:55296\n"
"v_mfma_f32_16x16x32_bf16 v[244:247], v[244:247], v[144:147], 0\n"
"s_waitcnt lgkmcnt(6)\n"
"v_cvt_pk_bf16_f32 v253, v254, v255\n"
"v_cvt_pk_bf16_f32 v254, v98, v99\n"
"v_mfma_f32_16x16x32_bf16 v[112:115], v[112:115], v[144:147], 0\n"
"v_cvt_pk_bf16_f32 v255, v100, v101\n"
"v_cvt_pk_bf16_f32 v106, v106, v107\n"
"v_mfma_f32_16x16x32_bf16 v[240:243], a[224:227], v[72:75], 0\n"
"s_waitcnt lgkmcnt(5)\n"
"ds_read_b128 v[72:75], v192 offset:33792\n"
"ds_read_b64 v[92:93], v179 offset:13568\n"
"v_mfma_f32_16x16x32_bf16 v[244:247], v[80:83], v[156:159], v[244:247]\n"
"s_waitcnt lgkmcnt(6)\n"
"ds_read_b128 v[136:139], v178 offset:27008\n"
"v_cvt_pk_bf16_f32 v107, v108, v109\n"
"v_mfma_f32_16x16x32_bf16 v[112:115], v[84:87], v[158:161], v[112:115]\n"
"s_waitcnt lgkmcnt(6)\n"
"ds_read_b128 v[120:123], v179 offset:27008\n"
"v_min_u32 v252, -2, v252\n"
"v_mfma_f32_16x16x32_bf16 v[248:251], v[248:251], v[144:147], 0\n"
"s_waitcnt lgkmcnt(6)\n"
"ds_read_b64 v[98:99], v178 offset:27072\n"
"v_min_u32 v253, -2, v253\n"
"v_mfma_f32_16x16x32_bf16 v[116:119], v[116:119], v[144:147], 0\n"
"s_waitcnt lgkmcnt(6)\n"
"ds_read_b64 v[100:101], v179 offset:27072\n"
"v_min_u32 v254, -2, v254\n"
"v_mfma_f32_16x16x32_bf16 v[248:251], v[88:91], v[156:159], v[248:251]\n"
"v_min_u32 v255, -2, v255\n"
"buffer_store_dwordx4 v[252:255], v192, s[28:31], s37 offen sc1\n"
"ds_read_b128 v[140:143], v180\n"
"ds_read_b128 v[124:127], v181\n"
"ds_read_b128 v[76:79], v202 offset:43008\n"
"v_min_u32 v106, -2, v106\n"
"v_min_u32 v107, -2, v107\n"
"v_max_f32 v244, v244, v112\n"
"v_max_f32 v245, v245, v113\n"
"v_max_f32 v246, v246, v114\n"
"v_max_f32 v247, v247, v115\n"
"v_max3_f32 v170, v244, v245, v246\n"
"v_mov_b64 v[252:253], 0\n"
"v_mov_b64 v[254:255], 0\n"
"s_waitcnt lgkmcnt(6)\n"
"v_mfma_f32_16x16x32_bf16 v[116:119], v[92:95], v[158:161], v[116:119]\n"
"s_waitcnt lgkmcnt(5)\n"
"buffer_store_dwordx2 v[106:107], v193, s[28:31], s37 offen offset:1024 sc1\n"
"v_mfma_f32_16x16x32_bf16 v[136:139], v[136:139], v[144:147], 0\n"
"s_waitcnt lgkmcnt(4)\n"
"ds_read_b64 v[106:107], v180 offset:64\n"
"ds_read_b64 v[108:109], v181 offset:64\n"
"v_mfma_f32_16x16x32_bf16 v[120:123], v[120:123], v[144:147], 0\n"
"s_waitcnt lgkmcnt(5)\n"
"ds_write_b128 v198, v[228:231]\n"
"v_mfma_f32_16x16x32_bf16 v[136:139], v[96:99], v[156:159], v[136:139]\n"
"s_waitcnt lgkmcnt(5)\n"
"v_max_f32 v248, v248, v116\n"
"v_max_f32 v249, v249, v117\n"
"v_mfma_f32_16x16x32_bf16 v[120:123], v[100:103], v[158:161], v[120:123]\n"
"s_waitcnt vmcnt(3) lgkmcnt(4)\n"
"v_max_f32 v250, v250, v118\n"
"v_add3_u32 v200, v200, s50, -10\n"
"v_mfma_f32_16x16x32_bf16 v[140:143], v[140:143], v[144:147], 0\n"
"s_waitcnt lgkmcnt(3)\n"
"v_cmp_lt_u32 vcc, 64, v200\n"
"v_max_f32 v251, v251, v119\n"
"v_mfma_f32_16x16x32_bf16 v[124:127], v[124:127], v[144:147], 0\n"
"s_add_u32 s37, s37, 1572864\n"
"ds_write_b64 v203, a[208:209] offset:55296\n"
"buffer_store_dwordx4 v[76:79], v194, s[24:27], s36 offen sc1\n"
"s_nop 1\n"
"ds_read_b128 v[76:79], v192 offset:31744\n"
"ds_read_b64_tr_b16 v[128:129], v182 offset:128\n"
"ds_read_b128 v[68:71], v202 offset:47104\n"
"v_max3_f32 v170, v170, v247, v248\n"
"v_max3_f32 v170, v170, v249, v250\n"
"v_max_f32 v136, v136, v120\n"
"v_max_f32 v137, v137, v121\n"
"v_max_f32 v138, v138, v122\n"
"v_max_f32 v139, v139, v123\n"
"v_max3_f32 v170, v170, v251, v136\n"
"v_max3_f32 v170, v170, v137, v138\n"
"v_mov_b64 v[98:99], 0\n"
"v_mov_b64 v[100:101], 0\n"
"s_waitcnt lgkmcnt(5)\n"
"v_mfma_f32_16x16x32_bf16 v[140:143], v[104:107], v[156:159], v[140:143]\n"
"s_cbranch_vccnz .Lp9%=\n"
".Lr9%=:\n"
"v_mfma_f32_16x16x32_bf16 v[124:127], v[108:111], v[158:161], v[124:127]\n"
"buffer_load_dwordx4 a[144:147], v192, s[28:31], s12 offen sc1\n"
"ds_read_b64_tr_b16 v[130:131], v183 offset:128\n"
"ds_read_b64_tr_b16 v[132:133], v182 offset:896\n"
"ds_read_b64_tr_b16 v[134:135], v183 offset:896\n"
"v_mov_b64 v[106:107], 0\n"
"v_mov_b64 v[108:109], 0\n"
"ds_read_b64_tr_b16 v[112:113], v182 offset:136\n"
"s_nop 0\n"
"v_max_f32 v140, v140, v124\n"
"v_max_f32 v141, v141, v125\n"
"v_max_f32 v142, v142, v126\n"
"v_max_f32 v143, v143, v127\n"
"v_max3_f32 v170, v170, v139, v140\n"
"v_max3_f32 v170, v170, v141, v142\n"
"v_max_f32 v171, v170, v143\n"
"v_max_f32 v170, v170, v143\n"
"s_waitcnt lgkmcnt(6)\n"
"v_mfma_f32_16x16x32_bf16 v[232:235], a[228:231], v[76:79], v[232:235]\n"
"s_waitcnt lgkmcnt(4)\n"
"buffer_load_dwordx2 a[148:149], v193, s[28:31], s12 offen offset:1024 sc1\n"
"ds_read_b64_tr_b16 v[114:115], v183 offset:136\n"
"v_mfma_f32_16x16x32_bf16 v[236:239], a[232:235], v[76:79], v[236:239]\n"
"ds_read_b64_tr_b16 v[116:117], v182 offset:904\n"
"ds_write_b128 v202, a[210:213] offset:36864\n"
"v_mfma_f32_16x16x32_bf16 v[240:243], a[236:239], v[76:79], v[240:243]\n"
"buffer_load_dwordx4 a[150:153], v192, s[28:31], s13 offen sc1\n"
"v_permlane16_swap_b32 v170, v171\n"
"v_max_f32 v170, v170, v171\n"
"v_mov_b32 v171, v170\n"
"ds_read_b128 v[76:79], v192 offset:34816\n"
"ds_read_b64_tr_b16 v[118:119], v183 offset:904\n"
"ds_read_b64_tr_b16 v[120:121], v182 offset:72\n"
"v_permlane32_swap_b32 v170, v171\n"
"v_max_f32 v170, v170, v171\n"
"ds_read_b64_tr_b16 v[122:123], v183 offset:72\n"
"buffer_load_dwordx2 a[154:155], v193, s[28:31], s13 offen offset:1024 sc1\n"
"ds_read_b64_tr_b16 v[124:125], v182 offset:840\n"
"v_mul_f32 v170, 0xbe553b95, v170\n"
"v_fmamk_f32 v244, v244, 0x3e553b95, v170\n"
"v_fmamk_f32 v245, v245, 0x3e553b95, v170\n"
"ds_read_b64_tr_b16 v[126:127], v183 offset:840\n"
"v_fmamk_f32 v246, v246, 0x3e553b95, v170\n"
"v_fmamk_f32 v247, v247, 0x3e553b95, v170\n"
"ds_write_b64 v203, a[214:215] offset:36864\n"
"v_exp_f32 v244, v244\n"
"buffer_store_dwordx4 v[68:71], v195, s[24:27], s36 offen sc1\n"
"s_nop 1\n"
"ds_read_b128 v[68:71], v192 offset:32768\n"
"s_add_u32 s36, s36, s33\n"
"v_exp_f32 v245, v245\n"
"v_exp_f32 v246, v246\n"
"v_exp_f32 v247, v247\n"
"v_cvt_pk_bf16_f32 v82, v244, v246\n"
"v_cvt_pk_bf16_f32 v83, v245, v247\n"
"v_fmamk_f32 v248, v248, 0x3e553b95, v170\n"
"v_fmamk_f32 v249, v249, 0x3e553b95, v170\n"
"v_fmamk_f32 v250, v250, 0x3e553b95, v170\n"
"v_fmamk_f32 v251, v251, 0x3e553b95, v170\n"
"v_exp_f32 v248, v248\n"
"v_exp_f32 v249, v249\n"
"v_exp_f32 v250, v250\n"
"v_exp_f32 v251, v251\n"
"v_cvt_pk_bf16_f32 v84, v248, v250\n"
"v_cvt_pk_bf16_f32 v85, v249, v251\n"
"v_fmamk_f32 v136, v136, 0x3e553b95, v170\n"
"v_fmamk_f32 v137, v137, 0x3e553b95, v170\n"
"v_fmamk_f32 v138, v138, 0x3e553b95, v170\n"
"v_fmamk_f32 v139, v139, 0x3e553b95, v170\n"
"v_exp_f32 v136, v136\n"
"v_exp_f32 v137, v137\n"
"v_exp_f32 v138, v138\n"
"v_exp_f32 v139, v139\n"
"v_cvt_pk_bf16_f32 v90, v136, v138\n"
"v_cvt_pk_bf16_f32 v91, v137, v139\n"
"v_fmamk_f32 v140, v140, 0x3e553b95, v170\n"
"v_fmamk_f32 v141, v141, 0x3e553b95, v170\n"
"v_fmamk_f32 v142, v142, 0x3e553b95, v170\n"
"v_fmamk_f32 v143, v143, 0x3e553b95, v170\n"
"v_exp_f32 v140, v140\n"
"v_exp_f32 v141, v141\n"
"v_exp_f32 v142, v142\n"
"v_exp_f32 v143, v143\n"
"v_cvt_pk_bf16_f32 v92, v140, v142\n"
"v_cvt_pk_bf16_f32 v93, v141, v143\n"
"s_waitcnt lgkmcnt(0)\n"
"v_mfma_f32_16x16x32_bf16 v[232:235], a[240:243], v[68:71], v[232:235]\n"
"ds_read_b64_tr_b16 v[244:245], v182 offset:27136\n"
"ds_read_b64_tr_b16 v[246:247], v183 offset:27136\n"
"v_mfma_f32_16x16x32_bf16 v[236:239], a[244:247], v[68:71], v[236:239]\n"
"ds_read_b64_tr_b16 v[248:249], v182 offset:27904\n"
"v_mfma_f32_16x16x32_bf16 v[240:243], a[248:251], v[68:71], v[240:243]\n"
"ds_read_b128 v[68:71], v192 offset:35840\n"
"v_smfmac_f32_16x16x64_bf16 v[252:255], v[82:85], v[128:135], v66\n"
"ds_read_b64_tr_b16 v[250:251], v183 offset:27904\n"
"v_mfma_f32_16x16x32_bf16 v[232:235], a[252:255], v[72:75], v[232:235]\n"
"ds_read_b64_tr_b16 v[128:129], v182 offset:27144\n"
"v_smfmac_f32_16x16x64_bf16 v[98:101], v[82:85], v[112:119], v66\n"
"ds_read_b64_tr_b16 v[130:131], v183 offset:27144\n"
"v_mfma_f32_16x16x32_bf16 v[236:239], v[0:3], v[72:75], v[236:239]\n"
"ds_write_b128 v198, v[208:211] offset:64\n"
"v_smfmac_f32_16x16x64_bf16 v[106:109], v[82:85], v[120:127], v66\n"
"v_mfma_f32_16x16x32_bf16 v[240:243], v[4:7], v[72:75], v[240:243]\n"
"ds_read_b64_tr_b16 v[132:133], v182 offset:27912\n"
"v_mfma_f32_16x16x32_bf16 v[82:85], v[172:175], v[82:85], 0\n"
"ds_read_b64_tr_b16 v[134:135], v183 offset:27912\n"
"buffer_load_dword v201, off, s[28:31], s39 sc1\n"
"v_mfma_f32_16x16x32_bf16 v[232:235], v[8:11], v[76:79], v[232:235]\n"
"s_waitcnt lgkmcnt(6)\n"
"ds_read_b64_tr_b16 v[112:113], v182 offset:27080\n"
"ds_read_b64_tr_b16 v[114:115], v183 offset:27080\n"
"v_mfma_f32_16x16x32_bf16 v[236:239], v[12:15], v[76:79], v[236:239]\n"
"s_waitcnt lgkmcnt(7)\n"
"ds_read_b64_tr_b16 v[116:117], v182 offset:27848\n"
".Lic7%=:\n"
"s_cbranch_execz .Lib1%=\n"
"v_mfma_f32_16x16x32_bf16 v[240:243], v[16:19], v[76:79], v[240:243]\n"
"ds_read_b64_tr_b16 v[118:119], v183 offset:27848\n"
"v_mfma_f32_16x16x32_bf16 v[232:235], v[20:23], v[68:71], v[232:235]\n"
"ds_write_b128 v198, v[212:215] offset:128\n"
"v_mfma_f32_16x16x32_bf16 v[236:239], v[24:27], v[68:71], v[236:239]\n"
"v_smfmac_f32_16x16x64_bf16 v[252:255], v[90:93], v[244:251], v66\n"
"ds_read_b128 v[120:123], v178 offset:13504\n"
"v_mfma_f32_16x16x32_bf16 v[240:243], v[28:31], v[68:71], v[240:243]\n"
"s_waitcnt lgkmcnt(6)\n"
"ds_read_b128 v[136:139], v179 offset:13504\n"
"ds_read_b128 v[124:127], v178 offset:27008\n"
"v_smfmac_f32_16x16x64_bf16 v[98:101], v[90:93], v[128:135], v66\n"
"v_cvt_pk_bf16_f32 v232, v232, v233\n"
"v_cvt_pk_bf16_f32 v233, v234, v235\n"
"v_cvt_pk_bf16_f32 v234, v236, v237\n"
"v_cvt_pk_bf16_f32 v252, v252, v253\n"
"v_cvt_pk_bf16_f32 v253, v254, v255\n"
"v_cvt_pk_bf16_f32 v235, v238, v239\n"
"v_cvt_pk_bf16_f32 v240, v240, v241\n"
"v_cvt_pk_bf16_f32 v241, v242, v243\n"
"v_cvt_pk_bf16_f32 v98, v98, v99\n"
"v_cvt_pk_bf16_f32 v99, v100, v101\n"
"ds_write_b128 v196, v[232:235] offset:49152\n"
"s_waitcnt lgkmcnt(5)\n"
"v_smfmac_f32_16x16x64_bf16 v[106:109], v[90:93], v[112:119], v66\n"
"s_waitcnt lgkmcnt(4)\n"
"s_barrier\n"
"v_mfma_f32_16x16x32_bf16 v[90:93], v[172:175], v[90:93], v[82:85]\n"
"s_waitcnt lgkmcnt(2)\n"
"ds_read_b128 v[72:75], v192 offset:55296\n"
"ds_read_b128 v[76:79], v192 offset:56320\n"
"v_mfma_f32_16x16x32_bf16 v[120:123], v[120:123], v[148:151], 0\n"
"s_waitcnt lgkmcnt(3)\n"
"ds_read_b128 v[68:71], v192 offset:57344\n"
"v_cvt_pk_bf16_f32 v106, v106, v107\n"
"v_mfma_f32_16x16x32_bf16 v[136:139], v[136:139], v[148:151], 0\n"
"ds_write_b64 v197, v[240:241] offset:49152\n"
"v_mfma_f32_16x16x32_bf16 v[124:127], v[124:127], v[148:151], 0\n"
"v_rcp_f32 v171, v90\n"
"v_cvt_pk_bf16_f32 v107, v108, v109\n"
"v_cvt_pk_bf16_f32 v171, v171, v171\n"
"v_and_b32 v64, v171, v176\n"
"v_and_b32 v65, v171, v177\n"
"s_nop 1\n"
"v_mfma_f32_16x16x16_bf16 v[252:255], v[252:253], v[64:65], 0\n"
"ds_read_b64 v[82:83], v178 offset:13568\n"
"ds_read_b64 v[84:85], v179 offset:13568\n"
"v_mfma_f32_16x16x16_bf16 v[98:101], v[98:99], v[64:65], 0\n"
"ds_read_b128 v[140:143], v179 offset:27008\n"
"v_mfma_f32_16x16x16_bf16 v[106:109], v[106:107], v[64:65], 0\n"
"s_waitcnt lgkmcnt(6)\n"
"ds_read_b64 v[90:91], v178 offset:27072\n"
"v_mfma_f32_16x16x32_bf16 v[232:235], a[216:219], v[72:75], 0\n"
"s_waitcnt lgkmcnt(6)\n"
"ds_read_b64 v[92:93], v179 offset:27072\n"
"v_cvt_pk_bf16_f32 v252, v252, v253\n"
"v_mfma_f32_16x16x32_bf16 v[236:239], a[220:223], v[72:75], 0\n"
"ds_read_b128 v[244:247], v180\n"
"v_cvt_pk_bf16_f32 v253, v254, v255\n"
"v_mfma_f32_16x16x32_bf16 v[240:243], a[224:227], v[72:75], 0\n"
"buffer_load_dwordx4 v[228:231], v192, s[28:31], s14 offen sc1\n"
"ds_read_b128 v[72:75], v192 offset:58368\n"
"v_mfma_f32_16x16x32_bf16 v[232:235], a[228:231], v[76:79], v[232:235]\n"
"s_waitcnt lgkmcnt(8)\n"
"ds_read_b128 v[128:131], v181\n"
"v_cvt_pk_bf16_f32 v254, v98, v99\n"
"v_mfma_f32_16x16x32_bf16 v[236:239], a[232:235], v[76:79], v[236:239]\n"
"ds_read_b64 v[98:99], v180 offset:64\n"
"v_cvt_pk_bf16_f32 v255, v100, v101\n"
"v_mfma_f32_16x16x32_bf16 v[240:243], a[236:239], v[76:79], v[240:243]\n"
"s_waitcnt lgkmcnt(8)\n"
"buffer_load_dwordx2 v[152:153], v193, s[28:31], s14 offen offset:1024 sc1\n"
"ds_read_b128 v[76:79], v192 offset:59392\n"
"v_mfma_f32_16x16x32_bf16 v[120:123], v[80:83], v[160:163], v[120:123]\n"
"s_waitcnt lgkmcnt(8)\n"
"ds_read_b64 v[100:101], v181 offset:64\n"
"v_cvt_pk_bf16_f32 v106, v106, v107\n"
"v_mfma_f32_16x16x32_bf16 v[136:139], v[84:87], v[162:165], v[136:139]\n"
"s_waitcnt lgkmcnt(8)\n"
"ds_read_b128 v[248:251], v180 offset:13504\n"
"v_cvt_pk_bf16_f32 v107, v108, v109\n"
"v_mfma_f32_16x16x32_bf16 v[232:235], a[240:243], v[68:71], v[232:235]\n"
"s_waitcnt lgkmcnt(8)\n"
"ds_read_b128 v[132:135], v181 offset:13504\n"
"v_min_u32 v252, -2, v252\n"
"v_mfma_f32_16x16x32_bf16 v[236:239], a[244:247], v[68:71], v[236:239]\n"
"ds_read_b64 v[108:109], v181 offset:13568\n"
"v_min_u32 v253, -2, v253\n"
"v_mfma_f32_16x16x32_bf16 v[240:243], a[248:251], v[68:71], v[240:243]\n"
"s_waitcnt lgkmcnt(8)\n"
"ds_read_b128 v[68:71], v192 offset:60416\n"
"v_min_u32 v254, -2, v254\n"
"v_mfma_f32_16x16x32_bf16 v[140:143], v[140:143], v[148:151], 0\n"
"s_waitcnt lgkmcnt(8)\n"
"v_min_u32 v255, -2, v255\n"
"v_min_u32 v106, -2, v106\n"
"v_mfma_f32_16x16x32_bf16 v[124:127], v[88:91], v[160:163], v[124:127]\n"
"s_waitcnt lgkmcnt(7)\n"
"buffer_store_dwordx4 v[252:255], v192, s[28:31], s37 offen sc1\n"
"v_mfma_f32_16x16x32_bf16 v[140:143], v[92:95], v[162:165], v[140:143]\n"
"v_min_u32 v107, -2, v107\n"
"v_max_f32 v120, v120, v136\n"
"v_mfma_f32_16x16x32_bf16 v[232:235], a[252:255], v[72:75], v[232:235]\n"
"s_waitcnt lgkmcnt(5)\n"
"ds_read_b64_tr_b16 v[112:113], v182 offset:13632\n"
"buffer_store_dwordx2 v[106:107], v193, s[28:31], s37 offen offset:1024 sc1\n"
"v_mfma_f32_16x16x32_bf16 v[236:239], v[0:3], v[72:75], v[236:239]\n"
"s_waitcnt lgkmcnt(3)\n"
"ds_read_b64 v[106:107], v180 offset:13568\n"
"ds_read_b64_tr_b16 v[114:115], v183 offset:13632\n"
"v_mfma_f32_16x16x32_bf16 v[240:243], v[4:7], v[72:75], v[240:243]\n"
"s_add_u32 s37, s37, 1572864\n"
"ds_read_b128 v[72:75], v192 offset:36864\n"
"v_max_f32 v121, v121, v137\n"
"v_mfma_f32_16x16x32_bf16 v[244:247], v[244:247], v[148:151], 0\n"
"ds_read_b64_tr_b16 v[116:117], v182 offset:14400\n"
"v_max_f32 v122, v122, v138\n"
"v_mfma_f32_16x16x32_bf16 v[232:235], v[8:11], v[76:79], v[232:235]\n"
"s_waitcnt lgkmcnt(5)\n"
"ds_read_b64_tr_b16 v[118:119], v183 offset:14400\n"
"v_max_f32 v123, v123, v139\n"
"v_mfma_f32_16x16x32_bf16 v[236:239], v[12:15], v[76:79], v[236:239]\n"
"ds_read_b64_tr_b16 v[136:137], v182 offset:13640\n"
"v_max3_f32 v170, v120, v121, v122\n"
"v_mfma_f32_16x16x32_bf16 v[240:243], v[16:19], v[76:79], v[240:243]\n"
"ds_read_b128 v[76:79], v192 offset:37888\n"
"v_max_f32 v124, v124, v140\n"
"v_mfma_f32_16x16x32_bf16 v[128:131], v[128:131], v[148:151], 0\n"
"ds_read_b64_tr_b16 v[138:139], v183 offset:13640\n"
"v_max_f32 v125, v125, v141\n"
"v_mfma_f32_16x16x32_bf16 v[232:235], v[20:23], v[68:71], v[232:235]\n"
"ds_read_b64_tr_b16 v[140:141], v182 offset:14408\n"
"v_max_f32 v126, v126, v142\n"
"v_mfma_f32_16x16x32_bf16 v[236:239], v[24:27], v[68:71], v[236:239]\n"
"v_max_f32 v127, v127, v143\n"
"v_max3_f32 v170, v170, v123, v124\n"
"v_mfma_f32_16x16x32_bf16 v[240:243], v[28:31], v[68:71], v[240:243]\n"
"ds_read_b128 v[68:71], v192 offset:38912\n"
"ds_read_b64_tr_b16 v[142:143], v183 offset:14408\n"
"v_mfma_f32_16x16x32_bf16 v[244:247], v[96:99], v[160:163], v[244:247]\n"
"s_waitcnt lgkmcnt(8)\n"
"v_max3_f32 v170, v170, v125, v126\n"
"v_mov_b64 v[252:253], 0\n"
"v_mfma_f32_16x16x32_bf16 v[128:131], v[100:103], v[162:165], v[128:131]\n"
"s_waitcnt vmcnt(9)\n"
"v_mov_b64 v[254:255], 0\n"
"v_mov_b64 v[98:99], 0\n"
"v_mfma_f32_16x16x32_bf16 v[248:251], v[248:251], v[148:151], 0\n"
"ds_write_b128 v202, a[144:147] offset:24576\n"
"v_mfma_f32_16x16x32_bf16 v[204:207], a[216:219], v[72:75], 0\n"
"s_waitcnt lgkmcnt(5)\n"
"v_mov_b64 v[100:101], 0\n"
"v_max_f32 v244, v244, v128\n"
"v_mfma_f32_16x16x32_bf16 v[208:211], a[220:223], v[72:75], 0\n"
"v_max_f32 v245, v245, v129\n"
"v_max_f32 v246, v246, v130\n"
"v_mfma_f32_16x16x32_bf16 v[212:215], a[224:227], v[72:75], 0\n"
"ds_read_b128 v[72:75], v192 offset:39936\n"
"ds_read_b64_tr_b16 v[128:129], v182 offset:13576\n"
"v_mfma_f32_16x16x32_bf16 v[132:135], v[132:135], v[148:151], 0\n"
"s_waitcnt vmcnt(6)\n"
"ds_write_b64 v203, a[148:149] offset:24576\n"
"v_mfma_f32_16x16x32_bf16 v[204:207], a[228:231], v[76:79], v[204:207]\n"
"s_waitcnt lgkmcnt(5)\n"
"v_max_f32 v247, v247, v131\n"
"v_max3_f32 v170, v170, v127, v244\n"
"v_mfma_f32_16x16x32_bf16 v[208:211], a[232:235], v[76:79], v[208:211]\n"
"ds_write_b128 v202, a[150:153] offset:30720\n"
"v_mfma_f32_16x16x32_bf16 v[212:215], a[236:239], v[76:79], v[212:215]\n"
"v_max3_f32 v170, v170, v245, v246\n"
"v_cvt_pk_bf16_f32 v232, v232, v233\n"
"v_mfma_f32_16x16x32_bf16 v[248:251], v[104:107], v[160:163], v[248:251]\n"
"v_mov_b64 v[106:107], 0\n"
"v_cvt_pk_bf16_f32 v233, v234, v235\n"
"v_mfma_f32_16x16x32_bf16 v[204:207], a[240:243], v[68:71], v[204:207]\n"
"ds_write_b64 v203, a[154:155] offset:30720\n"
"v_mfma_f32_16x16x32_bf16 v[208:211], a[244:247], v[68:71], v[208:211]\n"
"s_waitcnt lgkmcnt(4)\n"
"v_cvt_pk_bf16_f32 v234, v236, v237\n"
"v_cvt_pk_bf16_f32 v235, v238, v239\n"
"v_mfma_f32_16x16x32_bf16 v[212:215], a[248:251], v[68:71], v[212:215]\n"
"ds_read_b128 v[76:79], v192 offset:40960\n"
"ds_read_b128 v[68:71], v192 offset:41984\n"
"v_mfma_f32_16x16x32_bf16 v[132:135], v[108:111], v[162:165], v[132:135]\n"
"ds_read_b64_tr_b16 v[130:131], v183 offset:13576\n"
"v_mov_b64 v[108:109], 0\n"
"v_mfma_f32_16x16x32_bf16 v[204:207], a[252:255], v[72:75], v[204:207]\n"
"ds_write_b128 v196, v[232:235]\n"
"v_mfma_f32_16x16x32_bf16 v[208:211], v[0:3], v[72:75], v[208:211]\n"
"s_waitcnt vmcnt(4)\n"
"v_add3_u32 v201, v201, s50, -11\n"
"v_cmp_lt_u32 vcc, 64, v201\n"
"v_mfma_f32_16x16x32_bf16 v[212:215], v[4:7], v[72:75], v[212:215]\n"
"v_max_f32 v248, v248, v132\n"
"v_max_f32 v249, v249, v133\n"
"v_max_f32 v250, v250, v134\n"
"v_max_f32 v251, v251, v135\n"
"v_max3_f32 v170, v170, v247, v248\n"
"v_max3_f32 v170, v170, v249, v250\n"
"v_max_f32 v171, v170, v251\n"
"v_max_f32 v170, v170, v251\n"
"v_cvt_pk_bf16_f32 v240, v240, v241\n"
"v_cvt_pk_bf16_f32 v241, v242, v243\n"
"v_permlane16_swap_b32 v170, v171\n"
"v_max_f32 v170, v170, v171\n"
"v_mov_b32 v171, v170\n"
"s_waitcnt lgkmcnt(3)\n"
"v_mfma_f32_16x16x32_bf16 v[204:207], v[8:11], v[76:79], v[204:207]\n"
"s_waitcnt lgkmcnt(2)\n"
"ds_read_b64_tr_b16 v[132:133], v182 offset:14344\n"
"ds_read_b64_tr_b16 v[134:135], v183 offset:14344\n"
"v_mfma_f32_16x16x32_bf16 v[208:211], v[12:15], v[76:79], v[208:211]\n"
"s_cbranch_vccnz .Lp10%=\n"
".Lr10%=:\n"
"v_mfma_f32_16x16x32_bf16 v[212:215], v[16:19], v[76:79], v[212:215]\n"
"s_barrier\n"
"v_mfma_f32_16x16x32_bf16 v[204:207], v[20:23], v[68:71], v[204:207]\n"
"buffer_load_dwordx4 a[162:165], v192, s[28:31], s15 offen sc1\n"
"ds_read_b128 v[32:35], v192 offset:25600\n"
"v_mfma_f32_16x16x32_bf16 v[208:211], v[24:27], v[68:71], v[208:211]\n"
"ds_read_b128 v[36:39], v192 offset:26624\n"
"ds_read_b128 v[40:43], v192 offset:27648\n"
"v_mfma_f32_16x16x32_bf16 v[212:215], v[28:31], v[68:71], v[212:215]\n"
"ds_read_b128 v[68:71], v192 offset:24576\n"
"v_permlane32_swap_b32 v170, v171\n"
"ds_read_b128 v[44:47], v192 offset:28672\n"
"buffer_load_dwordx2 a[166:167], v193, s[28:31], s15 offen offset:1024 sc1\n"
"ds_read_b128 v[48:51], v192 offset:29696\n"
"v_max_f32 v170, v170, v171\n"
"v_mul_f32 v170, 0xbe553b95, v170\n"
"v_fmamk_f32 v120, v120, 0x3e553b95, v170\n"
"ds_read_b128 v[52:55], v192 offset:30720\n"
"v_fmamk_f32 v121, v121, 0x3e553b95, v170\n"
"v_fmamk_f32 v122, v122, 0x3e553b95, v170\n"
"v_fmamk_f32 v123, v123, 0x3e553b95, v170\n"
"v_exp_f32 v120, v120\n"
"v_exp_f32 v121, v121\n"
"v_exp_f32 v122, v122\n"
"v_exp_f32 v123, v123\n"
"v_cvt_pk_bf16_f32 v82, v120, v122\n"
"v_cvt_pk_bf16_f32 v83, v121, v123\n"
"v_fmamk_f32 v124, v124, 0x3e553b95, v170\n"
"v_fmamk_f32 v125, v125, 0x3e553b95, v170\n"
"v_fmamk_f32 v126, v126, 0x3e553b95, v170\n"
"v_fmamk_f32 v127, v127, 0x3e553b95, v170\n"
"v_exp_f32 v124, v124\n"
"v_exp_f32 v125, v125\n"
"v_exp_f32 v126, v126\n"
"v_exp_f32 v127, v127\n"
"v_cvt_pk_bf16_f32 v84, v124, v126\n"
"v_cvt_pk_bf16_f32 v85, v125, v127\n"
"v_fmamk_f32 v244, v244, 0x3e553b95, v170\n"
"v_fmamk_f32 v245, v245, 0x3e553b95, v170\n"
"v_smfmac_f32_16x16x64_bf16 v[252:255], v[82:85], v[112:119], v66\n"
"s_waitcnt lgkmcnt(3)\n"
"ds_read_b64_tr_b16 v[120:121], v182 offset:40640\n"
"v_smfmac_f32_16x16x64_bf16 v[98:101], v[82:85], v[136:143], v66\n"
"s_waitcnt lgkmcnt(1)\n"
"ds_read_b64_tr_b16 v[122:123], v183 offset:40640\n"
"v_smfmac_f32_16x16x64_bf16 v[106:109], v[82:85], v[128:135], v66\n"
"ds_read_b64_tr_b16 v[124:125], v182 offset:41408\n"
"v_mfma_f32_16x16x32_bf16 v[82:85], v[172:175], v[82:85], 0\n"
"ds_read_b64_tr_b16 v[126:127], v183 offset:41408\n"
"ds_write_b64 v197, v[240:241]\n"
"v_mfma_f32_16x16x32_bf16 v[216:219], a[216:219], v[68:71], 0\n"
"v_fmamk_f32 v246, v246, 0x3e553b95, v170\n"
"v_fmamk_f32 v247, v247, 0x3e553b95, v170\n"
"v_mfma_f32_16x16x32_bf16 v[220:223], a[220:223], v[68:71], 0\n"
"ds_read_b64_tr_b16 v[112:113], v182 offset:40648\n"
"ds_read_b64_tr_b16 v[114:115], v183 offset:40648\n"
"v_mfma_f32_16x16x32_bf16 v[224:227], a[224:227], v[68:71], 0\n"
"ds_read_b128 v[68:71], v192 offset:33792\n"
"v_fmamk_f32 v248, v248, 0x3e553b95, v170\n"
"v_mfma_f32_16x16x32_bf16 v[216:219], a[228:231], v[32:35], v[216:219]\n"
"ds_read_b64_tr_b16 v[116:117], v182 offset:41416\n"
"v_fmamk_f32 v249, v249, 0x3e553b95, v170\n"
"v_mfma_f32_16x16x32_bf16 v[220:223], a[232:235], v[32:35], v[220:223]\n"
"ds_read_b64_tr_b16 v[118:119], v183 offset:41416\n"
"v_fmamk_f32 v250, v250, 0x3e553b95, v170\n"
"v_mfma_f32_16x16x32_bf16 v[224:227], a[236:239], v[32:35], v[224:227]\n"
"ds_read_b128 v[32:35], v192 offset:34816\n"
"v_fmamk_f32 v251, v251, 0x3e553b95, v170\n"
"v_mfma_f32_16x16x32_bf16 v[216:219], a[240:243], v[36:39], v[216:219]\n"
"ds_read_b64_tr_b16 v[136:137], v182 offset:40584\n"
"v_exp_f32 v244, v244\n"
"v_mfma_f32_16x16x32_bf16 v[220:223], a[244:247], v[36:39], v[220:223]\n"
"ds_read_b64_tr_b16 v[138:139], v183 offset:40584\n"
"ds_read_b64_tr_b16 v[140:141], v182 offset:41352\n"
"v_mfma_f32_16x16x32_bf16 v[224:227], a[248:251], v[36:39], v[224:227]\n"
"s_waitcnt vmcnt(5)\n"
"v_exp_f32 v245, v245\n"
"v_mfma_f32_16x16x32_bf16 v[216:219], a[252:255], v[40:43], v[216:219]\n"
"ds_read_b128 v[36:39], v192 offset:35840\n"
"ds_read_b64_tr_b16 v[142:143], v183 offset:41352\n"
"v_mfma_f32_16x16x32_bf16 v[220:223], v[0:3], v[40:43], v[220:223]\n"
"v_exp_f32 v246, v246\n"
"v_mfma_f32_16x16x32_bf16 v[224:227], v[4:7], v[40:43], v[224:227]\n"
"v_exp_f32 v247, v247\n"
"v_mfma_f32_16x16x32_bf16 v[216:219], v[8:11], v[44:47], v[216:219]\n"
"v_cvt_pk_bf16_f32 v90, v244, v246\n"
"v_cvt_pk_bf16_f32 v91, v245, v247\n"
"v_mfma_f32_16x16x32_bf16 v[220:223], v[12:15], v[44:47], v[220:223]\n"
"v_exp_f32 v248, v248\n"
"v_mfma_f32_16x16x32_bf16 v[224:227], v[16:19], v[44:47], v[224:227]\n"
"v_exp_f32 v249, v249\n"
"v_mfma_f32_16x16x32_bf16 v[216:219], v[20:23], v[48:51], v[216:219]\n"
"v_exp_f32 v250, v250\n"
"v_mfma_f32_16x16x32_bf16 v[220:223], v[24:27], v[48:51], v[220:223]\n"
"v_exp_f32 v251, v251\n"
"v_mfma_f32_16x16x32_bf16 v[224:227], v[28:31], v[48:51], v[224:227]\n"
"v_cvt_pk_bf16_f32 v92, v248, v250\n"
"v_cvt_pk_bf16_f32 v93, v249, v251\n"
"v_mfma_f32_16x16x32_bf16 v[232:235], a[216:219], v[52:55], 0\n"
"s_waitcnt lgkmcnt(0)\n"
"ds_read_b128 v[72:75], v202 offset:49152\n"
"ds_read_b128 v[76:79], v202 offset:53248\n"
"v_smfmac_f32_16x16x64_bf16 v[252:255], v[90:93], v[120:127], v66\n"
"v_cvt_pk_bf16_f32 v204, v204, v205\n"
"v_smfmac_f32_16x16x64_bf16 v[98:101], v[90:93], v[112:119], v66\n"
"v_cvt_pk_bf16_f32 v205, v206, v207\n"
"v_smfmac_f32_16x16x64_bf16 v[106:109], v[90:93], v[136:143], v66\n"
"v_cvt_pk_bf16_f32 v206, v208, v209\n"
"v_mfma_f32_16x16x32_bf16 v[90:93], v[172:175], v[90:93], v[82:85]\n"
"v_cvt_pk_bf16_f32 v207, v210, v211\n"
"v_cvt_pk_bf16_f32 v212, v212, v213\n"
"v_mfma_f32_16x16x32_bf16 v[236:239], a[220:223], v[52:55], 0\n"
"ds_write_b128 v196, v[204:207] offset:12288\n"
"v_mfma_f32_16x16x32_bf16 v[240:243], a[224:227], v[52:55], 0\n"
"v_cvt_pk_bf16_f32 v252, v252, v253\n"
"v_cvt_pk_bf16_f32 v253, v254, v255\n"
"buffer_load_dwordx4 v[144:147], v192, s[28:31], s16 offen sc1\n"
"v_rcp_f32 v171, v90\n"
"v_cvt_pk_bf16_f32 v98, v98, v99\n"
"v_cvt_pk_bf16_f32 v171, v171, v171\n"
"v_and_b32 v64, v171, v176\n"
"v_and_b32 v65, v171, v177\n"
"v_cvt_pk_bf16_f32 v99, v100, v101\n"
"v_cvt_pk_bf16_f32 v106, v106, v107\n"
"v_mfma_f32_16x16x16_bf16 v[252:255], v[252:253], v[64:65], 0\n"
"s_waitcnt lgkmcnt(1)\n"
"v_cvt_pk_bf16_f32 v107, v108, v109\n"
"v_cvt_pk_bf16_f32 v213, v214, v215\n"
"v_mfma_f32_16x16x16_bf16 v[98:101], v[98:99], v[64:65], 0\n"
"buffer_load_dwordx2 v[154:155], v193, s[28:31], s16 offen offset:1024 sc1\n"
"ds_write_b64 v197, v[212:213] offset:12288\n"
"v_mfma_f32_16x16x16_bf16 v[106:109], v[106:107], v[64:65], 0\n"
"s_nop 0\n"
"v_cvt_pk_bf16_f32 v252, v252, v253\n"
"v_cvt_pk_bf16_f32 v253, v254, v255\n"
"v_min_u32 v252, -2, v252\n"
"s_nop 0\n"
"v_cvt_pk_bf16_f32 v254, v98, v99\n"
"v_cvt_pk_bf16_f32 v255, v100, v101\n"
"v_min_u32 v253, -2, v253\n"
"v_cvt_pk_bf16_f32 v106, v106, v107\n"
"v_cvt_pk_bf16_f32 v107, v108, v109\n"
"v_min_u32 v254, -2, v254\n"
"v_min_u32 v255, -2, v255\n"
"buffer_store_dwordx4 v[252:255], v192, s[28:31], s37 offen sc1\n"
"v_min_u32 v106, -2, v106\n"
"v_min_u32 v107, -2, v107\n"
"v_cvt_pk_bf16_f32 v216, v216, v217\n"
"v_cvt_pk_bf16_f32 v217, v218, v219\n"
"v_cvt_pk_bf16_f32 v218, v220, v221\n"
"v_cvt_pk_bf16_f32 v219, v222, v223\n"
"v_cvt_pk_bf16_f32 v224, v224, v225\n"
"buffer_store_dwordx2 v[106:107], v193, s[28:31], s37 offen offset:1024 sc1\n"
"s_waitcnt vmcnt(5)\n"
"ds_write_b128 v202, a[162:165] offset:36864\n"
"s_add_u32 s37, s37, 1572864\n"
"v_cvt_pk_bf16_f32 v225, v226, v227\n"
"v_max3_u32 v200, v228, v229, v230\n"
"v_max3_u32 v200, v200, v231, v152\n"
"v_max_u32 v200, v200, v153\n"
"buffer_store_dwordx4 v[72:75], v194, s[24:27], s36 offen sc1\n"
"s_waitcnt vmcnt(5)\n"
"ds_write_b64 v203, a[166:167] offset:36864\n"
"v_cmp_eq_u32 vcc, -1, v200\n"
"ds_read_b128 v[72:75], v192 offset:31744\n"
"ds_write_b128 v196, v[216:219] offset:43008\n"
"buffer_store_dwordx4 v[76:79], v195, s[24:27], s36 offen sc1\n"
"s_add_u32 s36, s36, s33\n"
"s_nop 0\n"
"s_cbranch_vccnz .Lp11%=\n"
".Lr11%=:\n"
"ds_write_b128 v202, v[228:231] offset:18432\n"
"ds_write_b64 v203, v[152:153] offset:18432\n"
"s_waitcnt lgkmcnt(3)\n"
"v_mfma_f32_16x16x32_bf16 v[232:235], a[228:231], v[72:75], v[232:235]\n"
"ds_read_b128 v[76:79], v192 offset:32768\n"
"ds_write_b64 v197, v[224:225] offset:43008\n"
"v_mfma_f32_16x16x32_bf16 v[236:239], a[232:235], v[72:75], v[236:239]\n"
"v_mfma_f32_16x16x32_bf16 v[240:243], a[236:239], v[72:75], v[240:243]\n"
"s_waitcnt lgkmcnt(1)\n"
"v_mfma_f32_16x16x32_bf16 v[232:235], a[240:243], v[76:79], v[232:235]\n"
"s_barrier\n"
"v_mfma_f32_16x16x32_bf16 v[236:239], a[244:247], v[76:79], v[236:239]\n"
"ds_read_b128 v[72:75], v192 offset:18432\n"
"ds_read_b128 v[40:43], v202\n"
"v_mfma_f32_16x16x32_bf16 v[240:243], a[248:251], v[76:79], v[240:243]\n"
"ds_read_b128 v[76:79], v192 offset:19456\n"
"v_mfma_f32_16x16x32_bf16 v[232:235], a[252:255], v[68:71], v[232:235]\n"
"ds_read_b128 v[44:47], v202 offset:4096\n"
"v_mfma_f32_16x16x32_bf16 v[236:239], v[0:3], v[68:71], v[236:239]\n"
"ds_read_b128 v[48:51], v202 offset:12288\n"
"v_mfma_f32_16x16x32_bf16 v[240:243], v[4:7], v[68:71], v[240:243]\n"
"ds_read_b128 v[68:71], v192 offset:20480\n"
"v_mfma_f32_16x16x32_bf16 v[232:235], v[8:11], v[32:35], v[232:235]\n"
"ds_read_b128 v[52:55], v202 offset:16384\n"
"v_mfma_f32_16x16x32_bf16 v[236:239], v[12:15], v[32:35], v[236:239]\n"
"v_mfma_f32_16x16x32_bf16 v[240:243], v[16:19], v[32:35], v[240:243]\n"
"ds_read_b128 v[32:35], v192 offset:21504\n"
"v_mfma_f32_16x16x32_bf16 v[232:235], v[20:23], v[36:39], v[232:235]\n"
"s_waitcnt lgkmcnt(7)\n"
"v_mfma_f32_16x16x32_bf16 v[236:239], v[24:27], v[36:39], v[236:239]\n"
"s_waitcnt lgkmcnt(6)\n"
"buffer_store_dwordx4 v[40:43], v194, s[24:27], s36 offen sc1\n"
"v_mfma_f32_16x16x32_bf16 v[240:243], v[28:31], v[36:39], v[240:243]\n"
"s_waitcnt lgkmcnt(4)\n"
"ds_read_b128 v[36:39], v192 offset:22528\n"
"ds_read_b128 v[40:43], v192 offset:23552\n"
"v_mfma_f32_16x16x32_bf16 v[204:207], a[216:219], v[72:75], 0\n"
"v_cvt_pk_bf16_f32 v232, v232, v233\n"
"v_cvt_pk_bf16_f32 v233, v234, v235\n"
"v_mfma_f32_16x16x32_bf16 v[208:211], a[220:223], v[72:75], 0\n"
"buffer_store_dwordx4 v[44:47], v195, s[24:27], s36 offen sc1\n"
"v_mfma_f32_16x16x32_bf16 v[212:215], a[224:227], v[72:75], 0\n"
"s_add_u32 s36, s36, s33\n"
"v_cvt_pk_bf16_f32 v234, v236, v237\n"
"v_cvt_pk_bf16_f32 v235, v238, v239\n"
"v_mfma_f32_16x16x32_bf16 v[204:207], a[228:231], v[76:79], v[204:207]\n"
"s_waitcnt lgkmcnt(3)\n"
"ds_write_b128 v196, v[232:235] offset:6144\n"
"v_mfma_f32_16x16x32_bf16 v[208:211], a[232:235], v[76:79], v[208:211]\n"
"buffer_store_dwordx4 v[48:51], v194, s[24:27], s36 offen sc1\n"
"v_mfma_f32_16x16x32_bf16 v[212:215], a[236:239], v[76:79], v[212:215]\n"
"v_cvt_pk_bf16_f32 v240, v240, v241\n"
"v_cvt_pk_bf16_f32 v241, v242, v243\n"
"v_mfma_f32_16x16x32_bf16 v[204:207], a[240:243], v[68:71], v[204:207]\n"
"s_waitcnt lgkmcnt(3)\n"
"ds_write_b64 v197, v[240:241] offset:6144\n"
"v_mfma_f32_16x16x32_bf16 v[208:211], a[244:247], v[68:71], v[208:211]\n"
"buffer_store_dwordx4 v[52:55], v195, s[24:27], s36 offen sc1\n"
"v_mfma_f32_16x16x32_bf16 v[212:215], a[248:251], v[68:71], v[212:215]\n"
"s_waitcnt vmcnt(9)\n"
"v_max3_u32 v200, v144, v145, v146\n"
"s_add_u32 s36, s36, s33\n"
"v_mfma_f32_16x16x32_bf16 v[204:207], a[252:255], v[32:35], v[204:207]\n"
"s_waitcnt vmcnt(8) lgkmcnt(3)\n"
"v_max3_u32 v200, v200, v147, v154\n"
"v_max_u32 v200, v200, v155\n"
"v_mfma_f32_16x16x32_bf16 v[208:211], v[0:3], v[32:35], v[208:211]\n"
"s_add_u32 s8, s8, s36\n"
"v_cmp_eq_u32 vcc, -1, v200\n"
"s_add_u32 s9, s9, s36\n"
"v_mfma_f32_16x16x32_bf16 v[212:215], v[4:7], v[32:35], v[212:215]\n"
"s_add_u32 s10, s10, s36\n"
"s_add_u32 s11, s11, s36\n"
"s_add_u32 s20, s20, s36\n"
"v_mfma_f32_16x16x32_bf16 v[204:207], v[8:11], v[36:39], v[204:207]\n"
"s_waitcnt lgkmcnt(0)\n"
"s_cbranch_vccnz .Lp12%=\n"
".Lr12%=:\n"
"v_mfma_f32_16x16x32_bf16 v[208:211], v[12:15], v[36:39], v[208:211]\n"
"s_barrier\n"
"v_mfma_f32_16x16x32_bf16 v[212:215], v[16:19], v[36:39], v[212:215]\n"
"s_add_u32 s21, s21, s36\n"
"ds_write_b128 v202, v[144:147] offset:49152\n"
"v_mfma_f32_16x16x32_bf16 v[204:207], v[20:23], v[40:43], v[204:207]\n"
"v_mfma_f32_16x16x32_bf16 v[208:211], v[24:27], v[40:43], v[208:211]\n"
"ds_write_b64 v203, v[154:155] offset:49152\n"
"v_mfma_f32_16x16x32_bf16 v[212:215], v[28:31], v[40:43], v[212:215]\n"
"s_nop 4\n"
"v_cvt_pk_bf16_f32 v204, v204, v205\n"
"ds_read_b128 v[76:79], v192 offset:36864\n"
"ds_read_b128 v[68:71], v192 offset:37888\n"
"ds_read_b128 v[32:35], v192 offset:38912\n"
"v_cvt_pk_bf16_f32 v205, v206, v207\n"
"v_cvt_pk_bf16_f32 v206, v208, v209\n"
"v_cvt_pk_bf16_f32 v207, v210, v211\n"
"ds_read_b128 v[36:39], v192 offset:39936\n"
"v_cvt_pk_bf16_f32 v212, v212, v213\n"
"ds_read_b128 v[40:43], v192 offset:40960\n"
"v_cvt_pk_bf16_f32 v213, v214, v215\n"
"ds_read_b128 v[44:47], v202 offset:43008\n"
"ds_write_b128 v196, v[204:207] offset:55296\n"
"buffer_load_dwordx4 v[56:59], v192, s[28:31], s17 offen sc1\n"
"ds_write_b64 v197, v[212:213] offset:55296\n"
"s_waitcnt lgkmcnt(7)\n"
"v_mfma_f32_16x16x32_bf16 v[216:219], a[216:219], v[76:79], 0\n"
"s_waitcnt lgkmcnt(6)\n"
"ds_read_b128 v[48:51], v202 offset:47104\n"
"v_mfma_f32_16x16x32_bf16 v[220:223], a[220:223], v[76:79], 0\n"
"ds_read_b128 v[52:55], v202 offset:6144\n"
"buffer_load_dwordx2 v[60:61], v193, s[28:31], s17 offen offset:1024 sc1\n"
"v_mfma_f32_16x16x32_bf16 v[224:227], a[224:227], v[76:79], 0\n"
"ds_read_b128 v[72:75], v202 offset:10240\n"
"v_mfma_f32_16x16x32_bf16 v[216:219], a[228:231], v[68:71], v[216:219]\n"
"s_waitcnt lgkmcnt(5)\n"
"v_mfma_f32_16x16x32_bf16 v[220:223], a[232:235], v[68:71], v[220:223]\n"
"v_mfma_f32_16x16x32_bf16 v[224:227], a[236:239], v[68:71], v[224:227]\n"
"buffer_store_dwordx4 v[44:47], v194, s[24:27], s8 offen sc1\n"
"v_mfma_f32_16x16x32_bf16 v[216:219], a[240:243], v[32:35], v[216:219]\n"
"s_waitcnt lgkmcnt(3)\n"
"ds_read_b128 v[44:47], v192 offset:41984\n"
"s_barrier\n"
"v_mfma_f32_16x16x32_bf16 v[220:223], a[244:247], v[32:35], v[220:223]\n"
"s_waitcnt lgkmcnt(2)\n"
"ds_read_b128 v[76:79], v192 offset:50176\n"
"ds_read_b128 v[68:71], v192 offset:51200\n"
"v_mfma_f32_16x16x32_bf16 v[224:227], a[248:251], v[32:35], v[224:227]\n"
"s_waitcnt lgkmcnt(3)\n"
"ds_read_b128 v[32:35], v192 offset:52224\n"
"buffer_store_dwordx4 v[48:51], v195, s[24:27], s8 offen sc1\n"
"v_mfma_f32_16x16x32_bf16 v[216:219], a[252:255], v[36:39], v[216:219]\n"
"s_nop 0\n"
"ds_read_b128 v[48:51], v202 offset:55296\n"
"v_mfma_f32_16x16x32_bf16 v[220:223], v[0:3], v[36:39], v[220:223]\n"
"v_mfma_f32_16x16x32_bf16 v[224:227], v[4:7], v[36:39], v[224:227]\n"
"ds_read_b128 v[36:39], v192 offset:53248\n"
"v_mfma_f32_16x16x32_bf16 v[216:219], v[8:11], v[40:43], v[216:219]\n"
"s_waitcnt lgkmcnt(5)\n"
"buffer_store_dwordx4 v[52:55], v194, s[24:27], s9 offen sc1\n"
"v_mfma_f32_16x16x32_bf16 v[220:223], v[12:15], v[40:43], v[220:223]\n"
"s_nop 0\n"
"ds_read_b128 v[52:55], v202 offset:59392\n"
"v_mfma_f32_16x16x32_bf16 v[224:227], v[16:19], v[40:43], v[224:227]\n"
"ds_read_b128 v[40:43], v192 offset:54272\n"
"v_mfma_f32_16x16x32_bf16 v[216:219], v[20:23], v[44:47], v[216:219]\n"
"v_mfma_f32_16x16x32_bf16 v[220:223], v[24:27], v[44:47], v[220:223]\n"
"s_waitcnt lgkmcnt(3)\n"
"buffer_store_dwordx4 v[72:75], v195, s[24:27], s9 offen sc1\n"
"v_mfma_f32_16x16x32_bf16 v[224:227], v[28:31], v[44:47], v[224:227]\n"
"s_nop 0\n"
"ds_read_b128 v[72:75], v192 offset:49152\n"
"s_nop 1\n"
"v_cvt_pk_bf16_f32 v216, v216, v217\n"
"v_cvt_pk_bf16_f32 v217, v218, v219\n"
"v_cvt_pk_bf16_f32 v218, v220, v221\n"
"v_cvt_pk_bf16_f32 v219, v222, v223\n"
"v_cvt_pk_bf16_f32 v224, v224, v225\n"
"v_cvt_pk_bf16_f32 v225, v226, v227\n"
"ds_write_b128 v196, v[216:219] offset:30720\n"
"buffer_store_dwordx4 v[48:51], v194, s[24:27], s10 offen sc1\n"
"ds_write_b64 v197, v[224:225] offset:30720\n"
"s_waitcnt lgkmcnt(4)\n"
"buffer_store_dwordx4 v[52:55], v195, s[24:27], s10 offen sc1\n"
"s_waitcnt lgkmcnt(2)\n"
"v_mfma_f32_16x16x32_bf16 v[232:235], a[216:219], v[72:75], 0\n"
"v_mfma_f32_16x16x32_bf16 v[236:239], a[220:223], v[72:75], 0\n"
"v_mfma_f32_16x16x32_bf16 v[240:243], a[224:227], v[72:75], 0\n"
"v_mfma_f32_16x16x32_bf16 v[232:235], a[228:231], v[76:79], v[232:235]\n"
"v_mfma_f32_16x16x32_bf16 v[236:239], a[232:235], v[76:79], v[236:239]\n"
"v_mfma_f32_16x16x32_bf16 v[240:243], a[236:239], v[76:79], v[240:243]\n"
"v_mfma_f32_16x16x32_bf16 v[232:235], a[240:243], v[68:71], v[232:235]\n"
"v_mfma_f32_16x16x32_bf16 v[240:243], a[248:251], v[68:71], v[240:243]\n"
"v_mfma_f32_16x16x32_bf16 v[236:239], a[244:247], v[68:71], v[236:239]\n"
"v_mfma_f32_16x16x32_bf16 v[232:235], a[252:255], v[32:35], v[232:235]\n"
"v_mfma_f32_16x16x32_bf16 v[240:243], v[4:7], v[32:35], v[240:243]\n"
"v_mfma_f32_16x16x32_bf16 v[236:239], v[0:3], v[32:35], v[236:239]\n"
"v_mfma_f32_16x16x32_bf16 v[232:235], v[8:11], v[36:39], v[232:235]\n"
"v_mfma_f32_16x16x32_bf16 v[240:243], v[16:19], v[36:39], v[240:243]\n"
"v_mfma_f32_16x16x32_bf16 v[236:239], v[12:15], v[36:39], v[236:239]\n"
"v_mfma_f32_16x16x32_bf16 v[232:235], v[20:23], v[40:43], v[232:235]\n"
"v_mfma_f32_16x16x32_bf16 v[240:243], v[28:31], v[40:43], v[240:243]\n"
"v_mfma_f32_16x16x32_bf16 v[236:239], v[24:27], v[40:43], v[236:239]\n"
"s_nop 5\n"
"v_cvt_pk_bf16_f32 v232, v232, v233\n"
"v_cvt_pk_bf16_f32 v233, v234, v235\n"
"v_cvt_pk_bf16_f32 v240, v240, v241\n"
"v_cvt_pk_bf16_f32 v234, v236, v237\n"
"v_cvt_pk_bf16_f32 v235, v238, v239\n"
"v_cvt_pk_bf16_f32 v241, v242, v243\n"
"ds_write_b128 v196, v[232:235] offset:24576\n"
"s_waitcnt vmcnt(7)\n"
"v_max3_u32 v200, v56, v57, v58\n"
"ds_write_b64 v197, v[240:241] offset:24576\n"
"s_waitcnt vmcnt(6)\n"
"v_max3_u32 v200, v200, v59, v60\n"
"v_max_u32 v200, v200, v61\n"
"v_cmp_eq_u32 vcc, -1, v200\n"
"s_nop 4\n"
"s_cbranch_vccnz .Lp13%=\n"
".Lr13%=:\n"
"ds_write_b128 v202, v[56:59]\n"
"ds_write_b64 v203, v[60:61]\n"
"s_waitcnt lgkmcnt(0)\n"
"s_barrier\n"
"ds_read_b128 v[76:79], v192\n"
"ds_read_b128 v[68:71], v192 offset:1024\n"
"ds_read_b128 v[32:35], v192 offset:2048\n"
"ds_read_b128 v[36:39], v192 offset:3072\n"
"ds_read_b128 v[40:43], v192 offset:4096\n"
"ds_read_b128 v[44:47], v202 offset:30720\n"
"ds_read_b128 v[48:51], v202 offset:34816\n"
"ds_read_b128 v[52:55], v202 offset:24576\n"
"ds_read_b128 v[72:75], v202 offset:28672\n"
"s_waitcnt lgkmcnt(8)\n"
"v_mfma_f32_16x16x32_bf16 v[204:207], a[216:219], v[76:79], 0\n"
"s_waitcnt lgkmcnt(7)\n"
"v_mfma_f32_16x16x32_bf16 v[212:215], a[224:227], v[76:79], 0\n"
"v_mfma_f32_16x16x32_bf16 v[208:211], a[220:223], v[76:79], 0\n"
"v_mfma_f32_16x16x32_bf16 v[204:207], a[228:231], v[68:71], v[204:207]\n"
"s_waitcnt lgkmcnt(6)\n"
"v_mfma_f32_16x16x32_bf16 v[212:215], a[236:239], v[68:71], v[212:215]\n"
"s_waitcnt lgkmcnt(3)\n"
"buffer_store_dwordx4 v[44:47], v194, s[24:27], s11 offen sc1\n"
"v_mfma_f32_16x16x32_bf16 v[208:211], a[232:235], v[68:71], v[208:211]\n"
"s_waitcnt lgkmcnt(1)\n"
"ds_read_b128 v[44:47], v192 offset:5120\n"
"v_mfma_f32_16x16x32_bf16 v[204:207], a[240:243], v[32:35], v[204:207]\n"
"s_waitcnt lgkmcnt(1)\n"
"v_mfma_f32_16x16x32_bf16 v[212:215], a[248:251], v[32:35], v[212:215]\n"
"buffer_store_dwordx4 v[48:51], v195, s[24:27], s11 offen sc1\n"
"v_mfma_f32_16x16x32_bf16 v[208:211], a[244:247], v[32:35], v[208:211]\n"
"v_mfma_f32_16x16x32_bf16 v[204:207], a[252:255], v[36:39], v[204:207]\n"
"v_mfma_f32_16x16x32_bf16 v[212:215], v[4:7], v[36:39], v[212:215]\n"
"v_mfma_f32_16x16x32_bf16 v[208:211], v[0:3], v[36:39], v[208:211]\n"
"buffer_store_dwordx4 v[52:55], v194, s[24:27], s20 offen sc1\n"
"v_mfma_f32_16x16x32_bf16 v[204:207], v[8:11], v[40:43], v[204:207]\n"
"s_waitcnt lgkmcnt(0)\n"
"v_mfma_f32_16x16x32_bf16 v[212:215], v[16:19], v[40:43], v[212:215]\n"
"v_mfma_f32_16x16x32_bf16 v[208:211], v[12:15], v[40:43], v[208:211]\n"
"buffer_store_dwordx4 v[72:75], v195, s[24:27], s20 offen sc1\n"
"v_mfma_f32_16x16x32_bf16 v[204:207], v[20:23], v[44:47], v[204:207]\n"
"v_mfma_f32_16x16x32_bf16 v[212:215], v[28:31], v[44:47], v[212:215]\n"
"v_mfma_f32_16x16x32_bf16 v[208:211], v[24:27], v[44:47], v[208:211]\n"
"s_nop 5\n"
"v_cvt_pk_bf16_f32 v204, v204, v205\n"
"v_cvt_pk_bf16_f32 v205, v206, v207\n"
"v_cvt_pk_bf16_f32 v212, v212, v213\n"
"v_cvt_pk_bf16_f32 v206, v208, v209\n"
"v_cvt_pk_bf16_f32 v207, v210, v211\n"
"v_cvt_pk_bf16_f32 v213, v214, v215\n"
"ds_write_b128 v196, v[204:207] offset:12288\n"
"ds_write_b64 v197, v[212:213] offset:12288\n"
"s_waitcnt lgkmcnt(0)\n"
"s_barrier\n"
"ds_read_b128 v[48:51], v202 offset:12288\n"
"ds_read_b128 v[52:55], v202 offset:16384\n"
"s_waitcnt lgkmcnt(1)\n"
"buffer_store_dwordx4 v[48:51], v194, s[24:27], s21 offen sc1\n"
"s_waitcnt lgkmcnt(0)\n"
"buffer_store_dwordx4 v[52:55], v195, s[24:27], s21 offen sc1\n"
"s_branch .Lend%=\n"
".Lp1%=:\n"
"s_sub_u32 s42, s42, 1\n"
"s_cbranch_scc1 .Lt1%=\n"
"s_sleep 1\n"
"buffer_load_dword v200, off, s[28:31], s39 sc1\n"
"s_waitcnt vmcnt(0)\n"
"v_add3_u32 v200, v200, s50, -2\n"
"v_cmp_lt_u32 vcc, 64, v200\n"
"s_nop 4\n"
"s_cbranch_vccnz .Lp1%=\n"
"s_branch .Lr1%=\n"
".Lt1%=:\n"
"s_mov_b32 s42, 0\n"
"s_branch .Lr1%=\n"
".Lp3%=:\n"
"s_sub_u32 s42, s42, 1\n"
"s_cbranch_scc1 .Lt3%=\n"
"s_sleep 1\n"
"buffer_load_dword v200, off, s[28:31], s39 sc1\n"
"s_waitcnt vmcnt(0)\n"
"v_add3_u32 v200, v200, s50, -4\n"
"v_cmp_lt_u32 vcc, 64, v200\n"
"s_nop 4\n"
"s_cbranch_vccnz .Lp3%=\n"
"s_branch .Lr3%=\n"
".Lt3%=:\n"
"s_mov_b32 s42, 0\n"
"s_branch .Lr3%=\n"
".Lp4%=:\n"
"s_sub_u32 s42, s42, 1\n"
"s_cbranch_scc1 .Lt4%=\n"
"s_sleep 1\n"
"buffer_load_dword v200, off, s[28:31], s39 sc1\n"
"s_waitcnt vmcnt(0)\n"
"v_add3_u32 v200, v200, s50, -5\n"
"v_cmp_lt_u32 vcc, 64, v200\n"
"s_nop 4\n"
"s_cbranch_vccnz .Lp4%=\n"
"s_branch .Lr4%=\n"
".Lt4%=:\n"
"s_mov_b32 s42, 0\n"
"s_branch .Lr4%=\n"
".Lp6%=:\n"
"s_sub_u32 s42, s42, 1\n"
"s_cbranch_scc1 .Lt6%=\n"
"s_sleep 1\n"
"buffer_load_dword v200, off, s[28:31], s39 sc1\n"
"s_waitcnt vmcnt(0)\n"
"v_add3_u32 v200, v200, s50, -7\n"
"v_cmp_lt_u32 vcc, 64, v200\n"
"s_nop 4\n"
"s_cbranch_vccnz .Lp6%=\n"
"s_branch .Lr6%=\n"
".Lt6%=:\n"
"s_mov_b32 s42, 0\n"
"s_branch .Lr6%=\n"
".Lp7%=:\n"
"s_sub_u32 s42, s42, 1\n"
"s_cbranch_scc1 .Lt7%=\n"
"s_sleep 1\n"
"buffer_load_dword v200, off, s[28:31], s39 sc1\n"
"s_waitcnt vmcnt(0)\n"
"v_add3_u32 v200, v200, s50, -8\n"
"v_cmp_lt_u32 vcc, 64, v200\n"
"s_nop 4\n"
"s_cbranch_vccnz .Lp7%=\n"
"s_branch .Lr7%=\n"
".Lt7%=:\n"
"s_mov_b32 s42, 0\n"
"s_branch .Lr7%=\n"
".Lp9%=:\n"
"s_sub_u32 s42, s42, 1\n"
"s_cbranch_scc1 .Lt9%=\n"
"s_sleep 1\n"
"buffer_load_dword v200, off, s[28:31], s39 sc1\n"
"s_waitcnt vmcnt(0)\n"
"v_add3_u32 v200, v200, s50, -10\n"
"v_cmp_lt_u32 vcc, 64, v200\n"
"s_nop 4\n"
"s_cbranch_vccnz .Lp9%=\n"
"s_branch .Lr9%=\n"
".Lt9%=:\n"
"s_mov_b32 s42, 0\n"
"s_branch .Lr9%=\n"
".Lp10%=:\n"
"s_sub_u32 s42, s42, 1\n"
"s_cbranch_scc1 .Lt10%=\n"
"s_sleep 1\n"
"buffer_load_dword v201, off, s[28:31], s39 sc1\n"
"s_waitcnt vmcnt(0)\n"
"v_add3_u32 v201, v201, s50, -11\n"
"v_cmp_lt_u32 vcc, 64, v201\n"
"s_nop 4\n"
"s_cbranch_vccnz .Lp10%=\n"
"s_branch .Lr10%=\n"
".Lt10%=:\n"
"s_mov_b32 s42, 0\n"
"s_branch .Lr10%=\n"
".Lp11%=:\n"
"s_sub_u32 s42, s42, 1\n"
"s_cbranch_scc1 .Lt11%=\n"
"buffer_load_dwordx4 v[228:231], v192, s[28:31], s14 offen sc1\n"
"buffer_load_dwordx2 v[152:153], v193, s[28:31], s14 offen offset:1024 sc1\n"
"s_waitcnt vmcnt(0)\n"
"v_max3_u32 v200, v228, v229, v230\n"
"v_max3_u32 v200, v200, v231, v152\n"
"v_max_u32 v200, v200, v153\n"
"v_cmp_eq_u32 vcc, -1, v200\n"
"s_nop 4\n"
"s_cbranch_vccnz .Lp11%=\n"
"s_branch .Lr11%=\n"
".Lt11%=:\n"
"s_mov_b32 s42, 0\n"
"s_branch .Lr11%=\n"
".Lp12%=:\n"
"s_sub_u32 s42, s42, 1\n"
"s_cbranch_scc1 .Lt12%=\n"
"buffer_load_dwordx4 v[144:147], v192, s[28:31], s16 offen sc1\n"
"buffer_load_dwordx2 v[154:155], v193, s[28:31], s16 offen offset:1024 sc1\n"
"s_waitcnt vmcnt(0)\n"
"v_max3_u32 v200, v144, v145, v146\n"
"v_max3_u32 v200, v200, v147, v154\n"
"v_max_u32 v200, v200, v155\n"
"v_cmp_eq_u32 vcc, -1, v200\n"
"s_nop 4\n"
"s_cbranch_vccnz .Lp12%=\n"
"s_branch .Lr12%=\n"
".Lt12%=:\n"
"s_mov_b32 s42, 0\n"
"s_branch .Lr12%=\n"
".Lp13%=:\n"
"s_sub_u32 s42, s42, 1\n"
"s_cbranch_scc1 .Lt13%=\n"
"buffer_load_dwordx4 v[56:59], v192, s[28:31], s17 offen sc1\n"
"buffer_load_dwordx2 v[60:61], v193, s[28:31], s17 offen offset:1024 sc1\n"
"s_waitcnt vmcnt(0)\n"
"v_max3_u32 v200, v56, v57, v58\n"
"v_max3_u32 v200, v200, v59, v60\n"
"v_max_u32 v200, v200, v61\n"
"v_cmp_eq_u32 vcc, -1, v200\n"
"s_nop 4\n"
"s_cbranch_vccnz .Lp13%=\n"
"s_branch .Lr13%=\n"
".Lt13%=:\n"
"s_mov_b32 s42, 0\n"
"s_branch .Lr13%=\n"
".Lend%=:\n"
      : "+{v31}"(tid), "+{s2}"(bid), "+{s[0:1]}"(ka)
      : "i"((__attribute__((address_space(3))) char*)smem)
      : "memory", "scc", "vcc", VCLOB, ACLOB, SCLOB);
}

extern "C" void kernel_launch(void* const* d_in, const int* in_sizes, int n_in,
                              void* d_out, int out_size, void* d_ws, size_t ws_size,
                              hipStream_t stream) {
  const uint16_t* x = (const uint16_t*)d_in[0];
  const uint16_t* wqkv = (const uint16_t*)d_in[1];
  const uint16_t* wo = (const uint16_t*)d_in[2];
  const uint16_t* rpb = (const uint16_t*)d_in[3];
  dinat_hs<<<256, 256, 0, stream>>>(x, wqkv, wo, rpb, (uint16_t*)d_out, (char*)d_ws + (2 << 20)
  );
}
```

```cpp
#include <hip/hip_runtime.h>
#include <hip/hip_bf16.h>
#include <stdint.h>

#define VCLOB "v0", "v1", "v2", "v3", "v4", "v5", "v6", "v7", "v8", "v9", "v10", "v11", "v12", "v13", "v14", "v15", "v16", "v17", "v18", "v19", "v20", "v21", "v22", "v23", "v24", "v25", "v26", "v27", "v28", "v29", "v30", "v32", "v33", "v34", "v35", "v36", "v37", "v38", "v39", "v40", "v41", "v42", "v43", "v44", "v45", "v46", "v47", "v48", "v49", "v50", "v51", "v52", "v53", "v54", "v55", "v56", "v57", "v58", "v59", "v60", "v61", "v62", "v63", "v64", "v65", "v66", "v67", "v68", "v69", "v70", "v71", "v72", "v73", "v74", "v75", "v76", "v77", "v78", "v79", "v80", "v81", "v82", "v83", "v84", "v85", "v86", "v87", "v88", "v89", "v90", "v91", "v92", "v93", "v94", "v95", "v96", "v97", "v98", "v99", "v100", "v101", "v102", "v103", "v104", "v105", "v106", "v107", "v108", "v109", "v110", "v111", "v112", "v113", "v114", "v115", "v116", "v117", "v118", "v119", "v120", "v121", "v122", "v123", "v124", "v125", "v126", "v127", "v128", "v129", "v130", "v131", "v132", "v133", "v134", "v135", "v136", "v137", "v138", "v139", "v140", "v141", "v142", "v143", "v144", "v145", "v146", "v147", "v148", "v149", "v150", "v151", "v152", "v153", "v154", "v155", "v156", "v157", "v158", "v159", "v160", "v161", "v162", "v163", "v164", "v165", "v166", "v167", "v168", "v169", "v170", "v171", "v172", "v173", "v174", "v175", "v176", "v177", "v178", "v179", "v180", "v181", "v182", "v183", "v184", "v185", "v186", "v187", "v188", "v189", "v190", "v191", "v192", "v193", "v194", "v195", "v196", "v197", "v198", "v199", "v200", "v201", "v202", "v203", "v204", "v205", "v206", "v207", "v208", "v209", "v210", "v211", "v212", "v213", "v214", "v215", "v216", "v217", "v218", "v219", "v220", "v221", "v222", "v223", "v224", "v225", "v226", "v227", "v228", "v229", "v230", "v231", "v232", "v233", "v234", "v235", "v236", "v237", "v238", "v239", "v240", "v241", "v242", "v243", "v244", "v245", "v246", "v247", "v248", "v249", "v250", "v251", "v252", "v253", "v254", "v255"
#define ACLOB "a0", "a1", "a2", "a3", "a4", "a5", "a6", "a7", "a8", "a9", "a10", "a11", "a12", "a13", "a14", "a15", "a16", "a17", "a18", "a19", "a20", "a21", "a22", "a23", "a24", "a25", "a26", "a27", "a28", "a29", "a30", "a31", "a32", "a33", "a34", "a35", "a36", "a37", "a38", "a39", "a40", "a41", "a42", "a43", "a44", "a45", "a46", "a47", "a48", "a49", "a50", "a51", "a52", "a53", "a54", "a55", "a56", "a57", "a58", "a59", "a60", "a61", "a62", "a63", "a64", "a65", "a66", "a67", "a68", "a69", "a70", "a71", "a72", "a73", "a74", "a75", "a76", "a77", "a78", "a79", "a80", "a81", "a82", "a83", "a84", "a85", "a86", "a87", "a88", "a89", "a90", "a91", "a92", "a93", "a94", "a95", "a96", "a97", "a98", "a99", "a100", "a101", "a102", "a103", "a104", "a105", "a106", "a107", "a108", "a109", "a110", "a111", "a112", "a113", "a114", "a115", "a116", "a117", "a118", "a119", "a120", "a121", "a122", "a123", "a124", "a125", "a126", "a127", "a128", "a129", "a130", "a131", "a132", "a133", "a134", "a135", "a136", "a137", "a138", "a139", "a140", "a141", "a142", "a143", "a144", "a145", "a146", "a147", "a148", "a149", "a150", "a151", "a152", "a153", "a154", "a155", "a156", "a157", "a158", "a159", "a160", "a161", "a162", "a163", "a164", "a165", "a166", "a167", "a168", "a169", "a170", "a171", "a172", "a173", "a174", "a175", "a176", "a177", "a178", "a179", "a180", "a181", "a182", "a183", "a184", "a185", "a186", "a187", "a188", "a189", "a190", "a191", "a192", "a193", "a194", "a195", "a196", "a197", "a198", "a199", "a200", "a201", "a202", "a203", "a204", "a205", "a206", "a207", "a208", "a209", "a210", "a211", "a212", "a213", "a214", "a215", "a216", "a217", "a218", "a219", "a220", "a221", "a222", "a223", "a224", "a225", "a226", "a227", "a228", "a229", "a230", "a231", "a232", "a233", "a234", "a235", "a236", "a237", "a238", "a239", "a240", "a241", "a242", "a243", "a244", "a245", "a246", "a247", "a248", "a249", "a250", "a251", "a252", "a253", "a254", "a255"
#define SCLOB "s3", "s4", "s5", "s6", "s7", "s8", "s9", "s10", "s11", "s12", "s13", "s14", "s15", "s16", "s17", "s18", "s19", "s20", "s21", "s22", "s23", "s24", "s25", "s26", "s27", "s28", "s29", "s30", "s31", "s32", "s33", "s34", "s35", "s36", "s37", "s38", "s39", "s40", "s41", "s42", "s43", "s44", "s45", "s46", "s47", "s48", "s49", "s50", "s51", "s52", "s53", "s54", "s55", "s56", "s57", "s58", "s59", "s60", "s61", "s62", "s63", "s64", "s65", "s66", "s67", "s68", "s69", "s70", "s71", "s72", "s73", "s74", "s75", "s76", "s77", "s78", "s79", "s80", "s81", "s82", "s83", "s84", "s85", "s86", "s87", "s88", "s89", "s90", "s91", "s92", "s93", "s94", "s95", "s96", "s97", "s98", "s99", "s100", "s101"
#define LDS_BYTES 163392

__global__ __launch_bounds__(256, 1) __attribute__((aligned(4096))) void dinat_hs(const uint16_t* __restrict__ x, const uint16_t* __restrict__ wqkv,
                                                   const uint16_t* __restrict__ wo, const uint16_t* __restrict__ rpb,
                                                   uint16_t* __restrict__ out, char* __restrict__ ws
                                                   ) {
  __shared__ __attribute__((aligned(16))) char smem[LDS_BYTES];
  unsigned tid = threadIdx.x, bid = blockIdx.x;
  auto ka = __builtin_amdgcn_kernarg_segment_ptr();
  asm volatile(
"s_load_dwordx2 s[8:9], s[0:1], 0\n"
"s_load_dwordx2 s[12:13], s[0:1], 8\n"
"s_load_dwordx2 s[16:17], s[0:1], 16\n"
"s_load_dwordx2 s[20:21], s[0:1], 24\n"
"s_load_dwordx2 s[24:25], s[0:1], 32\n"
"s_load_dwordx2 s[28:29], s[0:1], 40\n"
"s_getpc_b64 s[4:5]\n"
".Lpc%=:\n"
"s_bfe_u32 s53, s2, 0x20005\n"
"s_and_b32 s54, s2, 1\n"
"s_lshr_b32 s55, s2, 7\n"
"s_mul_i32 s59, s53, 27\n"
"v_lshrrev_b32 v113, 6, v31\n"
"s_max_u32 s59, s59, 1\n"
"s_mul_i32 s58, s55, 4816896\n"
"s_mul_i32 s71, s54, 14278656\n"
"s_nop 0\n"
"v_readfirstlane_b32 s51, v113\n"
"s_sub_u32 s59, s59, 1\n"
"s_add_u32 s58, s58, s71\n"
"v_and_b32 v112, 63, v31\n"
"s_mul_i32 s72, s59, 768\n"
"s_lshr_b32 s71, s2, 3\n"
"s_add_u32 s58, s58, s72\n"
"s_and_b32 s73, s2, 4\n"
"s_lshl2_add_u32 s71, s71, s51\n"
"v_min_u32 v121, 2, v112\n"
"s_xnor_b32 s56, s54, s55\n"
"s_mul_i32 s73, s73, 21504\n"
"s_and_b32 s74, s2, 2\n"
"s_mul_i32 s72, s71, 384\n"
"v_lshlrev_b32 v121, 7, v121\n"
"s_and_b32 s56, s56, 1\n"
"s_add_u32 s58, s58, s73\n"
"s_mul_i32 s74, s74, 192\n"
"v_add_u32 v121, s72, v121\n"
"s_bfe_u32 s52, s2, 0x20003\n"
"s_mul_i32 s57, s54, -344064\n"
"s_add_u32 s58, s58, s74\n"
"v_lshlrev_b32 v120, 4, v112\n"
"s_mul_i32 s85, s56, 3\n"
"v_min_u32 v121, .Lend%=-.Lpc%=-4, v121\n"
"s_add_u32 s57, s57, 172032\n"
"s_lshl_b32 s61, s51, 3\n"
"v_lshrrev_b32 v114, 3, v112\n"
"v_xor_b32 v115, v120, v112\n"
"s_sub_u32 s86, s85, 3\n"
"s_mov_b32 m0, 163136\n"
"s_mul_i32 s73, s52, 338\n"
"global_load_lds_dword v121, s[4:5]\n"
"s_and_b32 s74, s51, 1\n"
"s_lshr_b32 s84, s2, 5\n"
"v_and_b32 v115, 0x70, v115\n"
"v_add_u32 v114, s61, v114\n"
"s_mul_i32 s86, s86, s57\n"
"s_and_b32 s73, s73, -4\n"
"s_mul_i32 s74, s74, 88\n"
"v_mul_u32_u24 v123, 0x2aab, v112\n"
"v_add_u32 v124, 64, v112\n"
"s_mov_b32 s22, 1352\n"
"s_mov_b32 s23, 0x20000\n"
"s_add_u32 s84, s84, s52\n"
"v_xor_b32 v116, 64, v115\n"
"v_mul_u32_u24 v117, 0x300, v114\n"
"s_add_u32 s86, s86, s58\n"
"v_max_u32 v118, s85, v114\n"
"s_add_u32 s73, s73, s74\n"
"v_lshlrev_b32 v122, 2, v112\n"
"v_lshrrev_b32 v123, 16, v123\n"
"v_mul_u32_u24 v124, 0x2aab, v124\n"
"s_and_b32 s65, s51, 1\n"
"s_mul_i32 s84, s84, 2\n"
"v_add3_u32 v119, v117, s86, v116\n"
"v_lshlrev_b32 v126, 1, v123\n"
"v_lshrrev_b32 v124, 16, v124\n"
"s_lshr_b32 s66, s51, 1\n"
"s_mul_i32 s70, s52, 96\n"
"s_mul_i32 s85, s65, 384\n"
"s_lshr_b32 s75, 0x9249200, s84\n"
"v_and_b32 v126, 24, v126\n"
"v_lshlrev_b32 v127, 1, v124\n"
"s_add_u32 s85, s85, s70\n"
"s_mul_i32 s86, s66, 36864\n"
"s_movk_i32 s69, 1152\n"
"s_and_b32 s75, s75, 0x180\n"
"v_and_or_b32 v126, v123, 3, v126\n"
"v_and_b32 v127, 24, v127\n"
"v_add_u32 v125, 128, v112\n"
"s_add_u32 s85, s85, s86\n"
"v_mad_u32_u24 v126, v126, s69, v120\n"
"v_mul_u32_u24 v27, 96, v123\n"
"v_and_or_b32 v127, v124, 3, v127\n"
"v_lshrrev_b32 v128, 2, v124\n"
"v_mul_u32_u24 v125, 0x2aab, v125\n"
"s_add_u32 s85, s85, 384\n"
"s_mul_i32 s65, s65, 3072\n"
"s_mul_i32 s71, s66, 6752\n"
"s_mov_b32 s14, 221184\n"
"s_mov_b32 s15, 0x20000\n"
"v_sub_u32 v27, v126, v27\n"
"v_and_or_b32 v127, v128, 4, v127\n"
"v_lshrrev_b32 v125, 16, v125\n"
"s_add_u32 s71, s71, s65\n"
"v_mad_u32_u24 v127, v127, s69, v120\n"
"v_mul_u32_u24 v28, 96, v124\n"
"v_lshlrev_b32 v129, 1, v125\n"
"v_sub_u32 v28, v127, v28\n"
"v_and_b32 v129, 24, v129\n"
"v_add_u32 v28, 1024, v28\n"
"v_and_or_b32 v129, v125, 3, v129\n"
"v_mad_u32_u24 v129, v129, s69, v120\n"
"v_mul_u32_u24 v29, 96, v125\n"
"s_sub_u32 s64, 111, s59\n"
"v_sub_u32 v29, v129, v29\n"
"s_min_u32 s64, s64, 32\n"
"v_add_u32 v29, 6656, v29\n"
"s_mul_i32 s64, s64, 768\n"
"v_add_u32 v118, -3, v118\n"
"s_add_u32 s64, s64, s58\n"
"s_mul_i32 s50, s51, 6752\n"
"v_mul_i32_i24 v118, s57, v118\n"
"s_mov_b32 s10, 19267584\n"
"s_mov_b32 s11, 0x20000\n"
"s_add_u32 s50, s50, 27008\n"
"v_add3_u32 v118, v118, s64, v115\n"
"s_lshl_b32 s72, s56, 3\n"
"s_sub_u32 s72, s72, 2\n"
"s_mul_i32 s72, s72, s57\n"
"s_mul_i32 s34, s51, 12288\n"
"s_add_u32 s72, s72, s58\n"
"s_add_u32 s34, s34, 81024\n"
"v_add3_u32 v130, v117, s72, v115\n"
"s_lshr_b32 s76, 0x12492480, s84\n"
"s_waitcnt lgkmcnt(0)\n"
"s_add_u32 m0, s74, 162768\n"
"s_lshr_b32 s77, 0x4924900, s84\n"
"buffer_load_dword v122, s[20:23], s73 offen lds\n"
"s_mul_i32 s73, s75, 576\n"
"s_add_u32 s73, s73, s85\n"
"s_add_u32 m0, s71, 0\n"
"s_add_u32 s74, s72, s57\n"
"buffer_load_dwordx4 v27, s[12:15], s73 offen lds\n"
"s_and_b32 s60, s2, 7\n"
"s_lshr_b32 s84, s2, 5\n"
"v_add3_u32 v131, v117, s74, v116\n"
"s_and_b32 s76, s76, 0x180\n"
"s_lshl3_add_u32 s60, s60, s84\n"
"s_mul_i32 s84, s76, 576\n"
"s_add_u32 m0, s71, 1024\n"
"s_add_u32 s84, s84, s85\n"
"s_and_b32 s77, s77, 0x180\n"
"s_mul_i32 s86, s77, 576\n"
"s_add_u32 s86, s86, s85\n"
"s_mul_i32 s72, s54, 172032\n"
"buffer_load_dwordx4 v28, s[12:15], s73 offen lds\n"
"s_sub_u32 s36, s58, s72\n"
"s_add_u32 s32, s36, s75\n"
"v_add3_u32 v190, v117, s72, v115\n"
"s_sub_u32 s74, 172032, s72\n"
"v_add3_u32 v191, v117, s74, v116\n"
"s_add_u32 s0, s36, s76\n"
"s_add_u32 s1, s36, s77\n"
"s_mul_i32 s35, s51, 6144\n"
"s_lshl_b32 s33, s57, 1\n"
"s_add_u32 m0, s71, 2048\n"
"s_add_u32 s35, s35, 130176\n"
"v_and_b32 v134, 3, v112\n"
"v_lshrrev_b32 v137, 1, v112\n"
"v_and_or_b32 v137, v137, 4, v134\n"
"v_bfe_u32 v136, v112, 2, 1\n"
"buffer_load_dwordx4 v29, s[12:15], s73 offen lds\n"
"v_lshrrev_b32 v140, 1, v137\n"
"v_lshrrev_b32 v132, 4, v112\n"
"v_lshl_add_u32 v85, v136, 2, v140\n"
"v_xor_b32 v85, v85, v132\n"
"v_lshlrev_b32 v85, 4, v85\n"
"v_lshl_add_u32 v85, v137, 7, v85\n"
"v_mul_u32_u24 v25, 0xc00, v136\n"
"s_add_u32 m0, s50, 0\n"
"v_add_u32 v25, v25, v85\n"
"v_add_u32 v188, s34, v25\n"
"v_xad_u32 v189, v25, 64, s34\n"
"v_xor_b32 v71, 64, v25\n"
"v_add_u32 v70, s35, v25\n"
"buffer_load_dwordx4 v118, s[8:11], s75 offen lds\n"
"v_add_u32 v71, s35, v71\n"
"s_mul_i32 s3, s65, 12\n"
"s_add_u32 s70, s70, s3\n"
"s_mul_i32 s81, s77, 576\n"
"s_add_u32 s81, s81, s70\n"
"s_sub_u32 s3, s76, s75\n"
"s_mul_i32 s3, s3, s66\n"
"s_add_u32 m0, s50, 3072\n"
"s_add_u32 s3, s3, s75\n"
"s_mul_i32 s3, s3, 576\n"
"s_add_u32 s3, s3, s70\n"
"v_lshrrev_b32 v84, 2, v112\n"
"v_mul_u32_u24 v84, 96, v84\n"
"buffer_load_dwordx4 v119, s[8:11], s75 offen lds\n"
"v_lshl_add_u32 v23, v134, 3, v84\n"
"s_mov_b32 s42, 0x80000\n"
"v_add_u32 v72, 54016, v23\n"
"v_min_u32 v96, 0xdc, v31\n"
"v_mul_u32_u24 v97, 0xf1, v96\n"
"s_mul_i32 s85, s54, -52\n"
"v_lshrrev_b32 v97, 12, v97\n"
"s_add_u32 m0, s34, 6144\n"
"s_sub_u32 s85, s85, 8\n"
"v_lshlrev_b32 v30, 3, v96\n"
"v_mul_i32_i24 v97, s85, v97\n"
"v_lshl_add_u32 v96, v96, 1, v97\n"
"v_xor_b32 v69, 64, v25\n"
"buffer_load_dwordx4 v130, s[8:11], s75 offen lds\n"
"v_add_u32 v30, 160896, v30\n"
"v_add_u32 v68, s50, v25\n"
"v_add_u32 v69, s50, v69\n"
"v_add_u32 v142, s61, v137\n"
"v_bfe_u32 v86, v142, 1, 1\n"
"v_mul_u32_u24 v87, 43, v142\n"
"s_movk_i32 s68, 6752\n"
"s_add_u32 m0, s34, 9216\n"
"v_and_b32 v133, 48, v112\n"
"v_xor_b32 v86, v86, v132\n"
"v_lshrrev_b32 v87, 9, v87\n"
"s_movk_i32 s67, 192\n"
"v_lshlrev_b32 v86, 4, v86\n"
"buffer_load_dwordx4 v131, s[8:11], s75 offen lds\n"
"v_mul_u32_u24 v87, 80832, v87\n"
"v_mad_u32_u24 v21, v142, s68, v133\n"
"v_mad_u32_u24 v141, v142, s67, v86\n"
"v_sub_u32 v21, v21, v87\n"
"v_and_b32 v88, 1, v112\n"
"s_mul_i32 s48, s56, 13504\n"
"v_add_u32 v21, 6144, v21\n"
"s_add_u32 m0, s71, 13504\n"
"v_add_u32 v187, 74272, v141\n"
"v_add_u32 v198, 60768, v141\n"
"v_mad_u32_u24 v88, v88, s68, v133\n"
"s_mov_b32 s6, 0xf0f0f0f0\n"
"s_mov_b32 s7, 0xf0f0f0f0\n"
"buffer_load_dwordx4 v27, s[12:15], s84 offen lds\n"
"s_mov_b64 exec, s[6:7]\n"
"v_add_u32 v21, s48, v141\n"
"v_mov_b32 v187, v141\n"
"v_add_u32 v198, 60544, v88\n"
"s_mov_b64 exec, -1\n"
"s_lshl_b32 s85, s66, 10\n"
"v_add_u32 v192, 99456, v120\n"
"v_bfe_u32 v138, v112, 4, 1\n"
"v_lshrrev_b32 v139, 3, v112\n"
"s_add_u32 m0, s71, 14528\n"
"v_and_b32 v139, 4, v139\n"
"v_sub_u32 v94, v136, v138\n"
"s_add_u32 s62, s59, s61\n"
"s_mul_i32 s49, s56, 272\n"
"v_mul_i32_i24 v94, 0x88, v94\n"
"buffer_load_dwordx4 v28, s[12:15], s84 offen lds\n"
"v_sub_u32 v95, v137, v139\n"
"s_sub_u32 s63, 108, s62\n"
"v_lshl_add_u32 v94, v95, 3, v94\n"
"s_sub_u32 s62, 3, s62\n"
"v_add_u32 v150, 1, v139\n"
"v_add_u32 v151, 2, v139\n"
"v_add_u32 v152, 3, v139\n"
"s_add_u32 m0, s71, 15552\n"
"v_max_i32 v149, s62, v139\n"
"v_max_i32 v150, s62, v150\n"
"v_max_i32 v151, s62, v151\n"
"v_max_i32 v152, s62, v152\n"
"v_mov_b32 v153, 0x7f\n"
"buffer_load_dwordx4 v29, s[12:15], s84 offen lds\n"
"s_add_u32 s84, s49, 161064\n"
"v_add_u32 v20, s84, v94\n"
"s_lshl_b32 s84, s51, 10\n"
"v_min_i32 v149, s63, v149\n"
"v_min_i32 v150, s63, v150\n"
"v_min_i32 v151, s63, v151\n"
"v_min_i32 v152, s63, v152\n"
"s_add_u32 m0, s50, 1024\n"
"v_add_u32 v202, s84, v192\n"
"v_lshlrev_b32 v149, v149, v153\n"
"v_lshlrev_b32 v150, v150, v153\n"
"v_lshlrev_b32 v151, v151, v153\n"
"v_lshlrev_b32 v152, v152, v153\n"
"buffer_load_dwordx4 v118, s[8:11], s76 offen lds\n"
"s_sub_u32 s74, 3, s61\n"
"s_mov_b32 s84, 0xc0000007\n"
"s_cmp_eq_u32 s53, 0\n"
"s_cselect_b32 s84, 0xe0000000, s84\n"
"s_cmp_eq_u32 s53, 3\n"
"s_cselect_b32 s84, 7, s84\n"
"v_lshrrev_b32 v149, v137, v149\n"
"s_add_u32 m0, s50, 4096\n"
"v_lshrrev_b32 v150, v137, v150\n"
"v_lshrrev_b32 v151, v137, v151\n"
"v_lshrrev_b32 v152, v137, v152\n"
"s_max_i32 s74, s74, s62\n"
"s_mov_b32 s72, 0xffff\n"
"buffer_load_dwordx4 v119, s[8:11], s76 offen lds\n"
"v_bfe_i32 v98, v149, 0, 1\n"
"v_bfe_i32 v99, v150, 0, 1\n"
"v_bfe_i32 v100, v151, 0, 1\n"
"v_bfe_i32 v101, v152, 0, 1\n"
"v_bfe_i32 v102, v149, 8, 1\n"
"v_bfe_i32 v103, v150, 8, 1\n"
"v_bfe_i32 v56, v151, 8, 1\n"
"v_bfe_i32 v57, v152, 8, 1\n"
"v_max_i32 v58, s74, v137\n"
"v_bfi_b32 v0, s72, v98, v99\n"
"v_bfi_b32 v1, s72, v100, v101\n"
"v_bfi_b32 v2, s72, v102, v103\n"
"v_bfi_b32 v3, s72, v56, v57\n"
"s_mul_i32 s74, s51, 1536\n"
"v_and_b32 v250, 15, v112\n"
"s_lshl2_add_u32 s72, s60, s51\n"
"v_mul_u32_u24 v250, 0x180, v250\n"
"v_and_b32 v251, 7, v112\n"
"s_mul_i32 s37, s72, 6144\n"
"v_add_u32 v250, 99456, v250\n"
"v_lshrrev_b32 v253, 5, v112\n"
"s_lshl_b32 s72, s51, 6\n"
"v_lshlrev_b32 v254, 3, v134\n"
"v_lshl_add_u32 v255, v23, 2, s72\n"
"v_sub_u32 v255, v255, v254\n"
"v_sub_u32 v255, v255, v254\n"
"v_add_u32 v236, 130176, v255\n"
"v_sub_u32 v84, v236, v254\n"
"s_lshl_b32 s73, s48, 2\n"
"v_mad_u32_u24 v186, v136, s68, v141\n"
"s_add_u32 s73, s73, 6752\n"
"v_add_u32 v22, s73, v186\n"
"s_sub_u32 s73, 28, s61\n"
"s_min_i32 s73, s73, s63\n"
"v_min_i32 v58, s73, v58\n"
"v_lshlrev_b32 v58, v58, -1\n"
"v_lshrrev_b32 v58, v139, v58\n"
"v_not_b32 v58, v58\n"
"v_and_b32 v59, 5, v58\n"
"v_sub_u32 v61, v136, v138\n"
"v_and_b32 v60, 10, v58\n"
"v_lshlrev_b32 v59, 1, v59\n"
"v_ashrrev_i32 v149, 31, v61\n"
"v_add_u32 v150, -1, v61\n"
"v_mov_b32 v160, 0x40de\n"
"v_mov_b32 v161, 0\n"
"v_lshlrev_b32 v63, 4, v134\n"
"v_mov_b64 v[156:157], 0\n"
"s_mov_b32 s6, 0x00f0000f\n"
"s_mov_b32 s7, 0xf0000f00\n"
"v_lshl_or_b32 v59, v60, 4, v59\n"
"v_not_b32 v149, v149\n"
"v_ashrrev_i32 v150, 31, v150\n"
"v_or_b32 v62, s56, v138\n"
"s_mov_b64 exec, s[6:7]\n"
"v_lshlrev_b64 v[156:157], v63, v[160:161]\n"
"s_mov_b64 exec, -1\n"
"v_or_b32 v59, 0x44, v59\n"
"v_and_b32 v12, v149, v0\n"
"v_and_b32 v16, v150, v0\n"
"v_and_b32 v13, v149, v1\n"
"v_and_b32 v17, v150, v1\n"
"v_and_b32 v14, v149, v2\n"
"v_and_b32 v18, v150, v2\n"
"v_and_b32 v15, v149, v3\n"
"v_and_b32 v19, v150, v3\n"
"v_sub_u32 v62, v136, v62\n"
"v_mov_b32 v160, 0xffff\n"
"s_mov_b64 exec, 0\n"
"s_cmp_eq_u32 s51, 0\n"
"s_cbranch_scc1 .Lic0%=\n"
"s_cmp_eq_u32 s51, 1\n"
"s_cbranch_scc1 .Lic1%=\n"
"s_cmp_eq_u32 s51, 2\n"
"s_cbranch_scc1 .Lic2%=\n"
"s_branch .Lic3%=\n"
".Lib0%=:\n"
"s_mov_b64 exec, -1\n"
"v_mov_b64 v[176:177], 0\n"
"v_lshl_or_b32 v66, v59, 8, v59\n"
"v_ashrrev_i32 v149, 31, v62\n"
"v_add_u32 v150, -1, v62\n"
"s_mov_b64 exec, s[6:7]\n"
"v_lshlrev_b64 v[176:177], v63, v[160:161]\n"
"s_mov_b64 exec, -1\n"
"s_add_u32 m0, s34, 7168\n"
"s_mov_b32 s30, 23724032\n"
"buffer_load_dwordx4 v130, s[8:11], s76 offen lds\n"
"s_mov_b32 s31, 0x20000\n"
"v_and_b32 v146, 8, v112\n"
"v_lshl_add_u32 v146, v136, 1, v146\n"
"v_add3_u32 v146, v146, s61, 1\n"
"v_lshrrev_b32 v145, 5, v112\n"
"v_max_u32 v144, 3, v142\n"
"v_mul_u32_u24 v145, s68, v145\n"
"v_bfe_u32 v92, v146, 1, 1\n"
"v_add_u32 v144, -3, v144\n"
"v_add_u32 v145, -768, v145\n"
"v_xor_b32 v92, v92, v134\n"
"v_mov_b32 v143, s48\n"
"v_bfe_u32 v90, v144, 1, 1\n"
"s_add_u32 m0, s34, 10240\n"
"v_lshl_add_u32 v92, v92, 4, v145\n"
"buffer_load_dwordx4 v131, s[8:11], s76 offen lds\n"
"v_mad_u32_u24 v143, v136, s68, v143\n"
"v_xor_b32 v90, v90, v132\n"
"v_cmp_eq_u32 s[46:47], 35, v146\n"
"v_mad_u32_u24 v184, v146, s67, v92\n"
"v_lshl_add_u32 v90, v90, 4, v143\n"
"v_add_u32 v146, 1, v146\n"
"v_mad_u32_u24 v178, v144, s67, v90\n"
"v_bfe_u32 v93, v146, 1, 1\n"
"v_add_u32 v144, 5, v142\n"
"v_xor_b32 v93, v93, v134\n"
"v_min_u32 v144, 32, v144\n"
"s_add_u32 m0, s71, 60768\n"
"v_mul_u32_u24 v147, 13504, v138\n"
"buffer_load_dwordx4 v27, s[12:15], s86 offen lds\n"
"s_waitcnt vmcnt(12)\n"
"s_barrier\n"
"ds_read_b64_tr_b16 a[0:1], v23\n"
"ds_read_b64_tr_b16 a[2:3], v23 offset:1536\n"
"ds_read_b64_tr_b16 a[4:5], v23 offset:32\n"
"ds_read_b64_tr_b16 a[6:7], v23 offset:1568\n"
"ds_read_b64_tr_b16 a[8:9], v23 offset:64\n"
"ds_read_b64_tr_b16 a[10:11], v23 offset:1600\n"
"ds_read_b64_tr_b16 a[72:73], v23 offset:3072\n"
"s_add_u32 m0, s71, 61792\n"
"ds_read_b64_tr_b16 a[74:75], v23 offset:4608\n"
"buffer_load_dwordx4 v28, s[12:15], s86 offen lds\n"
"ds_read_b64_tr_b16 a[76:77], v23 offset:3104\n"
"v_lshl_add_u32 v93, v93, 4, v145\n"
"ds_read_b64_tr_b16 a[78:79], v23 offset:4640\n"
"ds_read_b64_tr_b16 a[80:81], v23 offset:3136\n"
"ds_read_b64_tr_b16 a[82:83], v23 offset:4672\n"
"v_bfe_u32 v91, v144, 1, 1\n"
"ds_read_b64_tr_b16 a[12:13], v23 offset:6752\n"
"ds_read_b64_tr_b16 a[14:15], v23 offset:8288\n"
"ds_read_b64_tr_b16 a[16:17], v23 offset:6784\n"
"s_add_u32 m0, s71, 62816\n"
"ds_read_b64_tr_b16 a[18:19], v23 offset:8320\n"
"buffer_load_dwordx4 v29, s[12:15], s86 offen lds\n"
"s_waitcnt vmcnt(12)\n"
"ds_read_b128 a[144:147], v68\n"
"s_mul_i32 s71, s54, 312\n"
"s_and_b32 s86, s52, 1\n"
"ds_read_b128 a[148:151], v69\n"
"s_lshl1_add_u32 s71, s86, s71\n"
"s_add_u32 s71, s71, 162760\n"
"v_add_u32 v24, s71, v96\n"
"ds_read_b64_tr_b16 a[20:21], v23 offset:6816\n"
"ds_read_b64_tr_b16 a[22:23], v23 offset:8352\n"
"ds_read_b64_tr_b16 a[84:85], v23 offset:9824\n"
"s_add_u32 m0, s50, 2048\n"
"ds_read_b64_tr_b16 a[86:87], v23 offset:11360\n"
"buffer_load_dwordx4 v118, s[8:11], s77 offen lds\n"
"ds_read_b64_tr_b16 a[88:89], v23 offset:9856\n"
"s_and_b32 s86, s51, 1\n"
"ds_read_b64_tr_b16 a[90:91], v23 offset:11392\n"
"s_lshl3_add_u32 s85, s86, s85\n"
"ds_read_b64_tr_b16 a[92:93], v23 offset:9888\n"
"s_add_u32 s85, s85, 4096\n"
"ds_read_b64_tr_b16 a[94:95], v23 offset:11424\n"
"v_add_u32 v203, s85, v192\n"
"ds_read_u16 v240, v24 offset:6\n"
"ds_read_u16 v241, v24 offset:4\n"
"ds_read_u16 v242, v24 offset:2\n"
"v_add_u32 v147, s48, v147\n"
"v_mul_i32_i24 v148, -67520, v138\n"
"v_mad_u32_u24 v185, v146, s67, v93\n"
"v_bfe_u32 v135, v112, 2, 2\n"
"v_xor_b32 v91, v91, v132\n"
"v_add_u32 v148, 67520, v148\n"
"v_add_u32 v183, v185, v147\n"
"v_add_u32 v185, v185, v148\n"
"v_lshl_add_u32 v91, v91, 4, v143\n"
"s_mov_b64 exec, s[46:47]\n"
"v_mad_u32_u24 v185, v138, s67, v185\n"
"s_mov_b64 exec, -1\n"
"v_mad_u32_u24 v179, v144, s67, v91\n"
"v_cmp_eq_u32 s[44:45], 32, v144\n"
"v_add_u32 v182, v184, v147\n"
"v_not_b32 v149, v149\n"
"v_add_u32 v184, v184, v148\n"
"v_and_b32 v4, v149, v0\n"
"v_and_b32 v5, v149, v1\n"
"v_and_b32 v6, v149, v2\n"
"v_and_b32 v7, v149, v3\n"
"v_ashrrev_i32 v150, 31, v150\n"
"v_and_b32 v8, v150, v0\n"
"v_and_b32 v9, v150, v1\n"
"v_and_b32 v10, v150, v2\n"
"v_and_b32 v11, v150, v3\n"
"v_lshrrev_b32 v86, 2, v112\n"
"v_sub_u32 v87, 11, v112\n"
"v_and_b32 v87, 0x80000000, v87\n"
"v_and_b32 v89, 1, v114\n"
"v_add_u32 v89, 29, v89\n"
"v_mul_i32_i24 v89, s57, v89\n"
"v_add3_u32 v199, v89, s64, v116\n"
"v_mov_b32 v26, 0xf149f149\n"
"v_lshlrev_b32 v193, 3, v112\n"
"v_mov_b64 v[160:161], v[156:157]\n"
"v_add_u32 v180, 40512, v178\n"
"v_add_u32 v181, 40512, v179\n"
"v_mov_b32 v172, 0x3f803f80\n"
"v_mov_b32 v173, v172\n"
"v_mov_b64 v[164:165], v[156:157]\n"
"v_mov_b64 v[168:169], v[156:157]\n"
"v_mov_b64 v[174:175], v[172:173]\n"
"v_add_u32 v193, 99456, v193\n"
"s_waitcnt vmcnt(11) lgkmcnt(1)\n"
"v_mfma_f32_16x16x32_bf16 v[204:207], a[0:3], a[144:147], 0\n"
"s_add_u32 m0, s50, 5120\n"
"ds_read_b128 a[168:171], v188 offset:6144\n"
"buffer_load_dwordx4 v119, s[8:11], s77 offen lds\n"
"v_mfma_f32_16x16x32_bf16 v[208:211], a[4:7], a[144:147], 0\n"
"s_lshl_b32 s85, s51, 6\n"
"ds_read_b128 a[172:175], v189 offset:6144\n"
"ds_read_u16 v243, v24\n"
"v_mfma_f32_16x16x32_bf16 v[212:215], a[8:11], a[144:147], 0\n"
"s_add_u32 m0, s34, 8192\n"
"v_lshl_or_b32 v240, v241, 16, v240\n"
"v_add_u32 v81, s85, v112\n"
"v_mfma_f32_16x16x32_bf16 v[216:219], a[72:75], a[144:147], 0\n"
"s_lshl_b32 s86, s52, 3\n"
"v_add_u32 v154, s85, v112\n"
"v_add_u32 v81, 0x100, v81\n"
"v_mfma_f32_16x16x32_bf16 v[220:223], a[76:79], a[144:147], 0\n"
"s_lshl2_add_u32 s71, s60, s52\n"
"buffer_load_dwordx4 v130, s[8:11], s77 offen lds\n"
"v_mul_u32_u24 v155, 0xaab, v154\n"
"v_mfma_f32_16x16x32_bf16 v[224:227], a[80:83], a[144:147], 0\n"
"s_mul_i32 s38, s71, 6144\n"
"v_mul_u32_u24 v82, 0xaab, v81\n"
"v_lshrrev_b32 v155, 16, v155\n"
"v_mfma_f32_16x16x32_bf16 v[204:207], a[12:15], a[148:151], v[204:207]\n"
"s_add_u32 m0, s34, 11264\n"
"v_lshrrev_b32 v82, 16, v82\n"
"v_mul_i32_i24 v80, -24, v155\n"
"v_mfma_f32_16x16x32_bf16 v[208:211], a[16:19], a[148:151], v[208:211]\n"
"s_lshl_b32 s71, s51, 2\n"
"v_mul_i32_i24 v83, -24, v82\n"
"v_add_u32 v154, v154, v80\n"
"v_mfma_f32_16x16x32_bf16 v[212:215], a[20:23], a[148:151], v[212:215]\n"
"s_waitcnt lgkmcnt(2)\n"
"buffer_load_dwordx4 v131, s[8:11], s77 offen lds\n"
"v_add_u32 v81, v81, v83\n"
"v_mfma_f32_16x16x32_bf16 v[216:219], a[84:87], a[148:151], v[216:219]\n"
"s_waitcnt lgkmcnt(0)\n"
"v_and_b32 v80, 7, v155\n"
"v_and_b32 v83, 7, v82\n"
"v_mfma_f32_16x16x32_bf16 v[220:223], a[88:91], a[148:151], v[220:223]\n"
"s_waitcnt vmcnt(9)\n"
"v_lshl_or_b32 v241, v243, 16, v242\n"
"v_xor_b32 v154, v154, v80\n"
"v_mfma_f32_16x16x32_bf16 v[224:227], a[92:95], a[148:151], v[224:227]\n"
"s_add_u32 m0, s34, 0\n"
"ds_read_b128 a[152:155], v68 offset:1024\n"
"buffer_load_dwordx4 v190, s[8:11], s32 offen lds\n"
"v_mfma_f32_16x16x32_bf16 v[104:107], a[72:75], a[168:171], 0\n"
"s_add_u32 s38, s38, s74\n"
"ds_read_b128 a[156:159], v69 offset:1024\n"
"ds_write_b64 v30, v[240:241]\n"
"v_mfma_f32_16x16x32_bf16 v[108:111], a[76:79], a[168:171], 0\n"
"s_lshl1_add_u32 s74, s51, 16\n"
"v_xor_b32 v81, v81, v83\n"
"v_lshrrev_b32 v80, 1, v155\n"
"v_mfma_f32_16x16x32_bf16 v[228:231], a[80:83], a[168:171], 0\n"
"s_add_u32 m0, s34, 3072\n"
"v_lshrrev_b32 v83, 1, v82\n"
"v_and_b32 v80, 4, v80\n"
"v_mfma_f32_16x16x32_bf16 v[104:107], a[84:87], a[172:175], v[104:107]\n"
"s_add_u32 s38, s38, 31616\n"
"buffer_load_dwordx4 v191, s[8:11], s32 offen lds\n"
"v_and_b32 v83, 4, v83\n"
"v_mfma_f32_16x16x32_bf16 v[108:111], a[88:91], a[172:175], v[108:111]\n"
"s_add_u32 s32, s32, s33\n"
"v_and_or_b32 v80, v155, 3, v80\n"
"v_and_or_b32 v83, v82, 3, v83\n"
"v_mfma_f32_16x16x32_bf16 v[228:231], a[92:95], a[172:175], v[228:231]\n"
"s_waitcnt vmcnt(9)\n"
"v_add_u32 v80, s86, v80\n"
"s_add_u32 m0, s34, 1024\n"
"v_add_u32 v83, s86, v83\n"
"ds_read_b128 a[176:179], v188 offset:7168\n"
"ds_read_b128 a[180:183], v189 offset:7168\n"
"buffer_load_dwordx4 v190, s[8:11], s0 offen lds\n"
"v_lshrrev_b32 v244, v80, s84\n"
"v_lshrrev_b32 v245, 4, v155\n"
"v_bfe_u32 v246, v155, 2, 1\n"
"v_lshrrev_b32 v247, v83, s84\n"
"v_lshrrev_b32 v248, 4, v82\n"
"v_bfe_u32 v249, v82, 2, 1\n"
"v_or_b32 v244, v244, v245\n"
"v_xor_b32 v246, s54, v246\n"
"v_or_b32 v247, v247, v248\n"
"s_add_u32 m0, s34, 4096\n"
"v_xor_b32 v249, s54, v249\n"
"v_lshlrev_b32 v244, 31, v244\n"
"v_mul_u32_u24 v246, 172032, v246\n"
"v_mul_u32_u24 v80, 0x300, v80\n"
"v_lshlrev_b32 v247, 31, v247\n"
"buffer_load_dwordx4 v191, s[8:11], s0 offen lds\n"
"s_add_u32 s0, s0, s33\n"
"v_mul_u32_u24 v249, 172032, v249\n"
"v_mul_u32_u24 v83, 0x300, v83\n"
"v_add_u32 v252, s71, v132\n"
"v_add3_u32 v244, v244, v246, v80\n"
"v_add3_u32 v247, v247, v249, v83\n"
"v_xor_b32 v252, v252, v251\n"
"s_add_u32 m0, s34, 2048\n"
"v_lshl_add_u32 v194, v154, 4, v244\n"
"v_lshl_add_u32 v195, v81, 4, v247\n"
"v_mfma_f32_16x16x32_bf16 v[244:247], a[0:3], a[168:171], 0\n"
"s_add_u32 s39, s38, 22119552\n"
"v_lshl_add_u32 v196, v252, 4, v250\n"
"v_add_u32 v253, s74, v253\n"
"v_mfma_f32_16x16x32_bf16 v[244:247], a[12:15], a[172:175], v[244:247]\n"
"s_and_b32 s85, s52, 1\n"
"buffer_load_dwordx4 v190, s[8:11], s1 offen lds\n"
"v_lshl_add_u32 v250, v138, 3, v250\n"
"v_xor_b32 v253, v253, v251\n"
"s_mov_b64 exec, 0\n"
"s_cmp_eq_u32 s51, 0\n"
"s_cbranch_scc1 .Lic4%=\n"
"s_cmp_eq_u32 s51, 1\n"
"s_cbranch_scc1 .Lic5%=\n"
"s_cmp_eq_u32 s51, 2\n"
"s_cbranch_scc1 .Lic6%=\n"
"s_branch .Lic7%=\n"
".Lib1%=:\n"
"s_mov_b64 exec, -1\n"
"s_add_u32 m0, s34, 5120\n"
"s_barrier\n"
"buffer_load_dwordx4 v191, s[8:11], s1 offen lds\n"
"s_add_u32 s1, s1, s33\n"
"ds_read_b64_tr_b16 a[24:25], v23 offset:13504\n"
"ds_read_b64_tr_b16 a[26:27], v23 offset:15040\n"
"ds_read_b64_tr_b16 a[28:29], v23 offset:13536\n"
"ds_read_b64_tr_b16 a[30:31], v23 offset:15072\n"
"s_add_u32 m0, s35, 0\n"
"ds_read_b64_tr_b16 a[32:33], v23 offset:13568\n"
"ds_read_b64_tr_b16 a[34:35], v23 offset:15104\n"
"ds_read_b64_tr_b16 a[96:97], v23 offset:16576\n"
"ds_read_b64_tr_b16 a[98:99], v23 offset:18112\n"
"s_waitcnt vmcnt(10)\n"
"buffer_load_dwordx4 v190, s[8:11], s32 offen lds\n"
"ds_read_b64_tr_b16 a[100:101], v23 offset:16608\n"
"ds_read_b64_tr_b16 a[102:103], v23 offset:18144\n"
"s_barrier\n"
"ds_read_b64_tr_b16 a[104:105], v23 offset:16640\n"
"ds_read_b64_tr_b16 a[106:107], v23 offset:18176\n"
"ds_read_b64_tr_b16 a[36:37], v23 offset:20256\n"
"v_lshl_add_u32 v197, v253, 4, v250\n"
"v_mfma_f32_16x16x32_bf16 v[248:251], a[4:7], a[168:171], 0\n"
"s_add_u32 m0, s35, 3072\n"
"ds_read_b64_tr_b16 a[38:39], v23 offset:21792\n"
"ds_read_b64_tr_b16 a[40:41], v23 offset:20288\n"
"v_mfma_f32_16x16x32_bf16 v[252:255], a[8:11], a[168:171], 0\n"
"s_waitcnt vmcnt(9)\n"
"buffer_load_dwordx4 v191, s[8:11], s32 offen lds\n"
"ds_read_b64_tr_b16 a[42:43], v23 offset:21824\n"
"v_mfma_f32_16x16x32_bf16 v[248:251], a[16:19], a[172:175], v[248:251]\n"
"s_waitcnt lgkmcnt(14)\n"
"ds_read_b128 a[160:163], v68 offset:2048\n"
"ds_read_b128 a[164:167], v69 offset:2048\n"
"v_mfma_f32_16x16x32_bf16 v[204:207], a[24:27], a[152:155], v[204:207]\n"
"s_waitcnt lgkmcnt(14)\n"
"ds_read_b64_tr_b16 a[44:45], v23 offset:20320\n"
"v_add_u32 v68, 130176, v23\n"
"v_mfma_f32_16x16x32_bf16 v[208:211], a[28:31], a[152:155], v[208:211]\n"
"s_waitcnt lgkmcnt(13)\n"
"ds_read_b64_tr_b16 a[46:47], v23 offset:21856\n"
"ds_read_b64_tr_b16 a[108:109], v23 offset:23328\n"
"v_mfma_f32_16x16x32_bf16 v[212:215], a[32:35], a[152:155], v[212:215]\n"
"s_waitcnt lgkmcnt(13)\n"
"ds_read_b64_tr_b16 a[110:111], v23 offset:24864\n"
"ds_read_b64_tr_b16 a[112:113], v23 offset:23360\n"
"v_mfma_f32_16x16x32_bf16 v[216:219], a[96:99], a[152:155], v[216:219]\n"
"s_waitcnt lgkmcnt(13)\n"
"ds_read_b64_tr_b16 a[114:115], v23 offset:24896\n"
"ds_read_b64_tr_b16 a[116:117], v23 offset:23392\n"
"v_mfma_f32_16x16x32_bf16 v[220:223], a[100:103], a[152:155], v[220:223]\n"
"s_waitcnt lgkmcnt(13)\n"
"ds_read_b64_tr_b16 a[118:119], v23 offset:24928\n"
"ds_read_b64_tr_b16 a[48:49], v23 offset:60768\n"
"v_mfma_f32_16x16x32_bf16 v[224:227], a[104:107], a[152:155], v[224:227]\n"
"s_waitcnt lgkmcnt(13)\n"
"ds_read_b64_tr_b16 a[50:51], v23 offset:62304\n"
"ds_read_b64_tr_b16 a[52:53], v23 offset:60800\n"
"v_mfma_f32_16x16x32_bf16 v[204:207], a[36:39], a[156:159], v[204:207]\n"
"s_waitcnt vmcnt(8) lgkmcnt(13)\n"
"ds_read_b64_tr_b16 a[54:55], v23 offset:62336\n"
"ds_read_b64_tr_b16 a[56:57], v23 offset:60832\n"
"v_mfma_f32_16x16x32_bf16 v[208:211], a[40:43], a[156:159], v[208:211]\n"
"s_add_u32 m0, s35, 1024\n"
"ds_read_b128 a[184:187], v188 offset:8192\n"
"buffer_load_dwordx4 v190, s[8:11], s0 offen lds\n"
"v_mfma_f32_16x16x32_bf16 v[252:255], a[20:23], a[172:175], v[252:255]\n"
"s_add_u32 s32, s32, s33\n"
"ds_read_b128 a[188:191], v189 offset:8192\n"
"s_mul_i32 s20, s33, 4\n"
"v_mfma_f32_16x16x32_bf16 v[244:247], a[24:27], a[176:179], v[244:247]\n"
"s_waitcnt lgkmcnt(13)\n"
"ds_read_b64_tr_b16 a[58:59], v23 offset:62368\n"
"ds_read_b64_tr_b16 a[120:121], v72 offset:9824\n"
"v_mfma_f32_16x16x32_bf16 v[212:215], a[44:47], a[156:159], v[212:215]\n"
"s_waitcnt lgkmcnt(13)\n"
"ds_read_b64_tr_b16 a[122:123], v72 offset:11360\n"
"ds_read_b64_tr_b16 a[124:125], v72 offset:9856\n"
"v_mfma_f32_16x16x32_bf16 v[216:219], a[108:111], a[156:159], v[216:219]\n"
"s_waitcnt lgkmcnt(13)\n"
"ds_read_b64_tr_b16 a[126:127], v72 offset:11392\n"
"ds_read_b64_tr_b16 a[128:129], v72 offset:9888\n"
"v_mfma_f32_16x16x32_bf16 v[220:223], a[112:115], a[156:159], v[220:223]\n"
"s_waitcnt lgkmcnt(13)\n"
"ds_read_b64_tr_b16 a[130:131], v72 offset:11424\n"
"ds_read_b64_tr_b16 a[60:61], v72 offset:13504\n"
"v_mfma_f32_16x16x32_bf16 v[224:227], a[116:119], a[156:159], v[224:227]\n"
"s_waitcnt lgkmcnt(13)\n"
"ds_read_b64_tr_b16 a[62:63], v72 offset:15040\n"
"ds_read_b64_tr_b16 a[64:65], v72 offset:13536\n"
"v_mfma_f32_16x16x32_bf16 v[204:207], a[48:51], a[160:163], v[204:207]\n"
"s_waitcnt lgkmcnt(13)\n"
"ds_read_b64_tr_b16 a[66:67], v72 offset:15072\n"
"ds_read_b64_tr_b16 a[68:69], v72 offset:13568\n"
"v_mfma_f32_16x16x32_bf16 v[208:211], a[52:55], a[160:163], v[208:211]\n"
"s_add_u32 m0, s35, 4096\n"
"ds_read_b64_tr_b16 a[70:71], v72 offset:15104\n"
"buffer_load_dwordx4 v191, s[8:11], s0 offen lds\n"
"v_mfma_f32_16x16x32_bf16 v[248:251], a[28:31], a[176:179], v[248:251]\n"
"s_add_u32 s0, s0, s33\n"
"ds_read_b64_tr_b16 a[132:133], v72 offset:16576\n"
"ds_read_b64_tr_b16 a[134:135], v72 offset:18112\n"
"v_mfma_f32_16x16x32_bf16 v[252:255], a[32:35], a[176:179], v[252:255]\n"
"s_waitcnt lgkmcnt(14)\n"
"ds_read_b64_tr_b16 a[136:137], v72 offset:16608\n"
"ds_read_b64_tr_b16 a[138:139], v72 offset:18144\n"
"v_mfma_f32_16x16x32_bf16 v[212:215], a[56:59], a[160:163], v[212:215]\n"
"s_waitcnt lgkmcnt(14)\n"
"ds_read_b64_tr_b16 a[140:141], v72 offset:16640\n"
"ds_read_b64_tr_b16 a[142:143], v72 offset:18176\n"
"v_mfma_f32_16x16x32_bf16 v[216:219], a[120:123], a[160:163], v[216:219]\n"
"s_waitcnt lgkmcnt(14)\n"
"s_add_u32 m0, s35, 2048\n"
"s_mul_i32 s21, s33, 5\n"
"v_mfma_f32_16x16x32_bf16 v[220:223], a[124:127], a[160:163], v[220:223]\n"
"s_waitcnt lgkmcnt(12)\n"
"buffer_load_dwordx4 v190, s[8:11], s1 offen lds\n"
"v_mfma_f32_16x16x32_bf16 v[224:227], a[128:131], a[160:163], v[224:227]\n"
"s_waitcnt lgkmcnt(10)\n"
"s_lshl2_add_u32 s85, s85, s61\n"
"v_add_u32 v85, s85, v134\n"
"v_mfma_f32_16x16x32_bf16 v[204:207], a[60:63], a[164:167], v[204:207]\n"
"s_waitcnt lgkmcnt(8)\n"
"v_mul_u32_u24 v85, 0x300, v85\n"
"v_lshl_add_u32 v85, v86, 7, v85\n"
"v_mfma_f32_16x16x32_bf16 v[208:211], a[64:67], a[164:167], v[208:211]\n"
"s_waitcnt lgkmcnt(6)\n"
"v_or_b32 v67, v87, v85\n"
"v_mfma_f32_16x16x32_bf16 v[212:215], a[68:71], a[164:167], v[212:215]\n"
"s_waitcnt lgkmcnt(4)\n"
"v_cvt_pk_bf16_f32 v204, v204, v205\n"
"v_cvt_pk_bf16_f32 v205, v206, v207\n"
"v_mfma_f32_16x16x32_bf16 v[216:219], a[132:135], a[164:167], v[216:219]\n"
"s_waitcnt lgkmcnt(2)\n"
"v_cvt_pk_bf16_f32 v206, v208, v209\n"
"v_cvt_pk_bf16_f32 v207, v210, v211\n"
"v_mfma_f32_16x16x32_bf16 v[220:223], a[136:139], a[164:167], v[220:223]\n"
"s_waitcnt lgkmcnt(0)\n"
"v_cvt_pk_bf16_f32 v212, v212, v213\n"
"v_cvt_pk_bf16_f32 v213, v214, v215\n"
"v_mfma_f32_16x16x32_bf16 v[224:227], a[140:143], a[164:167], v[224:227]\n"
"s_add_u32 m0, s35, 5120\n"
"v_cvt_pk_bf16_f32 v216, v216, v217\n"
"v_cvt_pk_bf16_f32 v217, v218, v219\n"
"v_mfma_f32_16x16x32_bf16 v[104:107], a[96:99], a[176:179], v[104:107]\n"
"s_barrier\n"
"v_mfma_f32_16x16x32_bf16 v[108:111], a[100:103], a[176:179], v[108:111]\n"
"s_lshr_b32 s86, s52, 1\n"
"buffer_load_dwordx4 v191, s[8:11], s1 offen lds\n"
"ds_write_b128 v21, v[204:207]\n"
"v_mfma_f32_16x16x32_bf16 v[228:231], a[104:107], a[176:179], v[228:231]\n"
"s_add_u32 s1, s1, s33\n"
"v_cvt_pk_bf16_f32 v218, v220, v221\n"
"v_cvt_pk_bf16_f32 v219, v222, v223\n"
"v_mfma_f32_16x16x32_bf16 v[244:247], a[36:39], a[180:183], v[244:247]\n"
"s_mul_i32 s86, s86, 172032\n"
"v_cvt_pk_bf16_f32 v214, v224, v225\n"
"v_cvt_pk_bf16_f32 v215, v226, v227\n"
"v_mfma_f32_16x16x32_bf16 v[248:251], a[40:43], a[180:183], v[248:251]\n"
"s_add_u32 s86, s86, s36\n"
"ds_write_b128 v21, v[212:215] offset:64\n"
"v_mfma_f32_16x16x32_bf16 v[252:255], a[44:47], a[180:183], v[252:255]\n"
"s_mul_i32 s78, s33, 8\n"
"s_add_u32 s78, s78, s86\n"
"v_mfma_f32_16x16x32_bf16 v[104:107], a[108:111], a[180:183], v[104:107]\n"
"s_add_u32 m0, s34, 6144\n"
"s_mul_i32 s73, s52, 1536\n"
"buffer_load_dwordx4 v190, s[8:11], s32 offen lds\n"
"v_mfma_f32_16x16x32_bf16 v[108:111], a[112:115], a[180:183], v[108:111]\n"
"s_add_u32 s37, s37, s73\n"
"ds_write_b128 v21, v[216:219] offset:128\n"
"v_mfma_f32_16x16x32_bf16 v[228:231], a[116:119], a[180:183], v[228:231]\n"
"s_lshl_b32 s73, s51, 5\n"
"s_sub_u32 s73, 256, s73\n"
"v_add_u32 v235, s73, v84\n"
"v_mfma_f32_16x16x32_bf16 v[244:247], a[48:51], a[184:187], v[244:247]\n"
"s_waitcnt vmcnt(11)\n"
"s_add_u32 m0, s34, 9216\n"
"ds_read_b128 v[32:35], v188\n"
"v_mfma_f32_16x16x32_bf16 v[248:251], a[52:55], a[184:187], v[248:251]\n"
"s_mov_b32 s18, 73728\n"
"buffer_load_dwordx4 v191, s[8:11], s32 offen lds\n"
"ds_read_b128 v[36:39], v189\n"
"v_mfma_f32_16x16x32_bf16 v[252:255], a[56:59], a[184:187], v[252:255]\n"
"s_add_u32 s32, s32, s33\n"
"ds_read_b64 v[80:81], v20 offset:272\n"
"ds_read_b64 v[86:87], v20 offset:336\n"
"v_mfma_f32_16x16x32_bf16 v[104:107], a[120:123], a[184:187], v[104:107]\n"
"s_add_u32 s37, s37, 31616\n"
"ds_read_b64 v[88:89], v20 offset:544\n"
"ds_read_b64 v[94:95], v20 offset:608\n"
"v_mfma_f32_16x16x32_bf16 v[108:111], a[124:127], a[184:187], v[108:111]\n"
"s_add_u32 m0, s34, 7168\n"
"ds_read_b64 v[96:97], v20 offset:816\n"
"buffer_load_dwordx4 v190, s[8:11], s0 offen lds\n"
"v_mfma_f32_16x16x32_bf16 v[228:231], a[128:131], a[184:187], v[228:231]\n"
"s_mul_i32 s79, s51, 3072\n"
"ds_read_b64 v[102:103], v20 offset:880\n"
"s_add_u32 s43, s79, 0\n"
"v_mfma_f32_16x16x32_bf16 v[244:247], a[60:63], a[188:191], v[244:247]\n"
"s_mul_i32 s80, s66, 12288\n"
"s_mov_b32 s26, 19267584\n"
"v_mfma_f32_16x16x32_bf16 v[248:251], a[64:67], a[188:191], v[248:251]\n"
"s_mov_b32 s27, 0x20000\n"
"s_mov_b32 s19, 0x20000\n"
"v_mfma_f32_16x16x32_bf16 v[252:255], a[68:71], a[188:191], v[252:255]\n"
"s_waitcnt vmcnt(11) lgkmcnt(7)\n"
"ds_read_b128 v[40:43], v188 offset:1024\n"
"ds_read_b128 v[44:47], v189 offset:1024\n"
"v_mfma_f32_16x16x32_bf16 v[104:107], a[132:135], a[188:191], v[104:107]\n"
"s_waitcnt lgkmcnt(7)\n"
"v_cvt_pk_bf16_f32 v244, v244, v245\n"
"v_cvt_pk_bf16_f32 v245, v246, v247\n"
"v_mfma_f32_16x16x32_bf16 v[108:111], a[136:139], a[188:191], v[108:111]\n"
"s_waitcnt lgkmcnt(5)\n"
"v_cvt_pk_bf16_f32 v246, v248, v249\n"
"v_cvt_pk_bf16_f32 v247, v250, v251\n"
"v_mfma_f32_16x16x32_bf16 v[228:231], a[140:143], a[188:191], v[228:231]\n"
"s_waitcnt lgkmcnt(3)\n"
"ds_write_b128 v22, v[244:247]\n"
"v_mfma_f32_16x16x32_bf16 v[204:207], a[0:3], v[32:35], 0\n"
"s_waitcnt lgkmcnt(3)\n"
"v_cvt_pk_bf16_f32 v252, v252, v253\n"
"v_cvt_pk_bf16_f32 v253, v254, v255\n"
"v_mfma_f32_16x16x32_bf16 v[208:211], a[4:7], v[32:35], 0\n"
"s_add_u32 m0, s34, 10240\n"
"v_cvt_pk_bf16_f32 v104, v104, v105\n"
"v_cvt_pk_bf16_f32 v105, v106, v107\n"
"v_mfma_f32_16x16x32_bf16 v[212:215], a[8:11], v[32:35], 0\n"
"s_add_u32 s40, s37, 22119552\n"
"buffer_load_dwordx4 v191, s[8:11], s0 offen lds\n"
"v_cvt_pk_bf16_f32 v106, v108, v109\n"
"v_mfma_f32_16x16x32_bf16 v[216:219], a[72:75], v[32:35], 0\n"
"s_waitcnt vmcnt(10)\n"
"v_cvt_pk_bf16_f32 v107, v110, v111\n"
"v_cvt_pk_bf16_f32 v254, v228, v229\n"
"v_mfma_f32_16x16x32_bf16 v[220:223], a[76:79], v[32:35], 0\n"
"s_add_u32 m0, s34, 8192\n"
"ds_read_b128 v[48:51], v188 offset:2048\n"
"ds_read_b128 v[52:55], v189 offset:2048\n"
"v_mfma_f32_16x16x32_bf16 v[224:227], a[80:83], v[32:35], 0\n"
"s_add_u32 s0, s0, s33\n"
"buffer_load_dwordx4 v190, s[8:11], s1 offen lds\n"
"ds_write_b128 v22, v[104:107] offset:128\n"
"v_mfma_f32_16x16x32_bf16 v[204:207], a[12:15], v[36:39], v[204:207]\n"
"s_waitcnt lgkmcnt(5)\n"
"v_cvt_pk_bf16_f32 v255, v230, v231\n"
"v_bfi_b32 v80, v4, v80, v26\n"
"v_mfma_f32_16x16x32_bf16 v[208:211], a[16:19], v[36:39], v[208:211]\n"
"s_add_u32 m0, s34, 11264\n"
"v_bfi_b32 v81, v5, v81, v26\n"
"v_bfi_b32 v86, v6, v86, v26\n"
"v_mfma_f32_16x16x32_bf16 v[212:215], a[20:23], v[36:39], v[212:215]\n"
"s_sub_u32 s16, s16, 99456\n"
"s_subb_u32 s17, s17, 0\n"
"buffer_load_dwordx4 v191, s[8:11], s1 offen lds\n"
"v_mfma_f32_16x16x32_bf16 v[216:219], a[84:87], v[36:39], v[216:219]\n"
"s_add_u32 s1, s1, s33\n"
"ds_write_b128 v22, v[252:255] offset:64\n"
"v_mfma_f32_16x16x32_bf16 v[220:223], a[88:91], v[36:39], v[220:223]\n"
"s_add_u32 m0, s65, 154752\n"
"v_bfi_b32 v87, v7, v87, v26\n"
"v_bfi_b32 v88, v0, v88, v26\n"
"v_mfma_f32_16x16x32_bf16 v[224:227], a[92:95], v[36:39], v[224:227]\n"
"s_waitcnt vmcnt(10)\n"
"v_bfi_b32 v89, v1, v89, v26\n"
"v_bfi_b32 v94, v2, v94, v26\n"
"v_mfma_f32_16x16x32_bf16 v[204:207], a[24:27], v[40:43], v[204:207]\n"
"s_waitcnt lgkmcnt(5)\n"
"buffer_load_dwordx4 v27, s[12:15], s81 offen lds\n"
"ds_read_b128 a[216:219], v70\n"
"v_mfma_f32_16x16x32_bf16 v[208:211], a[28:31], v[40:43], v[208:211]\n"
"s_mov_b32 s18, 173184\n"
"ds_read_b128 a[220:223], v71\n"
"v_bfi_b32 v95, v3, v95, v26\n"
"v_mfma_f32_16x16x32_bf16 v[212:215], a[32:35], v[40:43], v[212:215]\n"
"s_add_u32 m0, s65, 155776\n"
"v_bfi_b32 v96, v0, v96, v26\n"
"v_bfi_b32 v97, v1, v97, v26\n"
"v_mfma_f32_16x16x32_bf16 v[216:219], a[96:99], v[40:43], v[216:219]\n"
"s_add_u32 s80, s80, s79\n"
"v_bfi_b32 v102, v2, v102, v26\n"
"v_bfi_b32 v103, v3, v103, v26\n"
"v_mfma_f32_16x16x32_bf16 v[220:223], a[100:103], v[40:43], v[220:223]\n"
"buffer_load_dwordx4 v28, s[12:15], s81 offen lds\n"
"v_mfma_f32_16x16x32_bf16 v[224:227], a[104:107], v[40:43], v[224:227]\n"
"v_mfma_f32_16x16x32_bf16 v[204:207], a[36:39], v[44:47], v[204:207]\n"
"s_waitcnt lgkmcnt(5)\n"
"s_add_u32 m0, s65, 156800\n"
"v_mfma_f32_16x16x32_bf16 v[208:211], a[40:43], v[44:47], v[208:211]\n"
"buffer_load_dwordx4 v29, s[12:15], s81 offen lds\n"
"v_mfma_f32_16x16x32_bf16 v[212:215], a[44:47], v[44:47], v[212:215]\n"
"s_waitcnt lgkmcnt(1)\n"
"v_mfma_f32_16x16x32_bf16 v[216:219], a[108:111], v[44:47], v[216:219]\n"
"v_mfma_f32_16x16x32_bf16 v[220:223], a[112:115], v[44:47], v[220:223]\n"
"v_mfma_f32_16x16x32_bf16 v[224:227], a[116:119], v[44:47], v[224:227]\n"
"v_mfma_f32_16x16x32_bf16 v[204:207], a[48:51], v[48:51], v[204:207]\n"
"v_mfma_f32_16x16x32_bf16 v[208:211], a[52:55], v[48:51], v[208:211]\n"
"v_mfma_f32_16x16x32_bf16 v[212:215], a[56:59], v[48:51], v[212:215]\n"
"v_mfma_f32_16x16x32_bf16 v[216:219], a[120:123], v[48:51], v[216:219]\n"
"v_mfma_f32_16x16x32_bf16 v[220:223], a[124:127], v[48:51], v[220:223]\n"
"v_mfma_f32_16x16x32_bf16 v[224:227], a[128:131], v[48:51], v[224:227]\n"
"v_mfma_f32_16x16x32_bf16 v[204:207], a[60:63], v[52:55], v[204:207]\n"
"v_mfma_f32_16x16x32_bf16 v[208:211], a[64:67], v[52:55], v[208:211]\n"
"v_mfma_f32_16x16x32_bf16 v[212:215], a[68:71], v[52:55], v[212:215]\n"
"v_mfma_f32_16x16x32_bf16 v[216:219], a[132:135], v[52:55], v[216:219]\n"
"v_mfma_f32_16x16x32_bf16 v[220:223], a[136:139], v[52:55], v[220:223]\n"
"v_mfma_f32_16x16x32_bf16 v[224:227], a[140:143], v[52:55], v[224:227]\n"
"v_mfma_f32_16x16x32_bf16 v[244:247], a[0:3], a[216:219], 0\n"
"s_waitcnt lgkmcnt(0)\n"
"s_nop 0\n"
"v_cvt_pk_bf16_f32 v204, v204, v205\n"
"v_mfma_f32_16x16x32_bf16 v[248:251], a[4:7], a[216:219], 0\n"
"v_cvt_pk_bf16_f32 v205, v206, v207\n"
"v_cvt_pk_bf16_f32 v206, v208, v209\n"
"v_mfma_f32_16x16x32_bf16 v[252:255], a[8:11], a[216:219], 0\n"
"v_cvt_pk_bf16_f32 v207, v210, v211\n"
"v_cvt_pk_bf16_f32 v212, v212, v213\n"
"v_mfma_f32_16x16x32_bf16 v[104:107], a[72:75], a[216:219], 0\n"
"ds_write_b128 v186, v[204:207] offset:20256\n"
"v_mfma_f32_16x16x32_bf16 v[108:111], a[76:79], a[216:219], 0\n"
"v_cvt_pk_bf16_f32 v213, v214, v215\n"
"v_cvt_pk_bf16_f32 v216, v216, v217\n"
"v_mfma_f32_16x16x32_bf16 v[228:231], a[80:83], a[216:219], 0\n"
"v_cvt_pk_bf16_f32 v217, v218, v219\n"
"v_cvt_pk_bf16_f32 v218, v220, v221\n"
"v_mfma_f32_16x16x32_bf16 v[244:247], a[12:15], a[220:223], v[244:247]\n"
"v_cvt_pk_bf16_f32 v219, v222, v223\n"
"v_cvt_pk_bf16_f32 v214, v224, v225\n"
"v_mfma_f32_16x16x32_bf16 v[248:251], a[16:19], a[220:223], v[248:251]\n"
"s_waitcnt vmcnt(11)\n"
"ds_write_b128 v186, v[216:219] offset:20384\n"
"v_mfma_f32_16x16x32_bf16 v[252:255], a[20:23], a[220:223], v[252:255]\n"
"v_cvt_pk_bf16_f32 v215, v226, v227\n"
"v_mfma_f32_16x16x32_bf16 v[104:107], a[84:87], a[220:223], v[104:107]\n"
"ds_read_b128 a[224:227], v70 offset:1024\n"
"v_mfma_f32_16x16x32_bf16 v[108:111], a[88:91], a[220:223], v[108:111]\n"
"ds_read_b128 a[228:231], v71 offset:1024\n"
"ds_write_b128 v186, v[212:215] offset:20320\n"
"v_mfma_f32_16x16x32_bf16 v[228:231], a[92:95], a[220:223], v[228:231]\n"
"s_waitcnt vmcnt(9)\n"
"ds_read_b128 a[232:235], v70 offset:2048\n"
"ds_read_b128 a[236:239], v71 offset:2048\n"
"s_waitcnt lgkmcnt(4)\n"
"v_mfma_f32_16x16x32_bf16 v[244:247], a[24:27], a[224:227], v[244:247]\n"
"s_waitcnt lgkmcnt(3)\n"
"v_mfma_f32_16x16x32_bf16 v[248:251], a[28:31], a[224:227], v[248:251]\n"
"v_mfma_f32_16x16x32_bf16 v[252:255], a[32:35], a[224:227], v[252:255]\n"
"v_mfma_f32_16x16x32_bf16 v[104:107], a[96:99], a[224:227], v[104:107]\n"
"v_mfma_f32_16x16x32_bf16 v[108:111], a[100:103], a[224:227], v[108:111]\n"
"s_waitcnt vmcnt(7)\n"
"v_mfma_f32_16x16x32_bf16 v[228:231], a[104:107], a[224:227], v[228:231]\n"
"s_waitcnt lgkmcnt(0)\n"
"ds_read_b128 a[240:243], v188 offset:6144\n"
"ds_read_b128 a[244:247], v189 offset:6144\n"
"v_mfma_f32_16x16x32_bf16 v[244:247], a[36:39], a[228:231], v[244:247]\n"
"s_add_u32 m0, s35, 0\n"
"s_nop 0\n"
"buffer_load_dwordx4 v27, s[12:15], s3 offen lds\n"
"v_mfma_f32_16x16x32_bf16 v[248:251], a[40:43], a[228:231], v[248:251]\n"
"v_mfma_f32_16x16x32_bf16 v[252:255], a[44:47], a[228:231], v[252:255]\n"
"s_add_u32 m0, s35, 1024\n"
"v_mfma_f32_16x16x32_bf16 v[104:107], a[108:111], a[228:231], v[104:107]\n"
"buffer_load_dwordx4 v28, s[12:15], s3 offen lds\n"
"v_mfma_f32_16x16x32_bf16 v[108:111], a[112:115], a[228:231], v[108:111]\n"
"v_mfma_f32_16x16x32_bf16 v[228:231], a[116:119], a[228:231], v[228:231]\n"
"v_mfma_f32_16x16x32_bf16 v[244:247], a[48:51], a[232:235], v[244:247]\n"
"s_add_u32 m0, s35, 2048\n"
"v_mfma_f32_16x16x32_bf16 v[248:251], a[52:55], a[232:235], v[248:251]\n"
"s_waitcnt lgkmcnt(1)\n"
"buffer_load_dwordx4 v29, s[12:15], s3 offen lds\n"
"v_mfma_f32_16x16x32_bf16 v[252:255], a[56:59], a[232:235], v[252:255]\n"
"s_waitcnt vmcnt(8)\n"
"ds_read_b128 a[248:251], v188 offset:7168\n"
"ds_read_b128 a[252:255], v189 offset:7168\n"
"v_mfma_f32_16x16x32_bf16 v[104:107], a[120:123], a[232:235], v[104:107]\n"
"v_mfma_f32_16x16x32_bf16 v[108:111], a[124:127], a[232:235], v[108:111]\n"
"s_add_u32 m0, s34, 0\n"
"v_mfma_f32_16x16x32_bf16 v[228:231], a[128:131], a[232:235], v[228:231]\n"
"buffer_load_dwordx4 v190, s[8:11], s32 offen lds\n"
"v_mfma_f32_16x16x32_bf16 v[244:247], a[60:63], a[236:239], v[244:247]\n"
"v_mfma_f32_16x16x32_bf16 v[248:251], a[64:67], a[236:239], v[248:251]\n"
"v_mfma_f32_16x16x32_bf16 v[252:255], a[68:71], a[236:239], v[252:255]\n"
"s_add_u32 m0, s34, 3072\n"
"v_mfma_f32_16x16x32_bf16 v[104:107], a[132:135], a[236:239], v[104:107]\n"
"buffer_load_dwordx4 v191, s[8:11], s32 offen lds\n"
"v_mfma_f32_16x16x32_bf16 v[108:111], a[136:139], a[236:239], v[108:111]\n"
"s_waitcnt vmcnt(8)\n"
"s_add_u32 s32, s32, s33\n"
"v_cvt_pk_bf16_f32 v244, v244, v245\n"
"v_mfma_f32_16x16x32_bf16 v[228:231], a[140:143], a[236:239], v[228:231]\n"
"ds_read_b128 v[56:59], v188 offset:8192\n"
"ds_read_b128 v[60:63], v189 offset:8192\n"
"v_mfma_f32_16x16x32_bf16 v[120:123], a[0:3], a[240:243], 0\n"
"s_waitcnt lgkmcnt(4)\n"
"v_cvt_pk_bf16_f32 v245, v246, v247\n"
"v_cvt_pk_bf16_f32 v246, v248, v249\n"
"v_mfma_f32_16x16x32_bf16 v[124:127], a[4:7], a[240:243], 0\n"
"s_add_u32 m0, s34, 1024\n"
"v_cvt_pk_bf16_f32 v247, v250, v251\n"
"v_cvt_pk_bf16_f32 v252, v252, v253\n"
"v_mfma_f32_16x16x32_bf16 v[128:131], a[8:11], a[240:243], 0\n"
"buffer_load_dwordx4 v190, s[8:11], s0 offen lds\n"
"ds_write_b128 v186, v[244:247] offset:33760\n"
"v_mfma_f32_16x16x32_bf16 v[132:135], a[72:75], a[240:243], 0\n"
"s_add_u32 m0, s34, 4096\n"
"v_cvt_pk_bf16_f32 v253, v254, v255\n"
"v_cvt_pk_bf16_f32 v104, v104, v105\n"
"v_mfma_f32_16x16x32_bf16 v[136:139], a[76:79], a[240:243], 0\n"
"v_cvt_pk_bf16_f32 v105, v106, v107\n"
"v_cvt_pk_bf16_f32 v106, v108, v109\n"
"v_mfma_f32_16x16x32_bf16 v[140:143], a[80:83], a[240:243], 0\n"
"buffer_load_dwordx4 v191, s[8:11], s0 offen lds\n"
"v_cvt_pk_bf16_f32 v107, v110, v111\n"
"v_mfma_f32_16x16x32_bf16 v[120:123], a[12:15], a[244:247], v[120:123]\n"
"s_waitcnt lgkmcnt(4)\n"
"ds_write_b128 v186, v[104:107] offset:33888\n"
"v_mfma_f32_16x16x32_bf16 v[124:127], a[16:19], a[244:247], v[124:127]\n"
"s_add_u32 m0, s34, 2048\n"
"v_cvt_pk_bf16_f32 v254, v228, v229\n"
"v_cvt_pk_bf16_f32 v255, v230, v231\n"
"v_mfma_f32_16x16x32_bf16 v[128:131], a[20:23], a[244:247], v[128:131]\n"
"s_add_u32 s0, s0, s33\n"
"buffer_load_dwordx4 v190, s[8:11], s1 offen lds\n"
"ds_write_b128 v186, v[252:255] offset:33824\n"
"v_mfma_f32_16x16x32_bf16 v[132:135], a[84:87], a[244:247], v[132:135]\n"
"v_mov_b64 v[252:253], 0\n"
"v_mov_b64 v[254:255], 0\n"
"v_mfma_f32_16x16x32_bf16 v[136:139], a[88:91], a[244:247], v[136:139]\n"
"s_add_u32 m0, s34, 5120\n"
"ds_read_b64 v[104:105], v20 offset:1088\n"
"v_mfma_f32_16x16x32_bf16 v[140:143], a[92:95], a[244:247], v[140:143]\n"
"buffer_load_dwordx4 v191, s[8:11], s1 offen lds\n"
"ds_read_b64 v[110:111], v20 offset:1152\n"
"v_mfma_f32_16x16x32_bf16 v[120:123], a[24:27], a[248:251], v[120:123]\n"
"s_waitcnt lgkmcnt(7)\n"
"v_subrev_u32 v20, s49, v20\n"
"s_add_u32 s1, s1, s33\n"
"v_mfma_f32_16x16x32_bf16 v[124:127], a[28:31], a[248:251], v[124:127]\n"
"s_add_u32 m0, s34, 6144\n"
"v_mfma_f32_16x16x32_bf16 v[128:131], a[32:35], a[248:251], v[128:131]\n"
"v_mfma_f32_16x16x32_bf16 v[132:135], a[96:99], a[248:251], v[132:135]\n"
"buffer_load_dwordx4 v190, s[8:11], s32 offen lds\n"
"v_mfma_f32_16x16x32_bf16 v[136:139], a[100:103], a[248:251], v[136:139]\n"
"v_mfma_f32_16x16x32_bf16 v[140:143], a[104:107], a[248:251], v[140:143]\n"
"s_waitcnt lgkmcnt(1)\n"
"v_mfma_f32_16x16x32_bf16 v[120:123], a[36:39], a[252:255], v[120:123]\n"
"s_waitcnt lgkmcnt(0)\n"
"v_bfi_b32 v104, v8, v104, v26\n"
"v_bfi_b32 v105, v9, v105, v26\n"
"v_mfma_f32_16x16x32_bf16 v[124:127], a[40:43], a[252:255], v[124:127]\n"
"v_bfi_b32 v110, v10, v110, v26\n"
"v_bfi_b32 v111, v11, v111, v26\n"
"v_mfma_f32_16x16x32_bf16 v[128:131], a[44:47], a[252:255], v[128:131]\n"
"v_mfma_f32_16x16x32_bf16 v[132:135], a[108:111], a[252:255], v[132:135]\n"
"v_mfma_f32_16x16x32_bf16 v[136:139], a[112:115], a[252:255], v[136:139]\n"
"v_mfma_f32_16x16x32_bf16 v[140:143], a[116:119], a[252:255], v[140:143]\n"
"v_mfma_f32_16x16x32_bf16 v[120:123], a[48:51], v[56:59], v[120:123]\n"
"s_add_u32 m0, s34, 9216\n"
"s_nop 0\n"
"buffer_load_dwordx4 v191, s[8:11], s32 offen lds\n"
"v_mfma_f32_16x16x32_bf16 v[124:127], a[52:55], v[56:59], v[124:127]\n"
"s_add_u32 s32, s32, s33\n"
"v_mfma_f32_16x16x32_bf16 v[128:131], a[56:59], v[56:59], v[128:131]\n"
"v_mfma_f32_16x16x32_bf16 v[132:135], a[120:123], v[56:59], v[132:135]\n"
"v_mfma_f32_16x16x32_bf16 v[136:139], a[124:127], v[56:59], v[136:139]\n"
"s_add_u32 m0, s34, 7168\n"
"s_nop 0\n"
"buffer_load_dwordx4 v190, s[8:11], s0 offen lds\n"
"v_mfma_f32_16x16x32_bf16 v[140:143], a[128:131], v[56:59], v[140:143]\n"
"v_mfma_f32_16x16x32_bf16 v[120:123], a[60:63], v[60:63], v[120:123]\n"
"v_mfma_f32_16x16x32_bf16 v[124:127], a[64:67], v[60:63], v[124:127]\n"
"v_mfma_f32_16x16x32_bf16 v[128:131], a[68:71], v[60:63], v[128:131]\n"
"s_add_u32 m0, s34, 10240\n"
"s_nop 0\n"
"buffer_load_dwordx4 v191, s[8:11], s0 offen lds\n"
"v_mfma_f32_16x16x32_bf16 v[132:135], a[132:135], v[60:63], v[132:135]\n"
"s_add_u32 s0, s0, s33\n"
"v_mfma_f32_16x16x32_bf16 v[136:139], a[136:139], v[60:63], v[136:139]\n"
"v_cvt_pk_bf16_f32 v120, v120, v121\n"
"v_mfma_f32_16x16x32_bf16 v[140:143], a[140:143], v[60:63], v[140:143]\n"
"v_cvt_pk_bf16_f32 v121, v122, v123\n"
"v_cvt_pk_bf16_f32 v122, v124, v125\n"
"v_cvt_pk_bf16_f32 v123, v126, v127\n"
"s_add_u32 m0, s34, 8192\n"
"v_cvt_pk_bf16_f32 v128, v128, v129\n"
"buffer_load_dwordx4 v190, s[8:11], s1 offen lds\n"
"v_cvt_pk_bf16_f32 v132, v132, v133\n"
"v_cvt_pk_bf16_f32 v129, v130, v131\n"
"v_cvt_pk_bf16_f32 v133, v134, v135\n"
"v_cvt_pk_bf16_f32 v134, v136, v137\n"
"v_cvt_pk_bf16_f32 v135, v138, v139\n"
"v_cvt_pk_bf16_f32 v130, v140, v141\n"
"v_cvt_pk_bf16_f32 v131, v142, v143\n"
"ds_write_b128 v186, v[120:123] offset:47264\n"
"s_add_u32 m0, s34, 11264\n"
"s_waitcnt vmcnt(11)\n"
"buffer_load_dwordx4 v191, s[8:11], s1 offen lds\n"
"s_add_u32 s1, s1, s33\n"
"s_barrier\n"
"ds_read_b64_tr_b16 a[144:145], v68\n"
"ds_read_b64_tr_b16 a[146:147], v68 offset:1536\n"
"ds_read_b64_tr_b16 a[148:149], v68 offset:32\n"
"ds_read_b64_tr_b16 a[150:151], v68 offset:1568\n"
"ds_read_b64_tr_b16 a[152:153], v68 offset:64\n"
"ds_read_b64_tr_b16 a[154:155], v68 offset:1600\n"
"ds_read_b64_tr_b16 a[156:157], v68 offset:6144\n"
"ds_read_b64_tr_b16 a[158:159], v68 offset:7680\n"
"ds_read_b64_tr_b16 a[160:161], v68 offset:6176\n"
"ds_read_b64_tr_b16 a[162:163], v68 offset:7712\n"
"ds_read_b64_tr_b16 a[164:165], v68 offset:6208\n"
"ds_read_b64_tr_b16 a[166:167], v68 offset:7744\n"
"ds_read_b64_tr_b16 a[168:169], v68 offset:12288\n"
"ds_read_b64_tr_b16 a[170:171], v68 offset:13824\n"
"ds_read_b64_tr_b16 a[172:173], v68 offset:12320\n"
"ds_read_b64_tr_b16 a[174:175], v68 offset:13856\n"
"ds_read_b64_tr_b16 a[176:177], v68 offset:12352\n"
"ds_read_b64_tr_b16 a[178:179], v68 offset:13888\n"
"ds_read_b64_tr_b16 a[180:181], v68 offset:18432\n"
"ds_read_b64_tr_b16 a[182:183], v68 offset:19968\n"
"s_waitcnt lgkmcnt(15)\n"
"v_mfma_f32_16x16x32_bf16 v[204:207], a[144:147], v[32:35], 0\n"
"ds_read_b64_tr_b16 a[184:185], v68 offset:18464\n"
"ds_read_b64_tr_b16 a[186:187], v68 offset:20000\n"
"v_mfma_f32_16x16x32_bf16 v[208:211], a[148:151], v[32:35], 0\n"
"s_waitcnt lgkmcnt(15)\n"
"ds_read_b64_tr_b16 a[188:189], v68 offset:18496\n"
"ds_read_b64_tr_b16 a[190:191], v68 offset:20032\n"
"v_mfma_f32_16x16x32_bf16 v[212:215], a[152:155], v[32:35], 0\n"
"s_waitcnt lgkmcnt(15)\n"
"ds_read_b64_tr_b16 a[192:193], v68 offset:24576\n"
"ds_read_b64_tr_b16 a[194:195], v68 offset:26112\n"
"v_mfma_f32_16x16x32_bf16 v[204:207], a[156:159], v[36:39], v[204:207]\n"
"s_waitcnt lgkmcnt(15)\n"
"ds_read_b64_tr_b16 a[196:197], v68 offset:24608\n"
"ds_read_b64_tr_b16 a[198:199], v68 offset:26144\n"
"v_mfma_f32_16x16x32_bf16 v[208:211], a[160:163], v[36:39], v[208:211]\n"
"s_waitcnt lgkmcnt(15)\n"
"ds_read_b64_tr_b16 a[200:201], v68 offset:24640\n"
"ds_read_b64_tr_b16 a[202:203], v68 offset:26176\n"
"v_mfma_f32_16x16x32_bf16 v[212:215], a[164:167], v[36:39], v[212:215]\n"
"s_waitcnt lgkmcnt(15)\n"
"ds_read_b64_tr_b16 a[204:205], v68 offset:27648\n"
"ds_read_b64_tr_b16 a[206:207], v68 offset:29184\n"
"v_mfma_f32_16x16x32_bf16 v[204:207], a[168:171], v[40:43], v[204:207]\n"
"s_waitcnt lgkmcnt(15)\n"
"ds_read_b64_tr_b16 a[208:209], v68 offset:27680\n"
"ds_read_b64_tr_b16 a[210:211], v68 offset:29216\n"
"v_mfma_f32_16x16x32_bf16 v[208:211], a[172:175], v[40:43], v[208:211]\n"
"s_waitcnt lgkmcnt(15)\n"
"ds_read_b64_tr_b16 a[212:213], v68 offset:27712\n"
"ds_read_b64_tr_b16 a[214:215], v68 offset:29248\n"
"v_mfma_f32_16x16x32_bf16 v[212:215], a[176:179], v[40:43], v[212:215]\n"
"s_waitcnt lgkmcnt(15)\n"
"buffer_load_dword v200, off, s[28:31], s39 sc1\n"
"ds_write_b128 v186, v[128:131] offset:47328\n"
"v_mfma_f32_16x16x32_bf16 v[204:207], a[180:183], v[44:47], v[204:207]\n"
"s_waitcnt lgkmcnt(13)\n"
"v_mfma_f32_16x16x32_bf16 v[208:211], a[184:187], v[44:47], v[208:211]\n"
"s_waitcnt lgkmcnt(11)\n"
"ds_write_b128 v186, v[132:135] offset:47392\n"
"v_mfma_f32_16x16x32_bf16 v[212:215], a[188:191], v[44:47], v[212:215]\n"
"s_waitcnt lgkmcnt(8)\n"
"v_mfma_f32_16x16x32_bf16 v[204:207], a[192:195], v[48:51], v[204:207]\n"
"s_waitcnt lgkmcnt(6)\n"
"v_mfma_f32_16x16x32_bf16 v[208:211], a[196:199], v[48:51], v[208:211]\n"
"s_waitcnt lgkmcnt(4)\n"
"v_mfma_f32_16x16x32_bf16 v[212:215], a[200:203], v[48:51], v[212:215]\n"
"s_waitcnt lgkmcnt(2)\n"
"v_mfma_f32_16x16x32_bf16 v[204:207], a[204:207], v[52:55], v[204:207]\n"
"v_mfma_f32_16x16x32_bf16 v[208:211], a[208:211], v[52:55], v[208:211]\n"
"v_mfma_f32_16x16x32_bf16 v[212:215], a[212:215], v[52:55], v[212:215]\n"
"v_mfma_f32_16x16x32_bf16 v[216:219], a[144:147], a[216:219], 0\n"
"s_waitcnt lgkmcnt(0)\n"
"s_barrier\n"
"v_mfma_f32_16x16x32_bf16 v[216:219], a[156:159], a[220:223], v[216:219]\n"
"ds_read_b128 v[112:115], v178\n"
"ds_read_b128 v[120:123], v179\n"
"v_mfma_f32_16x16x32_bf16 v[220:223], a[148:151], a[216:219], 0\n"
"ds_read_b64 v[82:83], v178 offset:64\n"
"v_cvt_pk_bf16_f32 v144, v204, v205\n"
"v_mfma_f32_16x16x32_bf16 v[216:219], a[168:171], a[224:227], v[216:219]\n"
"ds_read_b64 v[84:85], v179 offset:64\n"
"v_cvt_pk_bf16_f32 v145, v206, v207\n"
"v_mfma_f32_16x16x32_bf16 v[220:223], a[160:163], a[220:223], v[220:223]\n"
"ds_read_b128 v[116:119], v178 offset:13504\n"
"v_cvt_pk_bf16_f32 v146, v208, v209\n"
"v_mfma_f32_16x16x32_bf16 v[216:219], a[180:183], a[228:231], v[216:219]\n"
"ds_read_b128 v[124:127], v179 offset:13504\n"
"v_cvt_pk_bf16_f32 v147, v210, v211\n"
"v_mfma_f32_16x16x32_bf16 v[220:223], a[172:175], a[224:227], v[220:223]\n"
"ds_read_b64 v[90:91], v178 offset:13568\n"
"v_cvt_pk_bf16_f32 v158, v212, v213\n"
"v_mfma_f32_16x16x32_bf16 v[216:219], a[192:195], a[232:235], v[216:219]\n"
"ds_read_b64 v[92:93], v179 offset:13568\n"
"v_cvt_pk_bf16_f32 v159, v214, v215\n"
"v_mfma_f32_16x16x32_bf16 v[220:223], a[184:187], a[228:231], v[220:223]\n"
"ds_read_b128 v[128:131], v178 offset:27008\n"
"buffer_load_dword v237, v67, s[8:11], s78 offen\n"
"v_mfma_f32_16x16x32_bf16 v[216:219], a[204:207], a[236:239], v[216:219]\n"
"s_waitcnt lgkmcnt(8)\n"
"ds_read_b128 v[136:139], v179 offset:27008\n"
"s_add_u32 s78, s78, s33\n"
"v_mfma_f32_16x16x32_bf16 v[112:115], v[112:115], v[144:147], 0\n"
"s_waitcnt lgkmcnt(8)\n"
"ds_read_b64 v[98:99], v178 offset:27072\n"
"v_subrev_u32 v178, s48, v178\n"
"v_mfma_f32_16x16x32_bf16 v[120:123], v[120:123], v[144:147], 0\n"
"s_waitcnt lgkmcnt(8)\n"
"ds_read_b64 v[100:101], v179 offset:27072\n"
"v_cvt_pk_bf16_f32 v148, v216, v217\n"
"v_mfma_f32_16x16x32_bf16 v[112:115], v[80:83], v[156:159], v[112:115]\n"
"s_waitcnt lgkmcnt(8)\n"
"ds_read_b128 v[132:135], v180\n"
"v_cvt_pk_bf16_f32 v149, v218, v219\n"
"v_mfma_f32_16x16x32_bf16 v[120:123], v[84:87], v[158:161], v[120:123]\n"
".Lic0%=:\n"
"s_cbranch_execz .Lib0%=\n"
"s_waitcnt lgkmcnt(8)\n"
"ds_read_b128 v[140:143], v181\n"
"buffer_load_dword v237, v67, s[8:11], s78 offen\n"
"v_mfma_f32_16x16x32_bf16 v[116:119], v[116:119], v[144:147], 0\n"
"s_waitcnt lgkmcnt(8)\n"
"ds_read_b64 v[106:107], v180 offset:64\n"
"v_subrev_u32 v179, s48, v179\n"
"v_mfma_f32_16x16x32_bf16 v[124:127], v[124:127], v[144:147], 0\n"
"s_waitcnt lgkmcnt(8)\n"
"ds_read_b64 v[108:109], v181 offset:64\n"
"v_subrev_u32 v180, s48, v180\n"
"v_mfma_f32_16x16x32_bf16 v[116:119], v[88:91], v[156:159], v[116:119]\n"
"s_waitcnt vmcnt(13) lgkmcnt(8)\n"
"ds_read_b64_tr_b16 v[244:245], v182 offset:128\n"
"v_subrev_u32 v181, s48, v181\n"
"v_mfma_f32_16x16x32_bf16 v[124:127], v[92:95], v[158:161], v[124:127]\n"
"s_waitcnt lgkmcnt(8)\n"
"ds_read_b128 v[32:35], v188\n"
"v_max_f32 v112, v112, v120\n"
"v_mfma_f32_16x16x32_bf16 v[128:131], v[128:131], v[144:147], 0\n"
"s_waitcnt lgkmcnt(8)\n"
"ds_read_b128 v[36:39], v189\n"
"v_max_f32 v113, v113, v121\n"
"v_mfma_f32_16x16x32_bf16 v[136:139], v[136:139], v[144:147], 0\n"
"s_waitcnt lgkmcnt(8)\n"
"ds_read_b64_tr_b16 v[246:247], v183 offset:128\n"
"v_max_f32 v114, v114, v122\n"
"v_mfma_f32_16x16x32_bf16 v[128:131], v[96:99], v[156:159], v[128:131]\n"
"s_waitcnt lgkmcnt(8)\n"
"ds_read_b64_tr_b16 v[248:249], v182 offset:896\n"
"v_max_f32 v115, v115, v123\n"
"v_mfma_f32_16x16x32_bf16 v[136:139], v[100:103], v[158:161], v[136:139]\n"
"s_waitcnt lgkmcnt(8)\n"
"ds_read_b64_tr_b16 v[250:251], v183 offset:896\n"
"v_max3_f32 v170, v112, v113, v114\n"
"v_mfma_f32_16x16x32_bf16 v[132:135], v[132:135], v[144:147], 0\n"
"s_waitcnt lgkmcnt(8)\n"
"ds_read_b64_tr_b16 v[120:121], v182 offset:136\n"
"v_max_f32 v116, v116, v124\n"
"v_mfma_f32_16x16x32_bf16 v[140:143], v[140:143], v[144:147], 0\n"
"s_waitcnt lgkmcnt(8)\n"
"ds_read_b64_tr_b16 v[122:123], v183 offset:136\n"
"v_max_f32 v117, v117, v125\n"
"v_mfma_f32_16x16x32_bf16 v[132:135], v[104:107], v[156:159], v[132:135]\n"
"s_waitcnt lgkmcnt(8)\n"
"ds_read_b64_tr_b16 v[124:125], v182 offset:904\n"
"v_max_f32 v118, v118, v126\n"
"v_mfma_f32_16x16x32_bf16 v[140:143], v[108:111], v[158:161], v[140:143]\n"
"s_waitcnt vmcnt(11)\n"
"v_max_f32 v119, v119, v127\n"
"v_max3_f32 v170, v170, v115, v116\n"
"v_mfma_f32_16x16x32_bf16 v[220:223], a[196:199], a[232:235], v[220:223]\n"
"s_waitcnt lgkmcnt(7)\n"
"ds_read_b128 v[40:43], v188 offset:1024\n"
"ds_read_b128 v[44:47], v189 offset:1024\n"
"v_mfma_f32_16x16x32_bf16 v[220:223], a[208:211], a[236:239], v[220:223]\n"
"s_waitcnt lgkmcnt(8)\n"
"ds_read_b64_tr_b16 v[126:127], v183 offset:904\n"
"v_max3_f32 v170, v170, v117, v118\n"
"v_mfma_f32_16x16x32_bf16 v[212:215], a[0:3], v[32:35], 0\n"
"s_add_u32 m0, s34, 0\n"
"v_max_f32 v128, v128, v136\n"
"v_max_f32 v129, v129, v137\n"
"v_mfma_f32_16x16x32_bf16 v[216:219], a[4:7], v[32:35], 0\n"
"s_add_u32 s78, s78, s33\n"
"buffer_load_dwordx4 v190, s[8:11], s32 offen lds\n"
"v_cvt_pk_bf16_f32 v150, v220, v221\n"
"v_mfma_f32_16x16x32_bf16 v[212:215], a[12:15], v[36:39], v[212:215]\n"
"v_cvt_pk_bf16_f32 v151, v222, v223\n"
"v_max_f32 v130, v130, v138\n"
"v_mfma_f32_16x16x32_bf16 v[220:223], a[8:11], v[32:35], 0\n"
"ds_read_b64_tr_b16 v[136:137], v182 offset:72\n"
"v_max_f32 v131, v131, v139\n"
"v_mfma_f32_16x16x32_bf16 v[216:219], a[16:19], v[36:39], v[216:219]\n"
"ds_read_b64_tr_b16 v[138:139], v183 offset:72\n"
"v_max3_f32 v170, v170, v119, v128\n"
"v_mfma_f32_16x16x32_bf16 v[220:223], a[20:23], v[36:39], v[220:223]\n"
"s_waitcnt vmcnt(10)\n"
"ds_read_b128 v[48:51], v188 offset:2048\n"
"ds_read_b128 v[52:55], v189 offset:2048\n"
"v_mfma_f32_16x16x32_bf16 v[224:227], a[152:155], a[216:219], 0\n"
"s_waitcnt lgkmcnt(6)\n"
"v_max3_f32 v170, v170, v129, v130\n"
"v_max_f32 v132, v132, v140\n"
"v_mfma_f32_16x16x32_bf16 v[212:215], a[24:27], v[40:43], v[212:215]\n"
"s_waitcnt lgkmcnt(5)\n"
"v_max_f32 v133, v133, v141\n"
"v_max_f32 v134, v134, v142\n"
"v_mfma_f32_16x16x32_bf16 v[216:219], a[28:31], v[40:43], v[216:219]\n"
"ds_read_b64_tr_b16 v[140:141], v182 offset:840\n"
"v_max_f32 v135, v135, v143\n"
"v_mfma_f32_16x16x32_bf16 v[220:223], a[32:35], v[40:43], v[220:223]\n"
"s_add_u32 m0, s34, 3072\n"
"ds_read_b64_tr_b16 v[142:143], v183 offset:840\n"
"buffer_load_dwordx4 v191, s[8:11], s32 offen lds\n"
"v_mfma_f32_16x16x32_bf16 v[212:215], a[36:39], v[44:47], v[212:215]\n"
"s_add_u32 s32, s32, s33\n"
"v_max3_f32 v170, v170, v131, v132\n"
"v_max3_f32 v170, v170, v133, v134\n"
"v_mfma_f32_16x16x32_bf16 v[216:219], a[40:43], v[44:47], v[216:219]\n"
"v_mov_b64 v[98:99], 0\n"
"v_mov_b64 v[100:101], 0\n"
"v_mfma_f32_16x16x32_bf16 v[220:223], a[44:47], v[44:47], v[220:223]\n"
"v_mov_b64 v[106:107], 0\n"
"v_mov_b64 v[108:109], 0\n"
"v_mfma_f32_16x16x32_bf16 v[224:227], a[164:167], a[220:223], v[224:227]\n"
"s_waitcnt lgkmcnt(3)\n"
"v_max_f32 v171, v170, v135\n"
"v_max_f32 v170, v170, v135\n"
"v_mfma_f32_16x16x32_bf16 v[212:215], a[48:51], v[48:51], v[212:215]\n"
"s_waitcnt lgkmcnt(2)\n"
"v_permlane16_swap_b32 v170, v171\n"
"v_mfma_f32_16x16x32_bf16 v[216:219], a[52:55], v[48:51], v[216:219]\n"
"v_max_f32 v170, v170, v171\n"
"v_mov_b32 v171, v170\n"
"v_mfma_f32_16x16x32_bf16 v[220:223], a[56:59], v[48:51], v[220:223]\n"
"s_nop 0\n"
"v_permlane32_swap_b32 v170, v171\n"
"v_mfma_f32_16x16x32_bf16 v[212:215], a[60:63], v[52:55], v[212:215]\n"
"s_add_u32 m0, s34, 1024\n"
"v_max_f32 v170, v170, v171\n"
"v_mul_f32 v170, 0xbe553b95, v170\n"
"v_mfma_f32_16x16x32_bf16 v[216:219], a[64:67], v[52:55], v[216:219]\n"
"buffer_load_dwordx4 v190, s[8:11], s0 offen lds\n"
"v_fmamk_f32 v112, v112, 0x3e553b95, v170\n"
"v_mfma_f32_16x16x32_bf16 v[220:223], a[68:71], v[52:55], v[220:223]\n"
"v_fmamk_f32 v113, v113, 0x3e553b95, v170\n"
"v_cvt_pk_bf16_f32 v212, v212, v213\n"
"v_mfma_f32_16x16x32_bf16 v[228:231], a[144:147], a[240:243], 0\n"
"v_cvt_pk_bf16_f32 v213, v214, v215\n"
"v_fmamk_f32 v114, v114, 0x3e553b95, v170\n"
"v_mfma_f32_16x16x32_bf16 v[224:227], a[176:179], a[224:227], v[224:227]\n"
"v_cvt_pk_bf16_f32 v214, v216, v217\n"
"v_cvt_pk_bf16_f32 v215, v218, v219\n"
"v_mfma_f32_16x16x32_bf16 v[228:231], a[156:159], a[244:247], v[228:231]\n"
"ds_write_b128 v186, v[212:215] offset:60768\n"
"v_mfma_f32_16x16x32_bf16 v[204:207], a[148:151], a[240:243], 0\n"
"v_cvt_pk_bf16_f32 v220, v220, v221\n"
"v_cvt_pk_bf16_f32 v221, v222, v223\n"
"v_mfma_f32_16x16x32_bf16 v[224:227], a[188:191], a[228:231], v[224:227]\n"
"v_fmamk_f32 v115, v115, 0x3e553b95, v170\n"
"v_fmamk_f32 v116, v116, 0x3e553b95, v170\n"
"v_mfma_f32_16x16x32_bf16 v[228:231], a[168:171], a[248:251], v[228:231]\n"
"s_add_u32 m0, s34, 4096\n"
"v_exp_f32 v112, v112\n"
"v_mfma_f32_16x16x32_bf16 v[204:207], a[160:163], a[244:247], v[204:207]\n"
"buffer_load_dwordx4 v191, s[8:11], s0 offen lds\n"
"v_fmamk_f32 v117, v117, 0x3e553b95, v170\n"
"v_mfma_f32_16x16x32_bf16 v[224:227], a[200:203], a[232:235], v[224:227]\n"
"s_add_u32 s0, s0, s33\n"
"v_exp_f32 v113, v113\n"
"v_mfma_f32_16x16x32_bf16 v[228:231], a[180:183], a[252:255], v[228:231]\n"
"v_exp_f32 v114, v114\n"
"v_mfma_f32_16x16x32_bf16 v[204:207], a[172:175], a[248:251], v[204:207]\n"
"v_exp_f32 v115, v115\n"
"v_mfma_f32_16x16x32_bf16 v[224:227], a[212:215], a[236:239], v[224:227]\n"
"v_cvt_pk_bf16_f32 v82, v112, v114\n"
"v_cvt_pk_bf16_f32 v83, v113, v115\n"
"v_mfma_f32_16x16x32_bf16 v[228:231], a[192:195], v[56:59], v[228:231]\n"
"ds_read_b64_tr_b16 v[112:113], v182 offset:27136\n"
"ds_read_b64_tr_b16 v[114:115], v183 offset:27136\n"
"v_mfma_f32_16x16x32_bf16 v[204:207], a[184:187], a[252:255], v[204:207]\n"
"v_fmamk_f32 v118, v118, 0x3e553b95, v170\n"
"v_fmamk_f32 v119, v119, 0x3e553b95, v170\n"
"v_mfma_f32_16x16x32_bf16 v[228:231], a[204:207], v[60:63], v[228:231]\n"
"v_cvt_pk_bf16_f32 v162, v224, v225\n"
"v_cvt_pk_bf16_f32 v163, v226, v227\n"
"v_mfma_f32_16x16x32_bf16 v[204:207], a[196:199], v[56:59], v[204:207]\n"
"s_waitcnt vmcnt(6)\n"
"v_exp_f32 v116, v116\n"
"v_mfma_f32_16x16x32_bf16 v[204:207], a[208:211], v[60:63], v[204:207]\n"
"s_add_u32 m0, s34, 2048\n"
"v_and_b32 v200, 0xffffff00, v200\n"
"v_add_u32 v200, 0x100, v200\n"
"v_mfma_f32_16x16x32_bf16 v[224:227], a[72:75], v[32:35], 0\n"
"buffer_load_dwordx4 v190, s[8:11], s1 offen lds\n"
"v_sub_u32 v201, 0, v200\n"
"v_mfma_f32_16x16x32_bf16 v[224:227], a[84:87], v[36:39], v[224:227]\n"
"s_nop 0\n"
"v_readfirstlane_b32 s50, v201\n"
"v_mfma_f32_16x16x32_bf16 v[224:227], a[96:99], v[40:43], v[224:227]\n"
"v_cvt_pk_bf16_f32 v152, v228, v229\n"
"v_cvt_pk_bf16_f32 v153, v230, v231\n"
"v_mfma_f32_16x16x32_bf16 v[228:231], a[76:79], v[32:35], 0\n"
"s_mov_b64 exec, 1\n"
"buffer_store_dword v200, off, s[28:31], s39 offset:128 sc1\n"
"s_mov_b64 exec, -1\n"
"v_mfma_f32_16x16x32_bf16 v[228:231], a[88:91], v[36:39], v[228:231]\n"
"v_cvt_pk_bf16_f32 v154, v204, v205\n"
"v_cvt_pk_bf16_f32 v155, v206, v207\n"
"v_mfma_f32_16x16x32_bf16 v[204:207], a[80:83], v[32:35], 0\n"
"v_exp_f32 v117, v117\n"
"v_mfma_f32_16x16x32_bf16 v[204:207], a[92:95], v[36:39], v[204:207]\n"
"v_exp_f32 v118, v118\n"
"v_mfma_f32_16x16x32_bf16 v[228:231], a[100:103], v[40:43], v[228:231]\n"
"s_add_u32 m0, s34, 5120\n"
"v_exp_f32 v119, v119\n"
"v_mfma_f32_16x16x32_bf16 v[204:207], a[104:107], v[40:43], v[204:207]\n"
"buffer_load_dwordx4 v191, s[8:11], s1 offen lds\n"
"v_cvt_pk_bf16_f32 v84, v116, v118\n"
"v_mfma_f32_16x16x32_bf16 v[224:227], a[108:111], v[44:47], v[224:227]\n"
"s_add_u32 s1, s1, s33\n"
"v_cvt_pk_bf16_f32 v85, v117, v119\n"
"v_fmamk_f32 v128, v128, 0x3e553b95, v170\n"
"v_mfma_f32_16x16x32_bf16 v[228:231], a[112:115], v[44:47], v[228:231]\n"
"s_waitcnt lgkmcnt(3)\n"
"ds_read_b64_tr_b16 v[116:117], v182 offset:27904\n"
"ds_read_b64_tr_b16 v[118:119], v183 offset:27904\n"
"v_mfma_f32_16x16x32_bf16 v[204:207], a[116:119], v[44:47], v[204:207]\n"
"ds_read_b64 v[80:81], v20 offset:272\n"
"ds_read_b64 v[86:87], v20 offset:336\n"
"v_mfma_f32_16x16x32_bf16 v[224:227], a[120:123], v[48:51], v[224:227]\n"
"ds_read_b64 v[88:89], v20 offset:544\n"
"v_fmamk_f32 v129, v129, 0x3e553b95, v170\n"
"v_mfma_f32_16x16x32_bf16 v[228:231], a[124:127], v[48:51], v[228:231]\n"
"v_fmamk_f32 v130, v130, 0x3e553b95, v170\n"
"v_fmamk_f32 v131, v131, 0x3e553b95, v170\n"
"v_mfma_f32_16x16x32_bf16 v[204:207], a[128:131], v[48:51], v[204:207]\n"
"ds_read_b64 v[94:95], v20 offset:608\n"
"ds_read_b64 v[96:97], v20 offset:816\n"
"v_mfma_f32_16x16x32_bf16 v[224:227], a[132:135], v[52:55], v[224:227]\n"
"v_exp_f32 v128, v128\n"
"v_mfma_f32_16x16x32_bf16 v[228:231], a[136:139], v[52:55], v[228:231]\n"
"v_exp_f32 v129, v129\n"
"v_mfma_f32_16x16x32_bf16 v[204:207], a[140:143], v[52:55], v[204:207]\n"
"v_exp_f32 v130, v130\n"
"v_mfma_f32_16x16x32_bf16 v[208:211], a[152:155], a[240:243], 0\n"
"ds_read_b64 v[102:103], v20 offset:880\n"
"ds_read_b64 v[104:105], v20 offset:1088\n"
"v_mfma_f32_16x16x32_bf16 v[208:211], a[164:167], a[244:247], v[208:211]\n"
"s_waitcnt lgkmcnt(6)\n"
"v_cvt_pk_bf16_f32 v224, v224, v225\n"
"v_cvt_pk_bf16_f32 v225, v226, v227\n"
"v_mfma_f32_16x16x32_bf16 v[208:211], a[176:179], a[248:251], v[208:211]\n"
"s_waitcnt lgkmcnt(4)\n"
"v_cvt_pk_bf16_f32 v226, v228, v229\n"
"v_cvt_pk_bf16_f32 v227, v230, v231\n"
"v_mfma_f32_16x16x32_bf16 v[208:211], a[188:191], a[252:255], v[208:211]\n"
"s_waitcnt lgkmcnt(3)\n"
"ds_write_b128 v186, v[224:227] offset:60896\n"
"v_mfma_f32_16x16x32_bf16 v[208:211], a[200:203], v[56:59], v[208:211]\n"
"s_waitcnt lgkmcnt(3)\n"
"v_cvt_pk_bf16_f32 v222, v204, v205\n"
"v_cvt_pk_bf16_f32 v223, v206, v207\n"
"v_mfma_f32_16x16x32_bf16 v[208:211], a[212:215], v[60:63], v[208:211]\n"
"v_exp_f32 v131, v131\n"
"v_mfma_f32_16x16x32_bf16 v[216:219], a[148:151], v[32:35], 0\n"
"s_waitcnt lgkmcnt(2)\n"
"ds_write_b128 v186, v[220:223] offset:60832\n"
"v_mfma_f32_16x16x32_bf16 v[212:215], a[152:155], v[32:35], 0\n"
"s_waitcnt lgkmcnt(2)\n"
"v_cvt_pk_bf16_f32 v90, v128, v130\n"
"v_cvt_pk_bf16_f32 v91, v129, v131\n"
"v_mfma_f32_16x16x32_bf16 v[216:219], a[160:163], v[36:39], v[216:219]\n"
"v_cvt_pk_bf16_f32 v166, v208, v209\n"
"v_cvt_pk_bf16_f32 v167, v210, v211\n"
"v_mfma_f32_16x16x32_bf16 v[208:211], a[144:147], v[32:35], 0\n"
"ds_read_b128 v[32:35], v188 offset:6144\n"
"v_fmamk_f32 v132, v132, 0x3e553b95, v170\n"
"v_mfma_f32_16x16x32_bf16 v[208:211], a[156:159], v[36:39], v[208:211]\n"
"ds_read_b64 v[110:111], v20 offset:1152\n"
"v_fmamk_f32 v133, v133, 0x3e553b95, v170\n"
"v_mfma_f32_16x16x32_bf16 v[212:215], a[164:167], v[36:39], v[212:215]\n"
"ds_read_b128 v[36:39], v189 offset:6144\n"
"v_fmamk_f32 v134, v134, 0x3e553b95, v170\n"
"v_mfma_f32_16x16x32_bf16 v[208:211], a[168:171], v[40:43], v[208:211]\n"
"v_fmamk_f32 v135, v135, 0x3e553b95, v170\n"
"v_exp_f32 v132, v132\n"
"v_mfma_f32_16x16x32_bf16 v[216:219], a[172:175], v[40:43], v[216:219]\n"
"v_exp_f32 v133, v133\n"
"v_mfma_f32_16x16x32_bf16 v[212:215], a[176:179], v[40:43], v[212:215]\n"
"s_waitcnt lgkmcnt(3)\n"
"ds_read_b128 v[40:43], v188 offset:7168\n"
"ds_read_b128 v[128:131], v179 offset:13504\n"
"v_mfma_f32_16x16x32_bf16 v[208:211], a[180:183], v[44:47], v[208:211]\n"
"s_barrier\n"
"v_mfma_f32_16x16x32_bf16 v[216:219], a[184:187], v[44:47], v[216:219]\n"
"s_waitcnt lgkmcnt(4)\n"
"v_exp_f32 v134, v134\n"
"v_mfma_f32_16x16x32_bf16 v[212:215], a[188:191], v[44:47], v[212:215]\n"
"s_waitcnt lgkmcnt(3)\n"
"ds_read_b128 v[44:47], v189 offset:7168\n"
"v_cvt_pk_bf16_f32 v92, v132, v134\n"
"v_mfma_f32_16x16x32_bf16 v[208:211], a[192:195], v[48:51], v[208:211]\n"
"s_waitcnt lgkmcnt(3)\n"
"v_exp_f32 v135, v135\n"
"v_mfma_f32_16x16x32_bf16 v[216:219], a[196:199], v[48:51], v[216:219]\n"
"s_add_u32 m0, s79, 130176\n"
"v_cvt_pk_bf16_f32 v93, v133, v135\n"
"v_bfi_b32 v80, v4, v80, v26\n"
"v_mfma_f32_16x16x32_bf16 v[212:215], a[200:203], v[48:51], v[212:215]\n"
"buffer_load_dwordx4 v192, s[16:19], s43 offen lds\n"
"ds_read_b128 v[48:51], v188 offset:8192\n"
"v_mfma_f32_16x16x32_bf16 v[208:211], a[204:207], v[52:55], v[208:211]\n"
"ds_read_b128 v[132:135], v179 offset:27008\n"
"v_bfi_b32 v81, v5, v81, v26\n"
"v_mfma_f32_16x16x32_bf16 v[216:219], a[208:211], v[52:55], v[216:219]\n"
"v_bfi_b32 v86, v6, v86, v26\n"
"v_bfi_b32 v87, v7, v87, v26\n"
"v_smfmac_f32_16x16x64_bf16 v[252:255], v[82:85], v[244:251], v66\n"
"s_waitcnt lgkmcnt(3)\n"
"ds_read_b64_tr_b16 v[244:245], v182 offset:27144\n"
"v_mfma_f32_16x16x32_bf16 v[212:215], a[212:215], v[52:55], v[212:215]\n"
"ds_read_b128 v[52:55], v189 offset:8192\n"
"ds_read_b64_tr_b16 v[246:247], v183 offset:27144\n"
"v_smfmac_f32_16x16x64_bf16 v[98:101], v[82:85], v[120:127], v66\n"
"ds_read_b64_tr_b16 v[248:249], v182 offset:27912\n"
"v_smfmac_f32_16x16x64_bf16 v[106:109], v[82:85], v[136:143], v66\n"
"ds_read_b64_tr_b16 v[250:251], v183 offset:27912\n"
"v_mfma_f32_16x16x32_bf16 v[82:85], v[172:175], v[82:85], 0\n"
"ds_read_b64_tr_b16 v[120:121], v182 offset:27080\n"
"v_cvt_pk_bf16_f32 v144, v208, v209\n"
"v_mfma_f32_16x16x32_bf16 v[228:231], a[0:3], v[32:35], 0\n"
"s_waitcnt lgkmcnt(8)\n"
"ds_read_b64_tr_b16 v[122:123], v183 offset:27080\n"
"v_cvt_pk_bf16_f32 v145, v210, v211\n"
"v_mfma_f32_16x16x32_bf16 v[204:207], a[4:7], v[32:35], 0\n"
"s_waitcnt lgkmcnt(7)\n"
"ds_read_b64_tr_b16 v[124:125], v182 offset:27848\n"
"v_cvt_pk_bf16_f32 v146, v216, v217\n"
"v_mfma_f32_16x16x32_bf16 v[220:223], a[8:11], v[32:35], 0\n"
"s_add_u32 m0, s79, 130176\n"
"ds_read_b64_tr_b16 v[126:127], v183 offset:27848\n"
"buffer_load_dwordx4 v192, s[16:19], s43 offen offset:1024 lds\n"
"v_mfma_f32_16x16x32_bf16 v[228:231], a[12:15], v[36:39], v[228:231]\n"
"v_cvt_pk_bf16_f32 v147, v218, v219\n"
"v_cvt_pk_bf16_f32 v158, v212, v213\n"
"v_smfmac_f32_16x16x64_bf16 v[252:255], v[90:93], v[112:119], v66\n"
"v_cvt_pk_bf16_f32 v159, v214, v215\n"
"v_mfma_f32_16x16x32_bf16 v[204:207], a[16:19], v[36:39], v[204:207]\n"
"ds_read_b128 v[136:139], v178 offset:13504\n"
"ds_read_b128 v[140:143], v178 offset:27008\n"
"v_mfma_f32_16x16x32_bf16 v[220:223], a[20:23], v[36:39], v[220:223]\n"
"v_subrev_u32 v182, s48, v182\n"
"v_subrev_u32 v183, s48, v183\n"
"v_mfma_f32_16x16x32_bf16 v[228:231], a[24:27], v[40:43], v[228:231]\n"
"s_waitcnt lgkmcnt(6)\n"
"ds_read_b128 v[112:115], v180\n"
"v_cvt_pk_bf16_f32 v252, v252, v253\n"
"v_smfmac_f32_16x16x64_bf16 v[98:101], v[90:93], v[244:251], v66\n"
"v_cvt_pk_bf16_f32 v253, v254, v255\n"
"v_mfma_f32_16x16x32_bf16 v[204:207], a[28:31], v[40:43], v[204:207]\n"
"ds_read_b128 v[244:247], v181\n"
"ds_read_b128 v[116:119], v180 offset:13504\n"
"v_mfma_f32_16x16x32_bf16 v[220:223], a[32:35], v[40:43], v[220:223]\n"
"s_add_u32 m0, s79, 130176\n"
"ds_read_b128 v[248:251], v181 offset:13504\n"
"buffer_load_dwordx4 v192, s[16:19], s43 offen offset:2048 lds\n"
"v_mfma_f32_16x16x32_bf16 v[228:231], a[36:39], v[44:47], v[228:231]\n"
"s_waitcnt lgkmcnt(6)\n"
"v_cvt_pk_bf16_f32 v98, v98, v99\n"
"v_cvt_pk_bf16_f32 v99, v100, v101\n"
"v_smfmac_f32_16x16x64_bf16 v[106:109], v[90:93], v[120:127], v66\n"
"s_add_u32 s43, s79, 18432\n"
"v_bfi_b32 v88, v0, v88, v26\n"
"v_mfma_f32_16x16x32_bf16 v[90:93], v[172:175], v[90:93], v[82:85]\n"
"s_waitcnt lgkmcnt(5)\n"
"v_bfi_b32 v89, v1, v89, v26\n"
"v_bfi_b32 v94, v2, v94, v26\n"
"v_mfma_f32_16x16x32_bf16 v[204:207], a[40:43], v[44:47], v[204:207]\n"
"s_waitcnt lgkmcnt(4)\n"
"ds_read_b64 v[82:83], v178 offset:13568\n"
"ds_read_b64 v[84:85], v179 offset:13568\n"
"v_mfma_f32_16x16x32_bf16 v[220:223], a[44:47], v[44:47], v[220:223]\n"
"s_waitcnt lgkmcnt(5)\n"
"ds_read_b64 v[92:93], v179 offset:27072\n"
"v_cvt_pk_bf16_f32 v106, v106, v107\n"
"v_mfma_f32_16x16x32_bf16 v[228:231], a[48:51], v[48:51], v[228:231]\n"
"v_rcp_f32 v171, v90\n"
"v_mfma_f32_16x16x32_bf16 v[204:207], a[52:55], v[48:51], v[204:207]\n"
"s_waitcnt lgkmcnt(5)\n"
"ds_read_b64 v[90:91], v178 offset:27072\n"
"v_cvt_pk_bf16_f32 v171, v171, v171\n"
"v_mfma_f32_16x16x32_bf16 v[220:223], a[56:59], v[48:51], v[220:223]\n"
"s_waitcnt lgkmcnt(5)\n"
"v_and_b32 v64, v171, v176\n"
"v_and_b32 v65, v171, v177\n"
"v_mfma_f32_16x16x32_bf16 v[228:231], a[60:63], v[52:55], v[228:231]\n"
"s_waitcnt lgkmcnt(4)\n"
"v_cvt_pk_bf16_f32 v107, v108, v109\n"
"v_bfi_b32 v95, v3, v95, v26\n"
"v_mfma_f32_16x16x16_bf16 v[252:255], v[252:253], v[64:65], 0\n"
"s_add_u32 m0, s34, 6144\n"
"ds_read_b64_tr_b16 v[120:121], v182 offset:13632\n"
"buffer_load_dwordx4 v190, s[8:11], s32 offen lds\n"
"v_mfma_f32_16x16x16_bf16 v[98:101], v[98:99], v[64:65], 0\n"
"v_cvt_pk_bf16_f32 v228, v228, v229\n"
"v_cvt_pk_bf16_f32 v229, v230, v231\n"
"v_mfma_f32_16x16x16_bf16 v[106:109], v[106:107], v[64:65], 0\n"
"ds_read_b64_tr_b16 v[122:123], v183 offset:13632\n"
"v_cvt_pk_bf16_f32 v252, v252, v253\n"
"v_mfma_f32_16x16x32_bf16 v[204:207], a[64:67], v[52:55], v[204:207]\n"
"v_cvt_pk_bf16_f32 v253, v254, v255\n"
"v_bfi_b32 v96, v0, v96, v26\n"
"v_mfma_f32_16x16x32_bf16 v[220:223], a[68:71], v[52:55], v[220:223]\n"
"v_cvt_pk_bf16_f32 v254, v98, v99\n"
"v_cvt_pk_bf16_f32 v255, v100, v101\n"
"v_mfma_f32_16x16x32_bf16 v[224:227], a[72:75], v[32:35], 0\n"
"buffer_store_dwordx4 v[252:255], v192, s[28:31], s37 offen sc1\n"
"v_mfma_f32_16x16x32_bf16 v[208:211], a[76:79], v[32:35], 0\n"
"ds_read_b64 v[98:99], v180 offset:64\n"
"ds_read_b64 v[100:101], v181 offset:64\n"
"v_mfma_f32_16x16x32_bf16 v[216:219], a[80:83], v[32:35], 0\n"
"s_add_u32 m0, s34, 9216\n"
"v_cvt_pk_bf16_f32 v106, v106, v107\n"
"v_cvt_pk_bf16_f32 v107, v108, v109\n"
"v_mfma_f32_16x16x32_bf16 v[136:139], v[136:139], v[148:151], 0\n"
"s_waitcnt lgkmcnt(7)\n"
"buffer_load_dwordx4 v191, s[8:11], s32 offen lds\n"
"ds_read_b64 v[108:109], v181 offset:13568\n"
"v_mfma_f32_16x16x32_bf16 v[128:131], v[128:131], v[148:151], 0\n"
"s_waitcnt lgkmcnt(7)\n"
"v_cvt_pk_bf16_f32 v230, v204, v205\n"
"v_cvt_pk_bf16_f32 v231, v206, v207\n"
"v_mfma_f32_16x16x32_bf16 v[224:227], a[84:87], v[36:39], v[224:227]\n"
"s_add_u32 s32, s32, s33\n"
"ds_write_b128 v187, v[228:231]\n"
"v_mfma_f32_16x16x32_bf16 v[136:139], v[80:83], v[160:163], v[136:139]\n"
"buffer_store_dwordx2 v[106:107], v193, s[28:31], s37 offen offset:1024 sc1\n"
"v_mfma_f32_16x16x32_bf16 v[208:211], a[88:91], v[36:39], v[208:211]\n"
"s_add_u32 s37, s37, 1572864\n"
"ds_read_b64 v[106:107], v180 offset:13568\n"
"v_cvt_pk_bf16_f32 v220, v220, v221\n"
"v_mfma_f32_16x16x32_bf16 v[128:131], v[84:87], v[162:165], v[128:131]\n"
"v_cvt_pk_bf16_f32 v221, v222, v223\n"
"v_bfi_b32 v97, v1, v97, v26\n"
"v_mfma_f32_16x16x32_bf16 v[216:219], a[92:95], v[36:39], v[216:219]\n"
"ds_read_b64_tr_b16 v[124:125], v182 offset:14400\n"
"buffer_load_dword v237, v67, s[8:11], s78 offen\n"
"v_mfma_f32_16x16x32_bf16 v[140:143], v[140:143], v[148:151], 0\n"
"s_waitcnt lgkmcnt(8)\n"
"ds_read_b64_tr_b16 v[126:127], v183 offset:14400\n"
"v_bfi_b32 v102, v2, v102, v26\n"
"v_mfma_f32_16x16x32_bf16 v[224:227], a[96:99], v[40:43], v[224:227]\n"
"s_add_u32 s78, s78, s33\n"
"v_bfi_b32 v103, v3, v103, v26\n"
"v_bfi_b32 v104, v8, v104, v26\n"
"v_mfma_f32_16x16x32_bf16 v[132:135], v[132:135], v[148:151], 0\n"
"s_add_u32 m0, s34, 7168\n"
"v_bfi_b32 v105, v9, v105, v26\n"
"v_bfi_b32 v110, v10, v110, v26\n"
"v_mfma_f32_16x16x32_bf16 v[208:211], a[100:103], v[40:43], v[208:211]\n"
"v_bfi_b32 v111, v11, v111, v26\n"
"v_max_f32 v136, v136, v128\n"
"v_mfma_f32_16x16x32_bf16 v[140:143], v[88:91], v[160:163], v[140:143]\n"
"buffer_load_dwordx4 v190, s[8:11], s0 offen lds\n"
"v_max_f32 v137, v137, v129\n"
"v_mfma_f32_16x16x32_bf16 v[132:135], v[92:95], v[162:165], v[132:135]\n"
"ds_read_b64_tr_b16 v[128:129], v182 offset:13640\n"
"v_max_f32 v138, v138, v130\n"
"v_mfma_f32_16x16x32_bf16 v[216:219], a[104:107], v[40:43], v[216:219]\n"
"v_max_f32 v139, v139, v131\n"
"v_max3_f32 v170, v136, v137, v138\n"
"v_mfma_f32_16x16x32_bf16 v[224:227], a[108:111], v[44:47], v[224:227]\n"
"ds_read_b64_tr_b16 v[130:131], v183 offset:13640\n"
"buffer_load_dword v237, v67, s[8:11], s78 offen\n"
"v_mfma_f32_16x16x32_bf16 v[112:115], v[112:115], v[148:151], 0\n"
"s_waitcnt lgkmcnt(8)\n"
"v_max_f32 v140, v140, v132\n"
"v_max_f32 v141, v141, v133\n"
"v_mfma_f32_16x16x32_bf16 v[208:211], a[112:115], v[44:47], v[208:211]\n"
"s_add_u32 s78, s78, s33\n"
"ds_read_b64_tr_b16 v[132:133], v182 offset:14408\n"
"v_max_f32 v142, v142, v134\n"
"v_mfma_f32_16x16x32_bf16 v[244:247], v[244:247], v[148:151], 0\n"
"s_waitcnt lgkmcnt(8)\n"
"v_max_f32 v143, v143, v135\n"
"v_max3_f32 v170, v170, v139, v140\n"
"v_mfma_f32_16x16x32_bf16 v[216:219], a[116:119], v[44:47], v[216:219]\n"
"ds_read_b64_tr_b16 v[134:135], v183 offset:14408\n"
"v_max3_f32 v170, v170, v141, v142\n"
"v_mfma_f32_16x16x32_bf16 v[112:115], v[96:99], v[160:163], v[112:115]\n"
"s_add_u32 m0, s34, 10240\n"
"v_mov_b64 v[252:253], 0\n"
"v_mov_b64 v[254:255], 0\n"
"v_mfma_f32_16x16x32_bf16 v[244:247], v[100:103], v[162:165], v[244:247]\n"
"buffer_load_dwordx4 v191, s[8:11], s0 offen lds\n"
"v_mov_b64 v[98:99], 0\n"
"v_mfma_f32_16x16x32_bf16 v[224:227], a[120:123], v[48:51], v[224:227]\n"
"s_add_u32 s0, s0, s33\n"
"v_mov_b64 v[100:101], 0\n"
"v_mfma_f32_16x16x32_bf16 v[208:211], a[124:127], v[48:51], v[208:211]\n"
"ds_read_b64 v[80:81], v20 offset:272\n"
"ds_read_b64 v[86:87], v20 offset:336\n"
"v_mfma_f32_16x16x32_bf16 v[116:119], v[116:119], v[148:151], 0\n"
"s_waitcnt lgkmcnt(8)\n"
"v_max_f32 v112, v112, v244\n"
"v_max_f32 v113, v113, v245\n"
"v_mfma_f32_16x16x32_bf16 v[216:219], a[128:131], v[48:51], v[216:219]\n"
"ds_read_b64_tr_b16 v[244:245], v182 offset:13576\n"
"v_max_f32 v114, v114, v246\n"
"v_mfma_f32_16x16x32_bf16 v[248:251], v[248:251], v[148:151], 0\n"
"v_max_f32 v115, v115, v247\n"
"v_max3_f32 v170, v170, v143, v112\n"
"v_mfma_f32_16x16x32_bf16 v[116:119], v[104:107], v[160:163], v[116:119]\n"
"ds_read_b64_tr_b16 v[246:247], v183 offset:13576\n"
"v_max3_f32 v170, v170, v113, v114\n"
"v_mfma_f32_16x16x32_bf16 v[224:227], a[132:135], v[52:55], v[224:227]\n"
"v_mov_b64 v[106:107], 0\n"
"s_add_u32 m0, s34, 8192\n"
"v_mfma_f32_16x16x32_bf16 v[248:251], v[108:111], v[162:165], v[248:251]\n"
"buffer_load_dwordx4 v190, s[8:11], s1 offen lds\n"
"v_mov_b64 v[108:109], 0\n"
"v_mfma_f32_16x16x32_bf16 v[208:211], a[136:139], v[52:55], v[208:211]\n"
"s_waitcnt lgkmcnt(3)\n"
"ds_read_b64 v[104:105], v20 offset:1088\n"
"v_cvt_pk_bf16_f32 v224, v224, v225\n"
"v_mfma_f32_16x16x32_bf16 v[216:219], a[140:143], v[52:55], v[216:219]\n"
"s_waitcnt lgkmcnt(3)\n"
"v_cvt_pk_bf16_f32 v225, v226, v227\n"
"v_max_f32 v116, v116, v248\n"
"v_mfma_f32_16x16x32_bf16 v[212:215], a[144:147], v[32:35], 0\n"
"v_cvt_pk_bf16_f32 v226, v208, v209\n"
"v_cvt_pk_bf16_f32 v227, v210, v211\n"
"v_mfma_f32_16x16x32_bf16 v[204:207], a[148:151], v[32:35], 0\n"
"ds_write_b128 v187, v[224:227] offset:128\n"
"v_mfma_f32_16x16x32_bf16 v[228:231], a[152:155], v[32:35], 0\n"
"s_waitcnt vmcnt(17)\n"
"v_cvt_pk_bf16_f32 v222, v216, v217\n"
"v_cvt_pk_bf16_f32 v223, v218, v219\n"
"v_mfma_f32_16x16x32_bf16 v[212:215], a[156:159], v[36:39], v[212:215]\n"
"v_max_f32 v117, v117, v249\n"
"v_max_f32 v118, v118, v250\n"
"v_mfma_f32_16x16x32_bf16 v[204:207], a[160:163], v[36:39], v[204:207]\n"
"s_waitcnt lgkmcnt(1)\n"
"ds_read_b128 v[32:35], v188\n"
"ds_write_b128 v187, v[220:223] offset:64\n"
"v_mfma_f32_16x16x32_bf16 v[228:231], a[164:167], v[36:39], v[228:231]\n"
"s_add_u32 m0, s34, 11264\n"
"v_max_f32 v119, v119, v251\n"
"v_max3_f32 v170, v170, v115, v116\n"
"v_mfma_f32_16x16x32_bf16 v[212:215], a[168:171], v[40:43], v[212:215]\n"
"buffer_load_dwordx4 v191, s[8:11], s1 offen lds\n"
"ds_read_b128 v[36:39], v189\n"
"v_mfma_f32_16x16x32_bf16 v[204:207], a[172:175], v[40:43], v[204:207]\n"
"s_add_u32 s1, s1, s33\n"
"ds_read_b64_tr_b16 v[248:249], v182 offset:14344\n"
"ds_read_b64_tr_b16 v[250:251], v183 offset:14344\n"
"v_mfma_f32_16x16x32_bf16 v[228:231], a[176:179], v[40:43], v[228:231]\n"
"s_waitcnt vmcnt(16)\n"
"ds_read_b128 v[40:43], v188 offset:1024\n"
"v_max3_f32 v170, v170, v117, v118\n"
"v_mfma_f32_16x16x32_bf16 v[212:215], a[180:183], v[44:47], v[212:215]\n"
"v_max_f32 v171, v170, v119\n"
"v_max_f32 v170, v170, v119\n"
"v_mfma_f32_16x16x32_bf16 v[204:207], a[184:187], v[44:47], v[204:207]\n"
"s_nop 0\n"
"v_permlane16_swap_b32 v170, v171\n"
"v_mfma_f32_16x16x32_bf16 v[228:231], a[188:191], v[44:47], v[228:231]\n"
"s_waitcnt lgkmcnt(5)\n"
"ds_read_b128 v[44:47], v189 offset:1024\n"
"v_max_f32 v170, v170, v171\n"
"v_mfma_f32_16x16x32_bf16 v[212:215], a[192:195], v[48:51], v[212:215]\n"
"v_mov_b32 v171, v170\n"
"v_bfi_b32 v80, v12, v80, v26\n"
"v_mfma_f32_16x16x32_bf16 v[204:207], a[196:199], v[48:51], v[204:207]\n"
"v_permlane32_swap_b32 v170, v171\n"
"v_mfma_f32_16x16x32_bf16 v[228:231], a[200:203], v[48:51], v[228:231]\n"
"s_waitcnt vmcnt(13) lgkmcnt(4)\n"
"ds_read_b128 v[48:51], v188 offset:2048\n"
"v_max_f32 v170, v170, v171\n"
"v_mfma_f32_16x16x32_bf16 v[212:215], a[204:207], v[52:55], v[212:215]\n"
"s_add_u32 m0, s34, 0\n"
"v_mul_f32 v170, 0xbe553b95, v170\n"
"v_fmamk_f32 v136, v136, 0x3e553b95, v170\n"
"v_mfma_f32_16x16x32_bf16 v[204:207], a[208:211], v[52:55], v[204:207]\n"
"s_waitcnt vmcnt(10) lgkmcnt(3)\n"
"buffer_load_dwordx4 v190, s[8:11], s32 offen lds\n"
"v_fmamk_f32 v137, v137, 0x3e553b95, v170\n"
"v_mfma_f32_16x16x32_bf16 v[228:231], a[212:215], v[52:55], v[228:231]\n"
"s_barrier\n"
"v_mfma_f32_16x16x32_bf16 v[208:211], a[0:3], v[32:35], 0\n"
"ds_read_b128 v[52:55], v189 offset:2048\n"
"v_fmamk_f32 v138, v138, 0x3e553b95, v170\n"
"v_mfma_f32_16x16x32_bf16 v[216:219], a[4:7], v[32:35], 0\n"
"buffer_load_dword v201, off, s[28:31], s40 offset:128 sc1\n"
"v_fmamk_f32 v139, v139, 0x3e553b95, v170\n"
"v_mfma_f32_16x16x32_bf16 v[220:223], a[8:11], v[32:35], 0\n"
"v_exp_f32 v136, v136\n"
"v_mfma_f32_16x16x32_bf16 v[208:211], a[12:15], v[36:39], v[208:211]\n"
"s_waitcnt lgkmcnt(3)\n"
"v_exp_f32 v137, v137\n"
"v_mfma_f32_16x16x32_bf16 v[216:219], a[16:19], v[36:39], v[216:219]\n"
"v_exp_f32 v138, v138\n"
"v_mfma_f32_16x16x32_bf16 v[220:223], a[20:23], v[36:39], v[220:223]\n"
"v_exp_f32 v139, v139\n"
"v_mfma_f32_16x16x32_bf16 v[208:211], a[24:27], v[40:43], v[208:211]\n"
"s_waitcnt lgkmcnt(2)\n"
"v_cvt_pk_bf16_f32 v82, v136, v138\n"
"v_cvt_pk_bf16_f32 v83, v137, v139\n"
"v_mfma_f32_16x16x32_bf16 v[216:219], a[28:31], v[40:43], v[216:219]\n"
"s_add_u32 m0, s79, 142464\n"
"ds_read_b64_tr_b16 v[136:137], v182 offset:40640\n"
".Lic1%=:\n"
"s_cbranch_execz .Lib0%=\n"
"buffer_load_dwordx4 v192, s[16:19], s43 offen lds\n"
"v_mfma_f32_16x16x32_bf16 v[220:223], a[32:35], v[40:43], v[220:223]\n"
"ds_read_b64_tr_b16 v[138:139], v183 offset:40640\n"
"v_fmamk_f32 v140, v140, 0x3e553b95, v170\n"
"v_mfma_f32_16x16x32_bf16 v[208:211], a[36:39], v[44:47], v[208:211]\n"
"s_waitcnt lgkmcnt(3)\n"
"v_fmamk_f32 v141, v141, 0x3e553b95, v170\n"
"v_fmamk_f32 v142, v142, 0x3e553b95, v170\n"
"v_mfma_f32_16x16x32_bf16 v[216:219], a[40:43], v[44:47], v[216:219]\n"
"v_fmamk_f32 v143, v143, 0x3e553b95, v170\n"
"v_cvt_pk_bf16_f32 v148, v212, v213\n"
"v_mfma_f32_16x16x32_bf16 v[220:223], a[44:47], v[44:47], v[220:223]\n"
"v_exp_f32 v140, v140\n"
"v_mfma_f32_16x16x32_bf16 v[208:211], a[48:51], v[48:51], v[208:211]\n"
"s_waitcnt lgkmcnt(2)\n"
"v_exp_f32 v141, v141\n"
"v_mfma_f32_16x16x32_bf16 v[216:219], a[52:55], v[48:51], v[216:219]\n"
"v_exp_f32 v142, v142\n"
"v_mfma_f32_16x16x32_bf16 v[220:223], a[56:59], v[48:51], v[220:223]\n"
"v_exp_f32 v143, v143\n"
"v_mfma_f32_16x16x32_bf16 v[208:211], a[60:63], v[52:55], v[208:211]\n"
"s_add_u32 m0, s79, 142464\n"
"v_cvt_pk_bf16_f32 v84, v140, v142\n"
"v_cvt_pk_bf16_f32 v85, v141, v143\n"
"v_mfma_f32_16x16x32_bf16 v[216:219], a[64:67], v[52:55], v[216:219]\n"
"buffer_load_dwordx4 v192, s[16:19], s43 offen offset:1024 lds\n"
"ds_read_b64_tr_b16 v[140:141], v182 offset:41408\n"
"v_smfmac_f32_16x16x64_bf16 v[252:255], v[82:85], v[120:127], v66\n"
"ds_read_b64_tr_b16 v[142:143], v183 offset:41408\n"
"v_smfmac_f32_16x16x64_bf16 v[98:101], v[82:85], v[128:135], v66\n"
"ds_read_b64_tr_b16 v[120:121], v182 offset:40648\n"
"v_smfmac_f32_16x16x64_bf16 v[106:109], v[82:85], v[244:251], v66\n"
"ds_read_b64_tr_b16 v[122:123], v183 offset:40648\n"
"v_mfma_f32_16x16x32_bf16 v[82:85], v[172:175], v[82:85], 0\n"
"ds_read_b64_tr_b16 v[124:125], v182 offset:41416\n"
"ds_read_b64_tr_b16 v[126:127], v183 offset:41416\n"
"v_mfma_f32_16x16x32_bf16 v[220:223], a[68:71], v[52:55], v[220:223]\n"
"ds_read_b64_tr_b16 v[128:129], v182 offset:40584\n"
"v_cvt_pk_bf16_f32 v149, v214, v215\n"
"v_mfma_f32_16x16x32_bf16 v[224:227], a[72:75], v[32:35], 0\n"
"ds_read_b64_tr_b16 v[130:131], v183 offset:40584\n"
"v_cvt_pk_bf16_f32 v150, v204, v205\n"
"v_mfma_f32_16x16x32_bf16 v[212:215], a[76:79], v[32:35], 0\n"
"ds_read_b64_tr_b16 v[132:133], v182 offset:41352\n"
"v_cvt_pk_bf16_f32 v151, v206, v207\n"
"v_mfma_f32_16x16x32_bf16 v[204:207], a[80:83], v[32:35], 0\n"
"s_add_u32 m0, s79, 142464\n"
"ds_read_b64_tr_b16 v[134:135], v183 offset:41352\n"
"buffer_load_dwordx4 v192, s[16:19], s43 offen offset:2048 lds\n"
"v_mfma_f32_16x16x32_bf16 v[224:227], a[84:87], v[36:39], v[224:227]\n"
"s_add_u32 s43, s79, 36864\n"
"v_cvt_pk_bf16_f32 v162, v228, v229\n"
"v_cvt_pk_bf16_f32 v163, v230, v231\n"
"v_mfma_f32_16x16x32_bf16 v[212:215], a[88:91], v[36:39], v[212:215]\n"
"v_fmamk_f32 v112, v112, 0x3e553b95, v170\n"
"v_fmamk_f32 v113, v113, 0x3e553b95, v170\n"
"v_mfma_f32_16x16x32_bf16 v[204:207], a[92:95], v[36:39], v[204:207]\n"
"v_fmamk_f32 v114, v114, 0x3e553b95, v170\n"
"v_fmamk_f32 v115, v115, 0x3e553b95, v170\n"
"v_mfma_f32_16x16x32_bf16 v[224:227], a[96:99], v[40:43], v[224:227]\n"
"ds_read_b64 v[110:111], v20 offset:1152\n"
"ds_read_b128 v[244:247], v178 offset:27008\n"
"v_mfma_f32_16x16x32_bf16 v[212:215], a[100:103], v[40:43], v[212:215]\n"
"v_exp_f32 v112, v112\n"
"v_mfma_f32_16x16x32_bf16 v[204:207], a[104:107], v[40:43], v[204:207]\n"
"v_exp_f32 v113, v113\n"
"v_mfma_f32_16x16x32_bf16 v[224:227], a[108:111], v[44:47], v[224:227]\n"
"v_exp_f32 v114, v114\n"
"v_mfma_f32_16x16x32_bf16 v[212:215], a[112:115], v[44:47], v[212:215]\n"
"s_add_u32 m0, s34, 3072\n"
"v_exp_f32 v115, v115\n"
"v_mfma_f32_16x16x32_bf16 v[204:207], a[116:119], v[44:47], v[204:207]\n"
"buffer_load_dwordx4 v191, s[8:11], s32 offen lds\n"
"v_cvt_pk_bf16_f32 v90, v112, v114\n"
"v_mfma_f32_16x16x32_bf16 v[224:227], a[120:123], v[48:51], v[224:227]\n"
"s_add_u32 s32, s32, s33\n"
"v_cvt_pk_bf16_f32 v91, v113, v115\n"
"v_fmamk_f32 v116, v116, 0x3e553b95, v170\n"
"v_mfma_f32_16x16x32_bf16 v[212:215], a[124:127], v[48:51], v[212:215]\n"
"s_waitcnt lgkmcnt(1)\n"
"ds_read_b128 v[112:115], v179 offset:27008\n"
"v_fmamk_f32 v117, v117, 0x3e553b95, v170\n"
"v_mfma_f32_16x16x32_bf16 v[204:207], a[128:131], v[48:51], v[204:207]\n"
"s_waitcnt lgkmcnt(1)\n"
"v_fmamk_f32 v118, v118, 0x3e553b95, v170\n"
"v_fmamk_f32 v119, v119, 0x3e553b95, v170\n"
"v_mfma_f32_16x16x32_bf16 v[244:247], v[244:247], v[152:155], 0\n"
"v_exp_f32 v116, v116\n"
"v_mfma_f32_16x16x32_bf16 v[224:227], a[132:135], v[52:55], v[224:227]\n"
"v_exp_f32 v117, v117\n"
"v_mfma_f32_16x16x32_bf16 v[212:215], a[136:139], v[52:55], v[212:215]\n"
"ds_read_b128 v[248:251], v180\n"
"v_exp_f32 v118, v118\n"
"v_mfma_f32_16x16x32_bf16 v[204:207], a[140:143], v[52:55], v[204:207]\n"
"s_add_u32 m0, s34, 1024\n"
"v_exp_f32 v119, v119\n"
"v_mfma_f32_16x16x32_bf16 v[228:231], a[144:147], v[32:35], 0\n"
"buffer_load_dwordx4 v190, s[8:11], s0 offen lds\n"
"v_cvt_pk_bf16_f32 v92, v116, v118\n"
"v_mfma_f32_16x16x32_bf16 v[228:231], a[156:159], v[36:39], v[228:231]\n"
"s_waitcnt lgkmcnt(1)\n"
"v_cvt_pk_bf16_f32 v93, v117, v119\n"
"v_bfi_b32 v81, v13, v81, v26\n"
"v_mfma_f32_16x16x32_bf16 v[228:231], a[168:171], v[40:43], v[228:231]\n"
"ds_read_b128 v[116:119], v181\n"
"ds_read_b64_tr_b16 a[216:217], v236\n"
"v_smfmac_f32_16x16x64_bf16 v[252:255], v[90:93], v[136:143], v66\n"
"v_cvt_pk_bf16_f32 v208, v208, v209\n"
"v_smfmac_f32_16x16x64_bf16 v[98:101], v[90:93], v[120:127], v66\n"
"ds_read_b128 v[136:139], v180 offset:13504\n"
"v_smfmac_f32_16x16x64_bf16 v[106:109], v[90:93], v[128:135], v66\n"
"s_waitcnt lgkmcnt(3)\n"
"ds_read_b128 v[120:123], v181 offset:13504\n"
"v_mfma_f32_16x16x32_bf16 v[90:93], v[172:175], v[90:93], v[82:85]\n"
"ds_read_b128 v[140:143], v180 offset:27008\n"
"v_cvt_pk_bf16_f32 v252, v252, v253\n"
"v_mfma_f32_16x16x32_bf16 v[112:115], v[112:115], v[152:155], 0\n"
"ds_read_b64 v[82:83], v178 offset:27072\n"
"v_cvt_pk_bf16_f32 v253, v254, v255\n"
"v_mfma_f32_16x16x32_bf16 v[248:251], v[248:251], v[152:155], 0\n"
"s_add_u32 m0, s34, 4096\n"
"ds_read_b64 v[84:85], v179 offset:27072\n"
"buffer_load_dwordx4 v191, s[8:11], s0 offen lds\n"
"v_mfma_f32_16x16x32_bf16 v[228:231], a[180:183], v[44:47], v[228:231]\n"
"s_add_u32 s0, s0, s33\n"
"ds_read_b64 v[92:93], v181 offset:64\n"
"v_cvt_pk_bf16_f32 v98, v98, v99\n"
"v_mfma_f32_16x16x32_bf16 v[228:231], a[192:195], v[48:51], v[228:231]\n"
"s_waitcnt lgkmcnt(7)\n"
"v_rcp_f32 v171, v90\n"
"v_mfma_f32_16x16x32_bf16 v[116:119], v[116:119], v[152:155], 0\n"
"ds_read_b64 v[90:91], v180 offset:64\n"
"v_cvt_pk_bf16_f32 v171, v171, v171\n"
"v_mfma_f32_16x16x32_bf16 v[228:231], a[204:207], v[52:55], v[228:231]\n"
"v_and_b32 v64, v171, v176\n"
"v_and_b32 v65, v171, v177\n"
"v_cvt_pk_bf16_f32 v99, v100, v101\n"
"v_cvt_pk_bf16_f32 v106, v106, v107\n"
"v_mfma_f32_16x16x16_bf16 v[252:255], v[252:253], v[64:65], 0\n"
"s_waitcnt lgkmcnt(5)\n"
"ds_read_b128 v[124:127], v181 offset:27008\n"
"v_cvt_pk_bf16_f32 v107, v108, v109\n"
"v_mfma_f32_16x16x16_bf16 v[98:101], v[98:99], v[64:65], 0\n"
"s_waitcnt lgkmcnt(5)\n"
"ds_read_b64_tr_b16 v[128:129], v182 offset:27136\n"
"ds_read_b64_tr_b16 v[130:131], v183 offset:27136\n"
"v_mfma_f32_16x16x16_bf16 v[106:109], v[106:107], v[64:65], 0\n"
"s_waitcnt lgkmcnt(6)\n"
"v_cvt_pk_bf16_f32 v252, v252, v253\n"
"v_cvt_pk_bf16_f32 v253, v254, v255\n"
"v_mfma_f32_16x16x32_bf16 v[244:247], v[80:83], v[164:167], v[244:247]\n"
"s_add_u32 m0, s34, 2048\n"
"v_cvt_pk_bf16_f32 v254, v98, v99\n"
"v_cvt_pk_bf16_f32 v255, v100, v101\n"
"v_mfma_f32_16x16x32_bf16 v[136:139], v[136:139], v[152:155], 0\n"
"s_waitcnt lgkmcnt(4)\n"
"buffer_load_dwordx4 v190, s[8:11], s1 offen lds\n"
"ds_read_b64 v[98:99], v180 offset:13568\n"
"v_mfma_f32_16x16x32_bf16 v[116:119], v[92:95], v[166:169], v[116:119]\n"
"s_waitcnt lgkmcnt(4)\n"
"ds_read_b64 v[100:101], v181 offset:13568\n"
"v_cvt_pk_bf16_f32 v106, v106, v107\n"
"v_mfma_f32_16x16x32_bf16 v[248:251], v[88:91], v[164:167], v[248:251]\n"
"v_cvt_pk_bf16_f32 v107, v108, v109\n"
"v_bfi_b32 v86, v14, v86, v26\n"
"v_mfma_f32_16x16x32_bf16 v[120:123], v[120:123], v[152:155], 0\n"
"buffer_store_dwordx4 v[252:255], v192, s[28:31], s37 offen sc1\n"
"v_mfma_f32_16x16x32_bf16 v[140:143], v[140:143], v[152:155], 0\n"
"s_waitcnt lgkmcnt(4)\n"
"ds_read_b64 v[108:109], v181 offset:27072\n"
"ds_read_b64_tr_b16 v[132:133], v182 offset:27904\n"
"v_mfma_f32_16x16x32_bf16 v[124:127], v[124:127], v[152:155], 0\n"
"v_cvt_pk_bf16_f32 v209, v210, v211\n"
"v_bfi_b32 v87, v15, v87, v26\n"
"v_cvt_pk_bf16_f32 v210, v216, v217\n"
"v_bfi_b32 v104, v16, v104, v26\n"
"v_mfma_f32_16x16x32_bf16 v[112:115], v[84:87], v[166:169], v[112:115]\n"
"s_add_u32 m0, s34, 5120\n"
"buffer_store_dwordx2 v[106:107], v193, s[28:31], s37 offen offset:1024 sc1\n"
"ds_read_b64 v[106:107], v180 offset:27072\n"
"ds_read_b64_tr_b16 v[134:135], v183 offset:27904\n"
"v_cvt_pk_bf16_f32 v211, v218, v219\n"
"v_mfma_f32_16x16x32_bf16 v[216:219], a[148:151], v[32:35], 0\n"
"s_waitcnt lgkmcnt(4)\n"
"ds_write_b128 v186, v[208:211] offset:6752\n"
"v_mfma_f32_16x16x32_bf16 v[136:139], v[96:99], v[164:167], v[136:139]\n"
"s_add_u32 s37, s37, 1572864\n"
"buffer_load_dwordx4 v191, s[8:11], s1 offen lds\n"
"v_bfi_b32 v105, v17, v105, v26\n"
"v_mfma_f32_16x16x32_bf16 v[120:123], v[100:103], v[166:169], v[120:123]\n"
"s_add_u32 s1, s1, s33\n"
"v_cvt_pk_bf16_f32 v220, v220, v221\n"
"v_bfi_b32 v110, v18, v110, v26\n"
"v_mfma_f32_16x16x32_bf16 v[208:211], a[152:155], v[32:35], 0\n"
"s_waitcnt vmcnt(19)\n"
"ds_read_b128 v[32:35], v188 offset:6144\n"
"v_cvt_pk_bf16_f32 v221, v222, v223\n"
"v_mfma_f32_16x16x32_bf16 v[216:219], a[160:163], v[36:39], v[216:219]\n"
"v_bfi_b32 v111, v19, v111, v26\n"
"v_max_f32 v244, v244, v112\n"
"v_mfma_f32_16x16x32_bf16 v[208:211], a[164:167], v[36:39], v[208:211]\n"
"s_waitcnt lgkmcnt(5)\n"
"ds_read_b128 v[36:39], v189 offset:6144\n"
"ds_read_b64_tr_b16 a[218:219], v236 offset:6144\n"
"v_mfma_f32_16x16x32_bf16 v[124:127], v[108:111], v[166:169], v[124:127]\n"
"s_waitcnt lgkmcnt(5)\n"
"buffer_load_dword v237, v67, s[8:11], s78 offen\n"
"v_max_f32 v245, v245, v113\n"
"v_mfma_f32_16x16x32_bf16 v[140:143], v[104:107], v[164:167], v[140:143]\n"
"s_add_u32 s78, s78, s33\n"
"ds_read_b64_tr_b16 v[112:113], v182 offset:27144\n"
"v_max_f32 v246, v246, v114\n"
"v_mfma_f32_16x16x32_bf16 v[216:219], a[172:175], v[40:43], v[216:219]\n"
"ds_read_b64_tr_b16 a[220:221], v236 offset:8\n"
"v_max_f32 v247, v247, v115\n"
"v_mfma_f32_16x16x32_bf16 v[208:211], a[176:179], v[40:43], v[208:211]\n"
"s_waitcnt vmcnt(15)\n"
"v_max3_f32 v170, v244, v245, v246\n"
"v_max_f32 v248, v248, v116\n"
"v_mfma_f32_16x16x32_bf16 v[216:219], a[184:187], v[44:47], v[216:219]\n"
"ds_read_b128 v[40:43], v188 offset:7168\n"
"v_max_f32 v249, v249, v117\n"
"v_mfma_f32_16x16x32_bf16 v[208:211], a[188:191], v[44:47], v[208:211]\n"
"ds_read_b128 v[44:47], v189 offset:7168\n"
"v_max_f32 v250, v250, v118\n"
"v_mfma_f32_16x16x32_bf16 v[216:219], a[196:199], v[48:51], v[216:219]\n"
"ds_read_b64_tr_b16 v[114:115], v183 offset:27144\n"
"v_max_f32 v251, v251, v119\n"
"v_mfma_f32_16x16x32_bf16 v[208:211], a[200:203], v[48:51], v[208:211]\n"
"s_waitcnt lgkmcnt(6)\n"
"ds_read_b64_tr_b16 v[116:117], v182 offset:27912\n"
"v_max3_f32 v170, v170, v247, v248\n"
"v_mfma_f32_16x16x32_bf16 v[216:219], a[208:211], v[52:55], v[216:219]\n"
"s_add_u32 m0, s34, 6144\n"
"ds_read_b64_tr_b16 v[118:119], v183 offset:27912\n"
"buffer_load_dwordx4 v190, s[8:11], s32 offen lds\n"
"v_mfma_f32_16x16x32_bf16 v[208:211], a[212:215], v[52:55], v[208:211]\n"
"v_max3_f32 v170, v170, v249, v250\n"
"v_cvt_pk_bf16_f32 v224, v224, v225\n"
"v_cvt_pk_bf16_f32 v225, v226, v227\n"
"v_cvt_pk_bf16_f32 v226, v212, v213\n"
"v_max_f32 v136, v136, v120\n"
"v_cvt_pk_bf16_f32 v227, v214, v215\n"
"v_mfma_f32_16x16x32_bf16 v[212:215], a[0:3], v[32:35], 0\n"
"ds_write_b128 v186, v[224:227] offset:6880\n"
"v_mfma_f32_16x16x32_bf16 v[212:215], a[12:15], v[36:39], v[212:215]\n"
"s_waitcnt vmcnt(14) lgkmcnt(5)\n"
"v_max_f32 v137, v137, v121\n"
"v_cvt_pk_bf16_f32 v222, v204, v205\n"
"v_mfma_f32_16x16x32_bf16 v[212:215], a[24:27], v[40:43], v[212:215]\n"
"s_waitcnt lgkmcnt(4)\n"
"v_max_f32 v138, v138, v122\n"
"v_max_f32 v139, v139, v123\n"
"v_mfma_f32_16x16x32_bf16 v[212:215], a[36:39], v[44:47], v[212:215]\n"
"ds_read_b128 v[48:51], v188 offset:8192\n"
"ds_read_b128 v[52:55], v189 offset:8192\n"
"v_mfma_f32_16x16x32_bf16 v[224:227], a[72:75], v[32:35], 0\n"
"s_add_u32 m0, s34, 9216\n"
"ds_read_b64_tr_b16 v[120:121], v182 offset:27080\n"
"buffer_load_dwordx4 v191, s[8:11], s32 offen lds\n"
"v_mfma_f32_16x16x32_bf16 v[224:227], a[84:87], v[36:39], v[224:227]\n"
"s_add_u32 s32, s32, s33\n"
"ds_read_b64_tr_b16 v[122:123], v183 offset:27080\n"
"v_cvt_pk_bf16_f32 v223, v206, v207\n"
"v_mfma_f32_16x16x32_bf16 v[204:207], a[4:7], v[32:35], 0\n"
"ds_write_b128 v186, v[220:223] offset:6816\n"
"v_mfma_f32_16x16x32_bf16 v[220:223], a[8:11], v[32:35], 0\n"
"v_max3_f32 v170, v170, v251, v136\n"
"v_max3_f32 v170, v170, v137, v138\n"
"v_mfma_f32_16x16x32_bf16 v[204:207], a[16:19], v[36:39], v[204:207]\n"
"v_max_f32 v140, v140, v124\n"
"v_max_f32 v141, v141, v125\n"
"v_mfma_f32_16x16x32_bf16 v[220:223], a[20:23], v[36:39], v[220:223]\n"
"ds_read_b64_tr_b16 v[124:125], v182 offset:27848\n"
"ds_read_b64_tr_b16 a[222:223], v236 offset:6152\n"
"v_mfma_f32_16x16x32_bf16 v[204:207], a[28:31], v[40:43], v[204:207]\n"
"s_waitcnt vmcnt(13) lgkmcnt(6)\n"
"v_max_f32 v142, v142, v126\n"
"v_max_f32 v143, v143, v127\n"
"v_mfma_f32_16x16x32_bf16 v[220:223], a[32:35], v[40:43], v[220:223]\n"
"s_waitcnt lgkmcnt(5)\n"
"v_readfirstlane_b32 s6, v201\n"
"v_max3_f32 v170, v170, v139, v140\n"
"v_mfma_f32_16x16x32_bf16 v[204:207], a[40:43], v[44:47], v[204:207]\n"
"s_and_b32 s6, s6, 0xffffff00\n"
"ds_read_b64_tr_b16 v[126:127], v183 offset:27848\n"
"ds_read_b64_tr_b16 a[224:225], v235\n"
"v_mfma_f32_16x16x32_bf16 v[220:223], a[44:47], v[44:47], v[220:223]\n"
"s_add_u32 m0, s34, 7168\n"
"v_max3_f32 v170, v170, v141, v142\n"
"v_mov_b64 v[252:253], 0\n"
"v_mfma_f32_16x16x32_bf16 v[212:215], a[48:51], v[48:51], v[212:215]\n"
"buffer_load_dwordx4 v190, s[8:11], s0 offen lds\n"
"ds_read_b64_tr_b16 a[226:227], v235 offset:6144\n"
"v_mfma_f32_16x16x32_bf16 v[204:207], a[52:55], v[48:51], v[204:207]\n"
"v_mov_b64 v[254:255], 0\n"
"v_mov_b64 v[98:99], 0\n"
"v_mfma_f32_16x16x32_bf16 v[220:223], a[56:59], v[48:51], v[220:223]\n"
"v_mov_b64 v[100:101], 0\n"
"v_mov_b64 v[106:107], 0\n"
"v_mfma_f32_16x16x32_bf16 v[212:215], a[60:63], v[52:55], v[212:215]\n"
"v_mov_b64 v[108:109], 0\n"
"v_max_f32 v171, v170, v143\n"
"v_mfma_f32_16x16x32_bf16 v[204:207], a[64:67], v[52:55], v[204:207]\n"
"v_max_f32 v170, v170, v143\n"
"v_cvt_pk_bf16_f32 v152, v228, v229\n"
"v_mfma_f32_16x16x32_bf16 v[220:223], a[68:71], v[52:55], v[220:223]\n"
"v_permlane16_swap_b32 v170, v171\n"
"v_mfma_f32_16x16x32_bf16 v[224:227], a[96:99], v[40:43], v[224:227]\n"
"v_max_f32 v170, v170, v171\n"
"v_mov_b32 v171, v170\n"
"v_mfma_f32_16x16x32_bf16 v[224:227], a[108:111], v[44:47], v[224:227]\n"
"s_nop 0\n"
"v_permlane32_swap_b32 v170, v171\n"
"v_mfma_f32_16x16x32_bf16 v[224:227], a[120:123], v[48:51], v[224:227]\n"
"s_add_u32 m0, s34, 10240\n"
"v_max_f32 v170, v170, v171\n"
"v_mul_f32 v170, 0xbe553b95, v170\n"
"v_mfma_f32_16x16x32_bf16 v[224:227], a[132:135], v[52:55], v[224:227]\n"
"s_waitcnt vmcnt(11)\n"
"buffer_load_dwordx4 v191, s[8:11], s0 offen lds\n"
"s_add_u32 s0, s0, s33\n"
"v_fmamk_f32 v244, v244, 0x3e553b95, v170\n"
"v_fmamk_f32 v245, v245, 0x3e553b95, v170\n"
"v_fmamk_f32 v246, v246, 0x3e553b95, v170\n"
"v_fmamk_f32 v247, v247, 0x3e553b95, v170\n"
"v_exp_f32 v244, v244\n"
"v_exp_f32 v245, v245\n"
"v_exp_f32 v246, v246\n"
"v_exp_f32 v247, v247\n"
"v_cvt_pk_bf16_f32 v82, v244, v246\n"
"v_cvt_pk_bf16_f32 v83, v245, v247\n"
"v_fmamk_f32 v248, v248, 0x3e553b95, v170\n"
"v_fmamk_f32 v249, v249, 0x3e553b95, v170\n"
"v_fmamk_f32 v250, v250, 0x3e553b95, v170\n"
"v_fmamk_f32 v251, v251, 0x3e553b95, v170\n"
"v_exp_f32 v248, v248\n"
"v_exp_f32 v249, v249\n"
"v_exp_f32 v250, v250\n"
"v_exp_f32 v251, v251\n"
"s_add_u32 m0, s34, 8192\n"
"v_cvt_pk_bf16_f32 v84, v248, v250\n"
"buffer_load_dwordx4 v190, s[8:11], s1 offen lds\n"
"v_cvt_pk_bf16_f32 v85, v249, v251\n"
"v_cvt_pk_bf16_f32 v153, v230, v231\n"
"v_mfma_f32_16x16x32_bf16 v[228:231], a[76:79], v[32:35], 0\n"
"s_waitcnt lgkmcnt(0)\n"
"ds_read_b64_tr_b16 v[244:245], v182 offset:54144\n"
"ds_read_b64_tr_b16 v[246:247], v183 offset:54144\n"
"v_smfmac_f32_16x16x64_bf16 v[252:255], v[82:85], v[128:135], v66\n"
"s_barrier\n"
"v_smfmac_f32_16x16x64_bf16 v[98:101], v[82:85], v[112:119], v66\n"
"ds_read_b64_tr_b16 v[248:249], v182 offset:54912\n"
"v_smfmac_f32_16x16x64_bf16 v[106:109], v[82:85], v[120:127], v66\n"
"ds_read_b64_tr_b16 v[250:251], v183 offset:54912\n"
"v_mfma_f32_16x16x32_bf16 v[82:85], v[172:175], v[82:85], 0\n"
"ds_read_b64_tr_b16 v[128:129], v182 offset:54152\n"
"ds_read_b64_tr_b16 v[130:131], v183 offset:54152\n"
"v_mfma_f32_16x16x32_bf16 v[228:231], a[88:91], v[36:39], v[228:231]\n"
"ds_read_b64_tr_b16 v[132:133], v182 offset:54920\n"
"v_cvt_pk_bf16_f32 v154, v216, v217\n"
"v_mfma_f32_16x16x32_bf16 v[228:231], a[100:103], v[40:43], v[228:231]\n"
"s_add_u32 m0, s79, 130176\n"
"ds_read_b64_tr_b16 v[134:135], v183 offset:54920\n"
"buffer_load_dwordx4 v192, s[16:19], s43 offen lds\n"
"v_mfma_f32_16x16x32_bf16 v[228:231], a[112:115], v[44:47], v[228:231]\n"
"ds_read_b64_tr_b16 v[112:113], v182 offset:54088\n"
"v_cvt_pk_bf16_f32 v155, v218, v219\n"
"v_mfma_f32_16x16x32_bf16 v[216:219], a[80:83], v[32:35], 0\n"
"ds_read_b64_tr_b16 v[114:115], v183 offset:54088\n"
"v_cvt_pk_bf16_f32 v166, v208, v209\n"
"v_mfma_f32_16x16x32_bf16 v[216:219], a[92:95], v[36:39], v[216:219]\n"
"ds_read_b64_tr_b16 v[116:117], v182 offset:54856\n"
"v_cvt_pk_bf16_f32 v167, v210, v211\n"
"v_mfma_f32_16x16x32_bf16 v[216:219], a[104:107], v[40:43], v[216:219]\n"
"ds_read_b64_tr_b16 v[118:119], v183 offset:54856\n"
"v_fmamk_f32 v136, v136, 0x3e553b95, v170\n"
"v_mfma_f32_16x16x32_bf16 v[216:219], a[116:119], v[44:47], v[216:219]\n"
"v_fmamk_f32 v137, v137, 0x3e553b95, v170\n"
"v_fmamk_f32 v138, v138, 0x3e553b95, v170\n"
"v_mfma_f32_16x16x32_bf16 v[228:231], a[124:127], v[48:51], v[228:231]\n"
"v_fmamk_f32 v139, v139, 0x3e553b95, v170\n"
"v_fmamk_f32 v140, v140, 0x3e553b95, v170\n"
"v_mfma_f32_16x16x32_bf16 v[216:219], a[128:131], v[48:51], v[216:219]\n"
"ds_read_b128 v[120:123], v180\n"
"v_fmamk_f32 v141, v141, 0x3e553b95, v170\n"
"v_mfma_f32_16x16x32_bf16 v[228:231], a[136:139], v[52:55], v[228:231]\n"
"s_add_u32 m0, s79, 130176\n"
"v_exp_f32 v136, v136\n"
"v_mfma_f32_16x16x32_bf16 v[216:219], a[140:143], v[52:55], v[216:219]\n"
"buffer_load_dwordx4 v192, s[16:19], s43 offen offset:1024 lds\n"
"ds_read_b128 v[124:127], v180 offset:13504\n"
"v_mfma_f32_16x16x32_bf16 v[208:211], a[144:147], v[32:35], 0\n"
"v_exp_f32 v137, v137\n"
"v_mfma_f32_16x16x32_bf16 v[208:211], a[156:159], v[36:39], v[208:211]\n"
"v_exp_f32 v138, v138\n"
"v_mfma_f32_16x16x32_bf16 v[208:211], a[168:171], v[40:43], v[208:211]\n"
"v_exp_f32 v139, v139\n"
"v_mfma_f32_16x16x32_bf16 v[208:211], a[180:183], v[44:47], v[208:211]\n"
"v_cvt_pk_bf16_f32 v90, v136, v138\n"
"v_cvt_pk_bf16_f32 v91, v137, v139\n"
"v_mfma_f32_16x16x32_bf16 v[208:211], a[192:195], v[48:51], v[208:211]\n"
"ds_read_b128 v[136:139], v181\n"
"v_fmamk_f32 v142, v142, 0x3e553b95, v170\n"
"v_mfma_f32_16x16x32_bf16 v[208:211], a[204:207], v[52:55], v[208:211]\n"
"s_waitcnt lgkmcnt(2)\n"
"v_fmamk_f32 v143, v143, 0x3e553b95, v170\n"
"v_exp_f32 v140, v140\n"
"v_mfma_f32_16x16x32_bf16 v[120:123], v[120:123], v[144:147], 0\n"
"s_add_u32 m0, s79, 130176\n"
"v_exp_f32 v141, v141\n"
"v_exp_f32 v142, v142\n"
"v_exp_f32 v143, v143\n"
"v_cvt_pk_bf16_f32 v92, v140, v142\n"
"v_cvt_pk_bf16_f32 v93, v141, v143\n"
"s_mov_b64 exec, s[44:45]\n"
"v_add_u32 v179, 0xc0, v179\n"
"s_mov_b64 exec, -1\n"
"v_smfmac_f32_16x16x64_bf16 v[252:255], v[90:93], v[244:251], v66\n"
"s_waitcnt lgkmcnt(1)\n"
"buffer_load_dwordx4 v192, s[16:19], s43 offen offset:2048 lds\n"
"v_smfmac_f32_16x16x64_bf16 v[98:101], v[90:93], v[128:135], v66\n"
"s_add_u32 s43, s79, 55296\n"
"ds_read_b128 v[140:143], v181 offset:13504\n"
"v_smfmac_f32_16x16x64_bf16 v[106:109], v[90:93], v[112:119], v66\n"
"s_waitcnt lgkmcnt(1)\n"
"ds_read_b128 v[244:247], v180 offset:27008\n"
"v_mfma_f32_16x16x32_bf16 v[90:93], v[172:175], v[90:93], v[82:85]\n"
"ds_read_b128 v[128:131], v181 offset:27008\n"
"ds_read_b128 v[248:251], v178\n"
"v_mfma_f32_16x16x32_bf16 v[136:139], v[136:139], v[144:147], 0\n"
"ds_read_b64 v[82:83], v180 offset:64\n"
"v_cvt_pk_bf16_f32 v252, v252, v253\n"
"v_mfma_f32_16x16x32_bf16 v[124:127], v[124:127], v[144:147], 0\n"
"v_cvt_pk_bf16_f32 v253, v254, v255\n"
"s_nop 0\n"
"v_rcp_f32 v171, v90\n"
"v_cvt_pk_bf16_f32 v98, v98, v99\n"
"v_cvt_pk_bf16_f32 v171, v171, v171\n"
"v_and_b32 v64, v171, v176\n"
"v_and_b32 v65, v171, v177\n"
"v_cvt_pk_bf16_f32 v99, v100, v101\n"
"v_cvt_pk_bf16_f32 v106, v106, v107\n"
"v_mfma_f32_16x16x16_bf16 v[252:255], v[252:253], v[64:65], 0\n"
"s_add_u32 m0, s34, 11264\n"
"ds_read_b64 v[84:85], v181 offset:64\n"
"buffer_load_dwordx4 v191, s[8:11], s1 offen lds\n"
"v_mfma_f32_16x16x16_bf16 v[98:101], v[98:99], v[64:65], 0\n"
"s_waitcnt lgkmcnt(5)\n"
"ds_read_b64 v[90:91], v180 offset:13568\n"
"ds_read_b64 v[92:93], v181 offset:13568\n"
"v_mfma_f32_16x16x32_bf16 v[140:143], v[140:143], v[144:147], 0\n"
"s_waitcnt lgkmcnt(6)\n"
"ds_read_b128 v[132:135], v179\n"
"v_cvt_pk_bf16_f32 v107, v108, v109\n"
"v_mfma_f32_16x16x32_bf16 v[244:247], v[244:247], v[144:147], 0\n"
"s_waitcnt lgkmcnt(6)\n"
"v_cvt_pk_bf16_f32 v252, v252, v253\n"
"v_cvt_pk_bf16_f32 v253, v254, v255\n"
"v_mfma_f32_16x16x16_bf16 v[106:109], v[106:107], v[64:65], 0\n"
"s_waitcnt lgkmcnt(5)\n"
"v_cvt_pk_bf16_f32 v254, v98, v99\n"
"v_cvt_pk_bf16_f32 v255, v100, v101\n"
"v_mfma_f32_16x16x32_bf16 v[128:131], v[128:131], v[144:147], 0\n"
"s_waitcnt lgkmcnt(4)\n"
"buffer_store_dwordx4 v[252:255], v192, s[28:31], s37 offen sc1\n"
"v_mfma_f32_16x16x32_bf16 v[120:123], v[80:83], v[156:159], v[120:123]\n"
"s_waitcnt vmcnt(12)\n"
"v_or_b32 v201, 2, s6\n"
"v_cvt_pk_bf16_f32 v106, v106, v107\n"
"v_mfma_f32_16x16x32_bf16 v[248:251], v[248:251], v[144:147], 0\n"
"s_waitcnt lgkmcnt(3)\n"
"ds_read_b64 v[98:99], v180 offset:27072\n"
"s_mov_b64 exec, 1\n"
"buffer_store_dword v201, off, s[28:31], s40 sc1\n"
"s_mov_b64 exec, -1\n"
"v_mfma_f32_16x16x32_bf16 v[136:139], v[84:87], v[158:161], v[136:139]\n"
"s_waitcnt lgkmcnt(2)\n"
"ds_read_b64 v[100:101], v181 offset:27072\n"
"ds_read_b64_tr_b16 a[228:229], v236 offset:12288\n"
"v_mfma_f32_16x16x32_bf16 v[124:127], v[88:91], v[156:159], v[124:127]\n"
"s_waitcnt lgkmcnt(3)\n"
"v_cvt_pk_bf16_f32 v107, v108, v109\n"
"v_cvt_pk_bf16_f32 v212, v212, v213\n"
"v_mfma_f32_16x16x32_bf16 v[140:143], v[92:95], v[158:161], v[140:143]\n"
"s_add_u32 s1, s1, s33\n"
"buffer_store_dwordx2 v[106:107], v193, s[28:31], s37 offen offset:1024 sc1\n"
"v_mfma_f32_16x16x32_bf16 v[132:135], v[132:135], v[144:147], 0\n"
"s_add_u32 s37, s37, 1572864\n"
"v_cvt_pk_bf16_f32 v213, v214, v215\n"
"v_cvt_pk_bf16_f32 v214, v204, v205\n"
"v_cvt_pk_bf16_f32 v215, v206, v207\n"
"v_mfma_f32_16x16x32_bf16 v[204:207], a[148:151], v[32:35], 0\n"
"ds_write_b128 v186, v[212:215] offset:20256\n"
"v_mfma_f32_16x16x32_bf16 v[212:215], a[152:155], v[32:35], 0\n"
"s_waitcnt lgkmcnt(3)\n"
"v_cvt_pk_bf16_f32 v220, v220, v221\n"
"v_cvt_pk_bf16_f32 v221, v222, v223\n"
"v_mfma_f32_16x16x32_bf16 v[244:247], v[96:99], v[156:159], v[244:247]\n"
"s_waitcnt lgkmcnt(2)\n"
"buffer_load_dword v237, v67, s[8:11], s78 offen\n"
"ds_read_b128 v[32:35], v188\n"
"v_mfma_f32_16x16x32_bf16 v[204:207], a[160:163], v[36:39], v[204:207]\n"
"s_add_u32 s78, s78, s33\n"
"ds_read_b64 v[106:107], v178 offset:64\n"
"ds_read_b64 v[108:109], v179 offset:64\n"
"v_mfma_f32_16x16x32_bf16 v[128:131], v[100:103], v[158:161], v[128:131]\n"
"ds_read_b64_tr_b16 v[112:113], v182 offset:40640\n"
"v_max_f32 v120, v120, v136\n"
"v_mfma_f32_16x16x32_bf16 v[212:215], a[164:167], v[36:39], v[212:215]\n"
"ds_read_b128 v[36:39], v189\n"
"v_max_f32 v121, v121, v137\n"
"v_mfma_f32_16x16x32_bf16 v[204:207], a[172:175], v[40:43], v[204:207]\n"
"ds_read_b64_tr_b16 v[114:115], v183 offset:40640\n"
"v_max_f32 v122, v122, v138\n"
"v_mfma_f32_16x16x32_bf16 v[212:215], a[176:179], v[40:43], v[212:215]\n"
"ds_read_b128 v[40:43], v188 offset:1024\n"
"v_max_f32 v123, v123, v139\n"
"v_mfma_f32_16x16x32_bf16 v[204:207], a[184:187], v[44:47], v[204:207]\n"
"ds_read_b64_tr_b16 v[116:117], v182 offset:41408\n"
"v_max3_f32 v170, v120, v121, v122\n"
"v_mfma_f32_16x16x32_bf16 v[212:215], a[188:191], v[44:47], v[212:215]\n"
"ds_read_b128 v[44:47], v189 offset:1024\n"
"v_max_f32 v124, v124, v140\n"
"v_mfma_f32_16x16x32_bf16 v[204:207], a[196:199], v[48:51], v[204:207]\n"
"ds_read_b64_tr_b16 v[118:119], v183 offset:41408\n"
"v_max_f32 v125, v125, v141\n"
"v_mfma_f32_16x16x32_bf16 v[212:215], a[200:203], v[48:51], v[212:215]\n"
"s_waitcnt vmcnt(14) lgkmcnt(8)\n"
"ds_read_b128 v[48:51], v188 offset:2048\n"
"v_max_f32 v126, v126, v142\n"
"v_mfma_f32_16x16x32_bf16 v[248:251], v[104:107], v[156:159], v[248:251]\n"
"s_waitcnt lgkmcnt(8)\n"
"ds_read_b64_tr_b16 v[136:137], v182 offset:40648\n"
"v_max_f32 v127, v127, v143\n"
"v_mfma_f32_16x16x32_bf16 v[132:135], v[108:111], v[158:161], v[132:135]\n"
"ds_read_b64_tr_b16 v[138:139], v183 offset:40648\n"
"v_max3_f32 v170, v170, v123, v124\n"
"v_mfma_f32_16x16x32_bf16 v[204:207], a[208:211], v[52:55], v[204:207]\n"
"s_waitcnt lgkmcnt(8)\n"
"ds_read_b64_tr_b16 v[140:141], v182 offset:41416\n"
"v_max3_f32 v170, v170, v125, v126\n"
"v_mfma_f32_16x16x32_bf16 v[212:215], a[212:215], v[52:55], v[212:215]\n"
"s_add_u32 m0, s34, 0\n"
"ds_read_b128 v[52:55], v189 offset:2048\n"
"buffer_load_dwordx4 v190, s[8:11], s32 offen lds\n"
"v_max_f32 v244, v244, v128\n"
"v_max_f32 v245, v245, v129\n"
"v_cvt_pk_bf16_f32 v224, v224, v225\n"
"v_max_f32 v246, v246, v130\n"
"v_max_f32 v247, v247, v131\n"
"v_cvt_pk_bf16_f32 v225, v226, v227\n"
"v_max3_f32 v170, v170, v127, v244\n"
"v_cvt_pk_bf16_f32 v226, v228, v229\n"
"v_max3_f32 v170, v170, v245, v246\n"
"v_cvt_pk_bf16_f32 v227, v230, v231\n"
"v_mfma_f32_16x16x32_bf16 v[228:231], a[0:3], v[32:35], 0\n"
"ds_read_b64_tr_b16 v[142:143], v183 offset:41416\n"
"ds_read_b64_tr_b16 a[230:231], v236 offset:18432\n"
"v_mfma_f32_16x16x32_bf16 v[228:231], a[12:15], v[36:39], v[228:231]\n"
"s_waitcnt lgkmcnt(10)\n"
"v_max_f32 v248, v248, v132\n"
"v_max_f32 v249, v249, v133\n"
"v_mfma_f32_16x16x32_bf16 v[228:231], a[24:27], v[40:43], v[228:231]\n"
"s_waitcnt lgkmcnt(8)\n"
"ds_read_b64_tr_b16 v[128:129], v182 offset:40584\n"
"v_cvt_pk_bf16_f32 v222, v216, v217\n"
"v_mfma_f32_16x16x32_bf16 v[228:231], a[36:39], v[44:47], v[228:231]\n"
"s_waitcnt lgkmcnt(7)\n"
"ds_read_b64_tr_b16 v[130:131], v183 offset:40584\n"
"v_max_f32 v250, v250, v134\n"
"v_mfma_f32_16x16x32_bf16 v[228:231], a[48:51], v[48:51], v[228:231]\n"
"s_add_u32 m0, s34, 3072\n"
"ds_read_b64_tr_b16 v[132:133], v182 offset:41352\n"
"buffer_load_dwordx4 v191, s[8:11], s32 offen lds\n"
"ds_write_b128 v186, v[224:227] offset:20384\n"
"v_mfma_f32_16x16x32_bf16 v[224:227], a[72:75], v[32:35], 0\n"
"s_waitcnt lgkmcnt(6)\n"
"v_cvt_pk_bf16_f32 v223, v218, v219\n"
"v_max_f32 v251, v251, v135\n"
"v_mfma_f32_16x16x32_bf16 v[216:219], a[4:7], v[32:35], 0\n"
"s_add_u32 s32, s32, s33\n"
"v_max3_f32 v170, v170, v247, v248\n"
".Lic2%=:\n"
"s_cbranch_execz .Lib0%=\n"
"v_max3_f32 v170, v170, v249, v250\n"
"v_mfma_f32_16x16x32_bf16 v[216:219], a[16:19], v[36:39], v[216:219]\n"
"ds_write_b128 v186, v[220:223] offset:20320\n"
"v_mfma_f32_16x16x32_bf16 v[220:223], a[8:11], v[32:35], 0\n"
"v_mov_b64 v[252:253], 0\n"
"v_mov_b64 v[254:255], 0\n"
"v_mfma_f32_16x16x32_bf16 v[220:223], a[20:23], v[36:39], v[220:223]\n"
"v_mov_b64 v[98:99], 0\n"
"v_mov_b64 v[100:101], 0\n"
"v_mfma_f32_16x16x32_bf16 v[216:219], a[28:31], v[40:43], v[216:219]\n"
"s_add_u32 m0, s34, 1024\n"
"ds_read_b64_tr_b16 v[134:135], v183 offset:41352\n"
"buffer_load_dwordx4 v190, s[8:11], s0 offen lds\n"
"v_mfma_f32_16x16x32_bf16 v[220:223], a[32:35], v[40:43], v[220:223]\n"
"ds_read_b64_tr_b16 a[232:233], v236 offset:12296\n"
"v_mov_b64 v[106:107], 0\n"
"v_mfma_f32_16x16x32_bf16 v[216:219], a[40:43], v[44:47], v[216:219]\n"
"ds_read_b64_tr_b16 a[234:235], v236 offset:18440\n"
"v_mov_b64 v[108:109], 0\n"
"v_mfma_f32_16x16x32_bf16 v[220:223], a[44:47], v[44:47], v[220:223]\n"
"v_max_f32 v171, v170, v251\n"
"v_max_f32 v170, v170, v251\n"
"v_mfma_f32_16x16x32_bf16 v[216:219], a[52:55], v[48:51], v[216:219]\n"
"ds_read_b64_tr_b16 a[236:237], v235 offset:12288\n"
"v_permlane16_swap_b32 v170, v171\n"
"v_mfma_f32_16x16x32_bf16 v[220:223], a[56:59], v[48:51], v[220:223]\n"
"v_max_f32 v170, v170, v171\n"
"v_mov_b32 v171, v170\n"
"v_mfma_f32_16x16x32_bf16 v[228:231], a[60:63], v[52:55], v[228:231]\n"
"ds_read_b64_tr_b16 a[238:239], v235 offset:18432\n"
"v_permlane32_swap_b32 v170, v171\n"
"v_mfma_f32_16x16x32_bf16 v[216:219], a[64:67], v[52:55], v[216:219]\n"
"s_add_u32 m0, s34, 4096\n"
"v_max_f32 v170, v170, v171\n"
"v_mul_f32 v170, 0xbe553b95, v170\n"
"v_mfma_f32_16x16x32_bf16 v[220:223], a[68:71], v[52:55], v[220:223]\n"
"buffer_load_dwordx4 v191, s[8:11], s0 offen lds\n"
"v_fmamk_f32 v120, v120, 0x3e553b95, v170\n"
"v_mfma_f32_16x16x32_bf16 v[224:227], a[84:87], v[36:39], v[224:227]\n"
"s_add_u32 s0, s0, s33\n"
"v_fmamk_f32 v121, v121, 0x3e553b95, v170\n"
"v_fmamk_f32 v122, v122, 0x3e553b95, v170\n"
"v_mfma_f32_16x16x32_bf16 v[224:227], a[96:99], v[40:43], v[224:227]\n"
"v_fmamk_f32 v123, v123, 0x3e553b95, v170\n"
"v_fmamk_f32 v124, v124, 0x3e553b95, v170\n"
"v_mfma_f32_16x16x32_bf16 v[224:227], a[108:111], v[44:47], v[224:227]\n"
"v_exp_f32 v120, v120\n"
"v_mfma_f32_16x16x32_bf16 v[224:227], a[120:123], v[48:51], v[224:227]\n"
"v_exp_f32 v121, v121\n"
"v_mfma_f32_16x16x32_bf16 v[224:227], a[132:135], v[52:55], v[224:227]\n"
"v_exp_f32 v122, v122\n"
"v_exp_f32 v123, v123\n"
"v_cvt_pk_bf16_f32 v82, v120, v122\n"
"v_cvt_pk_bf16_f32 v83, v121, v123\n"
"v_fmamk_f32 v125, v125, 0x3e553b95, v170\n"
"v_fmamk_f32 v126, v126, 0x3e553b95, v170\n"
"s_add_u32 m0, s34, 2048\n"
"v_fmamk_f32 v127, v127, 0x3e553b95, v170\n"
"buffer_load_dwordx4 v190, s[8:11], s1 offen lds\n"
"v_exp_f32 v124, v124\n"
"v_exp_f32 v125, v125\n"
"v_exp_f32 v126, v126\n"
"v_exp_f32 v127, v127\n"
"v_cvt_pk_bf16_f32 v84, v124, v126\n"
"v_cvt_pk_bf16_f32 v85, v125, v127\n"
"v_cvt_pk_bf16_f32 v144, v208, v209\n"
"v_cvt_pk_bf16_f32 v145, v210, v211\n"
"v_smfmac_f32_16x16x64_bf16 v[252:255], v[82:85], v[112:119], v66\n"
"s_waitcnt vmcnt(10) lgkmcnt(0)\n"
"ds_read_b64_tr_b16 v[120:121], v184 offset:128\n"
"v_smfmac_f32_16x16x64_bf16 v[98:101], v[82:85], v[136:143], v66\n"
"s_barrier\n"
"v_smfmac_f32_16x16x64_bf16 v[106:109], v[82:85], v[128:135], v66\n"
"ds_read_b64_tr_b16 v[122:123], v185 offset:128\n"
"v_mfma_f32_16x16x32_bf16 v[82:85], v[172:175], v[82:85], 0\n"
"ds_read_b64_tr_b16 v[124:125], v184 offset:896\n"
"ds_read_b64_tr_b16 v[126:127], v185 offset:896\n"
"v_mfma_f32_16x16x32_bf16 v[208:211], a[76:79], v[32:35], 0\n"
"s_add_u32 m0, s79, 142464\n"
"ds_read_b64_tr_b16 v[112:113], v184 offset:136\n"
"buffer_load_dwordx4 v192, s[16:19], s43 offen lds\n"
"v_mfma_f32_16x16x32_bf16 v[208:211], a[88:91], v[36:39], v[208:211]\n"
"ds_read_b64_tr_b16 v[114:115], v185 offset:136\n"
"v_cvt_pk_bf16_f32 v146, v204, v205\n"
"v_mfma_f32_16x16x32_bf16 v[208:211], a[100:103], v[40:43], v[208:211]\n"
"ds_read_b64_tr_b16 v[116:117], v184 offset:904\n"
"v_cvt_pk_bf16_f32 v147, v206, v207\n"
"v_mfma_f32_16x16x32_bf16 v[204:207], a[80:83], v[32:35], 0\n"
"ds_read_b64_tr_b16 v[118:119], v185 offset:904\n"
"v_fmamk_f32 v244, v244, 0x3e553b95, v170\n"
"v_mfma_f32_16x16x32_bf16 v[204:207], a[92:95], v[36:39], v[204:207]\n"
"ds_read_b64_tr_b16 v[136:137], v184 offset:72\n"
"v_cvt_pk_bf16_f32 v158, v212, v213\n"
"v_mfma_f32_16x16x32_bf16 v[204:207], a[104:107], v[40:43], v[204:207]\n"
"ds_read_b64_tr_b16 v[138:139], v185 offset:72\n"
"v_fmamk_f32 v245, v245, 0x3e553b95, v170\n"
"v_mfma_f32_16x16x32_bf16 v[208:211], a[112:115], v[44:47], v[208:211]\n"
"ds_read_b64_tr_b16 v[140:141], v184 offset:840\n"
"v_cvt_pk_bf16_f32 v159, v214, v215\n"
"v_mfma_f32_16x16x32_bf16 v[204:207], a[116:119], v[44:47], v[204:207]\n"
"ds_read_b64_tr_b16 v[142:143], v185 offset:840\n"
"v_fmamk_f32 v246, v246, 0x3e553b95, v170\n"
"v_mfma_f32_16x16x32_bf16 v[208:211], a[124:127], v[48:51], v[208:211]\n"
"s_add_u32 m0, s79, 142464\n"
"ds_read_b128 v[128:131], v180 offset:13504\n"
"buffer_load_dwordx4 v192, s[16:19], s43 offen offset:1024 lds\n"
"v_mfma_f32_16x16x32_bf16 v[204:207], a[128:131], v[48:51], v[204:207]\n"
"v_fmamk_f32 v247, v247, 0x3e553b95, v170\n"
"v_fmamk_f32 v248, v248, 0x3e553b95, v170\n"
"v_mfma_f32_16x16x32_bf16 v[208:211], a[136:139], v[52:55], v[208:211]\n"
"v_exp_f32 v244, v244\n"
"v_mfma_f32_16x16x32_bf16 v[204:207], a[140:143], v[52:55], v[204:207]\n"
"v_exp_f32 v245, v245\n"
"v_mfma_f32_16x16x32_bf16 v[212:215], a[144:147], v[32:35], 0\n"
"v_exp_f32 v246, v246\n"
"v_mfma_f32_16x16x32_bf16 v[212:215], a[156:159], v[36:39], v[212:215]\n"
"v_exp_f32 v247, v247\n"
"v_mfma_f32_16x16x32_bf16 v[212:215], a[168:171], v[40:43], v[212:215]\n"
"v_cvt_pk_bf16_f32 v90, v244, v246\n"
"v_cvt_pk_bf16_f32 v91, v245, v247\n"
"v_mfma_f32_16x16x32_bf16 v[212:215], a[180:183], v[44:47], v[212:215]\n"
"ds_read_b128 v[244:247], v181 offset:13504\n"
"ds_read_b128 v[132:135], v180 offset:27008\n"
"v_mfma_f32_16x16x32_bf16 v[212:215], a[192:195], v[48:51], v[212:215]\n"
"s_add_u32 m0, s79, 142464\n"
"v_fmamk_f32 v249, v249, 0x3e553b95, v170\n"
"v_fmamk_f32 v250, v250, 0x3e553b95, v170\n"
"v_mfma_f32_16x16x32_bf16 v[212:215], a[204:207], v[52:55], v[212:215]\n"
"s_waitcnt lgkmcnt(2)\n"
"buffer_load_dwordx4 v192, s[16:19], s43 offen offset:2048 lds\n"
"v_fmamk_f32 v251, v251, 0x3e553b95, v170\n"
"v_mfma_f32_16x16x32_bf16 v[128:131], v[128:131], v[148:151], 0\n"
"s_add_u32 s43, s80, 12288\n"
"v_exp_f32 v248, v248\n"
"v_exp_f32 v249, v249\n"
"v_exp_f32 v250, v250\n"
"v_exp_f32 v251, v251\n"
"v_cvt_pk_bf16_f32 v92, v248, v250\n"
"v_cvt_pk_bf16_f32 v93, v249, v251\n"
"v_cvt_pk_bf16_f32 v228, v228, v229\n"
"v_cvt_pk_bf16_f32 v229, v230, v231\n"
"v_smfmac_f32_16x16x64_bf16 v[252:255], v[90:93], v[120:127], v66\n"
"ds_read_b128 v[248:251], v181 offset:27008\n"
"v_smfmac_f32_16x16x64_bf16 v[98:101], v[90:93], v[112:119], v66\n"
"s_waitcnt lgkmcnt(2)\n"
"ds_read_b128 v[120:123], v178\n"
"v_smfmac_f32_16x16x64_bf16 v[106:109], v[90:93], v[136:143], v66\n"
"s_waitcnt lgkmcnt(2)\n"
"ds_read_b128 v[112:115], v179\n"
"v_mfma_f32_16x16x32_bf16 v[90:93], v[172:175], v[90:93], v[82:85]\n"
"s_add_u32 m0, s34, 5120\n"
"ds_read_b128 v[124:127], v178 offset:13504\n"
"buffer_load_dwordx4 v191, s[8:11], s1 offen lds\n"
"v_mfma_f32_16x16x32_bf16 v[244:247], v[244:247], v[148:151], 0\n"
"s_add_u32 s1, s1, s33\n"
"ds_read_b64 v[82:83], v180 offset:13568\n"
"ds_read_b64 v[84:85], v181 offset:13568\n"
"v_mfma_f32_16x16x32_bf16 v[132:135], v[132:135], v[148:151], 0\n"
"v_rcp_f32 v171, v90\n"
"v_cvt_pk_bf16_f32 v252, v252, v253\n"
"v_cvt_pk_bf16_f32 v171, v171, v171\n"
"v_and_b32 v64, v171, v176\n"
"v_and_b32 v65, v171, v177\n"
"v_cvt_pk_bf16_f32 v253, v254, v255\n"
"v_cvt_pk_bf16_f32 v98, v98, v99\n"
"v_cvt_pk_bf16_f32 v99, v100, v101\n"
"v_mfma_f32_16x16x16_bf16 v[252:255], v[252:253], v[64:65], 0\n"
"s_waitcnt lgkmcnt(5)\n"
"ds_read_b64 v[90:91], v180 offset:27072\n"
"ds_read_b64 v[92:93], v181 offset:27072\n"
"v_mfma_f32_16x16x16_bf16 v[98:101], v[98:99], v[64:65], 0\n"
"s_waitcnt lgkmcnt(6)\n"
"ds_read_b128 v[116:119], v179 offset:13504\n"
"v_cvt_pk_bf16_f32 v106, v106, v107\n"
"v_mfma_f32_16x16x32_bf16 v[248:251], v[248:251], v[148:151], 0\n"
"s_waitcnt lgkmcnt(6)\n"
"v_cvt_pk_bf16_f32 v107, v108, v109\n"
"v_cvt_pk_bf16_f32 v252, v252, v253\n"
"v_mfma_f32_16x16x32_bf16 v[120:123], v[120:123], v[148:151], 0\n"
"s_waitcnt lgkmcnt(5)\n"
"v_cvt_pk_bf16_f32 v253, v254, v255\n"
"v_cvt_pk_bf16_f32 v254, v98, v99\n"
"v_mfma_f32_16x16x16_bf16 v[106:109], v[106:107], v[64:65], 0\n"
"s_waitcnt lgkmcnt(4)\n"
"buffer_load_dword v200, off, s[28:31], s39 sc1\n"
"ds_read_b64 v[98:99], v178 offset:64\n"
"v_mfma_f32_16x16x32_bf16 v[128:131], v[80:83], v[160:163], v[128:131]\n"
"s_waitcnt lgkmcnt(4)\n"
"ds_read_b64_tr_b16 a[240:241], v236\n"
"v_cvt_pk_bf16_f32 v255, v100, v101\n"
"v_mfma_f32_16x16x32_bf16 v[244:247], v[84:87], v[162:165], v[244:247]\n"
"buffer_store_dwordx4 v[252:255], v192, s[28:31], s37 offen sc1\n"
"v_mfma_f32_16x16x32_bf16 v[112:115], v[112:115], v[148:151], 0\n"
"s_waitcnt lgkmcnt(4)\n"
"ds_read_b64 v[100:101], v179 offset:64\n"
"v_cvt_pk_bf16_f32 v106, v106, v107\n"
"v_mfma_f32_16x16x32_bf16 v[132:135], v[88:91], v[160:163], v[132:135]\n"
"s_waitcnt lgkmcnt(4)\n"
"v_cvt_pk_bf16_f32 v107, v108, v109\n"
"v_cvt_pk_bf16_f32 v230, v216, v217\n"
"v_mfma_f32_16x16x32_bf16 v[248:251], v[92:95], v[162:165], v[248:251]\n"
"s_waitcnt lgkmcnt(3)\n"
"buffer_store_dwordx2 v[106:107], v193, s[28:31], s37 offen offset:1024 sc1\n"
"v_mfma_f32_16x16x32_bf16 v[124:127], v[124:127], v[148:151], 0\n"
"s_add_u32 s37, s37, 1572864\n"
"ds_read_b64 v[106:107], v178 offset:13568\n"
"ds_read_b64 v[108:109], v179 offset:13568\n"
"v_mfma_f32_16x16x32_bf16 v[116:119], v[116:119], v[148:151], 0\n"
"s_waitcnt lgkmcnt(4)\n"
"ds_read_b64_tr_b16 v[136:137], v182 offset:54144\n"
"v_cvt_pk_bf16_f32 v231, v218, v219\n"
"v_mfma_f32_16x16x32_bf16 v[120:123], v[96:99], v[160:163], v[120:123]\n"
"ds_write_b128 v186, v[228:231] offset:33760\n"
"v_mfma_f32_16x16x32_bf16 v[216:219], a[148:151], v[32:35], 0\n"
"v_cvt_pk_bf16_f32 v220, v220, v221\n"
"v_cvt_pk_bf16_f32 v221, v222, v223\n"
"v_mfma_f32_16x16x32_bf16 v[228:231], a[152:155], v[32:35], 0\n"
"s_waitcnt lgkmcnt(4)\n"
"buffer_load_dword v237, v67, s[8:11], s78 offen\n"
"ds_read_b128 v[32:35], v188 offset:6144\n"
"v_mfma_f32_16x16x32_bf16 v[112:115], v[100:103], v[162:165], v[112:115]\n"
"s_add_u32 s78, s78, s33\n"
"ds_read_b64_tr_b16 v[138:139], v183 offset:54144\n"
"ds_read_b64_tr_b16 v[140:141], v182 offset:54912\n"
"v_mfma_f32_16x16x32_bf16 v[216:219], a[160:163], v[36:39], v[216:219]\n"
"ds_read_b64_tr_b16 v[142:143], v183 offset:54912\n"
"v_max_f32 v128, v128, v244\n"
"v_mfma_f32_16x16x32_bf16 v[228:231], a[164:167], v[36:39], v[228:231]\n"
"s_waitcnt lgkmcnt(7)\n"
"ds_read_b128 v[36:39], v189 offset:6144\n"
"v_max_f32 v129, v129, v245\n"
"v_mfma_f32_16x16x32_bf16 v[124:127], v[104:107], v[160:163], v[124:127]\n"
"s_waitcnt lgkmcnt(7)\n"
"ds_read_b64_tr_b16 v[244:245], v182 offset:54152\n"
"v_max_f32 v130, v130, v246\n"
"v_mfma_f32_16x16x32_bf16 v[116:119], v[108:111], v[162:165], v[116:119]\n"
"ds_read_b64_tr_b16 a[242:243], v236 offset:6144\n"
"v_max_f32 v131, v131, v247\n"
"v_mfma_f32_16x16x32_bf16 v[216:219], a[172:175], v[40:43], v[216:219]\n"
"v_max3_f32 v170, v128, v129, v130\n"
"v_max_f32 v132, v132, v248\n"
"v_mfma_f32_16x16x32_bf16 v[228:231], a[176:179], v[40:43], v[228:231]\n"
"ds_read_b128 v[40:43], v188 offset:7168\n"
"v_max_f32 v133, v133, v249\n"
"v_mfma_f32_16x16x32_bf16 v[216:219], a[184:187], v[44:47], v[216:219]\n"
"ds_read_b64_tr_b16 v[246:247], v183 offset:54152\n"
"v_max_f32 v134, v134, v250\n"
"v_mfma_f32_16x16x32_bf16 v[228:231], a[188:191], v[44:47], v[228:231]\n"
"ds_read_b128 v[44:47], v189 offset:7168\n"
"v_max_f32 v135, v135, v251\n"
"v_mfma_f32_16x16x32_bf16 v[216:219], a[196:199], v[48:51], v[216:219]\n"
"ds_read_b64_tr_b16 v[248:249], v182 offset:54920\n"
"v_max3_f32 v170, v170, v131, v132\n"
"v_mfma_f32_16x16x32_bf16 v[228:231], a[200:203], v[48:51], v[228:231]\n"
"s_waitcnt vmcnt(17)\n"
"ds_read_b128 v[48:51], v188 offset:8192\n"
"v_max3_f32 v170, v170, v133, v134\n"
"v_mfma_f32_16x16x32_bf16 v[216:219], a[208:211], v[52:55], v[216:219]\n"
"s_waitcnt lgkmcnt(7)\n"
"ds_read_b64_tr_b16 v[250:251], v183 offset:54920\n"
"v_max_f32 v120, v120, v112\n"
"v_mfma_f32_16x16x32_bf16 v[228:231], a[212:215], v[52:55], v[228:231]\n"
"s_add_u32 m0, s34, 6144\n"
"ds_read_b128 v[52:55], v189 offset:8192\n"
"buffer_load_dwordx4 v190, s[8:11], s32 offen lds\n"
"v_max_f32 v121, v121, v113\n"
"v_max_f32 v122, v122, v114\n"
"v_max_f32 v123, v123, v115\n"
"v_max3_f32 v170, v170, v135, v120\n"
"v_cvt_pk_bf16_f32 v224, v224, v225\n"
"v_max3_f32 v170, v170, v121, v122\n"
"v_max_f32 v124, v124, v116\n"
"v_cvt_pk_bf16_f32 v225, v226, v227\n"
"v_max_f32 v125, v125, v117\n"
"v_cvt_pk_bf16_f32 v226, v208, v209\n"
"v_max_f32 v126, v126, v118\n"
"v_cvt_pk_bf16_f32 v227, v210, v211\n"
"v_mfma_f32_16x16x32_bf16 v[208:211], a[0:3], v[32:35], 0\n"
"ds_read_b64_tr_b16 v[112:113], v182 offset:54088\n"
"ds_read_b64_tr_b16 v[114:115], v183 offset:54088\n"
"v_mfma_f32_16x16x32_bf16 v[208:211], a[12:15], v[36:39], v[208:211]\n"
"s_waitcnt lgkmcnt(8)\n"
"ds_read_b64_tr_b16 v[116:117], v182 offset:54856\n"
"v_max_f32 v127, v127, v119\n"
"v_mfma_f32_16x16x32_bf16 v[208:211], a[24:27], v[40:43], v[208:211]\n"
"s_waitcnt lgkmcnt(7)\n"
"ds_read_b64_tr_b16 v[118:119], v183 offset:54856\n"
"v_max3_f32 v170, v170, v123, v124\n"
"v_mfma_f32_16x16x32_bf16 v[208:211], a[36:39], v[44:47], v[208:211]\n"
"s_waitcnt lgkmcnt(6)\n"
"ds_write_b128 v186, v[224:227] offset:33888\n"
"v_mfma_f32_16x16x32_bf16 v[208:211], a[48:51], v[48:51], v[208:211]\n"
"s_waitcnt lgkmcnt(5)\n"
"v_cvt_pk_bf16_f32 v222, v204, v205\n"
"v_max3_f32 v170, v170, v125, v126\n"
"v_mfma_f32_16x16x32_bf16 v[208:211], a[60:63], v[52:55], v[208:211]\n"
"s_add_u32 m0, s34, 9216\n"
"v_cvt_pk_bf16_f32 v223, v206, v207\n"
"s_mov_b64 exec, s[46:47]\n"
"v_add_u32 v183, 0xc0, v183\n"
"s_mov_b64 exec, -1\n"
"v_mfma_f32_16x16x32_bf16 v[204:207], a[4:7], v[32:35], 0\n"
"buffer_load_dwordx4 v191, s[8:11], s32 offen lds\n"
"ds_write_b128 v186, v[220:223] offset:33824\n"
"v_mfma_f32_16x16x32_bf16 v[220:223], a[8:11], v[32:35], 0\n"
"s_add_u32 s32, s32, s33\n"
"v_mov_b64 v[252:253], 0\n"
"v_mov_b64 v[254:255], 0\n"
"v_mfma_f32_16x16x32_bf16 v[204:207], a[16:19], v[36:39], v[204:207]\n"
"v_mov_b64 v[98:99], 0\n"
"v_mov_b64 v[100:101], 0\n"
"v_mfma_f32_16x16x32_bf16 v[220:223], a[20:23], v[36:39], v[220:223]\n"
"ds_read_b64_tr_b16 a[244:245], v236 offset:8\n"
"v_mov_b64 v[106:107], 0\n"
"v_mfma_f32_16x16x32_bf16 v[204:207], a[28:31], v[40:43], v[204:207]\n"
"ds_read_b64_tr_b16 a[246:247], v236 offset:6152\n"
"v_mov_b64 v[108:109], 0\n"
"v_mfma_f32_16x16x32_bf16 v[220:223], a[32:35], v[40:43], v[220:223]\n"
"v_max_f32 v171, v170, v127\n"
"v_max_f32 v170, v170, v127\n"
"v_mfma_f32_16x16x32_bf16 v[204:207], a[40:43], v[44:47], v[204:207]\n"
"ds_read_b64_tr_b16 a[248:249], v235\n"
"v_permlane16_swap_b32 v170, v171\n"
"v_mfma_f32_16x16x32_bf16 v[220:223], a[44:47], v[44:47], v[220:223]\n"
"s_add_u32 m0, s34, 7168\n"
"v_max_f32 v170, v170, v171\n"
"v_mov_b32 v171, v170\n"
"v_mfma_f32_16x16x32_bf16 v[204:207], a[52:55], v[48:51], v[204:207]\n"
"buffer_load_dwordx4 v190, s[8:11], s0 offen lds\n"
"ds_read_b64_tr_b16 a[250:251], v235 offset:6144\n"
"v_mfma_f32_16x16x32_bf16 v[220:223], a[56:59], v[48:51], v[220:223]\n"
"v_permlane32_swap_b32 v170, v171\n"
"v_mfma_f32_16x16x32_bf16 v[204:207], a[64:67], v[52:55], v[204:207]\n"
"v_max_f32 v170, v170, v171\n"
"v_mul_f32 v170, 0xbe553b95, v170\n"
"v_mfma_f32_16x16x32_bf16 v[220:223], a[68:71], v[52:55], v[220:223]\n"
"v_fmamk_f32 v128, v128, 0x3e553b95, v170\n"
"v_fmamk_f32 v129, v129, 0x3e553b95, v170\n"
"v_mfma_f32_16x16x32_bf16 v[224:227], a[72:75], v[32:35], 0\n"
"v_fmamk_f32 v130, v130, 0x3e553b95, v170\n"
"v_fmamk_f32 v131, v131, 0x3e553b95, v170\n"
"v_mfma_f32_16x16x32_bf16 v[224:227], a[84:87], v[36:39], v[224:227]\n"
"v_exp_f32 v128, v128\n"
"v_mfma_f32_16x16x32_bf16 v[224:227], a[96:99], v[40:43], v[224:227]\n"
"v_exp_f32 v129, v129\n"
"v_mfma_f32_16x16x32_bf16 v[224:227], a[108:111], v[44:47], v[224:227]\n"
"v_exp_f32 v130, v130\n"
"v_mfma_f32_16x16x32_bf16 v[224:227], a[120:123], v[48:51], v[224:227]\n"
"s_add_u32 m0, s34, 10240\n"
"v_exp_f32 v131, v131\n"
"v_mfma_f32_16x16x32_bf16 v[224:227], a[132:135], v[52:55], v[224:227]\n"
"buffer_load_dwordx4 v191, s[8:11], s0 offen lds\n"
"s_add_u32 s0, s0, s33\n"
"v_cvt_pk_bf16_f32 v82, v128, v130\n"
"v_cvt_pk_bf16_f32 v83, v129, v131\n"
"v_fmamk_f32 v132, v132, 0x3e553b95, v170\n"
"v_fmamk_f32 v133, v133, 0x3e553b95, v170\n"
"v_fmamk_f32 v134, v134, 0x3e553b95, v170\n"
"v_fmamk_f32 v135, v135, 0x3e553b95, v170\n"
"v_exp_f32 v132, v132\n"
"v_exp_f32 v133, v133\n"
"v_exp_f32 v134, v134\n"
"v_exp_f32 v135, v135\n"
"v_cvt_pk_bf16_f32 v84, v132, v134\n"
"v_cvt_pk_bf16_f32 v85, v133, v135\n"
"v_cvt_pk_bf16_f32 v148, v212, v213\n"
"v_cvt_pk_bf16_f32 v149, v214, v215\n"
"v_smfmac_f32_16x16x64_bf16 v[252:255], v[82:85], v[136:143], v66\n"
"s_waitcnt vmcnt(9) lgkmcnt(0)\n"
"ds_read_b64_tr_b16 v[128:129], v182 offset:128\n"
"v_smfmac_f32_16x16x64_bf16 v[98:101], v[82:85], v[244:251], v66\n"
"s_barrier\n"
"v_smfmac_f32_16x16x64_bf16 v[106:109], v[82:85], v[112:119], v66\n"
"s_add_u32 m0, s79, 130176\n"
"s_nop 0\n"
"buffer_load_dwordx4 v192, s[16:19], s43 offen lds\n"
"v_mfma_f32_16x16x32_bf16 v[82:85], v[172:175], v[82:85], 0\n"
"ds_read_b64_tr_b16 v[130:131], v183 offset:128\n"
"ds_read_b64_tr_b16 v[132:133], v182 offset:896\n"
"v_mfma_f32_16x16x32_bf16 v[212:215], a[76:79], v[32:35], 0\n"
"ds_read_b64_tr_b16 v[134:135], v183 offset:896\n"
"v_cvt_pk_bf16_f32 v150, v216, v217\n"
"v_mfma_f32_16x16x32_bf16 v[212:215], a[88:91], v[36:39], v[212:215]\n"
"ds_read_b64_tr_b16 v[136:137], v182 offset:136\n"
"v_cvt_pk_bf16_f32 v151, v218, v219\n"
"v_mfma_f32_16x16x32_bf16 v[216:219], a[80:83], v[32:35], 0\n"
"ds_read_b64_tr_b16 v[138:139], v183 offset:136\n"
"v_fmamk_f32 v120, v120, 0x3e553b95, v170\n"
"v_mfma_f32_16x16x32_bf16 v[216:219], a[92:95], v[36:39], v[216:219]\n"
"ds_read_b64_tr_b16 v[140:141], v182 offset:904\n"
"v_cvt_pk_bf16_f32 v162, v228, v229\n"
"v_mfma_f32_16x16x32_bf16 v[212:215], a[100:103], v[40:43], v[212:215]\n"
"ds_read_b64_tr_b16 v[142:143], v183 offset:904\n"
"v_fmamk_f32 v121, v121, 0x3e553b95, v170\n"
"v_mfma_f32_16x16x32_bf16 v[216:219], a[104:107], v[40:43], v[216:219]\n"
"ds_read_b64_tr_b16 v[244:245], v182 offset:72\n"
"v_cvt_pk_bf16_f32 v163, v230, v231\n"
"v_mfma_f32_16x16x32_bf16 v[212:215], a[112:115], v[44:47], v[212:215]\n"
"s_add_u32 m0, s79, 130176\n"
"ds_read_b64_tr_b16 v[246:247], v183 offset:72\n"
"buffer_load_dwordx4 v192, s[16:19], s43 offen offset:1024 lds\n"
"v_mfma_f32_16x16x32_bf16 v[216:219], a[116:119], v[44:47], v[216:219]\n"
"ds_read_b64_tr_b16 v[248:249], v182 offset:840\n"
"v_fmamk_f32 v122, v122, 0x3e553b95, v170\n"
"v_mfma_f32_16x16x32_bf16 v[212:215], a[124:127], v[48:51], v[212:215]\n"
"ds_read_b64_tr_b16 v[250:251], v183 offset:840\n"
"v_fmamk_f32 v123, v123, 0x3e553b95, v170\n"
"v_mfma_f32_16x16x32_bf16 v[216:219], a[128:131], v[48:51], v[216:219]\n"
"ds_read_b128 v[112:115], v180 offset:27008\n"
"v_fmamk_f32 v124, v124, 0x3e553b95, v170\n"
"v_mfma_f32_16x16x32_bf16 v[212:215], a[136:139], v[52:55], v[212:215]\n"
"v_exp_f32 v120, v120\n"
"v_mfma_f32_16x16x32_bf16 v[216:219], a[140:143], v[52:55], v[216:219]\n"
"v_exp_f32 v121, v121\n"
"v_mfma_f32_16x16x32_bf16 v[228:231], a[144:147], v[32:35], 0\n"
"v_exp_f32 v122, v122\n"
"v_mfma_f32_16x16x32_bf16 v[228:231], a[156:159], v[36:39], v[228:231]\n"
"v_exp_f32 v123, v123\n"
"v_mfma_f32_16x16x32_bf16 v[228:231], a[168:171], v[40:43], v[228:231]\n"
"s_add_u32 m0, s79, 130176\n"
"v_cvt_pk_bf16_f32 v90, v120, v122\n"
"v_cvt_pk_bf16_f32 v91, v121, v123\n"
"v_mfma_f32_16x16x32_bf16 v[228:231], a[180:183], v[44:47], v[228:231]\n"
"buffer_load_dwordx4 v192, s[16:19], s43 offen offset:2048 lds\n"
"ds_read_b128 v[120:123], v181 offset:27008\n"
"v_mfma_f32_16x16x32_bf16 v[228:231], a[192:195], v[48:51], v[228:231]\n"
"s_add_u32 s43, s80, 49152\n"
"v_fmamk_f32 v125, v125, 0x3e553b95, v170\n"
"v_fmamk_f32 v126, v126, 0x3e553b95, v170\n"
"v_mfma_f32_16x16x32_bf16 v[228:231], a[204:207], v[52:55], v[228:231]\n"
"ds_read_b128 v[116:119], v178\n"
"v_fmamk_f32 v127, v127, 0x3e553b95, v170\n"
"v_exp_f32 v124, v124\n"
"v_exp_f32 v125, v125\n"
"v_exp_f32 v126, v126\n"
"v_exp_f32 v127, v127\n"
"v_cvt_pk_bf16_f32 v92, v124, v126\n"
"v_cvt_pk_bf16_f32 v93, v125, v127\n"
"v_cvt_pk_bf16_f32 v208, v208, v209\n"
"v_cvt_pk_bf16_f32 v209, v210, v211\n"
"v_cvt_pk_bf16_f32 v210, v204, v205\n"
"v_cvt_pk_bf16_f32 v211, v206, v207\n"
"v_mfma_f32_16x16x32_bf16 v[204:207], a[148:151], v[32:35], 0\n"
"s_waitcnt lgkmcnt(2)\n"
"ds_read_b128 v[124:127], v179\n"
"ds_write_b128 v186, v[208:211] offset:47264\n"
"v_smfmac_f32_16x16x64_bf16 v[252:255], v[90:93], v[128:135], v66\n"
"s_waitcnt lgkmcnt(3)\n"
"v_cvt_pk_bf16_f32 v220, v220, v221\n"
"v_smfmac_f32_16x16x64_bf16 v[98:101], v[90:93], v[136:143], v66\n"
"s_waitcnt lgkmcnt(2)\n"
"ds_read_b128 v[128:131], v178 offset:13504\n"
"v_smfmac_f32_16x16x64_bf16 v[106:109], v[90:93], v[244:251], v66\n"
"s_add_u32 m0, s34, 8192\n"
"s_nop 0\n"
"buffer_load_dwordx4 v190, s[8:11], s1 offen lds\n"
"v_mfma_f32_16x16x32_bf16 v[90:93], v[172:175], v[90:93], v[82:85]\n"
"ds_read_b128 v[136:139], v179 offset:13504\n"
"ds_read_b128 v[132:135], v178 offset:27008\n"
"v_mfma_f32_16x16x32_bf16 v[112:115], v[112:115], v[152:155], 0\n"
"s_waitcnt vmcnt(11)\n"
"ds_read_b64 v[82:83], v180 offset:27072\n"
"v_add3_u32 v200, v200, s50, -2\n"
"v_mfma_f32_16x16x32_bf16 v[120:123], v[120:123], v[152:155], 0\n"
"v_cmp_lt_u32 vcc, 64, v200\n"
"v_cvt_pk_bf16_f32 v252, v252, v253\n"
"v_mfma_f32_16x16x32_bf16 v[116:119], v[116:119], v[152:155], 0\n"
"s_waitcnt lgkmcnt(5)\n"
"ds_read_b64 v[84:85], v181 offset:27072\n"
"ds_read_b64 v[92:93], v179 offset:64\n"
"v_mfma_f32_16x16x32_bf16 v[124:127], v[124:127], v[152:155], 0\n"
"s_cbranch_vccnz .Lp1%=\n"
".Lr1%=:\n"
"v_mfma_f32_16x16x32_bf16 v[208:211], a[152:155], v[32:35], 0\n"
"buffer_load_dword v201, off, s[28:31], s40 offset:128 sc1\n"
"ds_read_b128 v[32:35], v188\n"
"v_mfma_f32_16x16x32_bf16 v[204:207], a[160:163], v[36:39], v[204:207]\n"
"s_waitcnt lgkmcnt(6)\n"
"buffer_load_dwordx4 v[56:59], v192, s[28:31], s38 offen sc1\n"
"ds_read_b128 v[140:143], v179 offset:27008\n"
"v_mfma_f32_16x16x32_bf16 v[128:131], v[128:131], v[152:155], 0\n"
"v_rcp_f32 v171, v90\n"
"v_mfma_f32_16x16x32_bf16 v[208:211], a[164:167], v[36:39], v[208:211]\n"
"s_waitcnt lgkmcnt(6)\n"
"ds_read_b128 v[36:39], v189\n"
"ds_read_b64 v[90:91], v178 offset:64\n"
"v_mfma_f32_16x16x32_bf16 v[136:139], v[136:139], v[152:155], 0\n"
"s_waitcnt lgkmcnt(7)\n"
"v_cvt_pk_bf16_f32 v171, v171, v171\n"
"v_and_b32 v64, v171, v176\n"
"v_mfma_f32_16x16x32_bf16 v[132:135], v[132:135], v[152:155], 0\n"
"s_waitcnt lgkmcnt(6)\n"
"buffer_load_dwordx2 v[60:61], v193, s[28:31], s38 offen offset:1024 sc1\n"
"v_and_b32 v65, v171, v177\n"
"v_mfma_f32_16x16x32_bf16 v[112:115], v[80:83], v[164:167], v[112:115]\n"
"s_waitcnt lgkmcnt(5)\n"
"v_cvt_pk_bf16_f32 v253, v254, v255\n"
"v_cvt_pk_bf16_f32 v98, v98, v99\n"
"v_mfma_f32_16x16x32_bf16 v[120:123], v[84:87], v[166:169], v[120:123]\n"
"s_waitcnt lgkmcnt(4)\n"
"v_cvt_pk_bf16_f32 v99, v100, v101\n"
"v_cvt_pk_bf16_f32 v106, v106, v107\n"
"v_mfma_f32_16x16x16_bf16 v[252:255], v[252:253], v[64:65], 0\n"
"s_add_u32 m0, s34, 11264\n"
"v_cvt_pk_bf16_f32 v107, v108, v109\n"
"v_cvt_pk_bf16_f32 v221, v222, v223\n"
"v_mfma_f32_16x16x16_bf16 v[98:101], v[98:99], v[64:65], 0\n"
"s_waitcnt lgkmcnt(2)\n"
"buffer_load_dwordx4 v191, s[8:11], s1 offen lds\n"
"ds_read_b64_tr_b16 a[252:253], v236 offset:12288\n"
"v_mfma_f32_16x16x16_bf16 v[106:109], v[106:107], v[64:65], 0\n"
"s_add_u32 s1, s1, s33\n"
"v_cvt_pk_bf16_f32 v252, v252, v253\n"
"v_cvt_pk_bf16_f32 v253, v254, v255\n"
"v_mfma_f32_16x16x32_bf16 v[124:127], v[92:95], v[166:169], v[124:127]\n"
"s_add_u32 s38, s38, 1572864\n"
"v_cvt_pk_bf16_f32 v254, v98, v99\n"
"v_cvt_pk_bf16_f32 v255, v100, v101\n"
"v_mfma_f32_16x16x32_bf16 v[140:143], v[140:143], v[152:155], 0\n"
"s_waitcnt lgkmcnt(1)\n"
"ds_read_b64 v[98:99], v178 offset:13568\n"
"ds_read_b64 v[100:101], v179 offset:13568\n"
"v_mfma_f32_16x16x32_bf16 v[116:119], v[88:91], v[164:167], v[116:119]\n"
"buffer_store_dwordx4 v[252:255], v192, s[28:31], s37 offen sc1\n"
"v_mfma_f32_16x16x32_bf16 v[204:207], a[172:175], v[40:43], v[204:207]\n"
"v_cvt_pk_bf16_f32 v106, v106, v107\n"
"v_cvt_pk_bf16_f32 v107, v108, v109\n"
"v_mfma_f32_16x16x32_bf16 v[208:211], a[176:179], v[40:43], v[208:211]\n"
"ds_read_b128 v[40:43], v188 offset:1024\n"
"ds_read_b64 v[108:109], v179 offset:27072\n"
"v_mfma_f32_16x16x32_bf16 v[204:207], a[184:187], v[44:47], v[204:207]\n"
"s_add_u32 m0, s34, 0\n"
"buffer_store_dwordx2 v[106:107], v193, s[28:31], s37 offen offset:1024 sc1\n"
"v_mfma_f32_16x16x32_bf16 v[208:211], a[188:191], v[44:47], v[208:211]\n"
"s_add_u32 s37, s37, 1572864\n"
"ds_read_b128 v[44:47], v189 offset:1024\n"
"ds_read_b64 v[106:107], v178 offset:27072\n"
"v_mfma_f32_16x16x32_bf16 v[204:207], a[196:199], v[48:51], v[204:207]\n"
"ds_read_b64_tr_b16 v[244:245], v184 offset:128\n"
"v_max_f32 v112, v112, v120\n"
"v_mfma_f32_16x16x32_bf16 v[208:211], a[200:203], v[48:51], v[208:211]\n"
"s_waitcnt lgkmcnt(6)\n"
"ds_read_b128 v[48:51], v188 offset:2048\n"
"v_max_f32 v113, v113, v121\n"
"v_mfma_f32_16x16x32_bf16 v[128:131], v[96:99], v[164:167], v[128:131]\n"
"s_waitcnt lgkmcnt(6)\n"
"buffer_load_dwordx4 v190, s[8:11], s32 offen lds\n"
"ds_read_b64_tr_b16 v[246:247], v185 offset:128\n"
"v_mfma_f32_16x16x32_bf16 v[136:139], v[100:103], v[166:169], v[136:139]\n"
"ds_read_b64_tr_b16 v[248:249], v184 offset:896\n"
"v_max_f32 v114, v114, v122\n"
"v_mfma_f32_16x16x32_bf16 v[204:207], a[208:211], v[52:55], v[204:207]\n"
"ds_read_b64_tr_b16 v[250:251], v185 offset:896\n"
"v_max_f32 v115, v115, v123\n"
"v_mfma_f32_16x16x32_bf16 v[208:211], a[212:215], v[52:55], v[208:211]\n"
"s_waitcnt lgkmcnt(7)\n"
"ds_read_b128 v[52:55], v189 offset:2048\n"
"v_max3_f32 v170, v112, v113, v114\n"
"v_max_f32 v116, v116, v124\n"
"v_mfma_f32_16x16x32_bf16 v[140:143], v[108:111], v[166:169], v[140:143]\n"
"ds_read_b64_tr_b16 v[120:121], v184 offset:136\n"
"ds_read_b64_tr_b16 v[122:123], v185 offset:136\n"
"buffer_load_dword v237, v67, s[8:11], s78 offen\n"
"v_max_f32 v117, v117, v125\n"
"v_max_f32 v118, v118, v126\n"
"v_max_f32 v119, v119, v127\n"
"v_max3_f32 v170, v170, v115, v116\n"
"v_max3_f32 v170, v170, v117, v118\n"
"v_max_f32 v128, v128, v136\n"
"v_max_f32 v129, v129, v137\n"
"v_max_f32 v130, v130, v138\n"
"v_max_f32 v131, v131, v139\n"
"v_cvt_pk_bf16_f32 v224, v224, v225\n"
"v_max3_f32 v170, v170, v119, v128\n"
"v_max3_f32 v170, v170, v129, v130\n"
"v_cvt_pk_bf16_f32 v225, v226, v227\n"
"v_cvt_pk_bf16_f32 v226, v212, v213\n"
"v_cvt_pk_bf16_f32 v227, v214, v215\n"
"v_mfma_f32_16x16x32_bf16 v[212:215], a[0:3], v[32:35], 0\n"
"s_waitcnt lgkmcnt(8)\n"
"ds_read_b64_tr_b16 v[124:125], v184 offset:904\n"
"ds_read_b64_tr_b16 v[126:127], v185 offset:904\n"
"v_mfma_f32_16x16x32_bf16 v[132:135], v[104:107], v[164:167], v[132:135]\n"
"s_add_u32 m0, s34, 3072\n"
"ds_read_b64_tr_b16 a[254:255], v236 offset:18432\n"
"buffer_load_dwordx4 v191, s[8:11], s32 offen lds\n"
"v_mfma_f32_16x16x32_bf16 v[212:215], a[12:15], v[36:39], v[212:215]\n"
".Lic3%=:\n"
"s_cbranch_execz .Lib0%=\n"
"s_add_u32 s32, s32, s33\n"
"v_cvt_pk_bf16_f32 v222, v216, v217\n"
"v_cvt_pk_bf16_f32 v223, v218, v219\n"
"v_mfma_f32_16x16x32_bf16 v[216:219], a[4:7], v[32:35], 0\n"
"s_add_u32 s78, s78, s33\n"
"ds_read_b64_tr_b16 v[136:137], v184 offset:72\n"
"v_max_f32 v132, v132, v140\n"
"v_mfma_f32_16x16x32_bf16 v[216:219], a[16:19], v[36:39], v[216:219]\n"
"ds_write_b128 v186, v[220:223] offset:47328\n"
"v_mfma_f32_16x16x32_bf16 v[220:223], a[8:11], v[32:35], 0\n"
"v_max_f32 v133, v133, v141\n"
"v_max_f32 v134, v134, v142\n"
"v_mfma_f32_16x16x32_bf16 v[220:223], a[20:23], v[36:39], v[220:223]\n"
"v_max_f32 v135, v135, v143\n"
"v_max3_f32 v170, v170, v131, v132\n"
"v_mfma_f32_16x16x32_bf16 v[212:215], a[24:27], v[40:43], v[212:215]\n"
"ds_read_b64_tr_b16 v[138:139], v185 offset:72\n"
"ds_read_b64_tr_b16 v[140:141], v184 offset:840\n"
"v_mfma_f32_16x16x32_bf16 v[216:219], a[28:31], v[40:43], v[216:219]\n"
"s_waitcnt lgkmcnt(9)\n"
"ds_read_b64_tr_b16 v[142:143], v185 offset:840\n"
"v_max3_f32 v170, v170, v133, v134\n"
"v_mfma_f32_16x16x32_bf16 v[220:223], a[32:35], v[40:43], v[220:223]\n"
"s_add_u32 m0, s34, 1024\n"
"ds_write_b128 v186, v[224:227] offset:47392\n"
"v_mfma_f32_16x16x32_bf16 v[212:215], a[36:39], v[44:47], v[212:215]\n"
"buffer_load_dwordx4 v190, s[8:11], s0 offen lds\n"
"v_mov_b64 v[252:253], 0\n"
"v_mfma_f32_16x16x32_bf16 v[216:219], a[40:43], v[44:47], v[216:219]\n"
"v_mov_b64 v[254:255], 0\n"
"v_mov_b64 v[98:99], 0\n"
"v_mfma_f32_16x16x32_bf16 v[220:223], a[44:47], v[44:47], v[220:223]\n"
"ds_read_b64_tr_b16 v[0:1], v236 offset:12296\n"
"v_mov_b64 v[100:101], 0\n"
"v_mfma_f32_16x16x32_bf16 v[212:215], a[48:51], v[48:51], v[212:215]\n"
"ds_read_b64_tr_b16 v[2:3], v236 offset:18440\n"
"v_mov_b64 v[106:107], 0\n"
"v_mfma_f32_16x16x32_bf16 v[216:219], a[52:55], v[48:51], v[216:219]\n"
"v_mov_b64 v[108:109], 0\n"
"v_max_f32 v171, v170, v135\n"
"v_mfma_f32_16x16x32_bf16 v[220:223], a[56:59], v[48:51], v[220:223]\n"
"ds_read_b64_tr_b16 v[4:5], v235 offset:12288\n"
"v_max_f32 v170, v170, v135\n"
"v_mfma_f32_16x16x32_bf16 v[212:215], a[60:63], v[52:55], v[212:215]\n"
"s_nop 0\n"
"v_permlane16_swap_b32 v170, v171\n"
"v_mfma_f32_16x16x32_bf16 v[216:219], a[64:67], v[52:55], v[216:219]\n"
"ds_read_b64_tr_b16 v[6:7], v235 offset:18432\n"
"v_max_f32 v170, v170, v171\n"
"v_mfma_f32_16x16x32_bf16 v[220:223], a[68:71], v[52:55], v[220:223]\n"
"s_add_u32 m0, s34, 4096\n"
"v_mov_b32 v171, v170\n"
"v_cvt_pk_bf16_f32 v152, v228, v229\n"
"v_mfma_f32_16x16x32_bf16 v[224:227], a[72:75], v[32:35], 0\n"
"buffer_load_dwordx4 v191, s[8:11], s0 offen lds\n"
"v_permlane32_swap_b32 v170, v171\n"
"v_mfma_f32_16x16x32_bf16 v[224:227], a[84:87], v[36:39], v[224:227]\n"
"s_add_u32 s0, s0, s33\n"
"v_max_f32 v170, v170, v171\n"
"v_mul_f32 v170, 0xbe553b95, v170\n"
"v_mfma_f32_16x16x32_bf16 v[224:227], a[96:99], v[40:43], v[224:227]\n"
"v_fmamk_f32 v112, v112, 0x3e553b95, v170\n"
"v_fmamk_f32 v113, v113, 0x3e553b95, v170\n"
"v_mfma_f32_16x16x32_bf16 v[224:227], a[108:111], v[44:47], v[224:227]\n"
"v_fmamk_f32 v114, v114, 0x3e553b95, v170\n"
"v_fmamk_f32 v115, v115, 0x3e553b95, v170\n"
"v_mfma_f32_16x16x32_bf16 v[224:227], a[120:123], v[48:51], v[224:227]\n"
"v_exp_f32 v112, v112\n"
"v_mfma_f32_16x16x32_bf16 v[224:227], a[132:135], v[52:55], v[224:227]\n"
"v_exp_f32 v113, v113\n"
"v_exp_f32 v114, v114\n"
"v_exp_f32 v115, v115\n"
"v_cvt_pk_bf16_f32 v82, v112, v114\n"
"s_add_u32 m0, s34, 2048\n"
"v_cvt_pk_bf16_f32 v83, v113, v115\n"
"buffer_load_dwordx4 v190, s[8:11], s1 offen lds\n"
"v_fmamk_f32 v116, v116, 0x3e553b95, v170\n"
"v_fmamk_f32 v117, v117, 0x3e553b95, v170\n"
"v_fmamk_f32 v118, v118, 0x3e553b95, v170\n"
"v_fmamk_f32 v119, v119, 0x3e553b95, v170\n"
"v_exp_f32 v116, v116\n"
"v_exp_f32 v117, v117\n"
"v_exp_f32 v118, v118\n"
"v_exp_f32 v119, v119\n"
"v_cvt_pk_bf16_f32 v84, v116, v118\n"
"v_cvt_pk_bf16_f32 v85, v117, v119\n"
"v_cvt_pk_bf16_f32 v153, v230, v231\n"
"v_mfma_f32_16x16x32_bf16 v[228:231], a[76:79], v[32:35], 0\n"
"s_waitcnt vmcnt(13) lgkmcnt(0)\n"
"ds_read_b64_tr_b16 v[112:113], v182 offset:13632\n"
"ds_read_b64_tr_b16 v[114:115], v183 offset:13632\n"
"v_smfmac_f32_16x16x64_bf16 v[252:255], v[82:85], v[244:251], v66\n"
"s_barrier\n"
"v_smfmac_f32_16x16x64_bf16 v[98:101], v[82:85], v[120:127], v66\n"
"ds_read_b64_tr_b16 v[116:117], v182 offset:14400\n"
"v_smfmac_f32_16x16x64_bf16 v[106:109], v[82:85], v[136:143], v66\n"
"s_add_u32 m0, s79, 142464\n"
"s_nop 0\n"
"buffer_load_dwordx4 v192, s[16:19], s43 offen lds\n"
"v_mfma_f32_16x16x32_bf16 v[82:85], v[172:175], v[82:85], 0\n"
"ds_read_b64_tr_b16 v[118:119], v183 offset:14400\n"
"ds_read_b64_tr_b16 v[244:245], v182 offset:13640\n"
"v_mfma_f32_16x16x32_bf16 v[228:231], a[88:91], v[36:39], v[228:231]\n"
"ds_read_b64_tr_b16 v[246:247], v183 offset:13640\n"
"v_cvt_pk_bf16_f32 v154, v204, v205\n"
"v_mfma_f32_16x16x32_bf16 v[228:231], a[100:103], v[40:43], v[228:231]\n"
"ds_read_b64_tr_b16 v[248:249], v182 offset:14408\n"
"v_cvt_pk_bf16_f32 v155, v206, v207\n"
"v_mfma_f32_16x16x32_bf16 v[204:207], a[80:83], v[32:35], 0\n"
"ds_read_b64_tr_b16 v[250:251], v183 offset:14408\n"
"v_fmamk_f32 v128, v128, 0x3e553b95, v170\n"
"v_mfma_f32_16x16x32_bf16 v[204:207], a[92:95], v[36:39], v[204:207]\n"
"ds_read_b64_tr_b16 v[120:121], v182 offset:13576\n"
"v_fmamk_f32 v129, v129, 0x3e553b95, v170\n"
"v_mfma_f32_16x16x32_bf16 v[204:207], a[104:107], v[40:43], v[204:207]\n"
"ds_read_b64_tr_b16 v[122:123], v183 offset:13576\n"
"v_cvt_pk_bf16_f32 v166, v208, v209\n"
"v_mfma_f32_16x16x32_bf16 v[228:231], a[112:115], v[44:47], v[228:231]\n"
"ds_read_b64_tr_b16 v[124:125], v182 offset:14344\n"
"v_fmamk_f32 v130, v130, 0x3e553b95, v170\n"
"v_mfma_f32_16x16x32_bf16 v[204:207], a[116:119], v[44:47], v[204:207]\n"
"s_add_u32 m0, s79, 142464\n"
"ds_read_b64_tr_b16 v[126:127], v183 offset:14344\n"
"buffer_load_dwordx4 v192, s[16:19], s43 offen offset:1024 lds\n"
"v_mfma_f32_16x16x32_bf16 v[228:231], a[124:127], v[48:51], v[228:231]\n"
"v_cvt_pk_bf16_f32 v167, v210, v211\n"
"v_fmamk_f32 v131, v131, 0x3e553b95, v170\n"
"v_mfma_f32_16x16x32_bf16 v[204:207], a[128:131], v[48:51], v[204:207]\n"
"v_exp_f32 v128, v128\n"
"v_mfma_f32_16x16x32_bf16 v[228:231], a[136:139], v[52:55], v[228:231]\n"
"v_exp_f32 v129, v129\n"
"v_mfma_f32_16x16x32_bf16 v[204:207], a[140:143], v[52:55], v[204:207]\n"
"ds_read_b128 v[136:139], v178\n"
"v_fmamk_f32 v132, v132, 0x3e553b95, v170\n"
"v_mfma_f32_16x16x32_bf16 v[208:211], a[144:147], v[32:35], 0\n"
"v_exp_f32 v130, v130\n"
"v_mfma_f32_16x16x32_bf16 v[208:211], a[156:159], v[36:39], v[208:211]\n"
"v_exp_f32 v131, v131\n"
"v_mfma_f32_16x16x32_bf16 v[208:211], a[168:171], v[40:43], v[208:211]\n"
"v_cvt_pk_bf16_f32 v90, v128, v130\n"
"v_cvt_pk_bf16_f32 v91, v129, v131\n"
"v_mfma_f32_16x16x32_bf16 v[208:211], a[180:183], v[44:47], v[208:211]\n"
"s_add_u32 m0, s79, 142464\n"
"ds_read_b128 v[128:131], v179\n"
"buffer_load_dwordx4 v192, s[16:19], s43 offen offset:2048 lds\n"
"v_mfma_f32_16x16x32_bf16 v[208:211], a[192:195], v[48:51], v[208:211]\n"
"v_fmamk_f32 v133, v133, 0x3e553b95, v170\n"
"v_fmamk_f32 v134, v134, 0x3e553b95, v170\n"
"v_mfma_f32_16x16x32_bf16 v[208:211], a[204:207], v[52:55], v[208:211]\n"
"v_fmamk_f32 v135, v135, 0x3e553b95, v170\n"
"v_exp_f32 v132, v132\n"
"v_exp_f32 v133, v133\n"
"v_exp_f32 v134, v134\n"
"v_exp_f32 v135, v135\n"
"v_cvt_pk_bf16_f32 v92, v132, v134\n"
"v_cvt_pk_bf16_f32 v93, v133, v135\n"
"s_mov_b64 exec, s[44:45]\n"
"v_add_u32 v181, 0xc0, v181\n"
"s_mov_b64 exec, -1\n"
"v_cvt_pk_bf16_f32 v212, v212, v213\n"
"v_cvt_pk_bf16_f32 v213, v214, v215\n"
"v_cvt_pk_bf16_f32 v214, v216, v217\n"
"v_cvt_pk_bf16_f32 v215, v218, v219\n"
"v_mfma_f32_16x16x32_bf16 v[216:219], a[148:151], v[32:35], 0\n"
"s_waitcnt lgkmcnt(1)\n"
"ds_read_b128 v[140:143], v178 offset:13504\n"
"ds_read_b128 v[132:135], v179 offset:13504\n"
"v_smfmac_f32_16x16x64_bf16 v[252:255], v[90:93], v[112:119], v66\n"
"s_waitcnt lgkmcnt(2)\n"
"ds_write_b128 v186, v[212:215] offset:60768\n"
"v_smfmac_f32_16x16x64_bf16 v[98:101], v[90:93], v[244:251], v66\n"
"s_add_u32 m0, s34, 5120\n"
"s_nop 0\n"
"buffer_load_dwordx4 v191, s[8:11], s1 offen lds\n"
"v_smfmac_f32_16x16x64_bf16 v[106:109], v[90:93], v[120:127], v66\n"
"s_add_u32 s1, s1, s33\n"
"ds_read_b128 v[112:115], v178 offset:27008\n"
"v_mfma_f32_16x16x32_bf16 v[90:93], v[172:175], v[90:93], v[82:85]\n"
"s_waitcnt vmcnt(14)\n"
"v_readfirstlane_b32 s6, v201\n"
"v_cvt_pk_bf16_f32 v252, v252, v253\n"
"v_mfma_f32_16x16x32_bf16 v[136:139], v[136:139], v[144:147], 0\n"
"s_and_b32 s6, s6, 0xffffff00\n"
"v_or_b32 v201, 4, s6\n"
"v_cvt_pk_bf16_f32 v253, v254, v255\n"
"v_mfma_f32_16x16x32_bf16 v[128:131], v[128:131], v[144:147], 0\n"
"s_mov_b64 exec, 1\n"
"buffer_store_dword v201, off, s[28:31], s40 sc1\n"
"s_mov_b64 exec, -1\n"
"v_mfma_f32_16x16x32_bf16 v[212:215], a[152:155], v[32:35], 0\n"
"s_waitcnt lgkmcnt(2)\n"
"ds_read_b128 v[32:35], v188 offset:6144\n"
"ds_read_b64 v[82:83], v178 offset:64\n"
"v_mfma_f32_16x16x32_bf16 v[140:143], v[140:143], v[144:147], 0\n"
"s_waitcnt vmcnt(14)\n"
"ds_read_b64 v[84:85], v179 offset:64\n"
"v_cvt_pk_bf16_f32 v98, v98, v99\n"
"v_mfma_f32_16x16x32_bf16 v[132:135], v[132:135], v[144:147], 0\n"
"ds_read_b64 v[92:93], v179 offset:13568\n"
"v_cvt_pk_bf16_f32 v99, v100, v101\n"
"v_mfma_f32_16x16x32_bf16 v[216:219], a[160:163], v[36:39], v[216:219]\n"
"s_waitcnt lgkmcnt(4)\n"
"ds_write_b128 v202, v[56:59] offset:55296\n"
"v_mfma_f32_16x16x32_bf16 v[112:115], v[112:115], v[144:147], 0\n"
"buffer_load_dwordx4 v[56:59], v192, s[28:31], s38 offen sc1\n"
"v_cvt_pk_bf16_f32 v106, v106, v107\n"
"v_mfma_f32_16x16x32_bf16 v[212:215], a[164:167], v[36:39], v[212:215]\n"
"v_rcp_f32 v171, v90\n"
"v_mfma_f32_16x16x32_bf16 v[216:219], a[172:175], v[40:43], v[216:219]\n"
"ds_read_b128 v[36:39], v189 offset:6144\n"
"ds_read_b64 v[90:91], v178 offset:13568\n"
"v_mfma_f32_16x16x32_bf16 v[212:215], a[176:179], v[40:43], v[212:215]\n"
"ds_read_b128 v[40:43], v188 offset:7168\n"
"v_cvt_pk_bf16_f32 v171, v171, v171\n"
"v_mfma_f32_16x16x32_bf16 v[216:219], a[184:187], v[44:47], v[216:219]\n"
"s_waitcnt lgkmcnt(6)\n"
"ds_read_b128 v[244:247], v179 offset:27008\n"
"v_and_b32 v64, v171, v176\n"
"v_mfma_f32_16x16x32_bf16 v[136:139], v[80:83], v[156:159], v[136:139]\n"
"s_waitcnt lgkmcnt(6)\n"
"ds_read_b128 v[116:119], v180\n"
"v_and_b32 v65, v171, v177\n"
"v_mfma_f32_16x16x32_bf16 v[128:131], v[84:87], v[158:161], v[128:131]\n"
"s_waitcnt lgkmcnt(6)\n"
"ds_read_b128 v[248:251], v181\n"
"v_cvt_pk_bf16_f32 v107, v108, v109\n"
"v_mfma_f32_16x16x16_bf16 v[252:255], v[252:253], v[64:65], 0\n"
"ds_read_b64_tr_b16 v[8:9], v236\n"
"v_cvt_pk_bf16_f32 v220, v220, v221\n"
"v_mfma_f32_16x16x16_bf16 v[98:101], v[98:99], v[64:65], 0\n"
"s_waitcnt vmcnt(12)\n"
"v_or_b32 v201, 5, s6\n"
"v_cvt_pk_bf16_f32 v221, v222, v223\n"
"v_mfma_f32_16x16x16_bf16 v[106:109], v[106:107], v[64:65], 0\n"
"s_mov_b64 exec, 1\n"
"buffer_store_dword v201, off, s[28:31], s40 sc1\n"
"s_mov_b64 exec, -1\n"
"v_mfma_f32_16x16x32_bf16 v[132:135], v[92:95], v[158:161], v[132:135]\n"
"s_waitcnt lgkmcnt(5)\n"
"v_cvt_pk_bf16_f32 v252, v252, v253\n"
"v_cvt_pk_bf16_f32 v253, v254, v255\n"
"v_mfma_f32_16x16x32_bf16 v[140:143], v[88:91], v[156:159], v[140:143]\n"
"s_add_u32 m0, s34, 6144\n"
"v_cvt_pk_bf16_f32 v254, v98, v99\n"
"v_cvt_pk_bf16_f32 v255, v100, v101\n"
"v_mfma_f32_16x16x32_bf16 v[212:215], a[188:191], v[44:47], v[212:215]\n"
"s_waitcnt lgkmcnt(3)\n"
"buffer_load_dwordx4 v190, s[8:11], s32 offen lds\n"
"ds_read_b128 v[44:47], v189 offset:7168\n"
"v_mfma_f32_16x16x32_bf16 v[244:247], v[244:247], v[144:147], 0\n"
"s_waitcnt lgkmcnt(3)\n"
"ds_read_b64 v[98:99], v178 offset:27072\n"
"ds_read_b64 v[100:101], v179 offset:27072\n"
"v_mfma_f32_16x16x32_bf16 v[116:119], v[116:119], v[144:147], 0\n"
"s_waitcnt lgkmcnt(4)\n"
"v_cvt_pk_bf16_f32 v106, v106, v107\n"
"v_cvt_pk_bf16_f32 v107, v108, v109\n"
"v_mfma_f32_16x16x32_bf16 v[248:251], v[248:251], v[144:147], 0\n"
"buffer_store_dwordx4 v[252:255], v192, s[28:31], s37 offen sc1\n"
"v_mfma_f32_16x16x32_bf16 v[216:219], a[196:199], v[48:51], v[216:219]\n"
"ds_read_b64 v[108:109], v181 offset:64\n"
"ds_read_b64_tr_b16 v[120:121], v182 offset:128\n"
"v_mfma_f32_16x16x32_bf16 v[212:215], a[200:203], v[48:51], v[212:215]\n"
"ds_read_b128 v[48:51], v188 offset:8192\n"
"v_max_f32 v136, v136, v128\n"
"v_mfma_f32_16x16x32_bf16 v[216:219], a[208:211], v[52:55], v[216:219]\n"
"ds_read_b64_tr_b16 v[122:123], v183 offset:128\n"
"buffer_store_dwordx2 v[106:107], v193, s[28:31], s37 offen offset:1024 sc1\n"
"v_mfma_f32_16x16x32_bf16 v[212:215], a[212:215], v[52:55], v[212:215]\n"
"s_add_u32 s37, s37, 1572864\n"
"ds_read_b128 v[52:55], v189 offset:8192\n"
"ds_read_b64 v[106:107], v180 offset:64\n"
"v_max_f32 v137, v137, v129\n"
"v_max_f32 v138, v138, v130\n"
"v_max_f32 v139, v139, v131\n"
"v_max3_f32 v170, v136, v137, v138\n"
"v_max_f32 v140, v140, v132\n"
"v_max_f32 v141, v141, v133\n"
"v_max_f32 v142, v142, v134\n"
"v_max_f32 v143, v143, v135\n"
"v_max3_f32 v170, v170, v139, v140\n"
"v_max3_f32 v170, v170, v141, v142\n"
"v_cvt_pk_bf16_f32 v224, v224, v225\n"
"v_cvt_pk_bf16_f32 v225, v226, v227\n"
"v_cvt_pk_bf16_f32 v226, v228, v229\n"
"v_cvt_pk_bf16_f32 v227, v230, v231\n"
"v_mfma_f32_16x16x32_bf16 v[228:231], a[0:3], v[32:35], 0\n"
"s_waitcnt lgkmcnt(5)\n"
"ds_read_b64_tr_b16 v[124:125], v182 offset:896\n"
"ds_read_b64_tr_b16 v[126:127], v183 offset:896\n"
"v_mfma_f32_16x16x32_bf16 v[112:115], v[96:99], v[156:159], v[112:115]\n"
"s_add_u32 m0, s34, 9216\n"
"ds_write_b64 v203, v[60:61] offset:55296\n"
"v_mfma_f32_16x16x32_bf16 v[244:247], v[100:103], v[158:161], v[244:247]\n"
"buffer_load_dwordx4 v191, s[8:11], s32 offen lds\n"
"v_cvt_pk_bf16_f32 v222, v204, v205\n"
"v_mfma_f32_16x16x32_bf16 v[248:251], v[108:111], v[158:161], v[248:251]\n"
"s_add_u32 s32, s32, s33\n"
"ds_read_b64_tr_b16 v[128:129], v182 offset:136\n"
"ds_read_b64_tr_b16 v[130:131], v183 offset:136\n"
"v_mfma_f32_16x16x32_bf16 v[228:231], a[12:15], v[36:39], v[228:231]\n"
"s_waitcnt lgkmcnt(5)\n"
"ds_read_b64_tr_b16 v[132:133], v182 offset:904\n"
"v_max_f32 v112, v112, v244\n"
"v_mfma_f32_16x16x32_bf16 v[228:231], a[24:27], v[40:43], v[228:231]\n"
"buffer_load_dwordx2 v[60:61], v193, s[28:31], s38 offen offset:1024 sc1\n"
"ds_read_b64_tr_b16 v[134:135], v183 offset:904\n"
"v_mfma_f32_16x16x32_bf16 v[116:119], v[104:107], v[156:159], v[116:119]\n"
"s_add_u32 s38, s38, 1572864\n"
"ds_read_b64_tr_b16 v[10:11], v236 offset:6144\n"
"v_max_f32 v113, v113, v245\n"
"v_mfma_f32_16x16x32_bf16 v[228:231], a[36:39], v[44:47], v[228:231]\n"
"v_max_f32 v114, v114, v246\n"
"v_max_f32 v115, v115, v247\n"
"v_mfma_f32_16x16x32_bf16 v[228:231], a[48:51], v[48:51], v[228:231]\n"
"ds_read_b64_tr_b16 v[244:245], v182 offset:72\n"
"v_max3_f32 v170, v170, v143, v112\n"
"v_mfma_f32_16x16x32_bf16 v[228:231], a[60:63], v[52:55], v[228:231]\n"
"s_add_u32 m0, s34, 7168\n"
"v_max3_f32 v170, v170, v113, v114\n"
"buffer_load_dwordx4 v190, s[8:11], s0 offen lds\n"
"v_max_f32 v116, v116, v248\n"
"v_max_f32 v117, v117, v249\n"
"v_max_f32 v118, v118, v250\n"
"v_cvt_pk_bf16_f32 v223, v206, v207\n"
"v_mfma_f32_16x16x32_bf16 v[204:207], a[4:7], v[32:35], 0\n"
"ds_write_b128 v186, v[220:223] offset:60832\n"
"v_mfma_f32_16x16x32_bf16 v[220:223], a[8:11], v[32:35], 0\n"
"v_max_f32 v119, v119, v251\n"
"v_max3_f32 v170, v170, v115, v116\n"
"v_mfma_f32_16x16x32_bf16 v[204:207], a[16:19], v[36:39], v[204:207]\n"
"v_max3_f32 v170, v170, v117, v118\n"
"v_mov_b64 v[252:253], 0\n"
"v_mfma_f32_16x16x32_bf16 v[220:223], a[20:23], v[36:39], v[220:223]\n"
"ds_read_b64_tr_b16 v[246:247], v183 offset:72\n"
"ds_read_b64_tr_b16 v[248:249], v182 offset:840\n"
"v_mfma_f32_16x16x32_bf16 v[204:207], a[28:31], v[40:43], v[204:207]\n"
"ds_read_b64_tr_b16 v[250:251], v183 offset:840\n"
"v_mov_b64 v[254:255], 0\n"
"v_mfma_f32_16x16x32_bf16 v[220:223], a[32:35], v[40:43], v[220:223]\n"
"s_add_u32 m0, s34, 10240\n"
"ds_write_b128 v186, v[224:227] offset:60896\n"
"v_mfma_f32_16x16x32_bf16 v[204:207], a[40:43], v[44:47], v[204:207]\n"
"buffer_load_dwordx4 v191, s[8:11], s0 offen lds\n"
"v_mov_b64 v[98:99], 0\n"
"v_mfma_f32_16x16x32_bf16 v[220:223], a[44:47], v[44:47], v[220:223]\n"
"s_add_u32 s0, s0, s33\n"
"v_mov_b64 v[100:101], 0\n"
"v_mov_b64 v[106:107], 0\n"
"v_mfma_f32_16x16x32_bf16 v[204:207], a[52:55], v[48:51], v[204:207]\n"
"ds_read_b64_tr_b16 v[12:13], v236 offset:8\n"
"v_mov_b64 v[108:109], 0\n"
"v_mfma_f32_16x16x32_bf16 v[220:223], a[56:59], v[48:51], v[220:223]\n"
"ds_read_b64_tr_b16 v[14:15], v236 offset:6152\n"
"v_max_f32 v171, v170, v119\n"
"v_mfma_f32_16x16x32_bf16 v[204:207], a[64:67], v[52:55], v[204:207]\n"
"v_max_f32 v170, v170, v119\n"
"v_cvt_pk_bf16_f32 v144, v208, v209\n"
"v_mfma_f32_16x16x32_bf16 v[220:223], a[68:71], v[52:55], v[220:223]\n"
"ds_read_b64_tr_b16 v[16:17], v235\n"
"v_permlane16_swap_b32 v170, v171\n"
"v_mfma_f32_16x16x32_bf16 v[224:227], a[72:75], v[32:35], 0\n"
"v_max_f32 v170, v170, v171\n"
"v_mov_b32 v171, v170\n"
"v_mfma_f32_16x16x32_bf16 v[224:227], a[84:87], v[36:39], v[224:227]\n"
"s_add_u32 m0, s34, 8192\n"
"ds_read_b64_tr_b16 v[18:19], v235 offset:6144\n"
"buffer_load_dwordx4 v190, s[8:11], s1 offen lds\n"
"v_mfma_f32_16x16x32_bf16 v[224:227], a[96:99], v[40:43], v[224:227]\n"
"v_permlane32_swap_b32 v170, v171\n"
"v_mfma_f32_16x16x32_bf16 v[224:227], a[108:111], v[44:47], v[224:227]\n"
"v_max_f32 v170, v170, v171\n"
"v_mul_f32 v170, 0xbe553b95, v170\n"
"v_mfma_f32_16x16x32_bf16 v[224:227], a[120:123], v[48:51], v[224:227]\n"
"v_fmamk_f32 v136, v136, 0x3e553b95, v170\n"
"v_fmamk_f32 v137, v137, 0x3e553b95, v170\n"
"v_mfma_f32_16x16x32_bf16 v[224:227], a[132:135], v[52:55], v[224:227]\n"
"v_fmamk_f32 v138, v138, 0x3e553b95, v170\n"
"v_fmamk_f32 v139, v139, 0x3e553b95, v170\n"
"v_exp_f32 v136, v136\n"
"v_exp_f32 v137, v137\n"
"v_exp_f32 v138, v138\n"
"v_exp_f32 v139, v139\n"
"v_cvt_pk_bf16_f32 v82, v136, v138\n"
"v_cvt_pk_bf16_f32 v83, v137, v139\n"
"v_fmamk_f32 v140, v140, 0x3e553b95, v170\n"
"s_add_u32 m0, s34, 11264\n"
"v_fmamk_f32 v141, v141, 0x3e553b95, v170\n"
"buffer_load_dwordx4 v191, s[8:11], s1 offen lds\n"
"s_add_u32 s1, s1, s33\n"
"v_fmamk_f32 v142, v142, 0x3e553b95, v170\n"
"v_fmamk_f32 v143, v143, 0x3e553b95, v170\n"
"v_exp_f32 v140, v140\n"
"v_exp_f32 v141, v141\n"
"v_exp_f32 v142, v142\n"
"v_exp_f32 v143, v143\n"
"v_cvt_pk_bf16_f32 v84, v140, v142\n"
"v_cvt_pk_bf16_f32 v85, v141, v143\n"
"v_fmamk_f32 v112, v112, 0x3e553b95, v170\n"
"v_fmamk_f32 v113, v113, 0x3e553b95, v170\n"
"v_fmamk_f32 v114, v114, 0x3e553b95, v170\n"
"v_fmamk_f32 v115, v115, 0x3e553b95, v170\n"
"v_fmamk_f32 v116, v116, 0x3e553b95, v170\n"
"v_fmamk_f32 v117, v117, 0x3e553b95, v170\n"
"v_fmamk_f32 v118, v118, 0x3e553b95, v170\n"
"v_fmamk_f32 v119, v119, 0x3e553b95, v170\n"
"v_exp_f32 v112, v112\n"
"v_exp_f32 v113, v113\n"
"v_exp_f32 v114, v114\n"
"v_exp_f32 v115, v115\n"
"v_exp_f32 v116, v116\n"
"v_exp_f32 v117, v117\n"
"v_exp_f32 v118, v118\n"
"v_exp_f32 v119, v119\n"
"v_cvt_pk_bf16_f32 v90, v112, v114\n"
"v_cvt_pk_bf16_f32 v91, v113, v115\n"
"v_cvt_pk_bf16_f32 v92, v116, v118\n"
"v_cvt_pk_bf16_f32 v93, v117, v119\n"
"v_cvt_pk_bf16_f32 v145, v210, v211\n"
"v_mfma_f32_16x16x32_bf16 v[208:211], a[76:79], v[32:35], 0\n"
"s_waitcnt vmcnt(13) lgkmcnt(0)\n"
"ds_read_b64_tr_b16 v[136:137], v182 offset:27136\n"
"ds_read_b64_tr_b16 v[138:139], v183 offset:27136\n"
"v_smfmac_f32_16x16x64_bf16 v[252:255], v[82:85], v[120:127], v66\n"
"s_barrier\n"
"v_smfmac_f32_16x16x64_bf16 v[98:101], v[82:85], v[128:135], v66\n"
"ds_read_b128 v[76:79], v192 offset:55296\n"
"v_smfmac_f32_16x16x64_bf16 v[106:109], v[82:85], v[244:251], v66\n"
"ds_read_b64_tr_b16 v[140:141], v182 offset:27904\n"
"v_mfma_f32_16x16x32_bf16 v[82:85], v[172:175], v[82:85], 0\n"
"ds_read_b64_tr_b16 v[142:143], v183 offset:27904\n"
"ds_read_b64_tr_b16 v[120:121], v182 offset:27144\n"
"v_mfma_f32_16x16x32_bf16 v[208:211], a[88:91], v[36:39], v[208:211]\n"
"ds_read_b64_tr_b16 v[122:123], v183 offset:27144\n"
"v_cvt_pk_bf16_f32 v146, v216, v217\n"
"v_mfma_f32_16x16x32_bf16 v[208:211], a[100:103], v[40:43], v[208:211]\n"
"ds_read_b64_tr_b16 v[124:125], v182 offset:27912\n"
"v_cvt_pk_bf16_f32 v147, v218, v219\n"
"v_mfma_f32_16x16x32_bf16 v[216:219], a[80:83], v[32:35], 0\n"
"ds_read_b64_tr_b16 v[126:127], v183 offset:27912\n"
"v_cvt_pk_bf16_f32 v158, v212, v213\n"
"v_mfma_f32_16x16x32_bf16 v[216:219], a[92:95], v[36:39], v[216:219]\n"
"ds_read_b64_tr_b16 v[128:129], v182 offset:27080\n"
"v_cvt_pk_bf16_f32 v159, v214, v215\n"
"v_mfma_f32_16x16x32_bf16 v[216:219], a[104:107], v[40:43], v[216:219]\n"
"ds_read_b64_tr_b16 v[130:131], v183 offset:27080\n"
"s_mov_b64 exec, s[46:47]\n"
"v_add_u32 v185, 0xc0, v185\n"
"s_mov_b64 exec, -1\n"
"v_mfma_f32_16x16x32_bf16 v[208:211], a[112:115], v[44:47], v[208:211]\n"
"s_waitcnt lgkmcnt(8)\n"
"ds_read_b64_tr_b16 v[132:133], v182 offset:27848\n"
"ds_read_b64_tr_b16 v[134:135], v183 offset:27848\n"
"v_mfma_f32_16x16x32_bf16 v[216:219], a[116:119], v[44:47], v[216:219]\n"
"s_waitcnt lgkmcnt(8)\n"
"v_cvt_pk_bf16_f32 v228, v228, v229\n"
"v_cvt_pk_bf16_f32 v229, v230, v231\n"
"v_mfma_f32_16x16x32_bf16 v[208:211], a[124:127], v[48:51], v[208:211]\n"
"ds_read_b128 v[244:247], v178 offset:13504\n"
"ds_read_b128 v[112:115], v179 offset:13504\n"
"v_smfmac_f32_16x16x64_bf16 v[252:255], v[90:93], v[136:143], v66\n"
"ds_read_b128 v[248:251], v178 offset:27008\n"
"v_mfma_f32_16x16x32_bf16 v[216:219], a[128:131], v[48:51], v[216:219]\n"
"v_cvt_pk_bf16_f32 v230, v204, v205\n"
"v_cvt_pk_bf16_f32 v231, v206, v207\n"
"v_mfma_f32_16x16x32_bf16 v[208:211], a[136:139], v[52:55], v[208:211]\n"
"s_waitcnt lgkmcnt(7)\n"
"ds_read_b128 v[116:119], v179 offset:27008\n"
"ds_write_b128 v187, v[228:231]\n"
"v_smfmac_f32_16x16x64_bf16 v[98:101], v[90:93], v[120:127], v66\n"
"buffer_load_dword v200, off, s[28:31], s39 sc1\n"
"v_mfma_f32_16x16x32_bf16 v[216:219], a[140:143], v[52:55], v[216:219]\n"
"v_cvt_pk_bf16_f32 v252, v252, v253\n"
"v_cvt_pk_bf16_f32 v253, v254, v255\n"
"v_mfma_f32_16x16x32_bf16 v[240:243], a[224:227], v[76:79], 0\n"
"s_waitcnt lgkmcnt(5)\n"
"ds_read_b128 v[136:139], v180\n"
"ds_read_b128 v[120:123], v181\n"
"v_mfma_f32_16x16x32_bf16 v[212:215], a[144:147], v[32:35], 0\n"
"s_waitcnt lgkmcnt(6)\n"
"v_cvt_pk_bf16_f32 v98, v98, v99\n"
"v_cvt_pk_bf16_f32 v99, v100, v101\n"
"v_smfmac_f32_16x16x64_bf16 v[106:109], v[90:93], v[128:135], v66\n"
"s_waitcnt vmcnt(11) lgkmcnt(5)\n"
"ds_read_b128 v[140:143], v180 offset:13504\n"
"v_mfma_f32_16x16x32_bf16 v[90:93], v[172:175], v[90:93], v[82:85]\n"
"s_waitcnt lgkmcnt(5)\n"
"ds_write_b128 v202, v[56:59] offset:36864\n"
"v_mfma_f32_16x16x32_bf16 v[244:247], v[244:247], v[148:151], 0\n"
"s_waitcnt lgkmcnt(5)\n"
"v_cvt_pk_bf16_f32 v220, v220, v221\n"
"v_cvt_pk_bf16_f32 v106, v106, v107\n"
"v_mfma_f32_16x16x32_bf16 v[112:115], v[112:115], v[148:151], 0\n"
"v_cvt_pk_bf16_f32 v107, v108, v109\n"
"v_rcp_f32 v171, v90\n"
"v_mfma_f32_16x16x32_bf16 v[248:251], v[248:251], v[148:151], 0\n"
"v_cvt_pk_bf16_f32 v171, v171, v171\n"
"v_and_b32 v64, v171, v176\n"
"v_mfma_f32_16x16x32_bf16 v[116:119], v[116:119], v[148:151], 0\n"
"s_waitcnt lgkmcnt(3)\n"
"v_and_b32 v65, v171, v177\n"
"v_cvt_pk_bf16_f32 v221, v222, v223\n"
"v_mfma_f32_16x16x32_bf16 v[136:139], v[136:139], v[148:151], 0\n"
"s_waitcnt lgkmcnt(2)\n"
"ds_read_b64 v[82:83], v178 offset:13568\n"
"ds_read_b64 v[84:85], v179 offset:13568\n"
"v_mfma_f32_16x16x16_bf16 v[252:255], v[252:253], v[64:65], 0\n"
"s_waitcnt lgkmcnt(3)\n"
"ds_read_b64 v[90:91], v178 offset:27072\n"
"v_cvt_pk_bf16_f32 v224, v224, v225\n"
"v_mfma_f32_16x16x16_bf16 v[98:101], v[98:99], v[64:65], 0\n"
"ds_read_b64 v[92:93], v179 offset:27072\n"
"v_cvt_pk_bf16_f32 v225, v226, v227\n"
"v_mfma_f32_16x16x16_bf16 v[106:109], v[106:107], v[64:65], 0\n"
"ds_read_b128 v[124:127], v181 offset:13504\n"
"v_cvt_pk_bf16_f32 v252, v252, v253\n"
"v_mfma_f32_16x16x32_bf16 v[120:123], v[120:123], v[148:151], 0\n"
"v_cvt_pk_bf16_f32 v253, v254, v255\n"
"v_cvt_pk_bf16_f32 v226, v208, v209\n"
"v_mfma_f32_16x16x32_bf16 v[140:143], v[140:143], v[148:151], 0\n"
"v_cvt_pk_bf16_f32 v254, v98, v99\n"
"v_cvt_pk_bf16_f32 v255, v100, v101\n"
"v_mfma_f32_16x16x32_bf16 v[204:207], a[148:151], v[32:35], 0\n"
"buffer_store_dwordx4 v[252:255], v192, s[28:31], s37 offen sc1\n"
"v_mfma_f32_16x16x32_bf16 v[228:231], a[152:155], v[32:35], 0\n"
"s_waitcnt lgkmcnt(4)\n"
"ds_read_b128 v[32:35], v188\n"
"v_cvt_pk_bf16_f32 v106, v106, v107\n"
"v_mfma_f32_16x16x32_bf16 v[244:247], v[80:83], v[160:163], v[244:247]\n"
"s_waitcnt lgkmcnt(4)\n"
"v_cvt_pk_bf16_f32 v107, v108, v109\n"
"v_cvt_pk_bf16_f32 v227, v210, v211\n"
"v_mfma_f32_16x16x32_bf16 v[112:115], v[84:87], v[162:165], v[112:115]\n"
"s_waitcnt lgkmcnt(3)\n"
"buffer_store_dwordx2 v[106:107], v193, s[28:31], s37 offen offset:1024 sc1\n"
"v_mfma_f32_16x16x32_bf16 v[248:251], v[88:91], v[160:163], v[248:251]\n"
"s_waitcnt lgkmcnt(2)\n"
"ds_read_b64 v[98:99], v180 offset:64\n"
"ds_read_b64 v[100:101], v181 offset:64\n"
"v_mfma_f32_16x16x32_bf16 v[116:119], v[92:95], v[162:165], v[116:119]\n"
"s_waitcnt lgkmcnt(3)\n"
"ds_read_b64 v[106:107], v180 offset:13568\n"
"v_max_f32 v244, v244, v112\n"
"v_mfma_f32_16x16x32_bf16 v[124:127], v[124:127], v[148:151], 0\n"
"s_add_u32 s37, s37, 1572864\n"
"ds_read_b64 v[108:109], v181 offset:13568\n"
"v_max_f32 v245, v245, v113\n"
"v_mfma_f32_16x16x32_bf16 v[212:215], a[156:159], v[36:39], v[212:215]\n"
"s_add_u32 s4, s37, 6291456\n"
"ds_read_b64_tr_b16 v[20:21], v236 offset:12288\n"
"v_max_f32 v246, v246, v114\n"
"v_mfma_f32_16x16x32_bf16 v[204:207], a[160:163], v[36:39], v[204:207]\n"
"s_waitcnt lgkmcnt(5)\n"
"v_max_f32 v247, v247, v115\n"
"v_max3_f32 v170, v244, v245, v246\n"
"v_mfma_f32_16x16x32_bf16 v[228:231], a[164:167], v[36:39], v[228:231]\n"
"ds_read_b128 v[36:39], v189\n"
"v_max_f32 v248, v248, v116\n"
"v_mfma_f32_16x16x32_bf16 v[212:215], a[168:171], v[40:43], v[212:215]\n"
"ds_read_b64_tr_b16 v[22:23], v236 offset:18432\n"
"v_max_f32 v249, v249, v117\n"
"v_mfma_f32_16x16x32_bf16 v[204:207], a[172:175], v[40:43], v[204:207]\n"
"v_max_f32 v250, v250, v118\n"
"v_max_f32 v251, v251, v119\n"
"v_mfma_f32_16x16x32_bf16 v[228:231], a[176:179], v[40:43], v[228:231]\n"
"s_waitcnt lgkmcnt(6)\n"
"ds_read_b128 v[40:43], v188 offset:1024\n"
"v_max3_f32 v170, v170, v247, v248\n"
"v_mfma_f32_16x16x32_bf16 v[136:139], v[96:99], v[160:163], v[136:139]\n"
"s_waitcnt lgkmcnt(6)\n"
"ds_read_b64_tr_b16 v[24:25], v236 offset:12296\n"
"v_max3_f32 v170, v170, v249, v250\n"
"v_mfma_f32_16x16x32_bf16 v[120:123], v[100:103], v[162:165], v[120:123]\n"
"s_waitcnt lgkmcnt(6)\n"
"v_cvt_pk_bf16_f32 v222, v216, v217\n"
"v_cvt_pk_bf16_f32 v223, v218, v219\n"
"v_mfma_f32_16x16x32_bf16 v[140:143], v[104:107], v[160:163], v[140:143]\n"
"s_waitcnt lgkmcnt(5)\n"
"ds_read_b64_tr_b16 v[128:129], v182 offset:13632\n"
"v_mov_b64 v[252:253], 0\n"
"v_mfma_f32_16x16x32_bf16 v[124:127], v[108:111], v[162:165], v[124:127]\n"
"ds_read_b64_tr_b16 v[130:131], v183 offset:13632\n"
"v_max_f32 v136, v136, v120\n"
"v_mfma_f32_16x16x32_bf16 v[212:215], a[180:183], v[44:47], v[212:215]\n"
".Lic4%=:\n"
"s_cbranch_execz .Lib1%=\n"
"ds_read_b64_tr_b16 v[132:133], v182 offset:14400\n"
"v_max_f32 v137, v137, v121\n"
"v_mfma_f32_16x16x32_bf16 v[204:207], a[184:187], v[44:47], v[204:207]\n"
"s_waitcnt lgkmcnt(6)\n"
"ds_read_b64_tr_b16 v[26:27], v236 offset:18440\n"
"v_max_f32 v138, v138, v122\n"
"v_mfma_f32_16x16x32_bf16 v[236:239], a[220:223], v[76:79], 0\n"
"s_add_u32 m0, s34, 0\n"
"v_max_f32 v139, v139, v123\n"
"v_max3_f32 v170, v170, v251, v136\n"
"v_mfma_f32_16x16x32_bf16 v[228:231], a[188:191], v[44:47], v[228:231]\n"
"s_waitcnt vmcnt(7)\n"
"buffer_load_dwordx4 v190, s[8:11], s32 offen lds\n"
"ds_read_b128 v[44:47], v189 offset:1024\n"
"v_mfma_f32_16x16x32_bf16 v[212:215], a[192:195], v[48:51], v[212:215]\n"
"ds_read_b64_tr_b16 v[134:135], v183 offset:14400\n"
"v_max3_f32 v170, v170, v137, v138\n"
"v_mfma_f32_16x16x32_bf16 v[204:207], a[196:199], v[48:51], v[204:207]\n"
"ds_read_b64_tr_b16 v[28:29], v235 offset:12288\n"
"v_max_f32 v140, v140, v124\n"
"v_mfma_f32_16x16x32_bf16 v[228:231], a[200:203], v[48:51], v[228:231]\n"
"v_max_f32 v141, v141, v125\n"
"v_max_f32 v142, v142, v126\n"
"v_mfma_f32_16x16x32_bf16 v[212:215], a[204:207], v[52:55], v[212:215]\n"
"ds_read_b128 v[48:51], v188 offset:2048\n"
"v_max_f32 v143, v143, v127\n"
"v_mfma_f32_16x16x32_bf16 v[204:207], a[208:211], v[52:55], v[204:207]\n"
"ds_read_b64_tr_b16 v[30:31], v235 offset:18432\n"
"v_max3_f32 v170, v170, v139, v140\n"
"v_mfma_f32_16x16x32_bf16 v[232:235], a[216:219], v[76:79], 0\n"
"v_mov_b64 v[68:69], -1\n"
"v_mov_b64 v[70:71], -1\n"
"v_mfma_f32_16x16x32_bf16 v[228:231], a[212:215], v[52:55], v[228:231]\n"
"ds_read_b128 v[76:79], v192 offset:58368\n"
"buffer_store_dwordx4 v[68:71], v192, s[28:31], s4 offen sc1\n"
"v_mfma_f32_16x16x32_bf16 v[208:211], a[0:3], v[32:35], 0\n"
"s_waitcnt lgkmcnt(5)\n"
"ds_read_b128 v[68:71], v192 offset:56320\n"
"ds_read_b128 v[52:55], v189 offset:2048\n"
"v_mfma_f32_16x16x32_bf16 v[216:219], a[4:7], v[32:35], 0\n"
"s_add_u32 m0, s34, 3072\n"
"ds_read_b64_tr_b16 v[112:113], v182 offset:13640\n"
"buffer_load_dwordx4 v191, s[8:11], s32 offen lds\n"
"v_mfma_f32_16x16x32_bf16 v[208:211], a[12:15], v[36:39], v[208:211]\n"
"s_add_u32 s32, s32, s33\n"
"ds_read_b64_tr_b16 v[114:115], v183 offset:13640\n"
"v_max3_f32 v170, v170, v141, v142\n"
"v_mfma_f32_16x16x32_bf16 v[216:219], a[16:19], v[36:39], v[216:219]\n"
"s_add_u32 s4, s37, 6291456\n"
"ds_read_b64_tr_b16 v[116:117], v182 offset:14408\n"
"v_mov_b64 v[254:255], 0\n"
"v_mfma_f32_16x16x32_bf16 v[208:211], a[24:27], v[40:43], v[208:211]\n"
"ds_read_b64_tr_b16 v[118:119], v183 offset:14408\n"
"v_mov_b64 v[98:99], 0\n"
"v_mfma_f32_16x16x32_bf16 v[216:219], a[28:31], v[40:43], v[216:219]\n"
"ds_write_b64 v203, v[60:61] offset:36864\n"
"v_mfma_f32_16x16x32_bf16 v[208:211], a[36:39], v[44:47], v[208:211]\n"
"s_waitcnt lgkmcnt(9)\n"
"v_mov_b64 v[100:101], 0\n"
"v_mov_b64 v[106:107], 0\n"
"v_mfma_f32_16x16x32_bf16 v[216:219], a[40:43], v[44:47], v[216:219]\n"
"s_waitcnt lgkmcnt(6)\n"
"ds_write_b128 v187, v[220:223] offset:64\n"
"v_mfma_f32_16x16x32_bf16 v[232:235], a[228:231], v[68:71], v[232:235]\n"
"s_waitcnt lgkmcnt(6)\n"
"v_mov_b64 v[108:109], 0\n"
"v_max_f32 v171, v170, v143\n"
"v_mfma_f32_16x16x32_bf16 v[236:239], a[232:235], v[68:71], v[236:239]\n"
"s_add_u32 m0, s34, 1024\n"
"v_max_f32 v170, v170, v143\n"
"v_cvt_pk_bf16_f32 v148, v212, v213\n"
"v_mfma_f32_16x16x32_bf16 v[240:243], a[236:239], v[68:71], v[240:243]\n"
"buffer_load_dwordx4 v190, s[8:11], s0 offen lds\n"
"ds_read_b128 v[68:71], v192 offset:59392\n"
"v_mfma_f32_16x16x32_bf16 v[220:223], a[8:11], v[32:35], 0\n"
"ds_read_b64_tr_b16 v[120:121], v182 offset:13576\n"
"ds_read_b64_tr_b16 v[122:123], v183 offset:13576\n"
"v_mfma_f32_16x16x32_bf16 v[220:223], a[20:23], v[36:39], v[220:223]\n"
"ds_read_b64_tr_b16 v[124:125], v182 offset:14344\n"
"v_permlane16_swap_b32 v170, v171\n"
"v_mfma_f32_16x16x32_bf16 v[220:223], a[32:35], v[40:43], v[220:223]\n"
"ds_read_b64_tr_b16 v[126:127], v183 offset:14344\n"
"v_max_f32 v170, v170, v171\n"
"v_mfma_f32_16x16x32_bf16 v[220:223], a[44:47], v[44:47], v[220:223]\n"
"ds_write_b128 v187, v[224:227] offset:128\n"
"v_mfma_f32_16x16x32_bf16 v[208:211], a[48:51], v[48:51], v[208:211]\n"
"v_mov_b32 v171, v170\n"
"v_cvt_pk_bf16_f32 v149, v214, v215\n"
"v_mfma_f32_16x16x32_bf16 v[216:219], a[52:55], v[48:51], v[216:219]\n"
"v_permlane32_swap_b32 v170, v171\n"
"v_mfma_f32_16x16x32_bf16 v[220:223], a[56:59], v[48:51], v[220:223]\n"
"s_add_u32 m0, s34, 4096\n"
"v_max_f32 v170, v170, v171\n"
"v_mul_f32 v170, 0xbe553b95, v170\n"
"v_mfma_f32_16x16x32_bf16 v[208:211], a[60:63], v[52:55], v[208:211]\n"
"buffer_load_dwordx4 v191, s[8:11], s0 offen lds\n"
"v_fmamk_f32 v244, v244, 0x3e553b95, v170\n"
"v_mfma_f32_16x16x32_bf16 v[216:219], a[64:67], v[52:55], v[216:219]\n"
"s_add_u32 s0, s0, s33\n"
"v_fmamk_f32 v245, v245, 0x3e553b95, v170\n"
"v_fmamk_f32 v246, v246, 0x3e553b95, v170\n"
"v_mfma_f32_16x16x32_bf16 v[220:223], a[68:71], v[52:55], v[220:223]\n"
"v_fmamk_f32 v247, v247, 0x3e553b95, v170\n"
"v_fmamk_f32 v248, v248, 0x3e553b95, v170\n"
"v_mfma_f32_16x16x32_bf16 v[224:227], a[72:75], v[32:35], 0\n"
"v_exp_f32 v244, v244\n"
"v_mfma_f32_16x16x32_bf16 v[212:215], a[76:79], v[32:35], 0\n"
"v_exp_f32 v245, v245\n"
"v_mfma_f32_16x16x32_bf16 v[224:227], a[84:87], v[36:39], v[224:227]\n"
"v_exp_f32 v246, v246\n"
"v_mfma_f32_16x16x32_bf16 v[212:215], a[88:91], v[36:39], v[212:215]\n"
"v_exp_f32 v247, v247\n"
"v_mfma_f32_16x16x32_bf16 v[224:227], a[96:99], v[40:43], v[224:227]\n"
"v_cvt_pk_bf16_f32 v82, v244, v246\n"
"v_cvt_pk_bf16_f32 v83, v245, v247\n"
"v_mfma_f32_16x16x32_bf16 v[212:215], a[100:103], v[40:43], v[212:215]\n"
"s_waitcnt vmcnt(7)\n"
"ds_read_b64_tr_b16 v[244:245], v182 offset:40640\n"
"v_add3_u32 v200, v200, s50, -4\n"
"v_mfma_f32_16x16x32_bf16 v[224:227], a[108:111], v[44:47], v[224:227]\n"
"s_add_u32 m0, s34, 2048\n"
"v_cmp_lt_u32 vcc, 64, v200\n"
"v_fmamk_f32 v249, v249, 0x3e553b95, v170\n"
"v_mfma_f32_16x16x32_bf16 v[212:215], a[112:115], v[44:47], v[212:215]\n"
"buffer_load_dwordx4 v190, s[8:11], s1 offen lds\n"
"ds_read_b64_tr_b16 v[246:247], v183 offset:40640\n"
"v_mfma_f32_16x16x32_bf16 v[224:227], a[120:123], v[48:51], v[224:227]\n"
"s_cbranch_vccnz .Lp3%=\n"
".Lr3%=:\n"
"v_mfma_f32_16x16x32_bf16 v[212:215], a[124:127], v[48:51], v[212:215]\n"
"v_fmamk_f32 v250, v250, 0x3e553b95, v170\n"
"v_fmamk_f32 v251, v251, 0x3e553b95, v170\n"
"v_mfma_f32_16x16x32_bf16 v[224:227], a[132:135], v[52:55], v[224:227]\n"
"buffer_load_dword v200, off, s[28:31], s39 sc1\n"
"v_exp_f32 v248, v248\n"
"v_mfma_f32_16x16x32_bf16 v[212:215], a[136:139], v[52:55], v[212:215]\n"
"v_exp_f32 v249, v249\n"
"buffer_load_dwordx4 v[56:59], v192, s[28:31], s38 offen sc1\n"
"v_exp_f32 v250, v250\n"
"v_exp_f32 v251, v251\n"
"v_cvt_pk_bf16_f32 v84, v248, v250\n"
"v_cvt_pk_bf16_f32 v85, v249, v251\n"
"v_cvt_pk_bf16_f32 v150, v204, v205\n"
"v_cvt_pk_bf16_f32 v151, v206, v207\n"
"v_smfmac_f32_16x16x64_bf16 v[252:255], v[82:85], v[128:135], v66\n"
"s_waitcnt lgkmcnt(3)\n"
"ds_read_b64_tr_b16 v[248:249], v182 offset:41408\n"
"v_smfmac_f32_16x16x64_bf16 v[98:101], v[82:85], v[112:119], v66\n"
"s_add_u32 m0, s34, 5120\n"
"s_nop 0\n"
"buffer_load_dwordx4 v191, s[8:11], s1 offen lds\n"
"v_smfmac_f32_16x16x64_bf16 v[106:109], v[82:85], v[120:127], v66\n"
"s_add_u32 s1, s1, s33\n"
"v_mov_b64 v[72:73], -1\n"
"v_mfma_f32_16x16x32_bf16 v[82:85], v[172:175], v[82:85], 0\n"
"ds_read_b64_tr_b16 v[250:251], v183 offset:41408\n"
"ds_read_b64_tr_b16 v[128:129], v182 offset:40648\n"
"v_mfma_f32_16x16x32_bf16 v[204:207], a[80:83], v[32:35], 0\n"
"ds_read_b64_tr_b16 v[130:131], v183 offset:40648\n"
"v_fmamk_f32 v136, v136, 0x3e553b95, v170\n"
"v_mfma_f32_16x16x32_bf16 v[204:207], a[92:95], v[36:39], v[204:207]\n"
"buffer_load_dwordx2 v[60:61], v193, s[28:31], s38 offen offset:1024 sc1\n"
"ds_read_b64_tr_b16 v[132:133], v182 offset:41416\n"
"v_mfma_f32_16x16x32_bf16 v[204:207], a[104:107], v[40:43], v[204:207]\n"
"s_add_u32 s38, s38, 1572864\n"
"ds_read_b64_tr_b16 v[134:135], v183 offset:41416\n"
"v_cvt_pk_bf16_f32 v162, v228, v229\n"
"v_mfma_f32_16x16x32_bf16 v[204:207], a[116:119], v[44:47], v[204:207]\n"
"ds_read_b64_tr_b16 v[112:113], v182 offset:40584\n"
"v_fmamk_f32 v137, v137, 0x3e553b95, v170\n"
"v_mfma_f32_16x16x32_bf16 v[204:207], a[128:131], v[48:51], v[204:207]\n"
"ds_read_b64_tr_b16 v[114:115], v183 offset:40584\n"
"v_cvt_pk_bf16_f32 v163, v230, v231\n"
"v_mfma_f32_16x16x32_bf16 v[204:207], a[140:143], v[52:55], v[204:207]\n"
"buffer_store_dwordx2 v[72:73], v193, s[28:31], s4 offen offset:1024 sc1\n"
"v_mfma_f32_16x16x32_bf16 v[228:231], a[144:147], v[32:35], 0\n"
"ds_read_b128 v[72:75], v192 offset:57344\n"
"ds_read_b64_tr_b16 v[116:117], v182 offset:41352\n"
"v_mfma_f32_16x16x32_bf16 v[228:231], a[156:159], v[36:39], v[228:231]\n"
"ds_read_b64_tr_b16 v[118:119], v183 offset:41352\n"
"v_fmamk_f32 v138, v138, 0x3e553b95, v170\n"
"v_mfma_f32_16x16x32_bf16 v[228:231], a[168:171], v[40:43], v[228:231]\n"
"v_fmamk_f32 v139, v139, 0x3e553b95, v170\n"
"v_fmamk_f32 v140, v140, 0x3e553b95, v170\n"
"v_mfma_f32_16x16x32_bf16 v[228:231], a[180:183], v[44:47], v[228:231]\n"
"v_exp_f32 v136, v136\n"
"v_mfma_f32_16x16x32_bf16 v[228:231], a[192:195], v[48:51], v[228:231]\n"
"v_exp_f32 v137, v137\n"
"v_mfma_f32_16x16x32_bf16 v[228:231], a[204:207], v[52:55], v[228:231]\n"
"v_fmamk_f32 v141, v141, 0x3e553b95, v170\n"
"v_fmamk_f32 v142, v142, 0x3e553b95, v170\n"
"v_fmamk_f32 v143, v143, 0x3e553b95, v170\n"
"v_exp_f32 v138, v138\n"
"v_exp_f32 v139, v139\n"
"v_exp_f32 v140, v140\n"
"v_exp_f32 v141, v141\n"
"v_cvt_pk_bf16_f32 v90, v136, v138\n"
"v_exp_f32 v142, v142\n"
"v_exp_f32 v143, v143\n"
"v_cvt_pk_bf16_f32 v91, v137, v139\n"
"v_cvt_pk_bf16_f32 v92, v140, v142\n"
"v_cvt_pk_bf16_f32 v93, v141, v143\n"
"v_cvt_pk_bf16_f32 v208, v208, v209\n"
"v_cvt_pk_bf16_f32 v209, v210, v211\n"
"v_cvt_pk_bf16_f32 v210, v216, v217\n"
"v_cvt_pk_bf16_f32 v211, v218, v219\n"
"v_mfma_f32_16x16x32_bf16 v[216:219], a[148:151], v[32:35], 0\n"
"s_waitcnt lgkmcnt(0)\n"
"ds_read_b128 v[120:123], v178 offset:27008\n"
"ds_read_b128 v[136:139], v179 offset:27008\n"
"v_mfma_f32_16x16x32_bf16 v[232:235], a[240:243], v[72:75], v[232:235]\n"
"ds_read_b128 v[124:127], v180\n"
"v_cvt_pk_bf16_f32 v220, v220, v221\n"
"v_mfma_f32_16x16x32_bf16 v[236:239], a[244:247], v[72:75], v[236:239]\n"
"ds_read_b128 v[140:143], v181\n"
"v_cvt_pk_bf16_f32 v221, v222, v223\n"
"v_mfma_f32_16x16x32_bf16 v[240:243], a[248:251], v[72:75], v[240:243]\n"
"ds_read_b128 v[72:75], v192 offset:60416\n"
"v_cvt_pk_bf16_f32 v224, v224, v225\n"
"v_mfma_f32_16x16x32_bf16 v[232:235], a[252:255], v[76:79], v[232:235]\n"
"v_cvt_pk_bf16_f32 v225, v226, v227\n"
"v_cvt_pk_bf16_f32 v226, v212, v213\n"
"v_mfma_f32_16x16x32_bf16 v[236:239], v[0:3], v[76:79], v[236:239]\n"
"v_cvt_pk_bf16_f32 v227, v214, v215\n"
"v_cvt_pk_bf16_f32 v222, v204, v205\n"
"v_mfma_f32_16x16x32_bf16 v[240:243], v[4:7], v[76:79], v[240:243]\n"
"v_cvt_pk_bf16_f32 v223, v206, v207\n"
"v_mfma_f32_16x16x32_bf16 v[232:235], v[8:11], v[68:71], v[232:235]\n"
"v_mfma_f32_16x16x32_bf16 v[240:243], v[16:19], v[68:71], v[240:243]\n"
"s_waitcnt vmcnt(11) lgkmcnt(4)\n"
"v_or_b32 v201, 7, s6\n"
"v_mfma_f32_16x16x32_bf16 v[236:239], v[12:15], v[68:71], v[236:239]\n"
"s_waitcnt lgkmcnt(3)\n"
"s_mov_b64 exec, 1\n"
"buffer_store_dword v201, off, s[28:31], s40 sc1\n"
"s_mov_b64 exec, -1\n"
"v_smfmac_f32_16x16x64_bf16 v[252:255], v[90:93], v[244:251], v66\n"
"s_waitcnt lgkmcnt(1)\n"
"ds_read_b128 v[244:247], v180 offset:13504\n"
"v_smfmac_f32_16x16x64_bf16 v[98:101], v[90:93], v[128:135], v66\n"
"s_waitcnt lgkmcnt(1)\n"
"ds_read_b128 v[128:131], v181 offset:13504\n"
"v_mfma_f32_16x16x32_bf16 v[232:235], v[20:23], v[72:75], v[232:235]\n"
"v_mfma_f32_16x16x32_bf16 v[240:243], v[28:31], v[72:75], v[240:243]\n"
"s_nop 0\n"
"v_cvt_pk_bf16_f32 v252, v252, v253\n"
"v_mfma_f32_16x16x32_bf16 v[236:239], v[24:27], v[72:75], v[236:239]\n"
"v_cvt_pk_bf16_f32 v253, v254, v255\n"
"v_cvt_pk_bf16_f32 v98, v98, v99\n"
"v_smfmac_f32_16x16x64_bf16 v[106:109], v[90:93], v[112:119], v66\n"
"v_cvt_pk_bf16_f32 v99, v100, v101\n"
"v_mfma_f32_16x16x32_bf16 v[90:93], v[172:175], v[90:93], v[82:85]\n"
"v_cvt_pk_bf16_f32 v232, v232, v233\n"
"v_cvt_pk_bf16_f32 v233, v234, v235\n"
"v_mfma_f32_16x16x32_bf16 v[120:123], v[120:123], v[152:155], 0\n"
"v_cvt_pk_bf16_f32 v240, v240, v241\n"
"v_cvt_pk_bf16_f32 v234, v236, v237\n"
"v_mfma_f32_16x16x32_bf16 v[136:139], v[136:139], v[152:155], 0\n"
"s_waitcnt lgkmcnt(1)\n"
"v_cvt_pk_bf16_f32 v235, v238, v239\n"
"v_cvt_pk_bf16_f32 v241, v242, v243\n"
"v_mfma_f32_16x16x32_bf16 v[124:127], v[124:127], v[152:155], 0\n"
"s_waitcnt lgkmcnt(0)\n"
"ds_write_b128 v196, v[232:235] offset:30720\n"
"v_mfma_f32_16x16x32_bf16 v[140:143], v[140:143], v[152:155], 0\n"
"v_rcp_f32 v171, v90\n"
"v_mfma_f32_16x16x32_bf16 v[244:247], v[244:247], v[152:155], 0\n"
"v_cvt_pk_bf16_f32 v171, v171, v171\n"
"v_and_b32 v64, v171, v176\n"
"v_mfma_f32_16x16x32_bf16 v[128:131], v[128:131], v[152:155], 0\n"
"ds_write_b64 v197, v[240:241] offset:30720\n"
"v_mfma_f32_16x16x32_bf16 v[216:219], a[160:163], v[36:39], v[216:219]\n"
"v_and_b32 v65, v171, v177\n"
"v_cvt_pk_bf16_f32 v106, v106, v107\n"
"v_mfma_f32_16x16x32_bf16 v[216:219], a[172:175], v[40:43], v[216:219]\n"
"ds_read_b64 v[82:83], v178 offset:27072\n"
"v_cvt_pk_bf16_f32 v107, v108, v109\n"
"v_mfma_f32_16x16x16_bf16 v[252:255], v[252:253], v[64:65], 0\n"
"ds_read_b64 v[84:85], v179 offset:27072\n"
"ds_read_b64 v[90:91], v180 offset:64\n"
"v_mfma_f32_16x16x16_bf16 v[98:101], v[98:99], v[64:65], 0\n"
"ds_read_b64 v[92:93], v181 offset:64\n"
"s_mov_b64 exec, s[44:45]\n"
"v_add_u32 v179, 0xc0, v179\n"
"s_mov_b64 exec, -1\n"
"v_mfma_f32_16x16x16_bf16 v[106:109], v[106:107], v[64:65], 0\n"
"v_cvt_pk_bf16_f32 v252, v252, v253\n"
"v_cvt_pk_bf16_f32 v253, v254, v255\n"
"v_mfma_f32_16x16x32_bf16 v[216:219], a[184:187], v[44:47], v[216:219]\n"
"v_cvt_pk_bf16_f32 v254, v98, v99\n"
"v_cvt_pk_bf16_f32 v255, v100, v101\n"
"v_mfma_f32_16x16x32_bf16 v[216:219], a[196:199], v[48:51], v[216:219]\n"
"s_waitcnt lgkmcnt(4)\n"
"buffer_store_dwordx4 v[252:255], v192, s[28:31], s37 offen sc1\n"
"v_mfma_f32_16x16x32_bf16 v[216:219], a[208:211], v[52:55], v[216:219]\n"
"s_waitcnt lgkmcnt(3)\n"
"v_cvt_pk_bf16_f32 v106, v106, v107\n"
"v_cvt_pk_bf16_f32 v107, v108, v109\n"
"v_mov_b64 v[252:253], 0\n"
"v_mfma_f32_16x16x32_bf16 v[120:123], v[80:83], v[164:167], v[120:123]\n"
"s_waitcnt lgkmcnt(2)\n"
"buffer_store_dwordx2 v[106:107], v193, s[28:31], s37 offen offset:1024 sc1\n"
"v_mfma_f32_16x16x32_bf16 v[136:139], v[84:87], v[166:169], v[136:139]\n"
"s_waitcnt lgkmcnt(1)\n"
"ds_read_b64 v[98:99], v180 offset:13568\n"
"ds_read_b64 v[100:101], v181 offset:13568\n"
"v_mfma_f32_16x16x32_bf16 v[124:127], v[88:91], v[164:167], v[124:127]\n"
"s_waitcnt lgkmcnt(2)\n"
"ds_read_b64_tr_b16 v[112:113], v182 offset:27136\n"
"v_mov_b64 v[254:255], 0\n"
"v_mfma_f32_16x16x32_bf16 v[140:143], v[92:95], v[166:169], v[140:143]\n"
"s_barrier\n"
"s_add_u32 s37, s37, 1572864\n"
"s_add_u32 s4, s37, 6291456\n"
"ds_write_b128 v186, v[208:211] offset:6752\n"
"v_mfma_f32_16x16x32_bf16 v[208:211], a[152:155], v[32:35], 0\n"
"v_max_f32 v120, v120, v136\n"
"v_max_f32 v121, v121, v137\n"
"v_mfma_f32_16x16x32_bf16 v[208:211], a[164:167], v[36:39], v[208:211]\n"
"v_max_f32 v122, v122, v138\n"
"v_max_f32 v123, v123, v139\n"
"v_mfma_f32_16x16x32_bf16 v[208:211], a[176:179], v[40:43], v[208:211]\n"
"ds_read_b128 v[32:35], v188 offset:6144\n"
"ds_read_b128 v[36:39], v189 offset:6144\n"
"v_mfma_f32_16x16x32_bf16 v[208:211], a[188:191], v[44:47], v[208:211]\n"
"s_waitcnt lgkmcnt(5)\n"
"ds_read_b128 v[40:43], v188 offset:7168\n"
"v_max3_f32 v170, v120, v121, v122\n"
"v_mfma_f32_16x16x32_bf16 v[244:247], v[96:99], v[164:167], v[244:247]\n"
"s_waitcnt lgkmcnt(5)\n"
"ds_read_b128 v[44:47], v189 offset:7168\n"
"v_max_f32 v124, v124, v140\n"
"v_mfma_f32_16x16x32_bf16 v[128:131], v[100:103], v[166:169], v[128:131]\n"
"ds_read_b128 v[248:251], v180 offset:27008\n"
"v_max_f32 v125, v125, v141\n"
"v_mfma_f32_16x16x32_bf16 v[208:211], a[200:203], v[48:51], v[208:211]\n"
"ds_read_b128 v[48:51], v188 offset:8192\n"
"v_max_f32 v126, v126, v142\n"
"v_mfma_f32_16x16x32_bf16 v[208:211], a[212:215], v[52:55], v[208:211]\n"
"ds_read_b128 v[52:55], v189 offset:8192\n"
"v_max_f32 v127, v127, v143\n"
"v_max3_f32 v170, v170, v123, v124\n"
"v_max3_f32 v170, v170, v125, v126\n"
"v_max_f32 v244, v244, v128\n"
"v_max_f32 v245, v245, v129\n"
"v_max_f32 v246, v246, v130\n"
"v_max_f32 v247, v247, v131\n"
"v_max3_f32 v170, v170, v127, v244\n"
"v_max3_f32 v170, v170, v245, v246\n"
"v_mov_b64 v[98:99], 0\n"
"v_mov_b64 v[100:101], 0\n"
"s_waitcnt lgkmcnt(6)\n"
"v_mfma_f32_16x16x32_bf16 v[212:215], a[0:3], v[32:35], 0\n"
"s_waitcnt lgkmcnt(5)\n"
"ds_read_b128 v[132:135], v181 offset:27008\n"
"ds_read_b64 v[106:107], v180 offset:27072\n"
"v_mfma_f32_16x16x32_bf16 v[204:207], a[4:7], v[32:35], 0\n"
"s_add_u32 m0, s34, 6144\n"
"ds_read_b64 v[108:109], v181 offset:27072\n"
"buffer_load_dwordx4 v190, s[8:11], s32 offen lds\n"
"v_mfma_f32_16x16x32_bf16 v[212:215], a[12:15], v[36:39], v[212:215]\n"
"s_waitcnt lgkmcnt(7)\n"
"ds_read_b128 v[72:75], v202 offset:30720\n"
"v_mfma_f32_16x16x32_bf16 v[204:207], a[16:19], v[36:39], v[204:207]\n"
"s_waitcnt lgkmcnt(6)\n"
"ds_read_b64_tr_b16 v[114:115], v183 offset:27136\n"
"v_mfma_f32_16x16x32_bf16 v[248:251], v[248:251], v[152:155], 0\n"
"ds_read_b64_tr_b16 v[116:117], v182 offset:27904\n"
"v_mfma_f32_16x16x32_bf16 v[212:215], a[24:27], v[40:43], v[212:215]\n"
"ds_read_b64_tr_b16 v[118:119], v183 offset:27904\n"
"v_mfma_f32_16x16x32_bf16 v[204:207], a[28:31], v[40:43], v[204:207]\n"
"v_mov_b64 v[76:77], -1\n"
"v_mov_b64 v[78:79], -1\n"
"v_mfma_f32_16x16x32_bf16 v[212:215], a[36:39], v[44:47], v[212:215]\n"
"s_waitcnt lgkmcnt(8)\n"
"buffer_store_dwordx4 v[76:79], v192, s[28:31], s4 offen sc1\n"
"v_mfma_f32_16x16x32_bf16 v[204:207], a[40:43], v[44:47], v[204:207]\n"
"s_waitcnt lgkmcnt(5)\n"
"ds_read_b128 v[76:79], v202 offset:34816\n"
"ds_read_b64_tr_b16 v[136:137], v182 offset:27144\n"
"v_mfma_f32_16x16x32_bf16 v[132:135], v[132:135], v[152:155], 0\n"
"s_waitcnt lgkmcnt(6)\n"
"ds_read_b64_tr_b16 v[138:139], v183 offset:27144\n"
"v_cvt_pk_bf16_f32 v152, v228, v229\n"
"v_mfma_f32_16x16x32_bf16 v[248:251], v[104:107], v[164:167], v[248:251]\n"
"s_waitcnt lgkmcnt(6)\n"
"ds_read_b64_tr_b16 v[140:141], v182 offset:27912\n"
"buffer_store_dwordx4 v[72:75], v194, s[24:27], s36 offen sc1\n"
"v_mfma_f32_16x16x32_bf16 v[132:135], v[108:111], v[166:169], v[132:135]\n"
"s_add_u32 s4, s37, 6291456\n"
"ds_read_b128 v[72:75], v192 offset:37888\n"
"ds_read_b64_tr_b16 v[142:143], v183 offset:27912\n"
"v_mfma_f32_16x16x32_bf16 v[212:215], a[48:51], v[48:51], v[212:215]\n"
"s_add_u32 m0, s34, 9216\n"
"ds_read_b64_tr_b16 v[128:129], v182 offset:27080\n"
"v_mov_b64 v[106:107], 0\n"
"v_mfma_f32_16x16x32_bf16 v[204:207], a[52:55], v[48:51], v[204:207]\n"
"buffer_load_dwordx4 v191, s[8:11], s32 offen lds\n"
"ds_write_b128 v186, v[220:223] offset:6816\n"
"v_mfma_f32_16x16x32_bf16 v[220:223], a[8:11], v[32:35], 0\n"
"s_waitcnt lgkmcnt(7)\n"
"v_max_f32 v248, v248, v132\n"
"v_max_f32 v249, v249, v133\n"
"v_mfma_f32_16x16x32_bf16 v[220:223], a[20:23], v[36:39], v[220:223]\n"
"s_add_u32 s32, s32, s33\n"
"v_max_f32 v250, v250, v134\n"
"v_max_f32 v251, v251, v135\n"
"v_mfma_f32_16x16x32_bf16 v[220:223], a[32:35], v[40:43], v[220:223]\n"
"buffer_store_dwordx4 v[76:79], v195, s[24:27], s36 offen sc1\n"
"v_mfma_f32_16x16x32_bf16 v[220:223], a[44:47], v[44:47], v[220:223]\n"
"s_waitcnt vmcnt(11)\n"
"v_add3_u32 v200, v200, s50, -5\n"
"v_cmp_lt_u32 vcc, 64, v200\n"
"v_mfma_f32_16x16x32_bf16 v[220:223], a[56:59], v[48:51], v[220:223]\n"
"s_add_u32 s36, s36, s33\n"
"ds_read_b128 v[76:79], v192 offset:38912\n"
"ds_write_b128 v202, v[56:59] offset:43008\n"
"v_mfma_f32_16x16x32_bf16 v[212:215], a[60:63], v[52:55], v[212:215]\n"
"s_cbranch_vccnz .Lp4%=\n"
".Lr4%=:\n"
"v_mfma_f32_16x16x32_bf16 v[204:207], a[64:67], v[52:55], v[204:207]\n"
"s_add_u32 m0, s34, 7168\n"
"buffer_load_dwordx4 v[56:59], v192, s[28:31], s38 offen sc1\n"
"ds_read_b64_tr_b16 v[130:131], v183 offset:27080\n"
"v_mfma_f32_16x16x32_bf16 v[220:223], a[68:71], v[52:55], v[220:223]\n"
"v_max3_f32 v170, v170, v247, v248\n"
"v_max3_f32 v170, v170, v249, v250\n"
"v_mov_b64 v[108:109], 0\n"
"ds_read_b64_tr_b16 v[132:133], v182 offset:27848\n"
"ds_read_b64_tr_b16 v[134:135], v183 offset:27848\n"
"ds_write_b128 v186, v[224:227] offset:6880\n"
"v_mfma_f32_16x16x32_bf16 v[224:227], a[72:75], v[32:35], 0\n"
"s_waitcnt vmcnt(10)\n"
"buffer_load_dwordx4 v190, s[8:11], s0 offen lds\n"
"v_max_f32 v171, v170, v251\n"
"v_mfma_f32_16x16x32_bf16 v[224:227], a[84:87], v[36:39], v[224:227]\n"
"v_max_f32 v170, v170, v251\n"
"v_cvt_pk_bf16_f32 v153, v230, v231\n"
"v_mfma_f32_16x16x32_bf16 v[228:231], a[76:79], v[32:35], 0\n"
"ds_write_b64 v203, v[60:61] offset:43008\n"
"v_mfma_f32_16x16x32_bf16 v[228:231], a[88:91], v[36:39], v[228:231]\n"
"buffer_load_dwordx2 v[60:61], v193, s[28:31], s38 offen offset:1024 sc1\n"
"v_permlane16_swap_b32 v170, v171\n"
"v_mfma_f32_16x16x32_bf16 v[224:227], a[96:99], v[40:43], v[224:227]\n"
"s_add_u32 s38, s38, 1572864\n"
"v_max_f32 v170, v170, v171\n"
"v_mov_b32 v171, v170\n"
"v_mfma_f32_16x16x32_bf16 v[228:231], a[100:103], v[40:43], v[228:231]\n"
"s_nop 0\n"
"v_permlane32_swap_b32 v170, v171\n"
"v_mfma_f32_16x16x32_bf16 v[224:227], a[108:111], v[44:47], v[224:227]\n"
"v_max_f32 v170, v170, v171\n"
"v_mul_f32 v170, 0xbe553b95, v170\n"
"v_mfma_f32_16x16x32_bf16 v[228:231], a[112:115], v[44:47], v[228:231]\n"
"s_add_u32 m0, s34, 10240\n"
"v_fmamk_f32 v120, v120, 0x3e553b95, v170\n"
"v_fmamk_f32 v121, v121, 0x3e553b95, v170\n"
"v_mfma_f32_16x16x32_bf16 v[224:227], a[120:123], v[48:51], v[224:227]\n"
"buffer_load_dwordx4 v191, s[8:11], s0 offen lds\n"
"v_fmamk_f32 v122, v122, 0x3e553b95, v170\n"
"v_mfma_f32_16x16x32_bf16 v[228:231], a[124:127], v[48:51], v[228:231]\n"
"s_add_u32 s0, s0, s33\n"
"v_fmamk_f32 v123, v123, 0x3e553b95, v170\n"
"v_fmamk_f32 v124, v124, 0x3e553b95, v170\n"
"v_mfma_f32_16x16x32_bf16 v[224:227], a[132:135], v[52:55], v[224:227]\n"
"v_exp_f32 v120, v120\n"
"v_mfma_f32_16x16x32_bf16 v[228:231], a[136:139], v[52:55], v[228:231]\n"
"v_exp_f32 v121, v121\n"
"v_exp_f32 v122, v122\n"
"v_exp_f32 v123, v123\n"
"v_cvt_pk_bf16_f32 v82, v120, v122\n"
"v_cvt_pk_bf16_f32 v83, v121, v123\n"
"v_fmamk_f32 v125, v125, 0x3e553b95, v170\n"
"v_fmamk_f32 v126, v126, 0x3e553b95, v170\n"
"v_fmamk_f32 v127, v127, 0x3e553b95, v170\n"
"v_exp_f32 v124, v124\n"
"v_exp_f32 v125, v125\n"
"v_exp_f32 v126, v126\n"
"s_add_u32 m0, s34, 8192\n"
"v_exp_f32 v127, v127\n"
"buffer_load_dwordx4 v190, s[8:11], s1 offen lds\n"
"v_cvt_pk_bf16_f32 v84, v124, v126\n"
"v_cvt_pk_bf16_f32 v85, v125, v127\n"
"v_cvt_pk_bf16_f32 v154, v216, v217\n"
"v_cvt_pk_bf16_f32 v155, v218, v219\n"
"v_smfmac_f32_16x16x64_bf16 v[252:255], v[82:85], v[112:119], v66\n"
"s_waitcnt lgkmcnt(2)\n"
"ds_read_b64_tr_b16 v[120:121], v182 offset:54144\n"
"v_smfmac_f32_16x16x64_bf16 v[98:101], v[82:85], v[136:143], v66\n"
"ds_read_b64_tr_b16 v[122:123], v183 offset:54144\n"
"v_smfmac_f32_16x16x64_bf16 v[106:109], v[82:85], v[128:135], v66\n"
"v_mov_b64 v[68:69], -1\n"
"v_mfma_f32_16x16x32_bf16 v[82:85], v[172:175], v[82:85], 0\n"
"buffer_store_dwordx2 v[68:69], v193, s[28:31], s4 offen offset:1024 sc1\n"
"v_mfma_f32_16x16x32_bf16 v[216:219], a[80:83], v[32:35], 0\n"
"ds_read_b128 v[68:71], v192 offset:36864\n"
"ds_read_b64_tr_b16 v[124:125], v182 offset:54912\n"
"v_mfma_f32_16x16x32_bf16 v[216:219], a[92:95], v[36:39], v[216:219]\n"
"ds_read_b64_tr_b16 v[126:127], v183 offset:54912\n"
"v_fmamk_f32 v244, v244, 0x3e553b95, v170\n"
"v_mfma_f32_16x16x32_bf16 v[216:219], a[104:107], v[40:43], v[216:219]\n"
"s_add_u32 m0, s34, 11264\n"
"buffer_load_dword v200, off, s[28:31], s39 sc1\n"
"ds_read_b64_tr_b16 v[112:113], v182 offset:54152\n"
"v_mfma_f32_16x16x32_bf16 v[216:219], a[116:119], v[44:47], v[216:219]\n"
"ds_read_b64_tr_b16 v[114:115], v183 offset:54152\n"
"buffer_load_dwordx4 v191, s[8:11], s1 offen lds\n"
"v_mfma_f32_16x16x32_bf16 v[216:219], a[128:131], v[48:51], v[216:219]\n"
"s_add_u32 s1, s1, s33\n"
"ds_read_b64_tr_b16 v[116:117], v182 offset:54920\n"
"v_fmamk_f32 v245, v245, 0x3e553b95, v170\n"
"v_mfma_f32_16x16x32_bf16 v[216:219], a[140:143], v[52:55], v[216:219]\n"
"ds_read_b64_tr_b16 v[118:119], v183 offset:54920\n"
"v_cvt_pk_bf16_f32 v166, v208, v209\n"
"v_fmamk_f32 v246, v246, 0x3e553b95, v170\n"
"v_cvt_pk_bf16_f32 v167, v210, v211\n"
"v_mfma_f32_16x16x32_bf16 v[208:211], a[144:147], v[32:35], 0\n"
"ds_read_b64_tr_b16 v[136:137], v182 offset:54088\n"
"ds_read_b64_tr_b16 v[138:139], v183 offset:54088\n"
"v_mfma_f32_16x16x32_bf16 v[208:211], a[156:159], v[36:39], v[208:211]\n"
"s_waitcnt lgkmcnt(8)\n"
"ds_read_b64_tr_b16 v[140:141], v182 offset:54856\n"
"v_fmamk_f32 v247, v247, 0x3e553b95, v170\n"
"v_mfma_f32_16x16x32_bf16 v[232:235], a[216:219], v[68:71], 0\n"
"ds_read_b64_tr_b16 v[142:143], v183 offset:54856\n"
"v_fmamk_f32 v248, v248, 0x3e553b95, v170\n"
"v_mfma_f32_16x16x32_bf16 v[236:239], a[220:223], v[68:71], 0\n"
"v_exp_f32 v244, v244\n"
"v_mfma_f32_16x16x32_bf16 v[240:243], a[224:227], v[68:71], 0\n"
"ds_read_b128 v[68:71], v192 offset:39936\n"
"v_fmamk_f32 v249, v249, 0x3e553b95, v170\n"
"v_mfma_f32_16x16x32_bf16 v[232:235], a[228:231], v[72:75], v[232:235]\n"
"v_exp_f32 v245, v245\n"
"v_mfma_f32_16x16x32_bf16 v[236:239], a[232:235], v[72:75], v[236:239]\n"
"v_exp_f32 v246, v246\n"
"v_mfma_f32_16x16x32_bf16 v[240:243], a[236:239], v[72:75], v[240:243]\n"
"ds_read_b128 v[72:75], v192 offset:40960\n"
"v_fmamk_f32 v250, v250, 0x3e553b95, v170\n"
"v_mfma_f32_16x16x32_bf16 v[232:235], a[240:243], v[76:79], v[232:235]\n"
"v_fmamk_f32 v251, v251, 0x3e553b95, v170\n"
"v_cvt_pk_bf16_f32 v90, v244, v246\n"
"v_mfma_f32_16x16x32_bf16 v[236:239], a[244:247], v[76:79], v[236:239]\n"
"v_exp_f32 v247, v247\n"
"v_mfma_f32_16x16x32_bf16 v[240:243], a[248:251], v[76:79], v[240:243]\n"
"ds_read_b128 v[76:79], v192 offset:41984\n"
"v_cvt_pk_bf16_f32 v91, v245, v247\n"
"v_mfma_f32_16x16x32_bf16 v[208:211], a[168:171], v[40:43], v[208:211]\n"
"v_exp_f32 v248, v248\n"
"v_mfma_f32_16x16x32_bf16 v[208:211], a[180:183], v[44:47], v[208:211]\n"
"s_waitcnt lgkmcnt(2)\n"
"v_exp_f32 v249, v249\n"
"v_mfma_f32_16x16x32_bf16 v[232:235], a[252:255], v[68:71], v[232:235]\n"
"v_exp_f32 v250, v250\n"
"v_mfma_f32_16x16x32_bf16 v[240:243], v[4:7], v[68:71], v[240:243]\n"
"v_exp_f32 v251, v251\n"
"v_mfma_f32_16x16x32_bf16 v[236:239], v[0:3], v[68:71], v[236:239]\n"
"s_waitcnt lgkmcnt(1)\n"
"v_cvt_pk_bf16_f32 v92, v248, v250\n"
"v_cvt_pk_bf16_f32 v93, v249, v251\n"
"v_mfma_f32_16x16x32_bf16 v[232:235], v[8:11], v[72:75], v[232:235]\n"
"ds_read_b128 v[128:131], v180\n"
"ds_read_b128 v[244:247], v181\n"
"v_mfma_f32_16x16x32_bf16 v[240:243], v[16:19], v[72:75], v[240:243]\n"
"ds_read_b128 v[132:135], v180 offset:13504\n"
"v_cvt_pk_bf16_f32 v212, v212, v213\n"
"v_mfma_f32_16x16x32_bf16 v[236:239], v[12:15], v[72:75], v[236:239]\n"
"s_waitcnt vmcnt(13) lgkmcnt(3)\n"
"ds_read_b128 v[248:251], v181 offset:13504\n"
"v_cvt_pk_bf16_f32 v213, v214, v215\n"
"v_mfma_f32_16x16x32_bf16 v[232:235], v[20:23], v[76:79], v[232:235]\n"
"v_or_b32 v201, 8, s6\n"
"v_cvt_pk_bf16_f32 v214, v204, v205\n"
"v_mfma_f32_16x16x32_bf16 v[240:243], v[28:31], v[76:79], v[240:243]\n"
"s_mov_b64 exec, 1\n"
"buffer_store_dword v201, off, s[28:31], s40 sc1\n"
"s_mov_b64 exec, -1\n"
"v_mfma_f32_16x16x32_bf16 v[236:239], v[24:27], v[76:79], v[236:239]\n"
"v_cvt_pk_bf16_f32 v215, v206, v207\n"
"v_cvt_pk_bf16_f32 v232, v232, v233\n"
"v_smfmac_f32_16x16x64_bf16 v[252:255], v[90:93], v[120:127], v66\n"
"v_cvt_pk_bf16_f32 v233, v234, v235\n"
"v_smfmac_f32_16x16x64_bf16 v[98:101], v[90:93], v[112:119], v66\n"
"s_waitcnt lgkmcnt(3)\n"
"v_cvt_pk_bf16_f32 v240, v240, v241\n"
"v_smfmac_f32_16x16x64_bf16 v[106:109], v[90:93], v[136:143], v66\n"
"s_waitcnt lgkmcnt(2)\n"
"v_cvt_pk_bf16_f32 v234, v236, v237\n"
"v_mfma_f32_16x16x32_bf16 v[90:93], v[172:175], v[90:93], v[82:85]\n"
"s_waitcnt lgkmcnt(1)\n"
"v_cvt_pk_bf16_f32 v235, v238, v239\n"
"v_cvt_pk_bf16_f32 v241, v242, v243\n"
"v_mfma_f32_16x16x32_bf16 v[128:131], v[128:131], v[144:147], 0\n"
"s_waitcnt lgkmcnt(0)\n"
"ds_write_b128 v196, v[232:235] offset:55296\n"
"v_mfma_f32_16x16x32_bf16 v[244:247], v[244:247], v[144:147], 0\n"
".Lic5%=:\n"
"s_cbranch_execz .Lib1%=\n"
"v_cvt_pk_bf16_f32 v252, v252, v253\n"
"v_cvt_pk_bf16_f32 v253, v254, v255\n"
"v_mfma_f32_16x16x32_bf16 v[132:135], v[132:135], v[144:147], 0\n"
"v_rcp_f32 v171, v90\n"
"v_mfma_f32_16x16x32_bf16 v[248:251], v[248:251], v[144:147], 0\n"
"ds_write_b64 v197, v[240:241] offset:55296\n"
"v_mfma_f32_16x16x32_bf16 v[204:207], a[148:151], v[32:35], 0\n"
"v_cvt_pk_bf16_f32 v171, v171, v171\n"
"v_and_b32 v64, v171, v176\n"
"v_mfma_f32_16x16x32_bf16 v[204:207], a[160:163], v[36:39], v[204:207]\n"
"v_and_b32 v65, v171, v177\n"
"v_cvt_pk_bf16_f32 v98, v98, v99\n"
"v_mfma_f32_16x16x32_bf16 v[204:207], a[172:175], v[40:43], v[204:207]\n"
"v_cvt_pk_bf16_f32 v99, v100, v101\n"
"v_cvt_pk_bf16_f32 v106, v106, v107\n"
"v_mfma_f32_16x16x16_bf16 v[252:255], v[252:253], v[64:65], 0\n"
"v_cvt_pk_bf16_f32 v107, v108, v109\n"
"v_cvt_pk_bf16_f32 v220, v220, v221\n"
"v_mfma_f32_16x16x16_bf16 v[98:101], v[98:99], v[64:65], 0\n"
"ds_read_b64 v[82:83], v180 offset:64\n"
"ds_read_b64 v[84:85], v181 offset:64\n"
"v_mfma_f32_16x16x16_bf16 v[106:109], v[106:107], v[64:65], 0\n"
"ds_read_b64 v[90:91], v180 offset:13568\n"
"v_cvt_pk_bf16_f32 v221, v222, v223\n"
"v_mfma_f32_16x16x32_bf16 v[204:207], a[184:187], v[44:47], v[204:207]\n"
"s_waitcnt lgkmcnt(3)\n"
"v_cvt_pk_bf16_f32 v252, v252, v253\n"
"v_cvt_pk_bf16_f32 v253, v254, v255\n"
"v_mfma_f32_16x16x32_bf16 v[208:211], a[192:195], v[48:51], v[208:211]\n"
"s_barrier\n"
"v_mfma_f32_16x16x32_bf16 v[204:207], a[196:199], v[48:51], v[204:207]\n"
"v_cvt_pk_bf16_f32 v254, v98, v99\n"
"v_cvt_pk_bf16_f32 v255, v100, v101\n"
"v_mfma_f32_16x16x32_bf16 v[208:211], a[204:207], v[52:55], v[208:211]\n"
"buffer_store_dwordx4 v[252:255], v192, s[28:31], s37 offen sc1\n"
"v_mfma_f32_16x16x32_bf16 v[204:207], a[208:211], v[52:55], v[204:207]\n"
"v_cvt_pk_bf16_f32 v106, v106, v107\n"
"v_cvt_pk_bf16_f32 v107, v108, v109\n"
"v_cvt_pk_bf16_f32 v224, v224, v225\n"
"v_cvt_pk_bf16_f32 v225, v226, v227\n"
"v_cvt_pk_bf16_f32 v226, v228, v229\n"
"v_cvt_pk_bf16_f32 v227, v230, v231\n"
"v_cvt_pk_bf16_f32 v222, v216, v217\n"
"v_cvt_pk_bf16_f32 v223, v218, v219\n"
"v_mov_b64 v[252:253], 0\n"
"v_mov_b64 v[254:255], 0\n"
"s_waitcnt lgkmcnt(1)\n"
"v_mfma_f32_16x16x32_bf16 v[128:131], v[80:83], v[156:159], v[128:131]\n"
"s_waitcnt lgkmcnt(0)\n"
"buffer_store_dwordx2 v[106:107], v193, s[28:31], s37 offen offset:1024 sc1\n"
"v_mfma_f32_16x16x32_bf16 v[244:247], v[84:87], v[158:161], v[244:247]\n"
"s_add_u32 s37, s37, 1572864\n"
"ds_write_b128 v186, v[212:215] offset:20256\n"
"v_mfma_f32_16x16x32_bf16 v[132:135], v[88:91], v[156:159], v[132:135]\n"
"s_add_u32 s4, s37, 6291456\n"
"v_mfma_f32_16x16x32_bf16 v[212:215], a[152:155], v[32:35], 0\n"
"ds_read_b128 v[32:35], v188\n"
"v_mfma_f32_16x16x32_bf16 v[212:215], a[164:167], v[36:39], v[212:215]\n"
"ds_read_b128 v[36:39], v189\n"
"ds_read_b64 v[92:93], v181 offset:13568\n"
"v_mfma_f32_16x16x32_bf16 v[212:215], a[176:179], v[40:43], v[212:215]\n"
"ds_read_b128 v[40:43], v188 offset:1024\n"
"v_max_f32 v128, v128, v244\n"
"v_mfma_f32_16x16x32_bf16 v[212:215], a[188:191], v[44:47], v[212:215]\n"
"ds_read_b128 v[44:47], v189 offset:1024\n"
"v_max_f32 v129, v129, v245\n"
"v_mfma_f32_16x16x32_bf16 v[212:215], a[200:203], v[48:51], v[212:215]\n"
"ds_read_b128 v[48:51], v188 offset:2048\n"
"v_max_f32 v130, v130, v246\n"
"v_mfma_f32_16x16x32_bf16 v[212:215], a[212:215], v[52:55], v[212:215]\n"
"ds_read_b128 v[52:55], v189 offset:2048\n"
"v_max_f32 v131, v131, v247\n"
"v_max3_f32 v170, v128, v129, v130\n"
"ds_read_b128 v[120:123], v180 offset:27008\n"
"ds_read_b128 v[112:115], v181 offset:27008\n"
"ds_read_b64 v[98:99], v180 offset:27072\n"
"s_waitcnt lgkmcnt(9)\n"
"v_mfma_f32_16x16x32_bf16 v[228:231], a[0:3], v[32:35], 0\n"
"s_waitcnt lgkmcnt(8)\n"
"ds_read_b64 v[100:101], v181 offset:27072\n"
"s_add_u32 m0, s34, 0\n"
"v_mfma_f32_16x16x32_bf16 v[216:219], a[4:7], v[32:35], 0\n"
"s_waitcnt vmcnt(10) lgkmcnt(8)\n"
"buffer_load_dwordx4 v190, s[8:11], s32 offen lds\n"
"ds_write_b128 v202, v[56:59] offset:30720\n"
"v_mfma_f32_16x16x32_bf16 v[248:251], v[92:95], v[158:161], v[248:251]\n"
"s_add_u32 s32, s32, s33\n"
"v_mfma_f32_16x16x32_bf16 v[228:231], a[12:15], v[36:39], v[228:231]\n"
"s_waitcnt lgkmcnt(8)\n"
"buffer_load_dwordx4 v[56:59], v192, s[28:31], s38 offen sc1\n"
"ds_read_b128 v[124:127], v178\n"
"v_mfma_f32_16x16x32_bf16 v[216:219], a[16:19], v[36:39], v[216:219]\n"
"ds_read_b128 v[116:119], v179\n"
"ds_read_b64 v[106:107], v178 offset:64\n"
"v_mfma_f32_16x16x32_bf16 v[228:231], a[24:27], v[40:43], v[228:231]\n"
"s_waitcnt lgkmcnt(7)\n"
"ds_read_b128 v[76:79], v202 offset:55296\n"
"v_max_f32 v132, v132, v248\n"
"v_mfma_f32_16x16x32_bf16 v[120:123], v[120:123], v[144:147], 0\n"
"s_waitcnt lgkmcnt(7)\n"
"ds_read_b64 v[108:109], v179 offset:64\n"
"v_max_f32 v133, v133, v249\n"
"v_mfma_f32_16x16x32_bf16 v[112:115], v[112:115], v[144:147], 0\n"
"s_waitcnt lgkmcnt(7)\n"
"ds_read_b64_tr_b16 v[136:137], v182 offset:40640\n"
"v_max_f32 v134, v134, v250\n"
"v_mfma_f32_16x16x32_bf16 v[120:123], v[96:99], v[156:159], v[120:123]\n"
"s_waitcnt lgkmcnt(7)\n"
"v_mov_b64 v[68:69], -1\n"
"v_mov_b64 v[70:71], -1\n"
"v_mfma_f32_16x16x32_bf16 v[112:115], v[100:103], v[158:161], v[112:115]\n"
"s_add_u32 m0, s34, 3072\n"
"buffer_store_dwordx4 v[68:71], v192, s[28:31], s4 offen sc1\n"
"v_mfma_f32_16x16x32_bf16 v[216:219], a[28:31], v[40:43], v[216:219]\n"
"s_waitcnt vmcnt(11) lgkmcnt(5)\n"
"ds_read_b128 v[68:71], v202 offset:59392\n"
"ds_read_b64_tr_b16 v[138:139], v183 offset:40640\n"
"v_mfma_f32_16x16x32_bf16 v[124:127], v[124:127], v[144:147], 0\n"
"s_waitcnt lgkmcnt(6)\n"
"ds_read_b64_tr_b16 v[140:141], v182 offset:41408\n"
"buffer_load_dwordx4 v199, s[8:11], s75 offen lds\n"
"v_mfma_f32_16x16x32_bf16 v[116:119], v[116:119], v[144:147], 0\n"
"s_waitcnt lgkmcnt(6)\n"
"ds_read_b64_tr_b16 v[142:143], v183 offset:41408\n"
"v_max_f32 v135, v135, v251\n"
"v_mfma_f32_16x16x32_bf16 v[124:127], v[104:107], v[156:159], v[124:127]\n"
"s_waitcnt lgkmcnt(6)\n"
"ds_read_b64_tr_b16 v[244:245], v182 offset:40648\n"
"v_max3_f32 v170, v170, v131, v132\n"
"v_mfma_f32_16x16x32_bf16 v[228:231], a[36:39], v[44:47], v[228:231]\n"
"s_waitcnt lgkmcnt(6)\n"
"ds_read_b64_tr_b16 v[246:247], v183 offset:40648\n"
"v_max3_f32 v170, v170, v133, v134\n"
"v_mfma_f32_16x16x32_bf16 v[116:119], v[108:111], v[158:161], v[116:119]\n"
"s_add_u32 s4, s37, 6291456\n"
"ds_read_b64_tr_b16 v[248:249], v182 offset:41416\n"
"buffer_store_dwordx4 v[76:79], v194, s[24:27], s36 offen sc1\n"
"v_mfma_f32_16x16x32_bf16 v[216:219], a[40:43], v[44:47], v[216:219]\n"
"s_nop 0\n"
"ds_read_b128 v[76:79], v192 offset:44032\n"
"ds_read_b64_tr_b16 v[250:251], v183 offset:41416\n"
"v_mfma_f32_16x16x32_bf16 v[228:231], a[48:51], v[48:51], v[228:231]\n"
"s_waitcnt lgkmcnt(8)\n"
"ds_write_b64 v203, v[60:61] offset:30720\n"
"v_mfma_f32_16x16x32_bf16 v[216:219], a[52:55], v[48:51], v[216:219]\n"
"s_add_u32 m0, s34, 1024\n"
"v_max_f32 v120, v120, v112\n"
"v_max_f32 v121, v121, v113\n"
"v_mfma_f32_16x16x32_bf16 v[228:231], a[60:63], v[52:55], v[228:231]\n"
"buffer_load_dwordx4 v190, s[8:11], s0 offen lds\n"
"ds_write_b128 v186, v[220:223] offset:20320\n"
"v_mfma_f32_16x16x32_bf16 v[220:223], a[8:11], v[32:35], 0\n"
"s_add_u32 s0, s0, s33\n"
"v_max_f32 v122, v122, v114\n"
"v_max_f32 v123, v123, v115\n"
"v_mfma_f32_16x16x32_bf16 v[220:223], a[20:23], v[36:39], v[220:223]\n"
"v_max3_f32 v170, v170, v135, v120\n"
"v_max3_f32 v170, v170, v121, v122\n"
"v_mfma_f32_16x16x32_bf16 v[220:223], a[32:35], v[40:43], v[220:223]\n"
"buffer_load_dwordx2 v[60:61], v193, s[28:31], s38 offen offset:1024 sc1\n"
"ds_read_b64_tr_b16 v[112:113], v182 offset:40584\n"
"v_mfma_f32_16x16x32_bf16 v[220:223], a[44:47], v[44:47], v[220:223]\n"
"s_add_u32 s38, s38, 1572864\n"
"ds_read_b64_tr_b16 v[114:115], v183 offset:40584\n"
"ds_write_b128 v186, v[224:227] offset:20384\n"
"v_mfma_f32_16x16x32_bf16 v[220:223], a[56:59], v[48:51], v[220:223]\n"
"s_waitcnt vmcnt(11)\n"
"v_add3_u32 v200, v200, s50, -7\n"
"v_cmp_lt_u32 vcc, 64, v200\n"
"v_mfma_f32_16x16x32_bf16 v[216:219], a[64:67], v[52:55], v[216:219]\n"
"s_add_u32 m0, s34, 4096\n"
"buffer_store_dwordx4 v[68:71], v195, s[24:27], s36 offen sc1\n"
"v_mfma_f32_16x16x32_bf16 v[220:223], a[68:71], v[52:55], v[220:223]\n"
"s_nop 0\n"
"s_cbranch_vccnz .Lp6%=\n"
".Lr6%=:\n"
"v_mfma_f32_16x16x32_bf16 v[224:227], a[72:75], v[32:35], 0\n"
"s_add_u32 s36, s36, s33\n"
"ds_read_b128 v[68:71], v192 offset:45056\n"
"v_max_f32 v124, v124, v116\n"
"v_mfma_f32_16x16x32_bf16 v[224:227], a[84:87], v[36:39], v[224:227]\n"
"v_max_f32 v125, v125, v117\n"
"v_max_f32 v126, v126, v118\n"
"v_mfma_f32_16x16x32_bf16 v[224:227], a[96:99], v[40:43], v[224:227]\n"
"buffer_load_dwordx4 v199, s[8:11], s76 offen lds\n"
"ds_read_b64_tr_b16 v[116:117], v182 offset:41352\n"
"v_mfma_f32_16x16x32_bf16 v[224:227], a[108:111], v[44:47], v[224:227]\n"
"v_max_f32 v127, v127, v119\n"
"v_max3_f32 v170, v170, v123, v124\n"
"v_mfma_f32_16x16x32_bf16 v[224:227], a[120:123], v[48:51], v[224:227]\n"
"ds_read_b64_tr_b16 v[118:119], v183 offset:41352\n"
"v_max3_f32 v170, v170, v125, v126\n"
"v_mfma_f32_16x16x32_bf16 v[224:227], a[132:135], v[52:55], v[224:227]\n"
"v_mov_b64 v[98:99], 0\n"
"v_mov_b64 v[100:101], 0\n"
"v_mov_b64 v[106:107], 0\n"
"v_mov_b64 v[108:109], 0\n"
"v_max_f32 v171, v170, v127\n"
"v_max_f32 v170, v170, v127\n"
"v_cvt_pk_bf16_f32 v144, v208, v209\n"
"v_cvt_pk_bf16_f32 v145, v210, v211\n"
"v_mfma_f32_16x16x32_bf16 v[208:211], a[76:79], v[32:35], 0\n"
"v_permlane16_swap_b32 v170, v171\n"
"v_mfma_f32_16x16x32_bf16 v[208:211], a[88:91], v[36:39], v[208:211]\n"
"v_max_f32 v170, v170, v171\n"
"v_mov_b32 v171, v170\n"
"v_mfma_f32_16x16x32_bf16 v[208:211], a[100:103], v[40:43], v[208:211]\n"
"s_add_u32 m0, s34, 2048\n"
"v_permlane32_swap_b32 v170, v171\n"
"v_mfma_f32_16x16x32_bf16 v[208:211], a[112:115], v[44:47], v[208:211]\n"
"buffer_load_dwordx4 v190, s[8:11], s1 offen lds\n"
"v_max_f32 v170, v170, v171\n"
"v_mfma_f32_16x16x32_bf16 v[208:211], a[124:127], v[48:51], v[208:211]\n"
"s_add_u32 s1, s1, s33\n"
"v_mul_f32 v170, 0xbe553b95, v170\n"
"v_fmamk_f32 v128, v128, 0x3e553b95, v170\n"
"v_mfma_f32_16x16x32_bf16 v[208:211], a[136:139], v[52:55], v[208:211]\n"
"v_fmamk_f32 v129, v129, 0x3e553b95, v170\n"
"v_fmamk_f32 v130, v130, 0x3e553b95, v170\n"
"v_fmamk_f32 v131, v131, 0x3e553b95, v170\n"
"v_exp_f32 v128, v128\n"
"v_exp_f32 v129, v129\n"
"v_exp_f32 v130, v130\n"
"v_exp_f32 v131, v131\n"
"v_cvt_pk_bf16_f32 v82, v128, v130\n"
"v_cvt_pk_bf16_f32 v83, v129, v131\n"
"v_fmamk_f32 v132, v132, 0x3e553b95, v170\n"
"v_fmamk_f32 v133, v133, 0x3e553b95, v170\n"
"v_fmamk_f32 v134, v134, 0x3e553b95, v170\n"
"v_fmamk_f32 v135, v135, 0x3e553b95, v170\n"
"v_exp_f32 v132, v132\n"
"v_exp_f32 v133, v133\n"
"s_add_u32 m0, s34, 5120\n"
"v_exp_f32 v134, v134\n"
"buffer_load_dwordx4 v199, s[8:11], s77 offen lds\n"
"v_exp_f32 v135, v135\n"
"v_cvt_pk_bf16_f32 v84, v132, v134\n"
"v_cvt_pk_bf16_f32 v85, v133, v135\n"
"v_cvt_pk_bf16_f32 v146, v204, v205\n"
"v_cvt_pk_bf16_f32 v147, v206, v207\n"
"v_mfma_f32_16x16x32_bf16 v[204:207], a[80:83], v[32:35], 0\n"
"s_waitcnt lgkmcnt(0)\n"
"v_mov_b64 v[72:73], -1\n"
"v_fmamk_f32 v120, v120, 0x3e553b95, v170\n"
"v_smfmac_f32_16x16x64_bf16 v[252:255], v[82:85], v[136:143], v66\n"
"s_mul_i32 s8, s33, 0\n"
"buffer_store_dwordx2 v[72:73], v193, s[28:31], s4 offen offset:1024 sc1\n"
"v_smfmac_f32_16x16x64_bf16 v[98:101], v[82:85], v[244:251], v66\n"
"s_mul_i32 s9, s33, 1\n"
"ds_read_b128 v[72:75], v192 offset:43008\n"
"v_smfmac_f32_16x16x64_bf16 v[106:109], v[82:85], v[112:119], v66\n"
"s_mul_i32 s10, s33, 3\n"
"ds_read_b64_tr_b16 v[128:129], v184 offset:128\n"
"v_mfma_f32_16x16x32_bf16 v[82:85], v[172:175], v[82:85], 0\n"
"s_mul_i32 s11, s33, 2\n"
"ds_read_b64_tr_b16 v[130:131], v185 offset:128\n"
"buffer_load_dword v200, off, s[28:31], s39 sc1\n"
"v_mfma_f32_16x16x32_bf16 v[204:207], a[92:95], v[36:39], v[204:207]\n"
"ds_read_b64_tr_b16 v[132:133], v184 offset:896\n"
"ds_read_b64_tr_b16 v[134:135], v185 offset:896\n"
"v_mfma_f32_16x16x32_bf16 v[204:207], a[104:107], v[40:43], v[204:207]\n"
"ds_read_b64_tr_b16 v[136:137], v184 offset:136\n"
"v_cvt_pk_bf16_f32 v158, v212, v213\n"
"v_mfma_f32_16x16x32_bf16 v[204:207], a[116:119], v[44:47], v[204:207]\n"
"ds_read_b64_tr_b16 v[138:139], v185 offset:136\n"
"v_fmamk_f32 v121, v121, 0x3e553b95, v170\n"
"v_mfma_f32_16x16x32_bf16 v[204:207], a[128:131], v[48:51], v[204:207]\n"
"ds_read_b64_tr_b16 v[140:141], v184 offset:904\n"
"v_cvt_pk_bf16_f32 v159, v214, v215\n"
"v_mfma_f32_16x16x32_bf16 v[204:207], a[140:143], v[52:55], v[204:207]\n"
"ds_read_b64_tr_b16 v[142:143], v185 offset:904\n"
"v_fmamk_f32 v122, v122, 0x3e553b95, v170\n"
"v_mfma_f32_16x16x32_bf16 v[212:215], a[144:147], v[32:35], 0\n"
"s_waitcnt lgkmcnt(8)\n"
"ds_read_b64_tr_b16 v[244:245], v184 offset:72\n"
"v_fmamk_f32 v123, v123, 0x3e553b95, v170\n"
"v_mfma_f32_16x16x32_bf16 v[232:235], a[216:219], v[72:75], 0\n"
"ds_read_b64_tr_b16 v[246:247], v185 offset:72\n"
"v_fmamk_f32 v124, v124, 0x3e553b95, v170\n"
"v_mfma_f32_16x16x32_bf16 v[236:239], a[220:223], v[72:75], 0\n"
"ds_read_b64_tr_b16 v[248:249], v184 offset:840\n"
"v_fmamk_f32 v125, v125, 0x3e553b95, v170\n"
"v_mfma_f32_16x16x32_bf16 v[240:243], a[224:227], v[72:75], 0\n"
"ds_read_b128 v[72:75], v192 offset:46080\n"
"v_fmamk_f32 v126, v126, 0x3e553b95, v170\n"
"v_mfma_f32_16x16x32_bf16 v[232:235], a[228:231], v[76:79], v[232:235]\n"
"ds_read_b64_tr_b16 v[250:251], v185 offset:840\n"
"v_fmamk_f32 v127, v127, 0x3e553b95, v170\n"
"v_mfma_f32_16x16x32_bf16 v[236:239], a[232:235], v[76:79], v[236:239]\n"
"v_exp_f32 v120, v120\n"
"v_mfma_f32_16x16x32_bf16 v[240:243], a[236:239], v[76:79], v[240:243]\n"
"ds_read_b128 v[76:79], v192 offset:47104\n"
"ds_read_b128 v[112:115], v180 offset:13504\n"
"v_mfma_f32_16x16x32_bf16 v[232:235], a[240:243], v[68:71], v[232:235]\n"
"v_exp_f32 v121, v121\n"
"v_mfma_f32_16x16x32_bf16 v[236:239], a[244:247], v[68:71], v[236:239]\n"
"v_exp_f32 v122, v122\n"
"v_mfma_f32_16x16x32_bf16 v[240:243], a[248:251], v[68:71], v[240:243]\n"
"ds_read_b128 v[68:71], v192 offset:48128\n"
"v_cvt_pk_bf16_f32 v90, v120, v122\n"
"v_mfma_f32_16x16x32_bf16 v[212:215], a[156:159], v[36:39], v[212:215]\n"
"v_exp_f32 v123, v123\n"
"v_mfma_f32_16x16x32_bf16 v[212:215], a[168:171], v[40:43], v[212:215]\n"
"v_cvt_pk_bf16_f32 v91, v121, v123\n"
"v_exp_f32 v124, v124\n"
"v_mfma_f32_16x16x32_bf16 v[212:215], a[180:183], v[44:47], v[212:215]\n"
"s_waitcnt lgkmcnt(4)\n"
"ds_read_b128 v[120:123], v181 offset:13504\n"
"ds_read_b128 v[116:119], v180 offset:27008\n"
"v_mfma_f32_16x16x32_bf16 v[232:235], a[252:255], v[72:75], v[232:235]\n"
"v_exp_f32 v125, v125\n"
"v_mfma_f32_16x16x32_bf16 v[240:243], v[4:7], v[72:75], v[240:243]\n"
"s_waitcnt lgkmcnt(4)\n"
"v_exp_f32 v126, v126\n"
"v_mfma_f32_16x16x32_bf16 v[236:239], v[0:3], v[72:75], v[236:239]\n"
"s_waitcnt lgkmcnt(3)\n"
"v_exp_f32 v127, v127\n"
"v_mfma_f32_16x16x32_bf16 v[232:235], v[8:11], v[76:79], v[232:235]\n"
"s_waitcnt lgkmcnt(2)\n"
"v_cvt_pk_bf16_f32 v92, v124, v126\n"
"v_cvt_pk_bf16_f32 v93, v125, v127\n"
"v_mfma_f32_16x16x32_bf16 v[240:243], v[16:19], v[76:79], v[240:243]\n"
"ds_read_b128 v[124:127], v181 offset:27008\n"
"v_cvt_pk_bf16_f32 v228, v228, v229\n"
"v_mfma_f32_16x16x32_bf16 v[236:239], v[12:15], v[76:79], v[236:239]\n"
"v_cvt_pk_bf16_f32 v229, v230, v231\n"
"v_cvt_pk_bf16_f32 v230, v216, v217\n"
"v_mfma_f32_16x16x32_bf16 v[232:235], v[20:23], v[68:71], v[232:235]\n"
"v_cvt_pk_bf16_f32 v231, v218, v219\n"
"v_cvt_pk_bf16_f32 v220, v220, v221\n"
"v_mfma_f32_16x16x32_bf16 v[240:243], v[28:31], v[68:71], v[240:243]\n"
"s_waitcnt lgkmcnt(2)\n"
"v_cvt_pk_bf16_f32 v221, v222, v223\n"
"v_cvt_pk_bf16_f32 v224, v224, v225\n"
"v_mfma_f32_16x16x32_bf16 v[236:239], v[24:27], v[68:71], v[236:239]\n"
"s_waitcnt lgkmcnt(1)\n"
"v_cvt_pk_bf16_f32 v232, v232, v233\n"
"v_cvt_pk_bf16_f32 v233, v234, v235\n"
"v_smfmac_f32_16x16x64_bf16 v[252:255], v[90:93], v[128:135], v66\n"
"v_cvt_pk_bf16_f32 v240, v240, v241\n"
"v_smfmac_f32_16x16x64_bf16 v[98:101], v[90:93], v[136:143], v66\n"
"v_cvt_pk_bf16_f32 v241, v242, v243\n"
"v_smfmac_f32_16x16x64_bf16 v[106:109], v[90:93], v[244:251], v66\n"
"ds_write_b64 v197, v[240:241] offset:49152\n"
"v_mfma_f32_16x16x32_bf16 v[90:93], v[172:175], v[90:93], v[82:85]\n"
"s_waitcnt lgkmcnt(1)\n"
"v_cvt_pk_bf16_f32 v234, v236, v237\n"
"v_cvt_pk_bf16_f32 v235, v238, v239\n"
"v_mfma_f32_16x16x32_bf16 v[112:115], v[112:115], v[148:151], 0\n"
"ds_write_b128 v196, v[232:235] offset:49152\n"
"v_mfma_f32_16x16x32_bf16 v[120:123], v[120:123], v[148:151], 0\n"
"v_cvt_pk_bf16_f32 v252, v252, v253\n"
"v_cvt_pk_bf16_f32 v253, v254, v255\n"
"v_mfma_f32_16x16x32_bf16 v[116:119], v[116:119], v[148:151], 0\n"
"v_rcp_f32 v171, v90\n"
"v_mfma_f32_16x16x32_bf16 v[124:127], v[124:127], v[148:151], 0\n"
"ds_read_b64 v[82:83], v180 offset:13568\n"
"ds_read_b64 v[84:85], v181 offset:13568\n"
"v_mfma_f32_16x16x32_bf16 v[216:219], a[148:151], v[32:35], 0\n"
"ds_read_b64 v[90:91], v180 offset:27072\n"
"v_cvt_pk_bf16_f32 v171, v171, v171\n"
"v_mfma_f32_16x16x32_bf16 v[216:219], a[160:163], v[36:39], v[216:219]\n"
"ds_read_b64 v[92:93], v181 offset:27072\n"
"v_and_b32 v64, v171, v176\n"
"v_mfma_f32_16x16x32_bf16 v[216:219], a[172:175], v[40:43], v[216:219]\n"
"ds_read_b128 v[128:131], v178\n"
"v_and_b32 v65, v171, v177\n"
"v_mfma_f32_16x16x32_bf16 v[216:219], a[184:187], v[44:47], v[216:219]\n"
"ds_read_b128 v[136:139], v179\n"
"v_cvt_pk_bf16_f32 v98, v98, v99\n"
"v_mfma_f32_16x16x16_bf16 v[252:255], v[252:253], v[64:65], 0\n"
"s_waitcnt lgkmcnt(6)\n"
"v_cvt_pk_bf16_f32 v99, v100, v101\n"
"v_cvt_pk_bf16_f32 v106, v106, v107\n"
"v_mfma_f32_16x16x32_bf16 v[216:219], a[196:199], v[48:51], v[216:219]\n"
"s_barrier\n"
"v_mfma_f32_16x16x16_bf16 v[98:101], v[98:99], v[64:65], 0\n"
"s_waitcnt vmcnt(11)\n"
"ds_read_b128 v[68:71], v192 offset:30720\n"
"ds_write_b128 v186, v[228:231] offset:33760\n"
"v_mfma_f32_16x16x32_bf16 v[228:231], a[152:155], v[32:35], 0\n"
"s_waitcnt lgkmcnt(6)\n"
"v_cvt_pk_bf16_f32 v107, v108, v109\n"
"v_cvt_pk_bf16_f32 v252, v252, v253\n"
"v_mfma_f32_16x16x32_bf16 v[112:115], v[80:83], v[160:163], v[112:115]\n"
"s_waitcnt lgkmcnt(5)\n"
"v_cvt_pk_bf16_f32 v253, v254, v255\n"
"v_cvt_pk_bf16_f32 v254, v98, v99\n"
"v_mfma_f32_16x16x16_bf16 v[106:109], v[106:107], v[64:65], 0\n"
"s_waitcnt lgkmcnt(4)\n"
"ds_read_b128 v[32:35], v188 offset:6144\n"
"ds_read_b64 v[98:99], v178 offset:64\n"
"v_mfma_f32_16x16x32_bf16 v[120:123], v[84:87], v[162:165], v[120:123]\n"
"s_waitcnt lgkmcnt(5)\n"
"ds_write_b128 v202, v[56:59] offset:55296\n"
"v_mfma_f32_16x16x32_bf16 v[116:119], v[88:91], v[160:163], v[116:119]\n"
"s_waitcnt lgkmcnt(5)\n"
"v_cvt_pk_bf16_f32 v255, v100, v101\n"
"v_cvt_pk_bf16_f32 v106, v106, v107\n"
"v_mfma_f32_16x16x32_bf16 v[124:127], v[92:95], v[162:165], v[124:127]\n"
"s_waitcnt lgkmcnt(4)\n"
"buffer_store_dwordx4 v[252:255], v192, s[28:31], s37 offen sc1\n"
"v_mfma_f32_16x16x32_bf16 v[128:131], v[128:131], v[148:151], 0\n"
"ds_read_b64 v[100:101], v179 offset:64\n"
"ds_read_b128 v[132:135], v178 offset:13504\n"
"v_mfma_f32_16x16x32_bf16 v[136:139], v[136:139], v[148:151], 0\n"
"ds_read_b128 v[140:143], v179 offset:13504\n"
"v_cvt_pk_bf16_f32 v107, v108, v109\n"
"v_mfma_f32_16x16x32_bf16 v[228:231], a[164:167], v[36:39], v[228:231]\n"
"buffer_store_dwordx2 v[106:107], v193, s[28:31], s37 offen offset:1024 sc1\n"
"v_mfma_f32_16x16x32_bf16 v[228:231], a[176:179], v[40:43], v[228:231]\n"
"s_waitcnt lgkmcnt(4)\n"
"ds_read_b128 v[36:39], v189 offset:6144\n"
"ds_read_b128 v[40:43], v188 offset:7168\n"
"v_mfma_f32_16x16x32_bf16 v[128:131], v[96:99], v[160:163], v[128:131]\n"
"s_add_u32 s37, s37, 1572864\n"
"ds_read_b64 v[106:107], v178 offset:13568\n"
"v_max_f32 v112, v112, v120\n"
"v_mfma_f32_16x16x32_bf16 v[228:231], a[188:191], v[44:47], v[228:231]\n"
"ds_read_b128 v[44:47], v189 offset:7168\n"
"v_max_f32 v113, v113, v121\n"
"v_mfma_f32_16x16x32_bf16 v[228:231], a[200:203], v[48:51], v[228:231]\n"
"buffer_load_dwordx4 a[198:201], v192, s[28:31], s38 offen sc1\n"
"ds_read_b64 v[108:109], v179 offset:13568\n"
"v_mfma_f32_16x16x32_bf16 v[232:235], a[216:219], v[68:71], 0\n"
"s_waitcnt lgkmcnt(7)\n"
"ds_read_b128 v[72:75], v202 offset:49152\n"
"v_max_f32 v114, v114, v122\n"
"v_mfma_f32_16x16x32_bf16 v[236:239], a[220:223], v[68:71], 0\n"
"s_waitcnt lgkmcnt(7)\n"
"ds_read_b64_tr_b16 v[244:245], v182 offset:54144\n"
"v_max_f32 v115, v115, v123\n"
"v_mfma_f32_16x16x32_bf16 v[136:139], v[100:103], v[162:165], v[136:139]\n"
"s_waitcnt lgkmcnt(7)\n"
"buffer_load_dwordx2 a[202:203], v193, s[28:31], s38 offen offset:1024 sc1\n"
"ds_read_b64_tr_b16 v[246:247], v183 offset:54144\n"
"v_mfma_f32_16x16x32_bf16 v[132:135], v[132:135], v[148:151], 0\n"
"s_waitcnt vmcnt(10)\n"
"ds_read_b64_tr_b16 v[248:249], v182 offset:54912\n"
"v_max3_f32 v170, v112, v113, v114\n"
"v_mfma_f32_16x16x32_bf16 v[140:143], v[140:143], v[148:151], 0\n"
"s_add_u32 s38, s38, 1572864\n"
"ds_read_b128 v[76:79], v202 offset:53248\n"
"v_max_f32 v116, v116, v124\n"
"v_mfma_f32_16x16x32_bf16 v[240:243], a[224:227], v[68:71], 0\n"
"ds_read_b128 v[68:71], v192 offset:33792\n"
"v_max_f32 v117, v117, v125\n"
"v_mfma_f32_16x16x32_bf16 v[212:215], a[192:195], v[48:51], v[212:215]\n"
"s_waitcnt lgkmcnt(8)\n"
"ds_read_b128 v[48:51], v188 offset:8192\n"
"v_max_f32 v118, v118, v126\n"
"v_mfma_f32_16x16x32_bf16 v[132:135], v[104:107], v[160:163], v[132:135]\n"
"ds_read_b64_tr_b16 v[250:251], v183 offset:54912\n"
"v_max_f32 v119, v119, v127\n"
"v_mfma_f32_16x16x32_bf16 v[212:215], a[204:207], v[52:55], v[212:215]\n"
"s_waitcnt lgkmcnt(8)\n"
"buffer_load_dwordx4 a[204:207], v192, s[28:31], s38 offen sc1\n"
"ds_read_b64_tr_b16 v[120:121], v182 offset:54152\n"
"v_mfma_f32_16x16x32_bf16 v[140:143], v[108:111], v[162:165], v[140:143]\n"
"s_waitcnt lgkmcnt(8)\n"
"ds_read_b64_tr_b16 v[122:123], v183 offset:54152\n"
"v_max3_f32 v170, v170, v115, v116\n"
"v_mfma_f32_16x16x32_bf16 v[216:219], a[208:211], v[52:55], v[216:219]\n"
"ds_read_b64_tr_b16 v[124:125], v182 offset:54920\n"
"v_max3_f32 v170, v170, v117, v118\n"
"v_mfma_f32_16x16x32_bf16 v[228:231], a[212:215], v[52:55], v[228:231]\n"
"buffer_load_dwordx2 a[208:209], v193, s[28:31], s38 offen offset:1024 sc1\n"
"ds_read_b128 v[52:55], v189 offset:8192\n"
"ds_read_b64_tr_b16 v[126:127], v183 offset:54920\n"
"v_max_f32 v128, v128, v136\n"
"v_max_f32 v129, v129, v137\n"
"v_max_f32 v130, v130, v138\n"
"v_max_f32 v131, v131, v139\n"
"v_max3_f32 v170, v170, v119, v128\n"
"v_max3_f32 v170, v170, v129, v130\n"
"v_max_f32 v132, v132, v140\n"
"v_cvt_pk_bf16_f32 v225, v226, v227\n"
"v_max_f32 v133, v133, v141\n"
"v_cvt_pk_bf16_f32 v226, v208, v209\n"
"v_max_f32 v134, v134, v142\n"
"v_cvt_pk_bf16_f32 v227, v210, v211\n"
"v_mfma_f32_16x16x32_bf16 v[208:211], a[0:3], v[32:35], 0\n"
"s_waitcnt lgkmcnt(8)\n"
"buffer_store_dwordx4 v[72:75], v194, s[24:27], s36 offen sc1\n"
"v_mfma_f32_16x16x32_bf16 v[208:211], a[12:15], v[36:39], v[208:211]\n"
"s_add_u32 s38, s38, 1572864\n"
"ds_read_b128 v[72:75], v192 offset:31744\n"
"ds_write_b64 v203, v[60:61] offset:55296\n"
"v_mfma_f32_16x16x32_bf16 v[208:211], a[24:27], v[40:43], v[208:211]\n"
"v_max_f32 v135, v135, v143\n"
"v_max3_f32 v170, v170, v131, v132\n"
"v_mfma_f32_16x16x32_bf16 v[208:211], a[36:39], v[44:47], v[208:211]\n"
"s_waitcnt lgkmcnt(8)\n"
"buffer_store_dwordx4 v[76:79], v195, s[24:27], s36 offen sc1\n"
"v_mfma_f32_16x16x32_bf16 v[208:211], a[48:51], v[48:51], v[208:211]\n"
"s_waitcnt lgkmcnt(3)\n"
"ds_read_b128 v[76:79], v192 offset:32768\n"
"ds_read_b64_tr_b16 v[136:137], v182 offset:54088\n"
"v_mfma_f32_16x16x32_bf16 v[208:211], a[60:63], v[52:55], v[208:211]\n"
"s_add_u32 s36, s36, s33\n"
"v_cvt_pk_bf16_f32 v222, v204, v205\n"
"v_max3_f32 v170, v170, v133, v134\n"
"v_cvt_pk_bf16_f32 v223, v206, v207\n"
"v_mfma_f32_16x16x32_bf16 v[204:207], a[4:7], v[32:35], 0\n"
"ds_read_b64_tr_b16 v[138:139], v183 offset:54088\n"
"ds_write_b128 v186, v[220:223] offset:33824\n"
"v_mfma_f32_16x16x32_bf16 v[220:223], a[8:11], v[32:35], 0\n"
"s_waitcnt lgkmcnt(5)\n"
"v_mov_b64 v[252:253], 0\n"
"v_mov_b64 v[254:255], 0\n"
"v_mfma_f32_16x16x32_bf16 v[232:235], a[228:231], v[72:75], v[232:235]\n"
"v_mov_b64 v[98:99], 0\n"
"v_mov_b64 v[100:101], 0\n"
"v_mfma_f32_16x16x32_bf16 v[236:239], a[232:235], v[72:75], v[236:239]\n"
"ds_read_b64_tr_b16 v[140:141], v182 offset:54856\n"
"ds_read_b64_tr_b16 v[142:143], v183 offset:54856\n"
"v_mfma_f32_16x16x32_bf16 v[240:243], a[236:239], v[72:75], v[240:243]\n"
"s_waitcnt lgkmcnt(5)\n"
"ds_read_b128 v[72:75], v192 offset:34816\n"
"s_mov_b64 exec, s[46:47]\n"
"v_add_u32 v183, 0xc0, v183\n"
"s_mov_b64 exec, -1\n"
"v_mfma_f32_16x16x32_bf16 v[232:235], a[240:243], v[76:79], v[232:235]\n"
"ds_write_b128 v186, v[224:227] offset:33888\n"
"v_mfma_f32_16x16x32_bf16 v[236:239], a[244:247], v[76:79], v[236:239]\n"
"v_mov_b64 v[106:107], 0\n"
"v_mov_b64 v[108:109], 0\n"
"v_mfma_f32_16x16x32_bf16 v[240:243], a[248:251], v[76:79], v[240:243]\n"
"s_waitcnt vmcnt(8)\n"
"v_max_f32 v171, v170, v135\n"
"v_add3_u32 v200, v200, s50, -8\n"
"v_mfma_f32_16x16x32_bf16 v[232:235], a[252:255], v[68:71], v[232:235]\n"
"v_cmp_lt_u32 vcc, 64, v200\n"
"v_max_f32 v170, v170, v135\n"
"v_mfma_f32_16x16x32_bf16 v[240:243], v[4:7], v[68:71], v[240:243]\n"
"ds_read_b128 v[76:79], v192 offset:35840\n"
"v_permlane16_swap_b32 v170, v171\n"
"v_mfma_f32_16x16x32_bf16 v[236:239], v[0:3], v[68:71], v[236:239]\n"
"s_cbranch_vccnz .Lp7%=\n"
".Lr7%=:\n"
"v_mfma_f32_16x16x32_bf16 v[204:207], a[16:19], v[36:39], v[204:207]\n"
"s_waitcnt lgkmcnt(2)\n"
"buffer_load_dwordx4 a[210:213], v192, s[28:31], s38 offen sc1\n"
"v_max_f32 v170, v170, v171\n"
"v_mfma_f32_16x16x32_bf16 v[232:235], v[8:11], v[72:75], v[232:235]\n"
"v_mov_b32 v171, v170\n"
"v_cvt_pk_bf16_f32 v148, v212, v213\n"
"v_mfma_f32_16x16x32_bf16 v[240:243], v[16:19], v[72:75], v[240:243]\n"
"v_permlane32_swap_b32 v170, v171\n"
"v_mfma_f32_16x16x32_bf16 v[236:239], v[12:15], v[72:75], v[236:239]\n"
"buffer_load_dwordx2 a[214:215], v193, s[28:31], s38 offen offset:1024 sc1\n"
"v_max_f32 v170, v170, v171\n"
"v_mfma_f32_16x16x32_bf16 v[220:223], a[20:23], v[36:39], v[220:223]\n"
"s_add_u32 s38, s38, 1572864\n"
"v_mul_f32 v170, 0xbe553b95, v170\n"
"v_fmamk_f32 v112, v112, 0x3e553b95, v170\n"
"v_mfma_f32_16x16x32_bf16 v[204:207], a[28:31], v[40:43], v[204:207]\n"
"s_waitcnt lgkmcnt(0)\n"
"v_fmamk_f32 v113, v113, 0x3e553b95, v170\n"
"v_fmamk_f32 v114, v114, 0x3e553b95, v170\n"
"v_mfma_f32_16x16x32_bf16 v[220:223], a[32:35], v[40:43], v[220:223]\n"
"s_add_u32 s12, s38, 0\n"
"v_fmamk_f32 v115, v115, 0x3e553b95, v170\n"
"v_fmamk_f32 v116, v116, 0x3e553b95, v170\n"
"v_mfma_f32_16x16x32_bf16 v[232:235], v[20:23], v[76:79], v[232:235]\n"
"s_add_u32 s13, s38, 1572864\n"
"v_exp_f32 v112, v112\n"
"v_mfma_f32_16x16x32_bf16 v[240:243], v[28:31], v[76:79], v[240:243]\n"
"s_add_u32 s15, s38, 3145728\n"
"v_exp_f32 v113, v113\n"
"v_mfma_f32_16x16x32_bf16 v[236:239], v[24:27], v[76:79], v[236:239]\n"
"s_add_u32 s14, s38, 4718592\n"
"v_exp_f32 v114, v114\n"
"v_mfma_f32_16x16x32_bf16 v[204:207], a[40:43], v[44:47], v[204:207]\n"
"s_add_u32 s16, s38, 6291456\n"
"v_exp_f32 v115, v115\n"
"v_mfma_f32_16x16x32_bf16 v[220:223], a[44:47], v[44:47], v[220:223]\n"
"s_add_u32 s17, s38, 7864320\n"
"v_cvt_pk_bf16_f32 v82, v112, v114\n"
"v_cvt_pk_bf16_f32 v83, v113, v115\n"
"v_mfma_f32_16x16x32_bf16 v[204:207], a[52:55], v[48:51], v[204:207]\n"
"ds_read_b64_tr_b16 v[112:113], v182 offset:128\n"
"ds_read_b64_tr_b16 v[114:115], v183 offset:128\n"
"v_mfma_f32_16x16x32_bf16 v[220:223], a[56:59], v[48:51], v[220:223]\n"
"v_fmamk_f32 v117, v117, 0x3e553b95, v170\n"
"v_fmamk_f32 v118, v118, 0x3e553b95, v170\n"
"v_mfma_f32_16x16x32_bf16 v[204:207], a[64:67], v[52:55], v[204:207]\n"
"v_fmamk_f32 v119, v119, 0x3e553b95, v170\n"
"v_exp_f32 v116, v116\n"
"v_mfma_f32_16x16x32_bf16 v[220:223], a[68:71], v[52:55], v[220:223]\n"
".Lic6%=:\n"
"s_cbranch_execz .Lib1%=\n"
"v_exp_f32 v117, v117\n"
"v_mfma_f32_16x16x32_bf16 v[224:227], a[72:75], v[32:35], 0\n"
"v_exp_f32 v118, v118\n"
"v_mfma_f32_16x16x32_bf16 v[224:227], a[84:87], v[36:39], v[224:227]\n"
"v_exp_f32 v119, v119\n"
"v_mfma_f32_16x16x32_bf16 v[224:227], a[96:99], v[40:43], v[224:227]\n"
"v_cvt_pk_bf16_f32 v84, v116, v118\n"
"v_cvt_pk_bf16_f32 v85, v117, v119\n"
"v_mfma_f32_16x16x32_bf16 v[224:227], a[108:111], v[44:47], v[224:227]\n"
"ds_read_b64_tr_b16 v[116:117], v182 offset:896\n"
"ds_read_b64_tr_b16 v[118:119], v183 offset:896\n"
"v_smfmac_f32_16x16x64_bf16 v[252:255], v[82:85], v[244:251], v66\n"
"ds_read_b64_tr_b16 v[244:245], v182 offset:136\n"
"v_smfmac_f32_16x16x64_bf16 v[98:101], v[82:85], v[120:127], v66\n"
"ds_read_b64_tr_b16 v[246:247], v183 offset:136\n"
"v_smfmac_f32_16x16x64_bf16 v[106:109], v[82:85], v[136:143], v66\n"
"ds_read_b64_tr_b16 v[248:249], v182 offset:904\n"
"v_mfma_f32_16x16x32_bf16 v[82:85], v[172:175], v[82:85], 0\n"
"ds_read_b64_tr_b16 v[250:251], v183 offset:904\n"
"v_cvt_pk_bf16_f32 v232, v232, v233\n"
"v_mfma_f32_16x16x32_bf16 v[224:227], a[120:123], v[48:51], v[224:227]\n"
"ds_read_b64_tr_b16 v[120:121], v182 offset:72\n"
"v_cvt_pk_bf16_f32 v233, v234, v235\n"
"v_mfma_f32_16x16x32_bf16 v[224:227], a[132:135], v[52:55], v[224:227]\n"
"v_cvt_pk_bf16_f32 v240, v240, v241\n"
"v_cvt_pk_bf16_f32 v234, v236, v237\n"
"v_cvt_pk_bf16_f32 v235, v238, v239\n"
"v_cvt_pk_bf16_f32 v241, v242, v243\n"
"v_cvt_pk_bf16_f32 v149, v214, v215\n"
"v_mfma_f32_16x16x32_bf16 v[212:215], a[76:79], v[32:35], 0\n"
"ds_write_b128 v196, v[232:235] offset:36864\n"
"v_mfma_f32_16x16x32_bf16 v[212:215], a[88:91], v[36:39], v[212:215]\n"
"v_cvt_pk_bf16_f32 v150, v216, v217\n"
"v_cvt_pk_bf16_f32 v151, v218, v219\n"
"v_mfma_f32_16x16x32_bf16 v[216:219], a[80:83], v[32:35], 0\n"
"v_fmamk_f32 v128, v128, 0x3e553b95, v170\n"
"v_cvt_pk_bf16_f32 v162, v228, v229\n"
"v_mfma_f32_16x16x32_bf16 v[216:219], a[92:95], v[36:39], v[216:219]\n"
"ds_write_b64 v197, v[240:241] offset:36864\n"
"v_mfma_f32_16x16x32_bf16 v[212:215], a[100:103], v[40:43], v[212:215]\n"
"s_waitcnt vmcnt(8)\n"
"v_fmamk_f32 v129, v129, 0x3e553b95, v170\n"
"v_cvt_pk_bf16_f32 v163, v230, v231\n"
"v_mfma_f32_16x16x32_bf16 v[216:219], a[104:107], v[40:43], v[216:219]\n"
"v_or_b32 v201, 10, s6\n"
"v_fmamk_f32 v130, v130, 0x3e553b95, v170\n"
"v_mfma_f32_16x16x32_bf16 v[212:215], a[112:115], v[44:47], v[212:215]\n"
"s_mov_b64 exec, 1\n"
"buffer_store_dword v201, off, s[28:31], s40 sc1\n"
"s_mov_b64 exec, -1\n"
"v_mfma_f32_16x16x32_bf16 v[216:219], a[116:119], v[44:47], v[216:219]\n"
"ds_read_b128 v[32:35], v188\n"
"ds_read_b64_tr_b16 v[122:123], v183 offset:72\n"
"v_mfma_f32_16x16x32_bf16 v[212:215], a[124:127], v[48:51], v[212:215]\n"
"ds_read_b64_tr_b16 v[124:125], v182 offset:840\n"
"v_fmamk_f32 v131, v131, 0x3e553b95, v170\n"
"v_mfma_f32_16x16x32_bf16 v[216:219], a[128:131], v[48:51], v[216:219]\n"
"ds_read_b128 v[36:39], v189\n"
"v_fmamk_f32 v132, v132, 0x3e553b95, v170\n"
"v_mfma_f32_16x16x32_bf16 v[212:215], a[136:139], v[52:55], v[212:215]\n"
"s_waitcnt lgkmcnt(4)\n"
"ds_read_b64_tr_b16 v[126:127], v183 offset:840\n"
"v_fmamk_f32 v133, v133, 0x3e553b95, v170\n"
"v_mfma_f32_16x16x32_bf16 v[216:219], a[140:143], v[52:55], v[216:219]\n"
"s_barrier\n"
"ds_read_b128 v[76:79], v192 offset:55296\n"
"ds_read_b128 v[40:43], v188 offset:1024\n"
"v_exp_f32 v128, v128\n"
"v_exp_f32 v129, v129\n"
"v_exp_f32 v130, v130\n"
"v_exp_f32 v131, v131\n"
"v_cvt_pk_bf16_f32 v90, v128, v130\n"
"v_cvt_pk_bf16_f32 v91, v129, v131\n"
"v_fmamk_f32 v134, v134, 0x3e553b95, v170\n"
"v_fmamk_f32 v135, v135, 0x3e553b95, v170\n"
"v_exp_f32 v132, v132\n"
"v_exp_f32 v133, v133\n"
"v_exp_f32 v134, v134\n"
"v_exp_f32 v135, v135\n"
"v_cvt_pk_bf16_f32 v92, v132, v134\n"
"v_cvt_pk_bf16_f32 v93, v133, v135\n"
"v_cvt_pk_bf16_f32 v208, v208, v209\n"
"v_cvt_pk_bf16_f32 v209, v210, v211\n"
"v_smfmac_f32_16x16x64_bf16 v[252:255], v[90:93], v[112:119], v66\n"
"s_waitcnt lgkmcnt(2)\n"
"ds_read_b128 v[44:47], v189 offset:1024\n"
"v_smfmac_f32_16x16x64_bf16 v[98:101], v[90:93], v[244:251], v66\n"
"s_waitcnt lgkmcnt(2)\n"
"ds_read_b128 v[48:51], v188 offset:2048\n"
"v_smfmac_f32_16x16x64_bf16 v[106:109], v[90:93], v[120:127], v66\n"
"ds_read_b128 v[52:55], v189 offset:2048\n"
"v_mfma_f32_16x16x32_bf16 v[90:93], v[172:175], v[90:93], v[82:85]\n"
"ds_read_b128 v[136:139], v180 offset:27008\n"
"ds_read_b128 v[128:131], v181 offset:27008\n"
"v_mfma_f32_16x16x32_bf16 v[232:235], a[216:219], v[76:79], 0\n"
"ds_read_b64 v[82:83], v180 offset:27072\n"
"v_cvt_pk_bf16_f32 v252, v252, v253\n"
"v_mfma_f32_16x16x32_bf16 v[236:239], a[220:223], v[76:79], 0\n"
"ds_read_b64 v[84:85], v181 offset:27072\n"
"v_cvt_pk_bf16_f32 v253, v254, v255\n"
"v_mfma_f32_16x16x32_bf16 v[240:243], a[224:227], v[76:79], 0\n"
"ds_read_b128 v[76:79], v192 offset:58368\n"
"v_cvt_pk_bf16_f32 v98, v98, v99\n"
"v_mfma_f32_16x16x32_bf16 v[228:231], a[0:3], v[32:35], 0\n"
"ds_read_b128 v[140:143], v178\n"
"v_cvt_pk_bf16_f32 v99, v100, v101\n"
"v_mfma_f32_16x16x32_bf16 v[228:231], a[12:15], v[36:39], v[228:231]\n"
"s_waitcnt vmcnt(8) lgkmcnt(9)\n"
"ds_read_b128 v[132:135], v179\n"
"v_cvt_pk_bf16_f32 v106, v106, v107\n"
"v_mfma_f32_16x16x32_bf16 v[228:231], a[24:27], v[40:43], v[228:231]\n"
"s_waitcnt lgkmcnt(9)\n"
"ds_read_b64 v[92:93], v179 offset:64\n"
"v_cvt_pk_bf16_f32 v107, v108, v109\n"
"v_mfma_f32_16x16x32_bf16 v[228:231], a[36:39], v[44:47], v[228:231]\n"
"s_waitcnt lgkmcnt(9)\n"
"ds_read_b128 v[112:115], v178 offset:13504\n"
"v_rcp_f32 v171, v90\n"
"v_mfma_f32_16x16x32_bf16 v[228:231], a[48:51], v[48:51], v[228:231]\n"
"s_waitcnt vmcnt(7) lgkmcnt(8)\n"
"ds_read_b64 v[90:91], v178 offset:64\n"
"v_cvt_pk_bf16_f32 v171, v171, v171\n"
"v_mfma_f32_16x16x32_bf16 v[136:139], v[136:139], v[152:155], 0\n"
"s_waitcnt lgkmcnt(8)\n"
"ds_read_b128 v[244:247], v179 offset:13504\n"
"v_and_b32 v64, v171, v176\n"
"v_mfma_f32_16x16x32_bf16 v[128:131], v[128:131], v[152:155], 0\n"
"s_waitcnt lgkmcnt(8)\n"
"v_and_b32 v65, v171, v177\n"
"v_cvt_pk_bf16_f32 v210, v204, v205\n"
"v_mfma_f32_16x16x32_bf16 v[136:139], v[80:83], v[164:167], v[136:139]\n"
"s_waitcnt lgkmcnt(7)\n"
"ds_write_b128 v202, a[198:201] offset:30720\n"
"v_mfma_f32_16x16x16_bf16 v[252:255], v[252:253], v[64:65], 0\n"
"s_waitcnt lgkmcnt(6)\n"
"v_cvt_pk_bf16_f32 v211, v206, v207\n"
"v_cvt_pk_bf16_f32 v220, v220, v221\n"
"v_mfma_f32_16x16x16_bf16 v[98:101], v[98:99], v[64:65], 0\n"
"s_waitcnt lgkmcnt(5)\n"
"v_cvt_pk_bf16_f32 v221, v222, v223\n"
"v_cvt_pk_bf16_f32 v224, v224, v225\n"
"v_mfma_f32_16x16x16_bf16 v[106:109], v[106:107], v[64:65], 0\n"
"ds_read_b128 v[116:119], v178 offset:27008\n"
"ds_read_b128 v[248:251], v179 offset:27008\n"
"v_mfma_f32_16x16x32_bf16 v[128:131], v[84:87], v[166:169], v[128:131]\n"
"s_waitcnt lgkmcnt(5)\n"
"v_cvt_pk_bf16_f32 v252, v252, v253\n"
"v_cvt_pk_bf16_f32 v253, v254, v255\n"
"v_mfma_f32_16x16x32_bf16 v[140:143], v[140:143], v[152:155], 0\n"
"s_waitcnt lgkmcnt(4)\n"
"ds_read_b128 v[68:71], v202 offset:36864\n"
"v_cvt_pk_bf16_f32 v254, v98, v99\n"
"v_mfma_f32_16x16x32_bf16 v[132:135], v[132:135], v[152:155], 0\n"
"s_waitcnt lgkmcnt(4)\n"
"ds_read_b64 v[98:99], v178 offset:13568\n"
"ds_write_b128 v186, v[208:211] offset:47264\n"
"v_mfma_f32_16x16x32_bf16 v[140:143], v[88:91], v[164:167], v[140:143]\n"
"v_cvt_pk_bf16_f32 v255, v100, v101\n"
"v_cvt_pk_bf16_f32 v106, v106, v107\n"
"v_mfma_f32_16x16x32_bf16 v[132:135], v[92:95], v[166:169], v[132:135]\n"
"buffer_store_dwordx4 v[252:255], v192, s[28:31], s37 offen sc1\n"
"v_mfma_f32_16x16x32_bf16 v[112:115], v[112:115], v[152:155], 0\n"
"s_waitcnt lgkmcnt(4)\n"
"ds_read_b64 v[100:101], v179 offset:13568\n"
"v_cvt_pk_bf16_f32 v107, v108, v109\n"
"v_mfma_f32_16x16x32_bf16 v[244:247], v[244:247], v[152:155], 0\n"
"s_waitcnt lgkmcnt(4)\n"
"ds_read_b64 v[108:109], v179 offset:27072\n"
"buffer_store_dwordx2 v[106:107], v193, s[28:31], s37 offen offset:1024 sc1\n"
"v_mfma_f32_16x16x32_bf16 v[116:119], v[116:119], v[152:155], 0\n"
"s_waitcnt lgkmcnt(3)\n"
"ds_read_b64 v[106:107], v178 offset:27072\n"
"ds_read_b64_tr_b16 v[120:121], v184 offset:128\n"
"v_mfma_f32_16x16x32_bf16 v[248:251], v[248:251], v[152:155], 0\n"
"s_add_u32 s37, s37, 1572864\n"
"ds_read_b64_tr_b16 v[122:123], v185 offset:128\n"
"v_max_f32 v136, v136, v128\n"
"v_mfma_f32_16x16x32_bf16 v[112:115], v[96:99], v[164:167], v[112:115]\n"
"ds_read_b64_tr_b16 v[124:125], v184 offset:896\n"
"v_max_f32 v137, v137, v129\n"
"v_mfma_f32_16x16x32_bf16 v[204:207], a[4:7], v[32:35], 0\n"
"buffer_store_dwordx4 v[68:71], v194, s[24:27], s36 offen sc1\n"
"v_mfma_f32_16x16x32_bf16 v[208:211], a[8:11], v[32:35], 0\n"
"s_waitcnt lgkmcnt(5)\n"
"ds_read_b128 v[68:71], v192 offset:56320\n"
"ds_read_b128 v[72:75], v202 offset:40960\n"
"v_mfma_f32_16x16x32_bf16 v[204:207], a[16:19], v[36:39], v[204:207]\n"
"s_waitcnt lgkmcnt(6)\n"
"ds_read_b64_tr_b16 v[126:127], v185 offset:896\n"
"v_max_f32 v138, v138, v130\n"
"v_mfma_f32_16x16x32_bf16 v[244:247], v[100:103], v[166:169], v[244:247]\n"
"ds_read_b64_tr_b16 v[128:129], v184 offset:136\n"
"v_max_f32 v139, v139, v131\n"
"v_mfma_f32_16x16x32_bf16 v[248:251], v[108:111], v[166:169], v[248:251]\n"
"s_waitcnt lgkmcnt(7)\n"
"ds_read_b64_tr_b16 v[130:131], v185 offset:136\n"
"v_max3_f32 v170, v136, v137, v138\n"
"v_mfma_f32_16x16x32_bf16 v[116:119], v[104:107], v[164:167], v[116:119]\n"
"ds_write_b64 v203, a[202:203] offset:30720\n"
"v_mfma_f32_16x16x32_bf16 v[208:211], a[20:23], v[36:39], v[208:211]\n"
"v_max_f32 v140, v140, v132\n"
"v_max_f32 v141, v141, v133\n"
"v_mfma_f32_16x16x32_bf16 v[204:207], a[28:31], v[40:43], v[204:207]\n"
"ds_read_b64_tr_b16 v[132:133], v184 offset:904\n"
"v_max_f32 v142, v142, v134\n"
"v_mfma_f32_16x16x32_bf16 v[208:211], a[32:35], v[40:43], v[208:211]\n"
"s_waitcnt lgkmcnt(6)\n"
"v_max_f32 v143, v143, v135\n"
"v_max3_f32 v170, v170, v139, v140\n"
"v_mfma_f32_16x16x32_bf16 v[204:207], a[40:43], v[44:47], v[204:207]\n"
"s_waitcnt lgkmcnt(5)\n"
"ds_read_b64_tr_b16 v[134:135], v185 offset:904\n"
"v_max3_f32 v170, v170, v141, v142\n"
"v_mfma_f32_16x16x32_bf16 v[232:235], a[228:231], v[68:71], v[232:235]\n"
"buffer_store_dwordx4 v[72:75], v195, s[24:27], s36 offen sc1\n"
"v_mfma_f32_16x16x32_bf16 v[236:239], a[232:235], v[68:71], v[236:239]\n"
"s_add_u32 s36, s36, s33\n"
"ds_read_b128 v[72:75], v192 offset:57344\n"
"v_max_f32 v112, v112, v244\n"
"v_mfma_f32_16x16x32_bf16 v[240:243], a[236:239], v[68:71], v[240:243]\n"
"ds_read_b128 v[68:71], v192 offset:59392\n"
"v_max_f32 v113, v113, v245\n"
"v_mfma_f32_16x16x32_bf16 v[208:211], a[44:47], v[44:47], v[208:211]\n"
"ds_read_b64_tr_b16 v[244:245], v184 offset:72\n"
"v_max_f32 v114, v114, v246\n"
"v_mfma_f32_16x16x32_bf16 v[204:207], a[52:55], v[48:51], v[204:207]\n"
"v_max_f32 v115, v115, v247\n"
"v_max3_f32 v170, v170, v143, v112\n"
"v_mfma_f32_16x16x32_bf16 v[208:211], a[56:59], v[48:51], v[208:211]\n"
"ds_read_b64_tr_b16 v[246:247], v185 offset:72\n"
"v_max3_f32 v170, v170, v113, v114\n"
"v_mfma_f32_16x16x32_bf16 v[228:231], a[60:63], v[52:55], v[228:231]\n"
"v_max_f32 v116, v116, v248\n"
"v_max_f32 v117, v117, v249\n"
"v_mfma_f32_16x16x32_bf16 v[204:207], a[64:67], v[52:55], v[204:207]\n"
"ds_read_b64_tr_b16 v[248:249], v184 offset:840\n"
"v_max_f32 v118, v118, v250\n"
"v_mfma_f32_16x16x32_bf16 v[208:211], a[68:71], v[52:55], v[208:211]\n"
"v_max_f32 v119, v119, v251\n"
"v_max3_f32 v170, v170, v115, v116\n"
"v_max3_f32 v170, v170, v117, v118\n"
"v_mov_b64 v[252:253], 0\n"
"v_mov_b64 v[254:255], 0\n"
"v_mov_b64 v[98:99], 0\n"
"v_mov_b64 v[100:101], 0\n"
"v_mov_b64 v[106:107], 0\n"
"v_mov_b64 v[108:109], 0\n"
"v_max_f32 v171, v170, v119\n"
"v_max_f32 v170, v170, v119\n"
"v_cvt_pk_bf16_f32 v225, v226, v227\n"
"v_cvt_pk_bf16_f32 v226, v212, v213\n"
"v_permlane16_swap_b32 v170, v171\n"
"v_max_f32 v170, v170, v171\n"
"v_mov_b32 v171, v170\n"
"v_cvt_pk_bf16_f32 v227, v214, v215\n"
"v_mfma_f32_16x16x32_bf16 v[212:215], a[72:75], v[32:35], 0\n"
"s_waitcnt lgkmcnt(4)\n"
"ds_read_b64_tr_b16 v[250:251], v185 offset:840\n"
"ds_write_b128 v186, v[224:227] offset:47392\n"
"v_mfma_f32_16x16x32_bf16 v[232:235], a[240:243], v[72:75], v[232:235]\n"
"v_permlane32_swap_b32 v170, v171\n"
"v_mfma_f32_16x16x32_bf16 v[236:239], a[244:247], v[72:75], v[236:239]\n"
"v_max_f32 v170, v170, v171\n"
"v_mul_f32 v170, 0xbe553b95, v170\n"
"v_mfma_f32_16x16x32_bf16 v[240:243], a[248:251], v[72:75], v[240:243]\n"
"ds_read_b128 v[72:75], v192 offset:60416\n"
"v_fmamk_f32 v136, v136, 0x3e553b95, v170\n"
"v_mfma_f32_16x16x32_bf16 v[232:235], a[252:255], v[76:79], v[232:235]\n"
"s_waitcnt lgkmcnt(6)\n"
"v_fmamk_f32 v137, v137, 0x3e553b95, v170\n"
"v_fmamk_f32 v138, v138, 0x3e553b95, v170\n"
"v_mfma_f32_16x16x32_bf16 v[240:243], v[4:7], v[76:79], v[240:243]\n"
"v_fmamk_f32 v139, v139, 0x3e553b95, v170\n"
"v_fmamk_f32 v140, v140, 0x3e553b95, v170\n"
"v_mfma_f32_16x16x32_bf16 v[236:239], v[0:3], v[76:79], v[236:239]\n"
"v_exp_f32 v136, v136\n"
"v_mfma_f32_16x16x32_bf16 v[232:235], v[8:11], v[68:71], v[232:235]\n"
"v_exp_f32 v137, v137\n"
"v_mfma_f32_16x16x32_bf16 v[240:243], v[16:19], v[68:71], v[240:243]\n"
"v_exp_f32 v138, v138\n"
"v_mfma_f32_16x16x32_bf16 v[236:239], v[12:15], v[68:71], v[236:239]\n"
"v_exp_f32 v139, v139\n"
"v_mfma_f32_16x16x32_bf16 v[212:215], a[84:87], v[36:39], v[212:215]\n"
"v_cvt_pk_bf16_f32 v82, v136, v138\n"
"v_cvt_pk_bf16_f32 v83, v137, v139\n"
"v_mfma_f32_16x16x32_bf16 v[212:215], a[96:99], v[40:43], v[212:215]\n"
"s_waitcnt lgkmcnt(0)\n"
"ds_read_b64_tr_b16 v[136:137], v182 offset:13632\n"
"ds_read_b64_tr_b16 v[138:139], v183 offset:13632\n"
"v_mfma_f32_16x16x32_bf16 v[232:235], v[20:23], v[72:75], v[232:235]\n"
"buffer_load_dword v200, off, s[28:31], s39 sc1\n"
"v_fmamk_f32 v141, v141, 0x3e553b95, v170\n"
"v_mfma_f32_16x16x32_bf16 v[240:243], v[28:31], v[72:75], v[240:243]\n"
"v_fmamk_f32 v142, v142, 0x3e553b95, v170\n"
"v_fmamk_f32 v143, v143, 0x3e553b95, v170\n"
"v_mfma_f32_16x16x32_bf16 v[236:239], v[24:27], v[72:75], v[236:239]\n"
"v_exp_f32 v140, v140\n"
"v_mfma_f32_16x16x32_bf16 v[212:215], a[108:111], v[44:47], v[212:215]\n"
"v_exp_f32 v141, v141\n"
"v_mfma_f32_16x16x32_bf16 v[212:215], a[120:123], v[48:51], v[212:215]\n"
"v_exp_f32 v142, v142\n"
"v_mfma_f32_16x16x32_bf16 v[212:215], a[132:135], v[52:55], v[212:215]\n"
"v_exp_f32 v143, v143\n"
"v_cvt_pk_bf16_f32 v84, v140, v142\n"
"v_cvt_pk_bf16_f32 v85, v141, v143\n"
"v_cvt_pk_bf16_f32 v222, v216, v217\n"
"v_cvt_pk_bf16_f32 v223, v218, v219\n"
"v_smfmac_f32_16x16x64_bf16 v[252:255], v[82:85], v[120:127], v66\n"
"ds_read_b64_tr_b16 v[140:141], v182 offset:14400\n"
"v_smfmac_f32_16x16x64_bf16 v[98:101], v[82:85], v[128:135], v66\n"
"ds_read_b64_tr_b16 v[142:143], v183 offset:14400\n"
"v_smfmac_f32_16x16x64_bf16 v[106:109], v[82:85], v[244:251], v66\n"
"ds_read_b64_tr_b16 v[120:121], v182 offset:13640\n"
"v_mfma_f32_16x16x32_bf16 v[82:85], v[172:175], v[82:85], 0\n"
"ds_read_b64_tr_b16 v[122:123], v183 offset:13640\n"
"ds_read_b64_tr_b16 v[124:125], v182 offset:14408\n"
"v_mfma_f32_16x16x32_bf16 v[216:219], a[76:79], v[32:35], 0\n"
"ds_write_b128 v186, v[220:223] offset:47328\n"
"v_mfma_f32_16x16x32_bf16 v[220:223], a[80:83], v[32:35], 0\n"
"v_cvt_pk_bf16_f32 v232, v232, v233\n"
"v_cvt_pk_bf16_f32 v233, v234, v235\n"
"v_mfma_f32_16x16x32_bf16 v[216:219], a[88:91], v[36:39], v[216:219]\n"
"v_cvt_pk_bf16_f32 v240, v240, v241\n"
"v_cvt_pk_bf16_f32 v234, v236, v237\n"
"v_mfma_f32_16x16x32_bf16 v[220:223], a[92:95], v[36:39], v[220:223]\n"
"ds_read_b64_tr_b16 v[126:127], v183 offset:14408\n"
"ds_read_b64_tr_b16 v[128:129], v182 offset:13576\n"
"v_mfma_f32_16x16x32_bf16 v[216:219], a[100:103], v[40:43], v[216:219]\n"
"ds_read_b64_tr_b16 v[130:131], v183 offset:13576\n"
"v_cvt_pk_bf16_f32 v235, v238, v239\n"
"v_mfma_f32_16x16x32_bf16 v[220:223], a[104:107], v[40:43], v[220:223]\n"
"ds_read_b64_tr_b16 v[132:133], v182 offset:14344\n"
"v_cvt_pk_bf16_f32 v241, v242, v243\n"
"v_mfma_f32_16x16x32_bf16 v[216:219], a[112:115], v[44:47], v[216:219]\n"
"ds_read_b64_tr_b16 v[134:135], v183 offset:14344\n"
"v_fmamk_f32 v112, v112, 0x3e553b95, v170\n"
"v_mfma_f32_16x16x32_bf16 v[220:223], a[116:119], v[44:47], v[220:223]\n"
"ds_write_b128 v196, v[232:235] offset:43008\n"
"v_mfma_f32_16x16x32_bf16 v[216:219], a[124:127], v[48:51], v[216:219]\n"
"v_fmamk_f32 v113, v113, 0x3e553b95, v170\n"
"v_fmamk_f32 v114, v114, 0x3e553b95, v170\n"
"v_mfma_f32_16x16x32_bf16 v[220:223], a[128:131], v[48:51], v[220:223]\n"
"v_fmamk_f32 v115, v115, 0x3e553b95, v170\n"
"v_fmamk_f32 v116, v116, 0x3e553b95, v170\n"
"v_mfma_f32_16x16x32_bf16 v[216:219], a[136:139], v[52:55], v[216:219]\n"
"ds_write_b64 v197, v[240:241] offset:43008\n"
"v_mfma_f32_16x16x32_bf16 v[220:223], a[140:143], v[52:55], v[220:223]\n"
"v_exp_f32 v112, v112\n"
"v_exp_f32 v113, v113\n"
"v_exp_f32 v114, v114\n"
"v_exp_f32 v115, v115\n"
"v_cvt_pk_bf16_f32 v90, v112, v114\n"
"v_cvt_pk_bf16_f32 v91, v113, v115\n"
"v_fmamk_f32 v117, v117, 0x3e553b95, v170\n"
"v_fmamk_f32 v118, v118, 0x3e553b95, v170\n"
"v_fmamk_f32 v119, v119, 0x3e553b95, v170\n"
"v_exp_f32 v116, v116\n"
"v_exp_f32 v117, v117\n"
"v_exp_f32 v118, v118\n"
"v_exp_f32 v119, v119\n"
"v_cvt_pk_bf16_f32 v92, v116, v118\n"
"v_cvt_pk_bf16_f32 v93, v117, v119\n"
"s_mov_b64 exec, s[44:45]\n"
"v_add_u32 v181, 0xc0, v181\n"
"s_mov_b64 exec, -1\n"
"v_cvt_pk_bf16_f32 v228, v228, v229\n"
"v_cvt_pk_bf16_f32 v229, v230, v231\n"
"v_cvt_pk_bf16_f32 v230, v204, v205\n"
"v_cvt_pk_bf16_f32 v231, v206, v207\n"
"v_cvt_pk_bf16_f32 v208, v208, v209\n"
"v_cvt_pk_bf16_f32 v209, v210, v211\n"
"v_cvt_pk_bf16_f32 v212, v212, v213\n"
"v_cvt_pk_bf16_f32 v213, v214, v215\n"
"v_cvt_pk_bf16_f32 v214, v216, v217\n"
"v_cvt_pk_bf16_f32 v215, v218, v219\n"
"v_cvt_pk_bf16_f32 v210, v220, v221\n"
"v_cvt_pk_bf16_f32 v211, v222, v223\n"
"s_waitcnt vmcnt(3) lgkmcnt(0)\n"
"v_smfmac_f32_16x16x64_bf16 v[252:255], v[90:93], v[136:143], v66\n"
"s_barrier\n"
"v_smfmac_f32_16x16x64_bf16 v[98:101], v[90:93], v[120:127], v66\n"
"v_or_b32 v201, 11, s6\n"
"v_smfmac_f32_16x16x64_bf16 v[106:109], v[90:93], v[128:135], v66\n"
"s_mov_b64 exec, 1\n"
"buffer_store_dword v201, off, s[28:31], s40 sc1\n"
"s_mov_b64 exec, -1\n"
"v_mfma_f32_16x16x32_bf16 v[90:93], v[172:175], v[90:93], v[82:85]\n"
"ds_read_b128 v[72:75], v192 offset:30720\n"
"v_cvt_pk_bf16_f32 v252, v252, v253\n"
"v_cvt_pk_bf16_f32 v253, v254, v255\n"
"v_cvt_pk_bf16_f32 v98, v98, v99\n"
"v_cvt_pk_bf16_f32 v99, v100, v101\n"
"v_cvt_pk_bf16_f32 v106, v106, v107\n"
"v_cvt_pk_bf16_f32 v107, v108, v109\n"
"ds_read_b128 v[244:247], v178\n"
"v_rcp_f32 v171, v90\n"
"ds_read_b128 v[112:115], v179\n"
"v_cvt_pk_bf16_f32 v171, v171, v171\n"
"v_and_b32 v64, v171, v176\n"
"v_and_b32 v65, v171, v177\n"
"s_nop 1\n"
"v_mfma_f32_16x16x16_bf16 v[252:255], v[252:253], v[64:65], 0\n"
"ds_read_b64 v[82:83], v178 offset:64\n"
"ds_read_b64 v[84:85], v179 offset:64\n"
"v_mfma_f32_16x16x16_bf16 v[98:101], v[98:99], v[64:65], 0\n"
"ds_read_b128 v[248:251], v178 offset:13504\n"
"v_mfma_f32_16x16x16_bf16 v[106:109], v[106:107], v[64:65], 0\n"
"s_waitcnt lgkmcnt(5)\n"
"ds_read_b128 v[116:119], v179 offset:13504\n"
"v_mfma_f32_16x16x32_bf16 v[232:235], a[216:219], v[72:75], 0\n"
"ds_read_b64 v[90:91], v178 offset:13568\n"
"v_cvt_pk_bf16_f32 v252, v252, v253\n"
"v_mfma_f32_16x16x32_bf16 v[236:239], a[220:223], v[72:75], 0\n"
"s_waitcnt lgkmcnt(6)\n"
"ds_write_b128 v202, a[204:207] offset:55296\n"
"v_mfma_f32_16x16x32_bf16 v[244:247], v[244:247], v[144:147], 0\n"
"s_waitcnt lgkmcnt(6)\n"
"v_cvt_pk_bf16_f32 v253, v254, v255\n"
"v_cvt_pk_bf16_f32 v254, v98, v99\n"
"v_mfma_f32_16x16x32_bf16 v[112:115], v[112:115], v[144:147], 0\n"
"v_cvt_pk_bf16_f32 v255, v100, v101\n"
"v_cvt_pk_bf16_f32 v106, v106, v107\n"
"v_mfma_f32_16x16x32_bf16 v[240:243], a[224:227], v[72:75], 0\n"
"s_waitcnt lgkmcnt(5)\n"
"ds_read_b128 v[72:75], v192 offset:33792\n"
"ds_read_b64 v[92:93], v179 offset:13568\n"
"v_mfma_f32_16x16x32_bf16 v[244:247], v[80:83], v[156:159], v[244:247]\n"
"s_waitcnt lgkmcnt(6)\n"
"ds_read_b128 v[136:139], v178 offset:27008\n"
"v_cvt_pk_bf16_f32 v107, v108, v109\n"
"v_mfma_f32_16x16x32_bf16 v[112:115], v[84:87], v[158:161], v[112:115]\n"
"s_waitcnt lgkmcnt(6)\n"
"ds_read_b128 v[120:123], v179 offset:27008\n"
"v_min_u32 v252, -2, v252\n"
"v_mfma_f32_16x16x32_bf16 v[248:251], v[248:251], v[144:147], 0\n"
"s_waitcnt lgkmcnt(6)\n"
"ds_read_b64 v[98:99], v178 offset:27072\n"
"v_min_u32 v253, -2, v253\n"
"v_mfma_f32_16x16x32_bf16 v[116:119], v[116:119], v[144:147], 0\n"
"s_waitcnt lgkmcnt(6)\n"
"ds_read_b64 v[100:101], v179 offset:27072\n"
"v_min_u32 v254, -2, v254\n"
"v_mfma_f32_16x16x32_bf16 v[248:251], v[88:91], v[156:159], v[248:251]\n"
"v_min_u32 v255, -2, v255\n"
"buffer_store_dwordx4 v[252:255], v192, s[28:31], s37 offen sc1\n"
"ds_read_b128 v[140:143], v180\n"
"ds_read_b128 v[124:127], v181\n"
"ds_read_b128 v[76:79], v202 offset:43008\n"
"v_min_u32 v106, -2, v106\n"
"v_min_u32 v107, -2, v107\n"
"v_max_f32 v244, v244, v112\n"
"v_max_f32 v245, v245, v113\n"
"v_max_f32 v246, v246, v114\n"
"v_max_f32 v247, v247, v115\n"
"v_max3_f32 v170, v244, v245, v246\n"
"v_mov_b64 v[252:253], 0\n"
"v_mov_b64 v[254:255], 0\n"
"s_waitcnt lgkmcnt(6)\n"
"v_mfma_f32_16x16x32_bf16 v[116:119], v[92:95], v[158:161], v[116:119]\n"
"s_waitcnt lgkmcnt(5)\n"
"buffer_store_dwordx2 v[106:107], v193, s[28:31], s37 offen offset:1024 sc1\n"
"v_mfma_f32_16x16x32_bf16 v[136:139], v[136:139], v[144:147], 0\n"
"s_waitcnt lgkmcnt(4)\n"
"ds_read_b64 v[106:107], v180 offset:64\n"
"ds_read_b64 v[108:109], v181 offset:64\n"
"v_mfma_f32_16x16x32_bf16 v[120:123], v[120:123], v[144:147], 0\n"
"s_waitcnt lgkmcnt(5)\n"
"ds_write_b128 v198, v[228:231]\n"
"v_mfma_f32_16x16x32_bf16 v[136:139], v[96:99], v[156:159], v[136:139]\n"
"s_waitcnt lgkmcnt(5)\n"
"v_max_f32 v248, v248, v116\n"
"v_max_f32 v249, v249, v117\n"
"v_mfma_f32_16x16x32_bf16 v[120:123], v[100:103], v[158:161], v[120:123]\n"
"s_waitcnt vmcnt(3) lgkmcnt(4)\n"
"v_max_f32 v250, v250, v118\n"
"v_add3_u32 v200, v200, s50, -10\n"
"v_mfma_f32_16x16x32_bf16 v[140:143], v[140:143], v[144:147], 0\n"
"s_waitcnt lgkmcnt(3)\n"
"v_cmp_lt_u32 vcc, 64, v200\n"
"v_max_f32 v251, v251, v119\n"
"v_mfma_f32_16x16x32_bf16 v[124:127], v[124:127], v[144:147], 0\n"
"s_add_u32 s37, s37, 1572864\n"
"ds_write_b64 v203, a[208:209] offset:55296\n"
"buffer_store_dwordx4 v[76:79], v194, s[24:27], s36 offen sc1\n"
"s_nop 1\n"
"ds_read_b128 v[76:79], v192 offset:31744\n"
"ds_read_b64_tr_b16 v[128:129], v182 offset:128\n"
"ds_read_b128 v[68:71], v202 offset:47104\n"
"v_max3_f32 v170, v170, v247, v248\n"
"v_max3_f32 v170, v170, v249, v250\n"
"v_max_f32 v136, v136, v120\n"
"v_max_f32 v137, v137, v121\n"
"v_max_f32 v138, v138, v122\n"
"v_max_f32 v139, v139, v123\n"
"v_max3_f32 v170, v170, v251, v136\n"
"v_max3_f32 v170, v170, v137, v138\n"
"v_mov_b64 v[98:99], 0\n"
"v_mov_b64 v[100:101], 0\n"
"s_waitcnt lgkmcnt(5)\n"
"v_mfma_f32_16x16x32_bf16 v[140:143], v[104:107], v[156:159], v[140:143]\n"
"s_cbranch_vccnz .Lp9%=\n"
".Lr9%=:\n"
"v_mfma_f32_16x16x32_bf16 v[124:127], v[108:111], v[158:161], v[124:127]\n"
"buffer_load_dwordx4 a[144:147], v192, s[28:31], s12 offen sc1\n"
"ds_read_b64_tr_b16 v[130:131], v183 offset:128\n"
"ds_read_b64_tr_b16 v[132:133], v182 offset:896\n"
"ds_read_b64_tr_b16 v[134:135], v183 offset:896\n"
"v_mov_b64 v[106:107], 0\n"
"v_mov_b64 v[108:109], 0\n"
"ds_read_b64_tr_b16 v[112:113], v182 offset:136\n"
"s_nop 0\n"
"v_max_f32 v140, v140, v124\n"
"v_max_f32 v141, v141, v125\n"
"v_max_f32 v142, v142, v126\n"
"v_max_f32 v143, v143, v127\n"
"v_max3_f32 v170, v170, v139, v140\n"
"v_max3_f32 v170, v170, v141, v142\n"
"v_max_f32 v171, v170, v143\n"
"v_max_f32 v170, v170, v143\n"
"s_waitcnt lgkmcnt(6)\n"
"v_mfma_f32_16x16x32_bf16 v[232:235], a[228:231], v[76:79], v[232:235]\n"
"s_waitcnt lgkmcnt(4)\n"
"buffer_load_dwordx2 a[148:149], v193, s[28:31], s12 offen offset:1024 sc1\n"
"ds_read_b64_tr_b16 v[114:115], v183 offset:136\n"
"v_mfma_f32_16x16x32_bf16 v[236:239], a[232:235], v[76:79], v[236:239]\n"
"ds_read_b64_tr_b16 v[116:117], v182 offset:904\n"
"ds_write_b128 v202, a[210:213] offset:36864\n"
"v_mfma_f32_16x16x32_bf16 v[240:243], a[236:239], v[76:79], v[240:243]\n"
"buffer_load_dwordx4 a[150:153], v192, s[28:31], s13 offen sc1\n"
"v_permlane16_swap_b32 v170, v171\n"
"v_max_f32 v170, v170, v171\n"
"v_mov_b32 v171, v170\n"
"ds_read_b128 v[76:79], v192 offset:34816\n"
"ds_read_b64_tr_b16 v[118:119], v183 offset:904\n"
"ds_read_b64_tr_b16 v[120:121], v182 offset:72\n"
"v_permlane32_swap_b32 v170, v171\n"
"v_max_f32 v170, v170, v171\n"
"ds_read_b64_tr_b16 v[122:123], v183 offset:72\n"
"buffer_load_dwordx2 a[154:155], v193, s[28:31], s13 offen offset:1024 sc1\n"
"ds_read_b64_tr_b16 v[124:125], v182 offset:840\n"
"v_mul_f32 v170, 0xbe553b95, v170\n"
"v_fmamk_f32 v244, v244, 0x3e553b95, v170\n"
"v_fmamk_f32 v245, v245, 0x3e553b95, v170\n"
"ds_read_b64_tr_b16 v[126:127], v183 offset:840\n"
"v_fmamk_f32 v246, v246, 0x3e553b95, v170\n"
"v_fmamk_f32 v247, v247, 0x3e553b95, v170\n"
"ds_write_b64 v203, a[214:215] offset:36864\n"
"v_exp_f32 v244, v244\n"
"buffer_store_dwordx4 v[68:71], v195, s[24:27], s36 offen sc1\n"
"s_nop 1\n"
"ds_read_b128 v[68:71], v192 offset:32768\n"
"s_add_u32 s36, s36, s33\n"
"v_exp_f32 v245, v245\n"
"v_exp_f32 v246, v246\n"
"v_exp_f32 v247, v247\n"
"v_cvt_pk_bf16_f32 v82, v244, v246\n"
"v_cvt_pk_bf16_f32 v83, v245, v247\n"
"v_fmamk_f32 v248, v248, 0x3e553b95, v170\n"
"v_fmamk_f32 v249, v249, 0x3e553b95, v170\n"
"v_fmamk_f32 v250, v250, 0x3e553b95, v170\n"
"v_fmamk_f32 v251, v251, 0x3e553b95, v170\n"
"v_exp_f32 v248, v248\n"
"v_exp_f32 v249, v249\n"
"v_exp_f32 v250, v250\n"
"v_exp_f32 v251, v251\n"
"v_cvt_pk_bf16_f32 v84, v248, v250\n"
"v_cvt_pk_bf16_f32 v85, v249, v251\n"
"v_fmamk_f32 v136, v136, 0x3e553b95, v170\n"
"v_fmamk_f32 v137, v137, 0x3e553b95, v170\n"
"v_fmamk_f32 v138, v138, 0x3e553b95, v170\n"
"v_fmamk_f32 v139, v139, 0x3e553b95, v170\n"
"v_exp_f32 v136, v136\n"
"v_exp_f32 v137, v137\n"
"v_exp_f32 v138, v138\n"
"v_exp_f32 v139, v139\n"
"v_cvt_pk_bf16_f32 v90, v136, v138\n"
"v_cvt_pk_bf16_f32 v91, v137, v139\n"
"v_fmamk_f32 v140, v140, 0x3e553b95, v170\n"
"v_fmamk_f32 v141, v141, 0x3e553b95, v170\n"
"v_fmamk_f32 v142, v142, 0x3e553b95, v170\n"
"v_fmamk_f32 v143, v143, 0x3e553b95, v170\n"
"v_exp_f32 v140, v140\n"
"v_exp_f32 v141, v141\n"
"v_exp_f32 v142, v142\n"
"v_exp_f32 v143, v143\n"
"v_cvt_pk_bf16_f32 v92, v140, v142\n"
"v_cvt_pk_bf16_f32 v93, v141, v143\n"
"s_waitcnt lgkmcnt(0)\n"
"v_mfma_f32_16x16x32_bf16 v[232:235], a[240:243], v[68:71], v[232:235]\n"
"ds_read_b64_tr_b16 v[244:245], v182 offset:27136\n"
"ds_read_b64_tr_b16 v[246:247], v183 offset:27136\n"
"v_mfma_f32_16x16x32_bf16 v[236:239], a[244:247], v[68:71], v[236:239]\n"
"ds_read_b64_tr_b16 v[248:249], v182 offset:27904\n"
"v_mfma_f32_16x16x32_bf16 v[240:243], a[248:251], v[68:71], v[240:243]\n"
"ds_read_b128 v[68:71], v192 offset:35840\n"
"v_smfmac_f32_16x16x64_bf16 v[252:255], v[82:85], v[128:135], v66\n"
"ds_read_b64_tr_b16 v[250:251], v183 offset:27904\n"
"v_mfma_f32_16x16x32_bf16 v[232:235], a[252:255], v[72:75], v[232:235]\n"
"ds_read_b64_tr_b16 v[128:129], v182 offset:27144\n"
"v_smfmac_f32_16x16x64_bf16 v[98:101], v[82:85], v[112:119], v66\n"
"ds_read_b64_tr_b16 v[130:131], v183 offset:27144\n"
"v_mfma_f32_16x16x32_bf16 v[236:239], v[0:3], v[72:75], v[236:239]\n"
".Lic7%=:\n"
"s_cbranch_execz .Lib1%=\n"
"ds_write_b128 v198, v[208:211] offset:64\n"
"v_smfmac_f32_16x16x64_bf16 v[106:109], v[82:85], v[120:127], v66\n"
"v_mfma_f32_16x16x32_bf16 v[240:243], v[4:7], v[72:75], v[240:243]\n"
"ds_read_b64_tr_b16 v[132:133], v182 offset:27912\n"
"v_mfma_f32_16x16x32_bf16 v[82:85], v[172:175], v[82:85], 0\n"
"ds_read_b64_tr_b16 v[134:135], v183 offset:27912\n"
"buffer_load_dword v201, off, s[28:31], s39 sc1\n"
"v_mfma_f32_16x16x32_bf16 v[232:235], v[8:11], v[76:79], v[232:235]\n"
"s_waitcnt lgkmcnt(6)\n"
"ds_read_b64_tr_b16 v[112:113], v182 offset:27080\n"
"ds_read_b64_tr_b16 v[114:115], v183 offset:27080\n"
"v_mfma_f32_16x16x32_bf16 v[236:239], v[12:15], v[76:79], v[236:239]\n"
"s_waitcnt lgkmcnt(7)\n"
"ds_read_b64_tr_b16 v[116:117], v182 offset:27848\n"
"v_mfma_f32_16x16x32_bf16 v[240:243], v[16:19], v[76:79], v[240:243]\n"
"ds_read_b64_tr_b16 v[118:119], v183 offset:27848\n"
"v_mfma_f32_16x16x32_bf16 v[232:235], v[20:23], v[68:71], v[232:235]\n"
"ds_write_b128 v198, v[212:215] offset:128\n"
"v_mfma_f32_16x16x32_bf16 v[236:239], v[24:27], v[68:71], v[236:239]\n"
"v_smfmac_f32_16x16x64_bf16 v[252:255], v[90:93], v[244:251], v66\n"
"ds_read_b128 v[120:123], v178 offset:13504\n"
"v_mfma_f32_16x16x32_bf16 v[240:243], v[28:31], v[68:71], v[240:243]\n"
"s_waitcnt lgkmcnt(6)\n"
"ds_read_b128 v[136:139], v179 offset:13504\n"
"ds_read_b128 v[124:127], v178 offset:27008\n"
"v_smfmac_f32_16x16x64_bf16 v[98:101], v[90:93], v[128:135], v66\n"
"v_cvt_pk_bf16_f32 v232, v232, v233\n"
"v_cvt_pk_bf16_f32 v233, v234, v235\n"
"v_cvt_pk_bf16_f32 v234, v236, v237\n"
"v_cvt_pk_bf16_f32 v252, v252, v253\n"
"v_cvt_pk_bf16_f32 v253, v254, v255\n"
"v_cvt_pk_bf16_f32 v235, v238, v239\n"
"v_cvt_pk_bf16_f32 v240, v240, v241\n"
"v_cvt_pk_bf16_f32 v241, v242, v243\n"
"v_cvt_pk_bf16_f32 v98, v98, v99\n"
"v_cvt_pk_bf16_f32 v99, v100, v101\n"
"ds_write_b128 v196, v[232:235] offset:49152\n"
"s_waitcnt lgkmcnt(5)\n"
"v_smfmac_f32_16x16x64_bf16 v[106:109], v[90:93], v[112:119], v66\n"
"s_waitcnt lgkmcnt(4)\n"
"s_barrier\n"
"v_mfma_f32_16x16x32_bf16 v[90:93], v[172:175], v[90:93], v[82:85]\n"
"s_waitcnt lgkmcnt(2)\n"
"ds_read_b128 v[72:75], v192 offset:55296\n"
"ds_read_b128 v[76:79], v192 offset:56320\n"
"v_mfma_f32_16x16x32_bf16 v[120:123], v[120:123], v[148:151], 0\n"
"s_waitcnt lgkmcnt(3)\n"
"ds_read_b128 v[68:71], v192 offset:57344\n"
"v_cvt_pk_bf16_f32 v106, v106, v107\n"
"v_mfma_f32_16x16x32_bf16 v[136:139], v[136:139], v[148:151], 0\n"
"ds_write_b64 v197, v[240:241] offset:49152\n"
"v_mfma_f32_16x16x32_bf16 v[124:127], v[124:127], v[148:151], 0\n"
"v_rcp_f32 v171, v90\n"
"v_cvt_pk_bf16_f32 v107, v108, v109\n"
"v_cvt_pk_bf16_f32 v171, v171, v171\n"
"v_and_b32 v64, v171, v176\n"
"v_and_b32 v65, v171, v177\n"
"s_nop 1\n"
"v_mfma_f32_16x16x16_bf16 v[252:255], v[252:253], v[64:65], 0\n"
"ds_read_b64 v[82:83], v178 offset:13568\n"
"ds_read_b64 v[84:85], v179 offset:13568\n"
"v_mfma_f32_16x16x16_bf16 v[98:101], v[98:99], v[64:65], 0\n"
"ds_read_b128 v[140:143], v179 offset:27008\n"
"v_mfma_f32_16x16x16_bf16 v[106:109], v[106:107], v[64:65], 0\n"
"s_waitcnt lgkmcnt(6)\n"
"ds_read_b64 v[90:91], v178 offset:27072\n"
"v_mfma_f32_16x16x32_bf16 v[232:235], a[216:219], v[72:75], 0\n"
"s_waitcnt lgkmcnt(6)\n"
"ds_read_b64 v[92:93], v179 offset:27072\n"
"v_cvt_pk_bf16_f32 v252, v252, v253\n"
"v_mfma_f32_16x16x32_bf16 v[236:239], a[220:223], v[72:75], 0\n"
"ds_read_b128 v[244:247], v180\n"
"v_cvt_pk_bf16_f32 v253, v254, v255\n"
"v_mfma_f32_16x16x32_bf16 v[240:243], a[224:227], v[72:75], 0\n"
"buffer_load_dwordx4 v[228:231], v192, s[28:31], s14 offen sc1\n"
"ds_read_b128 v[72:75], v192 offset:58368\n"
"v_mfma_f32_16x16x32_bf16 v[232:235], a[228:231], v[76:79], v[232:235]\n"
"s_waitcnt lgkmcnt(8)\n"
"ds_read_b128 v[128:131], v181\n"
"v_cvt_pk_bf16_f32 v254, v98, v99\n"
"v_mfma_f32_16x16x32_bf16 v[236:239], a[232:235], v[76:79], v[236:239]\n"
"ds_read_b64 v[98:99], v180 offset:64\n"
"v_cvt_pk_bf16_f32 v255, v100, v101\n"
"v_mfma_f32_16x16x32_bf16 v[240:243], a[236:239], v[76:79], v[240:243]\n"
"s_waitcnt lgkmcnt(8)\n"
"buffer_load_dwordx2 v[152:153], v193, s[28:31], s14 offen offset:1024 sc1\n"
"ds_read_b128 v[76:79], v192 offset:59392\n"
"v_mfma_f32_16x16x32_bf16 v[120:123], v[80:83], v[160:163], v[120:123]\n"
"s_waitcnt lgkmcnt(8)\n"
"ds_read_b64 v[100:101], v181 offset:64\n"
"v_cvt_pk_bf16_f32 v106, v106, v107\n"
"v_mfma_f32_16x16x32_bf16 v[136:139], v[84:87], v[162:165], v[136:139]\n"
"s_waitcnt lgkmcnt(8)\n"
"ds_read_b128 v[248:251], v180 offset:13504\n"
"v_cvt_pk_bf16_f32 v107, v108, v109\n"
"v_mfma_f32_16x16x32_bf16 v[232:235], a[240:243], v[68:71], v[232:235]\n"
"s_waitcnt lgkmcnt(8)\n"
"ds_read_b128 v[132:135], v181 offset:13504\n"
"v_min_u32 v252, -2, v252\n"
"v_mfma_f32_16x16x32_bf16 v[236:239], a[244:247], v[68:71], v[236:239]\n"
"ds_read_b64 v[108:109], v181 offset:13568\n"
"v_min_u32 v253, -2, v253\n"
"v_mfma_f32_16x16x32_bf16 v[240:243], a[248:251], v[68:71], v[240:243]\n"
"s_waitcnt lgkmcnt(8)\n"
"ds_read_b128 v[68:71], v192 offset:60416\n"
"v_min_u32 v254, -2, v254\n"
"v_mfma_f32_16x16x32_bf16 v[140:143], v[140:143], v[148:151], 0\n"
"s_waitcnt lgkmcnt(8)\n"
"v_min_u32 v255, -2, v255\n"
"v_min_u32 v106, -2, v106\n"
"v_mfma_f32_16x16x32_bf16 v[124:127], v[88:91], v[160:163], v[124:127]\n"
"s_waitcnt lgkmcnt(7)\n"
"buffer_store_dwordx4 v[252:255], v192, s[28:31], s37 offen sc1\n"
"v_mfma_f32_16x16x32_bf16 v[140:143], v[92:95], v[162:165], v[140:143]\n"
"v_min_u32 v107, -2, v107\n"
"v_max_f32 v120, v120, v136\n"
"v_mfma_f32_16x16x32_bf16 v[232:235], a[252:255], v[72:75], v[232:235]\n"
"s_waitcnt lgkmcnt(5)\n"
"ds_read_b64_tr_b16 v[112:113], v182 offset:13632\n"
"buffer_store_dwordx2 v[106:107], v193, s[28:31], s37 offen offset:1024 sc1\n"
"v_mfma_f32_16x16x32_bf16 v[236:239], v[0:3], v[72:75], v[236:239]\n"
"s_waitcnt lgkmcnt(3)\n"
"ds_read_b64 v[106:107], v180 offset:13568\n"
"ds_read_b64_tr_b16 v[114:115], v183 offset:13632\n"
"v_mfma_f32_16x16x32_bf16 v[240:243], v[4:7], v[72:75], v[240:243]\n"
"s_add_u32 s37, s37, 1572864\n"
"ds_read_b128 v[72:75], v192 offset:36864\n"
"v_max_f32 v121, v121, v137\n"
"v_mfma_f32_16x16x32_bf16 v[244:247], v[244:247], v[148:151], 0\n"
"ds_read_b64_tr_b16 v[116:117], v182 offset:14400\n"
"v_max_f32 v122, v122, v138\n"
"v_mfma_f32_16x16x32_bf16 v[232:235], v[8:11], v[76:79], v[232:235]\n"
"s_waitcnt lgkmcnt(5)\n"
"ds_read_b64_tr_b16 v[118:119], v183 offset:14400\n"
"v_max_f32 v123, v123, v139\n"
"v_mfma_f32_16x16x32_bf16 v[236:239], v[12:15], v[76:79], v[236:239]\n"
"ds_read_b64_tr_b16 v[136:137], v182 offset:13640\n"
"v_max3_f32 v170, v120, v121, v122\n"
"v_mfma_f32_16x16x32_bf16 v[240:243], v[16:19], v[76:79], v[240:243]\n"
"ds_read_b128 v[76:79], v192 offset:37888\n"
"v_max_f32 v124, v124, v140\n"
"v_mfma_f32_16x16x32_bf16 v[128:131], v[128:131], v[148:151], 0\n"
"ds_read_b64_tr_b16 v[138:139], v183 offset:13640\n"
"v_max_f32 v125, v125, v141\n"
"v_mfma_f32_16x16x32_bf16 v[232:235], v[20:23], v[68:71], v[232:235]\n"
"ds_read_b64_tr_b16 v[140:141], v182 offset:14408\n"
"v_max_f32 v126, v126, v142\n"
"v_mfma_f32_16x16x32_bf16 v[236:239], v[24:27], v[68:71], v[236:239]\n"
"v_max_f32 v127, v127, v143\n"
"v_max3_f32 v170, v170, v123, v124\n"
"v_mfma_f32_16x16x32_bf16 v[240:243], v[28:31], v[68:71], v[240:243]\n"
"ds_read_b128 v[68:71], v192 offset:38912\n"
"ds_read_b64_tr_b16 v[142:143], v183 offset:14408\n"
"v_mfma_f32_16x16x32_bf16 v[244:247], v[96:99], v[160:163], v[244:247]\n"
"s_waitcnt lgkmcnt(8)\n"
"v_max3_f32 v170, v170, v125, v126\n"
"v_mov_b64 v[252:253], 0\n"
"v_mfma_f32_16x16x32_bf16 v[128:131], v[100:103], v[162:165], v[128:131]\n"
"s_waitcnt vmcnt(9)\n"
"v_mov_b64 v[254:255], 0\n"
"v_mov_b64 v[98:99], 0\n"
"v_mfma_f32_16x16x32_bf16 v[248:251], v[248:251], v[148:151], 0\n"
"ds_write_b128 v202, a[144:147] offset:24576\n"
"v_mfma_f32_16x16x32_bf16 v[204:207], a[216:219], v[72:75], 0\n"
"s_waitcnt lgkmcnt(5)\n"
"v_mov_b64 v[100:101], 0\n"
"v_max_f32 v244, v244, v128\n"
"v_mfma_f32_16x16x32_bf16 v[208:211], a[220:223], v[72:75], 0\n"
"v_max_f32 v245, v245, v129\n"
"v_max_f32 v246, v246, v130\n"
"v_mfma_f32_16x16x32_bf16 v[212:215], a[224:227], v[72:75], 0\n"
"ds_read_b128 v[72:75], v192 offset:39936\n"
"ds_read_b64_tr_b16 v[128:129], v182 offset:13576\n"
"v_mfma_f32_16x16x32_bf16 v[132:135], v[132:135], v[148:151], 0\n"
"s_waitcnt vmcnt(6)\n"
"ds_write_b64 v203, a[148:149] offset:24576\n"
"v_mfma_f32_16x16x32_bf16 v[204:207], a[228:231], v[76:79], v[204:207]\n"
"s_waitcnt lgkmcnt(5)\n"
"v_max_f32 v247, v247, v131\n"
"v_max3_f32 v170, v170, v127, v244\n"
"v_mfma_f32_16x16x32_bf16 v[208:211], a[232:235], v[76:79], v[208:211]\n"
"ds_write_b128 v202, a[150:153] offset:30720\n"
"v_mfma_f32_16x16x32_bf16 v[212:215], a[236:239], v[76:79], v[212:215]\n"
"v_max3_f32 v170, v170, v245, v246\n"
"v_cvt_pk_bf16_f32 v232, v232, v233\n"
"v_mfma_f32_16x16x32_bf16 v[248:251], v[104:107], v[160:163], v[248:251]\n"
"v_mov_b64 v[106:107], 0\n"
"v_cvt_pk_bf16_f32 v233, v234, v235\n"
"v_mfma_f32_16x16x32_bf16 v[204:207], a[240:243], v[68:71], v[204:207]\n"
"ds_write_b64 v203, a[154:155] offset:30720\n"
"v_mfma_f32_16x16x32_bf16 v[208:211], a[244:247], v[68:71], v[208:211]\n"
"s_waitcnt lgkmcnt(4)\n"
"v_cvt_pk_bf16_f32 v234, v236, v237\n"
"v_cvt_pk_bf16_f32 v235, v238, v239\n"
"v_mfma_f32_16x16x32_bf16 v[212:215], a[248:251], v[68:71], v[212:215]\n"
"ds_read_b128 v[76:79], v192 offset:40960\n"
"ds_read_b128 v[68:71], v192 offset:41984\n"
"v_mfma_f32_16x16x32_bf16 v[132:135], v[108:111], v[162:165], v[132:135]\n"
"ds_read_b64_tr_b16 v[130:131], v183 offset:13576\n"
"v_mov_b64 v[108:109], 0\n"
"v_mfma_f32_16x16x32_bf16 v[204:207], a[252:255], v[72:75], v[204:207]\n"
"ds_write_b128 v196, v[232:235]\n"
"v_mfma_f32_16x16x32_bf16 v[208:211], v[0:3], v[72:75], v[208:211]\n"
"s_waitcnt vmcnt(4)\n"
"v_add3_u32 v201, v201, s50, -11\n"
"v_cmp_lt_u32 vcc, 64, v201\n"
"v_mfma_f32_16x16x32_bf16 v[212:215], v[4:7], v[72:75], v[212:215]\n"
"v_max_f32 v248, v248, v132\n"
"v_max_f32 v249, v249, v133\n"
"v_max_f32 v250, v250, v134\n"
"v_max_f32 v251, v251, v135\n"
"v_max3_f32 v170, v170, v247, v248\n"
"v_max3_f32 v170, v170, v249, v250\n"
"v_max_f32 v171, v170, v251\n"
"v_max_f32 v170, v170, v251\n"
"v_cvt_pk_bf16_f32 v240, v240, v241\n"
"v_cvt_pk_bf16_f32 v241, v242, v243\n"
"v_permlane16_swap_b32 v170, v171\n"
"v_max_f32 v170, v170, v171\n"
"v_mov_b32 v171, v170\n"
"s_waitcnt lgkmcnt(3)\n"
"v_mfma_f32_16x16x32_bf16 v[204:207], v[8:11], v[76:79], v[204:207]\n"
"s_waitcnt lgkmcnt(2)\n"
"ds_read_b64_tr_b16 v[132:133], v182 offset:14344\n"
"ds_read_b64_tr_b16 v[134:135], v183 offset:14344\n"
"v_mfma_f32_16x16x32_bf16 v[208:211], v[12:15], v[76:79], v[208:211]\n"
"s_cbranch_vccnz .Lp10%=\n"
".Lr10%=:\n"
"v_mfma_f32_16x16x32_bf16 v[212:215], v[16:19], v[76:79], v[212:215]\n"
"s_barrier\n"
"v_mfma_f32_16x16x32_bf16 v[204:207], v[20:23], v[68:71], v[204:207]\n"
"buffer_load_dwordx4 a[162:165], v192, s[28:31], s15 offen sc1\n"
"ds_read_b128 v[32:35], v192 offset:25600\n"
"v_mfma_f32_16x16x32_bf16 v[208:211], v[24:27], v[68:71], v[208:211]\n"
"ds_read_b128 v[36:39], v192 offset:26624\n"
"ds_read_b128 v[40:43], v192 offset:27648\n"
"v_mfma_f32_16x16x32_bf16 v[212:215], v[28:31], v[68:71], v[212:215]\n"
"ds_read_b128 v[68:71], v192 offset:24576\n"
"v_permlane32_swap_b32 v170, v171\n"
"ds_read_b128 v[44:47], v192 offset:28672\n"
"buffer_load_dwordx2 a[166:167], v193, s[28:31], s15 offen offset:1024 sc1\n"
"ds_read_b128 v[48:51], v192 offset:29696\n"
"v_max_f32 v170, v170, v171\n"
"v_mul_f32 v170, 0xbe553b95, v170\n"
"v_fmamk_f32 v120, v120, 0x3e553b95, v170\n"
"ds_read_b128 v[52:55], v192 offset:30720\n"
"v_fmamk_f32 v121, v121, 0x3e553b95, v170\n"
"v_fmamk_f32 v122, v122, 0x3e553b95, v170\n"
"v_fmamk_f32 v123, v123, 0x3e553b95, v170\n"
"v_exp_f32 v120, v120\n"
"v_exp_f32 v121, v121\n"
"v_exp_f32 v122, v122\n"
"v_exp_f32 v123, v123\n"
"v_cvt_pk_bf16_f32 v82, v120, v122\n"
"v_cvt_pk_bf16_f32 v83, v121, v123\n"
"v_fmamk_f32 v124, v124, 0x3e553b95, v170\n"
"v_fmamk_f32 v125, v125, 0x3e553b95, v170\n"
"v_fmamk_f32 v126, v126, 0x3e553b95, v170\n"
"v_fmamk_f32 v127, v127, 0x3e553b95, v170\n"
"v_exp_f32 v124, v124\n"
"v_exp_f32 v125, v125\n"
"v_exp_f32 v126, v126\n"
"v_exp_f32 v127, v127\n"
"v_cvt_pk_bf16_f32 v84, v124, v126\n"
"v_cvt_pk_bf16_f32 v85, v125, v127\n"
"v_fmamk_f32 v244, v244, 0x3e553b95, v170\n"
"v_fmamk_f32 v245, v245, 0x3e553b95, v170\n"
"v_smfmac_f32_16x16x64_bf16 v[252:255], v[82:85], v[112:119], v66\n"
"s_waitcnt lgkmcnt(3)\n"
"ds_read_b64_tr_b16 v[120:121], v182 offset:40640\n"
"v_smfmac_f32_16x16x64_bf16 v[98:101], v[82:85], v[136:143], v66\n"
"s_waitcnt lgkmcnt(1)\n"
"ds_read_b64_tr_b16 v[122:123], v183 offset:40640\n"
"v_smfmac_f32_16x16x64_bf16 v[106:109], v[82:85], v[128:135], v66\n"
"ds_read_b64_tr_b16 v[124:125], v182 offset:41408\n"
"v_mfma_f32_16x16x32_bf16 v[82:85], v[172:175], v[82:85], 0\n"
"ds_read_b64_tr_b16 v[126:127], v183 offset:41408\n"
"ds_write_b64 v197, v[240:241]\n"
"v_mfma_f32_16x16x32_bf16 v[216:219], a[216:219], v[68:71], 0\n"
"v_fmamk_f32 v246, v246, 0x3e553b95, v170\n"
"v_fmamk_f32 v247, v247, 0x3e553b95, v170\n"
"v_mfma_f32_16x16x32_bf16 v[220:223], a[220:223], v[68:71], 0\n"
"ds_read_b64_tr_b16 v[112:113], v182 offset:40648\n"
"ds_read_b64_tr_b16 v[114:115], v183 offset:40648\n"
"v_mfma_f32_16x16x32_bf16 v[224:227], a[224:227], v[68:71], 0\n"
"ds_read_b128 v[68:71], v192 offset:33792\n"
"v_fmamk_f32 v248, v248, 0x3e553b95, v170\n"
"v_mfma_f32_16x16x32_bf16 v[216:219], a[228:231], v[32:35], v[216:219]\n"
"ds_read_b64_tr_b16 v[116:117], v182 offset:41416\n"
"v_fmamk_f32 v249, v249, 0x3e553b95, v170\n"
"v_mfma_f32_16x16x32_bf16 v[220:223], a[232:235], v[32:35], v[220:223]\n"
"ds_read_b64_tr_b16 v[118:119], v183 offset:41416\n"
"v_fmamk_f32 v250, v250, 0x3e553b95, v170\n"
"v_mfma_f32_16x16x32_bf16 v[224:227], a[236:239], v[32:35], v[224:227]\n"
"ds_read_b128 v[32:35], v192 offset:34816\n"
"v_fmamk_f32 v251, v251, 0x3e553b95, v170\n"
"v_mfma_f32_16x16x32_bf16 v[216:219], a[240:243], v[36:39], v[216:219]\n"
"ds_read_b64_tr_b16 v[136:137], v182 offset:40584\n"
"v_exp_f32 v244, v244\n"
"v_mfma_f32_16x16x32_bf16 v[220:223], a[244:247], v[36:39], v[220:223]\n"
"ds_read_b64_tr_b16 v[138:139], v183 offset:40584\n"
"ds_read_b64_tr_b16 v[140:141], v182 offset:41352\n"
"v_mfma_f32_16x16x32_bf16 v[224:227], a[248:251], v[36:39], v[224:227]\n"
"s_waitcnt vmcnt(5)\n"
"v_exp_f32 v245, v245\n"
"v_mfma_f32_16x16x32_bf16 v[216:219], a[252:255], v[40:43], v[216:219]\n"
"ds_read_b128 v[36:39], v192 offset:35840\n"
"ds_read_b64_tr_b16 v[142:143], v183 offset:41352\n"
"v_mfma_f32_16x16x32_bf16 v[220:223], v[0:3], v[40:43], v[220:223]\n"
"v_exp_f32 v246, v246\n"
"v_mfma_f32_16x16x32_bf16 v[224:227], v[4:7], v[40:43], v[224:227]\n"
"v_exp_f32 v247, v247\n"
"v_mfma_f32_16x16x32_bf16 v[216:219], v[8:11], v[44:47], v[216:219]\n"
"v_cvt_pk_bf16_f32 v90, v244, v246\n"
"v_cvt_pk_bf16_f32 v91, v245, v247\n"
"v_mfma_f32_16x16x32_bf16 v[220:223], v[12:15], v[44:47], v[220:223]\n"
"v_exp_f32 v248, v248\n"
"v_mfma_f32_16x16x32_bf16 v[224:227], v[16:19], v[44:47], v[224:227]\n"
"v_exp_f32 v249, v249\n"
"v_mfma_f32_16x16x32_bf16 v[216:219], v[20:23], v[48:51], v[216:219]\n"
"v_exp_f32 v250, v250\n"
"v_mfma_f32_16x16x32_bf16 v[220:223], v[24:27], v[48:51], v[220:223]\n"
"v_exp_f32 v251, v251\n"
"v_mfma_f32_16x16x32_bf16 v[224:227], v[28:31], v[48:51], v[224:227]\n"
"v_cvt_pk_bf16_f32 v92, v248, v250\n"
"v_cvt_pk_bf16_f32 v93, v249, v251\n"
"v_mfma_f32_16x16x32_bf16 v[232:235], a[216:219], v[52:55], 0\n"
"s_waitcnt lgkmcnt(0)\n"
"ds_read_b128 v[72:75], v202 offset:49152\n"
"ds_read_b128 v[76:79], v202 offset:53248\n"
"v_smfmac_f32_16x16x64_bf16 v[252:255], v[90:93], v[120:127], v66\n"
"v_cvt_pk_bf16_f32 v204, v204, v205\n"
"v_smfmac_f32_16x16x64_bf16 v[98:101], v[90:93], v[112:119], v66\n"
"v_cvt_pk_bf16_f32 v205, v206, v207\n"
"v_smfmac_f32_16x16x64_bf16 v[106:109], v[90:93], v[136:143], v66\n"
"v_cvt_pk_bf16_f32 v206, v208, v209\n"
"v_mfma_f32_16x16x32_bf16 v[90:93], v[172:175], v[90:93], v[82:85]\n"
"v_cvt_pk_bf16_f32 v207, v210, v211\n"
"v_cvt_pk_bf16_f32 v212, v212, v213\n"
"v_mfma_f32_16x16x32_bf16 v[236:239], a[220:223], v[52:55], 0\n"
"ds_write_b128 v196, v[204:207] offset:12288\n"
"v_mfma_f32_16x16x32_bf16 v[240:243], a[224:227], v[52:55], 0\n"
"v_cvt_pk_bf16_f32 v252, v252, v253\n"
"v_cvt_pk_bf16_f32 v253, v254, v255\n"
"buffer_load_dwordx4 v[144:147], v192, s[28:31], s16 offen sc1\n"
"v_rcp_f32 v171, v90\n"
"v_cvt_pk_bf16_f32 v98, v98, v99\n"
"v_cvt_pk_bf16_f32 v171, v171, v171\n"
"v_and_b32 v64, v171, v176\n"
"v_and_b32 v65, v171, v177\n"
"v_cvt_pk_bf16_f32 v99, v100, v101\n"
"v_cvt_pk_bf16_f32 v106, v106, v107\n"
"v_mfma_f32_16x16x16_bf16 v[252:255], v[252:253], v[64:65], 0\n"
"s_waitcnt lgkmcnt(1)\n"
"v_cvt_pk_bf16_f32 v107, v108, v109\n"
"v_cvt_pk_bf16_f32 v213, v214, v215\n"
"v_mfma_f32_16x16x16_bf16 v[98:101], v[98:99], v[64:65], 0\n"
"buffer_load_dwordx2 v[154:155], v193, s[28:31], s16 offen offset:1024 sc1\n"
"ds_write_b64 v197, v[212:213] offset:12288\n"
"v_mfma_f32_16x16x16_bf16 v[106:109], v[106:107], v[64:65], 0\n"
"s_nop 0\n"
"v_cvt_pk_bf16_f32 v252, v252, v253\n"
"v_cvt_pk_bf16_f32 v253, v254, v255\n"
"v_min_u32 v252, -2, v252\n"
"s_nop 0\n"
"v_cvt_pk_bf16_f32 v254, v98, v99\n"
"v_cvt_pk_bf16_f32 v255, v100, v101\n"
"v_min_u32 v253, -2, v253\n"
"v_cvt_pk_bf16_f32 v106, v106, v107\n"
"v_cvt_pk_bf16_f32 v107, v108, v109\n"
"v_min_u32 v254, -2, v254\n"
"v_min_u32 v255, -2, v255\n"
"buffer_store_dwordx4 v[252:255], v192, s[28:31], s37 offen sc1\n"
"v_min_u32 v106, -2, v106\n"
"v_min_u32 v107, -2, v107\n"
"v_cvt_pk_bf16_f32 v216, v216, v217\n"
"v_cvt_pk_bf16_f32 v217, v218, v219\n"
"v_cvt_pk_bf16_f32 v218, v220, v221\n"
"v_cvt_pk_bf16_f32 v219, v222, v223\n"
"v_cvt_pk_bf16_f32 v224, v224, v225\n"
"buffer_store_dwordx2 v[106:107], v193, s[28:31], s37 offen offset:1024 sc1\n"
"s_waitcnt vmcnt(5)\n"
"ds_write_b128 v202, a[162:165] offset:36864\n"
"s_add_u32 s37, s37, 1572864\n"
"v_cvt_pk_bf16_f32 v225, v226, v227\n"
"v_max3_u32 v200, v228, v229, v230\n"
"v_max3_u32 v200, v200, v231, v152\n"
"v_max_u32 v200, v200, v153\n"
"buffer_store_dwordx4 v[72:75], v194, s[24:27], s36 offen sc1\n"
"s_waitcnt vmcnt(5)\n"
"ds_write_b64 v203, a[166:167] offset:36864\n"
"v_cmp_eq_u32 vcc, -1, v200\n"
"ds_read_b128 v[72:75], v192 offset:31744\n"
"ds_write_b128 v196, v[216:219] offset:43008\n"
"buffer_store_dwordx4 v[76:79], v195, s[24:27], s36 offen sc1\n"
"s_add_u32 s36, s36, s33\n"
"s_nop 0\n"
"s_cbranch_vccnz .Lp11%=\n"
".Lr11%=:\n"
"ds_write_b128 v202, v[228:231] offset:18432\n"
"ds_write_b64 v203, v[152:153] offset:18432\n"
"s_waitcnt lgkmcnt(3)\n"
"v_mfma_f32_16x16x32_bf16 v[232:235], a[228:231], v[72:75], v[232:235]\n"
"ds_read_b128 v[76:79], v192 offset:32768\n"
"ds_write_b64 v197, v[224:225] offset:43008\n"
"v_mfma_f32_16x16x32_bf16 v[236:239], a[232:235], v[72:75], v[236:239]\n"
"v_mfma_f32_16x16x32_bf16 v[240:243], a[236:239], v[72:75], v[240:243]\n"
"s_waitcnt lgkmcnt(1)\n"
"v_mfma_f32_16x16x32_bf16 v[232:235], a[240:243], v[76:79], v[232:235]\n"
"s_barrier\n"
"v_mfma_f32_16x16x32_bf16 v[236:239], a[244:247], v[76:79], v[236:239]\n"
"ds_read_b128 v[72:75], v192 offset:18432\n"
"ds_read_b128 v[40:43], v202\n"
"v_mfma_f32_16x16x32_bf16 v[240:243], a[248:251], v[76:79], v[240:243]\n"
"ds_read_b128 v[76:79], v192 offset:19456\n"
"v_mfma_f32_16x16x32_bf16 v[232:235], a[252:255], v[68:71], v[232:235]\n"
"ds_read_b128 v[44:47], v202 offset:4096\n"
"v_mfma_f32_16x16x32_bf16 v[236:239], v[0:3], v[68:71], v[236:239]\n"
"ds_read_b128 v[48:51], v202 offset:12288\n"
"v_mfma_f32_16x16x32_bf16 v[240:243], v[4:7], v[68:71], v[240:243]\n"
"ds_read_b128 v[68:71], v192 offset:20480\n"
"v_mfma_f32_16x16x32_bf16 v[232:235], v[8:11], v[32:35], v[232:235]\n"
"ds_read_b128 v[52:55], v202 offset:16384\n"
"v_mfma_f32_16x16x32_bf16 v[236:239], v[12:15], v[32:35], v[236:239]\n"
"v_mfma_f32_16x16x32_bf16 v[240:243], v[16:19], v[32:35], v[240:243]\n"
"ds_read_b128 v[32:35], v192 offset:21504\n"
"v_mfma_f32_16x16x32_bf16 v[232:235], v[20:23], v[36:39], v[232:235]\n"
"s_waitcnt lgkmcnt(7)\n"
"v_mfma_f32_16x16x32_bf16 v[236:239], v[24:27], v[36:39], v[236:239]\n"
"s_waitcnt lgkmcnt(6)\n"
"buffer_store_dwordx4 v[40:43], v194, s[24:27], s36 offen sc1\n"
"v_mfma_f32_16x16x32_bf16 v[240:243], v[28:31], v[36:39], v[240:243]\n"
"s_waitcnt lgkmcnt(4)\n"
"ds_read_b128 v[36:39], v192 offset:22528\n"
"ds_read_b128 v[40:43], v192 offset:23552\n"
"v_mfma_f32_16x16x32_bf16 v[204:207], a[216:219], v[72:75], 0\n"
"v_cvt_pk_bf16_f32 v232, v232, v233\n"
"v_cvt_pk_bf16_f32 v233, v234, v235\n"
"v_mfma_f32_16x16x32_bf16 v[208:211], a[220:223], v[72:75], 0\n"
"buffer_store_dwordx4 v[44:47], v195, s[24:27], s36 offen sc1\n"
"v_mfma_f32_16x16x32_bf16 v[212:215], a[224:227], v[72:75], 0\n"
"s_add_u32 s36, s36, s33\n"
"v_cvt_pk_bf16_f32 v234, v236, v237\n"
"v_cvt_pk_bf16_f32 v235, v238, v239\n"
"v_mfma_f32_16x16x32_bf16 v[204:207], a[228:231], v[76:79], v[204:207]\n"
"s_waitcnt lgkmcnt(3)\n"
"ds_write_b128 v196, v[232:235] offset:6144\n"
"v_mfma_f32_16x16x32_bf16 v[208:211], a[232:235], v[76:79], v[208:211]\n"
"buffer_store_dwordx4 v[48:51], v194, s[24:27], s36 offen sc1\n"
"v_mfma_f32_16x16x32_bf16 v[212:215], a[236:239], v[76:79], v[212:215]\n"
"v_cvt_pk_bf16_f32 v240, v240, v241\n"
"v_cvt_pk_bf16_f32 v241, v242, v243\n"
"v_mfma_f32_16x16x32_bf16 v[204:207], a[240:243], v[68:71], v[204:207]\n"
"s_waitcnt lgkmcnt(3)\n"
"ds_write_b64 v197, v[240:241] offset:6144\n"
"v_mfma_f32_16x16x32_bf16 v[208:211], a[244:247], v[68:71], v[208:211]\n"
"buffer_store_dwordx4 v[52:55], v195, s[24:27], s36 offen sc1\n"
"v_mfma_f32_16x16x32_bf16 v[212:215], a[248:251], v[68:71], v[212:215]\n"
"s_waitcnt vmcnt(9)\n"
"v_max3_u32 v200, v144, v145, v146\n"
"s_add_u32 s36, s36, s33\n"
"v_mfma_f32_16x16x32_bf16 v[204:207], a[252:255], v[32:35], v[204:207]\n"
"s_waitcnt vmcnt(8) lgkmcnt(3)\n"
"v_max3_u32 v200, v200, v147, v154\n"
"v_max_u32 v200, v200, v155\n"
"v_mfma_f32_16x16x32_bf16 v[208:211], v[0:3], v[32:35], v[208:211]\n"
"s_add_u32 s8, s8, s36\n"
"v_cmp_eq_u32 vcc, -1, v200\n"
"s_add_u32 s9, s9, s36\n"
"v_mfma_f32_16x16x32_bf16 v[212:215], v[4:7], v[32:35], v[212:215]\n"
"s_add_u32 s10, s10, s36\n"
"s_add_u32 s11, s11, s36\n"
"s_add_u32 s20, s20, s36\n"
"v_mfma_f32_16x16x32_bf16 v[204:207], v[8:11], v[36:39], v[204:207]\n"
"s_waitcnt lgkmcnt(0)\n"
"s_cbranch_vccnz .Lp12%=\n"
".Lr12%=:\n"
"v_mfma_f32_16x16x32_bf16 v[208:211], v[12:15], v[36:39], v[208:211]\n"
"s_barrier\n"
"v_mfma_f32_16x16x32_bf16 v[212:215], v[16:19], v[36:39], v[212:215]\n"
"s_add_u32 s21, s21, s36\n"
"ds_write_b128 v202, v[144:147] offset:49152\n"
"v_mfma_f32_16x16x32_bf16 v[204:207], v[20:23], v[40:43], v[204:207]\n"
"v_mfma_f32_16x16x32_bf16 v[208:211], v[24:27], v[40:43], v[208:211]\n"
"ds_write_b64 v203, v[154:155] offset:49152\n"
"v_mfma_f32_16x16x32_bf16 v[212:215], v[28:31], v[40:43], v[212:215]\n"
"s_nop 4\n"
"v_cvt_pk_bf16_f32 v204, v204, v205\n"
"ds_read_b128 v[76:79], v192 offset:36864\n"
"ds_read_b128 v[68:71], v192 offset:37888\n"
"ds_read_b128 v[32:35], v192 offset:38912\n"
"v_cvt_pk_bf16_f32 v205, v206, v207\n"
"v_cvt_pk_bf16_f32 v206, v208, v209\n"
"v_cvt_pk_bf16_f32 v207, v210, v211\n"
"ds_read_b128 v[36:39], v192 offset:39936\n"
"v_cvt_pk_bf16_f32 v212, v212, v213\n"
"ds_read_b128 v[40:43], v192 offset:40960\n"
"v_cvt_pk_bf16_f32 v213, v214, v215\n"
"ds_read_b128 v[44:47], v202 offset:43008\n"
"ds_write_b128 v196, v[204:207] offset:55296\n"
"buffer_load_dwordx4 v[56:59], v192, s[28:31], s17 offen sc1\n"
"ds_write_b64 v197, v[212:213] offset:55296\n"
"s_waitcnt lgkmcnt(7)\n"
"v_mfma_f32_16x16x32_bf16 v[216:219], a[216:219], v[76:79], 0\n"
"s_waitcnt lgkmcnt(6)\n"
"ds_read_b128 v[48:51], v202 offset:47104\n"
"v_mfma_f32_16x16x32_bf16 v[220:223], a[220:223], v[76:79], 0\n"
"ds_read_b128 v[52:55], v202 offset:6144\n"
"buffer_load_dwordx2 v[60:61], v193, s[28:31], s17 offen offset:1024 sc1\n"
"v_mfma_f32_16x16x32_bf16 v[224:227], a[224:227], v[76:79], 0\n"
"ds_read_b128 v[72:75], v202 offset:10240\n"
"v_mfma_f32_16x16x32_bf16 v[216:219], a[228:231], v[68:71], v[216:219]\n"
"s_waitcnt lgkmcnt(5)\n"
"v_mfma_f32_16x16x32_bf16 v[220:223], a[232:235], v[68:71], v[220:223]\n"
"v_mfma_f32_16x16x32_bf16 v[224:227], a[236:239], v[68:71], v[224:227]\n"
"buffer_store_dwordx4 v[44:47], v194, s[24:27], s8 offen sc1\n"
"v_mfma_f32_16x16x32_bf16 v[216:219], a[240:243], v[32:35], v[216:219]\n"
"s_waitcnt lgkmcnt(3)\n"
"ds_read_b128 v[44:47], v192 offset:41984\n"
"s_barrier\n"
"v_mfma_f32_16x16x32_bf16 v[220:223], a[244:247], v[32:35], v[220:223]\n"
"s_waitcnt lgkmcnt(2)\n"
"ds_read_b128 v[76:79], v192 offset:50176\n"
"ds_read_b128 v[68:71], v192 offset:51200\n"
"v_mfma_f32_16x16x32_bf16 v[224:227], a[248:251], v[32:35], v[224:227]\n"
"s_waitcnt lgkmcnt(3)\n"
"ds_read_b128 v[32:35], v192 offset:52224\n"
"buffer_store_dwordx4 v[48:51], v195, s[24:27], s8 offen sc1\n"
"v_mfma_f32_16x16x32_bf16 v[216:219], a[252:255], v[36:39], v[216:219]\n"
"s_nop 0\n"
"ds_read_b128 v[48:51], v202 offset:55296\n"
"v_mfma_f32_16x16x32_bf16 v[220:223], v[0:3], v[36:39], v[220:223]\n"
"v_mfma_f32_16x16x32_bf16 v[224:227], v[4:7], v[36:39], v[224:227]\n"
"ds_read_b128 v[36:39], v192 offset:53248\n"
"v_mfma_f32_16x16x32_bf16 v[216:219], v[8:11], v[40:43], v[216:219]\n"
"s_waitcnt lgkmcnt(5)\n"
"buffer_store_dwordx4 v[52:55], v194, s[24:27], s9 offen sc1\n"
"v_mfma_f32_16x16x32_bf16 v[220:223], v[12:15], v[40:43], v[220:223]\n"
"s_nop 0\n"
"ds_read_b128 v[52:55], v202 offset:59392\n"
"v_mfma_f32_16x16x32_bf16 v[224:227], v[16:19], v[40:43], v[224:227]\n"
"ds_read_b128 v[40:43], v192 offset:54272\n"
"v_mfma_f32_16x16x32_bf16 v[216:219], v[20:23], v[44:47], v[216:219]\n"
"v_mfma_f32_16x16x32_bf16 v[220:223], v[24:27], v[44:47], v[220:223]\n"
"s_waitcnt lgkmcnt(3)\n"
"buffer_store_dwordx4 v[72:75], v195, s[24:27], s9 offen sc1\n"
"v_mfma_f32_16x16x32_bf16 v[224:227], v[28:31], v[44:47], v[224:227]\n"
"s_nop 0\n"
"ds_read_b128 v[72:75], v192 offset:49152\n"
"s_nop 1\n"
"v_cvt_pk_bf16_f32 v216, v216, v217\n"
"v_cvt_pk_bf16_f32 v217, v218, v219\n"
"v_cvt_pk_bf16_f32 v218, v220, v221\n"
"v_cvt_pk_bf16_f32 v219, v222, v223\n"
"v_cvt_pk_bf16_f32 v224, v224, v225\n"
"v_cvt_pk_bf16_f32 v225, v226, v227\n"
"ds_write_b128 v196, v[216:219] offset:30720\n"
"buffer_store_dwordx4 v[48:51], v194, s[24:27], s10 offen sc1\n"
"ds_write_b64 v197, v[224:225] offset:30720\n"
"s_waitcnt lgkmcnt(4)\n"
"buffer_store_dwordx4 v[52:55], v195, s[24:27], s10 offen sc1\n"
"s_waitcnt lgkmcnt(2)\n"
"v_mfma_f32_16x16x32_bf16 v[232:235], a[216:219], v[72:75], 0\n"
"v_mfma_f32_16x16x32_bf16 v[236:239], a[220:223], v[72:75], 0\n"
"v_mfma_f32_16x16x32_bf16 v[240:243], a[224:227], v[72:75], 0\n"
"v_mfma_f32_16x16x32_bf16 v[232:235], a[228:231], v[76:79], v[232:235]\n"
"v_mfma_f32_16x16x32_bf16 v[236:239], a[232:235], v[76:79], v[236:239]\n"
"v_mfma_f32_16x16x32_bf16 v[240:243], a[236:239], v[76:79], v[240:243]\n"
"v_mfma_f32_16x16x32_bf16 v[232:235], a[240:243], v[68:71], v[232:235]\n"
"v_mfma_f32_16x16x32_bf16 v[240:243], a[248:251], v[68:71], v[240:243]\n"
"v_mfma_f32_16x16x32_bf16 v[236:239], a[244:247], v[68:71], v[236:239]\n"
"v_mfma_f32_16x16x32_bf16 v[232:235], a[252:255], v[32:35], v[232:235]\n"
"v_mfma_f32_16x16x32_bf16 v[240:243], v[4:7], v[32:35], v[240:243]\n"
"v_mfma_f32_16x16x32_bf16 v[236:239], v[0:3], v[32:35], v[236:239]\n"
"v_mfma_f32_16x16x32_bf16 v[232:235], v[8:11], v[36:39], v[232:235]\n"
"v_mfma_f32_16x16x32_bf16 v[240:243], v[16:19], v[36:39], v[240:243]\n"
"v_mfma_f32_16x16x32_bf16 v[236:239], v[12:15], v[36:39], v[236:239]\n"
"v_mfma_f32_16x16x32_bf16 v[232:235], v[20:23], v[40:43], v[232:235]\n"
"v_mfma_f32_16x16x32_bf16 v[240:243], v[28:31], v[40:43], v[240:243]\n"
"v_mfma_f32_16x16x32_bf16 v[236:239], v[24:27], v[40:43], v[236:239]\n"
"s_nop 5\n"
"v_cvt_pk_bf16_f32 v232, v232, v233\n"
"v_cvt_pk_bf16_f32 v233, v234, v235\n"
"v_cvt_pk_bf16_f32 v240, v240, v241\n"
"v_cvt_pk_bf16_f32 v234, v236, v237\n"
"v_cvt_pk_bf16_f32 v235, v238, v239\n"
"v_cvt_pk_bf16_f32 v241, v242, v243\n"
"ds_write_b128 v196, v[232:235] offset:24576\n"
"s_waitcnt vmcnt(7)\n"
"v_max3_u32 v200, v56, v57, v58\n"
"ds_write_b64 v197, v[240:241] offset:24576\n"
"s_waitcnt vmcnt(6)\n"
"v_max3_u32 v200, v200, v59, v60\n"
"v_max_u32 v200, v200, v61\n"
"v_cmp_eq_u32 vcc, -1, v200\n"
"s_nop 4\n"
"s_cbranch_vccnz .Lp13%=\n"
".Lr13%=:\n"
"ds_write_b128 v202, v[56:59]\n"
"ds_write_b64 v203, v[60:61]\n"
"s_waitcnt lgkmcnt(0)\n"
"s_barrier\n"
"ds_read_b128 v[76:79], v192\n"
"ds_read_b128 v[68:71], v192 offset:1024\n"
"ds_read_b128 v[32:35], v192 offset:2048\n"
"ds_read_b128 v[36:39], v192 offset:3072\n"
"ds_read_b128 v[40:43], v192 offset:4096\n"
"ds_read_b128 v[44:47], v202 offset:30720\n"
"ds_read_b128 v[48:51], v202 offset:34816\n"
"ds_read_b128 v[52:55], v202 offset:24576\n"
"ds_read_b128 v[72:75], v202 offset:28672\n"
"s_waitcnt lgkmcnt(8)\n"
"v_mfma_f32_16x16x32_bf16 v[204:207], a[216:219], v[76:79], 0\n"
"s_waitcnt lgkmcnt(7)\n"
"v_mfma_f32_16x16x32_bf16 v[212:215], a[224:227], v[76:79], 0\n"
"v_mfma_f32_16x16x32_bf16 v[208:211], a[220:223], v[76:79], 0\n"
"v_mfma_f32_16x16x32_bf16 v[204:207], a[228:231], v[68:71], v[204:207]\n"
"s_waitcnt lgkmcnt(6)\n"
"v_mfma_f32_16x16x32_bf16 v[212:215], a[236:239], v[68:71], v[212:215]\n"
"s_waitcnt lgkmcnt(3)\n"
"buffer_store_dwordx4 v[44:47], v194, s[24:27], s11 offen sc1\n"
"v_mfma_f32_16x16x32_bf16 v[208:211], a[232:235], v[68:71], v[208:211]\n"
"s_waitcnt lgkmcnt(1)\n"
"ds_read_b128 v[44:47], v192 offset:5120\n"
"v_mfma_f32_16x16x32_bf16 v[204:207], a[240:243], v[32:35], v[204:207]\n"
"s_waitcnt lgkmcnt(1)\n"
"v_mfma_f32_16x16x32_bf16 v[212:215], a[248:251], v[32:35], v[212:215]\n"
"buffer_store_dwordx4 v[48:51], v195, s[24:27], s11 offen sc1\n"
"v_mfma_f32_16x16x32_bf16 v[208:211], a[244:247], v[32:35], v[208:211]\n"
"v_mfma_f32_16x16x32_bf16 v[204:207], a[252:255], v[36:39], v[204:207]\n"
"v_mfma_f32_16x16x32_bf16 v[212:215], v[4:7], v[36:39], v[212:215]\n"
"v_mfma_f32_16x16x32_bf16 v[208:211], v[0:3], v[36:39], v[208:211]\n"
"buffer_store_dwordx4 v[52:55], v194, s[24:27], s20 offen sc1\n"
"v_mfma_f32_16x16x32_bf16 v[204:207], v[8:11], v[40:43], v[204:207]\n"
"s_waitcnt lgkmcnt(0)\n"
"v_mfma_f32_16x16x32_bf16 v[212:215], v[16:19], v[40:43], v[212:215]\n"
"v_mfma_f32_16x16x32_bf16 v[208:211], v[12:15], v[40:43], v[208:211]\n"
"buffer_store_dwordx4 v[72:75], v195, s[24:27], s20 offen sc1\n"
"v_mfma_f32_16x16x32_bf16 v[204:207], v[20:23], v[44:47], v[204:207]\n"
"v_mfma_f32_16x16x32_bf16 v[212:215], v[28:31], v[44:47], v[212:215]\n"
"v_mfma_f32_16x16x32_bf16 v[208:211], v[24:27], v[44:47], v[208:211]\n"
"s_nop 5\n"
"v_cvt_pk_bf16_f32 v204, v204, v205\n"
"v_cvt_pk_bf16_f32 v205, v206, v207\n"
"v_cvt_pk_bf16_f32 v212, v212, v213\n"
"v_cvt_pk_bf16_f32 v206, v208, v209\n"
"v_cvt_pk_bf16_f32 v207, v210, v211\n"
"v_cvt_pk_bf16_f32 v213, v214, v215\n"
"ds_write_b128 v196, v[204:207] offset:12288\n"
"ds_write_b64 v197, v[212:213] offset:12288\n"
"s_waitcnt lgkmcnt(0)\n"
"s_barrier\n"
"ds_read_b128 v[48:51], v202 offset:12288\n"
"ds_read_b128 v[52:55], v202 offset:16384\n"
"s_waitcnt lgkmcnt(1)\n"
"buffer_store_dwordx4 v[48:51], v194, s[24:27], s21 offen sc1\n"
"s_waitcnt lgkmcnt(0)\n"
"buffer_store_dwordx4 v[52:55], v195, s[24:27], s21 offen sc1\n"
"s_branch .Lend%=\n"
".Lp1%=:\n"
"s_sub_u32 s42, s42, 1\n"
"s_cbranch_scc1 .Lt1%=\n"
"s_sleep 1\n"
"buffer_load_dword v200, off, s[28:31], s39 sc1\n"
"s_waitcnt vmcnt(0)\n"
"v_add3_u32 v200, v200, s50, -2\n"
"v_cmp_lt_u32 vcc, 64, v200\n"
"s_nop 4\n"
"s_cbranch_vccnz .Lp1%=\n"
"s_branch .Lr1%=\n"
".Lt1%=:\n"
"s_mov_b32 s42, 0\n"
"s_branch .Lr1%=\n"
".Lp3%=:\n"
"s_sub_u32 s42, s42, 1\n"
"s_cbranch_scc1 .Lt3%=\n"
"s_sleep 1\n"
"buffer_load_dword v200, off, s[28:31], s39 sc1\n"
"s_waitcnt vmcnt(0)\n"
"v_add3_u32 v200, v200, s50, -4\n"
"v_cmp_lt_u32 vcc, 64, v200\n"
"s_nop 4\n"
"s_cbranch_vccnz .Lp3%=\n"
"s_branch .Lr3%=\n"
".Lt3%=:\n"
"s_mov_b32 s42, 0\n"
"s_branch .Lr3%=\n"
".Lp4%=:\n"
"s_sub_u32 s42, s42, 1\n"
"s_cbranch_scc1 .Lt4%=\n"
"s_sleep 1\n"
"buffer_load_dword v200, off, s[28:31], s39 sc1\n"
"s_waitcnt vmcnt(0)\n"
"v_add3_u32 v200, v200, s50, -5\n"
"v_cmp_lt_u32 vcc, 64, v200\n"
"s_nop 4\n"
"s_cbranch_vccnz .Lp4%=\n"
"s_branch .Lr4%=\n"
".Lt4%=:\n"
"s_mov_b32 s42, 0\n"
"s_branch .Lr4%=\n"
".Lp6%=:\n"
"s_sub_u32 s42, s42, 1\n"
"s_cbranch_scc1 .Lt6%=\n"
"s_sleep 1\n"
"buffer_load_dword v200, off, s[28:31], s39 sc1\n"
"s_waitcnt vmcnt(0)\n"
"v_add3_u32 v200, v200, s50, -7\n"
"v_cmp_lt_u32 vcc, 64, v200\n"
"s_nop 4\n"
"s_cbranch_vccnz .Lp6%=\n"
"s_branch .Lr6%=\n"
".Lt6%=:\n"
"s_mov_b32 s42, 0\n"
"s_branch .Lr6%=\n"
".Lp7%=:\n"
"s_sub_u32 s42, s42, 1\n"
"s_cbranch_scc1 .Lt7%=\n"
"s_sleep 1\n"
"buffer_load_dword v200, off, s[28:31], s39 sc1\n"
"s_waitcnt vmcnt(0)\n"
"v_add3_u32 v200, v200, s50, -8\n"
"v_cmp_lt_u32 vcc, 64, v200\n"
"s_nop 4\n"
"s_cbranch_vccnz .Lp7%=\n"
"s_branch .Lr7%=\n"
".Lt7%=:\n"
"s_mov_b32 s42, 0\n"
"s_branch .Lr7%=\n"
".Lp9%=:\n"
"s_sub_u32 s42, s42, 1\n"
"s_cbranch_scc1 .Lt9%=\n"
"s_sleep 1\n"
"buffer_load_dword v200, off, s[28:31], s39 sc1\n"
"s_waitcnt vmcnt(0)\n"
"v_add3_u32 v200, v200, s50, -10\n"
"v_cmp_lt_u32 vcc, 64, v200\n"
"s_nop 4\n"
"s_cbranch_vccnz .Lp9%=\n"
"s_branch .Lr9%=\n"
".Lt9%=:\n"
"s_mov_b32 s42, 0\n"
"s_branch .Lr9%=\n"
".Lp10%=:\n"
"s_sub_u32 s42, s42, 1\n"
"s_cbranch_scc1 .Lt10%=\n"
"s_sleep 1\n"
"buffer_load_dword v201, off, s[28:31], s39 sc1\n"
"s_waitcnt vmcnt(0)\n"
"v_add3_u32 v201, v201, s50, -11\n"
"v_cmp_lt_u32 vcc, 64, v201\n"
"s_nop 4\n"
"s_cbranch_vccnz .Lp10%=\n"
"s_branch .Lr10%=\n"
".Lt10%=:\n"
"s_mov_b32 s42, 0\n"
"s_branch .Lr10%=\n"
".Lp11%=:\n"
"s_sub_u32 s42, s42, 1\n"
"s_cbranch_scc1 .Lt11%=\n"
"buffer_load_dwordx4 v[228:231], v192, s[28:31], s14 offen sc1\n"
"buffer_load_dwordx2 v[152:153], v193, s[28:31], s14 offen offset:1024 sc1\n"
"s_waitcnt vmcnt(0)\n"
"v_max3_u32 v200, v228, v229, v230\n"
"v_max3_u32 v200, v200, v231, v152\n"
"v_max_u32 v200, v200, v153\n"
"v_cmp_eq_u32 vcc, -1, v200\n"
"s_nop 4\n"
"s_cbranch_vccnz .Lp11%=\n"
"s_branch .Lr11%=\n"
".Lt11%=:\n"
"s_mov_b32 s42, 0\n"
"s_branch .Lr11%=\n"
".Lp12%=:\n"
"s_sub_u32 s42, s42, 1\n"
"s_cbranch_scc1 .Lt12%=\n"
"buffer_load_dwordx4 v[144:147], v192, s[28:31], s16 offen sc1\n"
"buffer_load_dwordx2 v[154:155], v193, s[28:31], s16 offen offset:1024 sc1\n"
"s_waitcnt vmcnt(0)\n"
"v_max3_u32 v200, v144, v145, v146\n"
"v_max3_u32 v200, v200, v147, v154\n"
"v_max_u32 v200, v200, v155\n"
"v_cmp_eq_u32 vcc, -1, v200\n"
"s_nop 4\n"
"s_cbranch_vccnz .Lp12%=\n"
"s_branch .Lr12%=\n"
".Lt12%=:\n"
"s_mov_b32 s42, 0\n"
"s_branch .Lr12%=\n"
".Lp13%=:\n"
"s_sub_u32 s42, s42, 1\n"
"s_cbranch_scc1 .Lt13%=\n"
"buffer_load_dwordx4 v[56:59], v192, s[28:31], s17 offen sc1\n"
"buffer_load_dwordx2 v[60:61], v193, s[28:31], s17 offen offset:1024 sc1\n"
"s_waitcnt vmcnt(0)\n"
"v_max3_u32 v200, v56, v57, v58\n"
"v_max3_u32 v200, v200, v59, v60\n"
"v_max_u32 v200, v200, v61\n"
"v_cmp_eq_u32 vcc, -1, v200\n"
"s_nop 4\n"
"s_cbranch_vccnz .Lp13%=\n"
"s_branch .Lr13%=\n"
".Lt13%=:\n"
"s_mov_b32 s42, 0\n"
"s_branch .Lr13%=\n"
".Lend%=:\n"
      : "+{v31}"(tid), "+{s2}"(bid), "+{s[0:1]}"(ka)
      : "i"((__attribute__((address_space(3))) char*)smem)
      : "memory", "scc", "vcc", VCLOB, ACLOB, SCLOB);
}

extern "C" void kernel_launch(void* const* d_in, const int* in_sizes, int n_in,
                              void* d_out, int out_size, void* d_ws, size_t ws_size,
                              hipStream_t stream) {
  const uint16_t* x = (const uint16_t*)d_in[0];
  const uint16_t* wqkv = (const uint16_t*)d_in[1];
  const uint16_t* wo = (const uint16_t*)d_in[2];
  const uint16_t* rpb = (const uint16_t*)d_in[3];
  dinat_hs<<<256, 256, 0, stream>>>(x, wqkv, wo, rpb, (uint16_t*)d_out, (char*)d_ws + (2 << 20)
  );
}
```

```cpp
#include <hip/hip_runtime.h>
#include <hip/hip_bf16.h>
#include <stdint.h>

#define VCLOB "v0", "v1", "v2", "v3", "v4", "v5", "v6", "v7", "v8", "v9", "v10", "v11", "v12", "v13", "v14", "v15", "v16", "v17", "v18", "v19", "v20", "v21", "v22", "v23", "v24", "v25", "v26", "v27", "v28", "v29", "v30", "v32", "v33", "v34", "v35", "v36", "v37", "v38", "v39", "v40", "v41", "v42", "v43", "v44", "v45", "v46", "v47", "v48", "v49", "v50", "v51", "v52", "v53", "v54", "v55", "v56", "v57", "v58", "v59", "v60", "v61", "v62", "v63", "v64", "v65", "v66", "v67", "v68", "v69", "v70", "v71", "v72", "v73", "v74", "v75", "v76", "v77", "v78", "v79", "v80", "v81", "v82", "v83", "v84", "v85", "v86", "v87", "v88", "v89", "v90", "v91", "v92", "v93", "v94", "v95", "v96", "v97", "v98", "v99", "v100", "v101", "v102", "v103", "v104", "v105", "v106", "v107", "v108", "v109", "v110", "v111", "v112", "v113", "v114", "v115", "v116", "v117", "v118", "v119", "v120", "v121", "v122", "v123", "v124", "v125", "v126", "v127", "v128", "v129", "v130", "v131", "v132", "v133", "v134", "v135", "v136", "v137", "v138", "v139", "v140", "v141", "v142", "v143", "v144", "v145", "v146", "v147", "v148", "v149", "v150", "v151", "v152", "v153", "v154", "v155", "v156", "v157", "v158", "v159", "v160", "v161", "v162", "v163", "v164", "v165", "v166", "v167", "v168", "v169", "v170", "v171", "v172", "v173", "v174", "v175", "v176", "v177", "v178", "v179", "v180", "v181", "v182", "v183", "v184", "v185", "v186", "v187", "v188", "v189", "v190", "v191", "v192", "v193", "v194", "v195", "v196", "v197", "v198", "v199", "v200", "v201", "v202", "v203", "v204", "v205", "v206", "v207", "v208", "v209", "v210", "v211", "v212", "v213", "v214", "v215", "v216", "v217", "v218", "v219", "v220", "v221", "v222", "v223", "v224", "v225", "v226", "v227", "v228", "v229", "v230", "v231", "v232", "v233", "v234", "v235", "v236", "v237", "v238", "v239", "v240", "v241", "v242", "v243", "v244", "v245", "v246", "v247", "v248", "v249", "v250", "v251", "v252", "v253", "v254", "v255"
#define ACLOB "a0", "a1", "a2", "a3", "a4", "a5", "a6", "a7", "a8", "a9", "a10", "a11", "a12", "a13", "a14", "a15", "a16", "a17", "a18", "a19", "a20", "a21", "a22", "a23", "a24", "a25", "a26", "a27", "a28", "a29", "a30", "a31", "a32", "a33", "a34", "a35", "a36", "a37", "a38", "a39", "a40", "a41", "a42", "a43", "a44", "a45", "a46", "a47", "a48", "a49", "a50", "a51", "a52", "a53", "a54", "a55", "a56", "a57", "a58", "a59", "a60", "a61", "a62", "a63", "a64", "a65", "a66", "a67", "a68", "a69", "a70", "a71", "a72", "a73", "a74", "a75", "a76", "a77", "a78", "a79", "a80", "a81", "a82", "a83", "a84", "a85", "a86", "a87", "a88", "a89", "a90", "a91", "a92", "a93", "a94", "a95", "a96", "a97", "a98", "a99", "a100", "a101", "a102", "a103", "a104", "a105", "a106", "a107", "a108", "a109", "a110", "a111", "a112", "a113", "a114", "a115", "a116", "a117", "a118", "a119", "a120", "a121", "a122", "a123", "a124", "a125", "a126", "a127", "a128", "a129", "a130", "a131", "a132", "a133", "a134", "a135", "a136", "a137", "a138", "a139", "a140", "a141", "a142", "a143", "a144", "a145", "a146", "a147", "a148", "a149", "a150", "a151", "a152", "a153", "a154", "a155", "a156", "a157", "a158", "a159", "a160", "a161", "a162", "a163", "a164", "a165", "a166", "a167", "a168", "a169", "a170", "a171", "a172", "a173", "a174", "a175", "a176", "a177", "a178", "a179", "a180", "a181", "a182", "a183", "a184", "a185", "a186", "a187", "a188", "a189", "a190", "a191", "a192", "a193", "a194", "a195", "a196", "a197", "a198", "a199", "a200", "a201", "a202", "a203", "a204", "a205", "a206", "a207", "a208", "a209", "a210", "a211", "a212", "a213", "a214", "a215", "a216", "a217", "a218", "a219", "a220", "a221", "a222", "a223", "a224", "a225", "a226", "a227", "a228", "a229", "a230", "a231", "a232", "a233", "a234", "a235", "a236", "a237", "a238", "a239", "a240", "a241", "a242", "a243", "a244", "a245", "a246", "a247", "a248", "a249", "a250", "a251", "a252", "a253", "a254", "a255"
#define SCLOB "s3", "s4", "s5", "s6", "s7", "s8", "s9", "s10", "s11", "s12", "s13", "s14", "s15", "s16", "s17", "s18", "s19", "s20", "s21", "s22", "s23", "s24", "s25", "s26", "s27", "s28", "s29", "s30", "s31", "s32", "s33", "s34", "s35", "s36", "s37", "s38", "s39", "s40", "s41", "s42", "s43", "s44", "s45", "s46", "s47", "s48", "s49", "s50", "s51", "s52", "s53", "s54", "s55", "s56", "s57", "s58", "s59", "s60", "s61", "s62", "s63", "s64", "s65", "s66", "s67", "s68", "s69", "s70", "s71", "s72", "s73", "s74", "s75", "s76", "s77", "s78", "s79", "s80", "s81", "s82", "s83", "s84", "s85", "s86", "s87", "s88", "s89", "s90", "s91", "s92", "s93", "s94", "s95", "s96", "s97", "s98", "s99", "s100", "s101"
#define LDS_BYTES 163392

__global__ __launch_bounds__(256, 1) __attribute__((aligned(4096))) void dinat_hs(const uint16_t* __restrict__ x, const uint16_t* __restrict__ wqkv,
                                                   const uint16_t* __restrict__ wo, const uint16_t* __restrict__ rpb,
                                                   uint16_t* __restrict__ out, char* __restrict__ ws
                                                   ) {
  __shared__ __attribute__((aligned(16))) char smem[LDS_BYTES];
  unsigned tid = threadIdx.x, bid = blockIdx.x;
  auto ka = __builtin_amdgcn_kernarg_segment_ptr();
  asm volatile(
"s_load_dwordx2 s[8:9], s[0:1], 0\n"
"s_load_dwordx2 s[12:13], s[0:1], 8\n"
"s_load_dwordx2 s[16:17], s[0:1], 16\n"
"s_load_dwordx2 s[20:21], s[0:1], 24\n"
"s_load_dwordx2 s[24:25], s[0:1], 32\n"
"s_load_dwordx2 s[28:29], s[0:1], 40\n"
"s_getpc_b64 s[4:5]\n"
".Lpc%=:\n"
"s_bfe_u32 s53, s2, 0x20005\n"
"s_and_b32 s54, s2, 1\n"
"s_lshr_b32 s55, s2, 7\n"
"s_mul_i32 s59, s53, 27\n"
"v_lshrrev_b32 v113, 6, v31\n"
"s_max_u32 s59, s59, 1\n"
"s_mul_i32 s58, s55, 4816896\n"
"s_mul_i32 s71, s54, 14278656\n"
"s_nop 0\n"
"v_readfirstlane_b32 s51, v113\n"
"s_sub_u32 s59, s59, 1\n"
"s_add_u32 s58, s58, s71\n"
"v_and_b32 v112, 63, v31\n"
"s_mul_i32 s72, s59, 768\n"
"s_lshr_b32 s71, s2, 3\n"
"s_add_u32 s58, s58, s72\n"
"s_and_b32 s73, s2, 4\n"
"s_lshl2_add_u32 s71, s71, s51\n"
"v_min_u32 v121, 2, v112\n"
"s_xnor_b32 s56, s54, s55\n"
"s_mul_i32 s73, s73, 21504\n"
"s_and_b32 s74, s2, 2\n"
"s_mul_i32 s72, s71, 384\n"
"v_lshlrev_b32 v121, 7, v121\n"
"s_and_b32 s56, s56, 1\n"
"s_add_u32 s58, s58, s73\n"
"s_mul_i32 s74, s74, 192\n"
"v_add_u32 v121, s72, v121\n"
"s_bfe_u32 s52, s2, 0x20003\n"
"s_mul_i32 s57, s54, -344064\n"
"s_add_u32 s58, s58, s74\n"
"v_lshlrev_b32 v120, 4, v112\n"
"s_mul_i32 s85, s56, 3\n"
"v_min_u32 v121, .Lend%=-.Lpc%=-4, v121\n"
"s_add_u32 s57, s57, 172032\n"
"s_lshl_b32 s61, s51, 3\n"
"v_lshrrev_b32 v114, 3, v112\n"
"v_xor_b32 v115, v120, v112\n"
"s_sub_u32 s86, s85, 3\n"
"s_mov_b32 m0, 163136\n"
"s_mul_i32 s73, s52, 338\n"
"global_load_lds_dword v121, s[4:5]\n"
"s_and_b32 s74, s51, 1\n"
"s_lshr_b32 s84, s2, 5\n"
"v_and_b32 v115, 0x70, v115\n"
"v_add_u32 v114, s61, v114\n"
"s_mul_i32 s86, s86, s57\n"
"s_and_b32 s73, s73, -4\n"
"s_mul_i32 s74, s74, 88\n"
"v_mul_u32_u24 v123, 0x2aab, v112\n"
"v_add_u32 v124, 64, v112\n"
"s_mov_b32 s22, 1352\n"
"s_mov_b32 s23, 0x20000\n"
"s_add_u32 s84, s84, 0\n"
"v_xor_b32 v116, 64, v115\n"
"v_mul_u32_u24 v117, 0x300, v114\n"
"s_add_u32 s86, s86, s58\n"
"v_max_u32 v118, s85, v114\n"
"s_add_u32 s73, s73, s74\n"
"v_lshlrev_b32 v122, 2, v112\n"
"v_lshrrev_b32 v123, 16, v123\n"
"v_mul_u32_u24 v124, 0x2aab, v124\n"
"s_and_b32 s65, s51, 1\n"
"s_mul_i32 s84, s84, 2\n"
"v_add3_u32 v119, v117, s86, v116\n"
"v_lshlrev_b32 v126, 1, v123\n"
"v_lshrrev_b32 v124, 16, v124\n"
"s_lshr_b32 s66, s51, 1\n"
"s_mul_i32 s70, s52, 96\n"
"s_mul_i32 s85, s65, 384\n"
"s_lshr_b32 s75, 0x9249200, s84\n"
"v_and_b32 v126, 24, v126\n"
"v_lshlrev_b32 v127, 1, v124\n"
"s_add_u32 s85, s85, s70\n"
"s_mul_i32 s86, s66, 36864\n"
"s_movk_i32 s69, 1152\n"
"s_and_b32 s75, s75, 0x180\n"
"v_and_or_b32 v126, v123, 3, v126\n"
"v_and_b32 v127, 24, v127\n"
"v_add_u32 v125, 128, v112\n"
"s_add_u32 s85, s85, s86\n"
"v_mad_u32_u24 v126, v126, s69, v120\n"
"v_mul_u32_u24 v27, 96, v123\n"
"v_and_or_b32 v127, v124, 3, v127\n"
"v_lshrrev_b32 v128, 2, v124\n"
"v_mul_u32_u24 v125, 0x2aab, v125\n"
"s_add_u32 s85, s85, 384\n"
"s_mul_i32 s65, s65, 3072\n"
"s_mul_i32 s71, s66, 6752\n"
"s_mov_b32 s14, 221184\n"
"s_mov_b32 s15, 0x20000\n"
"v_sub_u32 v27, v126, v27\n"
"v_and_or_b32 v127, v128, 4, v127\n"
"v_lshrrev_b32 v125, 16, v125\n"
"s_add_u32 s71, s71, s65\n"
"v_mad_u32_u24 v127, v127, s69, v120\n"
"v_mul_u32_u24 v28, 96, v124\n"
"v_lshlrev_b32 v129, 1, v125\n"
"v_sub_u32 v28, v127, v28\n"
"v_and_b32 v129, 24, v129\n"
"v_add_u32 v28, 1024, v28\n"
"v_and_or_b32 v129, v125, 3, v129\n"
"v_mad_u32_u24 v129, v129, s69, v120\n"
"v_mul_u32_u24 v29, 96, v125\n"
"s_sub_u32 s64, 111, s59\n"
"v_sub_u32 v29, v129, v29\n"
"s_min_u32 s64, s64, 32\n"
"v_add_u32 v29, 6656, v29\n"
"s_mul_i32 s64, s64, 768\n"
"v_add_u32 v118, -3, v118\n"
"s_add_u32 s64, s64, s58\n"
"s_mul_i32 s50, s51, 6752\n"
"v_mul_i32_i24 v118, s57, v118\n"
"s_mov_b32 s10, 19267584\n"
"s_mov_b32 s11, 0x20000\n"
"s_add_u32 s50, s50, 27008\n"
"v_add3_u32 v118, v118, s64, v115\n"
"s_lshr_b32 s76, 0x12492480, s84\n"
"s_and_b32 s76, s76, 0x180\n"
"s_lshr_b32 s77, 0x4924900, s84\n"
"s_and_b32 s77, s77, 0x180\n"
"s_lshl_b32 s72, s56, 3\n"
"s_sub_u32 s72, s72, 2\n"
"s_mul_i32 s72, s72, s57\n"
"s_mul_i32 s34, s51, 12288\n"
"s_waitcnt lgkmcnt(0)\n"
"s_add_u32 m0, s74, 162768\n"
"s_add_u32 s72, s72, s58\n"
"buffer_load_dword v122, s[20:23], s73 offen lds\n"
"s_mul_i32 s73, s75, 576\n"
"s_add_u32 s73, s73, s85\n"
"s_add_u32 m0, s71, 0\n"
"s_add_u32 s34, s34, 81024\n"
"buffer_load_dwordx4 v27, s[12:15], s73 offen lds\n"
"v_add3_u32 v130, v117, s72, v115\n"
"s_add_u32 s74, s72, s57\n"
"v_add3_u32 v131, v117, s74, v116\n"
"s_and_b32 s60, s2, 7\n"
"s_lshr_b32 s84, s2, 5\n"
"s_lshl3_add_u32 s60, s60, s84\n"
"s_add_u32 m0, s71, 1024\n"
"s_mul_i32 s84, s76, 576\n"
"s_add_u32 s84, s84, s85\n"
"s_mul_i32 s86, s77, 576\n"
"s_add_u32 s86, s86, s85\n"
"s_mul_i32 s72, s54, 172032\n"
"buffer_load_dwordx4 v28, s[12:15], s73 offen lds\n"
"s_sub_u32 s36, s58, s72\n"
"s_add_u32 s32, s36, s75\n"
"v_add3_u32 v190, v117, s72, v115\n"
"v_min_u32 v96, 0xdc, v31\n"
"v_and_b32 v134, 3, v112\n"
"v_lshrrev_b32 v137, 1, v112\n"
"v_mul_u32_u24 v97, 0xf1, v96\n"
"s_mul_i32 s85, s54, -52\n"
"v_and_or_b32 v137, v137, 4, v134\n"
"s_add_u32 m0, s71, 2048\n"
"s_sub_u32 s74, 172032, s72\n"
"v_lshrrev_b32 v97, 12, v97\n"
"s_sub_u32 s85, s85, 8\n"
"v_bfe_u32 v136, v112, 2, 1\n"
"v_lshrrev_b32 v140, 1, v137\n"
"buffer_load_dwordx4 v29, s[12:15], s73 offen lds\n"
"v_add3_u32 v191, v117, s74, v116\n"
"v_lshlrev_b32 v30, 3, v96\n"
"v_mul_i32_i24 v97, s85, v97\n"
"v_lshrrev_b32 v132, 4, v112\n"
"v_lshl_add_u32 v85, v136, 2, v140\n"
"v_lshl_add_u32 v96, v96, 1, v97\n"
"v_xor_b32 v85, v85, v132\n"
"s_add_u32 m0, s50, 0\n"
"v_lshlrev_b32 v85, 4, v85\n"
"s_add_u32 s0, s36, s76\n"
"v_lshl_add_u32 v85, v137, 7, v85\n"
"v_mul_u32_u24 v25, 0xc00, v136\n"
"v_add_u32 v25, v25, v85\n"
"buffer_load_dwordx4 v118, s[8:11], s75 offen lds\n"
"v_add_u32 v188, s34, v25\n"
"v_xad_u32 v189, v25, 64, s34\n"
"s_add_u32 s1, s36, s77\n"
"s_mul_i32 s35, s51, 6144\n"
"s_lshl_b32 s33, s57, 1\n"
"s_add_u32 s35, s35, 130176\n"
"v_lshrrev_b32 v84, 2, v112\n"
"s_add_u32 m0, s50, 3072\n"
"v_mul_u32_u24 v84, 96, v84\n"
"v_lshl_add_u32 v23, v134, 3, v84\n"
"v_xor_b32 v69, 64, v25\n"
"v_add_u32 v30, 160896, v30\n"
"v_add_u32 v72, 54016, v23\n"
"buffer_load_dwordx4 v119, s[8:11], s75 offen lds\n"
"v_add_u32 v68, s50, v25\n"
"v_add_u32 v69, s50, v69\n"
"s_mul_i32 s3, s65, 12\n"
"s_add_u32 s70, s70, s3\n"
"s_mul_i32 s81, s77, 576\n"
"s_add_u32 s81, s81, s70\n"
"s_sub_u32 s3, s76, s75\n"
"s_add_u32 m0, s50, 1024\n"
"s_mul_i32 s3, s3, s66\n"
"s_add_u32 s3, s3, s75\n"
"s_mul_i32 s41, s56, -60768\n"
"s_mul_i32 s43, s66, 6752\n"
"s_mul_i32 s3, s3, 576\n"
"buffer_load_dwordx4 v118, s[8:11], s76 offen lds\n"
"s_add_u32 s41, s41, 60768\n"
"s_add_u32 s43, s43, s65\n"
"s_add_u32 s3, s3, s70\n"
"s_add_u32 s43, s43, s41\n"
"s_mov_b32 s42, 0x80000\n"
"v_add_u32 v142, s61, v137\n"
"v_bfe_u32 v86, v142, 1, 1\n"
"v_mul_u32_u24 v87, 43, v142\n"
"s_movk_i32 s68, 6752\n"
"s_add_u32 m0, s50, 4096\n"
"v_and_b32 v133, 48, v112\n"
"v_xor_b32 v86, v86, v132\n"
"v_lshrrev_b32 v87, 9, v87\n"
"s_movk_i32 s67, 192\n"
"v_lshlrev_b32 v86, 4, v86\n"
"buffer_load_dwordx4 v119, s[8:11], s76 offen lds\n"
"v_mul_u32_u24 v87, 80832, v87\n"
"v_mad_u32_u24 v21, v142, s68, v133\n"
"v_mad_u32_u24 v141, v142, s67, v86\n"
"v_sub_u32 v21, v21, v87\n"
"v_and_b32 v88, 1, v112\n"
"s_mul_i32 s48, s56, 13504\n"
"v_add_u32 v21, 6144, v21\n"
"s_add_u32 m0, s50, 2048\n"
"v_add_u32 v187, 74272, v141\n"
"v_add_u32 v198, 60768, v141\n"
"v_mad_u32_u24 v88, v88, s68, v133\n"
"s_mov_b32 s6, 0xf0f0f0f0\n"
"s_mov_b32 s7, 0xf0f0f0f0\n"
"buffer_load_dwordx4 v118, s[8:11], s77 offen lds\n"
"s_mov_b64 exec, s[6:7]\n"
"v_add_u32 v21, s48, v141\n"
"v_mov_b32 v187, v141\n"
"v_add_u32 v198, 60544, v88\n"
"s_mov_b64 exec, -1\n"
"s_lshl_b32 s85, s66, 10\n"
"v_add_u32 v192, 99456, v120\n"
"v_bfe_u32 v138, v112, 4, 1\n"
"v_lshrrev_b32 v139, 3, v112\n"
"s_add_u32 m0, s50, 5120\n"
"v_and_b32 v139, 4, v139\n"
"v_sub_u32 v94, v136, v138\n"
"s_add_u32 s62, s59, s61\n"
"s_mul_i32 s49, s56, 272\n"
"v_mul_i32_i24 v94, 0x88, v94\n"
"buffer_load_dwordx4 v119, s[8:11], s77 offen lds\n"
"v_sub_u32 v95, v137, v139\n"
"s_sub_u32 s63, 108, s62\n"
"v_lshl_add_u32 v94, v95, 3, v94\n"
"s_sub_u32 s62, 3, s62\n"
"v_add_u32 v150, 1, v139\n"
"v_add_u32 v151, 2, v139\n"
"v_add_u32 v152, 3, v139\n"
"s_add_u32 m0, s34, 6144\n"
"v_max_i32 v149, s62, v139\n"
"v_max_i32 v150, s62, v150\n"
"v_max_i32 v151, s62, v151\n"
"v_max_i32 v152, s62, v152\n"
"v_mov_b32 v153, 0x7f\n"
"buffer_load_dwordx4 v130, s[8:11], s75 offen lds\n"
"v_min_i32 v149, s63, v149\n"
"v_min_i32 v150, s63, v150\n"
"v_min_i32 v151, s63, v151\n"
"v_min_i32 v152, s63, v152\n"
"v_lshlrev_b32 v149, v149, v153\n"
"v_lshlrev_b32 v150, v150, v153\n"
"v_lshlrev_b32 v151, v151, v153\n"
"s_add_u32 m0, s34, 9216\n"
"v_lshlrev_b32 v152, v152, v153\n"
"s_sub_u32 s74, 3, s61\n"
"v_lshrrev_b32 v149, v137, v149\n"
"v_lshrrev_b32 v150, v137, v150\n"
"v_lshrrev_b32 v151, v137, v151\n"
"buffer_load_dwordx4 v131, s[8:11], s75 offen lds\n"
"v_lshrrev_b32 v152, v137, v152\n"
"s_max_i32 s74, s74, s62\n"
"s_mov_b32 s72, 0xffff\n"
"v_bfe_i32 v98, v149, 0, 1\n"
"v_bfe_i32 v99, v150, 0, 1\n"
"v_bfe_i32 v100, v151, 0, 1\n"
"v_bfe_i32 v101, v152, 0, 1\n"
"s_add_u32 m0, s34, 7168\n"
"v_bfe_i32 v102, v149, 8, 1\n"
"v_bfe_i32 v103, v150, 8, 1\n"
"v_bfe_i32 v56, v151, 8, 1\n"
"v_bfe_i32 v57, v152, 8, 1\n"
"v_max_i32 v58, s74, v137\n"
"buffer_load_dwordx4 v130, s[8:11], s76 offen lds\n"
"v_bfi_b32 v0, s72, v98, v99\n"
"v_bfi_b32 v1, s72, v100, v101\n"
"v_bfi_b32 v2, s72, v102, v103\n"
"v_bfi_b32 v3, s72, v56, v57\n"
"s_mul_i32 s74, s51, 1536\n"
"v_and_b32 v250, 15, v112\n"
"s_lshl2_add_u32 s72, s60, s51\n"
"v_mul_u32_u24 v250, 0x180, v250\n"
"v_and_b32 v251, 7, v112\n"
"s_mul_i32 s37, s72, 6144\n"
"v_add_u32 v250, 99456, v250\n"
"v_lshrrev_b32 v253, 5, v112\n"
"s_lshl_b32 s72, s51, 6\n"
"v_lshlrev_b32 v254, 3, v134\n"
"v_lshl_add_u32 v255, v23, 2, s72\n"
"v_sub_u32 v255, v255, v254\n"
"v_sub_u32 v255, v255, v254\n"
"v_add_u32 v236, 130176, v255\n"
"v_sub_u32 v84, v236, v254\n"
"s_lshl_b32 s73, s48, 2\n"
"v_mad_u32_u24 v186, v136, s68, v141\n"
"s_add_u32 s73, s73, 6752\n"
"v_add_u32 v22, s73, v186\n"
"v_add_u32 v70, s35, v25\n"
"v_xor_b32 v71, 64, v25\n"
"v_add_u32 v71, s35, v71\n"
"s_sub_u32 s73, 28, s61\n"
"s_min_i32 s73, s73, s63\n"
"v_min_i32 v58, s73, v58\n"
"v_lshlrev_b32 v58, v58, -1\n"
"v_lshrrev_b32 v58, v139, v58\n"
"v_not_b32 v58, v58\n"
"v_and_b32 v59, 5, v58\n"
"v_sub_u32 v61, v136, v138\n"
"v_and_b32 v60, 10, v58\n"
"v_lshlrev_b32 v59, 1, v59\n"
"v_ashrrev_i32 v149, 31, v61\n"
"v_add_u32 v150, -1, v61\n"
"v_mov_b32 v160, 0x40de\n"
"v_mov_b32 v161, 0\n"
"v_lshlrev_b32 v63, 4, v134\n"
"v_mov_b64 v[156:157], 0\n"
"s_mov_b32 s6, 0x00f0000f\n"
"s_mov_b32 s7, 0xf0000f00\n"
"v_lshl_or_b32 v59, v60, 4, v59\n"
"v_not_b32 v149, v149\n"
"v_ashrrev_i32 v150, 31, v150\n"
"v_or_b32 v62, s56, v138\n"
"s_mov_b64 exec, s[6:7]\n"
"v_lshlrev_b64 v[156:157], v63, v[160:161]\n"
"s_mov_b64 exec, -1\n"
"v_or_b32 v59, 0x44, v59\n"
"v_and_b32 v12, v149, v0\n"
"v_and_b32 v16, v150, v0\n"
"v_and_b32 v13, v149, v1\n"
"v_and_b32 v17, v150, v1\n"
"v_and_b32 v14, v149, v2\n"
"v_and_b32 v18, v150, v2\n"
"v_and_b32 v15, v149, v3\n"
"v_and_b32 v19, v150, v3\n"
"v_sub_u32 v62, v136, v62\n"
"v_mov_b32 v160, 0xffff\n"
"v_mov_b64 v[176:177], 0\n"
"v_lshl_or_b32 v66, v59, 8, v59\n"
"v_ashrrev_i32 v149, 31, v62\n"
"v_add_u32 v150, -1, v62\n"
"s_mov_b64 exec, 0\n"
"s_cmp_eq_u32 s51, 0\n"
"s_cbranch_scc1 .Lic0%=\n"
"s_cmp_eq_u32 s51, 1\n"
"s_cbranch_scc1 .Lic1%=\n"
"s_cmp_eq_u32 s51, 2\n"
"s_cbranch_scc1 .Lic2%=\n"
"s_branch .Lic3%=\n"
".Lib0%=:\n"
"s_mov_b64 exec, -1\n"
"s_mov_b64 exec, s[6:7]\n"
"v_lshlrev_b64 v[176:177], v63, v[160:161]\n"
"s_mov_b64 exec, -1\n"
"s_mov_b32 s30, 23724032\n"
"s_mov_b32 s31, 0x20000\n"
"v_and_b32 v146, 8, v112\n"
"v_lshl_add_u32 v146, v136, 1, v146\n"
"s_add_u32 m0, s34, 10240\n"
"v_add3_u32 v146, v146, s61, 1\n"
"buffer_load_dwordx4 v131, s[8:11], s76 offen lds\n"
"v_lshrrev_b32 v145, 5, v112\n"
"v_max_u32 v144, 3, v142\n"
"v_mul_u32_u24 v145, s68, v145\n"
"v_bfe_u32 v92, v146, 1, 1\n"
"v_add_u32 v144, -3, v144\n"
"v_add_u32 v145, -768, v145\n"
"v_xor_b32 v92, v92, v134\n"
"v_mov_b32 v143, s48\n"
"v_bfe_u32 v90, v144, 1, 1\n"
"v_lshl_add_u32 v92, v92, 4, v145\n"
"v_mad_u32_u24 v143, v136, s68, v143\n"
"s_add_u32 m0, s34, 8192\n"
"v_xor_b32 v90, v90, v132\n"
"buffer_load_dwordx4 v130, s[8:11], s77 offen lds\n"
"v_cmp_eq_u32 s[46:47], 35, v146\n"
"v_mad_u32_u24 v184, v146, s67, v92\n"
"v_lshl_add_u32 v90, v90, 4, v143\n"
"v_add_u32 v146, 1, v146\n"
"v_mad_u32_u24 v178, v144, s67, v90\n"
"v_bfe_u32 v93, v146, 1, 1\n"
"v_add_u32 v144, 5, v142\n"
"v_xor_b32 v93, v93, v134\n"
"v_min_u32 v144, 32, v144\n"
"v_mul_u32_u24 v147, 13504, v138\n"
"v_lshl_add_u32 v93, v93, 4, v145\n"
"s_add_u32 m0, s34, 11264\n"
"v_bfe_u32 v91, v144, 1, 1\n"
"buffer_load_dwordx4 v131, s[8:11], s77 offen lds\n"
"s_waitcnt vmcnt(12)\n"
"s_barrier\n"
"ds_read_b64_tr_b16 a[0:1], v23\n"
"ds_read_b64_tr_b16 a[2:3], v23 offset:1536\n"
"ds_read_b64_tr_b16 a[4:5], v23 offset:32\n"
"ds_read_b64_tr_b16 a[6:7], v23 offset:1568\n"
"ds_read_b64_tr_b16 a[8:9], v23 offset:64\n"
"ds_read_b64_tr_b16 a[10:11], v23 offset:1600\n"
"ds_read_b64_tr_b16 a[72:73], v23 offset:3072\n"
"s_add_u32 m0, s71, 13504\n"
"ds_read_b64_tr_b16 a[74:75], v23 offset:4608\n"
"buffer_load_dwordx4 v27, s[12:15], s84 offen lds\n"
"ds_read_b64_tr_b16 a[76:77], v23 offset:3104\n"
"v_add_u32 v147, s48, v147\n"
"ds_read_b64_tr_b16 a[78:79], v23 offset:4640\n"
"ds_read_b64_tr_b16 a[80:81], v23 offset:3136\n"
"ds_read_b64_tr_b16 a[82:83], v23 offset:4672\n"
"v_mul_i32_i24 v148, -67520, v138\n"
"ds_read_b64_tr_b16 a[12:13], v23 offset:6752\n"
"ds_read_b64_tr_b16 a[14:15], v23 offset:8288\n"
"ds_read_b64_tr_b16 a[16:17], v23 offset:6784\n"
"s_add_u32 m0, s71, 14528\n"
"ds_read_b64_tr_b16 a[18:19], v23 offset:8320\n"
"buffer_load_dwordx4 v28, s[12:15], s84 offen lds\n"
"s_waitcnt vmcnt(12)\n"
"ds_read_b128 a[144:147], v68\n"
"v_mad_u32_u24 v185, v146, s67, v93\n"
"v_bfe_u32 v135, v112, 2, 2\n"
"ds_read_b128 a[148:151], v69\n"
"v_xor_b32 v91, v91, v132\n"
"ds_read_b64_tr_b16 a[20:21], v23 offset:6816\n"
"ds_read_b64_tr_b16 a[22:23], v23 offset:8352\n"
"ds_read_b64_tr_b16 a[84:85], v23 offset:9824\n"
"s_add_u32 m0, s71, 15552\n"
"ds_read_b64_tr_b16 a[86:87], v23 offset:11360\n"
"buffer_load_dwordx4 v29, s[12:15], s84 offen lds\n"
"ds_read_b64_tr_b16 a[88:89], v23 offset:9856\n"
"s_add_u32 s84, s49, 161064\n"
"ds_read_b64_tr_b16 a[90:91], v23 offset:11392\n"
"v_add_u32 v20, s84, v94\n"
"ds_read_b64_tr_b16 a[92:93], v23 offset:9888\n"
"ds_read_b64_tr_b16 a[94:95], v23 offset:11424\n"
"s_lshl_b32 s84, s51, 10\n"
"v_add_u32 v202, s84, v192\n"
"s_mov_b32 s84, 0xc0000007\n"
"s_cmp_eq_u32 s53, 0\n"
"s_cselect_b32 s84, 0xe0000000, s84\n"
"s_cmp_eq_u32 s53, 3\n"
"s_cselect_b32 s84, 7, s84\n"
"v_add_u32 v148, 67520, v148\n"
"v_add_u32 v183, v185, v147\n"
"v_add_u32 v185, v185, v148\n"
"s_mov_b64 exec, s[46:47]\n"
"v_mad_u32_u24 v185, v138, s67, v185\n"
"s_mov_b64 exec, -1\n"
"v_lshl_add_u32 v91, v91, 4, v143\n"
"v_mad_u32_u24 v179, v144, s67, v91\n"
"v_cmp_eq_u32 s[44:45], 32, v144\n"
"v_add_u32 v182, v184, v147\n"
"v_not_b32 v149, v149\n"
"v_add_u32 v184, v184, v148\n"
"v_and_b32 v4, v149, v0\n"
"v_and_b32 v5, v149, v1\n"
"v_and_b32 v6, v149, v2\n"
"v_and_b32 v7, v149, v3\n"
"v_ashrrev_i32 v150, 31, v150\n"
"v_and_b32 v8, v150, v0\n"
"v_and_b32 v9, v150, v1\n"
"v_and_b32 v10, v150, v2\n"
"v_and_b32 v11, v150, v3\n"
"v_lshrrev_b32 v86, 2, v112\n"
"v_sub_u32 v87, 11, v112\n"
"v_and_b32 v87, 0x80000000, v87\n"
"v_and_b32 v89, 1, v114\n"
"v_add_u32 v89, 29, v89\n"
"v_mul_i32_i24 v89, s57, v89\n"
"v_add3_u32 v199, v89, s64, v116\n"
"v_mov_b32 v26, 0xf149f149\n"
"v_lshlrev_b32 v193, 3, v112\n"
"v_mov_b64 v[160:161], v[156:157]\n"
"v_add_u32 v180, 40512, v178\n"
"v_add_u32 v181, 40512, v179\n"
"v_mov_b32 v172, 0x3f803f80\n"
"v_mov_b32 v173, v172\n"
"v_mov_b64 v[164:165], v[156:157]\n"
"v_mov_b64 v[168:169], v[156:157]\n"
"v_mov_b64 v[174:175], v[172:173]\n"
"v_add_u32 v193, 99456, v193\n"
"s_waitcnt vmcnt(11) lgkmcnt(9)\n"
"v_mfma_f32_16x16x32_bf16 v[204:207], a[0:3], a[144:147], 0\n"
"s_waitcnt lgkmcnt(8)\n"
"ds_read_b128 a[152:155], v68 offset:1024\n"
"ds_read_b128 a[156:159], v69 offset:1024\n"
"v_mfma_f32_16x16x32_bf16 v[208:211], a[4:7], a[144:147], 0\n"
"s_add_u32 m0, s71, 60768\n"
"s_mul_i32 s78, s33, 8\n"
"buffer_load_dwordx4 v27, s[12:15], s86 offen lds\n"
"v_mfma_f32_16x16x32_bf16 v[212:215], a[8:11], a[144:147], 0\n"
"s_waitcnt vmcnt(10) lgkmcnt(8)\n"
"s_mul_i32 s73, s52, 1536\n"
"ds_read_b128 a[160:163], v68 offset:2048\n"
"v_mfma_f32_16x16x32_bf16 v[216:219], a[72:75], a[144:147], 0\n"
"s_waitcnt lgkmcnt(7)\n"
"ds_read_b128 a[164:167], v69 offset:2048\n"
"v_add_u32 v68, s41, v23\n"
"v_mfma_f32_16x16x32_bf16 v[220:223], a[76:79], a[144:147], 0\n"
"s_waitcnt lgkmcnt(6)\n"
"v_add_u32 v69, 154752, v23\n"
"s_mul_i32 s20, s33, 3\n"
"v_mfma_f32_16x16x32_bf16 v[224:227], a[80:83], a[144:147], 0\n"
"s_waitcnt lgkmcnt(4)\n"
"s_add_u32 m0, s71, 61792\n"
"s_mul_i32 s21, s33, 5\n"
"v_mfma_f32_16x16x32_bf16 v[204:207], a[12:15], a[148:151], v[204:207]\n"
"s_add_u32 s37, s37, s73\n"
"buffer_load_dwordx4 v28, s[12:15], s86 offen lds\n"
"s_mul_i32 s22, s33, 6\n"
"v_mfma_f32_16x16x32_bf16 v[208:211], a[16:19], a[148:151], v[208:211]\n"
"s_lshl_b32 s73, s51, 5\n"
"s_sub_u32 s73, 256, s73\n"
"v_add_u32 v235, s73, v84\n"
"v_mfma_f32_16x16x32_bf16 v[212:215], a[20:23], a[148:151], v[212:215]\n"
"s_add_u32 m0, s71, 62816\n"
"s_mul_i32 s71, s54, 312\n"
"v_mfma_f32_16x16x32_bf16 v[216:219], a[84:87], a[148:151], v[216:219]\n"
"s_mov_b32 s18, 73728\n"
"buffer_load_dwordx4 v29, s[12:15], s86 offen lds\n"
"v_mfma_f32_16x16x32_bf16 v[220:223], a[88:91], a[148:151], v[220:223]\n"
"s_and_b32 s86, s52, 1\n"
"s_lshl1_add_u32 s71, s86, s71\n"
"v_mfma_f32_16x16x32_bf16 v[224:227], a[92:95], a[148:151], v[224:227]\n"
"s_add_u32 s71, s71, 162760\n"
"v_add_u32 v24, s71, v96\n"
"ds_read_u16 v240, v24 offset:6\n"
"ds_read_u16 v241, v24 offset:4\n"
"ds_read_u16 v242, v24 offset:2\n"
"ds_read_u16 v243, v24\n"
"s_and_b32 s86, s51, 1\n"
"s_lshl3_add_u32 s85, s86, s85\n"
"s_add_u32 s85, s85, 4096\n"
"v_add_u32 v203, s85, v192\n"
"s_lshl_b32 s85, s51, 6\n"
"v_add_u32 v81, s85, v112\n"
"v_add_u32 v154, s85, v112\n"
"v_add_u32 v81, 0x100, v81\n"
"v_mul_u32_u24 v155, 0xaab, v154\n"
"v_mul_u32_u24 v82, 0xaab, v81\n"
"v_lshrrev_b32 v155, 16, v155\n"
"v_lshrrev_b32 v82, 16, v82\n"
"v_mul_i32_i24 v80, -24, v155\n"
"v_mul_i32_i24 v83, -24, v82\n"
"v_add_u32 v154, v154, v80\n"
"v_add_u32 v81, v81, v83\n"
"v_and_b32 v80, 7, v155\n"
"v_and_b32 v83, 7, v82\n"
"v_xor_b32 v154, v154, v80\n"
"v_xor_b32 v81, v81, v83\n"
"v_lshrrev_b32 v80, 1, v155\n"
"v_lshrrev_b32 v83, 1, v82\n"
"v_and_b32 v80, 4, v80\n"
"v_and_b32 v83, 4, v83\n"
"s_lshl_b32 s86, s52, 3\n"
"v_and_or_b32 v80, v155, 3, v80\n"
"v_and_or_b32 v83, v82, 3, v83\n"
"v_add_u32 v80, s86, v80\n"
"s_waitcnt lgkmcnt(2)\n"
"v_lshl_or_b32 v240, v241, 16, v240\n"
"v_add_u32 v83, s86, v83\n"
"s_lshl2_add_u32 s71, s60, s52\n"
"s_waitcnt lgkmcnt(0)\n"
"v_lshl_or_b32 v241, v243, 16, v242\n"
"ds_write_b64 v30, v[240:241]\n"
"v_lshrrev_b32 v244, v80, s84\n"
"v_lshrrev_b32 v245, 4, v155\n"
"v_bfe_u32 v246, v155, 2, 1\n"
"v_lshrrev_b32 v247, v83, s84\n"
"v_lshrrev_b32 v248, 4, v82\n"
"v_bfe_u32 v249, v82, 2, 1\n"
"s_mul_i32 s38, s71, 6144\n"
"v_or_b32 v244, v244, v245\n"
"v_xor_b32 v246, s54, v246\n"
"v_or_b32 v247, v247, v248\n"
"v_xor_b32 v249, s54, v249\n"
"s_lshl_b32 s71, s51, 2\n"
"v_lshlrev_b32 v244, 31, v244\n"
"v_mul_u32_u24 v246, 172032, v246\n"
"v_mul_u32_u24 v80, 0x300, v80\n"
"v_lshlrev_b32 v247, 31, v247\n"
"v_mul_u32_u24 v249, 172032, v249\n"
"v_mul_u32_u24 v83, 0x300, v83\n"
"s_add_u32 s38, s38, s74\n"
"v_add_u32 v252, s71, v132\n"
"s_add_u32 m0, s34, 0\n"
"v_add3_u32 v244, v244, v246, v80\n"
"buffer_load_dwordx4 v190, s[8:11], s32 offen lds\n"
"v_add3_u32 v247, v247, v249, v83\n"
"v_xor_b32 v252, v252, v251\n"
"s_lshl1_add_u32 s74, s51, 16\n"
"s_mov_b64 exec, 0\n"
"s_cmp_eq_u32 s51, 0\n"
"s_cbranch_scc1 .Lic4%=\n"
"s_cmp_eq_u32 s51, 1\n"
"s_cbranch_scc1 .Lic5%=\n"
"s_cmp_eq_u32 s51, 2\n"
"s_cbranch_scc1 .Lic6%=\n"
"s_branch .Lic7%=\n"
".Lib1%=:\n"
"s_mov_b64 exec, -1\n"
"s_add_u32 m0, s34, 3072\n"
"s_waitcnt vmcnt(11)\n"
"buffer_load_dwordx4 v191, s[8:11], s32 offen lds\n"
"s_add_u32 s32, s32, s33\n"
"ds_read_b128 a[168:171], v188 offset:6144\n"
"ds_read_b128 a[172:175], v189 offset:6144\n"
"v_lshl_add_u32 v194, v154, 4, v244\n"
"v_lshl_add_u32 v195, v81, 4, v247\n"
"v_lshl_add_u32 v196, v252, 4, v250\n"
"v_add_u32 v253, s74, v253\n"
"v_lshl_add_u32 v250, v138, 3, v250\n"
"v_xor_b32 v253, v253, v251\n"
"v_lshl_add_u32 v197, v253, 4, v250\n"
"s_add_u32 s38, s38, 31616\n"
"s_add_u32 m0, s34, 1024\n"
"s_add_u32 s39, s38, 22119552\n"
"buffer_load_dwordx4 v190, s[8:11], s0 offen lds\n"
"s_and_b32 s85, s52, 1\n"
"s_lshl2_add_u32 s85, s85, s61\n"
"v_add_u32 v85, s85, v134\n"
"v_mul_u32_u24 v85, 0x300, v85\n"
"v_lshl_add_u32 v85, v86, 7, v85\n"
"v_or_b32 v67, v87, v85\n"
"s_lshr_b32 s86, s52, 1\n"
"s_mul_i32 s86, s86, 172032\n"
"s_add_u32 s86, s86, s36\n"
"s_add_u32 s78, s78, s86\n"
"s_add_u32 s37, s37, 31616\n"
"s_mul_i32 s79, s51, 3072\n"
"s_mul_i32 s80, s66, 12288\n"
"s_add_u32 m0, s34, 4096\n"
"s_mov_b32 s26, 19267584\n"
"buffer_load_dwordx4 v191, s[8:11], s0 offen lds\n"
"s_add_u32 s0, s0, s33\n"
"s_waitcnt vmcnt(12)\n"
"ds_read_b128 a[176:179], v188 offset:7168\n"
"ds_read_b128 a[180:183], v189 offset:7168\n"
"s_waitcnt lgkmcnt(3)\n"
"v_mfma_f32_16x16x32_bf16 v[244:247], a[0:3], a[168:171], 0\n"
"s_waitcnt lgkmcnt(2)\n"
"s_mov_b32 s27, 0x20000\n"
"v_mfma_f32_16x16x32_bf16 v[248:251], a[4:7], a[168:171], 0\n"
"s_mov_b32 s19, 0x20000\n"
"s_add_u32 s40, s37, 22119552\n"
"v_mfma_f32_16x16x32_bf16 v[252:255], a[8:11], a[168:171], 0\n"
"s_add_u32 m0, s34, 2048\n"
"s_sub_u32 s16, s16, 99456\n"
"s_subb_u32 s17, s17, 0\n"
"v_mfma_f32_16x16x32_bf16 v[104:107], a[72:75], a[168:171], 0\n"
"s_mov_b32 s18, 173184\n"
"buffer_load_dwordx4 v190, s[8:11], s1 offen lds\n"
"v_mfma_f32_16x16x32_bf16 v[108:111], a[76:79], a[168:171], 0\n"
"s_add_u32 s80, s80, s79\n"
"v_mfma_f32_16x16x32_bf16 v[228:231], a[80:83], a[168:171], 0\n"
"v_mfma_f32_16x16x32_bf16 v[244:247], a[12:15], a[172:175], v[244:247]\n"
"s_waitcnt vmcnt(11)\n"
"ds_read_b128 a[184:187], v188 offset:8192\n"
"ds_read_b128 a[188:191], v189 offset:8192\n"
"v_mfma_f32_16x16x32_bf16 v[248:251], a[16:19], a[172:175], v[248:251]\n"
"s_add_u32 m0, s34, 5120\n"
"s_nop 0\n"
"buffer_load_dwordx4 v191, s[8:11], s1 offen lds\n"
"v_mfma_f32_16x16x32_bf16 v[252:255], a[20:23], a[172:175], v[252:255]\n"
"s_add_u32 s1, s1, s33\n"
"v_mfma_f32_16x16x32_bf16 v[104:107], a[84:87], a[172:175], v[104:107]\n"
"v_mfma_f32_16x16x32_bf16 v[108:111], a[88:91], a[172:175], v[108:111]\n"
"s_add_u32 m0, s35, 0\n"
"s_nop 0\n"
"buffer_load_dwordx4 v190, s[8:11], s32 offen lds\n"
"v_mfma_f32_16x16x32_bf16 v[228:231], a[92:95], a[172:175], v[228:231]\n"
"s_add_u32 m0, s35, 3072\n"
"s_nop 0\n"
"buffer_load_dwordx4 v191, s[8:11], s32 offen lds\n"
"s_add_u32 s32, s32, s33\n"
"s_add_u32 m0, s35, 1024\n"
"s_waitcnt vmcnt(11)\n"
"buffer_load_dwordx4 v190, s[8:11], s0 offen lds\n"
"s_barrier\n"
"ds_read_b64_tr_b16 a[24:25], v23 offset:13504\n"
"ds_read_b64_tr_b16 a[26:27], v23 offset:15040\n"
"ds_read_b64_tr_b16 a[28:29], v23 offset:13536\n"
"ds_read_b64_tr_b16 a[30:31], v23 offset:15072\n"
"ds_read_b64_tr_b16 a[32:33], v23 offset:13568\n"
"ds_read_b64_tr_b16 a[34:35], v23 offset:15104\n"
"ds_read_b64_tr_b16 a[96:97], v23 offset:16576\n"
"ds_read_b64_tr_b16 a[98:99], v23 offset:18112\n"
"ds_read_b64_tr_b16 a[100:101], v23 offset:16608\n"
"ds_read_b64_tr_b16 a[102:103], v23 offset:18144\n"
"ds_read_b64_tr_b16 a[104:105], v23 offset:16640\n"
"s_add_u32 m0, s35, 4096\n"
"ds_read_b64_tr_b16 a[106:107], v23 offset:18176\n"
"buffer_load_dwordx4 v191, s[8:11], s0 offen lds\n"
"s_add_u32 s0, s0, s33\n"
"ds_read_b64_tr_b16 a[36:37], v23 offset:20256\n"
"ds_read_b64_tr_b16 a[38:39], v23 offset:21792\n"
"ds_read_b64_tr_b16 a[40:41], v23 offset:20288\n"
"ds_read_b64_tr_b16 a[42:43], v23 offset:21824\n"
"ds_read_b64_tr_b16 a[44:45], v23 offset:20320\n"
"ds_read_b64_tr_b16 a[46:47], v23 offset:21856\n"
"ds_read_b64_tr_b16 a[108:109], v23 offset:23328\n"
"s_add_u32 m0, s35, 2048\n"
"ds_read_b64_tr_b16 a[110:111], v23 offset:24864\n"
"buffer_load_dwordx4 v190, s[8:11], s1 offen lds\n"
"ds_read_b64_tr_b16 a[112:113], v23 offset:23360\n"
"s_waitcnt lgkmcnt(15)\n"
"v_mfma_f32_16x16x32_bf16 v[204:207], a[24:27], a[152:155], v[204:207]\n"
"ds_read_b64_tr_b16 a[114:115], v23 offset:24896\n"
"ds_read_b64_tr_b16 a[116:117], v23 offset:23392\n"
"v_mfma_f32_16x16x32_bf16 v[208:211], a[28:31], a[152:155], v[208:211]\n"
"s_waitcnt lgkmcnt(15)\n"
"ds_read_b64_tr_b16 a[118:119], v23 offset:24928\n"
"v_mfma_f32_16x16x32_bf16 v[212:215], a[32:35], a[152:155], v[212:215]\n"
"s_waitcnt vmcnt(11) lgkmcnt(14)\n"
"v_mfma_f32_16x16x32_bf16 v[216:219], a[96:99], a[152:155], v[216:219]\n"
"s_waitcnt lgkmcnt(12)\n"
"s_add_u32 m0, s35, 5120\n"
"v_mfma_f32_16x16x32_bf16 v[220:223], a[100:103], a[152:155], v[220:223]\n"
"s_waitcnt lgkmcnt(10)\n"
"buffer_load_dwordx4 v191, s[8:11], s1 offen lds\n"
"v_mfma_f32_16x16x32_bf16 v[224:227], a[104:107], a[152:155], v[224:227]\n"
"s_waitcnt lgkmcnt(8)\n"
"s_add_u32 s1, s1, s33\n"
"v_mfma_f32_16x16x32_bf16 v[204:207], a[36:39], a[156:159], v[204:207]\n"
"s_waitcnt lgkmcnt(6)\n"
"s_barrier\n"
"v_mfma_f32_16x16x32_bf16 v[208:211], a[40:43], a[156:159], v[208:211]\n"
"s_waitcnt lgkmcnt(4)\n"
"ds_read_b64_tr_b16 a[48:49], v23 offset:60768\n"
"ds_read_b64_tr_b16 a[50:51], v23 offset:62304\n"
"v_mfma_f32_16x16x32_bf16 v[212:215], a[44:47], a[156:159], v[212:215]\n"
"s_waitcnt lgkmcnt(4)\n"
"ds_read_b64_tr_b16 a[52:53], v23 offset:60800\n"
"ds_read_b64_tr_b16 a[54:55], v23 offset:62336\n"
"v_mfma_f32_16x16x32_bf16 v[216:219], a[108:111], a[156:159], v[216:219]\n"
"s_waitcnt lgkmcnt(4)\n"
"ds_read_b64_tr_b16 a[56:57], v23 offset:60832\n"
"ds_read_b64_tr_b16 a[58:59], v23 offset:62368\n"
"v_mfma_f32_16x16x32_bf16 v[220:223], a[112:115], a[156:159], v[220:223]\n"
"ds_read_b64_tr_b16 a[120:121], v72 offset:9824\n"
"ds_read_b64_tr_b16 a[122:123], v72 offset:11360\n"
"v_mfma_f32_16x16x32_bf16 v[224:227], a[116:119], a[156:159], v[224:227]\n"
"ds_read_b64_tr_b16 a[124:125], v72 offset:9856\n"
"ds_read_b64_tr_b16 a[126:127], v72 offset:11392\n"
"v_mfma_f32_16x16x32_bf16 v[244:247], a[24:27], a[176:179], v[244:247]\n"
"ds_read_b64_tr_b16 a[128:129], v72 offset:9888\n"
"ds_read_b64_tr_b16 a[130:131], v72 offset:11424\n"
"v_mfma_f32_16x16x32_bf16 v[248:251], a[28:31], a[176:179], v[248:251]\n"
"ds_read_b64_tr_b16 a[60:61], v72 offset:13504\n"
"ds_read_b64_tr_b16 a[62:63], v72 offset:15040\n"
"v_mfma_f32_16x16x32_bf16 v[252:255], a[32:35], a[176:179], v[252:255]\n"
"ds_read_b64_tr_b16 a[64:65], v72 offset:13536\n"
"ds_read_b64_tr_b16 a[66:67], v72 offset:15072\n"
"v_mfma_f32_16x16x32_bf16 v[104:107], a[96:99], a[176:179], v[104:107]\n"
"s_waitcnt lgkmcnt(14)\n"
"ds_read_b64_tr_b16 a[68:69], v72 offset:13568\n"
"ds_read_b64_tr_b16 a[70:71], v72 offset:15104\n"
"v_mfma_f32_16x16x32_bf16 v[204:207], a[48:51], a[160:163], v[204:207]\n"
"s_waitcnt lgkmcnt(14)\n"
"ds_read_b64_tr_b16 a[132:133], v72 offset:16576\n"
"ds_read_b64_tr_b16 a[134:135], v72 offset:18112\n"
"v_mfma_f32_16x16x32_bf16 v[208:211], a[52:55], a[160:163], v[208:211]\n"
"s_waitcnt lgkmcnt(14)\n"
"ds_read_b64_tr_b16 a[136:137], v72 offset:16608\n"
"ds_read_b64_tr_b16 a[138:139], v72 offset:18144\n"
"v_mfma_f32_16x16x32_bf16 v[212:215], a[56:59], a[160:163], v[212:215]\n"
"s_waitcnt lgkmcnt(14)\n"
"ds_read_b64_tr_b16 a[140:141], v72 offset:16640\n"
"ds_read_b64_tr_b16 a[142:143], v72 offset:18176\n"
"v_mfma_f32_16x16x32_bf16 v[216:219], a[120:123], a[160:163], v[216:219]\n"
"s_waitcnt lgkmcnt(14)\n"
"v_mfma_f32_16x16x32_bf16 v[220:223], a[124:127], a[160:163], v[220:223]\n"
"s_waitcnt lgkmcnt(12)\n"
"v_mfma_f32_16x16x32_bf16 v[224:227], a[128:131], a[160:163], v[224:227]\n"
"s_waitcnt lgkmcnt(10)\n"
"v_mfma_f32_16x16x32_bf16 v[204:207], a[60:63], a[164:167], v[204:207]\n"
"s_waitcnt lgkmcnt(8)\n"
"v_mfma_f32_16x16x32_bf16 v[208:211], a[64:67], a[164:167], v[208:211]\n"
"s_waitcnt lgkmcnt(6)\n"
"v_mfma_f32_16x16x32_bf16 v[212:215], a[68:71], a[164:167], v[212:215]\n"
"s_waitcnt lgkmcnt(4)\n"
"v_mfma_f32_16x16x32_bf16 v[216:219], a[132:135], a[164:167], v[216:219]\n"
"s_waitcnt lgkmcnt(2)\n"
"s_nop 0\n"
"v_cvt_pk_bf16_f32 v204, v204, v205\n"
"v_mfma_f32_16x16x32_bf16 v[220:223], a[136:139], a[164:167], v[220:223]\n"
"s_waitcnt lgkmcnt(0)\n"
"v_cvt_pk_bf16_f32 v205, v206, v207\n"
"v_cvt_pk_bf16_f32 v206, v208, v209\n"
"v_mfma_f32_16x16x32_bf16 v[224:227], a[140:143], a[164:167], v[224:227]\n"
"s_barrier\n"
"v_mfma_f32_16x16x32_bf16 v[108:111], a[100:103], a[176:179], v[108:111]\n"
"v_cvt_pk_bf16_f32 v207, v210, v211\n"
"v_cvt_pk_bf16_f32 v212, v212, v213\n"
"v_mfma_f32_16x16x32_bf16 v[228:231], a[104:107], a[176:179], v[228:231]\n"
"ds_write_b128 v21, v[204:207]\n"
"v_mfma_f32_16x16x32_bf16 v[244:247], a[36:39], a[180:183], v[244:247]\n"
"v_cvt_pk_bf16_f32 v213, v214, v215\n"
"v_cvt_pk_bf16_f32 v216, v216, v217\n"
"v_mfma_f32_16x16x32_bf16 v[248:251], a[40:43], a[180:183], v[248:251]\n"
"s_add_u32 m0, s34, 6144\n"
"v_cvt_pk_bf16_f32 v217, v218, v219\n"
"v_cvt_pk_bf16_f32 v218, v220, v221\n"
"v_mfma_f32_16x16x32_bf16 v[252:255], a[44:47], a[180:183], v[252:255]\n"
"buffer_load_dwordx4 v190, s[8:11], s32 offen lds\n"
"v_cvt_pk_bf16_f32 v219, v222, v223\n"
"v_mfma_f32_16x16x32_bf16 v[104:107], a[108:111], a[180:183], v[104:107]\n"
"ds_write_b128 v21, v[216:219] offset:128\n"
"v_mfma_f32_16x16x32_bf16 v[108:111], a[112:115], a[180:183], v[108:111]\n"
"s_waitcnt vmcnt(11)\n"
"v_cvt_pk_bf16_f32 v214, v224, v225\n"
"v_cvt_pk_bf16_f32 v215, v226, v227\n"
"v_mfma_f32_16x16x32_bf16 v[228:231], a[116:119], a[180:183], v[228:231]\n"
"s_add_u32 m0, s34, 9216\n"
"s_nop 0\n"
"buffer_load_dwordx4 v191, s[8:11], s32 offen lds\n"
"v_mfma_f32_16x16x32_bf16 v[244:247], a[48:51], a[184:187], v[244:247]\n"
"s_add_u32 s32, s32, s33\n"
"ds_read_b128 v[32:35], v188\n"
"ds_read_b128 v[36:39], v189\n"
"v_mfma_f32_16x16x32_bf16 v[248:251], a[52:55], a[184:187], v[248:251]\n"
"ds_write_b128 v21, v[212:215] offset:64\n"
"v_mfma_f32_16x16x32_bf16 v[252:255], a[56:59], a[184:187], v[252:255]\n"
"s_add_u32 m0, s34, 7168\n"
"s_nop 0\n"
"buffer_load_dwordx4 v190, s[8:11], s0 offen lds\n"
"v_mfma_f32_16x16x32_bf16 v[104:107], a[120:123], a[184:187], v[104:107]\n"
"ds_read_b64 v[80:81], v20 offset:272\n"
"v_mfma_f32_16x16x32_bf16 v[108:111], a[124:127], a[184:187], v[108:111]\n"
"ds_read_b64 v[86:87], v20 offset:336\n"
"ds_read_b64 v[88:89], v20 offset:544\n"
"v_mfma_f32_16x16x32_bf16 v[228:231], a[128:131], a[184:187], v[228:231]\n"
"s_waitcnt vmcnt(11)\n"
"ds_read_b64 v[94:95], v20 offset:608\n"
"ds_read_b128 v[40:43], v188 offset:1024\n"
"v_mfma_f32_16x16x32_bf16 v[244:247], a[60:63], a[188:191], v[244:247]\n"
"s_add_u32 m0, s34, 10240\n"
"ds_read_b128 v[44:47], v189 offset:1024\n"
"buffer_load_dwordx4 v191, s[8:11], s0 offen lds\n"
"v_mfma_f32_16x16x32_bf16 v[248:251], a[64:67], a[188:191], v[248:251]\n"
"s_waitcnt lgkmcnt(8)\n"
"ds_read_b64 v[96:97], v20 offset:816\n"
"ds_read_b64 v[102:103], v20 offset:880\n"
"v_mfma_f32_16x16x32_bf16 v[252:255], a[68:71], a[188:191], v[252:255]\n"
"s_add_u32 s0, s0, s33\n"
"v_cvt_pk_bf16_f32 v244, v244, v245\n"
"v_cvt_pk_bf16_f32 v245, v246, v247\n"
"v_mfma_f32_16x16x32_bf16 v[104:107], a[132:135], a[188:191], v[104:107]\n"
"s_add_u32 m0, s34, 8192\n"
"v_cvt_pk_bf16_f32 v246, v248, v249\n"
"v_cvt_pk_bf16_f32 v247, v250, v251\n"
"v_mfma_f32_16x16x32_bf16 v[108:111], a[136:139], a[188:191], v[108:111]\n"
"s_waitcnt lgkmcnt(7)\n"
"buffer_load_dwordx4 v190, s[8:11], s1 offen lds\n"
"ds_write_b128 v22, v[244:247]\n"
"v_mfma_f32_16x16x32_bf16 v[228:231], a[140:143], a[188:191], v[228:231]\n"
"s_waitcnt lgkmcnt(5)\n"
"v_cvt_pk_bf16_f32 v252, v252, v253\n"
"v_cvt_pk_bf16_f32 v253, v254, v255\n"
"v_mfma_f32_16x16x32_bf16 v[204:207], a[0:3], v[32:35], 0\n"
"s_waitcnt vmcnt(11)\n"
"v_cvt_pk_bf16_f32 v104, v104, v105\n"
"v_cvt_pk_bf16_f32 v105, v106, v107\n"
"v_mfma_f32_16x16x32_bf16 v[208:211], a[4:7], v[32:35], 0\n"
"s_add_u32 m0, s34, 11264\n"
"ds_read_b128 v[48:51], v188 offset:2048\n"
"buffer_load_dwordx4 v191, s[8:11], s1 offen lds\n"
"v_mfma_f32_16x16x32_bf16 v[212:215], a[8:11], v[32:35], 0\n"
"s_waitcnt lgkmcnt(2)\n"
"ds_read_b128 v[52:55], v189 offset:2048\n"
"v_cvt_pk_bf16_f32 v106, v108, v109\n"
"v_mfma_f32_16x16x32_bf16 v[216:219], a[72:75], v[32:35], 0\n"
"s_add_u32 s1, s1, s33\n"
"v_cvt_pk_bf16_f32 v107, v110, v111\n"
"v_cvt_pk_bf16_f32 v254, v228, v229\n"
"v_mfma_f32_16x16x32_bf16 v[220:223], a[76:79], v[32:35], 0\n"
"ds_write_b128 v22, v[104:107] offset:128\n"
"v_mfma_f32_16x16x32_bf16 v[224:227], a[80:83], v[32:35], 0\n"
"s_add_u32 m0, s65, 154752\n"
"v_cvt_pk_bf16_f32 v255, v230, v231\n"
"v_bfi_b32 v80, v4, v80, v26\n"
"v_mfma_f32_16x16x32_bf16 v[204:207], a[12:15], v[36:39], v[204:207]\n"
"buffer_load_dwordx4 v27, s[12:15], s81 offen lds\n"
"ds_write_b128 v22, v[252:255] offset:64\n"
"v_mfma_f32_16x16x32_bf16 v[208:211], a[16:19], v[36:39], v[208:211]\n"
"v_bfi_b32 v81, v5, v81, v26\n"
"v_bfi_b32 v86, v6, v86, v26\n"
"v_mfma_f32_16x16x32_bf16 v[212:215], a[20:23], v[36:39], v[212:215]\n"
"s_waitcnt vmcnt(11)\n"
"v_bfi_b32 v87, v7, v87, v26\n"
"v_bfi_b32 v88, v0, v88, v26\n"
"v_mfma_f32_16x16x32_bf16 v[216:219], a[84:87], v[36:39], v[216:219]\n"
"s_add_u32 m0, s65, 155776\n"
"ds_read_b128 a[216:219], v70\n"
"buffer_load_dwordx4 v28, s[12:15], s81 offen lds\n"
"v_mfma_f32_16x16x32_bf16 v[220:223], a[88:91], v[36:39], v[220:223]\n"
"ds_read_b128 a[220:223], v71\n"
"v_bfi_b32 v89, v1, v89, v26\n"
"v_mfma_f32_16x16x32_bf16 v[224:227], a[92:95], v[36:39], v[224:227]\n"
"v_bfi_b32 v94, v2, v94, v26\n"
"v_bfi_b32 v95, v3, v95, v26\n"
"v_mfma_f32_16x16x32_bf16 v[204:207], a[24:27], v[40:43], v[204:207]\n"
"s_add_u32 m0, s65, 156800\n"
"v_bfi_b32 v96, v0, v96, v26\n"
"v_bfi_b32 v97, v1, v97, v26\n"
"v_mfma_f32_16x16x32_bf16 v[208:211], a[28:31], v[40:43], v[208:211]\n"
"buffer_load_dwordx4 v29, s[12:15], s81 offen lds\n"
"v_bfi_b32 v102, v2, v102, v26\n"
"v_mfma_f32_16x16x32_bf16 v[212:215], a[32:35], v[40:43], v[212:215]\n"
"v_bfi_b32 v103, v3, v103, v26\n"
"v_mfma_f32_16x16x32_bf16 v[216:219], a[96:99], v[40:43], v[216:219]\n"
"v_mfma_f32_16x16x32_bf16 v[220:223], a[100:103], v[40:43], v[220:223]\n"
"v_mfma_f32_16x16x32_bf16 v[224:227], a[104:107], v[40:43], v[224:227]\n"
"s_waitcnt lgkmcnt(1)\n"
"v_mfma_f32_16x16x32_bf16 v[204:207], a[36:39], v[44:47], v[204:207]\n"
"v_mfma_f32_16x16x32_bf16 v[208:211], a[40:43], v[44:47], v[208:211]\n"
"v_mfma_f32_16x16x32_bf16 v[212:215], a[44:47], v[44:47], v[212:215]\n"
"v_mfma_f32_16x16x32_bf16 v[216:219], a[108:111], v[44:47], v[216:219]\n"
"v_mfma_f32_16x16x32_bf16 v[220:223], a[112:115], v[44:47], v[220:223]\n"
"v_mfma_f32_16x16x32_bf16 v[224:227], a[116:119], v[44:47], v[224:227]\n"
"v_mfma_f32_16x16x32_bf16 v[204:207], a[48:51], v[48:51], v[204:207]\n"
"v_mfma_f32_16x16x32_bf16 v[208:211], a[52:55], v[48:51], v[208:211]\n"
"v_mfma_f32_16x16x32_bf16 v[212:215], a[56:59], v[48:51], v[212:215]\n"
"v_mfma_f32_16x16x32_bf16 v[216:219], a[120:123], v[48:51], v[216:219]\n"
"v_mfma_f32_16x16x32_bf16 v[220:223], a[124:127], v[48:51], v[220:223]\n"
"v_mfma_f32_16x16x32_bf16 v[224:227], a[128:131], v[48:51], v[224:227]\n"
"v_mfma_f32_16x16x32_bf16 v[204:207], a[60:63], v[52:55], v[204:207]\n"
"v_mfma_f32_16x16x32_bf16 v[208:211], a[64:67], v[52:55], v[208:211]\n"
"v_mfma_f32_16x16x32_bf16 v[212:215], a[68:71], v[52:55], v[212:215]\n"
"v_mfma_f32_16x16x32_bf16 v[216:219], a[132:135], v[52:55], v[216:219]\n"
"v_mfma_f32_16x16x32_bf16 v[220:223], a[136:139], v[52:55], v[220:223]\n"
"v_mfma_f32_16x16x32_bf16 v[224:227], a[140:143], v[52:55], v[224:227]\n"
"v_mfma_f32_16x16x32_bf16 v[244:247], a[0:3], a[216:219], 0\n"
"s_waitcnt lgkmcnt(0)\n"
"s_nop 0\n"
"v_cvt_pk_bf16_f32 v204, v204, v205\n"
"v_mfma_f32_16x16x32_bf16 v[248:251], a[4:7], a[216:219], 0\n"
"v_cvt_pk_bf16_f32 v205, v206, v207\n"
"v_cvt_pk_bf16_f32 v206, v208, v209\n"
"v_mfma_f32_16x16x32_bf16 v[252:255], a[8:11], a[216:219], 0\n"
"v_cvt_pk_bf16_f32 v207, v210, v211\n"
"v_cvt_pk_bf16_f32 v212, v212, v213\n"
"v_mfma_f32_16x16x32_bf16 v[104:107], a[72:75], a[216:219], 0\n"
"ds_write_b128 v186, v[204:207] offset:20256\n"
"v_mfma_f32_16x16x32_bf16 v[108:111], a[76:79], a[216:219], 0\n"
"s_waitcnt vmcnt(11)\n"
"v_cvt_pk_bf16_f32 v213, v214, v215\n"
"v_cvt_pk_bf16_f32 v216, v216, v217\n"
"v_mfma_f32_16x16x32_bf16 v[228:231], a[80:83], a[216:219], 0\n"
"s_add_u32 m0, s43, 0\n"
"v_cvt_pk_bf16_f32 v217, v218, v219\n"
"v_cvt_pk_bf16_f32 v218, v220, v221\n"
"v_mfma_f32_16x16x32_bf16 v[244:247], a[12:15], a[220:223], v[244:247]\n"
"buffer_load_dwordx4 v27, s[12:15], s3 offen lds\n"
"ds_read_b128 a[224:227], v70 offset:1024\n"
"v_mfma_f32_16x16x32_bf16 v[248:251], a[16:19], a[220:223], v[248:251]\n"
"ds_read_b128 a[228:231], v71 offset:1024\n"
"v_cvt_pk_bf16_f32 v219, v222, v223\n"
"v_mfma_f32_16x16x32_bf16 v[252:255], a[20:23], a[220:223], v[252:255]\n"
"s_add_u32 m0, s43, 1024\n"
"ds_write_b128 v186, v[216:219] offset:20384\n"
"v_mfma_f32_16x16x32_bf16 v[104:107], a[84:87], a[220:223], v[104:107]\n"
"buffer_load_dwordx4 v28, s[12:15], s3 offen lds\n"
"v_cvt_pk_bf16_f32 v214, v224, v225\n"
"v_mfma_f32_16x16x32_bf16 v[108:111], a[88:91], a[220:223], v[108:111]\n"
"v_cvt_pk_bf16_f32 v215, v226, v227\n"
"v_mfma_f32_16x16x32_bf16 v[228:231], a[92:95], a[220:223], v[228:231]\n"
"s_waitcnt vmcnt(11)\n"
"ds_write_b128 v186, v[212:215] offset:20320\n"
"s_add_u32 m0, s43, 2048\n"
"s_add_u32 s43, s79, 0\n"
"buffer_load_dwordx4 v29, s[12:15], s3 offen lds\n"
"s_waitcnt lgkmcnt(3)\n"
"v_mfma_f32_16x16x32_bf16 v[244:247], a[24:27], a[224:227], v[244:247]\n"
"s_waitcnt lgkmcnt(2)\n"
"ds_read_b128 a[232:235], v70 offset:2048\n"
"ds_read_b128 a[236:239], v71 offset:2048\n"
"v_mfma_f32_16x16x32_bf16 v[248:251], a[28:31], a[224:227], v[248:251]\n"
"s_add_u32 m0, s34, 0\n"
"v_mfma_f32_16x16x32_bf16 v[252:255], a[32:35], a[224:227], v[252:255]\n"
"buffer_load_dwordx4 v190, s[8:11], s32 offen lds\n"
"v_mfma_f32_16x16x32_bf16 v[104:107], a[96:99], a[224:227], v[104:107]\n"
"v_mfma_f32_16x16x32_bf16 v[108:111], a[100:103], a[224:227], v[108:111]\n"
"v_mfma_f32_16x16x32_bf16 v[228:231], a[104:107], a[224:227], v[228:231]\n"
"s_waitcnt vmcnt(11)\n"
"s_add_u32 m0, s34, 3072\n"
"ds_read_b128 a[240:243], v188 offset:6144\n"
"v_mfma_f32_16x16x32_bf16 v[244:247], a[36:39], a[228:231], v[244:247]\n"
"buffer_load_dwordx4 v191, s[8:11], s32 offen lds\n"
"ds_read_b128 a[244:247], v189 offset:6144\n"
"v_mfma_f32_16x16x32_bf16 v[248:251], a[40:43], a[228:231], v[248:251]\n"
"s_add_u32 s32, s32, s33\n"
"v_mfma_f32_16x16x32_bf16 v[252:255], a[44:47], a[228:231], v[252:255]\n"
"s_waitcnt lgkmcnt(3)\n"
"v_mfma_f32_16x16x32_bf16 v[104:107], a[108:111], a[228:231], v[104:107]\n"
"s_add_u32 m0, s34, 1024\n"
"v_mfma_f32_16x16x32_bf16 v[108:111], a[112:115], a[228:231], v[108:111]\n"
"buffer_load_dwordx4 v190, s[8:11], s0 offen lds\n"
"v_mfma_f32_16x16x32_bf16 v[228:231], a[116:119], a[228:231], v[228:231]\n"
"v_mfma_f32_16x16x32_bf16 v[244:247], a[48:51], a[232:235], v[244:247]\n"
"s_waitcnt lgkmcnt(2)\n"
"v_mfma_f32_16x16x32_bf16 v[248:251], a[52:55], a[232:235], v[248:251]\n"
"s_waitcnt lgkmcnt(1)\n"
"s_add_u32 m0, s34, 4096\n"
"v_mfma_f32_16x16x32_bf16 v[252:255], a[56:59], a[232:235], v[252:255]\n"
"s_waitcnt vmcnt(11)\n"
"buffer_load_dwordx4 v191, s[8:11], s0 offen lds\n"
"s_add_u32 s0, s0, s33\n"
"v_mfma_f32_16x16x32_bf16 v[104:107], a[120:123], a[232:235], v[104:107]\n"
"ds_read_b128 a[248:251], v188 offset:7168\n"
"ds_read_b128 a[252:255], v189 offset:7168\n"
"v_mfma_f32_16x16x32_bf16 v[108:111], a[124:127], a[232:235], v[108:111]\n"
"v_mfma_f32_16x16x32_bf16 v[228:231], a[128:131], a[232:235], v[228:231]\n"
"s_add_u32 m0, s34, 2048\n"
"v_mfma_f32_16x16x32_bf16 v[244:247], a[60:63], a[236:239], v[244:247]\n"
"buffer_load_dwordx4 v190, s[8:11], s1 offen lds\n"
"v_mfma_f32_16x16x32_bf16 v[248:251], a[64:67], a[236:239], v[248:251]\n"
"v_mfma_f32_16x16x32_bf16 v[252:255], a[68:71], a[236:239], v[252:255]\n"
"v_mfma_f32_16x16x32_bf16 v[104:107], a[132:135], a[236:239], v[104:107]\n"
"s_add_u32 m0, s34, 5120\n"
"v_mfma_f32_16x16x32_bf16 v[108:111], a[136:139], a[236:239], v[108:111]\n"
"s_waitcnt vmcnt(11)\n"
"buffer_load_dwordx4 v191, s[8:11], s1 offen lds\n"
"v_cvt_pk_bf16_f32 v244, v244, v245\n"
"v_mfma_f32_16x16x32_bf16 v[228:231], a[140:143], a[236:239], v[228:231]\n"
"s_add_u32 s1, s1, s33\n"
"ds_read_b128 v[56:59], v188 offset:8192\n"
"ds_read_b128 v[60:63], v189 offset:8192\n"
"v_mfma_f32_16x16x32_bf16 v[120:123], a[0:3], a[240:243], 0\n"
"s_waitcnt lgkmcnt(4)\n"
"v_cvt_pk_bf16_f32 v245, v246, v247\n"
"v_cvt_pk_bf16_f32 v246, v248, v249\n"
"v_mfma_f32_16x16x32_bf16 v[124:127], a[4:7], a[240:243], 0\n"
"s_add_u32 m0, s34, 6144\n"
"v_cvt_pk_bf16_f32 v247, v250, v251\n"
"v_cvt_pk_bf16_f32 v252, v252, v253\n"
"v_mfma_f32_16x16x32_bf16 v[128:131], a[8:11], a[240:243], 0\n"
"buffer_load_dwordx4 v190, s[8:11], s32 offen lds\n"
"ds_write_b128 v186, v[244:247] offset:33760\n"
"v_mfma_f32_16x16x32_bf16 v[132:135], a[72:75], a[240:243], 0\n"
"v_cvt_pk_bf16_f32 v253, v254, v255\n"
"v_cvt_pk_bf16_f32 v104, v104, v105\n"
"v_mfma_f32_16x16x32_bf16 v[136:139], a[76:79], a[240:243], 0\n"
"v_cvt_pk_bf16_f32 v105, v106, v107\n"
"v_cvt_pk_bf16_f32 v106, v108, v109\n"
"v_mfma_f32_16x16x32_bf16 v[140:143], a[80:83], a[240:243], 0\n"
"v_cvt_pk_bf16_f32 v107, v110, v111\n"
"v_cvt_pk_bf16_f32 v254, v228, v229\n"
"v_mfma_f32_16x16x32_bf16 v[120:123], a[12:15], a[244:247], v[120:123]\n"
"s_waitcnt lgkmcnt(4)\n"
"ds_write_b128 v186, v[104:107] offset:33888\n"
"v_mfma_f32_16x16x32_bf16 v[124:127], a[16:19], a[244:247], v[124:127]\n"
"v_cvt_pk_bf16_f32 v255, v230, v231\n"
"v_mfma_f32_16x16x32_bf16 v[128:131], a[20:23], a[244:247], v[128:131]\n"
"ds_write_b128 v186, v[252:255] offset:33824\n"
"v_mfma_f32_16x16x32_bf16 v[132:135], a[84:87], a[244:247], v[132:135]\n"
"v_mov_b64 v[252:253], 0\n"
"v_mov_b64 v[254:255], 0\n"
"v_mfma_f32_16x16x32_bf16 v[136:139], a[88:91], a[244:247], v[136:139]\n"
"ds_read_b64 v[104:105], v20 offset:1088\n"
"v_mfma_f32_16x16x32_bf16 v[140:143], a[92:95], a[244:247], v[140:143]\n"
"ds_read_b64 v[110:111], v20 offset:1152\n"
"v_subrev_u32 v20, s49, v20\n"
"v_mfma_f32_16x16x32_bf16 v[120:123], a[24:27], a[248:251], v[120:123]\n"
"s_waitcnt lgkmcnt(7)\n"
"v_mfma_f32_16x16x32_bf16 v[124:127], a[28:31], a[248:251], v[124:127]\n"
"v_mfma_f32_16x16x32_bf16 v[128:131], a[32:35], a[248:251], v[128:131]\n"
"v_mfma_f32_16x16x32_bf16 v[132:135], a[96:99], a[248:251], v[132:135]\n"
"v_mfma_f32_16x16x32_bf16 v[136:139], a[100:103], a[248:251], v[136:139]\n"
"v_mfma_f32_16x16x32_bf16 v[140:143], a[104:107], a[248:251], v[140:143]\n"
"s_waitcnt lgkmcnt(1)\n"
"v_mfma_f32_16x16x32_bf16 v[120:123], a[36:39], a[252:255], v[120:123]\n"
"s_waitcnt lgkmcnt(0)\n"
"v_bfi_b32 v104, v8, v104, v26\n"
"v_bfi_b32 v105, v9, v105, v26\n"
"v_mfma_f32_16x16x32_bf16 v[124:127], a[40:43], a[252:255], v[124:127]\n"
"v_bfi_b32 v110, v10, v110, v26\n"
"v_bfi_b32 v111, v11, v111, v26\n"
"v_mfma_f32_16x16x32_bf16 v[128:131], a[44:47], a[252:255], v[128:131]\n"
"v_mfma_f32_16x16x32_bf16 v[132:135], a[108:111], a[252:255], v[132:135]\n"
"v_mfma_f32_16x16x32_bf16 v[136:139], a[112:115], a[252:255], v[136:139]\n"
"v_mfma_f32_16x16x32_bf16 v[140:143], a[116:119], a[252:255], v[140:143]\n"
"v_mfma_f32_16x16x32_bf16 v[120:123], a[48:51], v[56:59], v[120:123]\n"
"v_mfma_f32_16x16x32_bf16 v[124:127], a[52:55], v[56:59], v[124:127]\n"
"s_add_u32 m0, s34, 9216\n"
"s_nop 0\n"
"buffer_load_dwordx4 v191, s[8:11], s32 offen lds\n"
"v_mfma_f32_16x16x32_bf16 v[128:131], a[56:59], v[56:59], v[128:131]\n"
"s_add_u32 s32, s32, s33\n"
"v_mfma_f32_16x16x32_bf16 v[132:135], a[120:123], v[56:59], v[132:135]\n"
"v_mfma_f32_16x16x32_bf16 v[136:139], a[124:127], v[56:59], v[136:139]\n"
"v_mfma_f32_16x16x32_bf16 v[140:143], a[128:131], v[56:59], v[140:143]\n"
"s_add_u32 m0, s34, 7168\n"
"s_nop 0\n"
"buffer_load_dwordx4 v190, s[8:11], s0 offen lds\n"
"v_mfma_f32_16x16x32_bf16 v[120:123], a[60:63], v[60:63], v[120:123]\n"
"v_mfma_f32_16x16x32_bf16 v[124:127], a[64:67], v[60:63], v[124:127]\n"
"v_mfma_f32_16x16x32_bf16 v[128:131], a[68:71], v[60:63], v[128:131]\n"
"v_mfma_f32_16x16x32_bf16 v[132:135], a[132:135], v[60:63], v[132:135]\n"
"s_add_u32 m0, s34, 10240\n"
"s_nop 0\n"
"buffer_load_dwordx4 v191, s[8:11], s0 offen lds\n"
"v_mfma_f32_16x16x32_bf16 v[136:139], a[136:139], v[60:63], v[136:139]\n"
"s_add_u32 s0, s0, s33\n"
"v_cvt_pk_bf16_f32 v120, v120, v121\n"
"v_mfma_f32_16x16x32_bf16 v[140:143], a[140:143], v[60:63], v[140:143]\n"
"v_cvt_pk_bf16_f32 v121, v122, v123\n"
"v_cvt_pk_bf16_f32 v122, v124, v125\n"
"v_cvt_pk_bf16_f32 v123, v126, v127\n"
"v_cvt_pk_bf16_f32 v128, v128, v129\n"
"v_cvt_pk_bf16_f32 v132, v132, v133\n"
"v_cvt_pk_bf16_f32 v129, v130, v131\n"
"s_add_u32 m0, s34, 8192\n"
"v_cvt_pk_bf16_f32 v133, v134, v135\n"
"buffer_load_dwordx4 v190, s[8:11], s1 offen lds\n"
"v_cvt_pk_bf16_f32 v134, v136, v137\n"
"v_cvt_pk_bf16_f32 v135, v138, v139\n"
"v_cvt_pk_bf16_f32 v130, v140, v141\n"
"v_cvt_pk_bf16_f32 v131, v142, v143\n"
"ds_write_b128 v186, v[120:123] offset:47264\n"
"s_add_u32 m0, s34, 11264\n"
"s_nop 0\n"
"buffer_load_dwordx4 v191, s[8:11], s1 offen lds\n"
"s_add_u32 s1, s1, s33\n"
"s_waitcnt vmcnt(12)\n"
"s_barrier\n"
"ds_read_b64_tr_b16 a[144:145], v68\n"
"ds_read_b64_tr_b16 a[146:147], v68 offset:1536\n"
"ds_read_b64_tr_b16 a[148:149], v68 offset:32\n"
"ds_read_b64_tr_b16 a[150:151], v68 offset:1568\n"
"ds_read_b64_tr_b16 a[152:153], v68 offset:64\n"
"ds_read_b64_tr_b16 a[154:155], v68 offset:1600\n"
"ds_read_b64_tr_b16 a[156:157], v68 offset:3072\n"
"ds_read_b64_tr_b16 a[158:159], v68 offset:4608\n"
"ds_read_b64_tr_b16 a[160:161], v68 offset:3104\n"
"buffer_load_dword v200, off, s[28:31], s39 sc1\n"
"ds_read_b64_tr_b16 a[162:163], v68 offset:4640\n"
"ds_read_b64_tr_b16 a[164:165], v68 offset:3136\n"
"ds_read_b64_tr_b16 a[166:167], v68 offset:4672\n"
"ds_read_b64_tr_b16 a[168:169], v68 offset:6752\n"
"ds_read_b64_tr_b16 a[170:171], v68 offset:8288\n"
"ds_read_b64_tr_b16 a[172:173], v68 offset:6784\n"
"ds_read_b64_tr_b16 a[174:175], v68 offset:8320\n"
"ds_read_b64_tr_b16 a[176:177], v68 offset:6816\n"
"ds_read_b64_tr_b16 a[178:179], v68 offset:8352\n"
"ds_read_b64_tr_b16 a[180:181], v68 offset:9824\n"
"ds_read_b64_tr_b16 a[182:183], v68 offset:11360\n"
"s_waitcnt lgkmcnt(15)\n"
"v_mfma_f32_16x16x32_bf16 v[204:207], a[144:147], v[32:35], 0\n"
"ds_read_b64_tr_b16 a[184:185], v68 offset:9856\n"
"ds_read_b64_tr_b16 a[186:187], v68 offset:11392\n"
"v_mfma_f32_16x16x32_bf16 v[208:211], a[148:151], v[32:35], 0\n"
"s_waitcnt lgkmcnt(15)\n"
"ds_read_b64_tr_b16 a[188:189], v68 offset:9888\n"
"ds_read_b64_tr_b16 a[190:191], v68 offset:11424\n"
"v_mfma_f32_16x16x32_bf16 v[212:215], a[152:155], v[32:35], 0\n"
"s_waitcnt lgkmcnt(15)\n"
"ds_read_b64_tr_b16 a[192:193], v69\n"
"ds_read_b64_tr_b16 a[194:195], v69 offset:1536\n"
"v_mfma_f32_16x16x32_bf16 v[204:207], a[156:159], v[36:39], v[204:207]\n"
"s_waitcnt lgkmcnt(15)\n"
"ds_read_b64_tr_b16 a[196:197], v69 offset:32\n"
"ds_read_b64_tr_b16 a[198:199], v69 offset:1568\n"
"v_mfma_f32_16x16x32_bf16 v[208:211], a[160:163], v[36:39], v[208:211]\n"
"s_waitcnt lgkmcnt(15)\n"
"ds_read_b64_tr_b16 a[200:201], v69 offset:64\n"
"ds_read_b64_tr_b16 a[202:203], v69 offset:1600\n"
"v_mfma_f32_16x16x32_bf16 v[212:215], a[164:167], v[36:39], v[212:215]\n"
"s_waitcnt lgkmcnt(15)\n"
"ds_read_b64_tr_b16 a[204:205], v69 offset:3072\n"
"ds_read_b64_tr_b16 a[206:207], v69 offset:4608\n"
"v_mfma_f32_16x16x32_bf16 v[204:207], a[168:171], v[40:43], v[204:207]\n"
"s_waitcnt lgkmcnt(15)\n"
"ds_read_b64_tr_b16 a[208:209], v69 offset:3104\n"
"ds_read_b64_tr_b16 a[210:211], v69 offset:4640\n"
"v_mfma_f32_16x16x32_bf16 v[208:211], a[172:175], v[40:43], v[208:211]\n"
"s_waitcnt lgkmcnt(15)\n"
"ds_read_b64_tr_b16 a[212:213], v69 offset:3136\n"
"ds_read_b64_tr_b16 a[214:215], v69 offset:4672\n"
"v_mfma_f32_16x16x32_bf16 v[212:215], a[176:179], v[40:43], v[212:215]\n"
"s_waitcnt lgkmcnt(15)\n"
"ds_write_b128 v186, v[128:131] offset:47328\n"
"v_mfma_f32_16x16x32_bf16 v[204:207], a[180:183], v[44:47], v[204:207]\n"
"s_waitcnt lgkmcnt(13)\n"
"v_mfma_f32_16x16x32_bf16 v[208:211], a[184:187], v[44:47], v[208:211]\n"
"s_waitcnt lgkmcnt(11)\n"
"ds_write_b128 v186, v[132:135] offset:47392\n"
"v_mfma_f32_16x16x32_bf16 v[212:215], a[188:191], v[44:47], v[212:215]\n"
"s_waitcnt lgkmcnt(8)\n"
"v_mfma_f32_16x16x32_bf16 v[204:207], a[192:195], v[48:51], v[204:207]\n"
"s_waitcnt lgkmcnt(6)\n"
"v_mfma_f32_16x16x32_bf16 v[208:211], a[196:199], v[48:51], v[208:211]\n"
"s_waitcnt lgkmcnt(4)\n"
"v_mfma_f32_16x16x32_bf16 v[212:215], a[200:203], v[48:51], v[212:215]\n"
"s_waitcnt lgkmcnt(2)\n"
"v_mfma_f32_16x16x32_bf16 v[204:207], a[204:207], v[52:55], v[204:207]\n"
"v_mfma_f32_16x16x32_bf16 v[208:211], a[208:211], v[52:55], v[208:211]\n"
"v_mfma_f32_16x16x32_bf16 v[212:215], a[212:215], v[52:55], v[212:215]\n"
"v_mfma_f32_16x16x32_bf16 v[216:219], a[144:147], a[216:219], 0\n"
"s_waitcnt lgkmcnt(0)\n"
"s_barrier\n"
"v_mfma_f32_16x16x32_bf16 v[216:219], a[156:159], a[220:223], v[216:219]\n"
"s_waitcnt vmcnt(11)\n"
"ds_read_b128 v[112:115], v178\n"
"ds_read_b128 v[32:35], v188\n"
"v_mfma_f32_16x16x32_bf16 v[220:223], a[148:151], a[216:219], 0\n"
"ds_read_b128 v[36:39], v189\n"
"v_cvt_pk_bf16_f32 v144, v204, v205\n"
"v_mfma_f32_16x16x32_bf16 v[216:219], a[168:171], a[224:227], v[216:219]\n"
"ds_read_b128 v[120:123], v179\n"
"v_cvt_pk_bf16_f32 v145, v206, v207\n"
"v_mfma_f32_16x16x32_bf16 v[220:223], a[160:163], a[220:223], v[220:223]\n"
"ds_read_b64 v[82:83], v178 offset:64\n"
"v_cvt_pk_bf16_f32 v146, v208, v209\n"
"v_mfma_f32_16x16x32_bf16 v[216:219], a[180:183], a[228:231], v[216:219]\n"
"ds_read_b64 v[84:85], v179 offset:64\n"
"v_cvt_pk_bf16_f32 v147, v210, v211\n"
"v_mfma_f32_16x16x32_bf16 v[220:223], a[172:175], a[224:227], v[220:223]\n"
"ds_read_b128 v[116:119], v178 offset:13504\n"
"v_cvt_pk_bf16_f32 v158, v212, v213\n"
"v_mfma_f32_16x16x32_bf16 v[216:219], a[192:195], a[232:235], v[216:219]\n"
"ds_read_b128 v[124:127], v179 offset:13504\n"
"v_cvt_pk_bf16_f32 v159, v214, v215\n"
"v_mfma_f32_16x16x32_bf16 v[220:223], a[184:187], a[228:231], v[220:223]\n"
"ds_read_b64 v[90:91], v178 offset:13568\n"
"buffer_load_dword v237, v67, s[8:11], s78 offen\n"
"v_mfma_f32_16x16x32_bf16 v[216:219], a[204:207], a[236:239], v[216:219]\n"
"s_waitcnt vmcnt(10) lgkmcnt(8)\n"
"ds_read_b64 v[92:93], v179 offset:13568\n"
"s_add_u32 s78, s78, s33\n"
"v_mfma_f32_16x16x32_bf16 v[112:115], v[112:115], v[144:147], 0\n"
".Lic0%=:\n"
"s_cbranch_execz .Lib0%=\n"
"s_waitcnt lgkmcnt(8)\n"
"ds_read_b128 v[40:43], v188 offset:1024\n"
"v_mfma_f32_16x16x32_bf16 v[220:223], a[196:199], a[232:235], v[220:223]\n"
"s_waitcnt lgkmcnt(8)\n"
"ds_read_b128 v[44:47], v189 offset:1024\n"
"v_cvt_pk_bf16_f32 v148, v216, v217\n"
"v_mfma_f32_16x16x32_bf16 v[220:223], a[208:211], a[236:239], v[220:223]\n"
"s_waitcnt lgkmcnt(8)\n"
"ds_read_b128 v[128:131], v178 offset:27008\n"
"v_cvt_pk_bf16_f32 v149, v218, v219\n"
"v_mfma_f32_16x16x32_bf16 v[120:123], v[120:123], v[144:147], 0\n"
"s_waitcnt lgkmcnt(8)\n"
"ds_read_b128 v[136:139], v179 offset:27008\n"
"buffer_load_dword v237, v67, s[8:11], s78 offen\n"
"v_mfma_f32_16x16x32_bf16 v[112:115], v[80:83], v[156:159], v[112:115]\n"
"s_waitcnt lgkmcnt(8)\n"
"ds_read_b64 v[98:99], v178 offset:27072\n"
"v_cvt_pk_bf16_f32 v150, v220, v221\n"
"v_mfma_f32_16x16x32_bf16 v[120:123], v[84:87], v[158:161], v[120:123]\n"
"s_waitcnt lgkmcnt(8)\n"
"ds_read_b64 v[100:101], v179 offset:27072\n"
"v_cvt_pk_bf16_f32 v151, v222, v223\n"
"v_mfma_f32_16x16x32_bf16 v[116:119], v[116:119], v[144:147], 0\n"
"s_waitcnt lgkmcnt(8)\n"
"ds_read_b128 v[132:135], v180\n"
"v_subrev_u32 v178, s48, v178\n"
"v_mfma_f32_16x16x32_bf16 v[124:127], v[124:127], v[144:147], 0\n"
"s_waitcnt lgkmcnt(8)\n"
"ds_read_b128 v[140:143], v181\n"
"v_subrev_u32 v179, s48, v179\n"
"v_mfma_f32_16x16x32_bf16 v[116:119], v[88:91], v[156:159], v[116:119]\n"
"s_waitcnt lgkmcnt(8)\n"
"ds_read_b64 v[106:107], v180 offset:64\n"
"v_subrev_u32 v180, s48, v180\n"
"v_mfma_f32_16x16x32_bf16 v[124:127], v[92:95], v[158:161], v[124:127]\n"
"s_waitcnt vmcnt(9)\n"
"ds_read_b64 v[108:109], v181 offset:64\n"
"v_subrev_u32 v181, s48, v181\n"
"v_mfma_f32_16x16x32_bf16 v[212:215], a[0:3], v[32:35], 0\n"
"s_waitcnt lgkmcnt(8)\n"
"ds_read_b128 v[48:51], v188 offset:2048\n"
"v_max_f32 v112, v112, v120\n"
"v_mfma_f32_16x16x32_bf16 v[216:219], a[4:7], v[32:35], 0\n"
"s_waitcnt lgkmcnt(8)\n"
"ds_read_b128 v[52:55], v189 offset:2048\n"
"v_max_f32 v113, v113, v121\n"
"v_mfma_f32_16x16x32_bf16 v[128:131], v[128:131], v[144:147], 0\n"
"s_waitcnt lgkmcnt(8)\n"
"ds_read_b64_tr_b16 v[244:245], v182 offset:128\n"
"v_max_f32 v114, v114, v122\n"
"v_mfma_f32_16x16x32_bf16 v[136:139], v[136:139], v[144:147], 0\n"
"s_waitcnt lgkmcnt(8)\n"
"ds_read_b64_tr_b16 v[246:247], v183 offset:128\n"
"v_max_f32 v115, v115, v123\n"
"v_mfma_f32_16x16x32_bf16 v[128:131], v[96:99], v[156:159], v[128:131]\n"
"s_waitcnt lgkmcnt(8)\n"
"ds_read_b64_tr_b16 v[248:249], v182 offset:896\n"
"v_max3_f32 v170, v112, v113, v114\n"
"v_mfma_f32_16x16x32_bf16 v[136:139], v[100:103], v[158:161], v[136:139]\n"
"s_waitcnt lgkmcnt(8)\n"
"ds_read_b64_tr_b16 v[250:251], v183 offset:896\n"
"v_max_f32 v116, v116, v124\n"
"v_mfma_f32_16x16x32_bf16 v[132:135], v[132:135], v[144:147], 0\n"
"s_waitcnt lgkmcnt(8)\n"
"ds_read_b64_tr_b16 v[120:121], v182 offset:136\n"
"v_max_f32 v117, v117, v125\n"
"v_mfma_f32_16x16x32_bf16 v[140:143], v[140:143], v[144:147], 0\n"
"s_waitcnt lgkmcnt(8)\n"
"ds_read_b64_tr_b16 v[122:123], v183 offset:136\n"
"v_max_f32 v118, v118, v126\n"
"v_mfma_f32_16x16x32_bf16 v[132:135], v[104:107], v[156:159], v[132:135]\n"
"s_waitcnt lgkmcnt(8)\n"
"ds_read_b64_tr_b16 v[124:125], v182 offset:904\n"
"v_max_f32 v119, v119, v127\n"
"v_mfma_f32_16x16x32_bf16 v[140:143], v[108:111], v[158:161], v[140:143]\n"
"s_add_u32 m0, s34, 0\n"
"ds_read_b64_tr_b16 v[126:127], v183 offset:904\n"
"buffer_load_dwordx4 v190, s[8:11], s32 offen lds\n"
"v_mfma_f32_16x16x32_bf16 v[220:223], a[8:11], v[32:35], 0\n"
"s_add_u32 s78, s78, s33\n"
"v_max3_f32 v170, v170, v115, v116\n"
"v_max3_f32 v170, v170, v117, v118\n"
"v_mfma_f32_16x16x32_bf16 v[212:215], a[12:15], v[36:39], v[212:215]\n"
"v_max_f32 v128, v128, v136\n"
"v_max_f32 v129, v129, v137\n"
"v_mfma_f32_16x16x32_bf16 v[216:219], a[16:19], v[36:39], v[216:219]\n"
"ds_read_b64_tr_b16 v[136:137], v182 offset:72\n"
"v_max_f32 v130, v130, v138\n"
"v_mfma_f32_16x16x32_bf16 v[220:223], a[20:23], v[36:39], v[220:223]\n"
"v_max_f32 v131, v131, v139\n"
"v_max3_f32 v170, v170, v119, v128\n"
"v_mfma_f32_16x16x32_bf16 v[212:215], a[24:27], v[40:43], v[212:215]\n"
"ds_read_b64_tr_b16 v[138:139], v183 offset:72\n"
"v_max3_f32 v170, v170, v129, v130\n"
"v_mfma_f32_16x16x32_bf16 v[216:219], a[28:31], v[40:43], v[216:219]\n"
"v_max_f32 v132, v132, v140\n"
"v_max_f32 v133, v133, v141\n"
"v_mfma_f32_16x16x32_bf16 v[220:223], a[32:35], v[40:43], v[220:223]\n"
"ds_read_b64_tr_b16 v[140:141], v182 offset:840\n"
"v_max_f32 v134, v134, v142\n"
"v_mfma_f32_16x16x32_bf16 v[212:215], a[36:39], v[44:47], v[212:215]\n"
"s_waitcnt lgkmcnt(11)\n"
"v_max_f32 v135, v135, v143\n"
"v_max3_f32 v170, v170, v131, v132\n"
"v_mfma_f32_16x16x32_bf16 v[216:219], a[40:43], v[44:47], v[216:219]\n"
"s_add_u32 m0, s34, 3072\n"
"ds_read_b64_tr_b16 v[142:143], v183 offset:840\n"
"buffer_load_dwordx4 v191, s[8:11], s32 offen lds\n"
"v_mfma_f32_16x16x32_bf16 v[220:223], a[44:47], v[44:47], v[220:223]\n"
"s_add_u32 s32, s32, s33\n"
"v_max3_f32 v170, v170, v133, v134\n"
"v_mov_b64 v[98:99], 0\n"
"v_mfma_f32_16x16x32_bf16 v[212:215], a[48:51], v[48:51], v[212:215]\n"
"v_mov_b64 v[100:101], 0\n"
"v_mov_b64 v[106:107], 0\n"
"v_mfma_f32_16x16x32_bf16 v[216:219], a[52:55], v[48:51], v[216:219]\n"
"v_mov_b64 v[108:109], 0\n"
"v_max_f32 v171, v170, v135\n"
"v_mfma_f32_16x16x32_bf16 v[220:223], a[56:59], v[48:51], v[220:223]\n"
"v_max_f32 v170, v170, v135\n"
"v_mfma_f32_16x16x32_bf16 v[212:215], a[60:63], v[52:55], v[212:215]\n"
"s_nop 0\n"
"v_permlane16_swap_b32 v170, v171\n"
"v_mfma_f32_16x16x32_bf16 v[216:219], a[64:67], v[52:55], v[216:219]\n"
"v_max_f32 v170, v170, v171\n"
"v_mov_b32 v171, v170\n"
"v_mfma_f32_16x16x32_bf16 v[220:223], a[68:71], v[52:55], v[220:223]\n"
"s_nop 0\n"
"v_permlane32_swap_b32 v170, v171\n"
"v_mfma_f32_16x16x32_bf16 v[224:227], a[152:155], a[216:219], 0\n"
"s_add_u32 m0, s34, 1024\n"
"v_cvt_pk_bf16_f32 v212, v212, v213\n"
"v_cvt_pk_bf16_f32 v213, v214, v215\n"
"v_mfma_f32_16x16x32_bf16 v[224:227], a[164:167], a[220:223], v[224:227]\n"
"buffer_load_dwordx4 v190, s[8:11], s0 offen lds\n"
"v_cvt_pk_bf16_f32 v214, v216, v217\n"
"v_mfma_f32_16x16x32_bf16 v[228:231], a[144:147], a[240:243], 0\n"
"v_cvt_pk_bf16_f32 v215, v218, v219\n"
"v_cvt_pk_bf16_f32 v220, v220, v221\n"
"v_mfma_f32_16x16x32_bf16 v[224:227], a[176:179], a[224:227], v[224:227]\n"
"ds_write_b128 v186, v[212:215] offset:60768\n"
"v_mfma_f32_16x16x32_bf16 v[228:231], a[156:159], a[244:247], v[228:231]\n"
"v_cvt_pk_bf16_f32 v221, v222, v223\n"
"v_max_f32 v170, v170, v171\n"
"v_mfma_f32_16x16x32_bf16 v[204:207], a[148:151], a[240:243], 0\n"
"v_mul_f32 v170, 0xbe553b95, v170\n"
"v_fmamk_f32 v112, v112, 0x3e553b95, v170\n"
"v_mfma_f32_16x16x32_bf16 v[224:227], a[188:191], a[228:231], v[224:227]\n"
"v_fmamk_f32 v113, v113, 0x3e553b95, v170\n"
"v_fmamk_f32 v114, v114, 0x3e553b95, v170\n"
"v_mfma_f32_16x16x32_bf16 v[228:231], a[168:171], a[248:251], v[228:231]\n"
"v_fmamk_f32 v115, v115, 0x3e553b95, v170\n"
"v_fmamk_f32 v116, v116, 0x3e553b95, v170\n"
"v_mfma_f32_16x16x32_bf16 v[204:207], a[160:163], a[244:247], v[204:207]\n"
"s_add_u32 m0, s34, 4096\n"
"v_exp_f32 v112, v112\n"
"v_mfma_f32_16x16x32_bf16 v[224:227], a[200:203], a[232:235], v[224:227]\n"
"buffer_load_dwordx4 v191, s[8:11], s0 offen lds\n"
"v_fmamk_f32 v117, v117, 0x3e553b95, v170\n"
"v_mfma_f32_16x16x32_bf16 v[228:231], a[180:183], a[252:255], v[228:231]\n"
"s_add_u32 s0, s0, s33\n"
"v_exp_f32 v113, v113\n"
"v_mfma_f32_16x16x32_bf16 v[204:207], a[172:175], a[248:251], v[204:207]\n"
"v_exp_f32 v114, v114\n"
"v_mfma_f32_16x16x32_bf16 v[224:227], a[212:215], a[236:239], v[224:227]\n"
"v_exp_f32 v115, v115\n"
"v_mfma_f32_16x16x32_bf16 v[228:231], a[192:195], v[56:59], v[228:231]\n"
"v_cvt_pk_bf16_f32 v82, v112, v114\n"
"v_cvt_pk_bf16_f32 v83, v113, v115\n"
"v_mfma_f32_16x16x32_bf16 v[204:207], a[184:187], a[252:255], v[204:207]\n"
"ds_read_b64_tr_b16 v[112:113], v182 offset:27136\n"
"ds_read_b64_tr_b16 v[114:115], v183 offset:27136\n"
"v_mfma_f32_16x16x32_bf16 v[228:231], a[204:207], v[60:63], v[228:231]\n"
"v_cvt_pk_bf16_f32 v162, v224, v225\n"
"v_cvt_pk_bf16_f32 v163, v226, v227\n"
"v_mfma_f32_16x16x32_bf16 v[204:207], a[196:199], v[56:59], v[204:207]\n"
"s_waitcnt vmcnt(6)\n"
"v_fmamk_f32 v118, v118, 0x3e553b95, v170\n"
"v_and_b32 v200, 0xffffff00, v200\n"
"v_mfma_f32_16x16x32_bf16 v[204:207], a[208:211], v[60:63], v[204:207]\n"
"s_add_u32 m0, s34, 2048\n"
"v_add_u32 v200, 0x100, v200\n"
"v_sub_u32 v201, 0, v200\n"
"v_mfma_f32_16x16x32_bf16 v[224:227], a[72:75], v[32:35], 0\n"
"buffer_load_dwordx4 v190, s[8:11], s1 offen lds\n"
"s_nop 0\n"
"v_readfirstlane_b32 s50, v201\n"
"v_mfma_f32_16x16x32_bf16 v[224:227], a[84:87], v[36:39], v[224:227]\n"
"v_cvt_pk_bf16_f32 v152, v228, v229\n"
"v_cvt_pk_bf16_f32 v153, v230, v231\n"
"v_mfma_f32_16x16x32_bf16 v[228:231], a[76:79], v[32:35], 0\n"
"v_cvt_pk_bf16_f32 v154, v204, v205\n"
"v_cvt_pk_bf16_f32 v155, v206, v207\n"
"v_mfma_f32_16x16x32_bf16 v[204:207], a[80:83], v[32:35], 0\n"
"s_mov_b64 exec, 1\n"
"buffer_store_dword v200, off, s[28:31], s39 offset:128 sc1\n"
"s_mov_b64 exec, -1\n"
"v_mfma_f32_16x16x32_bf16 v[228:231], a[88:91], v[36:39], v[228:231]\n"
"v_fmamk_f32 v119, v119, 0x3e553b95, v170\n"
"v_exp_f32 v116, v116\n"
"v_mfma_f32_16x16x32_bf16 v[204:207], a[92:95], v[36:39], v[204:207]\n"
"v_exp_f32 v117, v117\n"
"v_mfma_f32_16x16x32_bf16 v[224:227], a[96:99], v[40:43], v[224:227]\n"
"v_exp_f32 v118, v118\n"
"v_mfma_f32_16x16x32_bf16 v[228:231], a[100:103], v[40:43], v[228:231]\n"
"s_add_u32 m0, s34, 5120\n"
"v_exp_f32 v119, v119\n"
"v_mfma_f32_16x16x32_bf16 v[204:207], a[104:107], v[40:43], v[204:207]\n"
"buffer_load_dwordx4 v191, s[8:11], s1 offen lds\n"
"v_cvt_pk_bf16_f32 v84, v116, v118\n"
"v_mfma_f32_16x16x32_bf16 v[224:227], a[108:111], v[44:47], v[224:227]\n"
"s_add_u32 s1, s1, s33\n"
"v_cvt_pk_bf16_f32 v85, v117, v119\n"
"v_fmamk_f32 v128, v128, 0x3e553b95, v170\n"
"v_mfma_f32_16x16x32_bf16 v[228:231], a[112:115], v[44:47], v[228:231]\n"
"s_waitcnt lgkmcnt(3)\n"
"ds_read_b64_tr_b16 v[116:117], v182 offset:27904\n"
"ds_read_b64_tr_b16 v[118:119], v183 offset:27904\n"
"v_mfma_f32_16x16x32_bf16 v[204:207], a[116:119], v[44:47], v[204:207]\n"
"ds_read_b64 v[80:81], v20 offset:272\n"
"ds_read_b64 v[86:87], v20 offset:336\n"
"v_mfma_f32_16x16x32_bf16 v[224:227], a[120:123], v[48:51], v[224:227]\n"
"ds_read_b64 v[88:89], v20 offset:544\n"
"v_fmamk_f32 v129, v129, 0x3e553b95, v170\n"
"v_mfma_f32_16x16x32_bf16 v[228:231], a[124:127], v[48:51], v[228:231]\n"
"v_fmamk_f32 v130, v130, 0x3e553b95, v170\n"
"v_fmamk_f32 v131, v131, 0x3e553b95, v170\n"
"v_mfma_f32_16x16x32_bf16 v[204:207], a[128:131], v[48:51], v[204:207]\n"
"ds_read_b64 v[94:95], v20 offset:608\n"
"ds_read_b64 v[96:97], v20 offset:816\n"
"v_mfma_f32_16x16x32_bf16 v[224:227], a[132:135], v[52:55], v[224:227]\n"
"v_exp_f32 v128, v128\n"
"v_mfma_f32_16x16x32_bf16 v[228:231], a[136:139], v[52:55], v[228:231]\n"
"v_exp_f32 v129, v129\n"
"v_mfma_f32_16x16x32_bf16 v[204:207], a[140:143], v[52:55], v[204:207]\n"
"v_exp_f32 v130, v130\n"
"v_mfma_f32_16x16x32_bf16 v[208:211], a[152:155], a[240:243], 0\n"
"ds_read_b64 v[102:103], v20 offset:880\n"
"ds_read_b64 v[104:105], v20 offset:1088\n"
"v_mfma_f32_16x16x32_bf16 v[208:211], a[164:167], a[244:247], v[208:211]\n"
"s_waitcnt lgkmcnt(6)\n"
"v_cvt_pk_bf16_f32 v224, v224, v225\n"
"v_cvt_pk_bf16_f32 v225, v226, v227\n"
"v_mfma_f32_16x16x32_bf16 v[208:211], a[176:179], a[248:251], v[208:211]\n"
"s_waitcnt lgkmcnt(4)\n"
"v_cvt_pk_bf16_f32 v226, v228, v229\n"
"v_cvt_pk_bf16_f32 v227, v230, v231\n"
"v_mfma_f32_16x16x32_bf16 v[208:211], a[188:191], a[252:255], v[208:211]\n"
"s_waitcnt lgkmcnt(3)\n"
"ds_write_b128 v186, v[224:227] offset:60896\n"
"v_mfma_f32_16x16x32_bf16 v[208:211], a[200:203], v[56:59], v[208:211]\n"
"s_waitcnt lgkmcnt(3)\n"
"v_cvt_pk_bf16_f32 v222, v204, v205\n"
"v_cvt_pk_bf16_f32 v223, v206, v207\n"
"v_mfma_f32_16x16x32_bf16 v[208:211], a[212:215], v[60:63], v[208:211]\n"
"v_exp_f32 v131, v131\n"
"v_mfma_f32_16x16x32_bf16 v[216:219], a[148:151], v[32:35], 0\n"
"s_waitcnt lgkmcnt(2)\n"
"ds_write_b128 v186, v[220:223] offset:60832\n"
"v_mfma_f32_16x16x32_bf16 v[212:215], a[152:155], v[32:35], 0\n"
"s_waitcnt lgkmcnt(2)\n"
"v_cvt_pk_bf16_f32 v90, v128, v130\n"
"v_cvt_pk_bf16_f32 v91, v129, v131\n"
"v_mfma_f32_16x16x32_bf16 v[216:219], a[160:163], v[36:39], v[216:219]\n"
"v_cvt_pk_bf16_f32 v166, v208, v209\n"
"v_cvt_pk_bf16_f32 v167, v210, v211\n"
"v_mfma_f32_16x16x32_bf16 v[208:211], a[144:147], v[32:35], 0\n"
"ds_read_b128 v[32:35], v188 offset:6144\n"
"v_fmamk_f32 v132, v132, 0x3e553b95, v170\n"
"v_mfma_f32_16x16x32_bf16 v[208:211], a[156:159], v[36:39], v[208:211]\n"
"ds_read_b64 v[110:111], v20 offset:1152\n"
"v_fmamk_f32 v133, v133, 0x3e553b95, v170\n"
"v_mfma_f32_16x16x32_bf16 v[212:215], a[164:167], v[36:39], v[212:215]\n"
"ds_read_b128 v[36:39], v189 offset:6144\n"
"v_fmamk_f32 v134, v134, 0x3e553b95, v170\n"
"v_mfma_f32_16x16x32_bf16 v[208:211], a[168:171], v[40:43], v[208:211]\n"
"v_fmamk_f32 v135, v135, 0x3e553b95, v170\n"
"v_exp_f32 v132, v132\n"
"v_mfma_f32_16x16x32_bf16 v[216:219], a[172:175], v[40:43], v[216:219]\n"
"v_exp_f32 v133, v133\n"
"v_mfma_f32_16x16x32_bf16 v[212:215], a[176:179], v[40:43], v[212:215]\n"
"s_waitcnt lgkmcnt(3)\n"
"ds_read_b128 v[40:43], v188 offset:7168\n"
"ds_read_b128 v[128:131], v179 offset:13504\n"
"v_mfma_f32_16x16x32_bf16 v[208:211], a[180:183], v[44:47], v[208:211]\n"
"s_barrier\n"
"v_mfma_f32_16x16x32_bf16 v[216:219], a[184:187], v[44:47], v[216:219]\n"
"s_waitcnt lgkmcnt(4)\n"
"v_exp_f32 v134, v134\n"
"v_mfma_f32_16x16x32_bf16 v[212:215], a[188:191], v[44:47], v[212:215]\n"
"s_waitcnt lgkmcnt(3)\n"
"ds_read_b128 v[44:47], v189 offset:7168\n"
"v_cvt_pk_bf16_f32 v92, v132, v134\n"
"v_mfma_f32_16x16x32_bf16 v[208:211], a[192:195], v[48:51], v[208:211]\n"
"s_waitcnt lgkmcnt(3)\n"
"v_exp_f32 v135, v135\n"
"v_mfma_f32_16x16x32_bf16 v[216:219], a[196:199], v[48:51], v[216:219]\n"
"s_add_u32 m0, s79, 130176\n"
"v_cvt_pk_bf16_f32 v93, v133, v135\n"
"v_bfi_b32 v80, v4, v80, v26\n"
"v_mfma_f32_16x16x32_bf16 v[212:215], a[200:203], v[48:51], v[212:215]\n"
"buffer_load_dwordx4 v192, s[16:19], s43 offen lds\n"
"ds_read_b128 v[48:51], v188 offset:8192\n"
"v_mfma_f32_16x16x32_bf16 v[208:211], a[204:207], v[52:55], v[208:211]\n"
"ds_read_b128 v[132:135], v179 offset:27008\n"
"v_bfi_b32 v81, v5, v81, v26\n"
"v_mfma_f32_16x16x32_bf16 v[216:219], a[208:211], v[52:55], v[216:219]\n"
"v_bfi_b32 v86, v6, v86, v26\n"
"v_bfi_b32 v87, v7, v87, v26\n"
"v_smfmac_f32_16x16x64_bf16 v[252:255], v[82:85], v[244:251], v66\n"
"s_waitcnt lgkmcnt(3)\n"
"ds_read_b64_tr_b16 v[244:245], v182 offset:27144\n"
"v_mfma_f32_16x16x32_bf16 v[212:215], a[212:215], v[52:55], v[212:215]\n"
"ds_read_b128 v[52:55], v189 offset:8192\n"
"ds_read_b64_tr_b16 v[246:247], v183 offset:27144\n"
"v_smfmac_f32_16x16x64_bf16 v[98:101], v[82:85], v[120:127], v66\n"
"ds_read_b64_tr_b16 v[248:249], v182 offset:27912\n"
"v_smfmac_f32_16x16x64_bf16 v[106:109], v[82:85], v[136:143], v66\n"
"ds_read_b64_tr_b16 v[250:251], v183 offset:27912\n"
"v_mfma_f32_16x16x32_bf16 v[82:85], v[172:175], v[82:85], 0\n"
"ds_read_b64_tr_b16 v[120:121], v182 offset:27080\n"
"v_cvt_pk_bf16_f32 v144, v208, v209\n"
"v_mfma_f32_16x16x32_bf16 v[228:231], a[0:3], v[32:35], 0\n"
"s_waitcnt lgkmcnt(8)\n"
"ds_read_b64_tr_b16 v[122:123], v183 offset:27080\n"
"v_cvt_pk_bf16_f32 v145, v210, v211\n"
"v_mfma_f32_16x16x32_bf16 v[204:207], a[4:7], v[32:35], 0\n"
"s_waitcnt lgkmcnt(7)\n"
"ds_read_b64_tr_b16 v[124:125], v182 offset:27848\n"
"v_cvt_pk_bf16_f32 v146, v216, v217\n"
"v_mfma_f32_16x16x32_bf16 v[220:223], a[8:11], v[32:35], 0\n"
"s_add_u32 m0, s79, 130176\n"
"ds_read_b64_tr_b16 v[126:127], v183 offset:27848\n"
"buffer_load_dwordx4 v192, s[16:19], s43 offen offset:1024 lds\n"
"v_mfma_f32_16x16x32_bf16 v[228:231], a[12:15], v[36:39], v[228:231]\n"
"v_cvt_pk_bf16_f32 v147, v218, v219\n"
"v_cvt_pk_bf16_f32 v158, v212, v213\n"
"v_smfmac_f32_16x16x64_bf16 v[252:255], v[90:93], v[112:119], v66\n"
"v_cvt_pk_bf16_f32 v159, v214, v215\n"
"v_mfma_f32_16x16x32_bf16 v[204:207], a[16:19], v[36:39], v[204:207]\n"
"ds_read_b128 v[136:139], v178 offset:13504\n"
"ds_read_b128 v[140:143], v178 offset:27008\n"
"v_mfma_f32_16x16x32_bf16 v[220:223], a[20:23], v[36:39], v[220:223]\n"
"v_subrev_u32 v182, s48, v182\n"
"v_subrev_u32 v183, s48, v183\n"
"v_mfma_f32_16x16x32_bf16 v[228:231], a[24:27], v[40:43], v[228:231]\n"
"s_waitcnt lgkmcnt(6)\n"
"ds_read_b128 v[112:115], v180\n"
"v_cvt_pk_bf16_f32 v252, v252, v253\n"
"v_smfmac_f32_16x16x64_bf16 v[98:101], v[90:93], v[244:251], v66\n"
"v_cvt_pk_bf16_f32 v253, v254, v255\n"
"v_mfma_f32_16x16x32_bf16 v[204:207], a[28:31], v[40:43], v[204:207]\n"
"ds_read_b128 v[244:247], v181\n"
"ds_read_b128 v[116:119], v180 offset:13504\n"
"v_mfma_f32_16x16x32_bf16 v[220:223], a[32:35], v[40:43], v[220:223]\n"
"s_add_u32 m0, s79, 130176\n"
"ds_read_b128 v[248:251], v181 offset:13504\n"
"buffer_load_dwordx4 v192, s[16:19], s43 offen offset:2048 lds\n"
"v_mfma_f32_16x16x32_bf16 v[228:231], a[36:39], v[44:47], v[228:231]\n"
"s_waitcnt lgkmcnt(6)\n"
"v_cvt_pk_bf16_f32 v98, v98, v99\n"
"v_cvt_pk_bf16_f32 v99, v100, v101\n"
"v_smfmac_f32_16x16x64_bf16 v[106:109], v[90:93], v[120:127], v66\n"
"s_add_u32 s43, s79, 18432\n"
"v_bfi_b32 v88, v0, v88, v26\n"
"v_mfma_f32_16x16x32_bf16 v[90:93], v[172:175], v[90:93], v[82:85]\n"
"s_waitcnt lgkmcnt(5)\n"
"v_bfi_b32 v89, v1, v89, v26\n"
"v_bfi_b32 v94, v2, v94, v26\n"
"v_mfma_f32_16x16x32_bf16 v[204:207], a[40:43], v[44:47], v[204:207]\n"
"s_waitcnt lgkmcnt(4)\n"
"ds_read_b64 v[82:83], v178 offset:13568\n"
"ds_read_b64 v[84:85], v179 offset:13568\n"
"v_mfma_f32_16x16x32_bf16 v[220:223], a[44:47], v[44:47], v[220:223]\n"
"s_waitcnt lgkmcnt(5)\n"
"ds_read_b64 v[92:93], v179 offset:27072\n"
"v_cvt_pk_bf16_f32 v106, v106, v107\n"
"v_mfma_f32_16x16x32_bf16 v[228:231], a[48:51], v[48:51], v[228:231]\n"
"v_rcp_f32 v171, v90\n"
"v_mfma_f32_16x16x32_bf16 v[204:207], a[52:55], v[48:51], v[204:207]\n"
"s_waitcnt lgkmcnt(5)\n"
"ds_read_b64 v[90:91], v178 offset:27072\n"
"v_cvt_pk_bf16_f32 v171, v171, v171\n"
"v_mfma_f32_16x16x32_bf16 v[220:223], a[56:59], v[48:51], v[220:223]\n"
"s_waitcnt lgkmcnt(5)\n"
"v_and_b32 v64, v171, v176\n"
"v_and_b32 v65, v171, v177\n"
"v_mfma_f32_16x16x32_bf16 v[228:231], a[60:63], v[52:55], v[228:231]\n"
"s_waitcnt lgkmcnt(4)\n"
"v_cvt_pk_bf16_f32 v107, v108, v109\n"
"v_bfi_b32 v95, v3, v95, v26\n"
"v_mfma_f32_16x16x16_bf16 v[252:255], v[252:253], v[64:65], 0\n"
"s_add_u32 m0, s34, 6144\n"
"ds_read_b64_tr_b16 v[120:121], v182 offset:13632\n"
"buffer_load_dwordx4 v190, s[8:11], s32 offen lds\n"
"v_mfma_f32_16x16x16_bf16 v[98:101], v[98:99], v[64:65], 0\n"
"v_cvt_pk_bf16_f32 v228, v228, v229\n"
"v_cvt_pk_bf16_f32 v229, v230, v231\n"
"v_mfma_f32_16x16x16_bf16 v[106:109], v[106:107], v[64:65], 0\n"
"ds_read_b64_tr_b16 v[122:123], v183 offset:13632\n"
"v_cvt_pk_bf16_f32 v252, v252, v253\n"
"v_mfma_f32_16x16x32_bf16 v[204:207], a[64:67], v[52:55], v[204:207]\n"
"v_cvt_pk_bf16_f32 v253, v254, v255\n"
"v_bfi_b32 v96, v0, v96, v26\n"
"v_mfma_f32_16x16x32_bf16 v[220:223], a[68:71], v[52:55], v[220:223]\n"
"v_cvt_pk_bf16_f32 v254, v98, v99\n"
"v_cvt_pk_bf16_f32 v255, v100, v101\n"
"v_mfma_f32_16x16x32_bf16 v[224:227], a[72:75], v[32:35], 0\n"
"buffer_store_dwordx4 v[252:255], v192, s[28:31], s37 offen sc1\n"
"v_mfma_f32_16x16x32_bf16 v[208:211], a[76:79], v[32:35], 0\n"
"ds_read_b64 v[98:99], v180 offset:64\n"
"ds_read_b64 v[100:101], v181 offset:64\n"
"v_mfma_f32_16x16x32_bf16 v[216:219], a[80:83], v[32:35], 0\n"
"s_add_u32 m0, s34, 9216\n"
"v_cvt_pk_bf16_f32 v106, v106, v107\n"
"v_cvt_pk_bf16_f32 v107, v108, v109\n"
"v_mfma_f32_16x16x32_bf16 v[136:139], v[136:139], v[148:151], 0\n"
"s_waitcnt lgkmcnt(7)\n"
"buffer_load_dwordx4 v191, s[8:11], s32 offen lds\n"
"ds_read_b64 v[108:109], v181 offset:13568\n"
"v_mfma_f32_16x16x32_bf16 v[128:131], v[128:131], v[148:151], 0\n"
"s_waitcnt lgkmcnt(7)\n"
"v_cvt_pk_bf16_f32 v230, v204, v205\n"
"v_cvt_pk_bf16_f32 v231, v206, v207\n"
"v_mfma_f32_16x16x32_bf16 v[224:227], a[84:87], v[36:39], v[224:227]\n"
"s_add_u32 s32, s32, s33\n"
"ds_write_b128 v187, v[228:231]\n"
"v_mfma_f32_16x16x32_bf16 v[136:139], v[80:83], v[160:163], v[136:139]\n"
"buffer_store_dwordx2 v[106:107], v193, s[28:31], s37 offen offset:1024 sc1\n"
"v_mfma_f32_16x16x32_bf16 v[208:211], a[88:91], v[36:39], v[208:211]\n"
"s_add_u32 s37, s37, 1572864\n"
"ds_read_b64 v[106:107], v180 offset:13568\n"
"v_cvt_pk_bf16_f32 v220, v220, v221\n"
"v_mfma_f32_16x16x32_bf16 v[128:131], v[84:87], v[162:165], v[128:131]\n"
"v_cvt_pk_bf16_f32 v221, v222, v223\n"
"v_bfi_b32 v97, v1, v97, v26\n"
"v_mfma_f32_16x16x32_bf16 v[216:219], a[92:95], v[36:39], v[216:219]\n"
"ds_read_b64_tr_b16 v[124:125], v182 offset:14400\n"
"buffer_load_dword v237, v67, s[8:11], s78 offen\n"
"v_mfma_f32_16x16x32_bf16 v[140:143], v[140:143], v[148:151], 0\n"
"s_waitcnt lgkmcnt(8)\n"
"ds_read_b64_tr_b16 v[126:127], v183 offset:14400\n"
"v_bfi_b32 v102, v2, v102, v26\n"
"v_mfma_f32_16x16x32_bf16 v[224:227], a[96:99], v[40:43], v[224:227]\n"
"s_add_u32 s78, s78, s33\n"
"v_bfi_b32 v103, v3, v103, v26\n"
"v_bfi_b32 v104, v8, v104, v26\n"
"v_mfma_f32_16x16x32_bf16 v[132:135], v[132:135], v[148:151], 0\n"
"s_add_u32 m0, s34, 7168\n"
"v_bfi_b32 v105, v9, v105, v26\n"
"v_bfi_b32 v110, v10, v110, v26\n"
"v_mfma_f32_16x16x32_bf16 v[208:211], a[100:103], v[40:43], v[208:211]\n"
"v_bfi_b32 v111, v11, v111, v26\n"
"v_max_f32 v136, v136, v128\n"
"v_mfma_f32_16x16x32_bf16 v[140:143], v[88:91], v[160:163], v[140:143]\n"
"buffer_load_dwordx4 v190, s[8:11], s0 offen lds\n"
"v_max_f32 v137, v137, v129\n"
"v_mfma_f32_16x16x32_bf16 v[132:135], v[92:95], v[162:165], v[132:135]\n"
"ds_read_b64_tr_b16 v[128:129], v182 offset:13640\n"
"v_max_f32 v138, v138, v130\n"
"v_mfma_f32_16x16x32_bf16 v[216:219], a[104:107], v[40:43], v[216:219]\n"
"v_max_f32 v139, v139, v131\n"
"v_max3_f32 v170, v136, v137, v138\n"
"v_mfma_f32_16x16x32_bf16 v[224:227], a[108:111], v[44:47], v[224:227]\n"
"ds_read_b64_tr_b16 v[130:131], v183 offset:13640\n"
"buffer_load_dword v237, v67, s[8:11], s78 offen\n"
"v_mfma_f32_16x16x32_bf16 v[112:115], v[112:115], v[148:151], 0\n"
"s_waitcnt lgkmcnt(8)\n"
"v_max_f32 v140, v140, v132\n"
"v_max_f32 v141, v141, v133\n"
"v_mfma_f32_16x16x32_bf16 v[208:211], a[112:115], v[44:47], v[208:211]\n"
"s_add_u32 s78, s78, s33\n"
"ds_read_b64_tr_b16 v[132:133], v182 offset:14408\n"
"v_max_f32 v142, v142, v134\n"
"v_mfma_f32_16x16x32_bf16 v[244:247], v[244:247], v[148:151], 0\n"
"s_waitcnt lgkmcnt(8)\n"
"v_max_f32 v143, v143, v135\n"
"v_max3_f32 v170, v170, v139, v140\n"
"v_mfma_f32_16x16x32_bf16 v[216:219], a[116:119], v[44:47], v[216:219]\n"
"ds_read_b64_tr_b16 v[134:135], v183 offset:14408\n"
"v_max3_f32 v170, v170, v141, v142\n"
"v_mfma_f32_16x16x32_bf16 v[112:115], v[96:99], v[160:163], v[112:115]\n"
"s_add_u32 m0, s34, 10240\n"
"v_mov_b64 v[252:253], 0\n"
"v_mov_b64 v[254:255], 0\n"
"v_mfma_f32_16x16x32_bf16 v[244:247], v[100:103], v[162:165], v[244:247]\n"
"buffer_load_dwordx4 v191, s[8:11], s0 offen lds\n"
"v_mov_b64 v[98:99], 0\n"
"v_mfma_f32_16x16x32_bf16 v[224:227], a[120:123], v[48:51], v[224:227]\n"
"s_add_u32 s0, s0, s33\n"
"v_mov_b64 v[100:101], 0\n"
"v_mfma_f32_16x16x32_bf16 v[208:211], a[124:127], v[48:51], v[208:211]\n"
"ds_read_b64 v[80:81], v20 offset:272\n"
"ds_read_b64 v[86:87], v20 offset:336\n"
"v_mfma_f32_16x16x32_bf16 v[116:119], v[116:119], v[148:151], 0\n"
"s_waitcnt lgkmcnt(8)\n"
"v_max_f32 v112, v112, v244\n"
"v_max_f32 v113, v113, v245\n"
"v_mfma_f32_16x16x32_bf16 v[216:219], a[128:131], v[48:51], v[216:219]\n"
"ds_read_b64_tr_b16 v[244:245], v182 offset:13576\n"
"v_max_f32 v114, v114, v246\n"
"v_mfma_f32_16x16x32_bf16 v[248:251], v[248:251], v[148:151], 0\n"
"v_max_f32 v115, v115, v247\n"
"v_max3_f32 v170, v170, v143, v112\n"
"v_mfma_f32_16x16x32_bf16 v[116:119], v[104:107], v[160:163], v[116:119]\n"
"ds_read_b64_tr_b16 v[246:247], v183 offset:13576\n"
"v_max3_f32 v170, v170, v113, v114\n"
"v_mfma_f32_16x16x32_bf16 v[224:227], a[132:135], v[52:55], v[224:227]\n"
"v_mov_b64 v[106:107], 0\n"
"s_add_u32 m0, s34, 8192\n"
"v_mfma_f32_16x16x32_bf16 v[248:251], v[108:111], v[162:165], v[248:251]\n"
"buffer_load_dwordx4 v190, s[8:11], s1 offen lds\n"
"v_mov_b64 v[108:109], 0\n"
"v_mfma_f32_16x16x32_bf16 v[208:211], a[136:139], v[52:55], v[208:211]\n"
"s_waitcnt lgkmcnt(3)\n"
"ds_read_b64 v[104:105], v20 offset:1088\n"
"v_cvt_pk_bf16_f32 v224, v224, v225\n"
"v_mfma_f32_16x16x32_bf16 v[216:219], a[140:143], v[52:55], v[216:219]\n"
"s_waitcnt lgkmcnt(3)\n"
"v_cvt_pk_bf16_f32 v225, v226, v227\n"
"v_max_f32 v116, v116, v248\n"
"v_mfma_f32_16x16x32_bf16 v[212:215], a[144:147], v[32:35], 0\n"
"v_cvt_pk_bf16_f32 v226, v208, v209\n"
"v_cvt_pk_bf16_f32 v227, v210, v211\n"
"v_mfma_f32_16x16x32_bf16 v[204:207], a[148:151], v[32:35], 0\n"
"ds_write_b128 v187, v[224:227] offset:128\n"
"v_mfma_f32_16x16x32_bf16 v[228:231], a[152:155], v[32:35], 0\n"
"s_waitcnt vmcnt(17)\n"
"v_cvt_pk_bf16_f32 v222, v216, v217\n"
"v_cvt_pk_bf16_f32 v223, v218, v219\n"
"v_mfma_f32_16x16x32_bf16 v[212:215], a[156:159], v[36:39], v[212:215]\n"
"v_max_f32 v117, v117, v249\n"
"v_max_f32 v118, v118, v250\n"
"v_mfma_f32_16x16x32_bf16 v[204:207], a[160:163], v[36:39], v[204:207]\n"
"s_waitcnt lgkmcnt(1)\n"
"ds_read_b128 v[32:35], v188\n"
"ds_write_b128 v187, v[220:223] offset:64\n"
"v_mfma_f32_16x16x32_bf16 v[228:231], a[164:167], v[36:39], v[228:231]\n"
"s_add_u32 m0, s34, 11264\n"
"v_max_f32 v119, v119, v251\n"
"v_max3_f32 v170, v170, v115, v116\n"
"v_mfma_f32_16x16x32_bf16 v[212:215], a[168:171], v[40:43], v[212:215]\n"
"buffer_load_dwordx4 v191, s[8:11], s1 offen lds\n"
"ds_read_b128 v[36:39], v189\n"
"v_mfma_f32_16x16x32_bf16 v[204:207], a[172:175], v[40:43], v[204:207]\n"
"s_add_u32 s1, s1, s33\n"
"ds_read_b64_tr_b16 v[248:249], v182 offset:14344\n"
"ds_read_b64_tr_b16 v[250:251], v183 offset:14344\n"
"v_mfma_f32_16x16x32_bf16 v[228:231], a[176:179], v[40:43], v[228:231]\n"
"s_waitcnt vmcnt(16)\n"
"ds_read_b128 v[40:43], v188 offset:1024\n"
"v_max3_f32 v170, v170, v117, v118\n"
"v_mfma_f32_16x16x32_bf16 v[212:215], a[180:183], v[44:47], v[212:215]\n"
"v_max_f32 v171, v170, v119\n"
"v_max_f32 v170, v170, v119\n"
"v_mfma_f32_16x16x32_bf16 v[204:207], a[184:187], v[44:47], v[204:207]\n"
"s_nop 0\n"
"v_permlane16_swap_b32 v170, v171\n"
"v_mfma_f32_16x16x32_bf16 v[228:231], a[188:191], v[44:47], v[228:231]\n"
"s_waitcnt lgkmcnt(5)\n"
"ds_read_b128 v[44:47], v189 offset:1024\n"
"v_max_f32 v170, v170, v171\n"
"v_mfma_f32_16x16x32_bf16 v[212:215], a[192:195], v[48:51], v[212:215]\n"
"v_mov_b32 v171, v170\n"
"v_bfi_b32 v80, v12, v80, v26\n"
"v_mfma_f32_16x16x32_bf16 v[204:207], a[196:199], v[48:51], v[204:207]\n"
"v_permlane32_swap_b32 v170, v171\n"
"v_mfma_f32_16x16x32_bf16 v[228:231], a[200:203], v[48:51], v[228:231]\n"
"s_waitcnt vmcnt(13) lgkmcnt(4)\n"
"ds_read_b128 v[48:51], v188 offset:2048\n"
"v_max_f32 v170, v170, v171\n"
"v_mfma_f32_16x16x32_bf16 v[212:215], a[204:207], v[52:55], v[212:215]\n"
"s_add_u32 m0, s34, 0\n"
"v_mul_f32 v170, 0xbe553b95, v170\n"
"v_fmamk_f32 v136, v136, 0x3e553b95, v170\n"
"v_mfma_f32_16x16x32_bf16 v[204:207], a[208:211], v[52:55], v[204:207]\n"
"s_waitcnt vmcnt(10) lgkmcnt(3)\n"
"buffer_load_dwordx4 v190, s[8:11], s32 offen lds\n"
"v_fmamk_f32 v137, v137, 0x3e553b95, v170\n"
"v_mfma_f32_16x16x32_bf16 v[228:231], a[212:215], v[52:55], v[228:231]\n"
"s_barrier\n"
"v_mfma_f32_16x16x32_bf16 v[208:211], a[0:3], v[32:35], 0\n"
"ds_read_b128 v[52:55], v189 offset:2048\n"
"v_fmamk_f32 v138, v138, 0x3e553b95, v170\n"
"v_mfma_f32_16x16x32_bf16 v[216:219], a[4:7], v[32:35], 0\n"
"buffer_load_dword v201, off, s[28:31], s40 offset:128 sc1\n"
"v_fmamk_f32 v139, v139, 0x3e553b95, v170\n"
"v_mfma_f32_16x16x32_bf16 v[220:223], a[8:11], v[32:35], 0\n"
".Lic1%=:\n"
"s_cbranch_execz .Lib0%=\n"
"v_exp_f32 v136, v136\n"
"v_mfma_f32_16x16x32_bf16 v[208:211], a[12:15], v[36:39], v[208:211]\n"
"s_waitcnt lgkmcnt(3)\n"
"v_exp_f32 v137, v137\n"
"v_mfma_f32_16x16x32_bf16 v[216:219], a[16:19], v[36:39], v[216:219]\n"
"v_exp_f32 v138, v138\n"
"v_mfma_f32_16x16x32_bf16 v[220:223], a[20:23], v[36:39], v[220:223]\n"
"v_exp_f32 v139, v139\n"
"v_mfma_f32_16x16x32_bf16 v[208:211], a[24:27], v[40:43], v[208:211]\n"
"s_waitcnt lgkmcnt(2)\n"
"v_cvt_pk_bf16_f32 v82, v136, v138\n"
"v_cvt_pk_bf16_f32 v83, v137, v139\n"
"v_mfma_f32_16x16x32_bf16 v[216:219], a[28:31], v[40:43], v[216:219]\n"
"s_add_u32 m0, s79, 142464\n"
"ds_read_b64_tr_b16 v[136:137], v182 offset:40640\n"
"buffer_load_dwordx4 v192, s[16:19], s43 offen lds\n"
"v_mfma_f32_16x16x32_bf16 v[220:223], a[32:35], v[40:43], v[220:223]\n"
"ds_read_b64_tr_b16 v[138:139], v183 offset:40640\n"
"v_fmamk_f32 v140, v140, 0x3e553b95, v170\n"
"v_mfma_f32_16x16x32_bf16 v[208:211], a[36:39], v[44:47], v[208:211]\n"
"s_waitcnt lgkmcnt(3)\n"
"v_fmamk_f32 v141, v141, 0x3e553b95, v170\n"
"v_fmamk_f32 v142, v142, 0x3e553b95, v170\n"
"v_mfma_f32_16x16x32_bf16 v[216:219], a[40:43], v[44:47], v[216:219]\n"
"v_fmamk_f32 v143, v143, 0x3e553b95, v170\n"
"v_cvt_pk_bf16_f32 v148, v212, v213\n"
"v_mfma_f32_16x16x32_bf16 v[220:223], a[44:47], v[44:47], v[220:223]\n"
"v_exp_f32 v140, v140\n"
"v_mfma_f32_16x16x32_bf16 v[208:211], a[48:51], v[48:51], v[208:211]\n"
"s_waitcnt lgkmcnt(2)\n"
"v_exp_f32 v141, v141\n"
"v_mfma_f32_16x16x32_bf16 v[216:219], a[52:55], v[48:51], v[216:219]\n"
"v_exp_f32 v142, v142\n"
"v_mfma_f32_16x16x32_bf16 v[220:223], a[56:59], v[48:51], v[220:223]\n"
"v_exp_f32 v143, v143\n"
"v_mfma_f32_16x16x32_bf16 v[208:211], a[60:63], v[52:55], v[208:211]\n"
"s_add_u32 m0, s79, 142464\n"
"v_cvt_pk_bf16_f32 v84, v140, v142\n"
"v_cvt_pk_bf16_f32 v85, v141, v143\n"
"v_mfma_f32_16x16x32_bf16 v[216:219], a[64:67], v[52:55], v[216:219]\n"
"buffer_load_dwordx4 v192, s[16:19], s43 offen offset:1024 lds\n"
"ds_read_b64_tr_b16 v[140:141], v182 offset:41408\n"
"v_smfmac_f32_16x16x64_bf16 v[252:255], v[82:85], v[120:127], v66\n"
"ds_read_b64_tr_b16 v[142:143], v183 offset:41408\n"
"v_smfmac_f32_16x16x64_bf16 v[98:101], v[82:85], v[128:135], v66\n"
"ds_read_b64_tr_b16 v[120:121], v182 offset:40648\n"
"v_smfmac_f32_16x16x64_bf16 v[106:109], v[82:85], v[244:251], v66\n"
"ds_read_b64_tr_b16 v[122:123], v183 offset:40648\n"
"v_mfma_f32_16x16x32_bf16 v[82:85], v[172:175], v[82:85], 0\n"
"ds_read_b64_tr_b16 v[124:125], v182 offset:41416\n"
"ds_read_b64_tr_b16 v[126:127], v183 offset:41416\n"
"v_mfma_f32_16x16x32_bf16 v[220:223], a[68:71], v[52:55], v[220:223]\n"
"ds_read_b64_tr_b16 v[128:129], v182 offset:40584\n"
"v_cvt_pk_bf16_f32 v149, v214, v215\n"
"v_mfma_f32_16x16x32_bf16 v[224:227], a[72:75], v[32:35], 0\n"
"ds_read_b64_tr_b16 v[130:131], v183 offset:40584\n"
"v_cvt_pk_bf16_f32 v150, v204, v205\n"
"v_mfma_f32_16x16x32_bf16 v[212:215], a[76:79], v[32:35], 0\n"
"ds_read_b64_tr_b16 v[132:133], v182 offset:41352\n"
"v_cvt_pk_bf16_f32 v151, v206, v207\n"
"v_mfma_f32_16x16x32_bf16 v[204:207], a[80:83], v[32:35], 0\n"
"s_add_u32 m0, s79, 142464\n"
"ds_read_b64_tr_b16 v[134:135], v183 offset:41352\n"
"buffer_load_dwordx4 v192, s[16:19], s43 offen offset:2048 lds\n"
"v_mfma_f32_16x16x32_bf16 v[224:227], a[84:87], v[36:39], v[224:227]\n"
"s_add_u32 s43, s79, 36864\n"
"v_cvt_pk_bf16_f32 v162, v228, v229\n"
"v_cvt_pk_bf16_f32 v163, v230, v231\n"
"v_mfma_f32_16x16x32_bf16 v[212:215], a[88:91], v[36:39], v[212:215]\n"
"v_fmamk_f32 v112, v112, 0x3e553b95, v170\n"
"v_fmamk_f32 v113, v113, 0x3e553b95, v170\n"
"v_mfma_f32_16x16x32_bf16 v[204:207], a[92:95], v[36:39], v[204:207]\n"
"v_fmamk_f32 v114, v114, 0x3e553b95, v170\n"
"v_fmamk_f32 v115, v115, 0x3e553b95, v170\n"
"v_mfma_f32_16x16x32_bf16 v[224:227], a[96:99], v[40:43], v[224:227]\n"
"ds_read_b64 v[110:111], v20 offset:1152\n"
"ds_read_b128 v[244:247], v178 offset:27008\n"
"v_mfma_f32_16x16x32_bf16 v[212:215], a[100:103], v[40:43], v[212:215]\n"
"v_exp_f32 v112, v112\n"
"v_mfma_f32_16x16x32_bf16 v[204:207], a[104:107], v[40:43], v[204:207]\n"
"v_exp_f32 v113, v113\n"
"v_mfma_f32_16x16x32_bf16 v[224:227], a[108:111], v[44:47], v[224:227]\n"
"v_exp_f32 v114, v114\n"
"v_mfma_f32_16x16x32_bf16 v[212:215], a[112:115], v[44:47], v[212:215]\n"
"s_add_u32 m0, s34, 3072\n"
"v_exp_f32 v115, v115\n"
"v_mfma_f32_16x16x32_bf16 v[204:207], a[116:119], v[44:47], v[204:207]\n"
"buffer_load_dwordx4 v191, s[8:11], s32 offen lds\n"
"v_cvt_pk_bf16_f32 v90, v112, v114\n"
"v_mfma_f32_16x16x32_bf16 v[224:227], a[120:123], v[48:51], v[224:227]\n"
"s_add_u32 s32, s32, s33\n"
"v_cvt_pk_bf16_f32 v91, v113, v115\n"
"v_fmamk_f32 v116, v116, 0x3e553b95, v170\n"
"v_mfma_f32_16x16x32_bf16 v[212:215], a[124:127], v[48:51], v[212:215]\n"
"s_waitcnt lgkmcnt(1)\n"
"ds_read_b128 v[112:115], v179 offset:27008\n"
"v_fmamk_f32 v117, v117, 0x3e553b95, v170\n"
"v_mfma_f32_16x16x32_bf16 v[204:207], a[128:131], v[48:51], v[204:207]\n"
"s_waitcnt lgkmcnt(1)\n"
"v_fmamk_f32 v118, v118, 0x3e553b95, v170\n"
"v_fmamk_f32 v119, v119, 0x3e553b95, v170\n"
"v_mfma_f32_16x16x32_bf16 v[244:247], v[244:247], v[152:155], 0\n"
"v_exp_f32 v116, v116\n"
"v_mfma_f32_16x16x32_bf16 v[224:227], a[132:135], v[52:55], v[224:227]\n"
"v_exp_f32 v117, v117\n"
"v_mfma_f32_16x16x32_bf16 v[212:215], a[136:139], v[52:55], v[212:215]\n"
"ds_read_b128 v[248:251], v180\n"
"v_exp_f32 v118, v118\n"
"v_mfma_f32_16x16x32_bf16 v[204:207], a[140:143], v[52:55], v[204:207]\n"
"s_add_u32 m0, s34, 1024\n"
"v_exp_f32 v119, v119\n"
"v_mfma_f32_16x16x32_bf16 v[228:231], a[144:147], v[32:35], 0\n"
"buffer_load_dwordx4 v190, s[8:11], s0 offen lds\n"
"v_cvt_pk_bf16_f32 v92, v116, v118\n"
"v_mfma_f32_16x16x32_bf16 v[228:231], a[156:159], v[36:39], v[228:231]\n"
"s_waitcnt lgkmcnt(1)\n"
"v_cvt_pk_bf16_f32 v93, v117, v119\n"
"v_bfi_b32 v81, v13, v81, v26\n"
"v_mfma_f32_16x16x32_bf16 v[228:231], a[168:171], v[40:43], v[228:231]\n"
"ds_read_b128 v[116:119], v181\n"
"ds_read_b64_tr_b16 a[216:217], v236\n"
"v_smfmac_f32_16x16x64_bf16 v[252:255], v[90:93], v[136:143], v66\n"
"v_cvt_pk_bf16_f32 v208, v208, v209\n"
"v_smfmac_f32_16x16x64_bf16 v[98:101], v[90:93], v[120:127], v66\n"
"ds_read_b128 v[136:139], v180 offset:13504\n"
"v_smfmac_f32_16x16x64_bf16 v[106:109], v[90:93], v[128:135], v66\n"
"s_waitcnt lgkmcnt(3)\n"
"ds_read_b128 v[120:123], v181 offset:13504\n"
"v_mfma_f32_16x16x32_bf16 v[90:93], v[172:175], v[90:93], v[82:85]\n"
"ds_read_b128 v[140:143], v180 offset:27008\n"
"v_cvt_pk_bf16_f32 v252, v252, v253\n"
"v_mfma_f32_16x16x32_bf16 v[112:115], v[112:115], v[152:155], 0\n"
"ds_read_b64 v[82:83], v178 offset:27072\n"
"v_cvt_pk_bf16_f32 v253, v254, v255\n"
"v_mfma_f32_16x16x32_bf16 v[248:251], v[248:251], v[152:155], 0\n"
"s_add_u32 m0, s34, 4096\n"
"ds_read_b64 v[84:85], v179 offset:27072\n"
"buffer_load_dwordx4 v191, s[8:11], s0 offen lds\n"
"v_mfma_f32_16x16x32_bf16 v[228:231], a[180:183], v[44:47], v[228:231]\n"
"s_add_u32 s0, s0, s33\n"
"ds_read_b64 v[92:93], v181 offset:64\n"
"v_cvt_pk_bf16_f32 v98, v98, v99\n"
"v_mfma_f32_16x16x32_bf16 v[228:231], a[192:195], v[48:51], v[228:231]\n"
"s_waitcnt lgkmcnt(7)\n"
"v_rcp_f32 v171, v90\n"
"v_mfma_f32_16x16x32_bf16 v[116:119], v[116:119], v[152:155], 0\n"
"ds_read_b64 v[90:91], v180 offset:64\n"
"v_cvt_pk_bf16_f32 v171, v171, v171\n"
"v_mfma_f32_16x16x32_bf16 v[228:231], a[204:207], v[52:55], v[228:231]\n"
"v_and_b32 v64, v171, v176\n"
"v_and_b32 v65, v171, v177\n"
"v_cvt_pk_bf16_f32 v99, v100, v101\n"
"v_cvt_pk_bf16_f32 v106, v106, v107\n"
"v_mfma_f32_16x16x16_bf16 v[252:255], v[252:253], v[64:65], 0\n"
"s_waitcnt lgkmcnt(5)\n"
"ds_read_b128 v[124:127], v181 offset:27008\n"
"v_cvt_pk_bf16_f32 v107, v108, v109\n"
"v_mfma_f32_16x16x16_bf16 v[98:101], v[98:99], v[64:65], 0\n"
"s_waitcnt lgkmcnt(5)\n"
"ds_read_b64_tr_b16 v[128:129], v182 offset:27136\n"
"ds_read_b64_tr_b16 v[130:131], v183 offset:27136\n"
"v_mfma_f32_16x16x16_bf16 v[106:109], v[106:107], v[64:65], 0\n"
"s_waitcnt lgkmcnt(6)\n"
"v_cvt_pk_bf16_f32 v252, v252, v253\n"
"v_cvt_pk_bf16_f32 v253, v254, v255\n"
"v_mfma_f32_16x16x32_bf16 v[244:247], v[80:83], v[164:167], v[244:247]\n"
"s_add_u32 m0, s34, 2048\n"
"v_cvt_pk_bf16_f32 v254, v98, v99\n"
"v_cvt_pk_bf16_f32 v255, v100, v101\n"
"v_mfma_f32_16x16x32_bf16 v[136:139], v[136:139], v[152:155], 0\n"
"s_waitcnt lgkmcnt(4)\n"
"buffer_load_dwordx4 v190, s[8:11], s1 offen lds\n"
"ds_read_b64 v[98:99], v180 offset:13568\n"
"v_mfma_f32_16x16x32_bf16 v[116:119], v[92:95], v[166:169], v[116:119]\n"
"s_waitcnt lgkmcnt(4)\n"
"ds_read_b64 v[100:101], v181 offset:13568\n"
"v_cvt_pk_bf16_f32 v106, v106, v107\n"
"v_mfma_f32_16x16x32_bf16 v[248:251], v[88:91], v[164:167], v[248:251]\n"
"v_cvt_pk_bf16_f32 v107, v108, v109\n"
"v_bfi_b32 v86, v14, v86, v26\n"
"v_mfma_f32_16x16x32_bf16 v[120:123], v[120:123], v[152:155], 0\n"
"buffer_store_dwordx4 v[252:255], v192, s[28:31], s37 offen sc1\n"
"v_mfma_f32_16x16x32_bf16 v[140:143], v[140:143], v[152:155], 0\n"
"s_waitcnt lgkmcnt(4)\n"
"ds_read_b64 v[108:109], v181 offset:27072\n"
"ds_read_b64_tr_b16 v[132:133], v182 offset:27904\n"
"v_mfma_f32_16x16x32_bf16 v[124:127], v[124:127], v[152:155], 0\n"
"v_cvt_pk_bf16_f32 v209, v210, v211\n"
"v_bfi_b32 v87, v15, v87, v26\n"
"v_cvt_pk_bf16_f32 v210, v216, v217\n"
"v_bfi_b32 v104, v16, v104, v26\n"
"v_mfma_f32_16x16x32_bf16 v[112:115], v[84:87], v[166:169], v[112:115]\n"
"s_add_u32 m0, s34, 5120\n"
"buffer_store_dwordx2 v[106:107], v193, s[28:31], s37 offen offset:1024 sc1\n"
"ds_read_b64 v[106:107], v180 offset:27072\n"
"ds_read_b64_tr_b16 v[134:135], v183 offset:27904\n"
"v_cvt_pk_bf16_f32 v211, v218, v219\n"
"v_mfma_f32_16x16x32_bf16 v[216:219], a[148:151], v[32:35], 0\n"
"s_waitcnt lgkmcnt(4)\n"
"ds_write_b128 v186, v[208:211] offset:6752\n"
"v_mfma_f32_16x16x32_bf16 v[136:139], v[96:99], v[164:167], v[136:139]\n"
"s_add_u32 s37, s37, 1572864\n"
"buffer_load_dwordx4 v191, s[8:11], s1 offen lds\n"
"v_bfi_b32 v105, v17, v105, v26\n"
"v_mfma_f32_16x16x32_bf16 v[120:123], v[100:103], v[166:169], v[120:123]\n"
"s_add_u32 s1, s1, s33\n"
"v_cvt_pk_bf16_f32 v220, v220, v221\n"
"v_bfi_b32 v110, v18, v110, v26\n"
"v_mfma_f32_16x16x32_bf16 v[208:211], a[152:155], v[32:35], 0\n"
"s_waitcnt vmcnt(19)\n"
"ds_read_b128 v[32:35], v188 offset:6144\n"
"v_cvt_pk_bf16_f32 v221, v222, v223\n"
"v_mfma_f32_16x16x32_bf16 v[216:219], a[160:163], v[36:39], v[216:219]\n"
"v_bfi_b32 v111, v19, v111, v26\n"
"v_max_f32 v244, v244, v112\n"
"v_mfma_f32_16x16x32_bf16 v[208:211], a[164:167], v[36:39], v[208:211]\n"
"s_waitcnt lgkmcnt(5)\n"
"ds_read_b128 v[36:39], v189 offset:6144\n"
"ds_read_b64_tr_b16 a[218:219], v236 offset:6144\n"
"v_mfma_f32_16x16x32_bf16 v[124:127], v[108:111], v[166:169], v[124:127]\n"
"s_waitcnt lgkmcnt(5)\n"
"buffer_load_dword v237, v67, s[8:11], s78 offen\n"
"v_max_f32 v245, v245, v113\n"
"v_mfma_f32_16x16x32_bf16 v[140:143], v[104:107], v[164:167], v[140:143]\n"
"s_add_u32 s78, s78, s33\n"
"ds_read_b64_tr_b16 v[112:113], v182 offset:27144\n"
"v_max_f32 v246, v246, v114\n"
"v_mfma_f32_16x16x32_bf16 v[216:219], a[172:175], v[40:43], v[216:219]\n"
"ds_read_b64_tr_b16 a[220:221], v236 offset:8\n"
"v_max_f32 v247, v247, v115\n"
"v_mfma_f32_16x16x32_bf16 v[208:211], a[176:179], v[40:43], v[208:211]\n"
"s_waitcnt vmcnt(15)\n"
"v_max3_f32 v170, v244, v245, v246\n"
"v_max_f32 v248, v248, v116\n"
"v_mfma_f32_16x16x32_bf16 v[216:219], a[184:187], v[44:47], v[216:219]\n"
"ds_read_b128 v[40:43], v188 offset:7168\n"
"v_max_f32 v249, v249, v117\n"
"v_mfma_f32_16x16x32_bf16 v[208:211], a[188:191], v[44:47], v[208:211]\n"
"ds_read_b128 v[44:47], v189 offset:7168\n"
"v_max_f32 v250, v250, v118\n"
"v_mfma_f32_16x16x32_bf16 v[216:219], a[196:199], v[48:51], v[216:219]\n"
"ds_read_b64_tr_b16 v[114:115], v183 offset:27144\n"
"v_max_f32 v251, v251, v119\n"
"v_mfma_f32_16x16x32_bf16 v[208:211], a[200:203], v[48:51], v[208:211]\n"
"s_waitcnt lgkmcnt(6)\n"
"ds_read_b64_tr_b16 v[116:117], v182 offset:27912\n"
"v_max3_f32 v170, v170, v247, v248\n"
"v_mfma_f32_16x16x32_bf16 v[216:219], a[208:211], v[52:55], v[216:219]\n"
"s_add_u32 m0, s34, 6144\n"
"ds_read_b64_tr_b16 v[118:119], v183 offset:27912\n"
"buffer_load_dwordx4 v190, s[8:11], s32 offen lds\n"
"v_mfma_f32_16x16x32_bf16 v[208:211], a[212:215], v[52:55], v[208:211]\n"
"v_max3_f32 v170, v170, v249, v250\n"
"v_cvt_pk_bf16_f32 v224, v224, v225\n"
"v_cvt_pk_bf16_f32 v225, v226, v227\n"
"v_cvt_pk_bf16_f32 v226, v212, v213\n"
"v_max_f32 v136, v136, v120\n"
"v_cvt_pk_bf16_f32 v227, v214, v215\n"
"v_mfma_f32_16x16x32_bf16 v[212:215], a[0:3], v[32:35], 0\n"
"ds_write_b128 v186, v[224:227] offset:6880\n"
"v_mfma_f32_16x16x32_bf16 v[212:215], a[12:15], v[36:39], v[212:215]\n"
"s_waitcnt vmcnt(14) lgkmcnt(5)\n"
"v_max_f32 v137, v137, v121\n"
"v_cvt_pk_bf16_f32 v222, v204, v205\n"
"v_mfma_f32_16x16x32_bf16 v[212:215], a[24:27], v[40:43], v[212:215]\n"
"s_waitcnt lgkmcnt(4)\n"
"v_max_f32 v138, v138, v122\n"
"v_max_f32 v139, v139, v123\n"
"v_mfma_f32_16x16x32_bf16 v[212:215], a[36:39], v[44:47], v[212:215]\n"
"ds_read_b128 v[48:51], v188 offset:8192\n"
"ds_read_b128 v[52:55], v189 offset:8192\n"
"v_mfma_f32_16x16x32_bf16 v[224:227], a[72:75], v[32:35], 0\n"
"s_add_u32 m0, s34, 9216\n"
"ds_read_b64_tr_b16 v[120:121], v182 offset:27080\n"
"buffer_load_dwordx4 v191, s[8:11], s32 offen lds\n"
"v_mfma_f32_16x16x32_bf16 v[224:227], a[84:87], v[36:39], v[224:227]\n"
"s_add_u32 s32, s32, s33\n"
"ds_read_b64_tr_b16 v[122:123], v183 offset:27080\n"
"v_cvt_pk_bf16_f32 v223, v206, v207\n"
"v_mfma_f32_16x16x32_bf16 v[204:207], a[4:7], v[32:35], 0\n"
"ds_write_b128 v186, v[220:223] offset:6816\n"
"v_mfma_f32_16x16x32_bf16 v[220:223], a[8:11], v[32:35], 0\n"
"v_max3_f32 v170, v170, v251, v136\n"
"v_max3_f32 v170, v170, v137, v138\n"
"v_mfma_f32_16x16x32_bf16 v[204:207], a[16:19], v[36:39], v[204:207]\n"
"v_max_f32 v140, v140, v124\n"
"v_max_f32 v141, v141, v125\n"
"v_mfma_f32_16x16x32_bf16 v[220:223], a[20:23], v[36:39], v[220:223]\n"
"ds_read_b64_tr_b16 v[124:125], v182 offset:27848\n"
"ds_read_b64_tr_b16 a[222:223], v236 offset:6152\n"
"v_mfma_f32_16x16x32_bf16 v[204:207], a[28:31], v[40:43], v[204:207]\n"
"s_waitcnt vmcnt(13) lgkmcnt(6)\n"
"v_max_f32 v142, v142, v126\n"
"v_max_f32 v143, v143, v127\n"
"v_mfma_f32_16x16x32_bf16 v[220:223], a[32:35], v[40:43], v[220:223]\n"
"s_waitcnt lgkmcnt(5)\n"
"v_readfirstlane_b32 s6, v201\n"
"v_max3_f32 v170, v170, v139, v140\n"
"v_mfma_f32_16x16x32_bf16 v[204:207], a[40:43], v[44:47], v[204:207]\n"
"s_and_b32 s6, s6, 0xffffff00\n"
"ds_read_b64_tr_b16 v[126:127], v183 offset:27848\n"
"ds_read_b64_tr_b16 a[224:225], v235\n"
"v_mfma_f32_16x16x32_bf16 v[220:223], a[44:47], v[44:47], v[220:223]\n"
"s_add_u32 m0, s34, 7168\n"
"v_max3_f32 v170, v170, v141, v142\n"
"v_mov_b64 v[252:253], 0\n"
"v_mfma_f32_16x16x32_bf16 v[212:215], a[48:51], v[48:51], v[212:215]\n"
"buffer_load_dwordx4 v190, s[8:11], s0 offen lds\n"
"ds_read_b64_tr_b16 a[226:227], v235 offset:6144\n"
"v_mfma_f32_16x16x32_bf16 v[204:207], a[52:55], v[48:51], v[204:207]\n"
"v_mov_b64 v[254:255], 0\n"
"v_mov_b64 v[98:99], 0\n"
"v_mfma_f32_16x16x32_bf16 v[220:223], a[56:59], v[48:51], v[220:223]\n"
"v_mov_b64 v[100:101], 0\n"
"v_mov_b64 v[106:107], 0\n"
"v_mfma_f32_16x16x32_bf16 v[212:215], a[60:63], v[52:55], v[212:215]\n"
"v_mov_b64 v[108:109], 0\n"
"v_max_f32 v171, v170, v143\n"
"v_mfma_f32_16x16x32_bf16 v[204:207], a[64:67], v[52:55], v[204:207]\n"
"v_max_f32 v170, v170, v143\n"
"v_cvt_pk_bf16_f32 v152, v228, v229\n"
"v_mfma_f32_16x16x32_bf16 v[220:223], a[68:71], v[52:55], v[220:223]\n"
"v_permlane16_swap_b32 v170, v171\n"
"v_mfma_f32_16x16x32_bf16 v[224:227], a[96:99], v[40:43], v[224:227]\n"
"v_max_f32 v170, v170, v171\n"
"v_mov_b32 v171, v170\n"
"v_mfma_f32_16x16x32_bf16 v[224:227], a[108:111], v[44:47], v[224:227]\n"
"s_nop 0\n"
"v_permlane32_swap_b32 v170, v171\n"
"v_mfma_f32_16x16x32_bf16 v[224:227], a[120:123], v[48:51], v[224:227]\n"
"s_add_u32 m0, s34, 10240\n"
"v_max_f32 v170, v170, v171\n"
"v_mul_f32 v170, 0xbe553b95, v170\n"
"v_mfma_f32_16x16x32_bf16 v[224:227], a[132:135], v[52:55], v[224:227]\n"
"s_waitcnt vmcnt(11)\n"
"buffer_load_dwordx4 v191, s[8:11], s0 offen lds\n"
"s_add_u32 s0, s0, s33\n"
"v_fmamk_f32 v244, v244, 0x3e553b95, v170\n"
"v_fmamk_f32 v245, v245, 0x3e553b95, v170\n"
"v_fmamk_f32 v246, v246, 0x3e553b95, v170\n"
"v_fmamk_f32 v247, v247, 0x3e553b95, v170\n"
"v_exp_f32 v244, v244\n"
"v_exp_f32 v245, v245\n"
"v_exp_f32 v246, v246\n"
"v_exp_f32 v247, v247\n"
"v_cvt_pk_bf16_f32 v82, v244, v246\n"
"v_cvt_pk_bf16_f32 v83, v245, v247\n"
"v_fmamk_f32 v248, v248, 0x3e553b95, v170\n"
"v_fmamk_f32 v249, v249, 0x3e553b95, v170\n"
"v_fmamk_f32 v250, v250, 0x3e553b95, v170\n"
"v_fmamk_f32 v251, v251, 0x3e553b95, v170\n"
"v_exp_f32 v248, v248\n"
"v_exp_f32 v249, v249\n"
"v_exp_f32 v250, v250\n"
"v_exp_f32 v251, v251\n"
"s_add_u32 m0, s34, 8192\n"
"v_cvt_pk_bf16_f32 v84, v248, v250\n"
"buffer_load_dwordx4 v190, s[8:11], s1 offen lds\n"
"v_cvt_pk_bf16_f32 v85, v249, v251\n"
"v_cvt_pk_bf16_f32 v153, v230, v231\n"
"v_mfma_f32_16x16x32_bf16 v[228:231], a[76:79], v[32:35], 0\n"
"s_waitcnt lgkmcnt(0)\n"
"ds_read_b64_tr_b16 v[244:245], v182 offset:54144\n"
"ds_read_b64_tr_b16 v[246:247], v183 offset:54144\n"
"v_smfmac_f32_16x16x64_bf16 v[252:255], v[82:85], v[128:135], v66\n"
"s_barrier\n"
"v_smfmac_f32_16x16x64_bf16 v[98:101], v[82:85], v[112:119], v66\n"
"ds_read_b64_tr_b16 v[248:249], v182 offset:54912\n"
"v_smfmac_f32_16x16x64_bf16 v[106:109], v[82:85], v[120:127], v66\n"
"ds_read_b64_tr_b16 v[250:251], v183 offset:54912\n"
"v_mfma_f32_16x16x32_bf16 v[82:85], v[172:175], v[82:85], 0\n"
"ds_read_b64_tr_b16 v[128:129], v182 offset:54152\n"
"ds_read_b64_tr_b16 v[130:131], v183 offset:54152\n"
"v_mfma_f32_16x16x32_bf16 v[228:231], a[88:91], v[36:39], v[228:231]\n"
"ds_read_b64_tr_b16 v[132:133], v182 offset:54920\n"
"v_cvt_pk_bf16_f32 v154, v216, v217\n"
"v_mfma_f32_16x16x32_bf16 v[228:231], a[100:103], v[40:43], v[228:231]\n"
"s_add_u32 m0, s79, 130176\n"
"ds_read_b64_tr_b16 v[134:135], v183 offset:54920\n"
"buffer_load_dwordx4 v192, s[16:19], s43 offen lds\n"
"v_mfma_f32_16x16x32_bf16 v[228:231], a[112:115], v[44:47], v[228:231]\n"
"ds_read_b64_tr_b16 v[112:113], v182 offset:54088\n"
"v_cvt_pk_bf16_f32 v155, v218, v219\n"
"v_mfma_f32_16x16x32_bf16 v[216:219], a[80:83], v[32:35], 0\n"
"ds_read_b64_tr_b16 v[114:115], v183 offset:54088\n"
"v_cvt_pk_bf16_f32 v166, v208, v209\n"
"v_mfma_f32_16x16x32_bf16 v[216:219], a[92:95], v[36:39], v[216:219]\n"
"ds_read_b64_tr_b16 v[116:117], v182 offset:54856\n"
"v_cvt_pk_bf16_f32 v167, v210, v211\n"
"v_mfma_f32_16x16x32_bf16 v[216:219], a[104:107], v[40:43], v[216:219]\n"
"ds_read_b64_tr_b16 v[118:119], v183 offset:54856\n"
"v_fmamk_f32 v136, v136, 0x3e553b95, v170\n"
"v_mfma_f32_16x16x32_bf16 v[216:219], a[116:119], v[44:47], v[216:219]\n"
"v_fmamk_f32 v137, v137, 0x3e553b95, v170\n"
"v_fmamk_f32 v138, v138, 0x3e553b95, v170\n"
"v_mfma_f32_16x16x32_bf16 v[228:231], a[124:127], v[48:51], v[228:231]\n"
"v_fmamk_f32 v139, v139, 0x3e553b95, v170\n"
"v_fmamk_f32 v140, v140, 0x3e553b95, v170\n"
"v_mfma_f32_16x16x32_bf16 v[216:219], a[128:131], v[48:51], v[216:219]\n"
"ds_read_b128 v[120:123], v180\n"
"v_fmamk_f32 v141, v141, 0x3e553b95, v170\n"
"v_mfma_f32_16x16x32_bf16 v[228:231], a[136:139], v[52:55], v[228:231]\n"
"s_add_u32 m0, s79, 130176\n"
"v_exp_f32 v136, v136\n"
"v_mfma_f32_16x16x32_bf16 v[216:219], a[140:143], v[52:55], v[216:219]\n"
"buffer_load_dwordx4 v192, s[16:19], s43 offen offset:1024 lds\n"
"ds_read_b128 v[124:127], v180 offset:13504\n"
"v_mfma_f32_16x16x32_bf16 v[208:211], a[144:147], v[32:35], 0\n"
"v_exp_f32 v137, v137\n"
"v_mfma_f32_16x16x32_bf16 v[208:211], a[156:159], v[36:39], v[208:211]\n"
"v_exp_f32 v138, v138\n"
"v_mfma_f32_16x16x32_bf16 v[208:211], a[168:171], v[40:43], v[208:211]\n"
"v_exp_f32 v139, v139\n"
"v_mfma_f32_16x16x32_bf16 v[208:211], a[180:183], v[44:47], v[208:211]\n"
"v_cvt_pk_bf16_f32 v90, v136, v138\n"
"v_cvt_pk_bf16_f32 v91, v137, v139\n"
"v_mfma_f32_16x16x32_bf16 v[208:211], a[192:195], v[48:51], v[208:211]\n"
"ds_read_b128 v[136:139], v181\n"
"v_fmamk_f32 v142, v142, 0x3e553b95, v170\n"
"v_mfma_f32_16x16x32_bf16 v[208:211], a[204:207], v[52:55], v[208:211]\n"
"s_waitcnt lgkmcnt(2)\n"
"v_fmamk_f32 v143, v143, 0x3e553b95, v170\n"
"v_exp_f32 v140, v140\n"
"v_mfma_f32_16x16x32_bf16 v[120:123], v[120:123], v[144:147], 0\n"
"s_add_u32 m0, s79, 130176\n"
"v_exp_f32 v141, v141\n"
"v_exp_f32 v142, v142\n"
"v_exp_f32 v143, v143\n"
"v_cvt_pk_bf16_f32 v92, v140, v142\n"
"v_cvt_pk_bf16_f32 v93, v141, v143\n"
"s_mov_b64 exec, s[44:45]\n"
"v_add_u32 v179, 0xc0, v179\n"
"s_mov_b64 exec, -1\n"
"v_smfmac_f32_16x16x64_bf16 v[252:255], v[90:93], v[244:251], v66\n"
"s_waitcnt lgkmcnt(1)\n"
"buffer_load_dwordx4 v192, s[16:19], s43 offen offset:2048 lds\n"
"v_smfmac_f32_16x16x64_bf16 v[98:101], v[90:93], v[128:135], v66\n"
"s_add_u32 s43, s79, 55296\n"
"ds_read_b128 v[140:143], v181 offset:13504\n"
"v_smfmac_f32_16x16x64_bf16 v[106:109], v[90:93], v[112:119], v66\n"
"s_waitcnt lgkmcnt(1)\n"
"ds_read_b128 v[244:247], v180 offset:27008\n"
"v_mfma_f32_16x16x32_bf16 v[90:93], v[172:175], v[90:93], v[82:85]\n"
"ds_read_b128 v[128:131], v181 offset:27008\n"
"ds_read_b128 v[248:251], v178\n"
"v_mfma_f32_16x16x32_bf16 v[136:139], v[136:139], v[144:147], 0\n"
"ds_read_b64 v[82:83], v180 offset:64\n"
"v_cvt_pk_bf16_f32 v252, v252, v253\n"
"v_mfma_f32_16x16x32_bf16 v[124:127], v[124:127], v[144:147], 0\n"
"v_cvt_pk_bf16_f32 v253, v254, v255\n"
"s_nop 0\n"
"v_rcp_f32 v171, v90\n"
"v_cvt_pk_bf16_f32 v98, v98, v99\n"
"v_cvt_pk_bf16_f32 v171, v171, v171\n"
"v_and_b32 v64, v171, v176\n"
"v_and_b32 v65, v171, v177\n"
"v_cvt_pk_bf16_f32 v99, v100, v101\n"
"v_cvt_pk_bf16_f32 v106, v106, v107\n"
"v_mfma_f32_16x16x16_bf16 v[252:255], v[252:253], v[64:65], 0\n"
"s_add_u32 m0, s34, 11264\n"
"ds_read_b64 v[84:85], v181 offset:64\n"
"buffer_load_dwordx4 v191, s[8:11], s1 offen lds\n"
"v_mfma_f32_16x16x16_bf16 v[98:101], v[98:99], v[64:65], 0\n"
"s_waitcnt lgkmcnt(5)\n"
"ds_read_b64 v[90:91], v180 offset:13568\n"
"ds_read_b64 v[92:93], v181 offset:13568\n"
"v_mfma_f32_16x16x32_bf16 v[140:143], v[140:143], v[144:147], 0\n"
"s_waitcnt lgkmcnt(6)\n"
"ds_read_b128 v[132:135], v179\n"
"v_cvt_pk_bf16_f32 v107, v108, v109\n"
"v_mfma_f32_16x16x32_bf16 v[244:247], v[244:247], v[144:147], 0\n"
"s_waitcnt lgkmcnt(6)\n"
"v_cvt_pk_bf16_f32 v252, v252, v253\n"
"v_cvt_pk_bf16_f32 v253, v254, v255\n"
"v_mfma_f32_16x16x16_bf16 v[106:109], v[106:107], v[64:65], 0\n"
"s_waitcnt lgkmcnt(5)\n"
"v_cvt_pk_bf16_f32 v254, v98, v99\n"
"v_cvt_pk_bf16_f32 v255, v100, v101\n"
"v_mfma_f32_16x16x32_bf16 v[128:131], v[128:131], v[144:147], 0\n"
"s_waitcnt lgkmcnt(4)\n"
"buffer_store_dwordx4 v[252:255], v192, s[28:31], s37 offen sc1\n"
"v_mfma_f32_16x16x32_bf16 v[120:123], v[80:83], v[156:159], v[120:123]\n"
"s_waitcnt vmcnt(12)\n"
"v_or_b32 v201, 2, s6\n"
"v_cvt_pk_bf16_f32 v106, v106, v107\n"
"v_mfma_f32_16x16x32_bf16 v[248:251], v[248:251], v[144:147], 0\n"
"s_waitcnt lgkmcnt(3)\n"
"ds_read_b64 v[98:99], v180 offset:27072\n"
"s_mov_b64 exec, 1\n"
"buffer_store_dword v201, off, s[28:31], s40 sc1\n"
"s_mov_b64 exec, -1\n"
"v_mfma_f32_16x16x32_bf16 v[136:139], v[84:87], v[158:161], v[136:139]\n"
"s_waitcnt lgkmcnt(2)\n"
"ds_read_b64 v[100:101], v181 offset:27072\n"
"ds_read_b64_tr_b16 a[228:229], v236 offset:12288\n"
"v_mfma_f32_16x16x32_bf16 v[124:127], v[88:91], v[156:159], v[124:127]\n"
"s_waitcnt lgkmcnt(3)\n"
"v_cvt_pk_bf16_f32 v107, v108, v109\n"
"v_cvt_pk_bf16_f32 v212, v212, v213\n"
"v_mfma_f32_16x16x32_bf16 v[140:143], v[92:95], v[158:161], v[140:143]\n"
"s_add_u32 s1, s1, s33\n"
"buffer_store_dwordx2 v[106:107], v193, s[28:31], s37 offen offset:1024 sc1\n"
"v_mfma_f32_16x16x32_bf16 v[132:135], v[132:135], v[144:147], 0\n"
"s_add_u32 s37, s37, 1572864\n"
"v_cvt_pk_bf16_f32 v213, v214, v215\n"
"v_cvt_pk_bf16_f32 v214, v204, v205\n"
"v_cvt_pk_bf16_f32 v215, v206, v207\n"
"v_mfma_f32_16x16x32_bf16 v[204:207], a[148:151], v[32:35], 0\n"
"ds_write_b128 v186, v[212:215] offset:20256\n"
"v_mfma_f32_16x16x32_bf16 v[212:215], a[152:155], v[32:35], 0\n"
"s_waitcnt lgkmcnt(3)\n"
"v_cvt_pk_bf16_f32 v220, v220, v221\n"
"v_cvt_pk_bf16_f32 v221, v222, v223\n"
"v_mfma_f32_16x16x32_bf16 v[244:247], v[96:99], v[156:159], v[244:247]\n"
"s_waitcnt lgkmcnt(2)\n"
"buffer_load_dword v237, v67, s[8:11], s78 offen\n"
"ds_read_b128 v[32:35], v188\n"
"v_mfma_f32_16x16x32_bf16 v[204:207], a[160:163], v[36:39], v[204:207]\n"
"s_add_u32 s78, s78, s33\n"
"ds_read_b64 v[106:107], v178 offset:64\n"
"ds_read_b64 v[108:109], v179 offset:64\n"
"v_mfma_f32_16x16x32_bf16 v[128:131], v[100:103], v[158:161], v[128:131]\n"
"ds_read_b64_tr_b16 v[112:113], v182 offset:40640\n"
"v_max_f32 v120, v120, v136\n"
"v_mfma_f32_16x16x32_bf16 v[212:215], a[164:167], v[36:39], v[212:215]\n"
"ds_read_b128 v[36:39], v189\n"
"v_max_f32 v121, v121, v137\n"
"v_mfma_f32_16x16x32_bf16 v[204:207], a[172:175], v[40:43], v[204:207]\n"
"ds_read_b64_tr_b16 v[114:115], v183 offset:40640\n"
"v_max_f32 v122, v122, v138\n"
"v_mfma_f32_16x16x32_bf16 v[212:215], a[176:179], v[40:43], v[212:215]\n"
"ds_read_b128 v[40:43], v188 offset:1024\n"
"v_max_f32 v123, v123, v139\n"
"v_mfma_f32_16x16x32_bf16 v[204:207], a[184:187], v[44:47], v[204:207]\n"
"ds_read_b64_tr_b16 v[116:117], v182 offset:41408\n"
"v_max3_f32 v170, v120, v121, v122\n"
"v_mfma_f32_16x16x32_bf16 v[212:215], a[188:191], v[44:47], v[212:215]\n"
"ds_read_b128 v[44:47], v189 offset:1024\n"
"v_max_f32 v124, v124, v140\n"
"v_mfma_f32_16x16x32_bf16 v[204:207], a[196:199], v[48:51], v[204:207]\n"
"ds_read_b64_tr_b16 v[118:119], v183 offset:41408\n"
"v_max_f32 v125, v125, v141\n"
"v_mfma_f32_16x16x32_bf16 v[212:215], a[200:203], v[48:51], v[212:215]\n"
"s_waitcnt vmcnt(14) lgkmcnt(8)\n"
"ds_read_b128 v[48:51], v188 offset:2048\n"
"v_max_f32 v126, v126, v142\n"
"v_mfma_f32_16x16x32_bf16 v[248:251], v[104:107], v[156:159], v[248:251]\n"
"s_waitcnt lgkmcnt(8)\n"
"ds_read_b64_tr_b16 v[136:137], v182 offset:40648\n"
"v_max_f32 v127, v127, v143\n"
"v_mfma_f32_16x16x32_bf16 v[132:135], v[108:111], v[158:161], v[132:135]\n"
"ds_read_b64_tr_b16 v[138:139], v183 offset:40648\n"
"v_max3_f32 v170, v170, v123, v124\n"
"v_mfma_f32_16x16x32_bf16 v[204:207], a[208:211], v[52:55], v[204:207]\n"
"s_waitcnt lgkmcnt(8)\n"
"ds_read_b64_tr_b16 v[140:141], v182 offset:41416\n"
"v_max3_f32 v170, v170, v125, v126\n"
"v_mfma_f32_16x16x32_bf16 v[212:215], a[212:215], v[52:55], v[212:215]\n"
"s_add_u32 m0, s34, 0\n"
"ds_read_b128 v[52:55], v189 offset:2048\n"
"buffer_load_dwordx4 v190, s[8:11], s32 offen lds\n"
"v_max_f32 v244, v244, v128\n"
"v_max_f32 v245, v245, v129\n"
"v_cvt_pk_bf16_f32 v224, v224, v225\n"
"v_max_f32 v246, v246, v130\n"
"v_max_f32 v247, v247, v131\n"
"v_cvt_pk_bf16_f32 v225, v226, v227\n"
"v_max3_f32 v170, v170, v127, v244\n"
"v_cvt_pk_bf16_f32 v226, v228, v229\n"
"v_max3_f32 v170, v170, v245, v246\n"
"v_cvt_pk_bf16_f32 v227, v230, v231\n"
"v_mfma_f32_16x16x32_bf16 v[228:231], a[0:3], v[32:35], 0\n"
"ds_read_b64_tr_b16 v[142:143], v183 offset:41416\n"
"ds_read_b64_tr_b16 a[230:231], v236 offset:18432\n"
"v_mfma_f32_16x16x32_bf16 v[228:231], a[12:15], v[36:39], v[228:231]\n"
"s_waitcnt lgkmcnt(10)\n"
"v_max_f32 v248, v248, v132\n"
"v_max_f32 v249, v249, v133\n"
"v_mfma_f32_16x16x32_bf16 v[228:231], a[24:27], v[40:43], v[228:231]\n"
"s_waitcnt lgkmcnt(8)\n"
"ds_read_b64_tr_b16 v[128:129], v182 offset:40584\n"
"v_cvt_pk_bf16_f32 v222, v216, v217\n"
"v_mfma_f32_16x16x32_bf16 v[228:231], a[36:39], v[44:47], v[228:231]\n"
"s_waitcnt lgkmcnt(7)\n"
"ds_read_b64_tr_b16 v[130:131], v183 offset:40584\n"
"v_max_f32 v250, v250, v134\n"
"v_mfma_f32_16x16x32_bf16 v[228:231], a[48:51], v[48:51], v[228:231]\n"
"s_add_u32 m0, s34, 3072\n"
"ds_read_b64_tr_b16 v[132:133], v182 offset:41352\n"
".Lic2%=:\n"
"s_cbranch_execz .Lib0%=\n"
"buffer_load_dwordx4 v191, s[8:11], s32 offen lds\n"
"ds_write_b128 v186, v[224:227] offset:20384\n"
"v_mfma_f32_16x16x32_bf16 v[224:227], a[72:75], v[32:35], 0\n"
"s_waitcnt lgkmcnt(6)\n"
"v_cvt_pk_bf16_f32 v223, v218, v219\n"
"v_max_f32 v251, v251, v135\n"
"v_mfma_f32_16x16x32_bf16 v[216:219], a[4:7], v[32:35], 0\n"
"s_add_u32 s32, s32, s33\n"
"v_max3_f32 v170, v170, v247, v248\n"
"v_max3_f32 v170, v170, v249, v250\n"
"v_mfma_f32_16x16x32_bf16 v[216:219], a[16:19], v[36:39], v[216:219]\n"
"ds_write_b128 v186, v[220:223] offset:20320\n"
"v_mfma_f32_16x16x32_bf16 v[220:223], a[8:11], v[32:35], 0\n"
"v_mov_b64 v[252:253], 0\n"
"v_mov_b64 v[254:255], 0\n"
"v_mfma_f32_16x16x32_bf16 v[220:223], a[20:23], v[36:39], v[220:223]\n"
"v_mov_b64 v[98:99], 0\n"
"v_mov_b64 v[100:101], 0\n"
"v_mfma_f32_16x16x32_bf16 v[216:219], a[28:31], v[40:43], v[216:219]\n"
"s_add_u32 m0, s34, 1024\n"
"ds_read_b64_tr_b16 v[134:135], v183 offset:41352\n"
"buffer_load_dwordx4 v190, s[8:11], s0 offen lds\n"
"v_mfma_f32_16x16x32_bf16 v[220:223], a[32:35], v[40:43], v[220:223]\n"
"ds_read_b64_tr_b16 a[232:233], v236 offset:12296\n"
"v_mov_b64 v[106:107], 0\n"
"v_mfma_f32_16x16x32_bf16 v[216:219], a[40:43], v[44:47], v[216:219]\n"
"ds_read_b64_tr_b16 a[234:235], v236 offset:18440\n"
"v_mov_b64 v[108:109], 0\n"
"v_mfma_f32_16x16x32_bf16 v[220:223], a[44:47], v[44:47], v[220:223]\n"
"v_max_f32 v171, v170, v251\n"
"v_max_f32 v170, v170, v251\n"
"v_mfma_f32_16x16x32_bf16 v[216:219], a[52:55], v[48:51], v[216:219]\n"
"ds_read_b64_tr_b16 a[236:237], v235 offset:12288\n"
"v_permlane16_swap_b32 v170, v171\n"
"v_mfma_f32_16x16x32_bf16 v[220:223], a[56:59], v[48:51], v[220:223]\n"
"v_max_f32 v170, v170, v171\n"
"v_mov_b32 v171, v170\n"
"v_mfma_f32_16x16x32_bf16 v[228:231], a[60:63], v[52:55], v[228:231]\n"
"ds_read_b64_tr_b16 a[238:239], v235 offset:18432\n"
"v_permlane32_swap_b32 v170, v171\n"
"v_mfma_f32_16x16x32_bf16 v[216:219], a[64:67], v[52:55], v[216:219]\n"
"s_add_u32 m0, s34, 4096\n"
"v_max_f32 v170, v170, v171\n"
"v_mul_f32 v170, 0xbe553b95, v170\n"
"v_mfma_f32_16x16x32_bf16 v[220:223], a[68:71], v[52:55], v[220:223]\n"
"buffer_load_dwordx4 v191, s[8:11], s0 offen lds\n"
"v_fmamk_f32 v120, v120, 0x3e553b95, v170\n"
"v_mfma_f32_16x16x32_bf16 v[224:227], a[84:87], v[36:39], v[224:227]\n"
"s_add_u32 s0, s0, s33\n"
"v_fmamk_f32 v121, v121, 0x3e553b95, v170\n"
"v_fmamk_f32 v122, v122, 0x3e553b95, v170\n"
"v_mfma_f32_16x16x32_bf16 v[224:227], a[96:99], v[40:43], v[224:227]\n"
"v_fmamk_f32 v123, v123, 0x3e553b95, v170\n"
"v_fmamk_f32 v124, v124, 0x3e553b95, v170\n"
"v_mfma_f32_16x16x32_bf16 v[224:227], a[108:111], v[44:47], v[224:227]\n"
"v_exp_f32 v120, v120\n"
"v_mfma_f32_16x16x32_bf16 v[224:227], a[120:123], v[48:51], v[224:227]\n"
"v_exp_f32 v121, v121\n"
"v_mfma_f32_16x16x32_bf16 v[224:227], a[132:135], v[52:55], v[224:227]\n"
"v_exp_f32 v122, v122\n"
"v_exp_f32 v123, v123\n"
"v_cvt_pk_bf16_f32 v82, v120, v122\n"
"v_cvt_pk_bf16_f32 v83, v121, v123\n"
"v_fmamk_f32 v125, v125, 0x3e553b95, v170\n"
"v_fmamk_f32 v126, v126, 0x3e553b95, v170\n"
"s_add_u32 m0, s34, 2048\n"
"v_fmamk_f32 v127, v127, 0x3e553b95, v170\n"
"buffer_load_dwordx4 v190, s[8:11], s1 offen lds\n"
"v_exp_f32 v124, v124\n"
"v_exp_f32 v125, v125\n"
"v_exp_f32 v126, v126\n"
"v_exp_f32 v127, v127\n"
"v_cvt_pk_bf16_f32 v84, v124, v126\n"
"v_cvt_pk_bf16_f32 v85, v125, v127\n"
"v_cvt_pk_bf16_f32 v144, v208, v209\n"
"v_cvt_pk_bf16_f32 v145, v210, v211\n"
"v_smfmac_f32_16x16x64_bf16 v[252:255], v[82:85], v[112:119], v66\n"
"s_waitcnt vmcnt(10) lgkmcnt(0)\n"
"ds_read_b64_tr_b16 v[120:121], v184 offset:128\n"
"v_smfmac_f32_16x16x64_bf16 v[98:101], v[82:85], v[136:143], v66\n"
"s_barrier\n"
"v_smfmac_f32_16x16x64_bf16 v[106:109], v[82:85], v[128:135], v66\n"
"ds_read_b64_tr_b16 v[122:123], v185 offset:128\n"
"v_mfma_f32_16x16x32_bf16 v[82:85], v[172:175], v[82:85], 0\n"
"ds_read_b64_tr_b16 v[124:125], v184 offset:896\n"
"ds_read_b64_tr_b16 v[126:127], v185 offset:896\n"
"v_mfma_f32_16x16x32_bf16 v[208:211], a[76:79], v[32:35], 0\n"
"s_add_u32 m0, s79, 142464\n"
"ds_read_b64_tr_b16 v[112:113], v184 offset:136\n"
"buffer_load_dwordx4 v192, s[16:19], s43 offen lds\n"
"v_mfma_f32_16x16x32_bf16 v[208:211], a[88:91], v[36:39], v[208:211]\n"
"ds_read_b64_tr_b16 v[114:115], v185 offset:136\n"
"v_cvt_pk_bf16_f32 v146, v204, v205\n"
"v_mfma_f32_16x16x32_bf16 v[208:211], a[100:103], v[40:43], v[208:211]\n"
"ds_read_b64_tr_b16 v[116:117], v184 offset:904\n"
"v_cvt_pk_bf16_f32 v147, v206, v207\n"
"v_mfma_f32_16x16x32_bf16 v[204:207], a[80:83], v[32:35], 0\n"
"ds_read_b64_tr_b16 v[118:119], v185 offset:904\n"
"v_fmamk_f32 v244, v244, 0x3e553b95, v170\n"
"v_mfma_f32_16x16x32_bf16 v[204:207], a[92:95], v[36:39], v[204:207]\n"
"ds_read_b64_tr_b16 v[136:137], v184 offset:72\n"
"v_cvt_pk_bf16_f32 v158, v212, v213\n"
"v_mfma_f32_16x16x32_bf16 v[204:207], a[104:107], v[40:43], v[204:207]\n"
"ds_read_b64_tr_b16 v[138:139], v185 offset:72\n"
"v_fmamk_f32 v245, v245, 0x3e553b95, v170\n"
"v_mfma_f32_16x16x32_bf16 v[208:211], a[112:115], v[44:47], v[208:211]\n"
"ds_read_b64_tr_b16 v[140:141], v184 offset:840\n"
"v_cvt_pk_bf16_f32 v159, v214, v215\n"
"v_mfma_f32_16x16x32_bf16 v[204:207], a[116:119], v[44:47], v[204:207]\n"
"ds_read_b64_tr_b16 v[142:143], v185 offset:840\n"
"v_fmamk_f32 v246, v246, 0x3e553b95, v170\n"
"v_mfma_f32_16x16x32_bf16 v[208:211], a[124:127], v[48:51], v[208:211]\n"
"s_add_u32 m0, s79, 142464\n"
"ds_read_b128 v[128:131], v180 offset:13504\n"
"buffer_load_dwordx4 v192, s[16:19], s43 offen offset:1024 lds\n"
"v_mfma_f32_16x16x32_bf16 v[204:207], a[128:131], v[48:51], v[204:207]\n"
"v_fmamk_f32 v247, v247, 0x3e553b95, v170\n"
"v_fmamk_f32 v248, v248, 0x3e553b95, v170\n"
"v_mfma_f32_16x16x32_bf16 v[208:211], a[136:139], v[52:55], v[208:211]\n"
"v_exp_f32 v244, v244\n"
"v_mfma_f32_16x16x32_bf16 v[204:207], a[140:143], v[52:55], v[204:207]\n"
"v_exp_f32 v245, v245\n"
"v_mfma_f32_16x16x32_bf16 v[212:215], a[144:147], v[32:35], 0\n"
"v_exp_f32 v246, v246\n"
"v_mfma_f32_16x16x32_bf16 v[212:215], a[156:159], v[36:39], v[212:215]\n"
"v_exp_f32 v247, v247\n"
"v_mfma_f32_16x16x32_bf16 v[212:215], a[168:171], v[40:43], v[212:215]\n"
"v_cvt_pk_bf16_f32 v90, v244, v246\n"
"v_cvt_pk_bf16_f32 v91, v245, v247\n"
"v_mfma_f32_16x16x32_bf16 v[212:215], a[180:183], v[44:47], v[212:215]\n"
"ds_read_b128 v[244:247], v181 offset:13504\n"
"ds_read_b128 v[132:135], v180 offset:27008\n"
"v_mfma_f32_16x16x32_bf16 v[212:215], a[192:195], v[48:51], v[212:215]\n"
"s_add_u32 m0, s79, 142464\n"
"v_fmamk_f32 v249, v249, 0x3e553b95, v170\n"
"v_fmamk_f32 v250, v250, 0x3e553b95, v170\n"
"v_mfma_f32_16x16x32_bf16 v[212:215], a[204:207], v[52:55], v[212:215]\n"
"s_waitcnt lgkmcnt(2)\n"
"buffer_load_dwordx4 v192, s[16:19], s43 offen offset:2048 lds\n"
"v_fmamk_f32 v251, v251, 0x3e553b95, v170\n"
"v_mfma_f32_16x16x32_bf16 v[128:131], v[128:131], v[148:151], 0\n"
"s_add_u32 s43, s80, 12288\n"
"v_exp_f32 v248, v248\n"
"v_exp_f32 v249, v249\n"
"v_exp_f32 v250, v250\n"
"v_exp_f32 v251, v251\n"
"v_cvt_pk_bf16_f32 v92, v248, v250\n"
"v_cvt_pk_bf16_f32 v93, v249, v251\n"
"v_cvt_pk_bf16_f32 v228, v228, v229\n"
"v_cvt_pk_bf16_f32 v229, v230, v231\n"
"v_smfmac_f32_16x16x64_bf16 v[252:255], v[90:93], v[120:127], v66\n"
"ds_read_b128 v[248:251], v181 offset:27008\n"
"v_smfmac_f32_16x16x64_bf16 v[98:101], v[90:93], v[112:119], v66\n"
"s_waitcnt lgkmcnt(2)\n"
"ds_read_b128 v[120:123], v178\n"
"v_smfmac_f32_16x16x64_bf16 v[106:109], v[90:93], v[136:143], v66\n"
"s_waitcnt lgkmcnt(2)\n"
"ds_read_b128 v[112:115], v179\n"
"v_mfma_f32_16x16x32_bf16 v[90:93], v[172:175], v[90:93], v[82:85]\n"
"s_add_u32 m0, s34, 5120\n"
"ds_read_b128 v[124:127], v178 offset:13504\n"
"buffer_load_dwordx4 v191, s[8:11], s1 offen lds\n"
"v_mfma_f32_16x16x32_bf16 v[244:247], v[244:247], v[148:151], 0\n"
"s_add_u32 s1, s1, s33\n"
"ds_read_b64 v[82:83], v180 offset:13568\n"
"ds_read_b64 v[84:85], v181 offset:13568\n"
"v_mfma_f32_16x16x32_bf16 v[132:135], v[132:135], v[148:151], 0\n"
"v_rcp_f32 v171, v90\n"
"v_cvt_pk_bf16_f32 v252, v252, v253\n"
"v_cvt_pk_bf16_f32 v171, v171, v171\n"
"v_and_b32 v64, v171, v176\n"
"v_and_b32 v65, v171, v177\n"
"v_cvt_pk_bf16_f32 v253, v254, v255\n"
"v_cvt_pk_bf16_f32 v98, v98, v99\n"
"v_cvt_pk_bf16_f32 v99, v100, v101\n"
"v_mfma_f32_16x16x16_bf16 v[252:255], v[252:253], v[64:65], 0\n"
"s_waitcnt lgkmcnt(5)\n"
"ds_read_b64 v[90:91], v180 offset:27072\n"
"ds_read_b64 v[92:93], v181 offset:27072\n"
"v_mfma_f32_16x16x16_bf16 v[98:101], v[98:99], v[64:65], 0\n"
"s_waitcnt lgkmcnt(6)\n"
"ds_read_b128 v[116:119], v179 offset:13504\n"
"v_cvt_pk_bf16_f32 v106, v106, v107\n"
"v_mfma_f32_16x16x32_bf16 v[248:251], v[248:251], v[148:151], 0\n"
"s_waitcnt lgkmcnt(6)\n"
"v_cvt_pk_bf16_f32 v107, v108, v109\n"
"v_cvt_pk_bf16_f32 v252, v252, v253\n"
"v_mfma_f32_16x16x32_bf16 v[120:123], v[120:123], v[148:151], 0\n"
"s_waitcnt lgkmcnt(5)\n"
"v_cvt_pk_bf16_f32 v253, v254, v255\n"
"v_cvt_pk_bf16_f32 v254, v98, v99\n"
"v_mfma_f32_16x16x16_bf16 v[106:109], v[106:107], v[64:65], 0\n"
"s_waitcnt lgkmcnt(4)\n"
"buffer_load_dword v200, off, s[28:31], s39 sc1\n"
"ds_read_b64 v[98:99], v178 offset:64\n"
"v_mfma_f32_16x16x32_bf16 v[128:131], v[80:83], v[160:163], v[128:131]\n"
"s_waitcnt lgkmcnt(4)\n"
"ds_read_b64_tr_b16 a[240:241], v236\n"
"v_cvt_pk_bf16_f32 v255, v100, v101\n"
"v_mfma_f32_16x16x32_bf16 v[244:247], v[84:87], v[162:165], v[244:247]\n"
"buffer_store_dwordx4 v[252:255], v192, s[28:31], s37 offen sc1\n"
"v_mfma_f32_16x16x32_bf16 v[112:115], v[112:115], v[148:151], 0\n"
"s_waitcnt lgkmcnt(4)\n"
"ds_read_b64 v[100:101], v179 offset:64\n"
"v_cvt_pk_bf16_f32 v106, v106, v107\n"
"v_mfma_f32_16x16x32_bf16 v[132:135], v[88:91], v[160:163], v[132:135]\n"
"s_waitcnt lgkmcnt(4)\n"
"v_cvt_pk_bf16_f32 v107, v108, v109\n"
"v_cvt_pk_bf16_f32 v230, v216, v217\n"
"v_mfma_f32_16x16x32_bf16 v[248:251], v[92:95], v[162:165], v[248:251]\n"
"s_waitcnt lgkmcnt(3)\n"
"buffer_store_dwordx2 v[106:107], v193, s[28:31], s37 offen offset:1024 sc1\n"
"v_mfma_f32_16x16x32_bf16 v[124:127], v[124:127], v[148:151], 0\n"
"s_add_u32 s37, s37, 1572864\n"
"ds_read_b64 v[106:107], v178 offset:13568\n"
"ds_read_b64 v[108:109], v179 offset:13568\n"
"v_mfma_f32_16x16x32_bf16 v[116:119], v[116:119], v[148:151], 0\n"
"s_waitcnt lgkmcnt(4)\n"
"ds_read_b64_tr_b16 v[136:137], v182 offset:54144\n"
"v_cvt_pk_bf16_f32 v231, v218, v219\n"
"v_mfma_f32_16x16x32_bf16 v[120:123], v[96:99], v[160:163], v[120:123]\n"
"ds_write_b128 v186, v[228:231] offset:33760\n"
"v_mfma_f32_16x16x32_bf16 v[216:219], a[148:151], v[32:35], 0\n"
"v_cvt_pk_bf16_f32 v220, v220, v221\n"
"v_cvt_pk_bf16_f32 v221, v222, v223\n"
"v_mfma_f32_16x16x32_bf16 v[228:231], a[152:155], v[32:35], 0\n"
"s_waitcnt lgkmcnt(4)\n"
"buffer_load_dword v237, v67, s[8:11], s78 offen\n"
"ds_read_b128 v[32:35], v188 offset:6144\n"
"v_mfma_f32_16x16x32_bf16 v[112:115], v[100:103], v[162:165], v[112:115]\n"
"s_add_u32 s78, s78, s33\n"
"ds_read_b64_tr_b16 v[138:139], v183 offset:54144\n"
"ds_read_b64_tr_b16 v[140:141], v182 offset:54912\n"
"v_mfma_f32_16x16x32_bf16 v[216:219], a[160:163], v[36:39], v[216:219]\n"
"ds_read_b64_tr_b16 v[142:143], v183 offset:54912\n"
"v_max_f32 v128, v128, v244\n"
"v_mfma_f32_16x16x32_bf16 v[228:231], a[164:167], v[36:39], v[228:231]\n"
"s_waitcnt lgkmcnt(7)\n"
"ds_read_b128 v[36:39], v189 offset:6144\n"
"v_max_f32 v129, v129, v245\n"
"v_mfma_f32_16x16x32_bf16 v[124:127], v[104:107], v[160:163], v[124:127]\n"
"s_waitcnt lgkmcnt(7)\n"
"ds_read_b64_tr_b16 v[244:245], v182 offset:54152\n"
"v_max_f32 v130, v130, v246\n"
"v_mfma_f32_16x16x32_bf16 v[116:119], v[108:111], v[162:165], v[116:119]\n"
"ds_read_b64_tr_b16 a[242:243], v236 offset:6144\n"
"v_max_f32 v131, v131, v247\n"
"v_mfma_f32_16x16x32_bf16 v[216:219], a[172:175], v[40:43], v[216:219]\n"
"v_max3_f32 v170, v128, v129, v130\n"
"v_max_f32 v132, v132, v248\n"
"v_mfma_f32_16x16x32_bf16 v[228:231], a[176:179], v[40:43], v[228:231]\n"
"ds_read_b128 v[40:43], v188 offset:7168\n"
"v_max_f32 v133, v133, v249\n"
"v_mfma_f32_16x16x32_bf16 v[216:219], a[184:187], v[44:47], v[216:219]\n"
"ds_read_b64_tr_b16 v[246:247], v183 offset:54152\n"
"v_max_f32 v134, v134, v250\n"
"v_mfma_f32_16x16x32_bf16 v[228:231], a[188:191], v[44:47], v[228:231]\n"
"ds_read_b128 v[44:47], v189 offset:7168\n"
"v_max_f32 v135, v135, v251\n"
"v_mfma_f32_16x16x32_bf16 v[216:219], a[196:199], v[48:51], v[216:219]\n"
"ds_read_b64_tr_b16 v[248:249], v182 offset:54920\n"
"v_max3_f32 v170, v170, v131, v132\n"
"v_mfma_f32_16x16x32_bf16 v[228:231], a[200:203], v[48:51], v[228:231]\n"
"s_waitcnt vmcnt(17)\n"
"ds_read_b128 v[48:51], v188 offset:8192\n"
"v_max3_f32 v170, v170, v133, v134\n"
"v_mfma_f32_16x16x32_bf16 v[216:219], a[208:211], v[52:55], v[216:219]\n"
"s_waitcnt lgkmcnt(7)\n"
"ds_read_b64_tr_b16 v[250:251], v183 offset:54920\n"
"v_max_f32 v120, v120, v112\n"
"v_mfma_f32_16x16x32_bf16 v[228:231], a[212:215], v[52:55], v[228:231]\n"
"s_add_u32 m0, s34, 6144\n"
"ds_read_b128 v[52:55], v189 offset:8192\n"
"buffer_load_dwordx4 v190, s[8:11], s32 offen lds\n"
"v_max_f32 v121, v121, v113\n"
"v_max_f32 v122, v122, v114\n"
"v_max_f32 v123, v123, v115\n"
"v_max3_f32 v170, v170, v135, v120\n"
"v_cvt_pk_bf16_f32 v224, v224, v225\n"
"v_max3_f32 v170, v170, v121, v122\n"
"v_max_f32 v124, v124, v116\n"
"v_cvt_pk_bf16_f32 v225, v226, v227\n"
"v_max_f32 v125, v125, v117\n"
"v_cvt_pk_bf16_f32 v226, v208, v209\n"
"v_max_f32 v126, v126, v118\n"
"v_cvt_pk_bf16_f32 v227, v210, v211\n"
"v_mfma_f32_16x16x32_bf16 v[208:211], a[0:3], v[32:35], 0\n"
"ds_read_b64_tr_b16 v[112:113], v182 offset:54088\n"
"ds_read_b64_tr_b16 v[114:115], v183 offset:54088\n"
"v_mfma_f32_16x16x32_bf16 v[208:211], a[12:15], v[36:39], v[208:211]\n"
"s_waitcnt lgkmcnt(8)\n"
"ds_read_b64_tr_b16 v[116:117], v182 offset:54856\n"
"v_max_f32 v127, v127, v119\n"
"v_mfma_f32_16x16x32_bf16 v[208:211], a[24:27], v[40:43], v[208:211]\n"
"s_waitcnt lgkmcnt(7)\n"
"ds_read_b64_tr_b16 v[118:119], v183 offset:54856\n"
"v_max3_f32 v170, v170, v123, v124\n"
"v_mfma_f32_16x16x32_bf16 v[208:211], a[36:39], v[44:47], v[208:211]\n"
"s_waitcnt lgkmcnt(6)\n"
"ds_write_b128 v186, v[224:227] offset:33888\n"
"v_mfma_f32_16x16x32_bf16 v[208:211], a[48:51], v[48:51], v[208:211]\n"
"s_waitcnt lgkmcnt(5)\n"
"v_cvt_pk_bf16_f32 v222, v204, v205\n"
"v_max3_f32 v170, v170, v125, v126\n"
"v_mfma_f32_16x16x32_bf16 v[208:211], a[60:63], v[52:55], v[208:211]\n"
"s_add_u32 m0, s34, 9216\n"
"v_cvt_pk_bf16_f32 v223, v206, v207\n"
"s_mov_b64 exec, s[46:47]\n"
"v_add_u32 v183, 0xc0, v183\n"
"s_mov_b64 exec, -1\n"
"v_mfma_f32_16x16x32_bf16 v[204:207], a[4:7], v[32:35], 0\n"
"buffer_load_dwordx4 v191, s[8:11], s32 offen lds\n"
"ds_write_b128 v186, v[220:223] offset:33824\n"
"v_mfma_f32_16x16x32_bf16 v[220:223], a[8:11], v[32:35], 0\n"
"s_add_u32 s32, s32, s33\n"
"v_mov_b64 v[252:253], 0\n"
"v_mov_b64 v[254:255], 0\n"
"v_mfma_f32_16x16x32_bf16 v[204:207], a[16:19], v[36:39], v[204:207]\n"
"v_mov_b64 v[98:99], 0\n"
"v_mov_b64 v[100:101], 0\n"
"v_mfma_f32_16x16x32_bf16 v[220:223], a[20:23], v[36:39], v[220:223]\n"
"ds_read_b64_tr_b16 a[244:245], v236 offset:8\n"
"v_mov_b64 v[106:107], 0\n"
"v_mfma_f32_16x16x32_bf16 v[204:207], a[28:31], v[40:43], v[204:207]\n"
"ds_read_b64_tr_b16 a[246:247], v236 offset:6152\n"
"v_mov_b64 v[108:109], 0\n"
"v_mfma_f32_16x16x32_bf16 v[220:223], a[32:35], v[40:43], v[220:223]\n"
"v_max_f32 v171, v170, v127\n"
"v_max_f32 v170, v170, v127\n"
"v_mfma_f32_16x16x32_bf16 v[204:207], a[40:43], v[44:47], v[204:207]\n"
"ds_read_b64_tr_b16 a[248:249], v235\n"
"v_permlane16_swap_b32 v170, v171\n"
"v_mfma_f32_16x16x32_bf16 v[220:223], a[44:47], v[44:47], v[220:223]\n"
"s_add_u32 m0, s34, 7168\n"
"v_max_f32 v170, v170, v171\n"
"v_mov_b32 v171, v170\n"
"v_mfma_f32_16x16x32_bf16 v[204:207], a[52:55], v[48:51], v[204:207]\n"
"buffer_load_dwordx4 v190, s[8:11], s0 offen lds\n"
"ds_read_b64_tr_b16 a[250:251], v235 offset:6144\n"
"v_mfma_f32_16x16x32_bf16 v[220:223], a[56:59], v[48:51], v[220:223]\n"
"v_permlane32_swap_b32 v170, v171\n"
"v_mfma_f32_16x16x32_bf16 v[204:207], a[64:67], v[52:55], v[204:207]\n"
"v_max_f32 v170, v170, v171\n"
"v_mul_f32 v170, 0xbe553b95, v170\n"
"v_mfma_f32_16x16x32_bf16 v[220:223], a[68:71], v[52:55], v[220:223]\n"
"v_fmamk_f32 v128, v128, 0x3e553b95, v170\n"
"v_fmamk_f32 v129, v129, 0x3e553b95, v170\n"
"v_mfma_f32_16x16x32_bf16 v[224:227], a[72:75], v[32:35], 0\n"
"v_fmamk_f32 v130, v130, 0x3e553b95, v170\n"
"v_fmamk_f32 v131, v131, 0x3e553b95, v170\n"
"v_mfma_f32_16x16x32_bf16 v[224:227], a[84:87], v[36:39], v[224:227]\n"
"v_exp_f32 v128, v128\n"
"v_mfma_f32_16x16x32_bf16 v[224:227], a[96:99], v[40:43], v[224:227]\n"
"v_exp_f32 v129, v129\n"
"v_mfma_f32_16x16x32_bf16 v[224:227], a[108:111], v[44:47], v[224:227]\n"
"v_exp_f32 v130, v130\n"
"v_mfma_f32_16x16x32_bf16 v[224:227], a[120:123], v[48:51], v[224:227]\n"
"s_add_u32 m0, s34, 10240\n"
"v_exp_f32 v131, v131\n"
"v_mfma_f32_16x16x32_bf16 v[224:227], a[132:135], v[52:55], v[224:227]\n"
"buffer_load_dwordx4 v191, s[8:11], s0 offen lds\n"
"s_add_u32 s0, s0, s33\n"
"v_cvt_pk_bf16_f32 v82, v128, v130\n"
"v_cvt_pk_bf16_f32 v83, v129, v131\n"
"v_fmamk_f32 v132, v132, 0x3e553b95, v170\n"
"v_fmamk_f32 v133, v133, 0x3e553b95, v170\n"
"v_fmamk_f32 v134, v134, 0x3e553b95, v170\n"
"v_fmamk_f32 v135, v135, 0x3e553b95, v170\n"
"v_exp_f32 v132, v132\n"
"v_exp_f32 v133, v133\n"
"v_exp_f32 v134, v134\n"
"v_exp_f32 v135, v135\n"
"v_cvt_pk_bf16_f32 v84, v132, v134\n"
"v_cvt_pk_bf16_f32 v85, v133, v135\n"
"v_cvt_pk_bf16_f32 v148, v212, v213\n"
"v_cvt_pk_bf16_f32 v149, v214, v215\n"
"v_smfmac_f32_16x16x64_bf16 v[252:255], v[82:85], v[136:143], v66\n"
"s_waitcnt vmcnt(9) lgkmcnt(0)\n"
"ds_read_b64_tr_b16 v[128:129], v182 offset:128\n"
"v_smfmac_f32_16x16x64_bf16 v[98:101], v[82:85], v[244:251], v66\n"
"s_barrier\n"
"v_smfmac_f32_16x16x64_bf16 v[106:109], v[82:85], v[112:119], v66\n"
"s_add_u32 m0, s79, 130176\n"
"s_nop 0\n"
"buffer_load_dwordx4 v192, s[16:19], s43 offen lds\n"
"v_mfma_f32_16x16x32_bf16 v[82:85], v[172:175], v[82:85], 0\n"
"ds_read_b64_tr_b16 v[130:131], v183 offset:128\n"
"ds_read_b64_tr_b16 v[132:133], v182 offset:896\n"
"v_mfma_f32_16x16x32_bf16 v[212:215], a[76:79], v[32:35], 0\n"
"ds_read_b64_tr_b16 v[134:135], v183 offset:896\n"
"v_cvt_pk_bf16_f32 v150, v216, v217\n"
"v_mfma_f32_16x16x32_bf16 v[212:215], a[88:91], v[36:39], v[212:215]\n"
"ds_read_b64_tr_b16 v[136:137], v182 offset:136\n"
"v_cvt_pk_bf16_f32 v151, v218, v219\n"
"v_mfma_f32_16x16x32_bf16 v[216:219], a[80:83], v[32:35], 0\n"
"ds_read_b64_tr_b16 v[138:139], v183 offset:136\n"
"v_fmamk_f32 v120, v120, 0x3e553b95, v170\n"
"v_mfma_f32_16x16x32_bf16 v[216:219], a[92:95], v[36:39], v[216:219]\n"
"ds_read_b64_tr_b16 v[140:141], v182 offset:904\n"
"v_cvt_pk_bf16_f32 v162, v228, v229\n"
"v_mfma_f32_16x16x32_bf16 v[212:215], a[100:103], v[40:43], v[212:215]\n"
"ds_read_b64_tr_b16 v[142:143], v183 offset:904\n"
"v_fmamk_f32 v121, v121, 0x3e553b95, v170\n"
"v_mfma_f32_16x16x32_bf16 v[216:219], a[104:107], v[40:43], v[216:219]\n"
"ds_read_b64_tr_b16 v[244:245], v182 offset:72\n"
"v_cvt_pk_bf16_f32 v163, v230, v231\n"
"v_mfma_f32_16x16x32_bf16 v[212:215], a[112:115], v[44:47], v[212:215]\n"
"s_add_u32 m0, s79, 130176\n"
"ds_read_b64_tr_b16 v[246:247], v183 offset:72\n"
"buffer_load_dwordx4 v192, s[16:19], s43 offen offset:1024 lds\n"
"v_mfma_f32_16x16x32_bf16 v[216:219], a[116:119], v[44:47], v[216:219]\n"
"ds_read_b64_tr_b16 v[248:249], v182 offset:840\n"
"v_fmamk_f32 v122, v122, 0x3e553b95, v170\n"
"v_mfma_f32_16x16x32_bf16 v[212:215], a[124:127], v[48:51], v[212:215]\n"
"ds_read_b64_tr_b16 v[250:251], v183 offset:840\n"
"v_fmamk_f32 v123, v123, 0x3e553b95, v170\n"
"v_mfma_f32_16x16x32_bf16 v[216:219], a[128:131], v[48:51], v[216:219]\n"
"ds_read_b128 v[112:115], v180 offset:27008\n"
"v_fmamk_f32 v124, v124, 0x3e553b95, v170\n"
"v_mfma_f32_16x16x32_bf16 v[212:215], a[136:139], v[52:55], v[212:215]\n"
"v_exp_f32 v120, v120\n"
"v_mfma_f32_16x16x32_bf16 v[216:219], a[140:143], v[52:55], v[216:219]\n"
"v_exp_f32 v121, v121\n"
"v_mfma_f32_16x16x32_bf16 v[228:231], a[144:147], v[32:35], 0\n"
"v_exp_f32 v122, v122\n"
"v_mfma_f32_16x16x32_bf16 v[228:231], a[156:159], v[36:39], v[228:231]\n"
"v_exp_f32 v123, v123\n"
"v_mfma_f32_16x16x32_bf16 v[228:231], a[168:171], v[40:43], v[228:231]\n"
"s_add_u32 m0, s79, 130176\n"
"v_cvt_pk_bf16_f32 v90, v120, v122\n"
"v_cvt_pk_bf16_f32 v91, v121, v123\n"
"v_mfma_f32_16x16x32_bf16 v[228:231], a[180:183], v[44:47], v[228:231]\n"
"buffer_load_dwordx4 v192, s[16:19], s43 offen offset:2048 lds\n"
"ds_read_b128 v[120:123], v181 offset:27008\n"
"v_mfma_f32_16x16x32_bf16 v[228:231], a[192:195], v[48:51], v[228:231]\n"
"s_add_u32 s43, s80, 49152\n"
"v_fmamk_f32 v125, v125, 0x3e553b95, v170\n"
"v_fmamk_f32 v126, v126, 0x3e553b95, v170\n"
"v_mfma_f32_16x16x32_bf16 v[228:231], a[204:207], v[52:55], v[228:231]\n"
"ds_read_b128 v[116:119], v178\n"
"v_fmamk_f32 v127, v127, 0x3e553b95, v170\n"
"v_exp_f32 v124, v124\n"
"v_exp_f32 v125, v125\n"
"v_exp_f32 v126, v126\n"
"v_exp_f32 v127, v127\n"
"v_cvt_pk_bf16_f32 v92, v124, v126\n"
"v_cvt_pk_bf16_f32 v93, v125, v127\n"
"v_cvt_pk_bf16_f32 v208, v208, v209\n"
"v_cvt_pk_bf16_f32 v209, v210, v211\n"
"v_cvt_pk_bf16_f32 v210, v204, v205\n"
"v_cvt_pk_bf16_f32 v211, v206, v207\n"
"v_mfma_f32_16x16x32_bf16 v[204:207], a[148:151], v[32:35], 0\n"
"s_waitcnt lgkmcnt(2)\n"
"ds_read_b128 v[124:127], v179\n"
"ds_write_b128 v186, v[208:211] offset:47264\n"
"v_smfmac_f32_16x16x64_bf16 v[252:255], v[90:93], v[128:135], v66\n"
"s_waitcnt lgkmcnt(3)\n"
"v_cvt_pk_bf16_f32 v220, v220, v221\n"
"v_smfmac_f32_16x16x64_bf16 v[98:101], v[90:93], v[136:143], v66\n"
"s_waitcnt lgkmcnt(2)\n"
"ds_read_b128 v[128:131], v178 offset:13504\n"
"v_smfmac_f32_16x16x64_bf16 v[106:109], v[90:93], v[244:251], v66\n"
"s_add_u32 m0, s34, 8192\n"
"s_nop 0\n"
"buffer_load_dwordx4 v190, s[8:11], s1 offen lds\n"
"v_mfma_f32_16x16x32_bf16 v[90:93], v[172:175], v[90:93], v[82:85]\n"
"ds_read_b128 v[136:139], v179 offset:13504\n"
"ds_read_b128 v[132:135], v178 offset:27008\n"
"v_mfma_f32_16x16x32_bf16 v[112:115], v[112:115], v[152:155], 0\n"
"s_waitcnt vmcnt(11)\n"
"ds_read_b64 v[82:83], v180 offset:27072\n"
"v_add3_u32 v200, v200, s50, -2\n"
"v_mfma_f32_16x16x32_bf16 v[120:123], v[120:123], v[152:155], 0\n"
"v_cmp_lt_u32 vcc, 64, v200\n"
"v_cvt_pk_bf16_f32 v252, v252, v253\n"
"v_mfma_f32_16x16x32_bf16 v[116:119], v[116:119], v[152:155], 0\n"
"s_waitcnt lgkmcnt(5)\n"
"ds_read_b64 v[84:85], v181 offset:27072\n"
"ds_read_b64 v[92:93], v179 offset:64\n"
"v_mfma_f32_16x16x32_bf16 v[124:127], v[124:127], v[152:155], 0\n"
"s_cbranch_vccnz .Lp1%=\n"
".Lr1%=:\n"
"v_mfma_f32_16x16x32_bf16 v[208:211], a[152:155], v[32:35], 0\n"
"buffer_load_dword v201, off, s[28:31], s40 offset:128 sc1\n"
"ds_read_b128 v[32:35], v188\n"
"v_mfma_f32_16x16x32_bf16 v[204:207], a[160:163], v[36:39], v[204:207]\n"
"s_waitcnt lgkmcnt(6)\n"
"buffer_load_dwordx4 v[56:59], v192, s[28:31], s38 offen sc1\n"
"ds_read_b128 v[140:143], v179 offset:27008\n"
"v_mfma_f32_16x16x32_bf16 v[128:131], v[128:131], v[152:155], 0\n"
"v_rcp_f32 v171, v90\n"
"v_mfma_f32_16x16x32_bf16 v[208:211], a[164:167], v[36:39], v[208:211]\n"
"s_waitcnt lgkmcnt(6)\n"
"ds_read_b128 v[36:39], v189\n"
"ds_read_b64 v[90:91], v178 offset:64\n"
"v_mfma_f32_16x16x32_bf16 v[136:139], v[136:139], v[152:155], 0\n"
"s_waitcnt lgkmcnt(7)\n"
"v_cvt_pk_bf16_f32 v171, v171, v171\n"
"v_and_b32 v64, v171, v176\n"
"v_mfma_f32_16x16x32_bf16 v[132:135], v[132:135], v[152:155], 0\n"
"s_waitcnt lgkmcnt(6)\n"
"buffer_load_dwordx2 v[60:61], v193, s[28:31], s38 offen offset:1024 sc1\n"
"v_and_b32 v65, v171, v177\n"
"v_mfma_f32_16x16x32_bf16 v[112:115], v[80:83], v[164:167], v[112:115]\n"
"s_waitcnt lgkmcnt(5)\n"
"v_cvt_pk_bf16_f32 v253, v254, v255\n"
"v_cvt_pk_bf16_f32 v98, v98, v99\n"
"v_mfma_f32_16x16x32_bf16 v[120:123], v[84:87], v[166:169], v[120:123]\n"
"s_waitcnt lgkmcnt(4)\n"
"v_cvt_pk_bf16_f32 v99, v100, v101\n"
"v_cvt_pk_bf16_f32 v106, v106, v107\n"
"v_mfma_f32_16x16x16_bf16 v[252:255], v[252:253], v[64:65], 0\n"
"s_add_u32 m0, s34, 11264\n"
"v_cvt_pk_bf16_f32 v107, v108, v109\n"
"v_cvt_pk_bf16_f32 v221, v222, v223\n"
"v_mfma_f32_16x16x16_bf16 v[98:101], v[98:99], v[64:65], 0\n"
"s_waitcnt lgkmcnt(2)\n"
"buffer_load_dwordx4 v191, s[8:11], s1 offen lds\n"
"ds_read_b64_tr_b16 a[252:253], v236 offset:12288\n"
"v_mfma_f32_16x16x16_bf16 v[106:109], v[106:107], v[64:65], 0\n"
"s_add_u32 s1, s1, s33\n"
"v_cvt_pk_bf16_f32 v252, v252, v253\n"
"v_cvt_pk_bf16_f32 v253, v254, v255\n"
"v_mfma_f32_16x16x32_bf16 v[124:127], v[92:95], v[166:169], v[124:127]\n"
"s_add_u32 s38, s38, 1572864\n"
"v_cvt_pk_bf16_f32 v254, v98, v99\n"
"v_cvt_pk_bf16_f32 v255, v100, v101\n"
"v_mfma_f32_16x16x32_bf16 v[140:143], v[140:143], v[152:155], 0\n"
"s_waitcnt lgkmcnt(1)\n"
"ds_read_b64 v[98:99], v178 offset:13568\n"
"ds_read_b64 v[100:101], v179 offset:13568\n"
"v_mfma_f32_16x16x32_bf16 v[116:119], v[88:91], v[164:167], v[116:119]\n"
"buffer_store_dwordx4 v[252:255], v192, s[28:31], s37 offen sc1\n"
"v_mfma_f32_16x16x32_bf16 v[204:207], a[172:175], v[40:43], v[204:207]\n"
"v_cvt_pk_bf16_f32 v106, v106, v107\n"
"v_cvt_pk_bf16_f32 v107, v108, v109\n"
"v_mfma_f32_16x16x32_bf16 v[208:211], a[176:179], v[40:43], v[208:211]\n"
"ds_read_b128 v[40:43], v188 offset:1024\n"
"ds_read_b64 v[108:109], v179 offset:27072\n"
"v_mfma_f32_16x16x32_bf16 v[204:207], a[184:187], v[44:47], v[204:207]\n"
"s_add_u32 m0, s34, 0\n"
"buffer_store_dwordx2 v[106:107], v193, s[28:31], s37 offen offset:1024 sc1\n"
"v_mfma_f32_16x16x32_bf16 v[208:211], a[188:191], v[44:47], v[208:211]\n"
"s_add_u32 s37, s37, 1572864\n"
"ds_read_b128 v[44:47], v189 offset:1024\n"
"ds_read_b64 v[106:107], v178 offset:27072\n"
"v_mfma_f32_16x16x32_bf16 v[204:207], a[196:199], v[48:51], v[204:207]\n"
"ds_read_b64_tr_b16 v[244:245], v184 offset:128\n"
"v_max_f32 v112, v112, v120\n"
"v_mfma_f32_16x16x32_bf16 v[208:211], a[200:203], v[48:51], v[208:211]\n"
"s_waitcnt lgkmcnt(6)\n"
"ds_read_b128 v[48:51], v188 offset:2048\n"
"v_max_f32 v113, v113, v121\n"
"v_mfma_f32_16x16x32_bf16 v[128:131], v[96:99], v[164:167], v[128:131]\n"
"s_waitcnt lgkmcnt(6)\n"
"buffer_load_dwordx4 v190, s[8:11], s32 offen lds\n"
"ds_read_b64_tr_b16 v[246:247], v185 offset:128\n"
"v_mfma_f32_16x16x32_bf16 v[136:139], v[100:103], v[166:169], v[136:139]\n"
"ds_read_b64_tr_b16 v[248:249], v184 offset:896\n"
"v_max_f32 v114, v114, v122\n"
"v_mfma_f32_16x16x32_bf16 v[204:207], a[208:211], v[52:55], v[204:207]\n"
"ds_read_b64_tr_b16 v[250:251], v185 offset:896\n"
"v_max_f32 v115, v115, v123\n"
"v_mfma_f32_16x16x32_bf16 v[208:211], a[212:215], v[52:55], v[208:211]\n"
"s_waitcnt lgkmcnt(7)\n"
"ds_read_b128 v[52:55], v189 offset:2048\n"
"v_max3_f32 v170, v112, v113, v114\n"
"v_max_f32 v116, v116, v124\n"
"v_mfma_f32_16x16x32_bf16 v[140:143], v[108:111], v[166:169], v[140:143]\n"
"ds_read_b64_tr_b16 v[120:121], v184 offset:136\n"
"ds_read_b64_tr_b16 v[122:123], v185 offset:136\n"
"buffer_load_dword v237, v67, s[8:11], s78 offen\n"
"v_max_f32 v117, v117, v125\n"
"v_max_f32 v118, v118, v126\n"
"v_max_f32 v119, v119, v127\n"
"v_max3_f32 v170, v170, v115, v116\n"
"v_max3_f32 v170, v170, v117, v118\n"
"v_max_f32 v128, v128, v136\n"
"v_max_f32 v129, v129, v137\n"
"v_max_f32 v130, v130, v138\n"
"v_max_f32 v131, v131, v139\n"
"v_cvt_pk_bf16_f32 v224, v224, v225\n"
"v_max3_f32 v170, v170, v119, v128\n"
"v_max3_f32 v170, v170, v129, v130\n"
"v_cvt_pk_bf16_f32 v225, v226, v227\n"
"v_cvt_pk_bf16_f32 v226, v212, v213\n"
".Lic3%=:\n"
"s_cbranch_execz .Lib0%=\n"
"v_cvt_pk_bf16_f32 v227, v214, v215\n"
"v_mfma_f32_16x16x32_bf16 v[212:215], a[0:3], v[32:35], 0\n"
"s_waitcnt lgkmcnt(8)\n"
"ds_read_b64_tr_b16 v[124:125], v184 offset:904\n"
"ds_read_b64_tr_b16 v[126:127], v185 offset:904\n"
"v_mfma_f32_16x16x32_bf16 v[132:135], v[104:107], v[164:167], v[132:135]\n"
"s_add_u32 m0, s34, 3072\n"
"ds_read_b64_tr_b16 a[254:255], v236 offset:18432\n"
"buffer_load_dwordx4 v191, s[8:11], s32 offen lds\n"
"v_mfma_f32_16x16x32_bf16 v[212:215], a[12:15], v[36:39], v[212:215]\n"
"s_add_u32 s32, s32, s33\n"
"v_cvt_pk_bf16_f32 v222, v216, v217\n"
"v_cvt_pk_bf16_f32 v223, v218, v219\n"
"v_mfma_f32_16x16x32_bf16 v[216:219], a[4:7], v[32:35], 0\n"
"s_add_u32 s78, s78, s33\n"
"ds_read_b64_tr_b16 v[136:137], v184 offset:72\n"
"v_max_f32 v132, v132, v140\n"
"v_mfma_f32_16x16x32_bf16 v[216:219], a[16:19], v[36:39], v[216:219]\n"
"ds_write_b128 v186, v[220:223] offset:47328\n"
"v_mfma_f32_16x16x32_bf16 v[220:223], a[8:11], v[32:35], 0\n"
"v_max_f32 v133, v133, v141\n"
"v_max_f32 v134, v134, v142\n"
"v_mfma_f32_16x16x32_bf16 v[220:223], a[20:23], v[36:39], v[220:223]\n"
"v_max_f32 v135, v135, v143\n"
"v_max3_f32 v170, v170, v131, v132\n"
"v_mfma_f32_16x16x32_bf16 v[212:215], a[24:27], v[40:43], v[212:215]\n"
"ds_read_b64_tr_b16 v[138:139], v185 offset:72\n"
"ds_read_b64_tr_b16 v[140:141], v184 offset:840\n"
"v_mfma_f32_16x16x32_bf16 v[216:219], a[28:31], v[40:43], v[216:219]\n"
"s_waitcnt lgkmcnt(9)\n"
"ds_read_b64_tr_b16 v[142:143], v185 offset:840\n"
"v_max3_f32 v170, v170, v133, v134\n"
"v_mfma_f32_16x16x32_bf16 v[220:223], a[32:35], v[40:43], v[220:223]\n"
"s_add_u32 m0, s34, 1024\n"
"ds_write_b128 v186, v[224:227] offset:47392\n"
"v_mfma_f32_16x16x32_bf16 v[212:215], a[36:39], v[44:47], v[212:215]\n"
"buffer_load_dwordx4 v190, s[8:11], s0 offen lds\n"
"v_mov_b64 v[252:253], 0\n"
"v_mfma_f32_16x16x32_bf16 v[216:219], a[40:43], v[44:47], v[216:219]\n"
"v_mov_b64 v[254:255], 0\n"
"v_mov_b64 v[98:99], 0\n"
"v_mfma_f32_16x16x32_bf16 v[220:223], a[44:47], v[44:47], v[220:223]\n"
"ds_read_b64_tr_b16 v[0:1], v236 offset:12296\n"
"v_mov_b64 v[100:101], 0\n"
"v_mfma_f32_16x16x32_bf16 v[212:215], a[48:51], v[48:51], v[212:215]\n"
"ds_read_b64_tr_b16 v[2:3], v236 offset:18440\n"
"v_mov_b64 v[106:107], 0\n"
"v_mfma_f32_16x16x32_bf16 v[216:219], a[52:55], v[48:51], v[216:219]\n"
"v_mov_b64 v[108:109], 0\n"
"v_max_f32 v171, v170, v135\n"
"v_mfma_f32_16x16x32_bf16 v[220:223], a[56:59], v[48:51], v[220:223]\n"
"ds_read_b64_tr_b16 v[4:5], v235 offset:12288\n"
"v_max_f32 v170, v170, v135\n"
"v_mfma_f32_16x16x32_bf16 v[212:215], a[60:63], v[52:55], v[212:215]\n"
"s_nop 0\n"
"v_permlane16_swap_b32 v170, v171\n"
"v_mfma_f32_16x16x32_bf16 v[216:219], a[64:67], v[52:55], v[216:219]\n"
"ds_read_b64_tr_b16 v[6:7], v235 offset:18432\n"
"v_max_f32 v170, v170, v171\n"
"v_mfma_f32_16x16x32_bf16 v[220:223], a[68:71], v[52:55], v[220:223]\n"
"s_add_u32 m0, s34, 4096\n"
"v_mov_b32 v171, v170\n"
"v_cvt_pk_bf16_f32 v152, v228, v229\n"
"v_mfma_f32_16x16x32_bf16 v[224:227], a[72:75], v[32:35], 0\n"
"buffer_load_dwordx4 v191, s[8:11], s0 offen lds\n"
"v_permlane32_swap_b32 v170, v171\n"
"v_mfma_f32_16x16x32_bf16 v[224:227], a[84:87], v[36:39], v[224:227]\n"
"s_add_u32 s0, s0, s33\n"
"v_max_f32 v170, v170, v171\n"
"v_mul_f32 v170, 0xbe553b95, v170\n"
"v_mfma_f32_16x16x32_bf16 v[224:227], a[96:99], v[40:43], v[224:227]\n"
"v_fmamk_f32 v112, v112, 0x3e553b95, v170\n"
"v_fmamk_f32 v113, v113, 0x3e553b95, v170\n"
"v_mfma_f32_16x16x32_bf16 v[224:227], a[108:111], v[44:47], v[224:227]\n"
"v_fmamk_f32 v114, v114, 0x3e553b95, v170\n"
"v_fmamk_f32 v115, v115, 0x3e553b95, v170\n"
"v_mfma_f32_16x16x32_bf16 v[224:227], a[120:123], v[48:51], v[224:227]\n"
"v_exp_f32 v112, v112\n"
"v_mfma_f32_16x16x32_bf16 v[224:227], a[132:135], v[52:55], v[224:227]\n"
"v_exp_f32 v113, v113\n"
"v_exp_f32 v114, v114\n"
"v_exp_f32 v115, v115\n"
"v_cvt_pk_bf16_f32 v82, v112, v114\n"
"s_add_u32 m0, s34, 2048\n"
"v_cvt_pk_bf16_f32 v83, v113, v115\n"
"buffer_load_dwordx4 v190, s[8:11], s1 offen lds\n"
"v_fmamk_f32 v116, v116, 0x3e553b95, v170\n"
"v_fmamk_f32 v117, v117, 0x3e553b95, v170\n"
"v_fmamk_f32 v118, v118, 0x3e553b95, v170\n"
"v_fmamk_f32 v119, v119, 0x3e553b95, v170\n"
"v_exp_f32 v116, v116\n"
"v_exp_f32 v117, v117\n"
"v_exp_f32 v118, v118\n"
"v_exp_f32 v119, v119\n"
"v_cvt_pk_bf16_f32 v84, v116, v118\n"
"v_cvt_pk_bf16_f32 v85, v117, v119\n"
"v_cvt_pk_bf16_f32 v153, v230, v231\n"
"v_mfma_f32_16x16x32_bf16 v[228:231], a[76:79], v[32:35], 0\n"
"s_waitcnt vmcnt(13) lgkmcnt(0)\n"
"ds_read_b64_tr_b16 v[112:113], v182 offset:13632\n"
"ds_read_b64_tr_b16 v[114:115], v183 offset:13632\n"
"v_smfmac_f32_16x16x64_bf16 v[252:255], v[82:85], v[244:251], v66\n"
"s_barrier\n"
"v_smfmac_f32_16x16x64_bf16 v[98:101], v[82:85], v[120:127], v66\n"
"ds_read_b64_tr_b16 v[116:117], v182 offset:14400\n"
"v_smfmac_f32_16x16x64_bf16 v[106:109], v[82:85], v[136:143], v66\n"
"s_add_u32 m0, s79, 142464\n"
"s_nop 0\n"
"buffer_load_dwordx4 v192, s[16:19], s43 offen lds\n"
"v_mfma_f32_16x16x32_bf16 v[82:85], v[172:175], v[82:85], 0\n"
"ds_read_b64_tr_b16 v[118:119], v183 offset:14400\n"
"ds_read_b64_tr_b16 v[244:245], v182 offset:13640\n"
"v_mfma_f32_16x16x32_bf16 v[228:231], a[88:91], v[36:39], v[228:231]\n"
"ds_read_b64_tr_b16 v[246:247], v183 offset:13640\n"
"v_cvt_pk_bf16_f32 v154, v204, v205\n"
"v_mfma_f32_16x16x32_bf16 v[228:231], a[100:103], v[40:43], v[228:231]\n"
"ds_read_b64_tr_b16 v[248:249], v182 offset:14408\n"
"v_cvt_pk_bf16_f32 v155, v206, v207\n"
"v_mfma_f32_16x16x32_bf16 v[204:207], a[80:83], v[32:35], 0\n"
"ds_read_b64_tr_b16 v[250:251], v183 offset:14408\n"
"v_fmamk_f32 v128, v128, 0x3e553b95, v170\n"
"v_mfma_f32_16x16x32_bf16 v[204:207], a[92:95], v[36:39], v[204:207]\n"
"ds_read_b64_tr_b16 v[120:121], v182 offset:13576\n"
"v_fmamk_f32 v129, v129, 0x3e553b95, v170\n"
"v_mfma_f32_16x16x32_bf16 v[204:207], a[104:107], v[40:43], v[204:207]\n"
"ds_read_b64_tr_b16 v[122:123], v183 offset:13576\n"
"v_cvt_pk_bf16_f32 v166, v208, v209\n"
"v_mfma_f32_16x16x32_bf16 v[228:231], a[112:115], v[44:47], v[228:231]\n"
"ds_read_b64_tr_b16 v[124:125], v182 offset:14344\n"
"v_fmamk_f32 v130, v130, 0x3e553b95, v170\n"
"v_mfma_f32_16x16x32_bf16 v[204:207], a[116:119], v[44:47], v[204:207]\n"
"s_add_u32 m0, s79, 142464\n"
"ds_read_b64_tr_b16 v[126:127], v183 offset:14344\n"
"buffer_load_dwordx4 v192, s[16:19], s43 offen offset:1024 lds\n"
"v_mfma_f32_16x16x32_bf16 v[228:231], a[124:127], v[48:51], v[228:231]\n"
"v_cvt_pk_bf16_f32 v167, v210, v211\n"
"v_fmamk_f32 v131, v131, 0x3e553b95, v170\n"
"v_mfma_f32_16x16x32_bf16 v[204:207], a[128:131], v[48:51], v[204:207]\n"
"v_exp_f32 v128, v128\n"
"v_mfma_f32_16x16x32_bf16 v[228:231], a[136:139], v[52:55], v[228:231]\n"
"v_exp_f32 v129, v129\n"
"v_mfma_f32_16x16x32_bf16 v[204:207], a[140:143], v[52:55], v[204:207]\n"
"ds_read_b128 v[136:139], v178\n"
"v_fmamk_f32 v132, v132, 0x3e553b95, v170\n"
"v_mfma_f32_16x16x32_bf16 v[208:211], a[144:147], v[32:35], 0\n"
"v_exp_f32 v130, v130\n"
"v_mfma_f32_16x16x32_bf16 v[208:211], a[156:159], v[36:39], v[208:211]\n"
"v_exp_f32 v131, v131\n"
"v_mfma_f32_16x16x32_bf16 v[208:211], a[168:171], v[40:43], v[208:211]\n"
"v_cvt_pk_bf16_f32 v90, v128, v130\n"
"v_cvt_pk_bf16_f32 v91, v129, v131\n"
"v_mfma_f32_16x16x32_bf16 v[208:211], a[180:183], v[44:47], v[208:211]\n"
"s_add_u32 m0, s79, 142464\n"
"ds_read_b128 v[128:131], v179\n"
"buffer_load_dwordx4 v192, s[16:19], s43 offen offset:2048 lds\n"
"v_mfma_f32_16x16x32_bf16 v[208:211], a[192:195], v[48:51], v[208:211]\n"
"v_fmamk_f32 v133, v133, 0x3e553b95, v170\n"
"v_fmamk_f32 v134, v134, 0x3e553b95, v170\n"
"v_mfma_f32_16x16x32_bf16 v[208:211], a[204:207], v[52:55], v[208:211]\n"
"v_fmamk_f32 v135, v135, 0x3e553b95, v170\n"
"v_exp_f32 v132, v132\n"
"v_exp_f32 v133, v133\n"
"v_exp_f32 v134, v134\n"
"v_exp_f32 v135, v135\n"
"v_cvt_pk_bf16_f32 v92, v132, v134\n"
"v_cvt_pk_bf16_f32 v93, v133, v135\n"
"s_mov_b64 exec, s[44:45]\n"
"v_add_u32 v181, 0xc0, v181\n"
"s_mov_b64 exec, -1\n"
"v_cvt_pk_bf16_f32 v212, v212, v213\n"
"v_cvt_pk_bf16_f32 v213, v214, v215\n"
"v_cvt_pk_bf16_f32 v214, v216, v217\n"
"v_cvt_pk_bf16_f32 v215, v218, v219\n"
"v_mfma_f32_16x16x32_bf16 v[216:219], a[148:151], v[32:35], 0\n"
"s_waitcnt lgkmcnt(1)\n"
"ds_read_b128 v[140:143], v178 offset:13504\n"
"ds_read_b128 v[132:135], v179 offset:13504\n"
"v_smfmac_f32_16x16x64_bf16 v[252:255], v[90:93], v[112:119], v66\n"
"s_waitcnt lgkmcnt(2)\n"
"ds_write_b128 v186, v[212:215] offset:60768\n"
"v_smfmac_f32_16x16x64_bf16 v[98:101], v[90:93], v[244:251], v66\n"
"s_add_u32 m0, s34, 5120\n"
"s_nop 0\n"
"buffer_load_dwordx4 v191, s[8:11], s1 offen lds\n"
"v_smfmac_f32_16x16x64_bf16 v[106:109], v[90:93], v[120:127], v66\n"
"s_add_u32 s1, s1, s33\n"
"ds_read_b128 v[112:115], v178 offset:27008\n"
"v_mfma_f32_16x16x32_bf16 v[90:93], v[172:175], v[90:93], v[82:85]\n"
"s_waitcnt vmcnt(14)\n"
"v_readfirstlane_b32 s6, v201\n"
"v_cvt_pk_bf16_f32 v252, v252, v253\n"
"v_mfma_f32_16x16x32_bf16 v[136:139], v[136:139], v[144:147], 0\n"
"s_and_b32 s6, s6, 0xffffff00\n"
"v_or_b32 v201, 4, s6\n"
"v_cvt_pk_bf16_f32 v253, v254, v255\n"
"v_mfma_f32_16x16x32_bf16 v[128:131], v[128:131], v[144:147], 0\n"
"s_mov_b64 exec, 1\n"
"buffer_store_dword v201, off, s[28:31], s40 sc1\n"
"s_mov_b64 exec, -1\n"
"v_mfma_f32_16x16x32_bf16 v[212:215], a[152:155], v[32:35], 0\n"
"s_waitcnt lgkmcnt(2)\n"
"ds_read_b128 v[32:35], v188 offset:6144\n"
"ds_read_b64 v[82:83], v178 offset:64\n"
"v_mfma_f32_16x16x32_bf16 v[140:143], v[140:143], v[144:147], 0\n"
"s_waitcnt vmcnt(14)\n"
"ds_read_b64 v[84:85], v179 offset:64\n"
"v_cvt_pk_bf16_f32 v98, v98, v99\n"
"v_mfma_f32_16x16x32_bf16 v[132:135], v[132:135], v[144:147], 0\n"
"ds_read_b64 v[92:93], v179 offset:13568\n"
"v_cvt_pk_bf16_f32 v99, v100, v101\n"
"v_mfma_f32_16x16x32_bf16 v[216:219], a[160:163], v[36:39], v[216:219]\n"
"s_waitcnt lgkmcnt(4)\n"
"ds_write_b128 v202, v[56:59] offset:55296\n"
"v_mfma_f32_16x16x32_bf16 v[112:115], v[112:115], v[144:147], 0\n"
"buffer_load_dwordx4 v[56:59], v192, s[28:31], s38 offen sc1\n"
"v_cvt_pk_bf16_f32 v106, v106, v107\n"
"v_mfma_f32_16x16x32_bf16 v[212:215], a[164:167], v[36:39], v[212:215]\n"
"v_rcp_f32 v171, v90\n"
"v_mfma_f32_16x16x32_bf16 v[216:219], a[172:175], v[40:43], v[216:219]\n"
"ds_read_b128 v[36:39], v189 offset:6144\n"
"ds_read_b64 v[90:91], v178 offset:13568\n"
"v_mfma_f32_16x16x32_bf16 v[212:215], a[176:179], v[40:43], v[212:215]\n"
"ds_read_b128 v[40:43], v188 offset:7168\n"
"v_cvt_pk_bf16_f32 v171, v171, v171\n"
"v_mfma_f32_16x16x32_bf16 v[216:219], a[184:187], v[44:47], v[216:219]\n"
"s_waitcnt lgkmcnt(6)\n"
"ds_read_b128 v[244:247], v179 offset:27008\n"
"v_and_b32 v64, v171, v176\n"
"v_mfma_f32_16x16x32_bf16 v[136:139], v[80:83], v[156:159], v[136:139]\n"
"s_waitcnt lgkmcnt(6)\n"
"ds_read_b128 v[116:119], v180\n"
"v_and_b32 v65, v171, v177\n"
"v_mfma_f32_16x16x32_bf16 v[128:131], v[84:87], v[158:161], v[128:131]\n"
"s_waitcnt lgkmcnt(6)\n"
"ds_read_b128 v[248:251], v181\n"
"v_cvt_pk_bf16_f32 v107, v108, v109\n"
"v_mfma_f32_16x16x16_bf16 v[252:255], v[252:253], v[64:65], 0\n"
"ds_read_b64_tr_b16 v[8:9], v236\n"
"v_cvt_pk_bf16_f32 v220, v220, v221\n"
"v_mfma_f32_16x16x16_bf16 v[98:101], v[98:99], v[64:65], 0\n"
"s_waitcnt vmcnt(12)\n"
"v_or_b32 v201, 5, s6\n"
"v_cvt_pk_bf16_f32 v221, v222, v223\n"
"v_mfma_f32_16x16x16_bf16 v[106:109], v[106:107], v[64:65], 0\n"
"s_mov_b64 exec, 1\n"
"buffer_store_dword v201, off, s[28:31], s40 sc1\n"
"s_mov_b64 exec, -1\n"
"v_mfma_f32_16x16x32_bf16 v[132:135], v[92:95], v[158:161], v[132:135]\n"
"s_waitcnt lgkmcnt(5)\n"
"v_cvt_pk_bf16_f32 v252, v252, v253\n"
"v_cvt_pk_bf16_f32 v253, v254, v255\n"
"v_mfma_f32_16x16x32_bf16 v[140:143], v[88:91], v[156:159], v[140:143]\n"
"s_add_u32 m0, s34, 6144\n"
"v_cvt_pk_bf16_f32 v254, v98, v99\n"
"v_cvt_pk_bf16_f32 v255, v100, v101\n"
"v_mfma_f32_16x16x32_bf16 v[212:215], a[188:191], v[44:47], v[212:215]\n"
"s_waitcnt lgkmcnt(3)\n"
"buffer_load_dwordx4 v190, s[8:11], s32 offen lds\n"
"ds_read_b128 v[44:47], v189 offset:7168\n"
"v_mfma_f32_16x16x32_bf16 v[244:247], v[244:247], v[144:147], 0\n"
"s_waitcnt lgkmcnt(3)\n"
"ds_read_b64 v[98:99], v178 offset:27072\n"
"ds_read_b64 v[100:101], v179 offset:27072\n"
"v_mfma_f32_16x16x32_bf16 v[116:119], v[116:119], v[144:147], 0\n"
"s_waitcnt lgkmcnt(4)\n"
"v_cvt_pk_bf16_f32 v106, v106, v107\n"
"v_cvt_pk_bf16_f32 v107, v108, v109\n"
"v_mfma_f32_16x16x32_bf16 v[248:251], v[248:251], v[144:147], 0\n"
"buffer_store_dwordx4 v[252:255], v192, s[28:31], s37 offen sc1\n"
"v_mfma_f32_16x16x32_bf16 v[216:219], a[196:199], v[48:51], v[216:219]\n"
"ds_read_b64 v[108:109], v181 offset:64\n"
"ds_read_b64_tr_b16 v[120:121], v182 offset:128\n"
"v_mfma_f32_16x16x32_bf16 v[212:215], a[200:203], v[48:51], v[212:215]\n"
"ds_read_b128 v[48:51], v188 offset:8192\n"
"v_max_f32 v136, v136, v128\n"
"v_mfma_f32_16x16x32_bf16 v[216:219], a[208:211], v[52:55], v[216:219]\n"
"ds_read_b64_tr_b16 v[122:123], v183 offset:128\n"
"buffer_store_dwordx2 v[106:107], v193, s[28:31], s37 offen offset:1024 sc1\n"
"v_mfma_f32_16x16x32_bf16 v[212:215], a[212:215], v[52:55], v[212:215]\n"
"s_add_u32 s37, s37, 1572864\n"
"ds_read_b128 v[52:55], v189 offset:8192\n"
"ds_read_b64 v[106:107], v180 offset:64\n"
"v_max_f32 v137, v137, v129\n"
"v_max_f32 v138, v138, v130\n"
"v_max_f32 v139, v139, v131\n"
"v_max3_f32 v170, v136, v137, v138\n"
"v_max_f32 v140, v140, v132\n"
"v_max_f32 v141, v141, v133\n"
"v_max_f32 v142, v142, v134\n"
"v_max_f32 v143, v143, v135\n"
"v_max3_f32 v170, v170, v139, v140\n"
"v_max3_f32 v170, v170, v141, v142\n"
"v_cvt_pk_bf16_f32 v224, v224, v225\n"
"v_cvt_pk_bf16_f32 v225, v226, v227\n"
"v_cvt_pk_bf16_f32 v226, v228, v229\n"
"v_cvt_pk_bf16_f32 v227, v230, v231\n"
"v_mfma_f32_16x16x32_bf16 v[228:231], a[0:3], v[32:35], 0\n"
"s_waitcnt lgkmcnt(5)\n"
"ds_read_b64_tr_b16 v[124:125], v182 offset:896\n"
"ds_read_b64_tr_b16 v[126:127], v183 offset:896\n"
"v_mfma_f32_16x16x32_bf16 v[112:115], v[96:99], v[156:159], v[112:115]\n"
"s_add_u32 m0, s34, 9216\n"
"ds_write_b64 v203, v[60:61] offset:55296\n"
"v_mfma_f32_16x16x32_bf16 v[244:247], v[100:103], v[158:161], v[244:247]\n"
"buffer_load_dwordx4 v191, s[8:11], s32 offen lds\n"
"v_cvt_pk_bf16_f32 v222, v204, v205\n"
"v_mfma_f32_16x16x32_bf16 v[248:251], v[108:111], v[158:161], v[248:251]\n"
"s_add_u32 s32, s32, s33\n"
"ds_read_b64_tr_b16 v[128:129], v182 offset:136\n"
"ds_read_b64_tr_b16 v[130:131], v183 offset:136\n"
"v_mfma_f32_16x16x32_bf16 v[228:231], a[12:15], v[36:39], v[228:231]\n"
"s_waitcnt lgkmcnt(5)\n"
"ds_read_b64_tr_b16 v[132:133], v182 offset:904\n"
"v_max_f32 v112, v112, v244\n"
"v_mfma_f32_16x16x32_bf16 v[228:231], a[24:27], v[40:43], v[228:231]\n"
"buffer_load_dwordx2 v[60:61], v193, s[28:31], s38 offen offset:1024 sc1\n"
"ds_read_b64_tr_b16 v[134:135], v183 offset:904\n"
"v_mfma_f32_16x16x32_bf16 v[116:119], v[104:107], v[156:159], v[116:119]\n"
"s_add_u32 s38, s38, 1572864\n"
"ds_read_b64_tr_b16 v[10:11], v236 offset:6144\n"
"v_max_f32 v113, v113, v245\n"
"v_mfma_f32_16x16x32_bf16 v[228:231], a[36:39], v[44:47], v[228:231]\n"
"v_max_f32 v114, v114, v246\n"
"v_max_f32 v115, v115, v247\n"
"v_mfma_f32_16x16x32_bf16 v[228:231], a[48:51], v[48:51], v[228:231]\n"
"ds_read_b64_tr_b16 v[244:245], v182 offset:72\n"
"v_max3_f32 v170, v170, v143, v112\n"
"v_mfma_f32_16x16x32_bf16 v[228:231], a[60:63], v[52:55], v[228:231]\n"
"s_add_u32 m0, s34, 7168\n"
"v_max3_f32 v170, v170, v113, v114\n"
"buffer_load_dwordx4 v190, s[8:11], s0 offen lds\n"
"v_max_f32 v116, v116, v248\n"
"v_max_f32 v117, v117, v249\n"
"v_max_f32 v118, v118, v250\n"
"v_cvt_pk_bf16_f32 v223, v206, v207\n"
"v_mfma_f32_16x16x32_bf16 v[204:207], a[4:7], v[32:35], 0\n"
"ds_write_b128 v186, v[220:223] offset:60832\n"
"v_mfma_f32_16x16x32_bf16 v[220:223], a[8:11], v[32:35], 0\n"
"v_max_f32 v119, v119, v251\n"
"v_max3_f32 v170, v170, v115, v116\n"
"v_mfma_f32_16x16x32_bf16 v[204:207], a[16:19], v[36:39], v[204:207]\n"
"v_max3_f32 v170, v170, v117, v118\n"
"v_mov_b64 v[252:253], 0\n"
"v_mfma_f32_16x16x32_bf16 v[220:223], a[20:23], v[36:39], v[220:223]\n"
"ds_read_b64_tr_b16 v[246:247], v183 offset:72\n"
"ds_read_b64_tr_b16 v[248:249], v182 offset:840\n"
"v_mfma_f32_16x16x32_bf16 v[204:207], a[28:31], v[40:43], v[204:207]\n"
"ds_read_b64_tr_b16 v[250:251], v183 offset:840\n"
"v_mov_b64 v[254:255], 0\n"
"v_mfma_f32_16x16x32_bf16 v[220:223], a[32:35], v[40:43], v[220:223]\n"
"s_add_u32 m0, s34, 10240\n"
"ds_write_b128 v186, v[224:227] offset:60896\n"
"v_mfma_f32_16x16x32_bf16 v[204:207], a[40:43], v[44:47], v[204:207]\n"
"buffer_load_dwordx4 v191, s[8:11], s0 offen lds\n"
"v_mov_b64 v[98:99], 0\n"
"v_mfma_f32_16x16x32_bf16 v[220:223], a[44:47], v[44:47], v[220:223]\n"
"s_add_u32 s0, s0, s33\n"
"v_mov_b64 v[100:101], 0\n"
"v_mov_b64 v[106:107], 0\n"
"v_mfma_f32_16x16x32_bf16 v[204:207], a[52:55], v[48:51], v[204:207]\n"
"ds_read_b64_tr_b16 v[12:13], v236 offset:8\n"
"v_mov_b64 v[108:109], 0\n"
"v_mfma_f32_16x16x32_bf16 v[220:223], a[56:59], v[48:51], v[220:223]\n"
"ds_read_b64_tr_b16 v[14:15], v236 offset:6152\n"
"v_max_f32 v171, v170, v119\n"
"v_mfma_f32_16x16x32_bf16 v[204:207], a[64:67], v[52:55], v[204:207]\n"
"v_max_f32 v170, v170, v119\n"
"v_cvt_pk_bf16_f32 v144, v208, v209\n"
"v_mfma_f32_16x16x32_bf16 v[220:223], a[68:71], v[52:55], v[220:223]\n"
"ds_read_b64_tr_b16 v[16:17], v235\n"
"v_permlane16_swap_b32 v170, v171\n"
"v_mfma_f32_16x16x32_bf16 v[224:227], a[72:75], v[32:35], 0\n"
"v_max_f32 v170, v170, v171\n"
"v_mov_b32 v171, v170\n"
"v_mfma_f32_16x16x32_bf16 v[224:227], a[84:87], v[36:39], v[224:227]\n"
"s_add_u32 m0, s34, 8192\n"
"ds_read_b64_tr_b16 v[18:19], v235 offset:6144\n"
"buffer_load_dwordx4 v190, s[8:11], s1 offen lds\n"
"v_mfma_f32_16x16x32_bf16 v[224:227], a[96:99], v[40:43], v[224:227]\n"
"v_permlane32_swap_b32 v170, v171\n"
"v_mfma_f32_16x16x32_bf16 v[224:227], a[108:111], v[44:47], v[224:227]\n"
"v_max_f32 v170, v170, v171\n"
"v_mul_f32 v170, 0xbe553b95, v170\n"
"v_mfma_f32_16x16x32_bf16 v[224:227], a[120:123], v[48:51], v[224:227]\n"
"v_fmamk_f32 v136, v136, 0x3e553b95, v170\n"
"v_fmamk_f32 v137, v137, 0x3e553b95, v170\n"
"v_mfma_f32_16x16x32_bf16 v[224:227], a[132:135], v[52:55], v[224:227]\n"
"v_fmamk_f32 v138, v138, 0x3e553b95, v170\n"
"v_fmamk_f32 v139, v139, 0x3e553b95, v170\n"
"v_exp_f32 v136, v136\n"
"v_exp_f32 v137, v137\n"
"v_exp_f32 v138, v138\n"
"v_exp_f32 v139, v139\n"
"v_cvt_pk_bf16_f32 v82, v136, v138\n"
"v_cvt_pk_bf16_f32 v83, v137, v139\n"
"v_fmamk_f32 v140, v140, 0x3e553b95, v170\n"
"s_add_u32 m0, s34, 11264\n"
"v_fmamk_f32 v141, v141, 0x3e553b95, v170\n"
"buffer_load_dwordx4 v191, s[8:11], s1 offen lds\n"
"s_add_u32 s1, s1, s33\n"
"v_fmamk_f32 v142, v142, 0x3e553b95, v170\n"
"v_fmamk_f32 v143, v143, 0x3e553b95, v170\n"
"v_exp_f32 v140, v140\n"
"v_exp_f32 v141, v141\n"
"v_exp_f32 v142, v142\n"
"v_exp_f32 v143, v143\n"
"v_cvt_pk_bf16_f32 v84, v140, v142\n"
"v_cvt_pk_bf16_f32 v85, v141, v143\n"
"v_fmamk_f32 v112, v112, 0x3e553b95, v170\n"
"v_fmamk_f32 v113, v113, 0x3e553b95, v170\n"
"v_fmamk_f32 v114, v114, 0x3e553b95, v170\n"
"v_fmamk_f32 v115, v115, 0x3e553b95, v170\n"
"v_fmamk_f32 v116, v116, 0x3e553b95, v170\n"
"v_fmamk_f32 v117, v117, 0x3e553b95, v170\n"
"v_fmamk_f32 v118, v118, 0x3e553b95, v170\n"
"v_fmamk_f32 v119, v119, 0x3e553b95, v170\n"
"v_exp_f32 v112, v112\n"
"v_exp_f32 v113, v113\n"
"v_exp_f32 v114, v114\n"
"v_exp_f32 v115, v115\n"
"v_exp_f32 v116, v116\n"
"v_exp_f32 v117, v117\n"
"v_exp_f32 v118, v118\n"
"v_exp_f32 v119, v119\n"
"v_cvt_pk_bf16_f32 v90, v112, v114\n"
"v_cvt_pk_bf16_f32 v91, v113, v115\n"
"v_cvt_pk_bf16_f32 v92, v116, v118\n"
"v_cvt_pk_bf16_f32 v93, v117, v119\n"
"v_cvt_pk_bf16_f32 v145, v210, v211\n"
"v_mfma_f32_16x16x32_bf16 v[208:211], a[76:79], v[32:35], 0\n"
"s_waitcnt vmcnt(13) lgkmcnt(0)\n"
"ds_read_b64_tr_b16 v[136:137], v182 offset:27136\n"
"ds_read_b64_tr_b16 v[138:139], v183 offset:27136\n"
"v_smfmac_f32_16x16x64_bf16 v[252:255], v[82:85], v[120:127], v66\n"
"s_barrier\n"
"v_smfmac_f32_16x16x64_bf16 v[98:101], v[82:85], v[128:135], v66\n"
"ds_read_b128 v[76:79], v192 offset:55296\n"
"v_smfmac_f32_16x16x64_bf16 v[106:109], v[82:85], v[244:251], v66\n"
"ds_read_b64_tr_b16 v[140:141], v182 offset:27904\n"
"v_mfma_f32_16x16x32_bf16 v[82:85], v[172:175], v[82:85], 0\n"
"ds_read_b64_tr_b16 v[142:143], v183 offset:27904\n"
"ds_read_b64_tr_b16 v[120:121], v182 offset:27144\n"
"v_mfma_f32_16x16x32_bf16 v[208:211], a[88:91], v[36:39], v[208:211]\n"
"ds_read_b64_tr_b16 v[122:123], v183 offset:27144\n"
"v_cvt_pk_bf16_f32 v146, v216, v217\n"
"v_mfma_f32_16x16x32_bf16 v[208:211], a[100:103], v[40:43], v[208:211]\n"
"ds_read_b64_tr_b16 v[124:125], v182 offset:27912\n"
"v_cvt_pk_bf16_f32 v147, v218, v219\n"
"v_mfma_f32_16x16x32_bf16 v[216:219], a[80:83], v[32:35], 0\n"
"ds_read_b64_tr_b16 v[126:127], v183 offset:27912\n"
"v_cvt_pk_bf16_f32 v158, v212, v213\n"
"v_mfma_f32_16x16x32_bf16 v[216:219], a[92:95], v[36:39], v[216:219]\n"
"ds_read_b64_tr_b16 v[128:129], v182 offset:27080\n"
"v_cvt_pk_bf16_f32 v159, v214, v215\n"
"v_mfma_f32_16x16x32_bf16 v[216:219], a[104:107], v[40:43], v[216:219]\n"
"ds_read_b64_tr_b16 v[130:131], v183 offset:27080\n"
"s_mov_b64 exec, s[46:47]\n"
"v_add_u32 v185, 0xc0, v185\n"
"s_mov_b64 exec, -1\n"
"v_mfma_f32_16x16x32_bf16 v[208:211], a[112:115], v[44:47], v[208:211]\n"
"s_waitcnt lgkmcnt(8)\n"
"ds_read_b64_tr_b16 v[132:133], v182 offset:27848\n"
"ds_read_b64_tr_b16 v[134:135], v183 offset:27848\n"
"v_mfma_f32_16x16x32_bf16 v[216:219], a[116:119], v[44:47], v[216:219]\n"
"s_waitcnt lgkmcnt(8)\n"
"v_cvt_pk_bf16_f32 v228, v228, v229\n"
"v_cvt_pk_bf16_f32 v229, v230, v231\n"
"v_mfma_f32_16x16x32_bf16 v[208:211], a[124:127], v[48:51], v[208:211]\n"
"ds_read_b128 v[244:247], v178 offset:13504\n"
"ds_read_b128 v[112:115], v179 offset:13504\n"
"v_smfmac_f32_16x16x64_bf16 v[252:255], v[90:93], v[136:143], v66\n"
"ds_read_b128 v[248:251], v178 offset:27008\n"
"v_mfma_f32_16x16x32_bf16 v[216:219], a[128:131], v[48:51], v[216:219]\n"
"v_cvt_pk_bf16_f32 v230, v204, v205\n"
"v_cvt_pk_bf16_f32 v231, v206, v207\n"
"v_mfma_f32_16x16x32_bf16 v[208:211], a[136:139], v[52:55], v[208:211]\n"
"s_waitcnt lgkmcnt(7)\n"
"ds_read_b128 v[116:119], v179 offset:27008\n"
"ds_write_b128 v187, v[228:231]\n"
"v_smfmac_f32_16x16x64_bf16 v[98:101], v[90:93], v[120:127], v66\n"
"buffer_load_dword v200, off, s[28:31], s39 sc1\n"
"v_mfma_f32_16x16x32_bf16 v[216:219], a[140:143], v[52:55], v[216:219]\n"
"v_cvt_pk_bf16_f32 v252, v252, v253\n"
"v_cvt_pk_bf16_f32 v253, v254, v255\n"
"v_mfma_f32_16x16x32_bf16 v[240:243], a[224:227], v[76:79], 0\n"
"s_waitcnt lgkmcnt(5)\n"
"ds_read_b128 v[136:139], v180\n"
"ds_read_b128 v[120:123], v181\n"
"v_mfma_f32_16x16x32_bf16 v[212:215], a[144:147], v[32:35], 0\n"
"s_waitcnt lgkmcnt(6)\n"
"v_cvt_pk_bf16_f32 v98, v98, v99\n"
"v_cvt_pk_bf16_f32 v99, v100, v101\n"
"v_smfmac_f32_16x16x64_bf16 v[106:109], v[90:93], v[128:135], v66\n"
"s_waitcnt vmcnt(11) lgkmcnt(5)\n"
"ds_read_b128 v[140:143], v180 offset:13504\n"
"v_mfma_f32_16x16x32_bf16 v[90:93], v[172:175], v[90:93], v[82:85]\n"
"s_waitcnt lgkmcnt(5)\n"
"ds_write_b128 v202, v[56:59] offset:36864\n"
"v_mfma_f32_16x16x32_bf16 v[244:247], v[244:247], v[148:151], 0\n"
"s_waitcnt lgkmcnt(5)\n"
"v_cvt_pk_bf16_f32 v220, v220, v221\n"
"v_cvt_pk_bf16_f32 v106, v106, v107\n"
"v_mfma_f32_16x16x32_bf16 v[112:115], v[112:115], v[148:151], 0\n"
"v_cvt_pk_bf16_f32 v107, v108, v109\n"
"v_rcp_f32 v171, v90\n"
"v_mfma_f32_16x16x32_bf16 v[248:251], v[248:251], v[148:151], 0\n"
"v_cvt_pk_bf16_f32 v171, v171, v171\n"
"v_and_b32 v64, v171, v176\n"
"v_mfma_f32_16x16x32_bf16 v[116:119], v[116:119], v[148:151], 0\n"
"s_waitcnt lgkmcnt(3)\n"
"v_and_b32 v65, v171, v177\n"
"v_cvt_pk_bf16_f32 v221, v222, v223\n"
"v_mfma_f32_16x16x32_bf16 v[136:139], v[136:139], v[148:151], 0\n"
"s_waitcnt lgkmcnt(2)\n"
"ds_read_b64 v[82:83], v178 offset:13568\n"
"ds_read_b64 v[84:85], v179 offset:13568\n"
"v_mfma_f32_16x16x16_bf16 v[252:255], v[252:253], v[64:65], 0\n"
"s_waitcnt lgkmcnt(3)\n"
"ds_read_b64 v[90:91], v178 offset:27072\n"
"v_cvt_pk_bf16_f32 v224, v224, v225\n"
"v_mfma_f32_16x16x16_bf16 v[98:101], v[98:99], v[64:65], 0\n"
"ds_read_b64 v[92:93], v179 offset:27072\n"
"v_cvt_pk_bf16_f32 v225, v226, v227\n"
"v_mfma_f32_16x16x16_bf16 v[106:109], v[106:107], v[64:65], 0\n"
"ds_read_b128 v[124:127], v181 offset:13504\n"
"v_cvt_pk_bf16_f32 v252, v252, v253\n"
"v_mfma_f32_16x16x32_bf16 v[120:123], v[120:123], v[148:151], 0\n"
"v_cvt_pk_bf16_f32 v253, v254, v255\n"
"v_cvt_pk_bf16_f32 v226, v208, v209\n"
"v_mfma_f32_16x16x32_bf16 v[140:143], v[140:143], v[148:151], 0\n"
"v_cvt_pk_bf16_f32 v254, v98, v99\n"
"v_cvt_pk_bf16_f32 v255, v100, v101\n"
"v_mfma_f32_16x16x32_bf16 v[204:207], a[148:151], v[32:35], 0\n"
"buffer_store_dwordx4 v[252:255], v192, s[28:31], s37 offen sc1\n"
"v_mfma_f32_16x16x32_bf16 v[228:231], a[152:155], v[32:35], 0\n"
"s_waitcnt lgkmcnt(4)\n"
"ds_read_b128 v[32:35], v188\n"
"v_cvt_pk_bf16_f32 v106, v106, v107\n"
"v_mfma_f32_16x16x32_bf16 v[244:247], v[80:83], v[160:163], v[244:247]\n"
"s_waitcnt lgkmcnt(4)\n"
"v_cvt_pk_bf16_f32 v107, v108, v109\n"
"v_cvt_pk_bf16_f32 v227, v210, v211\n"
"v_mfma_f32_16x16x32_bf16 v[112:115], v[84:87], v[162:165], v[112:115]\n"
"s_waitcnt lgkmcnt(3)\n"
"buffer_store_dwordx2 v[106:107], v193, s[28:31], s37 offen offset:1024 sc1\n"
"v_mfma_f32_16x16x32_bf16 v[248:251], v[88:91], v[160:163], v[248:251]\n"
"s_waitcnt lgkmcnt(2)\n"
"ds_read_b64 v[98:99], v180 offset:64\n"
"ds_read_b64 v[100:101], v181 offset:64\n"
"v_mfma_f32_16x16x32_bf16 v[116:119], v[92:95], v[162:165], v[116:119]\n"
"s_waitcnt lgkmcnt(3)\n"
"ds_read_b64 v[106:107], v180 offset:13568\n"
"v_max_f32 v244, v244, v112\n"
"v_mfma_f32_16x16x32_bf16 v[124:127], v[124:127], v[148:151], 0\n"
"s_add_u32 s37, s37, 1572864\n"
"ds_read_b64 v[108:109], v181 offset:13568\n"
"v_max_f32 v245, v245, v113\n"
"v_mfma_f32_16x16x32_bf16 v[212:215], a[156:159], v[36:39], v[212:215]\n"
"s_add_u32 s4, s37, 6291456\n"
"ds_read_b64_tr_b16 v[20:21], v236 offset:12288\n"
"v_max_f32 v246, v246, v114\n"
"v_mfma_f32_16x16x32_bf16 v[204:207], a[160:163], v[36:39], v[204:207]\n"
"s_waitcnt lgkmcnt(5)\n"
"v_max_f32 v247, v247, v115\n"
"v_max3_f32 v170, v244, v245, v246\n"
"v_mfma_f32_16x16x32_bf16 v[228:231], a[164:167], v[36:39], v[228:231]\n"
"ds_read_b128 v[36:39], v189\n"
"v_max_f32 v248, v248, v116\n"
"v_mfma_f32_16x16x32_bf16 v[212:215], a[168:171], v[40:43], v[212:215]\n"
"ds_read_b64_tr_b16 v[22:23], v236 offset:18432\n"
"v_max_f32 v249, v249, v117\n"
"v_mfma_f32_16x16x32_bf16 v[204:207], a[172:175], v[40:43], v[204:207]\n"
"v_max_f32 v250, v250, v118\n"
"v_max_f32 v251, v251, v119\n"
"v_mfma_f32_16x16x32_bf16 v[228:231], a[176:179], v[40:43], v[228:231]\n"
"s_waitcnt lgkmcnt(6)\n"
"ds_read_b128 v[40:43], v188 offset:1024\n"
"v_max3_f32 v170, v170, v247, v248\n"
"v_mfma_f32_16x16x32_bf16 v[136:139], v[96:99], v[160:163], v[136:139]\n"
"s_waitcnt lgkmcnt(6)\n"
"ds_read_b64_tr_b16 v[24:25], v236 offset:12296\n"
"v_max3_f32 v170, v170, v249, v250\n"
"v_mfma_f32_16x16x32_bf16 v[120:123], v[100:103], v[162:165], v[120:123]\n"
"s_waitcnt lgkmcnt(6)\n"
"v_cvt_pk_bf16_f32 v222, v216, v217\n"
"v_cvt_pk_bf16_f32 v223, v218, v219\n"
"v_mfma_f32_16x16x32_bf16 v[140:143], v[104:107], v[160:163], v[140:143]\n"
"s_waitcnt lgkmcnt(5)\n"
"ds_read_b64_tr_b16 v[128:129], v182 offset:13632\n"
".Lic4%=:\n"
"s_cbranch_execz .Lib1%=\n"
"v_mov_b64 v[252:253], 0\n"
"v_mfma_f32_16x16x32_bf16 v[124:127], v[108:111], v[162:165], v[124:127]\n"
"ds_read_b64_tr_b16 v[130:131], v183 offset:13632\n"
"v_max_f32 v136, v136, v120\n"
"v_mfma_f32_16x16x32_bf16 v[212:215], a[180:183], v[44:47], v[212:215]\n"
"ds_read_b64_tr_b16 v[132:133], v182 offset:14400\n"
"v_max_f32 v137, v137, v121\n"
"v_mfma_f32_16x16x32_bf16 v[204:207], a[184:187], v[44:47], v[204:207]\n"
"s_waitcnt lgkmcnt(6)\n"
"ds_read_b64_tr_b16 v[26:27], v236 offset:18440\n"
"v_max_f32 v138, v138, v122\n"
"v_mfma_f32_16x16x32_bf16 v[236:239], a[220:223], v[76:79], 0\n"
"s_add_u32 m0, s34, 0\n"
"v_max_f32 v139, v139, v123\n"
"v_max3_f32 v170, v170, v251, v136\n"
"v_mfma_f32_16x16x32_bf16 v[228:231], a[188:191], v[44:47], v[228:231]\n"
"s_waitcnt vmcnt(7)\n"
"buffer_load_dwordx4 v190, s[8:11], s32 offen lds\n"
"ds_read_b128 v[44:47], v189 offset:1024\n"
"v_mfma_f32_16x16x32_bf16 v[212:215], a[192:195], v[48:51], v[212:215]\n"
"ds_read_b64_tr_b16 v[134:135], v183 offset:14400\n"
"v_max3_f32 v170, v170, v137, v138\n"
"v_mfma_f32_16x16x32_bf16 v[204:207], a[196:199], v[48:51], v[204:207]\n"
"ds_read_b64_tr_b16 v[28:29], v235 offset:12288\n"
"v_max_f32 v140, v140, v124\n"
"v_mfma_f32_16x16x32_bf16 v[228:231], a[200:203], v[48:51], v[228:231]\n"
"v_max_f32 v141, v141, v125\n"
"v_max_f32 v142, v142, v126\n"
"v_mfma_f32_16x16x32_bf16 v[212:215], a[204:207], v[52:55], v[212:215]\n"
"ds_read_b128 v[48:51], v188 offset:2048\n"
"v_max_f32 v143, v143, v127\n"
"v_mfma_f32_16x16x32_bf16 v[204:207], a[208:211], v[52:55], v[204:207]\n"
"ds_read_b64_tr_b16 v[30:31], v235 offset:18432\n"
"v_max3_f32 v170, v170, v139, v140\n"
"v_mfma_f32_16x16x32_bf16 v[232:235], a[216:219], v[76:79], 0\n"
"v_mov_b64 v[68:69], -1\n"
"v_mov_b64 v[70:71], -1\n"
"v_mfma_f32_16x16x32_bf16 v[228:231], a[212:215], v[52:55], v[228:231]\n"
"ds_read_b128 v[76:79], v192 offset:58368\n"
"buffer_store_dwordx4 v[68:71], v192, s[28:31], s4 offen sc1\n"
"v_mfma_f32_16x16x32_bf16 v[208:211], a[0:3], v[32:35], 0\n"
"s_waitcnt lgkmcnt(5)\n"
"ds_read_b128 v[68:71], v192 offset:56320\n"
"ds_read_b128 v[52:55], v189 offset:2048\n"
"v_mfma_f32_16x16x32_bf16 v[216:219], a[4:7], v[32:35], 0\n"
"s_add_u32 m0, s34, 3072\n"
"ds_read_b64_tr_b16 v[112:113], v182 offset:13640\n"
"buffer_load_dwordx4 v191, s[8:11], s32 offen lds\n"
"v_mfma_f32_16x16x32_bf16 v[208:211], a[12:15], v[36:39], v[208:211]\n"
"s_add_u32 s32, s32, s33\n"
"ds_read_b64_tr_b16 v[114:115], v183 offset:13640\n"
"v_max3_f32 v170, v170, v141, v142\n"
"v_mfma_f32_16x16x32_bf16 v[216:219], a[16:19], v[36:39], v[216:219]\n"
"s_add_u32 s4, s37, 6291456\n"
"ds_read_b64_tr_b16 v[116:117], v182 offset:14408\n"
"v_mov_b64 v[254:255], 0\n"
"v_mfma_f32_16x16x32_bf16 v[208:211], a[24:27], v[40:43], v[208:211]\n"
"ds_read_b64_tr_b16 v[118:119], v183 offset:14408\n"
"v_mov_b64 v[98:99], 0\n"
"v_mfma_f32_16x16x32_bf16 v[216:219], a[28:31], v[40:43], v[216:219]\n"
"ds_write_b64 v203, v[60:61] offset:36864\n"
"v_mfma_f32_16x16x32_bf16 v[208:211], a[36:39], v[44:47], v[208:211]\n"
"s_waitcnt lgkmcnt(9)\n"
"v_mov_b64 v[100:101], 0\n"
"v_mov_b64 v[106:107], 0\n"
"v_mfma_f32_16x16x32_bf16 v[216:219], a[40:43], v[44:47], v[216:219]\n"
"s_waitcnt lgkmcnt(6)\n"
"ds_write_b128 v187, v[220:223] offset:64\n"
"v_mfma_f32_16x16x32_bf16 v[232:235], a[228:231], v[68:71], v[232:235]\n"
"s_waitcnt lgkmcnt(6)\n"
"v_mov_b64 v[108:109], 0\n"
"v_max_f32 v171, v170, v143\n"
"v_mfma_f32_16x16x32_bf16 v[236:239], a[232:235], v[68:71], v[236:239]\n"
"s_add_u32 m0, s34, 1024\n"
"v_max_f32 v170, v170, v143\n"
"v_cvt_pk_bf16_f32 v148, v212, v213\n"
"v_mfma_f32_16x16x32_bf16 v[240:243], a[236:239], v[68:71], v[240:243]\n"
"buffer_load_dwordx4 v190, s[8:11], s0 offen lds\n"
"ds_read_b128 v[68:71], v192 offset:59392\n"
"v_mfma_f32_16x16x32_bf16 v[220:223], a[8:11], v[32:35], 0\n"
"ds_read_b64_tr_b16 v[120:121], v182 offset:13576\n"
"ds_read_b64_tr_b16 v[122:123], v183 offset:13576\n"
"v_mfma_f32_16x16x32_bf16 v[220:223], a[20:23], v[36:39], v[220:223]\n"
"ds_read_b64_tr_b16 v[124:125], v182 offset:14344\n"
"v_permlane16_swap_b32 v170, v171\n"
"v_mfma_f32_16x16x32_bf16 v[220:223], a[32:35], v[40:43], v[220:223]\n"
"ds_read_b64_tr_b16 v[126:127], v183 offset:14344\n"
"v_max_f32 v170, v170, v171\n"
"v_mfma_f32_16x16x32_bf16 v[220:223], a[44:47], v[44:47], v[220:223]\n"
"ds_write_b128 v187, v[224:227] offset:128\n"
"v_mfma_f32_16x16x32_bf16 v[208:211], a[48:51], v[48:51], v[208:211]\n"
"v_mov_b32 v171, v170\n"
"v_cvt_pk_bf16_f32 v149, v214, v215\n"
"v_mfma_f32_16x16x32_bf16 v[216:219], a[52:55], v[48:51], v[216:219]\n"
"v_permlane32_swap_b32 v170, v171\n"
"v_mfma_f32_16x16x32_bf16 v[220:223], a[56:59], v[48:51], v[220:223]\n"
"s_add_u32 m0, s34, 4096\n"
"v_max_f32 v170, v170, v171\n"
"v_mul_f32 v170, 0xbe553b95, v170\n"
"v_mfma_f32_16x16x32_bf16 v[208:211], a[60:63], v[52:55], v[208:211]\n"
"buffer_load_dwordx4 v191, s[8:11], s0 offen lds\n"
"v_fmamk_f32 v244, v244, 0x3e553b95, v170\n"
"v_mfma_f32_16x16x32_bf16 v[216:219], a[64:67], v[52:55], v[216:219]\n"
"s_add_u32 s0, s0, s33\n"
"v_fmamk_f32 v245, v245, 0x3e553b95, v170\n"
"v_fmamk_f32 v246, v246, 0x3e553b95, v170\n"
"v_mfma_f32_16x16x32_bf16 v[220:223], a[68:71], v[52:55], v[220:223]\n"
"v_fmamk_f32 v247, v247, 0x3e553b95, v170\n"
"v_fmamk_f32 v248, v248, 0x3e553b95, v170\n"
"v_mfma_f32_16x16x32_bf16 v[224:227], a[72:75], v[32:35], 0\n"
"v_exp_f32 v244, v244\n"
"v_mfma_f32_16x16x32_bf16 v[212:215], a[76:79], v[32:35], 0\n"
"v_exp_f32 v245, v245\n"
"v_mfma_f32_16x16x32_bf16 v[224:227], a[84:87], v[36:39], v[224:227]\n"
"v_exp_f32 v246, v246\n"
"v_mfma_f32_16x16x32_bf16 v[212:215], a[88:91], v[36:39], v[212:215]\n"
"v_exp_f32 v247, v247\n"
"v_mfma_f32_16x16x32_bf16 v[224:227], a[96:99], v[40:43], v[224:227]\n"
"v_cvt_pk_bf16_f32 v82, v244, v246\n"
"v_cvt_pk_bf16_f32 v83, v245, v247\n"
"v_mfma_f32_16x16x32_bf16 v[212:215], a[100:103], v[40:43], v[212:215]\n"
"s_waitcnt vmcnt(7)\n"
"ds_read_b64_tr_b16 v[244:245], v182 offset:40640\n"
"v_add3_u32 v200, v200, s50, -4\n"
"v_mfma_f32_16x16x32_bf16 v[224:227], a[108:111], v[44:47], v[224:227]\n"
"s_add_u32 m0, s34, 2048\n"
"v_cmp_lt_u32 vcc, 64, v200\n"
"v_fmamk_f32 v249, v249, 0x3e553b95, v170\n"
"v_mfma_f32_16x16x32_bf16 v[212:215], a[112:115], v[44:47], v[212:215]\n"
"buffer_load_dwordx4 v190, s[8:11], s1 offen lds\n"
"ds_read_b64_tr_b16 v[246:247], v183 offset:40640\n"
"v_mfma_f32_16x16x32_bf16 v[224:227], a[120:123], v[48:51], v[224:227]\n"
"s_cbranch_vccnz .Lp3%=\n"
".Lr3%=:\n"
"v_mfma_f32_16x16x32_bf16 v[212:215], a[124:127], v[48:51], v[212:215]\n"
"v_fmamk_f32 v250, v250, 0x3e553b95, v170\n"
"v_fmamk_f32 v251, v251, 0x3e553b95, v170\n"
"v_mfma_f32_16x16x32_bf16 v[224:227], a[132:135], v[52:55], v[224:227]\n"
"buffer_load_dword v200, off, s[28:31], s39 sc1\n"
"v_exp_f32 v248, v248\n"
"v_mfma_f32_16x16x32_bf16 v[212:215], a[136:139], v[52:55], v[212:215]\n"
"v_exp_f32 v249, v249\n"
"buffer_load_dwordx4 v[56:59], v192, s[28:31], s38 offen sc1\n"
"v_exp_f32 v250, v250\n"
"v_exp_f32 v251, v251\n"
"v_cvt_pk_bf16_f32 v84, v248, v250\n"
"v_cvt_pk_bf16_f32 v85, v249, v251\n"
"v_cvt_pk_bf16_f32 v150, v204, v205\n"
"v_cvt_pk_bf16_f32 v151, v206, v207\n"
"v_smfmac_f32_16x16x64_bf16 v[252:255], v[82:85], v[128:135], v66\n"
"s_waitcnt lgkmcnt(3)\n"
"ds_read_b64_tr_b16 v[248:249], v182 offset:41408\n"
"v_smfmac_f32_16x16x64_bf16 v[98:101], v[82:85], v[112:119], v66\n"
"s_add_u32 m0, s34, 5120\n"
"s_nop 0\n"
"buffer_load_dwordx4 v191, s[8:11], s1 offen lds\n"
"v_smfmac_f32_16x16x64_bf16 v[106:109], v[82:85], v[120:127], v66\n"
"s_add_u32 s1, s1, s33\n"
"v_mov_b64 v[72:73], -1\n"
"v_mfma_f32_16x16x32_bf16 v[82:85], v[172:175], v[82:85], 0\n"
"ds_read_b64_tr_b16 v[250:251], v183 offset:41408\n"
"ds_read_b64_tr_b16 v[128:129], v182 offset:40648\n"
"v_mfma_f32_16x16x32_bf16 v[204:207], a[80:83], v[32:35], 0\n"
"ds_read_b64_tr_b16 v[130:131], v183 offset:40648\n"
"v_fmamk_f32 v136, v136, 0x3e553b95, v170\n"
"v_mfma_f32_16x16x32_bf16 v[204:207], a[92:95], v[36:39], v[204:207]\n"
"buffer_load_dwordx2 v[60:61], v193, s[28:31], s38 offen offset:1024 sc1\n"
"ds_read_b64_tr_b16 v[132:133], v182 offset:41416\n"
"v_mfma_f32_16x16x32_bf16 v[204:207], a[104:107], v[40:43], v[204:207]\n"
"s_add_u32 s38, s38, 1572864\n"
"ds_read_b64_tr_b16 v[134:135], v183 offset:41416\n"
"v_cvt_pk_bf16_f32 v162, v228, v229\n"
"v_mfma_f32_16x16x32_bf16 v[204:207], a[116:119], v[44:47], v[204:207]\n"
"ds_read_b64_tr_b16 v[112:113], v182 offset:40584\n"
"v_fmamk_f32 v137, v137, 0x3e553b95, v170\n"
"v_mfma_f32_16x16x32_bf16 v[204:207], a[128:131], v[48:51], v[204:207]\n"
"ds_read_b64_tr_b16 v[114:115], v183 offset:40584\n"
"v_cvt_pk_bf16_f32 v163, v230, v231\n"
"v_mfma_f32_16x16x32_bf16 v[204:207], a[140:143], v[52:55], v[204:207]\n"
"buffer_store_dwordx2 v[72:73], v193, s[28:31], s4 offen offset:1024 sc1\n"
"v_mfma_f32_16x16x32_bf16 v[228:231], a[144:147], v[32:35], 0\n"
"ds_read_b128 v[72:75], v192 offset:57344\n"
"ds_read_b64_tr_b16 v[116:117], v182 offset:41352\n"
"v_mfma_f32_16x16x32_bf16 v[228:231], a[156:159], v[36:39], v[228:231]\n"
"ds_read_b64_tr_b16 v[118:119], v183 offset:41352\n"
"v_fmamk_f32 v138, v138, 0x3e553b95, v170\n"
"v_mfma_f32_16x16x32_bf16 v[228:231], a[168:171], v[40:43], v[228:231]\n"
"v_fmamk_f32 v139, v139, 0x3e553b95, v170\n"
"v_fmamk_f32 v140, v140, 0x3e553b95, v170\n"
"v_mfma_f32_16x16x32_bf16 v[228:231], a[180:183], v[44:47], v[228:231]\n"
"v_exp_f32 v136, v136\n"
"v_mfma_f32_16x16x32_bf16 v[228:231], a[192:195], v[48:51], v[228:231]\n"
"v_exp_f32 v137, v137\n"
"v_mfma_f32_16x16x32_bf16 v[228:231], a[204:207], v[52:55], v[228:231]\n"
"v_fmamk_f32 v141, v141, 0x3e553b95, v170\n"
"v_fmamk_f32 v142, v142, 0x3e553b95, v170\n"
"v_fmamk_f32 v143, v143, 0x3e553b95, v170\n"
"v_exp_f32 v138, v138\n"
"v_exp_f32 v139, v139\n"
"v_exp_f32 v140, v140\n"
"v_exp_f32 v141, v141\n"
"v_cvt_pk_bf16_f32 v90, v136, v138\n"
"v_exp_f32 v142, v142\n"
"v_exp_f32 v143, v143\n"
"v_cvt_pk_bf16_f32 v91, v137, v139\n"
"v_cvt_pk_bf16_f32 v92, v140, v142\n"
"v_cvt_pk_bf16_f32 v93, v141, v143\n"
"v_cvt_pk_bf16_f32 v208, v208, v209\n"
"v_cvt_pk_bf16_f32 v209, v210, v211\n"
"v_cvt_pk_bf16_f32 v210, v216, v217\n"
"v_cvt_pk_bf16_f32 v211, v218, v219\n"
"v_mfma_f32_16x16x32_bf16 v[216:219], a[148:151], v[32:35], 0\n"
"s_waitcnt lgkmcnt(0)\n"
"ds_read_b128 v[120:123], v178 offset:27008\n"
"ds_read_b128 v[136:139], v179 offset:27008\n"
"v_mfma_f32_16x16x32_bf16 v[232:235], a[240:243], v[72:75], v[232:235]\n"
"ds_read_b128 v[124:127], v180\n"
"v_cvt_pk_bf16_f32 v220, v220, v221\n"
"v_mfma_f32_16x16x32_bf16 v[236:239], a[244:247], v[72:75], v[236:239]\n"
"ds_read_b128 v[140:143], v181\n"
"v_cvt_pk_bf16_f32 v221, v222, v223\n"
"v_mfma_f32_16x16x32_bf16 v[240:243], a[248:251], v[72:75], v[240:243]\n"
"ds_read_b128 v[72:75], v192 offset:60416\n"
"v_cvt_pk_bf16_f32 v224, v224, v225\n"
"v_mfma_f32_16x16x32_bf16 v[232:235], a[252:255], v[76:79], v[232:235]\n"
"v_cvt_pk_bf16_f32 v225, v226, v227\n"
"v_cvt_pk_bf16_f32 v226, v212, v213\n"
"v_mfma_f32_16x16x32_bf16 v[236:239], v[0:3], v[76:79], v[236:239]\n"
"v_cvt_pk_bf16_f32 v227, v214, v215\n"
"v_cvt_pk_bf16_f32 v222, v204, v205\n"
"v_mfma_f32_16x16x32_bf16 v[240:243], v[4:7], v[76:79], v[240:243]\n"
"v_cvt_pk_bf16_f32 v223, v206, v207\n"
"v_mfma_f32_16x16x32_bf16 v[232:235], v[8:11], v[68:71], v[232:235]\n"
"v_mfma_f32_16x16x32_bf16 v[240:243], v[16:19], v[68:71], v[240:243]\n"
"s_waitcnt vmcnt(11) lgkmcnt(4)\n"
"v_or_b32 v201, 7, s6\n"
"v_mfma_f32_16x16x32_bf16 v[236:239], v[12:15], v[68:71], v[236:239]\n"
"s_waitcnt lgkmcnt(3)\n"
"s_mov_b64 exec, 1\n"
"buffer_store_dword v201, off, s[28:31], s40 sc1\n"
"s_mov_b64 exec, -1\n"
"v_smfmac_f32_16x16x64_bf16 v[252:255], v[90:93], v[244:251], v66\n"
"s_waitcnt lgkmcnt(1)\n"
"ds_read_b128 v[244:247], v180 offset:13504\n"
"v_smfmac_f32_16x16x64_bf16 v[98:101], v[90:93], v[128:135], v66\n"
"s_waitcnt lgkmcnt(1)\n"
"ds_read_b128 v[128:131], v181 offset:13504\n"
"v_mfma_f32_16x16x32_bf16 v[232:235], v[20:23], v[72:75], v[232:235]\n"
"v_mfma_f32_16x16x32_bf16 v[240:243], v[28:31], v[72:75], v[240:243]\n"
"s_nop 0\n"
"v_cvt_pk_bf16_f32 v252, v252, v253\n"
"v_mfma_f32_16x16x32_bf16 v[236:239], v[24:27], v[72:75], v[236:239]\n"
"v_cvt_pk_bf16_f32 v253, v254, v255\n"
"v_cvt_pk_bf16_f32 v98, v98, v99\n"
"v_smfmac_f32_16x16x64_bf16 v[106:109], v[90:93], v[112:119], v66\n"
"v_cvt_pk_bf16_f32 v99, v100, v101\n"
"v_mfma_f32_16x16x32_bf16 v[90:93], v[172:175], v[90:93], v[82:85]\n"
"v_cvt_pk_bf16_f32 v232, v232, v233\n"
"v_cvt_pk_bf16_f32 v233, v234, v235\n"
"v_mfma_f32_16x16x32_bf16 v[120:123], v[120:123], v[152:155], 0\n"
"v_cvt_pk_bf16_f32 v240, v240, v241\n"
"v_cvt_pk_bf16_f32 v234, v236, v237\n"
"v_mfma_f32_16x16x32_bf16 v[136:139], v[136:139], v[152:155], 0\n"
"s_waitcnt lgkmcnt(1)\n"
"v_cvt_pk_bf16_f32 v235, v238, v239\n"
"v_cvt_pk_bf16_f32 v241, v242, v243\n"
"v_mfma_f32_16x16x32_bf16 v[124:127], v[124:127], v[152:155], 0\n"
"s_waitcnt lgkmcnt(0)\n"
"ds_write_b128 v196, v[232:235] offset:30720\n"
"v_mfma_f32_16x16x32_bf16 v[140:143], v[140:143], v[152:155], 0\n"
"v_rcp_f32 v171, v90\n"
"v_mfma_f32_16x16x32_bf16 v[244:247], v[244:247], v[152:155], 0\n"
"v_cvt_pk_bf16_f32 v171, v171, v171\n"
"v_and_b32 v64, v171, v176\n"
"v_mfma_f32_16x16x32_bf16 v[128:131], v[128:131], v[152:155], 0\n"
"ds_write_b64 v197, v[240:241] offset:30720\n"
"v_mfma_f32_16x16x32_bf16 v[216:219], a[160:163], v[36:39], v[216:219]\n"
"v_and_b32 v65, v171, v177\n"
"v_cvt_pk_bf16_f32 v106, v106, v107\n"
"v_mfma_f32_16x16x32_bf16 v[216:219], a[172:175], v[40:43], v[216:219]\n"
"ds_read_b64 v[82:83], v178 offset:27072\n"
"v_cvt_pk_bf16_f32 v107, v108, v109\n"
"v_mfma_f32_16x16x16_bf16 v[252:255], v[252:253], v[64:65], 0\n"
"ds_read_b64 v[84:85], v179 offset:27072\n"
"ds_read_b64 v[90:91], v180 offset:64\n"
"v_mfma_f32_16x16x16_bf16 v[98:101], v[98:99], v[64:65], 0\n"
"ds_read_b64 v[92:93], v181 offset:64\n"
"s_mov_b64 exec, s[44:45]\n"
"v_add_u32 v179, 0xc0, v179\n"
"s_mov_b64 exec, -1\n"
"v_mfma_f32_16x16x16_bf16 v[106:109], v[106:107], v[64:65], 0\n"
"v_cvt_pk_bf16_f32 v252, v252, v253\n"
"v_cvt_pk_bf16_f32 v253, v254, v255\n"
"v_mfma_f32_16x16x32_bf16 v[216:219], a[184:187], v[44:47], v[216:219]\n"
"v_cvt_pk_bf16_f32 v254, v98, v99\n"
"v_cvt_pk_bf16_f32 v255, v100, v101\n"
"v_mfma_f32_16x16x32_bf16 v[216:219], a[196:199], v[48:51], v[216:219]\n"
"s_waitcnt lgkmcnt(4)\n"
"buffer_store_dwordx4 v[252:255], v192, s[28:31], s37 offen sc1\n"
"v_mfma_f32_16x16x32_bf16 v[216:219], a[208:211], v[52:55], v[216:219]\n"
"s_waitcnt lgkmcnt(3)\n"
"v_cvt_pk_bf16_f32 v106, v106, v107\n"
"v_cvt_pk_bf16_f32 v107, v108, v109\n"
"v_mov_b64 v[252:253], 0\n"
"v_mfma_f32_16x16x32_bf16 v[120:123], v[80:83], v[164:167], v[120:123]\n"
"s_waitcnt lgkmcnt(2)\n"
"buffer_store_dwordx2 v[106:107], v193, s[28:31], s37 offen offset:1024 sc1\n"
"v_mfma_f32_16x16x32_bf16 v[136:139], v[84:87], v[166:169], v[136:139]\n"
"s_waitcnt lgkmcnt(1)\n"
"ds_read_b64 v[98:99], v180 offset:13568\n"
"ds_read_b64 v[100:101], v181 offset:13568\n"
"v_mfma_f32_16x16x32_bf16 v[124:127], v[88:91], v[164:167], v[124:127]\n"
"s_waitcnt lgkmcnt(2)\n"
"ds_read_b64_tr_b16 v[112:113], v182 offset:27136\n"
"v_mov_b64 v[254:255], 0\n"
"v_mfma_f32_16x16x32_bf16 v[140:143], v[92:95], v[166:169], v[140:143]\n"
"s_barrier\n"
"s_add_u32 s37, s37, 1572864\n"
"s_add_u32 s4, s37, 6291456\n"
"ds_write_b128 v186, v[208:211] offset:6752\n"
"v_mfma_f32_16x16x32_bf16 v[208:211], a[152:155], v[32:35], 0\n"
"v_max_f32 v120, v120, v136\n"
"v_max_f32 v121, v121, v137\n"
"v_mfma_f32_16x16x32_bf16 v[208:211], a[164:167], v[36:39], v[208:211]\n"
"v_max_f32 v122, v122, v138\n"
"v_max_f32 v123, v123, v139\n"
"v_mfma_f32_16x16x32_bf16 v[208:211], a[176:179], v[40:43], v[208:211]\n"
"ds_read_b128 v[32:35], v188 offset:6144\n"
"ds_read_b128 v[36:39], v189 offset:6144\n"
"v_mfma_f32_16x16x32_bf16 v[208:211], a[188:191], v[44:47], v[208:211]\n"
"s_waitcnt lgkmcnt(5)\n"
"ds_read_b128 v[40:43], v188 offset:7168\n"
"v_max3_f32 v170, v120, v121, v122\n"
"v_mfma_f32_16x16x32_bf16 v[244:247], v[96:99], v[164:167], v[244:247]\n"
"s_waitcnt lgkmcnt(5)\n"
"ds_read_b128 v[44:47], v189 offset:7168\n"
"v_max_f32 v124, v124, v140\n"
"v_mfma_f32_16x16x32_bf16 v[128:131], v[100:103], v[166:169], v[128:131]\n"
"ds_read_b128 v[248:251], v180 offset:27008\n"
"v_max_f32 v125, v125, v141\n"
"v_mfma_f32_16x16x32_bf16 v[208:211], a[200:203], v[48:51], v[208:211]\n"
"ds_read_b128 v[48:51], v188 offset:8192\n"
"v_max_f32 v126, v126, v142\n"
"v_mfma_f32_16x16x32_bf16 v[208:211], a[212:215], v[52:55], v[208:211]\n"
"ds_read_b128 v[52:55], v189 offset:8192\n"
"v_max_f32 v127, v127, v143\n"
"v_max3_f32 v170, v170, v123, v124\n"
"v_max3_f32 v170, v170, v125, v126\n"
"v_max_f32 v244, v244, v128\n"
"v_max_f32 v245, v245, v129\n"
"v_max_f32 v246, v246, v130\n"
"v_max_f32 v247, v247, v131\n"
"v_max3_f32 v170, v170, v127, v244\n"
"v_max3_f32 v170, v170, v245, v246\n"
"v_mov_b64 v[98:99], 0\n"
"v_mov_b64 v[100:101], 0\n"
"s_waitcnt lgkmcnt(6)\n"
"v_mfma_f32_16x16x32_bf16 v[212:215], a[0:3], v[32:35], 0\n"
"s_waitcnt lgkmcnt(5)\n"
"ds_read_b128 v[132:135], v181 offset:27008\n"
"ds_read_b64 v[106:107], v180 offset:27072\n"
"v_mfma_f32_16x16x32_bf16 v[204:207], a[4:7], v[32:35], 0\n"
"s_add_u32 m0, s34, 6144\n"
"ds_read_b64 v[108:109], v181 offset:27072\n"
"buffer_load_dwordx4 v190, s[8:11], s32 offen lds\n"
"v_mfma_f32_16x16x32_bf16 v[212:215], a[12:15], v[36:39], v[212:215]\n"
"s_waitcnt lgkmcnt(7)\n"
"ds_read_b128 v[72:75], v202 offset:30720\n"
"v_mfma_f32_16x16x32_bf16 v[204:207], a[16:19], v[36:39], v[204:207]\n"
"s_waitcnt lgkmcnt(6)\n"
"ds_read_b64_tr_b16 v[114:115], v183 offset:27136\n"
"v_mfma_f32_16x16x32_bf16 v[248:251], v[248:251], v[152:155], 0\n"
"ds_read_b64_tr_b16 v[116:117], v182 offset:27904\n"
"v_mfma_f32_16x16x32_bf16 v[212:215], a[24:27], v[40:43], v[212:215]\n"
"ds_read_b64_tr_b16 v[118:119], v183 offset:27904\n"
"v_mfma_f32_16x16x32_bf16 v[204:207], a[28:31], v[40:43], v[204:207]\n"
"v_mov_b64 v[76:77], -1\n"
"v_mov_b64 v[78:79], -1\n"
"v_mfma_f32_16x16x32_bf16 v[212:215], a[36:39], v[44:47], v[212:215]\n"
"s_waitcnt lgkmcnt(8)\n"
"buffer_store_dwordx4 v[76:79], v192, s[28:31], s4 offen sc1\n"
"v_mfma_f32_16x16x32_bf16 v[204:207], a[40:43], v[44:47], v[204:207]\n"
"s_waitcnt lgkmcnt(5)\n"
"ds_read_b128 v[76:79], v202 offset:34816\n"
"ds_read_b64_tr_b16 v[136:137], v182 offset:27144\n"
"v_mfma_f32_16x16x32_bf16 v[132:135], v[132:135], v[152:155], 0\n"
"s_waitcnt lgkmcnt(6)\n"
"ds_read_b64_tr_b16 v[138:139], v183 offset:27144\n"
"v_cvt_pk_bf16_f32 v152, v228, v229\n"
"v_mfma_f32_16x16x32_bf16 v[248:251], v[104:107], v[164:167], v[248:251]\n"
"s_waitcnt lgkmcnt(6)\n"
"ds_read_b64_tr_b16 v[140:141], v182 offset:27912\n"
"buffer_store_dwordx4 v[72:75], v194, s[24:27], s36 offen sc1\n"
"v_mfma_f32_16x16x32_bf16 v[132:135], v[108:111], v[166:169], v[132:135]\n"
"s_add_u32 s4, s37, 6291456\n"
"ds_read_b128 v[72:75], v192 offset:37888\n"
"ds_read_b64_tr_b16 v[142:143], v183 offset:27912\n"
"v_mfma_f32_16x16x32_bf16 v[212:215], a[48:51], v[48:51], v[212:215]\n"
"s_add_u32 m0, s34, 9216\n"
"ds_read_b64_tr_b16 v[128:129], v182 offset:27080\n"
"v_mov_b64 v[106:107], 0\n"
"v_mfma_f32_16x16x32_bf16 v[204:207], a[52:55], v[48:51], v[204:207]\n"
"buffer_load_dwordx4 v191, s[8:11], s32 offen lds\n"
"ds_write_b128 v186, v[220:223] offset:6816\n"
"v_mfma_f32_16x16x32_bf16 v[220:223], a[8:11], v[32:35], 0\n"
"s_waitcnt lgkmcnt(7)\n"
"v_max_f32 v248, v248, v132\n"
"v_max_f32 v249, v249, v133\n"
"v_mfma_f32_16x16x32_bf16 v[220:223], a[20:23], v[36:39], v[220:223]\n"
"s_add_u32 s32, s32, s33\n"
"v_max_f32 v250, v250, v134\n"
"v_max_f32 v251, v251, v135\n"
"v_mfma_f32_16x16x32_bf16 v[220:223], a[32:35], v[40:43], v[220:223]\n"
"buffer_store_dwordx4 v[76:79], v195, s[24:27], s36 offen sc1\n"
"v_mfma_f32_16x16x32_bf16 v[220:223], a[44:47], v[44:47], v[220:223]\n"
"s_waitcnt vmcnt(11)\n"
"v_add3_u32 v200, v200, s50, -5\n"
"v_cmp_lt_u32 vcc, 64, v200\n"
"v_mfma_f32_16x16x32_bf16 v[220:223], a[56:59], v[48:51], v[220:223]\n"
"s_add_u32 s36, s36, s33\n"
"ds_read_b128 v[76:79], v192 offset:38912\n"
"ds_write_b128 v202, v[56:59] offset:43008\n"
"v_mfma_f32_16x16x32_bf16 v[212:215], a[60:63], v[52:55], v[212:215]\n"
"s_cbranch_vccnz .Lp4%=\n"
".Lr4%=:\n"
"v_mfma_f32_16x16x32_bf16 v[204:207], a[64:67], v[52:55], v[204:207]\n"
"s_add_u32 m0, s34, 7168\n"
"buffer_load_dwordx4 v[56:59], v192, s[28:31], s38 offen sc1\n"
"ds_read_b64_tr_b16 v[130:131], v183 offset:27080\n"
"v_mfma_f32_16x16x32_bf16 v[220:223], a[68:71], v[52:55], v[220:223]\n"
"v_max3_f32 v170, v170, v247, v248\n"
"v_max3_f32 v170, v170, v249, v250\n"
"v_mov_b64 v[108:109], 0\n"
"ds_read_b64_tr_b16 v[132:133], v182 offset:27848\n"
"ds_read_b64_tr_b16 v[134:135], v183 offset:27848\n"
"ds_write_b128 v186, v[224:227] offset:6880\n"
"v_mfma_f32_16x16x32_bf16 v[224:227], a[72:75], v[32:35], 0\n"
"s_waitcnt vmcnt(10)\n"
"buffer_load_dwordx4 v190, s[8:11], s0 offen lds\n"
"v_max_f32 v171, v170, v251\n"
"v_mfma_f32_16x16x32_bf16 v[224:227], a[84:87], v[36:39], v[224:227]\n"
"v_max_f32 v170, v170, v251\n"
"v_cvt_pk_bf16_f32 v153, v230, v231\n"
"v_mfma_f32_16x16x32_bf16 v[228:231], a[76:79], v[32:35], 0\n"
"ds_write_b64 v203, v[60:61] offset:43008\n"
"v_mfma_f32_16x16x32_bf16 v[228:231], a[88:91], v[36:39], v[228:231]\n"
"buffer_load_dwordx2 v[60:61], v193, s[28:31], s38 offen offset:1024 sc1\n"
"v_permlane16_swap_b32 v170, v171\n"
"v_mfma_f32_16x16x32_bf16 v[224:227], a[96:99], v[40:43], v[224:227]\n"
"s_add_u32 s38, s38, 1572864\n"
"v_max_f32 v170, v170, v171\n"
"v_mov_b32 v171, v170\n"
"v_mfma_f32_16x16x32_bf16 v[228:231], a[100:103], v[40:43], v[228:231]\n"
"s_nop 0\n"
"v_permlane32_swap_b32 v170, v171\n"
"v_mfma_f32_16x16x32_bf16 v[224:227], a[108:111], v[44:47], v[224:227]\n"
"v_max_f32 v170, v170, v171\n"
"v_mul_f32 v170, 0xbe553b95, v170\n"
"v_mfma_f32_16x16x32_bf16 v[228:231], a[112:115], v[44:47], v[228:231]\n"
"s_add_u32 m0, s34, 10240\n"
"v_fmamk_f32 v120, v120, 0x3e553b95, v170\n"
"v_fmamk_f32 v121, v121, 0x3e553b95, v170\n"
"v_mfma_f32_16x16x32_bf16 v[224:227], a[120:123], v[48:51], v[224:227]\n"
"buffer_load_dwordx4 v191, s[8:11], s0 offen lds\n"
"v_fmamk_f32 v122, v122, 0x3e553b95, v170\n"
"v_mfma_f32_16x16x32_bf16 v[228:231], a[124:127], v[48:51], v[228:231]\n"
"s_add_u32 s0, s0, s33\n"
"v_fmamk_f32 v123, v123, 0x3e553b95, v170\n"
"v_fmamk_f32 v124, v124, 0x3e553b95, v170\n"
"v_mfma_f32_16x16x32_bf16 v[224:227], a[132:135], v[52:55], v[224:227]\n"
"v_exp_f32 v120, v120\n"
"v_mfma_f32_16x16x32_bf16 v[228:231], a[136:139], v[52:55], v[228:231]\n"
"v_exp_f32 v121, v121\n"
"v_exp_f32 v122, v122\n"
"v_exp_f32 v123, v123\n"
"v_cvt_pk_bf16_f32 v82, v120, v122\n"
"v_cvt_pk_bf16_f32 v83, v121, v123\n"
"v_fmamk_f32 v125, v125, 0x3e553b95, v170\n"
"v_fmamk_f32 v126, v126, 0x3e553b95, v170\n"
"v_fmamk_f32 v127, v127, 0x3e553b95, v170\n"
"v_exp_f32 v124, v124\n"
"v_exp_f32 v125, v125\n"
"v_exp_f32 v126, v126\n"
"s_add_u32 m0, s34, 8192\n"
"v_exp_f32 v127, v127\n"
"buffer_load_dwordx4 v190, s[8:11], s1 offen lds\n"
"v_cvt_pk_bf16_f32 v84, v124, v126\n"
"v_cvt_pk_bf16_f32 v85, v125, v127\n"
"v_cvt_pk_bf16_f32 v154, v216, v217\n"
"v_cvt_pk_bf16_f32 v155, v218, v219\n"
"v_smfmac_f32_16x16x64_bf16 v[252:255], v[82:85], v[112:119], v66\n"
"s_waitcnt lgkmcnt(2)\n"
"ds_read_b64_tr_b16 v[120:121], v182 offset:54144\n"
"v_smfmac_f32_16x16x64_bf16 v[98:101], v[82:85], v[136:143], v66\n"
"ds_read_b64_tr_b16 v[122:123], v183 offset:54144\n"
"v_smfmac_f32_16x16x64_bf16 v[106:109], v[82:85], v[128:135], v66\n"
"v_mov_b64 v[68:69], -1\n"
"v_mfma_f32_16x16x32_bf16 v[82:85], v[172:175], v[82:85], 0\n"
"buffer_store_dwordx2 v[68:69], v193, s[28:31], s4 offen offset:1024 sc1\n"
"v_mfma_f32_16x16x32_bf16 v[216:219], a[80:83], v[32:35], 0\n"
"ds_read_b128 v[68:71], v192 offset:36864\n"
"ds_read_b64_tr_b16 v[124:125], v182 offset:54912\n"
"v_mfma_f32_16x16x32_bf16 v[216:219], a[92:95], v[36:39], v[216:219]\n"
"ds_read_b64_tr_b16 v[126:127], v183 offset:54912\n"
"v_fmamk_f32 v244, v244, 0x3e553b95, v170\n"
"v_mfma_f32_16x16x32_bf16 v[216:219], a[104:107], v[40:43], v[216:219]\n"
"s_add_u32 m0, s34, 11264\n"
"buffer_load_dword v200, off, s[28:31], s39 sc1\n"
"ds_read_b64_tr_b16 v[112:113], v182 offset:54152\n"
"v_mfma_f32_16x16x32_bf16 v[216:219], a[116:119], v[44:47], v[216:219]\n"
"ds_read_b64_tr_b16 v[114:115], v183 offset:54152\n"
"buffer_load_dwordx4 v191, s[8:11], s1 offen lds\n"
"v_mfma_f32_16x16x32_bf16 v[216:219], a[128:131], v[48:51], v[216:219]\n"
"s_add_u32 s1, s1, s33\n"
"ds_read_b64_tr_b16 v[116:117], v182 offset:54920\n"
"v_fmamk_f32 v245, v245, 0x3e553b95, v170\n"
"v_mfma_f32_16x16x32_bf16 v[216:219], a[140:143], v[52:55], v[216:219]\n"
"ds_read_b64_tr_b16 v[118:119], v183 offset:54920\n"
"v_cvt_pk_bf16_f32 v166, v208, v209\n"
"v_fmamk_f32 v246, v246, 0x3e553b95, v170\n"
"v_cvt_pk_bf16_f32 v167, v210, v211\n"
"v_mfma_f32_16x16x32_bf16 v[208:211], a[144:147], v[32:35], 0\n"
"ds_read_b64_tr_b16 v[136:137], v182 offset:54088\n"
"ds_read_b64_tr_b16 v[138:139], v183 offset:54088\n"
"v_mfma_f32_16x16x32_bf16 v[208:211], a[156:159], v[36:39], v[208:211]\n"
"s_waitcnt lgkmcnt(8)\n"
"ds_read_b64_tr_b16 v[140:141], v182 offset:54856\n"
"v_fmamk_f32 v247, v247, 0x3e553b95, v170\n"
"v_mfma_f32_16x16x32_bf16 v[232:235], a[216:219], v[68:71], 0\n"
"ds_read_b64_tr_b16 v[142:143], v183 offset:54856\n"
"v_fmamk_f32 v248, v248, 0x3e553b95, v170\n"
"v_mfma_f32_16x16x32_bf16 v[236:239], a[220:223], v[68:71], 0\n"
"v_exp_f32 v244, v244\n"
"v_mfma_f32_16x16x32_bf16 v[240:243], a[224:227], v[68:71], 0\n"
"ds_read_b128 v[68:71], v192 offset:39936\n"
"v_fmamk_f32 v249, v249, 0x3e553b95, v170\n"
"v_mfma_f32_16x16x32_bf16 v[232:235], a[228:231], v[72:75], v[232:235]\n"
"v_exp_f32 v245, v245\n"
"v_mfma_f32_16x16x32_bf16 v[236:239], a[232:235], v[72:75], v[236:239]\n"
"v_exp_f32 v246, v246\n"
"v_mfma_f32_16x16x32_bf16 v[240:243], a[236:239], v[72:75], v[240:243]\n"
"ds_read_b128 v[72:75], v192 offset:40960\n"
"v_fmamk_f32 v250, v250, 0x3e553b95, v170\n"
"v_mfma_f32_16x16x32_bf16 v[232:235], a[240:243], v[76:79], v[232:235]\n"
"v_fmamk_f32 v251, v251, 0x3e553b95, v170\n"
"v_cvt_pk_bf16_f32 v90, v244, v246\n"
"v_mfma_f32_16x16x32_bf16 v[236:239], a[244:247], v[76:79], v[236:239]\n"
"v_exp_f32 v247, v247\n"
"v_mfma_f32_16x16x32_bf16 v[240:243], a[248:251], v[76:79], v[240:243]\n"
"ds_read_b128 v[76:79], v192 offset:41984\n"
"v_cvt_pk_bf16_f32 v91, v245, v247\n"
"v_mfma_f32_16x16x32_bf16 v[208:211], a[168:171], v[40:43], v[208:211]\n"
"v_exp_f32 v248, v248\n"
"v_mfma_f32_16x16x32_bf16 v[208:211], a[180:183], v[44:47], v[208:211]\n"
"s_waitcnt lgkmcnt(2)\n"
"v_exp_f32 v249, v249\n"
"v_mfma_f32_16x16x32_bf16 v[232:235], a[252:255], v[68:71], v[232:235]\n"
"v_exp_f32 v250, v250\n"
"v_mfma_f32_16x16x32_bf16 v[240:243], v[4:7], v[68:71], v[240:243]\n"
"v_exp_f32 v251, v251\n"
"v_mfma_f32_16x16x32_bf16 v[236:239], v[0:3], v[68:71], v[236:239]\n"
"s_waitcnt lgkmcnt(1)\n"
"v_cvt_pk_bf16_f32 v92, v248, v250\n"
"v_cvt_pk_bf16_f32 v93, v249, v251\n"
"v_mfma_f32_16x16x32_bf16 v[232:235], v[8:11], v[72:75], v[232:235]\n"
"ds_read_b128 v[128:131], v180\n"
"ds_read_b128 v[244:247], v181\n"
"v_mfma_f32_16x16x32_bf16 v[240:243], v[16:19], v[72:75], v[240:243]\n"
"ds_read_b128 v[132:135], v180 offset:13504\n"
"v_cvt_pk_bf16_f32 v212, v212, v213\n"
"v_mfma_f32_16x16x32_bf16 v[236:239], v[12:15], v[72:75], v[236:239]\n"
"s_waitcnt vmcnt(13) lgkmcnt(3)\n"
"ds_read_b128 v[248:251], v181 offset:13504\n"
"v_cvt_pk_bf16_f32 v213, v214, v215\n"
"v_mfma_f32_16x16x32_bf16 v[232:235], v[20:23], v[76:79], v[232:235]\n"
"v_or_b32 v201, 8, s6\n"
"v_cvt_pk_bf16_f32 v214, v204, v205\n"
"v_mfma_f32_16x16x32_bf16 v[240:243], v[28:31], v[76:79], v[240:243]\n"
"s_mov_b64 exec, 1\n"
"buffer_store_dword v201, off, s[28:31], s40 sc1\n"
"s_mov_b64 exec, -1\n"
"v_mfma_f32_16x16x32_bf16 v[236:239], v[24:27], v[76:79], v[236:239]\n"
"v_cvt_pk_bf16_f32 v215, v206, v207\n"
"v_cvt_pk_bf16_f32 v232, v232, v233\n"
"v_smfmac_f32_16x16x64_bf16 v[252:255], v[90:93], v[120:127], v66\n"
"v_cvt_pk_bf16_f32 v233, v234, v235\n"
"v_smfmac_f32_16x16x64_bf16 v[98:101], v[90:93], v[112:119], v66\n"
"s_waitcnt lgkmcnt(3)\n"
"v_cvt_pk_bf16_f32 v240, v240, v241\n"
".Lic5%=:\n"
"s_cbranch_execz .Lib1%=\n"
"v_smfmac_f32_16x16x64_bf16 v[106:109], v[90:93], v[136:143], v66\n"
"s_waitcnt lgkmcnt(2)\n"
"v_cvt_pk_bf16_f32 v234, v236, v237\n"
"v_mfma_f32_16x16x32_bf16 v[90:93], v[172:175], v[90:93], v[82:85]\n"
"s_waitcnt lgkmcnt(1)\n"
"v_cvt_pk_bf16_f32 v235, v238, v239\n"
"v_cvt_pk_bf16_f32 v241, v242, v243\n"
"v_mfma_f32_16x16x32_bf16 v[128:131], v[128:131], v[144:147], 0\n"
"s_waitcnt lgkmcnt(0)\n"
"ds_write_b128 v196, v[232:235] offset:55296\n"
"v_mfma_f32_16x16x32_bf16 v[244:247], v[244:247], v[144:147], 0\n"
"v_cvt_pk_bf16_f32 v252, v252, v253\n"
"v_cvt_pk_bf16_f32 v253, v254, v255\n"
"v_mfma_f32_16x16x32_bf16 v[132:135], v[132:135], v[144:147], 0\n"
"v_rcp_f32 v171, v90\n"
"v_mfma_f32_16x16x32_bf16 v[248:251], v[248:251], v[144:147], 0\n"
"ds_write_b64 v197, v[240:241] offset:55296\n"
"v_mfma_f32_16x16x32_bf16 v[204:207], a[148:151], v[32:35], 0\n"
"v_cvt_pk_bf16_f32 v171, v171, v171\n"
"v_and_b32 v64, v171, v176\n"
"v_mfma_f32_16x16x32_bf16 v[204:207], a[160:163], v[36:39], v[204:207]\n"
"v_and_b32 v65, v171, v177\n"
"v_cvt_pk_bf16_f32 v98, v98, v99\n"
"v_mfma_f32_16x16x32_bf16 v[204:207], a[172:175], v[40:43], v[204:207]\n"
"v_cvt_pk_bf16_f32 v99, v100, v101\n"
"v_cvt_pk_bf16_f32 v106, v106, v107\n"
"v_mfma_f32_16x16x16_bf16 v[252:255], v[252:253], v[64:65], 0\n"
"v_cvt_pk_bf16_f32 v107, v108, v109\n"
"v_cvt_pk_bf16_f32 v220, v220, v221\n"
"v_mfma_f32_16x16x16_bf16 v[98:101], v[98:99], v[64:65], 0\n"
"ds_read_b64 v[82:83], v180 offset:64\n"
"ds_read_b64 v[84:85], v181 offset:64\n"
"v_mfma_f32_16x16x16_bf16 v[106:109], v[106:107], v[64:65], 0\n"
"ds_read_b64 v[90:91], v180 offset:13568\n"
"v_cvt_pk_bf16_f32 v221, v222, v223\n"
"v_mfma_f32_16x16x32_bf16 v[204:207], a[184:187], v[44:47], v[204:207]\n"
"s_waitcnt lgkmcnt(3)\n"
"v_cvt_pk_bf16_f32 v252, v252, v253\n"
"v_cvt_pk_bf16_f32 v253, v254, v255\n"
"v_mfma_f32_16x16x32_bf16 v[208:211], a[192:195], v[48:51], v[208:211]\n"
"s_barrier\n"
"v_mfma_f32_16x16x32_bf16 v[204:207], a[196:199], v[48:51], v[204:207]\n"
"v_cvt_pk_bf16_f32 v254, v98, v99\n"
"v_cvt_pk_bf16_f32 v255, v100, v101\n"
"v_mfma_f32_16x16x32_bf16 v[208:211], a[204:207], v[52:55], v[208:211]\n"
"buffer_store_dwordx4 v[252:255], v192, s[28:31], s37 offen sc1\n"
"v_mfma_f32_16x16x32_bf16 v[204:207], a[208:211], v[52:55], v[204:207]\n"
"v_cvt_pk_bf16_f32 v106, v106, v107\n"
"v_cvt_pk_bf16_f32 v107, v108, v109\n"
"v_cvt_pk_bf16_f32 v224, v224, v225\n"
"v_cvt_pk_bf16_f32 v225, v226, v227\n"
"v_cvt_pk_bf16_f32 v226, v228, v229\n"
"v_cvt_pk_bf16_f32 v227, v230, v231\n"
"v_cvt_pk_bf16_f32 v222, v216, v217\n"
"v_cvt_pk_bf16_f32 v223, v218, v219\n"
"v_mov_b64 v[252:253], 0\n"
"v_mov_b64 v[254:255], 0\n"
"s_waitcnt lgkmcnt(1)\n"
"v_mfma_f32_16x16x32_bf16 v[128:131], v[80:83], v[156:159], v[128:131]\n"
"s_waitcnt lgkmcnt(0)\n"
"buffer_store_dwordx2 v[106:107], v193, s[28:31], s37 offen offset:1024 sc1\n"
"v_mfma_f32_16x16x32_bf16 v[244:247], v[84:87], v[158:161], v[244:247]\n"
"s_add_u32 s37, s37, 1572864\n"
"ds_write_b128 v186, v[212:215] offset:20256\n"
"v_mfma_f32_16x16x32_bf16 v[132:135], v[88:91], v[156:159], v[132:135]\n"
"s_add_u32 s4, s37, 6291456\n"
"v_mfma_f32_16x16x32_bf16 v[212:215], a[152:155], v[32:35], 0\n"
"ds_read_b128 v[32:35], v188\n"
"v_mfma_f32_16x16x32_bf16 v[212:215], a[164:167], v[36:39], v[212:215]\n"
"ds_read_b128 v[36:39], v189\n"
"ds_read_b64 v[92:93], v181 offset:13568\n"
"v_mfma_f32_16x16x32_bf16 v[212:215], a[176:179], v[40:43], v[212:215]\n"
"ds_read_b128 v[40:43], v188 offset:1024\n"
"v_max_f32 v128, v128, v244\n"
"v_mfma_f32_16x16x32_bf16 v[212:215], a[188:191], v[44:47], v[212:215]\n"
"ds_read_b128 v[44:47], v189 offset:1024\n"
"v_max_f32 v129, v129, v245\n"
"v_mfma_f32_16x16x32_bf16 v[212:215], a[200:203], v[48:51], v[212:215]\n"
"ds_read_b128 v[48:51], v188 offset:2048\n"
"v_max_f32 v130, v130, v246\n"
"v_mfma_f32_16x16x32_bf16 v[212:215], a[212:215], v[52:55], v[212:215]\n"
"ds_read_b128 v[52:55], v189 offset:2048\n"
"v_max_f32 v131, v131, v247\n"
"v_max3_f32 v170, v128, v129, v130\n"
"ds_read_b128 v[120:123], v180 offset:27008\n"
"ds_read_b128 v[112:115], v181 offset:27008\n"
"ds_read_b64 v[98:99], v180 offset:27072\n"
"s_waitcnt lgkmcnt(9)\n"
"v_mfma_f32_16x16x32_bf16 v[228:231], a[0:3], v[32:35], 0\n"
"s_waitcnt lgkmcnt(8)\n"
"ds_read_b64 v[100:101], v181 offset:27072\n"
"s_add_u32 m0, s34, 0\n"
"v_mfma_f32_16x16x32_bf16 v[216:219], a[4:7], v[32:35], 0\n"
"s_waitcnt vmcnt(10) lgkmcnt(8)\n"
"buffer_load_dwordx4 v190, s[8:11], s32 offen lds\n"
"ds_write_b128 v202, v[56:59] offset:30720\n"
"v_mfma_f32_16x16x32_bf16 v[248:251], v[92:95], v[158:161], v[248:251]\n"
"s_add_u32 s32, s32, s33\n"
"v_mfma_f32_16x16x32_bf16 v[228:231], a[12:15], v[36:39], v[228:231]\n"
"s_waitcnt lgkmcnt(8)\n"
"buffer_load_dwordx4 v[56:59], v192, s[28:31], s38 offen sc1\n"
"ds_read_b128 v[124:127], v178\n"
"v_mfma_f32_16x16x32_bf16 v[216:219], a[16:19], v[36:39], v[216:219]\n"
"ds_read_b128 v[116:119], v179\n"
"ds_read_b64 v[106:107], v178 offset:64\n"
"v_mfma_f32_16x16x32_bf16 v[228:231], a[24:27], v[40:43], v[228:231]\n"
"s_waitcnt lgkmcnt(7)\n"
"ds_read_b128 v[76:79], v202 offset:55296\n"
"v_max_f32 v132, v132, v248\n"
"v_mfma_f32_16x16x32_bf16 v[120:123], v[120:123], v[144:147], 0\n"
"s_waitcnt lgkmcnt(7)\n"
"ds_read_b64 v[108:109], v179 offset:64\n"
"v_max_f32 v133, v133, v249\n"
"v_mfma_f32_16x16x32_bf16 v[112:115], v[112:115], v[144:147], 0\n"
"s_waitcnt lgkmcnt(7)\n"
"ds_read_b64_tr_b16 v[136:137], v182 offset:40640\n"
"v_max_f32 v134, v134, v250\n"
"v_mfma_f32_16x16x32_bf16 v[120:123], v[96:99], v[156:159], v[120:123]\n"
"s_waitcnt lgkmcnt(7)\n"
"v_mov_b64 v[68:69], -1\n"
"v_mov_b64 v[70:71], -1\n"
"v_mfma_f32_16x16x32_bf16 v[112:115], v[100:103], v[158:161], v[112:115]\n"
"s_add_u32 m0, s34, 3072\n"
"buffer_store_dwordx4 v[68:71], v192, s[28:31], s4 offen sc1\n"
"v_mfma_f32_16x16x32_bf16 v[216:219], a[28:31], v[40:43], v[216:219]\n"
"s_waitcnt vmcnt(11) lgkmcnt(5)\n"
"ds_read_b128 v[68:71], v202 offset:59392\n"
"ds_read_b64_tr_b16 v[138:139], v183 offset:40640\n"
"v_mfma_f32_16x16x32_bf16 v[124:127], v[124:127], v[144:147], 0\n"
"s_waitcnt lgkmcnt(6)\n"
"ds_read_b64_tr_b16 v[140:141], v182 offset:41408\n"
"buffer_load_dwordx4 v199, s[8:11], s75 offen lds\n"
"v_mfma_f32_16x16x32_bf16 v[116:119], v[116:119], v[144:147], 0\n"
"s_waitcnt lgkmcnt(6)\n"
"ds_read_b64_tr_b16 v[142:143], v183 offset:41408\n"
"v_max_f32 v135, v135, v251\n"
"v_mfma_f32_16x16x32_bf16 v[124:127], v[104:107], v[156:159], v[124:127]\n"
"s_waitcnt lgkmcnt(6)\n"
"ds_read_b64_tr_b16 v[244:245], v182 offset:40648\n"
"v_max3_f32 v170, v170, v131, v132\n"
"v_mfma_f32_16x16x32_bf16 v[228:231], a[36:39], v[44:47], v[228:231]\n"
"s_waitcnt lgkmcnt(6)\n"
"ds_read_b64_tr_b16 v[246:247], v183 offset:40648\n"
"v_max3_f32 v170, v170, v133, v134\n"
"v_mfma_f32_16x16x32_bf16 v[116:119], v[108:111], v[158:161], v[116:119]\n"
"s_add_u32 s4, s37, 6291456\n"
"ds_read_b64_tr_b16 v[248:249], v182 offset:41416\n"
"buffer_store_dwordx4 v[76:79], v194, s[24:27], s36 offen sc1\n"
"v_mfma_f32_16x16x32_bf16 v[216:219], a[40:43], v[44:47], v[216:219]\n"
"s_nop 0\n"
"ds_read_b128 v[76:79], v192 offset:44032\n"
"ds_read_b64_tr_b16 v[250:251], v183 offset:41416\n"
"v_mfma_f32_16x16x32_bf16 v[228:231], a[48:51], v[48:51], v[228:231]\n"
"s_waitcnt lgkmcnt(8)\n"
"ds_write_b64 v203, v[60:61] offset:30720\n"
"v_mfma_f32_16x16x32_bf16 v[216:219], a[52:55], v[48:51], v[216:219]\n"
"s_add_u32 m0, s34, 1024\n"
"v_max_f32 v120, v120, v112\n"
"v_max_f32 v121, v121, v113\n"
"v_mfma_f32_16x16x32_bf16 v[228:231], a[60:63], v[52:55], v[228:231]\n"
"buffer_load_dwordx4 v190, s[8:11], s0 offen lds\n"
"ds_write_b128 v186, v[220:223] offset:20320\n"
"v_mfma_f32_16x16x32_bf16 v[220:223], a[8:11], v[32:35], 0\n"
"s_add_u32 s0, s0, s33\n"
"v_max_f32 v122, v122, v114\n"
"v_max_f32 v123, v123, v115\n"
"v_mfma_f32_16x16x32_bf16 v[220:223], a[20:23], v[36:39], v[220:223]\n"
"v_max3_f32 v170, v170, v135, v120\n"
"v_max3_f32 v170, v170, v121, v122\n"
"v_mfma_f32_16x16x32_bf16 v[220:223], a[32:35], v[40:43], v[220:223]\n"
"buffer_load_dwordx2 v[60:61], v193, s[28:31], s38 offen offset:1024 sc1\n"
"ds_read_b64_tr_b16 v[112:113], v182 offset:40584\n"
"v_mfma_f32_16x16x32_bf16 v[220:223], a[44:47], v[44:47], v[220:223]\n"
"s_add_u32 s38, s38, 1572864\n"
"ds_read_b64_tr_b16 v[114:115], v183 offset:40584\n"
"ds_write_b128 v186, v[224:227] offset:20384\n"
"v_mfma_f32_16x16x32_bf16 v[220:223], a[56:59], v[48:51], v[220:223]\n"
"s_waitcnt vmcnt(11)\n"
"v_add3_u32 v200, v200, s50, -7\n"
"v_cmp_lt_u32 vcc, 64, v200\n"
"v_mfma_f32_16x16x32_bf16 v[216:219], a[64:67], v[52:55], v[216:219]\n"
"s_add_u32 m0, s34, 4096\n"
"buffer_store_dwordx4 v[68:71], v195, s[24:27], s36 offen sc1\n"
"v_mfma_f32_16x16x32_bf16 v[220:223], a[68:71], v[52:55], v[220:223]\n"
"s_nop 0\n"
"s_cbranch_vccnz .Lp6%=\n"
".Lr6%=:\n"
"v_mfma_f32_16x16x32_bf16 v[224:227], a[72:75], v[32:35], 0\n"
"s_add_u32 s36, s36, s33\n"
"ds_read_b128 v[68:71], v192 offset:45056\n"
"v_max_f32 v124, v124, v116\n"
"v_mfma_f32_16x16x32_bf16 v[224:227], a[84:87], v[36:39], v[224:227]\n"
"v_max_f32 v125, v125, v117\n"
"v_max_f32 v126, v126, v118\n"
"v_mfma_f32_16x16x32_bf16 v[224:227], a[96:99], v[40:43], v[224:227]\n"
"buffer_load_dwordx4 v199, s[8:11], s76 offen lds\n"
"ds_read_b64_tr_b16 v[116:117], v182 offset:41352\n"
"v_mfma_f32_16x16x32_bf16 v[224:227], a[108:111], v[44:47], v[224:227]\n"
"v_max_f32 v127, v127, v119\n"
"v_max3_f32 v170, v170, v123, v124\n"
"v_mfma_f32_16x16x32_bf16 v[224:227], a[120:123], v[48:51], v[224:227]\n"
"ds_read_b64_tr_b16 v[118:119], v183 offset:41352\n"
"v_max3_f32 v170, v170, v125, v126\n"
"v_mfma_f32_16x16x32_bf16 v[224:227], a[132:135], v[52:55], v[224:227]\n"
"v_mov_b64 v[98:99], 0\n"
"v_mov_b64 v[100:101], 0\n"
"v_mov_b64 v[106:107], 0\n"
"v_mov_b64 v[108:109], 0\n"
"v_max_f32 v171, v170, v127\n"
"v_max_f32 v170, v170, v127\n"
"v_cvt_pk_bf16_f32 v144, v208, v209\n"
"v_cvt_pk_bf16_f32 v145, v210, v211\n"
"v_mfma_f32_16x16x32_bf16 v[208:211], a[76:79], v[32:35], 0\n"
"v_permlane16_swap_b32 v170, v171\n"
"v_mfma_f32_16x16x32_bf16 v[208:211], a[88:91], v[36:39], v[208:211]\n"
"v_max_f32 v170, v170, v171\n"
"v_mov_b32 v171, v170\n"
"v_mfma_f32_16x16x32_bf16 v[208:211], a[100:103], v[40:43], v[208:211]\n"
"s_add_u32 m0, s34, 2048\n"
"v_permlane32_swap_b32 v170, v171\n"
"v_mfma_f32_16x16x32_bf16 v[208:211], a[112:115], v[44:47], v[208:211]\n"
"buffer_load_dwordx4 v190, s[8:11], s1 offen lds\n"
"v_max_f32 v170, v170, v171\n"
"v_mfma_f32_16x16x32_bf16 v[208:211], a[124:127], v[48:51], v[208:211]\n"
"s_add_u32 s1, s1, s33\n"
"v_mul_f32 v170, 0xbe553b95, v170\n"
"v_fmamk_f32 v128, v128, 0x3e553b95, v170\n"
"v_mfma_f32_16x16x32_bf16 v[208:211], a[136:139], v[52:55], v[208:211]\n"
"v_fmamk_f32 v129, v129, 0x3e553b95, v170\n"
"v_fmamk_f32 v130, v130, 0x3e553b95, v170\n"
"v_fmamk_f32 v131, v131, 0x3e553b95, v170\n"
"v_exp_f32 v128, v128\n"
"v_exp_f32 v129, v129\n"
"v_exp_f32 v130, v130\n"
"v_exp_f32 v131, v131\n"
"v_cvt_pk_bf16_f32 v82, v128, v130\n"
"v_cvt_pk_bf16_f32 v83, v129, v131\n"
"v_fmamk_f32 v132, v132, 0x3e553b95, v170\n"
"v_fmamk_f32 v133, v133, 0x3e553b95, v170\n"
"v_fmamk_f32 v134, v134, 0x3e553b95, v170\n"
"v_fmamk_f32 v135, v135, 0x3e553b95, v170\n"
"v_exp_f32 v132, v132\n"
"v_exp_f32 v133, v133\n"
"s_add_u32 m0, s34, 5120\n"
"v_exp_f32 v134, v134\n"
"buffer_load_dwordx4 v199, s[8:11], s77 offen lds\n"
"v_exp_f32 v135, v135\n"
"v_cvt_pk_bf16_f32 v84, v132, v134\n"
"v_cvt_pk_bf16_f32 v85, v133, v135\n"
"v_cvt_pk_bf16_f32 v146, v204, v205\n"
"v_cvt_pk_bf16_f32 v147, v206, v207\n"
"v_mfma_f32_16x16x32_bf16 v[204:207], a[80:83], v[32:35], 0\n"
"s_waitcnt lgkmcnt(0)\n"
"v_mov_b64 v[72:73], -1\n"
"v_fmamk_f32 v120, v120, 0x3e553b95, v170\n"
"v_smfmac_f32_16x16x64_bf16 v[252:255], v[82:85], v[136:143], v66\n"
"s_mul_i32 s8, s33, 0\n"
"buffer_store_dwordx2 v[72:73], v193, s[28:31], s4 offen offset:1024 sc1\n"
"v_smfmac_f32_16x16x64_bf16 v[98:101], v[82:85], v[244:251], v66\n"
"s_mul_i32 s9, s33, 1\n"
"ds_read_b128 v[72:75], v192 offset:43008\n"
"v_smfmac_f32_16x16x64_bf16 v[106:109], v[82:85], v[112:119], v66\n"
"s_mul_i32 s10, s33, 2\n"
"ds_read_b64_tr_b16 v[128:129], v184 offset:128\n"
"v_mfma_f32_16x16x32_bf16 v[82:85], v[172:175], v[82:85], 0\n"
"s_mul_i32 s11, s33, 4\n"
"ds_read_b64_tr_b16 v[130:131], v185 offset:128\n"
"buffer_load_dword v200, off, s[28:31], s39 sc1\n"
"v_mfma_f32_16x16x32_bf16 v[204:207], a[92:95], v[36:39], v[204:207]\n"
"ds_read_b64_tr_b16 v[132:133], v184 offset:896\n"
"ds_read_b64_tr_b16 v[134:135], v185 offset:896\n"
"v_mfma_f32_16x16x32_bf16 v[204:207], a[104:107], v[40:43], v[204:207]\n"
"ds_read_b64_tr_b16 v[136:137], v184 offset:136\n"
"v_cvt_pk_bf16_f32 v158, v212, v213\n"
"v_mfma_f32_16x16x32_bf16 v[204:207], a[116:119], v[44:47], v[204:207]\n"
"ds_read_b64_tr_b16 v[138:139], v185 offset:136\n"
"v_fmamk_f32 v121, v121, 0x3e553b95, v170\n"
"v_mfma_f32_16x16x32_bf16 v[204:207], a[128:131], v[48:51], v[204:207]\n"
"ds_read_b64_tr_b16 v[140:141], v184 offset:904\n"
"v_cvt_pk_bf16_f32 v159, v214, v215\n"
"v_mfma_f32_16x16x32_bf16 v[204:207], a[140:143], v[52:55], v[204:207]\n"
"ds_read_b64_tr_b16 v[142:143], v185 offset:904\n"
"v_fmamk_f32 v122, v122, 0x3e553b95, v170\n"
"v_mfma_f32_16x16x32_bf16 v[212:215], a[144:147], v[32:35], 0\n"
"s_waitcnt lgkmcnt(8)\n"
"ds_read_b64_tr_b16 v[244:245], v184 offset:72\n"
"v_fmamk_f32 v123, v123, 0x3e553b95, v170\n"
"v_mfma_f32_16x16x32_bf16 v[232:235], a[216:219], v[72:75], 0\n"
"ds_read_b64_tr_b16 v[246:247], v185 offset:72\n"
"v_fmamk_f32 v124, v124, 0x3e553b95, v170\n"
"v_mfma_f32_16x16x32_bf16 v[236:239], a[220:223], v[72:75], 0\n"
"ds_read_b64_tr_b16 v[248:249], v184 offset:840\n"
"v_fmamk_f32 v125, v125, 0x3e553b95, v170\n"
"v_mfma_f32_16x16x32_bf16 v[240:243], a[224:227], v[72:75], 0\n"
"ds_read_b128 v[72:75], v192 offset:46080\n"
"v_fmamk_f32 v126, v126, 0x3e553b95, v170\n"
"v_mfma_f32_16x16x32_bf16 v[232:235], a[228:231], v[76:79], v[232:235]\n"
"ds_read_b64_tr_b16 v[250:251], v185 offset:840\n"
"v_fmamk_f32 v127, v127, 0x3e553b95, v170\n"
"v_mfma_f32_16x16x32_bf16 v[236:239], a[232:235], v[76:79], v[236:239]\n"
"v_exp_f32 v120, v120\n"
"v_mfma_f32_16x16x32_bf16 v[240:243], a[236:239], v[76:79], v[240:243]\n"
"ds_read_b128 v[76:79], v192 offset:47104\n"
"ds_read_b128 v[112:115], v180 offset:13504\n"
"v_mfma_f32_16x16x32_bf16 v[232:235], a[240:243], v[68:71], v[232:235]\n"
"v_exp_f32 v121, v121\n"
"v_mfma_f32_16x16x32_bf16 v[236:239], a[244:247], v[68:71], v[236:239]\n"
"v_exp_f32 v122, v122\n"
"v_mfma_f32_16x16x32_bf16 v[240:243], a[248:251], v[68:71], v[240:243]\n"
"ds_read_b128 v[68:71], v192 offset:48128\n"
"v_cvt_pk_bf16_f32 v90, v120, v122\n"
"v_mfma_f32_16x16x32_bf16 v[212:215], a[156:159], v[36:39], v[212:215]\n"
"v_exp_f32 v123, v123\n"
"v_mfma_f32_16x16x32_bf16 v[212:215], a[168:171], v[40:43], v[212:215]\n"
"v_cvt_pk_bf16_f32 v91, v121, v123\n"
"v_exp_f32 v124, v124\n"
"v_mfma_f32_16x16x32_bf16 v[212:215], a[180:183], v[44:47], v[212:215]\n"
"s_waitcnt lgkmcnt(4)\n"
"ds_read_b128 v[120:123], v181 offset:13504\n"
"ds_read_b128 v[116:119], v180 offset:27008\n"
"v_mfma_f32_16x16x32_bf16 v[232:235], a[252:255], v[72:75], v[232:235]\n"
"v_exp_f32 v125, v125\n"
"v_mfma_f32_16x16x32_bf16 v[240:243], v[4:7], v[72:75], v[240:243]\n"
"s_waitcnt lgkmcnt(4)\n"
"v_exp_f32 v126, v126\n"
"v_mfma_f32_16x16x32_bf16 v[236:239], v[0:3], v[72:75], v[236:239]\n"
"s_waitcnt lgkmcnt(3)\n"
"v_exp_f32 v127, v127\n"
"v_mfma_f32_16x16x32_bf16 v[232:235], v[8:11], v[76:79], v[232:235]\n"
"s_waitcnt lgkmcnt(2)\n"
"v_cvt_pk_bf16_f32 v92, v124, v126\n"
"v_cvt_pk_bf16_f32 v93, v125, v127\n"
"v_mfma_f32_16x16x32_bf16 v[240:243], v[16:19], v[76:79], v[240:243]\n"
"ds_read_b128 v[124:127], v181 offset:27008\n"
"v_cvt_pk_bf16_f32 v228, v228, v229\n"
"v_mfma_f32_16x16x32_bf16 v[236:239], v[12:15], v[76:79], v[236:239]\n"
"v_cvt_pk_bf16_f32 v229, v230, v231\n"
"v_cvt_pk_bf16_f32 v230, v216, v217\n"
"v_mfma_f32_16x16x32_bf16 v[232:235], v[20:23], v[68:71], v[232:235]\n"
"v_cvt_pk_bf16_f32 v231, v218, v219\n"
"v_cvt_pk_bf16_f32 v220, v220, v221\n"
"v_mfma_f32_16x16x32_bf16 v[240:243], v[28:31], v[68:71], v[240:243]\n"
"s_waitcnt lgkmcnt(2)\n"
"v_cvt_pk_bf16_f32 v221, v222, v223\n"
"v_cvt_pk_bf16_f32 v224, v224, v225\n"
"v_mfma_f32_16x16x32_bf16 v[236:239], v[24:27], v[68:71], v[236:239]\n"
"s_waitcnt lgkmcnt(1)\n"
"v_cvt_pk_bf16_f32 v232, v232, v233\n"
"v_cvt_pk_bf16_f32 v233, v234, v235\n"
"v_smfmac_f32_16x16x64_bf16 v[252:255], v[90:93], v[128:135], v66\n"
"v_cvt_pk_bf16_f32 v240, v240, v241\n"
"v_smfmac_f32_16x16x64_bf16 v[98:101], v[90:93], v[136:143], v66\n"
"v_cvt_pk_bf16_f32 v241, v242, v243\n"
"v_smfmac_f32_16x16x64_bf16 v[106:109], v[90:93], v[244:251], v66\n"
"ds_write_b64 v197, v[240:241] offset:49152\n"
"v_mfma_f32_16x16x32_bf16 v[90:93], v[172:175], v[90:93], v[82:85]\n"
"s_waitcnt lgkmcnt(1)\n"
"v_cvt_pk_bf16_f32 v234, v236, v237\n"
"v_cvt_pk_bf16_f32 v235, v238, v239\n"
"v_mfma_f32_16x16x32_bf16 v[112:115], v[112:115], v[148:151], 0\n"
"ds_write_b128 v196, v[232:235] offset:49152\n"
"v_mfma_f32_16x16x32_bf16 v[120:123], v[120:123], v[148:151], 0\n"
"v_cvt_pk_bf16_f32 v252, v252, v253\n"
"v_cvt_pk_bf16_f32 v253, v254, v255\n"
"v_mfma_f32_16x16x32_bf16 v[116:119], v[116:119], v[148:151], 0\n"
"v_rcp_f32 v171, v90\n"
"v_mfma_f32_16x16x32_bf16 v[124:127], v[124:127], v[148:151], 0\n"
"ds_read_b64 v[82:83], v180 offset:13568\n"
"ds_read_b64 v[84:85], v181 offset:13568\n"
"v_mfma_f32_16x16x32_bf16 v[216:219], a[148:151], v[32:35], 0\n"
"ds_read_b64 v[90:91], v180 offset:27072\n"
"v_cvt_pk_bf16_f32 v171, v171, v171\n"
"v_mfma_f32_16x16x32_bf16 v[216:219], a[160:163], v[36:39], v[216:219]\n"
"ds_read_b64 v[92:93], v181 offset:27072\n"
"v_and_b32 v64, v171, v176\n"
"v_mfma_f32_16x16x32_bf16 v[216:219], a[172:175], v[40:43], v[216:219]\n"
"ds_read_b128 v[128:131], v178\n"
"v_and_b32 v65, v171, v177\n"
"v_mfma_f32_16x16x32_bf16 v[216:219], a[184:187], v[44:47], v[216:219]\n"
"ds_read_b128 v[136:139], v179\n"
"v_cvt_pk_bf16_f32 v98, v98, v99\n"
"v_mfma_f32_16x16x16_bf16 v[252:255], v[252:253], v[64:65], 0\n"
"s_waitcnt lgkmcnt(6)\n"
"v_cvt_pk_bf16_f32 v99, v100, v101\n"
"v_cvt_pk_bf16_f32 v106, v106, v107\n"
"v_mfma_f32_16x16x32_bf16 v[216:219], a[196:199], v[48:51], v[216:219]\n"
"s_barrier\n"
"v_mfma_f32_16x16x16_bf16 v[98:101], v[98:99], v[64:65], 0\n"
"s_waitcnt vmcnt(11)\n"
"ds_read_b128 v[68:71], v192 offset:30720\n"
"ds_write_b128 v186, v[228:231] offset:33760\n"
"v_mfma_f32_16x16x32_bf16 v[228:231], a[152:155], v[32:35], 0\n"
"s_waitcnt lgkmcnt(6)\n"
"v_cvt_pk_bf16_f32 v107, v108, v109\n"
"v_cvt_pk_bf16_f32 v252, v252, v253\n"
"v_mfma_f32_16x16x32_bf16 v[112:115], v[80:83], v[160:163], v[112:115]\n"
"s_waitcnt lgkmcnt(5)\n"
"v_cvt_pk_bf16_f32 v253, v254, v255\n"
"v_cvt_pk_bf16_f32 v254, v98, v99\n"
"v_mfma_f32_16x16x16_bf16 v[106:109], v[106:107], v[64:65], 0\n"
"s_waitcnt lgkmcnt(4)\n"
"ds_read_b128 v[32:35], v188 offset:6144\n"
"ds_read_b64 v[98:99], v178 offset:64\n"
"v_mfma_f32_16x16x32_bf16 v[120:123], v[84:87], v[162:165], v[120:123]\n"
"s_waitcnt lgkmcnt(5)\n"
"ds_write_b128 v202, v[56:59] offset:55296\n"
"v_mfma_f32_16x16x32_bf16 v[116:119], v[88:91], v[160:163], v[116:119]\n"
"s_waitcnt lgkmcnt(5)\n"
"v_cvt_pk_bf16_f32 v255, v100, v101\n"
"v_cvt_pk_bf16_f32 v106, v106, v107\n"
"v_mfma_f32_16x16x32_bf16 v[124:127], v[92:95], v[162:165], v[124:127]\n"
"s_waitcnt lgkmcnt(4)\n"
"buffer_store_dwordx4 v[252:255], v192, s[28:31], s37 offen sc1\n"
"v_mfma_f32_16x16x32_bf16 v[128:131], v[128:131], v[148:151], 0\n"
"ds_read_b64 v[100:101], v179 offset:64\n"
"ds_read_b128 v[132:135], v178 offset:13504\n"
"v_mfma_f32_16x16x32_bf16 v[136:139], v[136:139], v[148:151], 0\n"
"ds_read_b128 v[140:143], v179 offset:13504\n"
"v_cvt_pk_bf16_f32 v107, v108, v109\n"
"v_mfma_f32_16x16x32_bf16 v[228:231], a[164:167], v[36:39], v[228:231]\n"
"buffer_store_dwordx2 v[106:107], v193, s[28:31], s37 offen offset:1024 sc1\n"
"v_mfma_f32_16x16x32_bf16 v[228:231], a[176:179], v[40:43], v[228:231]\n"
"s_waitcnt lgkmcnt(4)\n"
"ds_read_b128 v[36:39], v189 offset:6144\n"
"ds_read_b128 v[40:43], v188 offset:7168\n"
"v_mfma_f32_16x16x32_bf16 v[128:131], v[96:99], v[160:163], v[128:131]\n"
"s_add_u32 s37, s37, 1572864\n"
"ds_read_b64 v[106:107], v178 offset:13568\n"
"v_max_f32 v112, v112, v120\n"
"v_mfma_f32_16x16x32_bf16 v[228:231], a[188:191], v[44:47], v[228:231]\n"
"ds_read_b128 v[44:47], v189 offset:7168\n"
"v_max_f32 v113, v113, v121\n"
"v_mfma_f32_16x16x32_bf16 v[228:231], a[200:203], v[48:51], v[228:231]\n"
"buffer_load_dwordx4 a[198:201], v192, s[28:31], s38 offen sc1\n"
"ds_read_b64 v[108:109], v179 offset:13568\n"
"v_mfma_f32_16x16x32_bf16 v[232:235], a[216:219], v[68:71], 0\n"
"s_waitcnt lgkmcnt(7)\n"
"ds_read_b128 v[72:75], v202 offset:49152\n"
"v_max_f32 v114, v114, v122\n"
"v_mfma_f32_16x16x32_bf16 v[236:239], a[220:223], v[68:71], 0\n"
"s_waitcnt lgkmcnt(7)\n"
"ds_read_b64_tr_b16 v[244:245], v182 offset:54144\n"
"v_max_f32 v115, v115, v123\n"
"v_mfma_f32_16x16x32_bf16 v[136:139], v[100:103], v[162:165], v[136:139]\n"
"s_waitcnt lgkmcnt(7)\n"
"buffer_load_dwordx2 a[202:203], v193, s[28:31], s38 offen offset:1024 sc1\n"
"ds_read_b64_tr_b16 v[246:247], v183 offset:54144\n"
"v_mfma_f32_16x16x32_bf16 v[132:135], v[132:135], v[148:151], 0\n"
"s_waitcnt vmcnt(10)\n"
"ds_read_b64_tr_b16 v[248:249], v182 offset:54912\n"
"v_max3_f32 v170, v112, v113, v114\n"
"v_mfma_f32_16x16x32_bf16 v[140:143], v[140:143], v[148:151], 0\n"
"s_add_u32 s38, s38, 1572864\n"
"ds_read_b128 v[76:79], v202 offset:53248\n"
"v_max_f32 v116, v116, v124\n"
"v_mfma_f32_16x16x32_bf16 v[240:243], a[224:227], v[68:71], 0\n"
"ds_read_b128 v[68:71], v192 offset:33792\n"
"v_max_f32 v117, v117, v125\n"
"v_mfma_f32_16x16x32_bf16 v[212:215], a[192:195], v[48:51], v[212:215]\n"
"s_waitcnt lgkmcnt(8)\n"
"ds_read_b128 v[48:51], v188 offset:8192\n"
"v_max_f32 v118, v118, v126\n"
"v_mfma_f32_16x16x32_bf16 v[132:135], v[104:107], v[160:163], v[132:135]\n"
"ds_read_b64_tr_b16 v[250:251], v183 offset:54912\n"
"v_max_f32 v119, v119, v127\n"
"v_mfma_f32_16x16x32_bf16 v[212:215], a[204:207], v[52:55], v[212:215]\n"
"s_waitcnt lgkmcnt(8)\n"
"buffer_load_dwordx4 a[204:207], v192, s[28:31], s38 offen sc1\n"
"ds_read_b64_tr_b16 v[120:121], v182 offset:54152\n"
"v_mfma_f32_16x16x32_bf16 v[140:143], v[108:111], v[162:165], v[140:143]\n"
"s_waitcnt lgkmcnt(8)\n"
"ds_read_b64_tr_b16 v[122:123], v183 offset:54152\n"
"v_max3_f32 v170, v170, v115, v116\n"
"v_mfma_f32_16x16x32_bf16 v[216:219], a[208:211], v[52:55], v[216:219]\n"
"ds_read_b64_tr_b16 v[124:125], v182 offset:54920\n"
"v_max3_f32 v170, v170, v117, v118\n"
"v_mfma_f32_16x16x32_bf16 v[228:231], a[212:215], v[52:55], v[228:231]\n"
"buffer_load_dwordx2 a[208:209], v193, s[28:31], s38 offen offset:1024 sc1\n"
"ds_read_b128 v[52:55], v189 offset:8192\n"
"ds_read_b64_tr_b16 v[126:127], v183 offset:54920\n"
"v_max_f32 v128, v128, v136\n"
"v_max_f32 v129, v129, v137\n"
"v_max_f32 v130, v130, v138\n"
"v_max_f32 v131, v131, v139\n"
"v_max3_f32 v170, v170, v119, v128\n"
"v_max3_f32 v170, v170, v129, v130\n"
"v_max_f32 v132, v132, v140\n"
"v_cvt_pk_bf16_f32 v225, v226, v227\n"
"v_max_f32 v133, v133, v141\n"
"v_cvt_pk_bf16_f32 v226, v208, v209\n"
"v_max_f32 v134, v134, v142\n"
"v_cvt_pk_bf16_f32 v227, v210, v211\n"
"v_mfma_f32_16x16x32_bf16 v[208:211], a[0:3], v[32:35], 0\n"
"s_waitcnt lgkmcnt(8)\n"
"buffer_store_dwordx4 v[72:75], v194, s[24:27], s36 offen sc1\n"
"v_mfma_f32_16x16x32_bf16 v[208:211], a[12:15], v[36:39], v[208:211]\n"
"s_add_u32 s38, s38, 1572864\n"
"ds_read_b128 v[72:75], v192 offset:31744\n"
"ds_write_b64 v203, v[60:61] offset:55296\n"
"v_mfma_f32_16x16x32_bf16 v[208:211], a[24:27], v[40:43], v[208:211]\n"
"s_add_u32 s12, s38, 0\n"
"v_max_f32 v135, v135, v143\n"
"v_max3_f32 v170, v170, v131, v132\n"
"v_mfma_f32_16x16x32_bf16 v[208:211], a[36:39], v[44:47], v[208:211]\n"
"s_waitcnt lgkmcnt(8)\n"
"buffer_store_dwordx4 v[76:79], v195, s[24:27], s36 offen sc1\n"
"v_mfma_f32_16x16x32_bf16 v[208:211], a[48:51], v[48:51], v[208:211]\n"
"s_waitcnt lgkmcnt(3)\n"
"ds_read_b128 v[76:79], v192 offset:32768\n"
"ds_read_b64_tr_b16 v[136:137], v182 offset:54088\n"
"v_mfma_f32_16x16x32_bf16 v[208:211], a[60:63], v[52:55], v[208:211]\n"
"s_add_u32 s36, s36, s33\n"
"v_cvt_pk_bf16_f32 v222, v204, v205\n"
"v_max3_f32 v170, v170, v133, v134\n"
"v_cvt_pk_bf16_f32 v223, v206, v207\n"
"v_mfma_f32_16x16x32_bf16 v[204:207], a[4:7], v[32:35], 0\n"
"s_add_u32 s13, s38, 1572864\n"
"ds_read_b64_tr_b16 v[138:139], v183 offset:54088\n"
"ds_write_b128 v186, v[220:223] offset:33824\n"
"v_mfma_f32_16x16x32_bf16 v[220:223], a[8:11], v[32:35], 0\n"
"s_waitcnt lgkmcnt(5)\n"
"v_mov_b64 v[252:253], 0\n"
"v_mov_b64 v[254:255], 0\n"
"v_mfma_f32_16x16x32_bf16 v[232:235], a[228:231], v[72:75], v[232:235]\n"
"s_add_u32 s14, s38, 3145728\n"
"v_mov_b64 v[98:99], 0\n"
"v_mov_b64 v[100:101], 0\n"
"v_mfma_f32_16x16x32_bf16 v[236:239], a[232:235], v[72:75], v[236:239]\n"
"s_add_u32 s16, s38, 4718592\n"
"ds_read_b64_tr_b16 v[140:141], v182 offset:54856\n"
"ds_read_b64_tr_b16 v[142:143], v183 offset:54856\n"
"v_mfma_f32_16x16x32_bf16 v[240:243], a[236:239], v[72:75], v[240:243]\n"
"s_waitcnt lgkmcnt(5)\n"
"ds_read_b128 v[72:75], v192 offset:34816\n"
"s_mov_b64 exec, s[46:47]\n"
"v_add_u32 v183, 0xc0, v183\n"
"s_mov_b64 exec, -1\n"
"v_mfma_f32_16x16x32_bf16 v[232:235], a[240:243], v[76:79], v[232:235]\n"
"s_add_u32 s15, s38, 6291456\n"
"ds_write_b128 v186, v[224:227] offset:33888\n"
"v_mfma_f32_16x16x32_bf16 v[236:239], a[244:247], v[76:79], v[236:239]\n"
"s_add_u32 s17, s38, 7864320\n"
"v_mov_b64 v[106:107], 0\n"
"v_mov_b64 v[108:109], 0\n"
"v_mfma_f32_16x16x32_bf16 v[240:243], a[248:251], v[76:79], v[240:243]\n"
"s_waitcnt vmcnt(8)\n"
"v_max_f32 v171, v170, v135\n"
"v_add3_u32 v200, v200, s50, -8\n"
"v_mfma_f32_16x16x32_bf16 v[232:235], a[252:255], v[68:71], v[232:235]\n"
"s_add_u32 s18, s38, 9437184\n"
"v_cmp_lt_u32 vcc, 64, v200\n"
"v_max_f32 v170, v170, v135\n"
"v_mfma_f32_16x16x32_bf16 v[240:243], v[4:7], v[68:71], v[240:243]\n"
"ds_read_b128 v[76:79], v192 offset:35840\n"
"v_permlane16_swap_b32 v170, v171\n"
"v_mfma_f32_16x16x32_bf16 v[236:239], v[0:3], v[68:71], v[236:239]\n"
"s_cbranch_vccnz .Lp7%=\n"
".Lr7%=:\n"
"v_mfma_f32_16x16x32_bf16 v[204:207], a[16:19], v[36:39], v[204:207]\n"
"s_waitcnt lgkmcnt(2)\n"
"buffer_load_dwordx4 a[144:147], v192, s[28:31], s12 offen sc1\n"
"v_max_f32 v170, v170, v171\n"
"v_mfma_f32_16x16x32_bf16 v[232:235], v[8:11], v[72:75], v[232:235]\n"
"v_mov_b32 v171, v170\n"
"v_cvt_pk_bf16_f32 v148, v212, v213\n"
"v_mfma_f32_16x16x32_bf16 v[240:243], v[16:19], v[72:75], v[240:243]\n"
"v_permlane32_swap_b32 v170, v171\n"
"v_mfma_f32_16x16x32_bf16 v[236:239], v[12:15], v[72:75], v[236:239]\n"
"buffer_load_dwordx2 a[148:149], v193, s[28:31], s12 offen offset:1024 sc1\n"
"v_max_f32 v170, v170, v171\n"
"v_mfma_f32_16x16x32_bf16 v[220:223], a[20:23], v[36:39], v[220:223]\n"
"v_mul_f32 v170, 0xbe553b95, v170\n"
"v_fmamk_f32 v112, v112, 0x3e553b95, v170\n"
"v_mfma_f32_16x16x32_bf16 v[204:207], a[28:31], v[40:43], v[204:207]\n"
"s_waitcnt lgkmcnt(0)\n"
"v_fmamk_f32 v113, v113, 0x3e553b95, v170\n"
"v_fmamk_f32 v114, v114, 0x3e553b95, v170\n"
"v_mfma_f32_16x16x32_bf16 v[220:223], a[32:35], v[40:43], v[220:223]\n"
"v_fmamk_f32 v115, v115, 0x3e553b95, v170\n"
"v_fmamk_f32 v116, v116, 0x3e553b95, v170\n"
"v_mfma_f32_16x16x32_bf16 v[232:235], v[20:23], v[76:79], v[232:235]\n"
"v_exp_f32 v112, v112\n"
"v_mfma_f32_16x16x32_bf16 v[240:243], v[28:31], v[76:79], v[240:243]\n"
"v_exp_f32 v113, v113\n"
"v_mfma_f32_16x16x32_bf16 v[236:239], v[24:27], v[76:79], v[236:239]\n"
"v_exp_f32 v114, v114\n"
"v_mfma_f32_16x16x32_bf16 v[204:207], a[40:43], v[44:47], v[204:207]\n"
"v_exp_f32 v115, v115\n"
"v_mfma_f32_16x16x32_bf16 v[220:223], a[44:47], v[44:47], v[220:223]\n"
"v_cvt_pk_bf16_f32 v82, v112, v114\n"
"v_cvt_pk_bf16_f32 v83, v113, v115\n"
"v_mfma_f32_16x16x32_bf16 v[204:207], a[52:55], v[48:51], v[204:207]\n"
".Lic6%=:\n"
"s_cbranch_execz .Lib1%=\n"
"ds_read_b64_tr_b16 v[112:113], v182 offset:128\n"
"ds_read_b64_tr_b16 v[114:115], v183 offset:128\n"
"v_mfma_f32_16x16x32_bf16 v[220:223], a[56:59], v[48:51], v[220:223]\n"
"v_fmamk_f32 v117, v117, 0x3e553b95, v170\n"
"v_fmamk_f32 v118, v118, 0x3e553b95, v170\n"
"v_mfma_f32_16x16x32_bf16 v[204:207], a[64:67], v[52:55], v[204:207]\n"
"v_fmamk_f32 v119, v119, 0x3e553b95, v170\n"
"v_exp_f32 v116, v116\n"
"v_mfma_f32_16x16x32_bf16 v[220:223], a[68:71], v[52:55], v[220:223]\n"
"v_exp_f32 v117, v117\n"
"v_mfma_f32_16x16x32_bf16 v[224:227], a[72:75], v[32:35], 0\n"
"v_exp_f32 v118, v118\n"
"v_mfma_f32_16x16x32_bf16 v[224:227], a[84:87], v[36:39], v[224:227]\n"
"v_exp_f32 v119, v119\n"
"v_mfma_f32_16x16x32_bf16 v[224:227], a[96:99], v[40:43], v[224:227]\n"
"v_cvt_pk_bf16_f32 v84, v116, v118\n"
"v_cvt_pk_bf16_f32 v85, v117, v119\n"
"v_mfma_f32_16x16x32_bf16 v[224:227], a[108:111], v[44:47], v[224:227]\n"
"ds_read_b64_tr_b16 v[116:117], v182 offset:896\n"
"ds_read_b64_tr_b16 v[118:119], v183 offset:896\n"
"v_smfmac_f32_16x16x64_bf16 v[252:255], v[82:85], v[244:251], v66\n"
"ds_read_b64_tr_b16 v[244:245], v182 offset:136\n"
"v_smfmac_f32_16x16x64_bf16 v[98:101], v[82:85], v[120:127], v66\n"
"ds_read_b64_tr_b16 v[246:247], v183 offset:136\n"
"v_smfmac_f32_16x16x64_bf16 v[106:109], v[82:85], v[136:143], v66\n"
"ds_read_b64_tr_b16 v[248:249], v182 offset:904\n"
"v_mfma_f32_16x16x32_bf16 v[82:85], v[172:175], v[82:85], 0\n"
"ds_read_b64_tr_b16 v[250:251], v183 offset:904\n"
"v_cvt_pk_bf16_f32 v232, v232, v233\n"
"v_mfma_f32_16x16x32_bf16 v[224:227], a[120:123], v[48:51], v[224:227]\n"
"ds_read_b64_tr_b16 v[120:121], v182 offset:72\n"
"v_cvt_pk_bf16_f32 v233, v234, v235\n"
"v_mfma_f32_16x16x32_bf16 v[224:227], a[132:135], v[52:55], v[224:227]\n"
"v_cvt_pk_bf16_f32 v240, v240, v241\n"
"v_cvt_pk_bf16_f32 v234, v236, v237\n"
"v_cvt_pk_bf16_f32 v235, v238, v239\n"
"v_cvt_pk_bf16_f32 v241, v242, v243\n"
"v_cvt_pk_bf16_f32 v149, v214, v215\n"
"v_mfma_f32_16x16x32_bf16 v[212:215], a[76:79], v[32:35], 0\n"
"ds_write_b128 v196, v[232:235] offset:36864\n"
"v_mfma_f32_16x16x32_bf16 v[212:215], a[88:91], v[36:39], v[212:215]\n"
"v_cvt_pk_bf16_f32 v150, v216, v217\n"
"v_cvt_pk_bf16_f32 v151, v218, v219\n"
"v_mfma_f32_16x16x32_bf16 v[216:219], a[80:83], v[32:35], 0\n"
"v_fmamk_f32 v128, v128, 0x3e553b95, v170\n"
"v_cvt_pk_bf16_f32 v162, v228, v229\n"
"v_mfma_f32_16x16x32_bf16 v[216:219], a[92:95], v[36:39], v[216:219]\n"
"ds_write_b64 v197, v[240:241] offset:36864\n"
"v_mfma_f32_16x16x32_bf16 v[212:215], a[100:103], v[40:43], v[212:215]\n"
"s_waitcnt vmcnt(8)\n"
"v_fmamk_f32 v129, v129, 0x3e553b95, v170\n"
"v_cvt_pk_bf16_f32 v163, v230, v231\n"
"v_mfma_f32_16x16x32_bf16 v[216:219], a[104:107], v[40:43], v[216:219]\n"
"v_or_b32 v201, 10, s6\n"
"v_fmamk_f32 v130, v130, 0x3e553b95, v170\n"
"v_mfma_f32_16x16x32_bf16 v[212:215], a[112:115], v[44:47], v[212:215]\n"
"s_mov_b64 exec, 1\n"
"buffer_store_dword v201, off, s[28:31], s40 sc1\n"
"s_mov_b64 exec, -1\n"
"v_mfma_f32_16x16x32_bf16 v[216:219], a[116:119], v[44:47], v[216:219]\n"
"ds_read_b128 v[32:35], v188\n"
"ds_read_b64_tr_b16 v[122:123], v183 offset:72\n"
"v_mfma_f32_16x16x32_bf16 v[212:215], a[124:127], v[48:51], v[212:215]\n"
"ds_read_b64_tr_b16 v[124:125], v182 offset:840\n"
"v_fmamk_f32 v131, v131, 0x3e553b95, v170\n"
"v_mfma_f32_16x16x32_bf16 v[216:219], a[128:131], v[48:51], v[216:219]\n"
"ds_read_b128 v[36:39], v189\n"
"v_fmamk_f32 v132, v132, 0x3e553b95, v170\n"
"v_mfma_f32_16x16x32_bf16 v[212:215], a[136:139], v[52:55], v[212:215]\n"
"s_waitcnt lgkmcnt(4)\n"
"ds_read_b64_tr_b16 v[126:127], v183 offset:840\n"
"v_fmamk_f32 v133, v133, 0x3e553b95, v170\n"
"v_mfma_f32_16x16x32_bf16 v[216:219], a[140:143], v[52:55], v[216:219]\n"
"s_barrier\n"
"ds_read_b128 v[76:79], v192 offset:55296\n"
"ds_read_b128 v[40:43], v188 offset:1024\n"
"v_exp_f32 v128, v128\n"
"v_exp_f32 v129, v129\n"
"v_exp_f32 v130, v130\n"
"v_exp_f32 v131, v131\n"
"v_cvt_pk_bf16_f32 v90, v128, v130\n"
"v_cvt_pk_bf16_f32 v91, v129, v131\n"
"v_fmamk_f32 v134, v134, 0x3e553b95, v170\n"
"v_fmamk_f32 v135, v135, 0x3e553b95, v170\n"
"v_exp_f32 v132, v132\n"
"v_exp_f32 v133, v133\n"
"v_exp_f32 v134, v134\n"
"v_exp_f32 v135, v135\n"
"v_cvt_pk_bf16_f32 v92, v132, v134\n"
"v_cvt_pk_bf16_f32 v93, v133, v135\n"
"v_cvt_pk_bf16_f32 v208, v208, v209\n"
"v_cvt_pk_bf16_f32 v209, v210, v211\n"
"v_smfmac_f32_16x16x64_bf16 v[252:255], v[90:93], v[112:119], v66\n"
"s_waitcnt lgkmcnt(2)\n"
"ds_read_b128 v[44:47], v189 offset:1024\n"
"v_smfmac_f32_16x16x64_bf16 v[98:101], v[90:93], v[244:251], v66\n"
"s_waitcnt lgkmcnt(2)\n"
"ds_read_b128 v[48:51], v188 offset:2048\n"
"v_smfmac_f32_16x16x64_bf16 v[106:109], v[90:93], v[120:127], v66\n"
"ds_read_b128 v[52:55], v189 offset:2048\n"
"v_mfma_f32_16x16x32_bf16 v[90:93], v[172:175], v[90:93], v[82:85]\n"
"ds_read_b128 v[136:139], v180 offset:27008\n"
"ds_read_b128 v[128:131], v181 offset:27008\n"
"v_mfma_f32_16x16x32_bf16 v[232:235], a[216:219], v[76:79], 0\n"
"ds_read_b64 v[82:83], v180 offset:27072\n"
"v_cvt_pk_bf16_f32 v252, v252, v253\n"
"v_mfma_f32_16x16x32_bf16 v[236:239], a[220:223], v[76:79], 0\n"
"ds_read_b64 v[84:85], v181 offset:27072\n"
"v_cvt_pk_bf16_f32 v253, v254, v255\n"
"v_mfma_f32_16x16x32_bf16 v[240:243], a[224:227], v[76:79], 0\n"
"ds_read_b128 v[76:79], v192 offset:58368\n"
"v_cvt_pk_bf16_f32 v98, v98, v99\n"
"v_mfma_f32_16x16x32_bf16 v[228:231], a[0:3], v[32:35], 0\n"
"ds_read_b128 v[140:143], v178\n"
"v_cvt_pk_bf16_f32 v99, v100, v101\n"
"v_mfma_f32_16x16x32_bf16 v[228:231], a[12:15], v[36:39], v[228:231]\n"
"s_waitcnt vmcnt(8) lgkmcnt(9)\n"
"ds_read_b128 v[132:135], v179\n"
"v_cvt_pk_bf16_f32 v106, v106, v107\n"
"v_mfma_f32_16x16x32_bf16 v[228:231], a[24:27], v[40:43], v[228:231]\n"
"s_waitcnt lgkmcnt(9)\n"
"ds_read_b64 v[92:93], v179 offset:64\n"
"v_cvt_pk_bf16_f32 v107, v108, v109\n"
"v_mfma_f32_16x16x32_bf16 v[228:231], a[36:39], v[44:47], v[228:231]\n"
"s_waitcnt lgkmcnt(9)\n"
"ds_read_b128 v[112:115], v178 offset:13504\n"
"v_rcp_f32 v171, v90\n"
"v_mfma_f32_16x16x32_bf16 v[228:231], a[48:51], v[48:51], v[228:231]\n"
"s_waitcnt vmcnt(7) lgkmcnt(8)\n"
"ds_read_b64 v[90:91], v178 offset:64\n"
"v_cvt_pk_bf16_f32 v171, v171, v171\n"
"v_mfma_f32_16x16x32_bf16 v[136:139], v[136:139], v[152:155], 0\n"
"s_waitcnt lgkmcnt(8)\n"
"ds_read_b128 v[244:247], v179 offset:13504\n"
"v_and_b32 v64, v171, v176\n"
"v_mfma_f32_16x16x32_bf16 v[128:131], v[128:131], v[152:155], 0\n"
"s_waitcnt lgkmcnt(8)\n"
"v_and_b32 v65, v171, v177\n"
"v_cvt_pk_bf16_f32 v210, v204, v205\n"
"v_mfma_f32_16x16x32_bf16 v[136:139], v[80:83], v[164:167], v[136:139]\n"
"s_waitcnt lgkmcnt(7)\n"
"ds_write_b128 v202, a[198:201] offset:30720\n"
"v_mfma_f32_16x16x16_bf16 v[252:255], v[252:253], v[64:65], 0\n"
"s_waitcnt lgkmcnt(6)\n"
"v_cvt_pk_bf16_f32 v211, v206, v207\n"
"v_cvt_pk_bf16_f32 v220, v220, v221\n"
"v_mfma_f32_16x16x16_bf16 v[98:101], v[98:99], v[64:65], 0\n"
"s_waitcnt lgkmcnt(5)\n"
"v_cvt_pk_bf16_f32 v221, v222, v223\n"
"v_cvt_pk_bf16_f32 v224, v224, v225\n"
"v_mfma_f32_16x16x16_bf16 v[106:109], v[106:107], v[64:65], 0\n"
"ds_read_b128 v[116:119], v178 offset:27008\n"
"ds_read_b128 v[248:251], v179 offset:27008\n"
"v_mfma_f32_16x16x32_bf16 v[128:131], v[84:87], v[166:169], v[128:131]\n"
"s_waitcnt lgkmcnt(5)\n"
"v_cvt_pk_bf16_f32 v252, v252, v253\n"
"v_cvt_pk_bf16_f32 v253, v254, v255\n"
"v_mfma_f32_16x16x32_bf16 v[140:143], v[140:143], v[152:155], 0\n"
"s_waitcnt lgkmcnt(4)\n"
"ds_read_b128 v[68:71], v202 offset:36864\n"
"v_cvt_pk_bf16_f32 v254, v98, v99\n"
"v_mfma_f32_16x16x32_bf16 v[132:135], v[132:135], v[152:155], 0\n"
"s_waitcnt lgkmcnt(4)\n"
"ds_read_b64 v[98:99], v178 offset:13568\n"
"ds_write_b128 v186, v[208:211] offset:47264\n"
"v_mfma_f32_16x16x32_bf16 v[140:143], v[88:91], v[164:167], v[140:143]\n"
"v_cvt_pk_bf16_f32 v255, v100, v101\n"
"v_cvt_pk_bf16_f32 v106, v106, v107\n"
"v_mfma_f32_16x16x32_bf16 v[132:135], v[92:95], v[166:169], v[132:135]\n"
"buffer_store_dwordx4 v[252:255], v192, s[28:31], s37 offen sc1\n"
"v_mfma_f32_16x16x32_bf16 v[112:115], v[112:115], v[152:155], 0\n"
"s_waitcnt lgkmcnt(4)\n"
"ds_read_b64 v[100:101], v179 offset:13568\n"
"v_cvt_pk_bf16_f32 v107, v108, v109\n"
"v_mfma_f32_16x16x32_bf16 v[244:247], v[244:247], v[152:155], 0\n"
"s_waitcnt lgkmcnt(4)\n"
"ds_read_b64 v[108:109], v179 offset:27072\n"
"buffer_store_dwordx2 v[106:107], v193, s[28:31], s37 offen offset:1024 sc1\n"
"v_mfma_f32_16x16x32_bf16 v[116:119], v[116:119], v[152:155], 0\n"
"s_waitcnt lgkmcnt(3)\n"
"ds_read_b64 v[106:107], v178 offset:27072\n"
"ds_read_b64_tr_b16 v[120:121], v184 offset:128\n"
"v_mfma_f32_16x16x32_bf16 v[248:251], v[248:251], v[152:155], 0\n"
"s_add_u32 s37, s37, 1572864\n"
"ds_read_b64_tr_b16 v[122:123], v185 offset:128\n"
"v_max_f32 v136, v136, v128\n"
"v_mfma_f32_16x16x32_bf16 v[112:115], v[96:99], v[164:167], v[112:115]\n"
"ds_read_b64_tr_b16 v[124:125], v184 offset:896\n"
"v_max_f32 v137, v137, v129\n"
"v_mfma_f32_16x16x32_bf16 v[204:207], a[4:7], v[32:35], 0\n"
"buffer_store_dwordx4 v[68:71], v194, s[24:27], s36 offen sc1\n"
"v_mfma_f32_16x16x32_bf16 v[208:211], a[8:11], v[32:35], 0\n"
"s_waitcnt lgkmcnt(5)\n"
"ds_read_b128 v[68:71], v192 offset:56320\n"
"ds_read_b128 v[72:75], v202 offset:40960\n"
"v_mfma_f32_16x16x32_bf16 v[204:207], a[16:19], v[36:39], v[204:207]\n"
"s_waitcnt lgkmcnt(6)\n"
"ds_read_b64_tr_b16 v[126:127], v185 offset:896\n"
"v_max_f32 v138, v138, v130\n"
"v_mfma_f32_16x16x32_bf16 v[244:247], v[100:103], v[166:169], v[244:247]\n"
"ds_read_b64_tr_b16 v[128:129], v184 offset:136\n"
"v_max_f32 v139, v139, v131\n"
"v_mfma_f32_16x16x32_bf16 v[248:251], v[108:111], v[166:169], v[248:251]\n"
"s_waitcnt lgkmcnt(7)\n"
"ds_read_b64_tr_b16 v[130:131], v185 offset:136\n"
"v_max3_f32 v170, v136, v137, v138\n"
"v_mfma_f32_16x16x32_bf16 v[116:119], v[104:107], v[164:167], v[116:119]\n"
"ds_write_b64 v203, a[202:203] offset:30720\n"
"v_mfma_f32_16x16x32_bf16 v[208:211], a[20:23], v[36:39], v[208:211]\n"
"v_max_f32 v140, v140, v132\n"
"v_max_f32 v141, v141, v133\n"
"v_mfma_f32_16x16x32_bf16 v[204:207], a[28:31], v[40:43], v[204:207]\n"
"ds_read_b64_tr_b16 v[132:133], v184 offset:904\n"
"v_max_f32 v142, v142, v134\n"
"v_mfma_f32_16x16x32_bf16 v[208:211], a[32:35], v[40:43], v[208:211]\n"
"s_waitcnt lgkmcnt(6)\n"
"v_max_f32 v143, v143, v135\n"
"v_max3_f32 v170, v170, v139, v140\n"
"v_mfma_f32_16x16x32_bf16 v[204:207], a[40:43], v[44:47], v[204:207]\n"
"s_waitcnt lgkmcnt(5)\n"
"ds_read_b64_tr_b16 v[134:135], v185 offset:904\n"
"v_max3_f32 v170, v170, v141, v142\n"
"v_mfma_f32_16x16x32_bf16 v[232:235], a[228:231], v[68:71], v[232:235]\n"
"buffer_store_dwordx4 v[72:75], v195, s[24:27], s36 offen sc1\n"
"v_mfma_f32_16x16x32_bf16 v[236:239], a[232:235], v[68:71], v[236:239]\n"
"s_add_u32 s36, s36, s33\n"
"ds_read_b128 v[72:75], v192 offset:57344\n"
"v_max_f32 v112, v112, v244\n"
"v_mfma_f32_16x16x32_bf16 v[240:243], a[236:239], v[68:71], v[240:243]\n"
"ds_read_b128 v[68:71], v192 offset:59392\n"
"v_max_f32 v113, v113, v245\n"
"v_mfma_f32_16x16x32_bf16 v[208:211], a[44:47], v[44:47], v[208:211]\n"
"ds_read_b64_tr_b16 v[244:245], v184 offset:72\n"
"v_max_f32 v114, v114, v246\n"
"v_mfma_f32_16x16x32_bf16 v[204:207], a[52:55], v[48:51], v[204:207]\n"
"v_max_f32 v115, v115, v247\n"
"v_max3_f32 v170, v170, v143, v112\n"
"v_mfma_f32_16x16x32_bf16 v[208:211], a[56:59], v[48:51], v[208:211]\n"
"ds_read_b64_tr_b16 v[246:247], v185 offset:72\n"
"v_max3_f32 v170, v170, v113, v114\n"
"v_mfma_f32_16x16x32_bf16 v[228:231], a[60:63], v[52:55], v[228:231]\n"
"v_max_f32 v116, v116, v248\n"
"v_max_f32 v117, v117, v249\n"
"v_mfma_f32_16x16x32_bf16 v[204:207], a[64:67], v[52:55], v[204:207]\n"
"ds_read_b64_tr_b16 v[248:249], v184 offset:840\n"
"v_max_f32 v118, v118, v250\n"
"v_mfma_f32_16x16x32_bf16 v[208:211], a[68:71], v[52:55], v[208:211]\n"
"v_max_f32 v119, v119, v251\n"
"v_max3_f32 v170, v170, v115, v116\n"
"v_max3_f32 v170, v170, v117, v118\n"
"v_mov_b64 v[252:253], 0\n"
"v_mov_b64 v[254:255], 0\n"
"v_mov_b64 v[98:99], 0\n"
"v_mov_b64 v[100:101], 0\n"
"v_mov_b64 v[106:107], 0\n"
"v_mov_b64 v[108:109], 0\n"
"v_max_f32 v171, v170, v119\n"
"v_max_f32 v170, v170, v119\n"
"v_cvt_pk_bf16_f32 v225, v226, v227\n"
"v_cvt_pk_bf16_f32 v226, v212, v213\n"
"v_permlane16_swap_b32 v170, v171\n"
"v_max_f32 v170, v170, v171\n"
"v_mov_b32 v171, v170\n"
"v_cvt_pk_bf16_f32 v227, v214, v215\n"
"v_mfma_f32_16x16x32_bf16 v[212:215], a[72:75], v[32:35], 0\n"
"s_waitcnt lgkmcnt(4)\n"
"ds_read_b64_tr_b16 v[250:251], v185 offset:840\n"
"ds_write_b128 v186, v[224:227] offset:47392\n"
"v_mfma_f32_16x16x32_bf16 v[232:235], a[240:243], v[72:75], v[232:235]\n"
"v_permlane32_swap_b32 v170, v171\n"
"v_mfma_f32_16x16x32_bf16 v[236:239], a[244:247], v[72:75], v[236:239]\n"
"v_max_f32 v170, v170, v171\n"
"v_mul_f32 v170, 0xbe553b95, v170\n"
"v_mfma_f32_16x16x32_bf16 v[240:243], a[248:251], v[72:75], v[240:243]\n"
"ds_read_b128 v[72:75], v192 offset:60416\n"
"v_fmamk_f32 v136, v136, 0x3e553b95, v170\n"
"v_mfma_f32_16x16x32_bf16 v[232:235], a[252:255], v[76:79], v[232:235]\n"
"s_waitcnt lgkmcnt(6)\n"
"v_fmamk_f32 v137, v137, 0x3e553b95, v170\n"
"v_fmamk_f32 v138, v138, 0x3e553b95, v170\n"
"v_mfma_f32_16x16x32_bf16 v[240:243], v[4:7], v[76:79], v[240:243]\n"
"v_fmamk_f32 v139, v139, 0x3e553b95, v170\n"
"v_fmamk_f32 v140, v140, 0x3e553b95, v170\n"
"v_mfma_f32_16x16x32_bf16 v[236:239], v[0:3], v[76:79], v[236:239]\n"
"v_exp_f32 v136, v136\n"
"v_mfma_f32_16x16x32_bf16 v[232:235], v[8:11], v[68:71], v[232:235]\n"
"v_exp_f32 v137, v137\n"
"v_mfma_f32_16x16x32_bf16 v[240:243], v[16:19], v[68:71], v[240:243]\n"
"v_exp_f32 v138, v138\n"
"v_mfma_f32_16x16x32_bf16 v[236:239], v[12:15], v[68:71], v[236:239]\n"
"v_exp_f32 v139, v139\n"
"v_mfma_f32_16x16x32_bf16 v[212:215], a[84:87], v[36:39], v[212:215]\n"
"v_cvt_pk_bf16_f32 v82, v136, v138\n"
"v_cvt_pk_bf16_f32 v83, v137, v139\n"
"v_mfma_f32_16x16x32_bf16 v[212:215], a[96:99], v[40:43], v[212:215]\n"
"s_waitcnt lgkmcnt(0)\n"
"ds_read_b64_tr_b16 v[136:137], v182 offset:13632\n"
"ds_read_b64_tr_b16 v[138:139], v183 offset:13632\n"
"v_mfma_f32_16x16x32_bf16 v[232:235], v[20:23], v[72:75], v[232:235]\n"
"buffer_load_dword v200, off, s[28:31], s39 sc1\n"
"v_fmamk_f32 v141, v141, 0x3e553b95, v170\n"
"v_mfma_f32_16x16x32_bf16 v[240:243], v[28:31], v[72:75], v[240:243]\n"
"v_fmamk_f32 v142, v142, 0x3e553b95, v170\n"
"v_fmamk_f32 v143, v143, 0x3e553b95, v170\n"
"v_mfma_f32_16x16x32_bf16 v[236:239], v[24:27], v[72:75], v[236:239]\n"
"v_exp_f32 v140, v140\n"
"v_mfma_f32_16x16x32_bf16 v[212:215], a[108:111], v[44:47], v[212:215]\n"
"v_exp_f32 v141, v141\n"
"v_mfma_f32_16x16x32_bf16 v[212:215], a[120:123], v[48:51], v[212:215]\n"
"v_exp_f32 v142, v142\n"
"v_mfma_f32_16x16x32_bf16 v[212:215], a[132:135], v[52:55], v[212:215]\n"
"v_exp_f32 v143, v143\n"
"v_cvt_pk_bf16_f32 v84, v140, v142\n"
"v_cvt_pk_bf16_f32 v85, v141, v143\n"
"v_cvt_pk_bf16_f32 v222, v216, v217\n"
"v_cvt_pk_bf16_f32 v223, v218, v219\n"
"v_smfmac_f32_16x16x64_bf16 v[252:255], v[82:85], v[120:127], v66\n"
"ds_read_b64_tr_b16 v[140:141], v182 offset:14400\n"
"v_smfmac_f32_16x16x64_bf16 v[98:101], v[82:85], v[128:135], v66\n"
"ds_read_b64_tr_b16 v[142:143], v183 offset:14400\n"
"v_smfmac_f32_16x16x64_bf16 v[106:109], v[82:85], v[244:251], v66\n"
"ds_read_b64_tr_b16 v[120:121], v182 offset:13640\n"
"v_mfma_f32_16x16x32_bf16 v[82:85], v[172:175], v[82:85], 0\n"
"ds_read_b64_tr_b16 v[122:123], v183 offset:13640\n"
"ds_read_b64_tr_b16 v[124:125], v182 offset:14408\n"
"v_mfma_f32_16x16x32_bf16 v[216:219], a[76:79], v[32:35], 0\n"
"ds_write_b128 v186, v[220:223] offset:47328\n"
"v_mfma_f32_16x16x32_bf16 v[220:223], a[80:83], v[32:35], 0\n"
"v_cvt_pk_bf16_f32 v232, v232, v233\n"
"v_cvt_pk_bf16_f32 v233, v234, v235\n"
"v_mfma_f32_16x16x32_bf16 v[216:219], a[88:91], v[36:39], v[216:219]\n"
"v_cvt_pk_bf16_f32 v240, v240, v241\n"
"v_cvt_pk_bf16_f32 v234, v236, v237\n"
"v_mfma_f32_16x16x32_bf16 v[220:223], a[92:95], v[36:39], v[220:223]\n"
"ds_read_b64_tr_b16 v[126:127], v183 offset:14408\n"
"ds_read_b64_tr_b16 v[128:129], v182 offset:13576\n"
"v_mfma_f32_16x16x32_bf16 v[216:219], a[100:103], v[40:43], v[216:219]\n"
"ds_read_b64_tr_b16 v[130:131], v183 offset:13576\n"
"v_cvt_pk_bf16_f32 v235, v238, v239\n"
"v_mfma_f32_16x16x32_bf16 v[220:223], a[104:107], v[40:43], v[220:223]\n"
"ds_read_b64_tr_b16 v[132:133], v182 offset:14344\n"
"v_cvt_pk_bf16_f32 v241, v242, v243\n"
"v_mfma_f32_16x16x32_bf16 v[216:219], a[112:115], v[44:47], v[216:219]\n"
"ds_read_b64_tr_b16 v[134:135], v183 offset:14344\n"
"v_fmamk_f32 v112, v112, 0x3e553b95, v170\n"
"v_mfma_f32_16x16x32_bf16 v[220:223], a[116:119], v[44:47], v[220:223]\n"
"ds_write_b128 v196, v[232:235] offset:43008\n"
"v_mfma_f32_16x16x32_bf16 v[216:219], a[124:127], v[48:51], v[216:219]\n"
"v_fmamk_f32 v113, v113, 0x3e553b95, v170\n"
"v_fmamk_f32 v114, v114, 0x3e553b95, v170\n"
"v_mfma_f32_16x16x32_bf16 v[220:223], a[128:131], v[48:51], v[220:223]\n"
"v_fmamk_f32 v115, v115, 0x3e553b95, v170\n"
"v_fmamk_f32 v116, v116, 0x3e553b95, v170\n"
"v_mfma_f32_16x16x32_bf16 v[216:219], a[136:139], v[52:55], v[216:219]\n"
"ds_write_b64 v197, v[240:241] offset:43008\n"
"v_mfma_f32_16x16x32_bf16 v[220:223], a[140:143], v[52:55], v[220:223]\n"
"v_exp_f32 v112, v112\n"
"v_exp_f32 v113, v113\n"
"v_exp_f32 v114, v114\n"
"v_exp_f32 v115, v115\n"
"v_cvt_pk_bf16_f32 v90, v112, v114\n"
"v_cvt_pk_bf16_f32 v91, v113, v115\n"
"v_fmamk_f32 v117, v117, 0x3e553b95, v170\n"
"v_fmamk_f32 v118, v118, 0x3e553b95, v170\n"
"v_fmamk_f32 v119, v119, 0x3e553b95, v170\n"
"v_exp_f32 v116, v116\n"
"v_exp_f32 v117, v117\n"
"v_exp_f32 v118, v118\n"
"v_exp_f32 v119, v119\n"
"v_cvt_pk_bf16_f32 v92, v116, v118\n"
"v_cvt_pk_bf16_f32 v93, v117, v119\n"
"s_mov_b64 exec, s[44:45]\n"
"v_add_u32 v181, 0xc0, v181\n"
"s_mov_b64 exec, -1\n"
"v_cvt_pk_bf16_f32 v228, v228, v229\n"
"v_cvt_pk_bf16_f32 v229, v230, v231\n"
"v_cvt_pk_bf16_f32 v230, v204, v205\n"
"v_cvt_pk_bf16_f32 v231, v206, v207\n"
"v_cvt_pk_bf16_f32 v208, v208, v209\n"
"v_cvt_pk_bf16_f32 v209, v210, v211\n"
"v_cvt_pk_bf16_f32 v212, v212, v213\n"
"v_cvt_pk_bf16_f32 v213, v214, v215\n"
"v_cvt_pk_bf16_f32 v214, v216, v217\n"
"v_cvt_pk_bf16_f32 v215, v218, v219\n"
"v_cvt_pk_bf16_f32 v210, v220, v221\n"
"v_cvt_pk_bf16_f32 v211, v222, v223\n"
"s_waitcnt vmcnt(3) lgkmcnt(0)\n"
"v_smfmac_f32_16x16x64_bf16 v[252:255], v[90:93], v[136:143], v66\n"
"s_barrier\n"
"v_smfmac_f32_16x16x64_bf16 v[98:101], v[90:93], v[120:127], v66\n"
"v_or_b32 v201, 11, s6\n"
"v_smfmac_f32_16x16x64_bf16 v[106:109], v[90:93], v[128:135], v66\n"
"s_mov_b64 exec, 1\n"
"buffer_store_dword v201, off, s[28:31], s40 sc1\n"
"s_mov_b64 exec, -1\n"
"v_mfma_f32_16x16x32_bf16 v[90:93], v[172:175], v[90:93], v[82:85]\n"
"ds_read_b128 v[72:75], v192 offset:30720\n"
"ds_write_b128 v202, a[144:147]\n"
"v_cvt_pk_bf16_f32 v252, v252, v253\n"
"v_cvt_pk_bf16_f32 v253, v254, v255\n"
"v_cvt_pk_bf16_f32 v98, v98, v99\n"
"v_cvt_pk_bf16_f32 v99, v100, v101\n"
"s_nop 1\n"
"v_rcp_f32 v171, v90\n"
"ds_write_b64 v203, a[148:149]\n"
"v_cvt_pk_bf16_f32 v171, v171, v171\n"
"v_and_b32 v64, v171, v176\n"
"v_and_b32 v65, v171, v177\n"
"v_cvt_pk_bf16_f32 v106, v106, v107\n"
"v_cvt_pk_bf16_f32 v107, v108, v109\n"
"v_mfma_f32_16x16x16_bf16 v[252:255], v[252:253], v[64:65], 0\n"
"ds_read_b128 v[244:247], v178\n"
"ds_read_b128 v[112:115], v179\n"
"v_mfma_f32_16x16x16_bf16 v[98:101], v[98:99], v[64:65], 0\n"
"s_waitcnt lgkmcnt(4)\n"
"ds_read_b64 v[82:83], v178 offset:64\n"
"v_mfma_f32_16x16x16_bf16 v[106:109], v[106:107], v[64:65], 0\n"
"ds_read_b64 v[84:85], v179 offset:64\n"
"v_mfma_f32_16x16x32_bf16 v[232:235], a[216:219], v[72:75], 0\n"
"ds_read_b128 v[248:251], v178 offset:13504\n"
"v_cvt_pk_bf16_f32 v252, v252, v253\n"
"v_mfma_f32_16x16x32_bf16 v[236:239], a[220:223], v[72:75], 0\n"
"ds_read_b128 v[116:119], v179 offset:13504\n"
"v_cvt_pk_bf16_f32 v253, v254, v255\n"
"v_mfma_f32_16x16x32_bf16 v[240:243], a[224:227], v[72:75], 0\n"
"ds_read_b128 v[72:75], v192 offset:33792\n"
"v_cvt_pk_bf16_f32 v254, v98, v99\n"
"v_cvt_pk_bf16_f32 v255, v100, v101\n"
"v_cvt_pk_bf16_f32 v106, v106, v107\n"
"v_cvt_pk_bf16_f32 v107, v108, v109\n"
"v_min_u32 v252, -2, v252\n"
"v_min_u32 v253, -2, v253\n"
"v_min_u32 v254, -2, v254\n"
"v_min_u32 v255, -2, v255\n"
"v_min_u32 v106, -2, v106\n"
"v_min_u32 v107, -2, v107\n"
"buffer_store_dwordx4 v[252:255], v192, s[28:31], s37 offen sc1\n"
"ds_read_b64 v[90:91], v178 offset:13568\n"
"ds_read_b64 v[92:93], v179 offset:13568\n"
"ds_read_b128 v[136:139], v178 offset:27008\n"
"v_mov_b64 v[252:253], 0\n"
"v_mov_b64 v[254:255], 0\n"
"s_waitcnt lgkmcnt(8)\n"
"v_mfma_f32_16x16x32_bf16 v[244:247], v[244:247], v[144:147], 0\n"
"s_waitcnt lgkmcnt(5)\n"
"buffer_store_dwordx2 v[106:107], v193, s[28:31], s37 offen offset:1024 sc1\n"
"v_mfma_f32_16x16x32_bf16 v[112:115], v[112:115], v[144:147], 0\n"
"s_waitcnt lgkmcnt(4)\n"
"ds_read_b128 v[120:123], v179 offset:27008\n"
"ds_read_b64 v[98:99], v178 offset:27072\n"
"v_mfma_f32_16x16x32_bf16 v[244:247], v[80:83], v[156:159], v[244:247]\n"
"s_add_u32 s37, s37, 1572864\n"
"ds_write_b128 v202, a[204:207] offset:55296\n"
"v_mfma_f32_16x16x32_bf16 v[112:115], v[84:87], v[158:161], v[112:115]\n"
"v_mfma_f32_16x16x32_bf16 v[248:251], v[248:251], v[144:147], 0\n"
"ds_read_b64 v[100:101], v179 offset:27072\n"
"v_mfma_f32_16x16x32_bf16 v[116:119], v[116:119], v[144:147], 0\n"
"s_waitcnt lgkmcnt(6)\n"
"ds_read_b128 v[140:143], v180\n"
"ds_read_b128 v[124:127], v181\n"
"v_mfma_f32_16x16x32_bf16 v[248:251], v[88:91], v[156:159], v[248:251]\n"
"s_waitcnt lgkmcnt(7)\n"
"ds_read_b64 v[106:107], v180 offset:64\n"
"v_max_f32 v244, v244, v112\n"
"v_mfma_f32_16x16x32_bf16 v[116:119], v[92:95], v[158:161], v[116:119]\n"
"s_waitcnt lgkmcnt(7)\n"
"ds_read_b128 v[76:79], v202 offset:43008\n"
"v_max_f32 v245, v245, v113\n"
"v_mfma_f32_16x16x32_bf16 v[136:139], v[136:139], v[144:147], 0\n"
"s_waitcnt lgkmcnt(7)\n"
"ds_read_b64 v[108:109], v181 offset:64\n"
"v_max_f32 v246, v246, v114\n"
"v_mfma_f32_16x16x32_bf16 v[120:123], v[120:123], v[144:147], 0\n"
"s_waitcnt lgkmcnt(7)\n"
"ds_write_b128 v198, v[228:231]\n"
"v_mfma_f32_16x16x32_bf16 v[136:139], v[96:99], v[156:159], v[136:139]\n"
"s_waitcnt vmcnt(3)\n"
"v_add3_u32 v200, v200, s50, -10\n"
"v_cmp_lt_u32 vcc, 64, v200\n"
"v_max_f32 v247, v247, v115\n"
"v_max3_f32 v170, v244, v245, v246\n"
"v_max_f32 v248, v248, v116\n"
"v_max_f32 v249, v249, v117\n"
"v_max_f32 v250, v250, v118\n"
"v_max_f32 v251, v251, v119\n"
"v_max3_f32 v170, v170, v247, v248\n"
"v_max3_f32 v170, v170, v249, v250\n"
"v_mov_b64 v[98:99], 0\n"
"s_waitcnt lgkmcnt(5)\n"
"v_mfma_f32_16x16x32_bf16 v[120:123], v[100:103], v[158:161], v[120:123]\n"
"s_waitcnt lgkmcnt(4)\n"
"ds_read_b64_tr_b16 v[128:129], v182 offset:128\n"
"ds_read_b128 v[68:71], v202 offset:47104\n"
"v_mfma_f32_16x16x32_bf16 v[140:143], v[140:143], v[144:147], 0\n"
"s_waitcnt lgkmcnt(5)\n"
"ds_read_b64_tr_b16 v[130:131], v183 offset:128\n"
"v_mov_b64 v[100:101], 0\n"
"v_mfma_f32_16x16x32_bf16 v[124:127], v[124:127], v[144:147], 0\n"
"s_waitcnt lgkmcnt(5)\n"
"buffer_store_dwordx4 v[76:79], v194, s[24:27], s36 offen sc1\n"
"v_mfma_f32_16x16x32_bf16 v[140:143], v[104:107], v[156:159], v[140:143]\n"
"s_waitcnt lgkmcnt(4)\n"
"ds_read_b128 v[76:79], v192 offset:31744\n"
"ds_read_b64_tr_b16 v[132:133], v182 offset:896\n"
"v_mfma_f32_16x16x32_bf16 v[124:127], v[108:111], v[158:161], v[124:127]\n"
"s_cbranch_vccnz .Lp9%=\n"
".Lr9%=:\n"
"ds_read_b64_tr_b16 v[134:135], v183 offset:896\n"
"buffer_load_dwordx4 a[150:153], v192, s[28:31], s13 offen sc1\n"
"ds_read_b64_tr_b16 v[112:113], v182 offset:136\n"
"v_max_f32 v136, v136, v120\n"
"v_max_f32 v137, v137, v121\n"
"v_max_f32 v138, v138, v122\n"
"v_max_f32 v139, v139, v123\n"
"v_max3_f32 v170, v170, v251, v136\n"
"ds_read_b64_tr_b16 v[114:115], v183 offset:136\n"
"ds_read_b64_tr_b16 v[116:117], v182 offset:904\n"
"ds_read_b64_tr_b16 v[118:119], v183 offset:904\n"
"v_max3_f32 v170, v170, v137, v138\n"
"v_max_f32 v140, v140, v124\n"
"v_max_f32 v141, v141, v125\n"
"v_max_f32 v142, v142, v126\n"
"v_max_f32 v143, v143, v127\n"
"v_max3_f32 v170, v170, v139, v140\n"
"v_max3_f32 v170, v170, v141, v142\n"
"v_mov_b64 v[106:107], 0\n"
"v_mov_b64 v[108:109], 0\n"
"v_max_f32 v171, v170, v143\n"
"v_max_f32 v170, v170, v143\n"
"buffer_load_dwordx2 a[154:155], v193, s[28:31], s13 offen offset:1024 sc1\n"
"ds_write_b64 v203, a[208:209] offset:55296\n"
"v_permlane16_swap_b32 v170, v171\n"
"v_max_f32 v170, v170, v171\n"
"v_mov_b32 v171, v170\n"
"s_waitcnt lgkmcnt(7)\n"
"v_mfma_f32_16x16x32_bf16 v[232:235], a[228:231], v[76:79], v[232:235]\n"
"ds_read_b64_tr_b16 v[120:121], v182 offset:72\n"
"buffer_load_dwordx4 a[156:159], v192, s[28:31], s14 offen sc1\n"
"v_mfma_f32_16x16x32_bf16 v[236:239], a[232:235], v[76:79], v[236:239]\n"
"ds_read_b64_tr_b16 v[122:123], v183 offset:72\n"
"ds_read_b64_tr_b16 v[124:125], v182 offset:840\n"
"v_mfma_f32_16x16x32_bf16 v[240:243], a[236:239], v[76:79], v[240:243]\n"
"ds_read_b128 v[76:79], v192 offset:34816\n"
"v_permlane32_swap_b32 v170, v171\n"
"ds_read_b64_tr_b16 v[126:127], v183 offset:840\n"
"v_max_f32 v170, v170, v171\n"
"v_mul_f32 v170, 0xbe553b95, v170\n"
"v_fmamk_f32 v244, v244, 0x3e553b95, v170\n"
"buffer_load_dwordx2 a[160:161], v193, s[28:31], s14 offen offset:1024 sc1\n"
"v_fmamk_f32 v245, v245, 0x3e553b95, v170\n"
"v_fmamk_f32 v246, v246, 0x3e553b95, v170\n"
"v_fmamk_f32 v247, v247, 0x3e553b95, v170\n"
"v_exp_f32 v244, v244\n"
"v_exp_f32 v245, v245\n"
"v_exp_f32 v246, v246\n"
"v_exp_f32 v247, v247\n"
"v_cvt_pk_bf16_f32 v82, v244, v246\n"
"v_cvt_pk_bf16_f32 v83, v245, v247\n"
"buffer_store_dwordx4 v[68:71], v195, s[24:27], s36 offen sc1\n"
"s_nop 1\n"
"ds_read_b128 v[68:71], v192 offset:32768\n"
"s_add_u32 s36, s36, s33\n"
"v_fmamk_f32 v248, v248, 0x3e553b95, v170\n"
"v_fmamk_f32 v249, v249, 0x3e553b95, v170\n"
"v_fmamk_f32 v250, v250, 0x3e553b95, v170\n"
"v_fmamk_f32 v251, v251, 0x3e553b95, v170\n"
"v_exp_f32 v248, v248\n"
"v_exp_f32 v249, v249\n"
"v_exp_f32 v250, v250\n"
"v_exp_f32 v251, v251\n"
"v_cvt_pk_bf16_f32 v84, v248, v250\n"
"v_cvt_pk_bf16_f32 v85, v249, v251\n"
"v_fmamk_f32 v136, v136, 0x3e553b95, v170\n"
"v_fmamk_f32 v137, v137, 0x3e553b95, v170\n"
"v_fmamk_f32 v138, v138, 0x3e553b95, v170\n"
"v_fmamk_f32 v139, v139, 0x3e553b95, v170\n"
"v_exp_f32 v136, v136\n"
"v_exp_f32 v137, v137\n"
"v_exp_f32 v138, v138\n"
"v_exp_f32 v139, v139\n"
"v_cvt_pk_bf16_f32 v90, v136, v138\n"
"v_cvt_pk_bf16_f32 v91, v137, v139\n"
"v_fmamk_f32 v140, v140, 0x3e553b95, v170\n"
"v_fmamk_f32 v141, v141, 0x3e553b95, v170\n"
"v_fmamk_f32 v142, v142, 0x3e553b95, v170\n"
"v_fmamk_f32 v143, v143, 0x3e553b95, v170\n"
"v_exp_f32 v140, v140\n"
"v_exp_f32 v141, v141\n"
"v_exp_f32 v142, v142\n"
"v_exp_f32 v143, v143\n"
"v_cvt_pk_bf16_f32 v92, v140, v142\n"
"v_cvt_pk_bf16_f32 v93, v141, v143\n"
"s_waitcnt lgkmcnt(0)\n"
"v_mfma_f32_16x16x32_bf16 v[232:235], a[240:243], v[68:71], v[232:235]\n"
"ds_read_b64_tr_b16 v[244:245], v182 offset:27136\n"
"ds_read_b64_tr_b16 v[246:247], v183 offset:27136\n"
"v_mfma_f32_16x16x32_bf16 v[236:239], a[244:247], v[68:71], v[236:239]\n"
".Lic7%=:\n"
"s_cbranch_execz .Lib1%=\n"
"ds_read_b64_tr_b16 v[248:249], v182 offset:27904\n"
"v_mfma_f32_16x16x32_bf16 v[240:243], a[248:251], v[68:71], v[240:243]\n"
"ds_read_b128 v[68:71], v192 offset:35840\n"
"v_smfmac_f32_16x16x64_bf16 v[252:255], v[82:85], v[128:135], v66\n"
"ds_read_b64_tr_b16 v[250:251], v183 offset:27904\n"
"v_mfma_f32_16x16x32_bf16 v[232:235], a[252:255], v[72:75], v[232:235]\n"
"ds_read_b64_tr_b16 v[128:129], v182 offset:27144\n"
"v_smfmac_f32_16x16x64_bf16 v[98:101], v[82:85], v[112:119], v66\n"
"ds_read_b64_tr_b16 v[130:131], v183 offset:27144\n"
"v_mfma_f32_16x16x32_bf16 v[236:239], v[0:3], v[72:75], v[236:239]\n"
"ds_write_b128 v198, v[208:211] offset:64\n"
"v_smfmac_f32_16x16x64_bf16 v[106:109], v[82:85], v[120:127], v66\n"
"v_mfma_f32_16x16x32_bf16 v[240:243], v[4:7], v[72:75], v[240:243]\n"
"ds_read_b64_tr_b16 v[132:133], v182 offset:27912\n"
"v_mfma_f32_16x16x32_bf16 v[82:85], v[172:175], v[82:85], 0\n"
"ds_read_b64_tr_b16 v[134:135], v183 offset:27912\n"
"ds_read_b64_tr_b16 v[112:113], v182 offset:27080\n"
"v_mfma_f32_16x16x32_bf16 v[232:235], v[8:11], v[76:79], v[232:235]\n"
"s_waitcnt lgkmcnt(7)\n"
"buffer_load_dword v201, off, s[28:31], s39 sc1\n"
"ds_read_b64_tr_b16 v[114:115], v183 offset:27080\n"
"v_mfma_f32_16x16x32_bf16 v[236:239], v[12:15], v[76:79], v[236:239]\n"
"s_waitcnt lgkmcnt(7)\n"
"ds_read_b64_tr_b16 v[116:117], v182 offset:27848\n"
"v_mfma_f32_16x16x32_bf16 v[240:243], v[16:19], v[76:79], v[240:243]\n"
"ds_read_b64_tr_b16 v[118:119], v183 offset:27848\n"
"v_mfma_f32_16x16x32_bf16 v[232:235], v[20:23], v[68:71], v[232:235]\n"
"ds_write_b128 v198, v[212:215] offset:128\n"
"v_mfma_f32_16x16x32_bf16 v[236:239], v[24:27], v[68:71], v[236:239]\n"
"v_smfmac_f32_16x16x64_bf16 v[252:255], v[90:93], v[244:251], v66\n"
"ds_read_b128 v[120:123], v178 offset:13504\n"
"v_mfma_f32_16x16x32_bf16 v[240:243], v[28:31], v[68:71], v[240:243]\n"
"s_waitcnt lgkmcnt(6)\n"
"ds_read_b128 v[136:139], v179 offset:13504\n"
"ds_read_b128 v[124:127], v178 offset:27008\n"
"v_smfmac_f32_16x16x64_bf16 v[98:101], v[90:93], v[128:135], v66\n"
"v_cvt_pk_bf16_f32 v232, v232, v233\n"
"v_cvt_pk_bf16_f32 v233, v234, v235\n"
"v_cvt_pk_bf16_f32 v234, v236, v237\n"
"v_cvt_pk_bf16_f32 v252, v252, v253\n"
"v_cvt_pk_bf16_f32 v253, v254, v255\n"
"v_cvt_pk_bf16_f32 v235, v238, v239\n"
"v_cvt_pk_bf16_f32 v240, v240, v241\n"
"v_cvt_pk_bf16_f32 v241, v242, v243\n"
"v_cvt_pk_bf16_f32 v98, v98, v99\n"
"v_cvt_pk_bf16_f32 v99, v100, v101\n"
"ds_write_b128 v196, v[232:235] offset:49152\n"
"s_waitcnt lgkmcnt(5)\n"
"v_smfmac_f32_16x16x64_bf16 v[106:109], v[90:93], v[112:119], v66\n"
"s_waitcnt lgkmcnt(4)\n"
"s_barrier\n"
"v_mfma_f32_16x16x32_bf16 v[90:93], v[172:175], v[90:93], v[82:85]\n"
"s_waitcnt lgkmcnt(2)\n"
"ds_read_b128 v[72:75], v192 offset:55296\n"
"ds_read_b128 v[76:79], v192 offset:56320\n"
"v_mfma_f32_16x16x32_bf16 v[120:123], v[120:123], v[148:151], 0\n"
"s_waitcnt lgkmcnt(3)\n"
"ds_read_b128 v[68:71], v192 offset:57344\n"
"v_cvt_pk_bf16_f32 v106, v106, v107\n"
"v_mfma_f32_16x16x32_bf16 v[136:139], v[136:139], v[148:151], 0\n"
"ds_write_b64 v197, v[240:241] offset:49152\n"
"v_mfma_f32_16x16x32_bf16 v[124:127], v[124:127], v[148:151], 0\n"
"v_rcp_f32 v171, v90\n"
"v_cvt_pk_bf16_f32 v107, v108, v109\n"
"v_cvt_pk_bf16_f32 v171, v171, v171\n"
"v_and_b32 v64, v171, v176\n"
"v_and_b32 v65, v171, v177\n"
"s_nop 1\n"
"v_mfma_f32_16x16x16_bf16 v[252:255], v[252:253], v[64:65], 0\n"
"buffer_load_dwordx4 v[228:231], v192, s[28:31], s15 offen sc1\n"
"ds_read_b64 v[82:83], v178 offset:13568\n"
"v_mfma_f32_16x16x16_bf16 v[98:101], v[98:99], v[64:65], 0\n"
"ds_read_b64 v[84:85], v179 offset:13568\n"
"ds_read_b128 v[140:143], v179 offset:27008\n"
"v_mfma_f32_16x16x16_bf16 v[106:109], v[106:107], v[64:65], 0\n"
"s_waitcnt lgkmcnt(6)\n"
"ds_read_b64 v[90:91], v178 offset:27072\n"
"v_cvt_pk_bf16_f32 v252, v252, v253\n"
"v_mfma_f32_16x16x32_bf16 v[232:235], a[216:219], v[72:75], 0\n"
"s_waitcnt lgkmcnt(6)\n"
"buffer_load_dwordx2 v[152:153], v193, s[28:31], s15 offen offset:1024 sc1\n"
"ds_read_b64 v[92:93], v179 offset:27072\n"
"v_mfma_f32_16x16x32_bf16 v[236:239], a[220:223], v[72:75], 0\n"
"ds_read_b128 v[244:247], v180\n"
"v_cvt_pk_bf16_f32 v253, v254, v255\n"
"v_mfma_f32_16x16x32_bf16 v[240:243], a[224:227], v[72:75], 0\n"
"ds_read_b128 v[72:75], v192 offset:58368\n"
"v_cvt_pk_bf16_f32 v254, v98, v99\n"
"v_mfma_f32_16x16x32_bf16 v[232:235], a[228:231], v[76:79], v[232:235]\n"
"s_waitcnt lgkmcnt(8)\n"
"ds_read_b128 v[128:131], v181\n"
"v_cvt_pk_bf16_f32 v255, v100, v101\n"
"v_mfma_f32_16x16x32_bf16 v[236:239], a[232:235], v[76:79], v[236:239]\n"
"ds_read_b128 v[248:251], v180 offset:13504\n"
"v_cvt_pk_bf16_f32 v106, v106, v107\n"
"v_mfma_f32_16x16x32_bf16 v[240:243], a[236:239], v[76:79], v[240:243]\n"
"s_waitcnt lgkmcnt(8)\n"
"ds_read_b128 v[76:79], v192 offset:59392\n"
"v_cvt_pk_bf16_f32 v107, v108, v109\n"
"v_mfma_f32_16x16x32_bf16 v[120:123], v[80:83], v[160:163], v[120:123]\n"
"s_waitcnt lgkmcnt(8)\n"
"ds_read_b128 v[132:135], v181 offset:13504\n"
"v_min_u32 v252, -2, v252\n"
"v_mfma_f32_16x16x32_bf16 v[136:139], v[84:87], v[162:165], v[136:139]\n"
"s_waitcnt lgkmcnt(8)\n"
"ds_read_b64 v[98:99], v180 offset:64\n"
"v_min_u32 v253, -2, v253\n"
"v_mfma_f32_16x16x32_bf16 v[140:143], v[140:143], v[148:151], 0\n"
"s_waitcnt lgkmcnt(8)\n"
"ds_read_b64 v[100:101], v181 offset:64\n"
"v_min_u32 v254, -2, v254\n"
"v_mfma_f32_16x16x32_bf16 v[124:127], v[88:91], v[160:163], v[124:127]\n"
"s_waitcnt lgkmcnt(8)\n"
"ds_read_b64 v[108:109], v181 offset:13568\n"
"v_min_u32 v255, -2, v255\n"
"v_mfma_f32_16x16x32_bf16 v[140:143], v[92:95], v[162:165], v[140:143]\n"
"s_waitcnt lgkmcnt(8)\n"
"buffer_store_dwordx4 v[252:255], v192, s[28:31], s37 offen sc1\n"
"v_mfma_f32_16x16x32_bf16 v[244:247], v[244:247], v[148:151], 0\n"
"s_waitcnt lgkmcnt(6)\n"
"ds_read_b64_tr_b16 v[112:113], v182 offset:13632\n"
"ds_read_b64_tr_b16 v[114:115], v183 offset:13632\n"
"v_mfma_f32_16x16x32_bf16 v[128:131], v[128:131], v[148:151], 0\n"
"s_waitcnt lgkmcnt(7)\n"
"ds_read_b64_tr_b16 v[116:117], v182 offset:14400\n"
"v_min_u32 v106, -2, v106\n"
"v_mfma_f32_16x16x32_bf16 v[248:251], v[248:251], v[148:151], 0\n"
"ds_read_b64_tr_b16 v[118:119], v183 offset:14400\n"
"v_min_u32 v107, -2, v107\n"
"v_mfma_f32_16x16x32_bf16 v[232:235], a[240:243], v[68:71], v[232:235]\n"
"s_waitcnt lgkmcnt(7)\n"
"buffer_store_dwordx2 v[106:107], v193, s[28:31], s37 offen offset:1024 sc1\n"
"v_mfma_f32_16x16x32_bf16 v[132:135], v[132:135], v[148:151], 0\n"
"s_waitcnt lgkmcnt(6)\n"
"ds_read_b64 v[106:107], v180 offset:13568\n"
"v_max_f32 v120, v120, v136\n"
"v_mfma_f32_16x16x32_bf16 v[236:239], a[244:247], v[68:71], v[236:239]\n"
"s_waitcnt lgkmcnt(6)\n"
"v_max_f32 v121, v121, v137\n"
"v_max_f32 v122, v122, v138\n"
"v_mfma_f32_16x16x32_bf16 v[240:243], a[248:251], v[68:71], v[240:243]\n"
"s_waitcnt lgkmcnt(5)\n"
"ds_read_b128 v[68:71], v192 offset:60416\n"
"ds_read_b64_tr_b16 v[136:137], v182 offset:13640\n"
"v_mfma_f32_16x16x32_bf16 v[244:247], v[96:99], v[160:163], v[244:247]\n"
"s_add_u32 s37, s37, 1572864\n"
"v_max_f32 v123, v123, v139\n"
"v_max_f32 v124, v124, v140\n"
"v_mfma_f32_16x16x32_bf16 v[128:131], v[100:103], v[162:165], v[128:131]\n"
"ds_read_b64_tr_b16 v[138:139], v183 offset:13640\n"
"v_max_f32 v125, v125, v141\n"
"v_mfma_f32_16x16x32_bf16 v[132:135], v[108:111], v[162:165], v[132:135]\n"
"ds_read_b64_tr_b16 v[140:141], v182 offset:14408\n"
"v_max_f32 v126, v126, v142\n"
"v_mfma_f32_16x16x32_bf16 v[232:235], a[252:255], v[72:75], v[232:235]\n"
"v_max_f32 v127, v127, v143\n"
"v_max3_f32 v170, v120, v121, v122\n"
"v_mfma_f32_16x16x32_bf16 v[240:243], v[4:7], v[72:75], v[240:243]\n"
"ds_read_b64_tr_b16 v[142:143], v183 offset:14408\n"
"v_max3_f32 v170, v170, v123, v124\n"
"v_mfma_f32_16x16x32_bf16 v[236:239], v[0:3], v[72:75], v[236:239]\n"
"s_waitcnt lgkmcnt(5)\n"
"v_max3_f32 v170, v170, v125, v126\n"
"v_max_f32 v244, v244, v128\n"
"v_mfma_f32_16x16x32_bf16 v[248:251], v[104:107], v[160:163], v[248:251]\n"
"v_max_f32 v245, v245, v129\n"
"v_max_f32 v246, v246, v130\n"
"v_mfma_f32_16x16x32_bf16 v[232:235], v[8:11], v[76:79], v[232:235]\n"
"s_waitcnt lgkmcnt(4)\n"
"ds_read_b64_tr_b16 v[128:129], v182 offset:13576\n"
"v_max3_f32 v170, v170, v127, v244\n"
"v_mfma_f32_16x16x32_bf16 v[240:243], v[16:19], v[76:79], v[240:243]\n"
"s_barrier\n"
"v_mfma_f32_16x16x32_bf16 v[236:239], v[12:15], v[76:79], v[236:239]\n"
"ds_read_b128 v[32:35], v192 offset:1024\n"
"ds_read_b128 v[36:39], v192 offset:2048\n"
"v_mfma_f32_16x16x32_bf16 v[232:235], v[20:23], v[68:71], v[232:235]\n"
"s_waitcnt vmcnt(9)\n"
"ds_write_b128 v202, a[150:153] offset:24576\n"
"v_mfma_f32_16x16x32_bf16 v[240:243], v[28:31], v[68:71], v[240:243]\n"
"v_max_f32 v247, v247, v131\n"
"v_max3_f32 v170, v170, v245, v246\n"
"v_mfma_f32_16x16x32_bf16 v[236:239], v[24:27], v[68:71], v[236:239]\n"
"v_max_f32 v248, v248, v132\n"
"ds_read_b128 v[68:71], v192\n"
"ds_read_b128 v[40:43], v192 offset:3072\n"
"ds_read_b128 v[44:47], v192 offset:4096\n"
"v_max_f32 v249, v249, v133\n"
"v_max_f32 v250, v250, v134\n"
"v_max3_f32 v170, v170, v247, v248\n"
"ds_read_b128 v[48:51], v192 offset:5120\n"
"v_max_f32 v251, v251, v135\n"
"v_max3_f32 v170, v170, v249, v250\n"
"s_waitcnt vmcnt(6)\n"
"ds_write_b64 v203, a[154:155] offset:24576\n"
"v_max_f32 v171, v170, v251\n"
"v_max_f32 v170, v170, v251\n"
"v_mov_b64 v[252:253], 0\n"
"v_mov_b64 v[254:255], 0\n"
"ds_write_b128 v202, a[156:159] offset:30720\n"
"v_permlane16_swap_b32 v170, v171\n"
"v_max_f32 v170, v170, v171\n"
"v_mov_b32 v171, v170\n"
"v_mov_b64 v[98:99], 0\n"
"v_mov_b64 v[100:101], 0\n"
"v_permlane32_swap_b32 v170, v171\n"
"v_max_f32 v170, v170, v171\n"
"v_mul_f32 v170, 0xbe553b95, v170\n"
"v_fmamk_f32 v120, v120, 0x3e553b95, v170\n"
"v_fmamk_f32 v121, v121, 0x3e553b95, v170\n"
"v_fmamk_f32 v122, v122, 0x3e553b95, v170\n"
"v_fmamk_f32 v123, v123, 0x3e553b95, v170\n"
"v_fmamk_f32 v124, v124, 0x3e553b95, v170\n"
"v_fmamk_f32 v125, v125, 0x3e553b95, v170\n"
"v_fmamk_f32 v126, v126, 0x3e553b95, v170\n"
"v_fmamk_f32 v127, v127, 0x3e553b95, v170\n"
"v_exp_f32 v120, v120\n"
"v_exp_f32 v121, v121\n"
"v_exp_f32 v122, v122\n"
"v_exp_f32 v123, v123\n"
"v_exp_f32 v124, v124\n"
"v_exp_f32 v125, v125\n"
"v_exp_f32 v126, v126\n"
"v_exp_f32 v127, v127\n"
"v_cvt_pk_bf16_f32 v82, v120, v122\n"
"v_cvt_pk_bf16_f32 v83, v121, v123\n"
"v_cvt_pk_bf16_f32 v84, v124, v126\n"
"v_cvt_pk_bf16_f32 v85, v125, v127\n"
"v_cvt_pk_bf16_f32 v232, v232, v233\n"
"v_cvt_pk_bf16_f32 v233, v234, v235\n"
"v_smfmac_f32_16x16x64_bf16 v[252:255], v[82:85], v[112:119], v66\n"
"s_waitcnt vmcnt(4) lgkmcnt(5)\n"
"v_add3_u32 v201, v201, s50, -11\n"
"v_smfmac_f32_16x16x64_bf16 v[98:101], v[82:85], v[136:143], v66\n"
"v_cmp_lt_u32 vcc, 64, v201\n"
"v_mfma_f32_16x16x32_bf16 v[216:219], a[216:219], v[68:71], 0\n"
"ds_write_b64 v203, a[160:161] offset:30720\n"
"v_mfma_f32_16x16x32_bf16 v[220:223], a[220:223], v[68:71], 0\n"
"s_nop 1\n"
"s_cbranch_vccnz .Lp10%=\n"
".Lr10%=:\n"
"v_mfma_f32_16x16x32_bf16 v[224:227], a[224:227], v[68:71], 0\n"
"buffer_load_dwordx4 a[168:171], v192, s[28:31], s16 offen sc1\n"
"ds_read_b64_tr_b16 v[130:131], v183 offset:13576\n"
"v_mfma_f32_16x16x32_bf16 v[216:219], a[228:231], v[32:35], v[216:219]\n"
"ds_read_b64_tr_b16 v[132:133], v182 offset:14344\n"
"ds_read_b64_tr_b16 v[134:135], v183 offset:14344\n"
"v_mfma_f32_16x16x32_bf16 v[220:223], a[232:235], v[32:35], v[220:223]\n"
"ds_read_b64_tr_b16 v[120:121], v182 offset:40640\n"
"v_cvt_pk_bf16_f32 v240, v240, v241\n"
"v_mfma_f32_16x16x32_bf16 v[224:227], a[236:239], v[32:35], v[224:227]\n"
"buffer_load_dwordx2 a[172:173], v193, s[28:31], s16 offen offset:1024 sc1\n"
"ds_read_b64_tr_b16 v[122:123], v183 offset:40640\n"
"v_mfma_f32_16x16x32_bf16 v[216:219], a[240:243], v[36:39], v[216:219]\n"
"s_waitcnt lgkmcnt(10)\n"
"ds_read_b64_tr_b16 v[124:125], v182 offset:41408\n"
"v_cvt_pk_bf16_f32 v234, v236, v237\n"
"v_mfma_f32_16x16x32_bf16 v[220:223], a[244:247], v[36:39], v[220:223]\n"
"ds_read_b64_tr_b16 v[126:127], v183 offset:41408\n"
"v_cvt_pk_bf16_f32 v235, v238, v239\n"
"v_mfma_f32_16x16x32_bf16 v[224:227], a[248:251], v[36:39], v[224:227]\n"
"ds_read_b64_tr_b16 v[112:113], v182 offset:40648\n"
"v_cvt_pk_bf16_f32 v241, v242, v243\n"
"v_mfma_f32_16x16x32_bf16 v[216:219], a[252:255], v[40:43], v[216:219]\n"
"s_waitcnt lgkmcnt(12)\n"
"ds_read_b64_tr_b16 v[114:115], v183 offset:40648\n"
"v_mov_b64 v[106:107], 0\n"
"v_mfma_f32_16x16x32_bf16 v[220:223], v[0:3], v[40:43], v[220:223]\n"
"ds_read_b64_tr_b16 v[116:117], v182 offset:41416\n"
"v_mov_b64 v[108:109], 0\n"
"v_mfma_f32_16x16x32_bf16 v[224:227], v[4:7], v[40:43], v[224:227]\n"
"ds_read_b64_tr_b16 v[118:119], v183 offset:41416\n"
"v_fmamk_f32 v244, v244, 0x3e553b95, v170\n"
"v_mfma_f32_16x16x32_bf16 v[216:219], v[8:11], v[44:47], v[216:219]\n"
"s_waitcnt lgkmcnt(8)\n"
"ds_read_b64_tr_b16 v[136:137], v182 offset:40584\n"
"v_fmamk_f32 v245, v245, 0x3e553b95, v170\n"
"v_smfmac_f32_16x16x64_bf16 v[106:109], v[82:85], v[128:135], v66\n"
"ds_read_b64_tr_b16 v[138:139], v183 offset:40584\n"
"v_mfma_f32_16x16x32_bf16 v[82:85], v[172:175], v[82:85], 0\n"
"ds_read_b64_tr_b16 v[140:141], v182 offset:41352\n"
"v_fmamk_f32 v246, v246, 0x3e553b95, v170\n"
"v_mfma_f32_16x16x32_bf16 v[220:223], v[12:15], v[44:47], v[220:223]\n"
"ds_read_b64_tr_b16 v[142:143], v183 offset:41352\n"
"v_fmamk_f32 v247, v247, 0x3e553b95, v170\n"
"v_mfma_f32_16x16x32_bf16 v[224:227], v[16:19], v[44:47], v[224:227]\n"
"ds_write_b128 v196, v[232:235] offset:36864\n"
"v_mfma_f32_16x16x32_bf16 v[216:219], v[20:23], v[48:51], v[216:219]\n"
"v_fmamk_f32 v248, v248, 0x3e553b95, v170\n"
"v_fmamk_f32 v249, v249, 0x3e553b95, v170\n"
"v_mfma_f32_16x16x32_bf16 v[220:223], v[24:27], v[48:51], v[220:223]\n"
"v_fmamk_f32 v250, v250, 0x3e553b95, v170\n"
"v_fmamk_f32 v251, v251, 0x3e553b95, v170\n"
"v_mfma_f32_16x16x32_bf16 v[224:227], v[28:31], v[48:51], v[224:227]\n"
"ds_write_b64 v197, v[240:241] offset:36864\n"
"v_exp_f32 v244, v244\n"
"v_exp_f32 v245, v245\n"
"ds_read_b128 v[72:75], v202 offset:49152\n"
"ds_read_b128 v[76:79], v202 offset:53248\n"
"v_exp_f32 v246, v246\n"
"v_exp_f32 v247, v247\n"
"v_exp_f32 v248, v248\n"
"v_exp_f32 v249, v249\n"
"v_exp_f32 v250, v250\n"
"v_exp_f32 v251, v251\n"
"v_cvt_pk_bf16_f32 v90, v244, v246\n"
"v_cvt_pk_bf16_f32 v91, v245, v247\n"
"v_cvt_pk_bf16_f32 v92, v248, v250\n"
"v_cvt_pk_bf16_f32 v93, v249, v251\n"
"v_cvt_pk_bf16_f32 v216, v216, v217\n"
"v_cvt_pk_bf16_f32 v217, v218, v219\n"
"v_cvt_pk_bf16_f32 v218, v220, v221\n"
"v_cvt_pk_bf16_f32 v219, v222, v223\n"
"v_cvt_pk_bf16_f32 v224, v224, v225\n"
"v_cvt_pk_bf16_f32 v225, v226, v227\n"
"s_waitcnt lgkmcnt(4)\n"
"v_smfmac_f32_16x16x64_bf16 v[252:255], v[90:93], v[120:127], v66\n"
"ds_write_b128 v196, v[216:219] offset:12288\n"
"v_smfmac_f32_16x16x64_bf16 v[98:101], v[90:93], v[112:119], v66\n"
"s_waitcnt lgkmcnt(1)\n"
"buffer_store_dwordx4 v[72:75], v194, s[24:27], s36 offen sc1\n"
"v_smfmac_f32_16x16x64_bf16 v[106:109], v[90:93], v[136:143], v66\n"
"ds_write_b64 v197, v[224:225] offset:12288\n"
"v_mfma_f32_16x16x32_bf16 v[90:93], v[172:175], v[90:93], v[82:85]\n"
"s_barrier\n"
"ds_read_b128 v[68:71], v192 offset:25600\n"
"ds_read_b128 v[32:35], v192 offset:26624\n"
"ds_read_b128 v[36:39], v192 offset:27648\n"
"v_cvt_pk_bf16_f32 v252, v252, v253\n"
"v_cvt_pk_bf16_f32 v253, v254, v255\n"
"s_nop 1\n"
"v_rcp_f32 v171, v90\n"
"ds_read_b128 v[40:43], v192 offset:28672\n"
"ds_read_b128 v[44:47], v192 offset:29696\n"
"v_cvt_pk_bf16_f32 v171, v171, v171\n"
"ds_read_b128 v[48:51], v192 offset:30720\n"
"v_and_b32 v64, v171, v176\n"
"v_and_b32 v65, v171, v177\n"
"v_cvt_pk_bf16_f32 v98, v98, v99\n"
"v_cvt_pk_bf16_f32 v99, v100, v101\n"
"v_mfma_f32_16x16x16_bf16 v[252:255], v[252:253], v[64:65], 0\n"
"buffer_store_dwordx4 v[76:79], v195, s[24:27], s36 offen sc1\n"
"v_mfma_f32_16x16x16_bf16 v[98:101], v[98:99], v[64:65], 0\n"
"s_add_u32 s36, s36, s33\n"
"ds_read_b128 v[76:79], v192 offset:24576\n"
"v_cvt_pk_bf16_f32 v106, v106, v107\n"
"v_cvt_pk_bf16_f32 v107, v108, v109\n"
"s_nop 1\n"
"v_mfma_f32_16x16x16_bf16 v[106:109], v[106:107], v[64:65], 0\n"
"buffer_load_dwordx4 v[144:147], v192, s[28:31], s17 offen sc1\n"
"v_cvt_pk_bf16_f32 v252, v252, v253\n"
"v_cvt_pk_bf16_f32 v253, v254, v255\n"
"v_cvt_pk_bf16_f32 v254, v98, v99\n"
"v_cvt_pk_bf16_f32 v255, v100, v101\n"
"v_min_u32 v252, -2, v252\n"
"v_min_u32 v253, -2, v253\n"
"v_min_u32 v254, -2, v254\n"
"v_cvt_pk_bf16_f32 v106, v106, v107\n"
"v_min_u32 v255, -2, v255\n"
"v_cvt_pk_bf16_f32 v107, v108, v109\n"
"v_min_u32 v106, -2, v106\n"
"v_min_u32 v107, -2, v107\n"
"ds_read_b128 v[52:55], v202 offset:36864\n"
"buffer_store_dwordx4 v[252:255], v192, s[28:31], s37 offen sc1\n"
"ds_read_b128 v[72:75], v202 offset:40960\n"
"s_waitcnt lgkmcnt(3)\n"
"v_mfma_f32_16x16x32_bf16 v[204:207], a[216:219], v[48:51], 0\n"
"s_waitcnt lgkmcnt(2)\n"
"v_mfma_f32_16x16x32_bf16 v[232:235], a[216:219], v[76:79], 0\n"
"v_mfma_f32_16x16x32_bf16 v[236:239], a[220:223], v[76:79], 0\n"
"buffer_store_dwordx2 v[106:107], v193, s[28:31], s37 offen offset:1024 sc1\n"
"v_mfma_f32_16x16x32_bf16 v[240:243], a[224:227], v[76:79], 0\n"
"s_add_u32 s37, s37, 1572864\n"
"ds_read_b128 v[76:79], v192 offset:33792\n"
"v_mfma_f32_16x16x32_bf16 v[232:235], a[228:231], v[68:71], v[232:235]\n"
"v_mfma_f32_16x16x32_bf16 v[236:239], a[232:235], v[68:71], v[236:239]\n"
"s_waitcnt lgkmcnt(2)\n"
"v_mfma_f32_16x16x32_bf16 v[240:243], a[236:239], v[68:71], v[240:243]\n"
"s_waitcnt lgkmcnt(1)\n"
"buffer_load_dwordx2 v[154:155], v193, s[28:31], s17 offen offset:1024 sc1\n"
"ds_read_b128 v[68:71], v192 offset:34816\n"
"v_mfma_f32_16x16x32_bf16 v[232:235], a[240:243], v[32:35], v[232:235]\n"
"s_waitcnt vmcnt(7)\n"
"v_mfma_f32_16x16x32_bf16 v[236:239], a[244:247], v[32:35], v[236:239]\n"
"ds_write_b128 v202, a[168:171] offset:49152\n"
"v_mfma_f32_16x16x32_bf16 v[240:243], a[248:251], v[32:35], v[240:243]\n"
"s_waitcnt vmcnt(6)\n"
"buffer_store_dwordx4 v[52:55], v194, s[24:27], s36 offen sc1\n"
"v_mfma_f32_16x16x32_bf16 v[232:235], a[252:255], v[36:39], v[232:235]\n"
"ds_write_b64 v203, a[172:173] offset:49152\n"
"v_mfma_f32_16x16x32_bf16 v[236:239], v[0:3], v[36:39], v[236:239]\n"
"v_max3_u32 v200, v228, v229, v230\n"
"v_max3_u32 v200, v200, v231, v152\n"
"v_mfma_f32_16x16x32_bf16 v[240:243], v[4:7], v[36:39], v[240:243]\n"
"ds_read_b128 v[52:55], v192 offset:31744\n"
"ds_read_b128 v[32:35], v192 offset:35840\n"
"v_mfma_f32_16x16x32_bf16 v[232:235], v[8:11], v[40:43], v[232:235]\n"
"buffer_store_dwordx4 v[72:75], v195, s[24:27], s36 offen sc1\n"
"v_mfma_f32_16x16x32_bf16 v[236:239], v[12:15], v[40:43], v[236:239]\n"
"s_add_u32 s36, s36, s33\n"
"ds_read_b128 v[72:75], v192 offset:32768\n"
"v_max_u32 v200, v200, v153\n"
"v_mfma_f32_16x16x32_bf16 v[240:243], v[16:19], v[40:43], v[240:243]\n"
"s_add_u32 s8, s8, s36\n"
"v_cmp_eq_u32 vcc, -1, v200\n"
"s_add_u32 s9, s9, s36\n"
"v_mfma_f32_16x16x32_bf16 v[232:235], v[20:23], v[44:47], v[232:235]\n"
"s_add_u32 s10, s10, s36\n"
"s_add_u32 s11, s11, s36\n"
"s_add_u32 s20, s20, s36\n"
"v_mfma_f32_16x16x32_bf16 v[236:239], v[24:27], v[44:47], v[236:239]\n"
"s_cbranch_vccnz .Lp11%=\n"
".Lr11%=:\n"
"v_mfma_f32_16x16x32_bf16 v[240:243], v[28:31], v[44:47], v[240:243]\n"
"s_add_u32 s21, s21, s36\n"
"ds_write_b128 v202, v[228:231] offset:18432\n"
"v_mfma_f32_16x16x32_bf16 v[208:211], a[220:223], v[48:51], 0\n"
"s_waitcnt lgkmcnt(3)\n"
"v_cvt_pk_bf16_f32 v232, v232, v233\n"
"v_cvt_pk_bf16_f32 v233, v234, v235\n"
"v_mfma_f32_16x16x32_bf16 v[212:215], a[224:227], v[48:51], 0\n"
"s_add_u32 s22, s22, s36\n"
"v_cvt_pk_bf16_f32 v234, v236, v237\n"
"v_cvt_pk_bf16_f32 v235, v238, v239\n"
"v_mfma_f32_16x16x32_bf16 v[204:207], a[228:231], v[52:55], v[204:207]\n"
"s_waitcnt lgkmcnt(1)\n"
"ds_write_b64 v203, v[152:153] offset:18432\n"
"v_mfma_f32_16x16x32_bf16 v[208:211], a[232:235], v[52:55], v[208:211]\n"
"v_cvt_pk_bf16_f32 v240, v240, v241\n"
"v_cvt_pk_bf16_f32 v241, v242, v243\n"
"v_mfma_f32_16x16x32_bf16 v[212:215], a[236:239], v[52:55], v[212:215]\n"
"ds_write_b128 v196, v[232:235] offset:43008\n"
"v_mfma_f32_16x16x32_bf16 v[204:207], a[240:243], v[72:75], v[204:207]\n"
"v_mfma_f32_16x16x32_bf16 v[208:211], a[244:247], v[72:75], v[208:211]\n"
"ds_write_b64 v197, v[240:241] offset:43008\n"
"v_mfma_f32_16x16x32_bf16 v[212:215], a[248:251], v[72:75], v[212:215]\n"
"v_mfma_f32_16x16x32_bf16 v[204:207], a[252:255], v[76:79], v[204:207]\n"
"s_waitcnt lgkmcnt(2)\n"
"v_mfma_f32_16x16x32_bf16 v[208:211], v[0:3], v[76:79], v[208:211]\n"
"s_barrier\n"
"v_mfma_f32_16x16x32_bf16 v[212:215], v[4:7], v[76:79], v[212:215]\n"
"ds_read_b128 v[44:47], v192 offset:18432\n"
"ds_read_b128 v[48:51], v192 offset:19456\n"
"v_mfma_f32_16x16x32_bf16 v[204:207], v[8:11], v[68:71], v[204:207]\n"
"ds_read_b128 v[52:55], v192 offset:20480\n"
"v_mfma_f32_16x16x32_bf16 v[208:211], v[12:15], v[68:71], v[208:211]\n"
"ds_read_b128 v[72:75], v192 offset:21504\n"
"v_mfma_f32_16x16x32_bf16 v[212:215], v[16:19], v[68:71], v[212:215]\n"
"ds_read_b128 v[76:79], v192 offset:22528\n"
"v_mfma_f32_16x16x32_bf16 v[204:207], v[20:23], v[32:35], v[204:207]\n"
"ds_read_b128 v[68:71], v192 offset:23552\n"
"v_mfma_f32_16x16x32_bf16 v[208:211], v[24:27], v[32:35], v[208:211]\n"
"ds_read_b128 v[36:39], v202 offset:12288\n"
"v_mfma_f32_16x16x32_bf16 v[212:215], v[28:31], v[32:35], v[212:215]\n"
"ds_read_b128 v[40:43], v202 offset:16384\n"
"s_nop 2\n"
"v_cvt_pk_bf16_f32 v204, v204, v205\n"
"v_cvt_pk_bf16_f32 v205, v206, v207\n"
"v_cvt_pk_bf16_f32 v206, v208, v209\n"
"v_cvt_pk_bf16_f32 v207, v210, v211\n"
"v_cvt_pk_bf16_f32 v212, v212, v213\n"
"v_cvt_pk_bf16_f32 v213, v214, v215\n"
"s_waitcnt lgkmcnt(7)\n"
"v_mfma_f32_16x16x32_bf16 v[216:219], a[216:219], v[44:47], 0\n"
"s_waitcnt lgkmcnt(6)\n"
"ds_write_b128 v196, v[204:207] offset:6144\n"
"v_mfma_f32_16x16x32_bf16 v[220:223], a[220:223], v[44:47], 0\n"
"v_mfma_f32_16x16x32_bf16 v[224:227], a[224:227], v[44:47], 0\n"
"ds_write_b64 v197, v[212:213] offset:6144\n"
"v_mfma_f32_16x16x32_bf16 v[216:219], a[228:231], v[48:51], v[216:219]\n"
"s_waitcnt lgkmcnt(3)\n"
"v_mfma_f32_16x16x32_bf16 v[220:223], a[232:235], v[48:51], v[220:223]\n"
"s_waitcnt lgkmcnt(2)\n"
"buffer_store_dwordx4 v[36:39], v194, s[24:27], s8 offen sc1\n"
"v_mfma_f32_16x16x32_bf16 v[224:227], a[236:239], v[48:51], v[224:227]\n"
"v_mfma_f32_16x16x32_bf16 v[216:219], a[240:243], v[52:55], v[216:219]\n"
"v_mfma_f32_16x16x32_bf16 v[220:223], a[244:247], v[52:55], v[220:223]\n"
"buffer_store_dwordx4 v[40:43], v195, s[24:27], s8 offen sc1\n"
"v_mfma_f32_16x16x32_bf16 v[224:227], a[248:251], v[52:55], v[224:227]\n"
"s_waitcnt vmcnt(7) lgkmcnt(0)\n"
"v_max3_u32 v200, v144, v145, v146\n"
"s_barrier\n"
"v_mfma_f32_16x16x32_bf16 v[216:219], a[252:255], v[72:75], v[216:219]\n"
"ds_read_b128 v[48:51], v192 offset:49152\n"
"ds_read_b128 v[52:55], v192 offset:50176\n"
"v_mfma_f32_16x16x32_bf16 v[220:223], v[0:3], v[72:75], v[220:223]\n"
"ds_read_b128 v[32:35], v202 offset:43008\n"
"v_mfma_f32_16x16x32_bf16 v[224:227], v[4:7], v[72:75], v[224:227]\n"
"ds_read_b128 v[72:75], v192 offset:51200\n"
"v_mfma_f32_16x16x32_bf16 v[216:219], v[8:11], v[76:79], v[216:219]\n"
"ds_read_b128 v[36:39], v202 offset:47104\n"
"v_mfma_f32_16x16x32_bf16 v[220:223], v[12:15], v[76:79], v[220:223]\n"
"ds_read_b128 v[40:43], v202 offset:6144\n"
"v_mfma_f32_16x16x32_bf16 v[224:227], v[16:19], v[76:79], v[224:227]\n"
"ds_read_b128 v[76:79], v192 offset:52224\n"
"v_mfma_f32_16x16x32_bf16 v[216:219], v[20:23], v[68:71], v[216:219]\n"
"ds_read_b128 v[44:47], v202 offset:10240\n"
"v_mfma_f32_16x16x32_bf16 v[220:223], v[24:27], v[68:71], v[220:223]\n"
"v_mfma_f32_16x16x32_bf16 v[224:227], v[28:31], v[68:71], v[224:227]\n"
"s_waitcnt lgkmcnt(7)\n"
"ds_read_b128 v[68:71], v192 offset:53248\n"
"v_mfma_f32_16x16x32_bf16 v[232:235], a[216:219], v[48:51], 0\n"
"s_waitcnt lgkmcnt(7)\n"
"s_nop 0\n"
"v_cvt_pk_bf16_f32 v216, v216, v217\n"
"v_mfma_f32_16x16x32_bf16 v[236:239], a[220:223], v[48:51], 0\n"
"s_waitcnt lgkmcnt(6)\n"
"buffer_store_dwordx4 v[32:35], v194, s[24:27], s9 offen sc1\n"
"v_mfma_f32_16x16x32_bf16 v[240:243], a[224:227], v[48:51], 0\n"
"s_waitcnt lgkmcnt(4)\n"
"ds_read_b128 v[32:35], v192 offset:54272\n"
"v_cvt_pk_bf16_f32 v217, v218, v219\n"
"v_mfma_f32_16x16x32_bf16 v[232:235], a[228:231], v[52:55], v[232:235]\n"
"s_waitcnt lgkmcnt(4)\n"
"v_cvt_pk_bf16_f32 v218, v220, v221\n"
"v_cvt_pk_bf16_f32 v219, v222, v223\n"
"v_mfma_f32_16x16x32_bf16 v[236:239], a[232:235], v[52:55], v[236:239]\n"
"ds_write_b128 v196, v[216:219] offset:55296\n"
"v_mfma_f32_16x16x32_bf16 v[240:243], a[236:239], v[52:55], v[240:243]\n"
"s_waitcnt lgkmcnt(3)\n"
"buffer_store_dwordx4 v[36:39], v195, s[24:27], s9 offen sc1\n"
"v_mfma_f32_16x16x32_bf16 v[232:235], a[240:243], v[72:75], v[232:235]\n"
"v_cvt_pk_bf16_f32 v224, v224, v225\n"
"v_cvt_pk_bf16_f32 v225, v226, v227\n"
"v_mfma_f32_16x16x32_bf16 v[236:239], a[244:247], v[72:75], v[236:239]\n"
"ds_write_b64 v197, v[224:225] offset:55296\n"
"v_mfma_f32_16x16x32_bf16 v[240:243], a[248:251], v[72:75], v[240:243]\n"
"buffer_store_dwordx4 v[40:43], v194, s[24:27], s10 offen sc1\n"
"v_mfma_f32_16x16x32_bf16 v[232:235], a[252:255], v[76:79], v[232:235]\n"
"s_waitcnt vmcnt(7) lgkmcnt(3)\n"
"v_max3_u32 v200, v200, v147, v154\n"
"v_mfma_f32_16x16x32_bf16 v[236:239], v[0:3], v[76:79], v[236:239]\n"
"v_max_u32 v200, v200, v155\n"
"v_cmp_eq_u32 vcc, -1, v200\n"
"v_mfma_f32_16x16x32_bf16 v[240:243], v[4:7], v[76:79], v[240:243]\n"
"buffer_store_dwordx4 v[44:47], v195, s[24:27], s10 offen sc1\n"
"v_mfma_f32_16x16x32_bf16 v[232:235], v[8:11], v[68:71], v[232:235]\n"
"s_waitcnt lgkmcnt(2)\n"
"s_nop 0\n"
"s_cbranch_vccnz .Lp12%=\n"
".Lr12%=:\n"
"v_mfma_f32_16x16x32_bf16 v[236:239], v[12:15], v[68:71], v[236:239]\n"
"ds_write_b128 v202, v[144:147] offset:30720\n"
"v_mfma_f32_16x16x32_bf16 v[240:243], v[16:19], v[68:71], v[240:243]\n"
"v_mfma_f32_16x16x32_bf16 v[232:235], v[20:23], v[32:35], v[232:235]\n"
"ds_write_b64 v203, v[154:155] offset:30720\n"
"v_mfma_f32_16x16x32_bf16 v[236:239], v[24:27], v[32:35], v[236:239]\n"
"v_mfma_f32_16x16x32_bf16 v[240:243], v[28:31], v[32:35], v[240:243]\n"
"s_nop 4\n"
"v_cvt_pk_bf16_f32 v232, v232, v233\n"
"v_cvt_pk_bf16_f32 v233, v234, v235\n"
"v_cvt_pk_bf16_f32 v234, v236, v237\n"
"v_cvt_pk_bf16_f32 v235, v238, v239\n"
"v_cvt_pk_bf16_f32 v240, v240, v241\n"
"buffer_load_dwordx4 v[56:59], v192, s[28:31], s18 offen sc1\n"
"v_cvt_pk_bf16_f32 v241, v242, v243\n"
"ds_write_b128 v196, v[232:235]\n"
"s_waitcnt lgkmcnt(1)\n"
"buffer_load_dwordx2 v[60:61], v193, s[28:31], s18 offen offset:1024 sc1\n"
"s_barrier\n"
"ds_read_b128 v[44:47], v192 offset:30720\n"
"ds_read_b128 v[48:51], v192 offset:31744\n"
"ds_read_b128 v[52:55], v192 offset:32768\n"
"ds_read_b128 v[72:75], v192 offset:33792\n"
"ds_read_b128 v[76:79], v192 offset:34816\n"
"ds_read_b128 v[68:71], v192 offset:35840\n"
"ds_read_b128 v[36:39], v202 offset:55296\n"
"ds_write_b64 v197, v[240:241]\n"
"ds_read_b128 v[40:43], v202 offset:59392\n"
"s_waitcnt lgkmcnt(8)\n"
"v_mfma_f32_16x16x32_bf16 v[204:207], a[216:219], v[44:47], 0\n"
"s_waitcnt lgkmcnt(7)\n"
"v_mfma_f32_16x16x32_bf16 v[212:215], a[224:227], v[44:47], 0\n"
"v_mfma_f32_16x16x32_bf16 v[208:211], a[220:223], v[44:47], 0\n"
"v_mfma_f32_16x16x32_bf16 v[204:207], a[228:231], v[48:51], v[204:207]\n"
"s_waitcnt lgkmcnt(6)\n"
"v_mfma_f32_16x16x32_bf16 v[212:215], a[236:239], v[48:51], v[212:215]\n"
"v_mfma_f32_16x16x32_bf16 v[208:211], a[232:235], v[48:51], v[208:211]\n"
"s_waitcnt lgkmcnt(2)\n"
"buffer_store_dwordx4 v[36:39], v194, s[24:27], s11 offen sc1\n"
"v_mfma_f32_16x16x32_bf16 v[204:207], a[240:243], v[52:55], v[204:207]\n"
"s_waitcnt lgkmcnt(0)\n"
"v_mfma_f32_16x16x32_bf16 v[212:215], a[248:251], v[52:55], v[212:215]\n"
"v_mfma_f32_16x16x32_bf16 v[208:211], a[244:247], v[52:55], v[208:211]\n"
"buffer_store_dwordx4 v[40:43], v195, s[24:27], s11 offen sc1\n"
"v_mfma_f32_16x16x32_bf16 v[204:207], a[252:255], v[72:75], v[204:207]\n"
"v_mfma_f32_16x16x32_bf16 v[212:215], v[4:7], v[72:75], v[212:215]\n"
"v_mfma_f32_16x16x32_bf16 v[208:211], v[0:3], v[72:75], v[208:211]\n"
"v_mfma_f32_16x16x32_bf16 v[204:207], v[8:11], v[76:79], v[204:207]\n"
"v_mfma_f32_16x16x32_bf16 v[212:215], v[16:19], v[76:79], v[212:215]\n"
"v_mfma_f32_16x16x32_bf16 v[208:211], v[12:15], v[76:79], v[208:211]\n"
"v_mfma_f32_16x16x32_bf16 v[204:207], v[20:23], v[68:71], v[204:207]\n"
"v_mfma_f32_16x16x32_bf16 v[212:215], v[28:31], v[68:71], v[212:215]\n"
"v_mfma_f32_16x16x32_bf16 v[208:211], v[24:27], v[68:71], v[208:211]\n"
"s_nop 5\n"
"v_cvt_pk_bf16_f32 v204, v204, v205\n"
"v_cvt_pk_bf16_f32 v205, v206, v207\n"
"v_cvt_pk_bf16_f32 v212, v212, v213\n"
"v_cvt_pk_bf16_f32 v206, v208, v209\n"
"v_cvt_pk_bf16_f32 v207, v210, v211\n"
"v_cvt_pk_bf16_f32 v213, v214, v215\n"
"ds_write_b128 v196, v[204:207] offset:36864\n"
"ds_write_b64 v197, v[212:213] offset:36864\n"
"s_waitcnt vmcnt(3)\n"
"v_max3_u32 v200, v56, v57, v58\n"
"s_waitcnt vmcnt(2)\n"
"v_max3_u32 v200, v200, v59, v60\n"
"v_max_u32 v200, v200, v61\n"
"v_cmp_eq_u32 vcc, -1, v200\n"
"s_nop 4\n"
"s_cbranch_vccnz .Lp13%=\n"
".Lr13%=:\n"
"ds_write_b128 v202, v[56:59] offset:24576\n"
"ds_write_b64 v203, v[60:61] offset:24576\n"
"s_waitcnt lgkmcnt(0)\n"
"s_barrier\n"
"ds_read_b128 v[48:51], v192 offset:24576\n"
"ds_read_b128 v[52:55], v192 offset:25600\n"
"ds_read_b128 v[72:75], v192 offset:26624\n"
"ds_read_b128 v[76:79], v192 offset:27648\n"
"ds_read_b128 v[68:71], v192 offset:28672\n"
"ds_read_b128 v[32:35], v202\n"
"ds_read_b128 v[36:39], v202 offset:4096\n"
"ds_read_b128 v[40:43], v202 offset:36864\n"
"ds_read_b128 v[44:47], v202 offset:40960\n"
"s_waitcnt lgkmcnt(8)\n"
"v_mfma_f32_16x16x32_bf16 v[216:219], a[216:219], v[48:51], 0\n"
"s_waitcnt lgkmcnt(7)\n"
"v_mfma_f32_16x16x32_bf16 v[224:227], a[224:227], v[48:51], 0\n"
"v_mfma_f32_16x16x32_bf16 v[220:223], a[220:223], v[48:51], 0\n"
"v_mfma_f32_16x16x32_bf16 v[216:219], a[228:231], v[52:55], v[216:219]\n"
"s_waitcnt lgkmcnt(6)\n"
"v_mfma_f32_16x16x32_bf16 v[224:227], a[236:239], v[52:55], v[224:227]\n"
"s_waitcnt lgkmcnt(3)\n"
"buffer_store_dwordx4 v[32:35], v194, s[24:27], s20 offen sc1\n"
"v_mfma_f32_16x16x32_bf16 v[220:223], a[232:235], v[52:55], v[220:223]\n"
"s_waitcnt lgkmcnt(1)\n"
"ds_read_b128 v[32:35], v192 offset:29696\n"
"v_mfma_f32_16x16x32_bf16 v[216:219], a[240:243], v[72:75], v[216:219]\n"
"s_waitcnt lgkmcnt(1)\n"
"v_mfma_f32_16x16x32_bf16 v[224:227], a[248:251], v[72:75], v[224:227]\n"
"buffer_store_dwordx4 v[36:39], v195, s[24:27], s20 offen sc1\n"
"v_mfma_f32_16x16x32_bf16 v[220:223], a[244:247], v[72:75], v[220:223]\n"
"v_mfma_f32_16x16x32_bf16 v[216:219], a[252:255], v[76:79], v[216:219]\n"
"v_mfma_f32_16x16x32_bf16 v[224:227], v[4:7], v[76:79], v[224:227]\n"
"v_mfma_f32_16x16x32_bf16 v[220:223], v[0:3], v[76:79], v[220:223]\n"
"buffer_store_dwordx4 v[40:43], v194, s[24:27], s21 offen sc1\n"
"v_mfma_f32_16x16x32_bf16 v[216:219], v[8:11], v[68:71], v[216:219]\n"
"s_waitcnt lgkmcnt(0)\n"
"v_mfma_f32_16x16x32_bf16 v[224:227], v[16:19], v[68:71], v[224:227]\n"
"v_mfma_f32_16x16x32_bf16 v[220:223], v[12:15], v[68:71], v[220:223]\n"
"buffer_store_dwordx4 v[44:47], v195, s[24:27], s21 offen sc1\n"
"v_mfma_f32_16x16x32_bf16 v[216:219], v[20:23], v[32:35], v[216:219]\n"
"v_mfma_f32_16x16x32_bf16 v[224:227], v[28:31], v[32:35], v[224:227]\n"
"v_mfma_f32_16x16x32_bf16 v[220:223], v[24:27], v[32:35], v[220:223]\n"
"s_nop 5\n"
"v_cvt_pk_bf16_f32 v216, v216, v217\n"
"v_cvt_pk_bf16_f32 v217, v218, v219\n"
"v_cvt_pk_bf16_f32 v224, v224, v225\n"
"v_cvt_pk_bf16_f32 v218, v220, v221\n"
"v_cvt_pk_bf16_f32 v219, v222, v223\n"
"v_cvt_pk_bf16_f32 v225, v226, v227\n"
"ds_write_b128 v196, v[216:219] offset:12288\n"
"ds_write_b64 v197, v[224:225] offset:12288\n"
"s_waitcnt lgkmcnt(0)\n"
"s_barrier\n"
"ds_read_b128 v[36:39], v202 offset:12288\n"
"ds_read_b128 v[40:43], v202 offset:16384\n"
"s_waitcnt lgkmcnt(1)\n"
"buffer_store_dwordx4 v[36:39], v194, s[24:27], s22 offen sc1\n"
"s_waitcnt lgkmcnt(0)\n"
"buffer_store_dwordx4 v[40:43], v195, s[24:27], s22 offen sc1\n"
"s_branch .Lend%=\n"
".Lp1%=:\n"
"s_sub_u32 s42, s42, 1\n"
"s_cbranch_scc1 .Lt1%=\n"
"s_sleep 1\n"
"buffer_load_dword v200, off, s[28:31], s39 sc1\n"
"s_waitcnt vmcnt(0)\n"
"v_add3_u32 v200, v200, s50, -2\n"
"v_cmp_lt_u32 vcc, 64, v200\n"
"s_nop 4\n"
"s_cbranch_vccnz .Lp1%=\n"
"s_branch .Lr1%=\n"
".Lt1%=:\n"
"s_mov_b32 s42, 0\n"
"s_branch .Lr1%=\n"
".Lp3%=:\n"
"s_sub_u32 s42, s42, 1\n"
"s_cbranch_scc1 .Lt3%=\n"
"s_sleep 1\n"
"buffer_load_dword v200, off, s[28:31], s39 sc1\n"
"s_waitcnt vmcnt(0)\n"
"v_add3_u32 v200, v200, s50, -4\n"
"v_cmp_lt_u32 vcc, 64, v200\n"
"s_nop 4\n"
"s_cbranch_vccnz .Lp3%=\n"
"s_branch .Lr3%=\n"
".Lt3%=:\n"
"s_mov_b32 s42, 0\n"
"s_branch .Lr3%=\n"
".Lp4%=:\n"
"s_sub_u32 s42, s42, 1\n"
"s_cbranch_scc1 .Lt4%=\n"
"s_sleep 1\n"
"buffer_load_dword v200, off, s[28:31], s39 sc1\n"
"s_waitcnt vmcnt(0)\n"
"v_add3_u32 v200, v200, s50, -5\n"
"v_cmp_lt_u32 vcc, 64, v200\n"
"s_nop 4\n"
"s_cbranch_vccnz .Lp4%=\n"
"s_branch .Lr4%=\n"
".Lt4%=:\n"
"s_mov_b32 s42, 0\n"
"s_branch .Lr4%=\n"
".Lp6%=:\n"
"s_sub_u32 s42, s42, 1\n"
"s_cbranch_scc1 .Lt6%=\n"
"s_sleep 1\n"
"buffer_load_dword v200, off, s[28:31], s39 sc1\n"
"s_waitcnt vmcnt(0)\n"
"v_add3_u32 v200, v200, s50, -7\n"
"v_cmp_lt_u32 vcc, 64, v200\n"
"s_nop 4\n"
"s_cbranch_vccnz .Lp6%=\n"
"s_branch .Lr6%=\n"
".Lt6%=:\n"
"s_mov_b32 s42, 0\n"
"s_branch .Lr6%=\n"
".Lp7%=:\n"
"s_sub_u32 s42, s42, 1\n"
"s_cbranch_scc1 .Lt7%=\n"
"s_sleep 1\n"
"buffer_load_dword v200, off, s[28:31], s39 sc1\n"
"s_waitcnt vmcnt(0)\n"
"v_add3_u32 v200, v200, s50, -8\n"
"v_cmp_lt_u32 vcc, 64, v200\n"
"s_nop 4\n"
"s_cbranch_vccnz .Lp7%=\n"
"s_branch .Lr7%=\n"
".Lt7%=:\n"
"s_mov_b32 s42, 0\n"
"s_branch .Lr7%=\n"
".Lp9%=:\n"
"s_sub_u32 s42, s42, 1\n"
"s_cbranch_scc1 .Lt9%=\n"
"s_sleep 1\n"
"buffer_load_dword v200, off, s[28:31], s39 sc1\n"
"s_waitcnt vmcnt(0)\n"
"v_add3_u32 v200, v200, s50, -10\n"
"v_cmp_lt_u32 vcc, 64, v200\n"
"s_nop 4\n"
"s_cbranch_vccnz .Lp9%=\n"
"s_branch .Lr9%=\n"
".Lt9%=:\n"
"s_mov_b32 s42, 0\n"
"s_branch .Lr9%=\n"
".Lp10%=:\n"
"s_sub_u32 s42, s42, 1\n"
"s_cbranch_scc1 .Lt10%=\n"
"s_sleep 1\n"
"buffer_load_dword v201, off, s[28:31], s39 sc1\n"
"s_waitcnt vmcnt(0)\n"
"v_add3_u32 v201, v201, s50, -11\n"
"v_cmp_lt_u32 vcc, 64, v201\n"
"s_nop 4\n"
"s_cbranch_vccnz .Lp10%=\n"
"s_branch .Lr10%=\n"
".Lt10%=:\n"
"s_mov_b32 s42, 0\n"
"s_branch .Lr10%=\n"
".Lp11%=:\n"
"s_sub_u32 s42, s42, 1\n"
"s_cbranch_scc1 .Lt11%=\n"
"buffer_load_dwordx4 v[228:231], v192, s[28:31], s15 offen sc1\n"
"buffer_load_dwordx2 v[152:153], v193, s[28:31], s15 offen offset:1024 sc1\n"
"s_waitcnt vmcnt(0)\n"
"v_max3_u32 v200, v228, v229, v230\n"
"v_max3_u32 v200, v200, v231, v152\n"
"v_max_u32 v200, v200, v153\n"
"v_cmp_eq_u32 vcc, -1, v200\n"
"s_nop 4\n"
"s_cbranch_vccnz .Lp11%=\n"
"s_branch .Lr11%=\n"
".Lt11%=:\n"
"s_mov_b32 s42, 0\n"
"s_branch .Lr11%=\n"
".Lp12%=:\n"
"s_sub_u32 s42, s42, 1\n"
"s_cbranch_scc1 .Lt12%=\n"
"buffer_load_dwordx4 v[144:147], v192, s[28:31], s17 offen sc1\n"
"buffer_load_dwordx2 v[154:155], v193, s[28:31], s17 offen offset:1024 sc1\n"
"s_waitcnt vmcnt(0)\n"
"v_max3_u32 v200, v144, v145, v146\n"
"v_max3_u32 v200, v200, v147, v154\n"
"v_max_u32 v200, v200, v155\n"
"v_cmp_eq_u32 vcc, -1, v200\n"
"s_nop 4\n"
"s_cbranch_vccnz .Lp12%=\n"
"s_branch .Lr12%=\n"
".Lt12%=:\n"
"s_mov_b32 s42, 0\n"
"s_branch .Lr12%=\n"
".Lp13%=:\n"
"s_sub_u32 s42, s42, 1\n"
"s_cbranch_scc1 .Lt13%=\n"
"buffer_load_dwordx4 v[56:59], v192, s[28:31], s18 offen sc1\n"
"buffer_load_dwordx2 v[60:61], v193, s[28:31], s18 offen offset:1024 sc1\n"
"s_waitcnt vmcnt(0)\n"
"v_max3_u32 v200, v56, v57, v58\n"
"v_max3_u32 v200, v200, v59, v60\n"
"v_max_u32 v200, v200, v61\n"
"v_cmp_eq_u32 vcc, -1, v200\n"
"s_nop 4\n"
"s_cbranch_vccnz .Lp13%=\n"
"s_branch .Lr13%=\n"
".Lt13%=:\n"
"s_mov_b32 s42, 0\n"
"s_branch .Lr13%=\n"
".Lend%=:\n"
      : "+{v31}"(tid), "+{s2}"(bid), "+{s[0:1]}"(ka)
      : "i"((__attribute__((address_space(3))) char*)smem)
      : "memory", "scc", "vcc", VCLOB, ACLOB, SCLOB);
}

extern "C" void kernel_launch(void* const* d_in, const int* in_sizes, int n_in,
                              void* d_out, int out_size, void* d_ws, size_t ws_size,
                              hipStream_t stream) {
  const uint16_t* x = (const uint16_t*)d_in[0];
  const uint16_t* wqkv = (const uint16_t*)d_in[1];
  const uint16_t* wo = (const uint16_t*)d_in[2];
  const uint16_t* rpb = (const uint16_t*)d_in[3];
  dinat_hs<<<256, 256, 0, stream>>>(x, wqkv, wo, rpb, (uint16_t*)d_out, (char*)d_ws + (2 << 20)
  );
}
```

```cpp
#include <hip/hip_runtime.h>
#include <hip/hip_bf16.h>
#include <stdint.h>

#define VCLOB "v0", "v1", "v2", "v3", "v4", "v5", "v6", "v7", "v8", "v9", "v10", "v11", "v12", "v13", "v14", "v15", "v16", "v17", "v18", "v19", "v20", "v21", "v22", "v23", "v24", "v25", "v26", "v27", "v28", "v29", "v30", "v32", "v33", "v34", "v35", "v36", "v37", "v38", "v39", "v40", "v41", "v42", "v43", "v44", "v45", "v46", "v47", "v48", "v49", "v50", "v51", "v52", "v53", "v54", "v55", "v56", "v57", "v58", "v59", "v60", "v61", "v62", "v63", "v64", "v65", "v66", "v67", "v68", "v69", "v70", "v71", "v72", "v73", "v74", "v75", "v76", "v77", "v78", "v79", "v80", "v81", "v82", "v83", "v84", "v85", "v86", "v87", "v88", "v89", "v90", "v91", "v92", "v93", "v94", "v95", "v96", "v97", "v98", "v99", "v100", "v101", "v102", "v103", "v104", "v105", "v106", "v107", "v108", "v109", "v110", "v111", "v112", "v113", "v114", "v115", "v116", "v117", "v118", "v119", "v120", "v121", "v122", "v123", "v124", "v125", "v126", "v127", "v128", "v129", "v130", "v131", "v132", "v133", "v134", "v135", "v136", "v137", "v138", "v139", "v140", "v141", "v142", "v143", "v144", "v145", "v146", "v147", "v148", "v149", "v150", "v151", "v152", "v153", "v154", "v155", "v156", "v157", "v158", "v159", "v160", "v161", "v162", "v163", "v164", "v165", "v166", "v167", "v168", "v169", "v170", "v171", "v172", "v173", "v174", "v175", "v176", "v177", "v178", "v179", "v180", "v181", "v182", "v183", "v184", "v185", "v186", "v187", "v188", "v189", "v190", "v191", "v192", "v193", "v194", "v195", "v196", "v197", "v198", "v199", "v200", "v201", "v202", "v203", "v204", "v205", "v206", "v207", "v208", "v209", "v210", "v211", "v212", "v213", "v214", "v215", "v216", "v217", "v218", "v219", "v220", "v221", "v222", "v223", "v224", "v225", "v226", "v227", "v228", "v229", "v230", "v231", "v232", "v233", "v234", "v235", "v236", "v237", "v238", "v239", "v240", "v241", "v242", "v243", "v244", "v245", "v246", "v247", "v248", "v249", "v250", "v251", "v252", "v253", "v254", "v255"
#define ACLOB "a0", "a1", "a2", "a3", "a4", "a5", "a6", "a7", "a8", "a9", "a10", "a11", "a12", "a13", "a14", "a15", "a16", "a17", "a18", "a19", "a20", "a21", "a22", "a23", "a24", "a25", "a26", "a27", "a28", "a29", "a30", "a31", "a32", "a33", "a34", "a35", "a36", "a37", "a38", "a39", "a40", "a41", "a42", "a43", "a44", "a45", "a46", "a47", "a48", "a49", "a50", "a51", "a52", "a53", "a54", "a55", "a56", "a57", "a58", "a59", "a60", "a61", "a62", "a63", "a64", "a65", "a66", "a67", "a68", "a69", "a70", "a71", "a72", "a73", "a74", "a75", "a76", "a77", "a78", "a79", "a80", "a81", "a82", "a83", "a84", "a85", "a86", "a87", "a88", "a89", "a90", "a91", "a92", "a93", "a94", "a95", "a96", "a97", "a98", "a99", "a100", "a101", "a102", "a103", "a104", "a105", "a106", "a107", "a108", "a109", "a110", "a111", "a112", "a113", "a114", "a115", "a116", "a117", "a118", "a119", "a120", "a121", "a122", "a123", "a124", "a125", "a126", "a127", "a128", "a129", "a130", "a131", "a132", "a133", "a134", "a135", "a136", "a137", "a138", "a139", "a140", "a141", "a142", "a143", "a144", "a145", "a146", "a147", "a148", "a149", "a150", "a151", "a152", "a153", "a154", "a155", "a156", "a157", "a158", "a159", "a160", "a161", "a162", "a163", "a164", "a165", "a166", "a167", "a168", "a169", "a170", "a171", "a172", "a173", "a174", "a175", "a176", "a177", "a178", "a179", "a180", "a181", "a182", "a183", "a184", "a185", "a186", "a187", "a188", "a189", "a190", "a191", "a192", "a193", "a194", "a195", "a196", "a197", "a198", "a199", "a200", "a201", "a202", "a203", "a204", "a205", "a206", "a207", "a208", "a209", "a210", "a211", "a212", "a213", "a214", "a215", "a216", "a217", "a218", "a219", "a220", "a221", "a222", "a223", "a224", "a225", "a226", "a227", "a228", "a229", "a230", "a231", "a232", "a233", "a234", "a235", "a236", "a237", "a238", "a239", "a240", "a241", "a242", "a243", "a244", "a245", "a246", "a247", "a248", "a249", "a250", "a251", "a252", "a253", "a254", "a255"
#define SCLOB "s3", "s4", "s5", "s6", "s7", "s8", "s9", "s10", "s11", "s12", "s13", "s14", "s15", "s16", "s17", "s18", "s19", "s20", "s21", "s22", "s23", "s24", "s25", "s26", "s27", "s28", "s29", "s30", "s31", "s32", "s33", "s34", "s35", "s36", "s37", "s38", "s39", "s40", "s41", "s42", "s43", "s44", "s45", "s46", "s47", "s48", "s49", "s50", "s51", "s52", "s53", "s54", "s55", "s56", "s57", "s58", "s59", "s60", "s61", "s62", "s63", "s64", "s65", "s66", "s67", "s68", "s69", "s70", "s71", "s72", "s73", "s74", "s75", "s76", "s77", "s78", "s79", "s80", "s81", "s82", "s83", "s84", "s85", "s86", "s87", "s88", "s89", "s90", "s91", "s92", "s93", "s94", "s95", "s96", "s97", "s98", "s99", "s100", "s101"
#define LDS_BYTES 163392

__global__ __launch_bounds__(256, 1) __attribute__((aligned(4096))) void dinat_hs(const uint16_t* __restrict__ x, const uint16_t* __restrict__ wqkv,
                                                   const uint16_t* __restrict__ wo, const uint16_t* __restrict__ rpb,
                                                   uint16_t* __restrict__ out, char* __restrict__ ws
                                                   ) {
  __shared__ __attribute__((aligned(16))) char smem[LDS_BYTES];
  unsigned tid = threadIdx.x, bid = blockIdx.x;
  auto ka = __builtin_amdgcn_kernarg_segment_ptr();
  asm volatile(
"s_load_dwordx2 s[8:9], s[0:1], 0\n"
"s_load_dwordx2 s[12:13], s[0:1], 8\n"
"s_load_dwordx2 s[16:17], s[0:1], 16\n"
"s_load_dwordx2 s[20:21], s[0:1], 24\n"
"s_load_dwordx2 s[24:25], s[0:1], 32\n"
"s_load_dwordx2 s[28:29], s[0:1], 40\n"
"s_getpc_b64 s[4:5]\n"
".Lpc%=:\n"
"s_bfe_u32 s53, s2, 0x20005\n"
"s_and_b32 s54, s2, 1\n"
"s_lshr_b32 s55, s2, 7\n"
"s_mul_i32 s59, s53, 27\n"
"v_lshrrev_b32 v113, 6, v31\n"
"s_max_u32 s59, s59, 1\n"
"s_mul_i32 s58, s55, 4816896\n"
"s_mul_i32 s71, s54, 14278656\n"
"s_nop 0\n"
"v_readfirstlane_b32 s51, v113\n"
"s_sub_u32 s59, s59, 1\n"
"s_add_u32 s58, s58, s71\n"
"v_and_b32 v112, 63, v31\n"
"s_mul_i32 s72, s59, 768\n"
"s_lshr_b32 s71, s2, 3\n"
"s_add_u32 s58, s58, s72\n"
"s_and_b32 s73, s2, 4\n"
"s_lshl2_add_u32 s71, s71, s51\n"
"v_min_u32 v121, 2, v112\n"
"s_xnor_b32 s56, s54, s55\n"
"s_mul_i32 s73, s73, 21504\n"
"s_and_b32 s74, s2, 2\n"
"s_mul_i32 s72, s71, 384\n"
"v_lshlrev_b32 v121, 7, v121\n"
"s_and_b32 s56, s56, 1\n"
"s_add_u32 s58, s58, s73\n"
"s_mul_i32 s74, s74, 192\n"
"v_add_u32 v121, s72, v121\n"
"s_bfe_u32 s52, s2, 0x20003\n"
"s_mul_i32 s57, s54, -344064\n"
"s_add_u32 s58, s58, s74\n"
"v_lshlrev_b32 v120, 4, v112\n"
"s_mul_i32 s85, s56, 3\n"
"v_min_u32 v121, .Lend%=-.Lpc%=-4, v121\n"
"s_add_u32 s57, s57, 172032\n"
"s_lshl_b32 s61, s51, 3\n"
"v_lshrrev_b32 v114, 3, v112\n"
"v_xor_b32 v115, v120, v112\n"
"s_sub_u32 s86, s85, 3\n"
"s_mov_b32 m0, 163136\n"
"s_mul_i32 s73, s52, 338\n"
"global_load_lds_dword v121, s[4:5]\n"
"s_and_b32 s74, s51, 1\n"
"s_lshr_b32 s84, s2, 5\n"
"v_and_b32 v115, 0x70, v115\n"
"v_add_u32 v114, s61, v114\n"
"s_mul_i32 s86, s86, s57\n"
"s_and_b32 s73, s73, -4\n"
"s_mul_i32 s74, s74, 88\n"
"v_mul_u32_u24 v123, 0x2aab, v112\n"
"v_add_u32 v124, 64, v112\n"
"s_mov_b32 s22, 1352\n"
"s_mov_b32 s23, 0x20000\n"
"s_add_u32 s84, s84, 0\n"
"v_xor_b32 v116, 64, v115\n"
"v_mul_u32_u24 v117, 0x300, v114\n"
"s_add_u32 s86, s86, s58\n"
"v_max_u32 v118, s85, v114\n"
"s_add_u32 s73, s73, s74\n"
"v_lshlrev_b32 v122, 2, v112\n"
"v_lshrrev_b32 v123, 16, v123\n"
"v_mul_u32_u24 v124, 0x2aab, v124\n"
"s_and_b32 s65, s51, 1\n"
"s_mul_i32 s84, s84, 2\n"
"v_add3_u32 v119, v117, s86, v116\n"
"v_lshlrev_b32 v126, 1, v123\n"
"v_lshrrev_b32 v124, 16, v124\n"
"s_lshr_b32 s66, s51, 1\n"
"s_mul_i32 s70, s52, 96\n"
"s_mul_i32 s85, s65, 384\n"
"s_lshr_b32 s75, 0x9249200, s84\n"
"v_and_b32 v126, 24, v126\n"
"v_lshlrev_b32 v127, 1, v124\n"
"s_add_u32 s85, s85, s70\n"
"s_mul_i32 s86, s66, 36864\n"
"s_movk_i32 s69, 1152\n"
"s_and_b32 s75, s75, 0x180\n"
"v_and_or_b32 v126, v123, 3, v126\n"
"v_and_b32 v127, 24, v127\n"
"v_add_u32 v125, 128, v112\n"
"s_add_u32 s85, s85, s86\n"
"v_mad_u32_u24 v126, v126, s69, v120\n"
"v_mul_u32_u24 v27, 96, v123\n"
"v_and_or_b32 v127, v124, 3, v127\n"
"v_lshrrev_b32 v128, 2, v124\n"
"v_mul_u32_u24 v125, 0x2aab, v125\n"
"s_add_u32 s85, s85, 384\n"
"s_mul_i32 s65, s65, 3072\n"
"s_mul_i32 s71, s66, 6752\n"
"s_mov_b32 s14, 221184\n"
"s_mov_b32 s15, 0x20000\n"
"v_sub_u32 v27, v126, v27\n"
"v_and_or_b32 v127, v128, 4, v127\n"
"v_lshrrev_b32 v125, 16, v125\n"
"s_add_u32 s71, s71, s65\n"
"v_mad_u32_u24 v127, v127, s69, v120\n"
"v_mul_u32_u24 v28, 96, v124\n"
"v_lshlrev_b32 v129, 1, v125\n"
"v_sub_u32 v28, v127, v28\n"
"v_and_b32 v129, 24, v129\n"
"v_add_u32 v28, 1024, v28\n"
"v_and_or_b32 v129, v125, 3, v129\n"
"v_mad_u32_u24 v129, v129, s69, v120\n"
"v_mul_u32_u24 v29, 96, v125\n"
"s_sub_u32 s64, 111, s59\n"
"v_sub_u32 v29, v129, v29\n"
"s_min_u32 s64, s64, 32\n"
"v_add_u32 v29, 6656, v29\n"
"s_mul_i32 s64, s64, 768\n"
"v_add_u32 v118, -3, v118\n"
"s_add_u32 s64, s64, s58\n"
"s_mul_i32 s50, s51, 6752\n"
"v_mul_i32_i24 v118, s57, v118\n"
"s_mov_b32 s10, 19267584\n"
"s_mov_b32 s11, 0x20000\n"
"s_add_u32 s50, s50, 27008\n"
"v_add3_u32 v118, v118, s64, v115\n"
"s_lshl_b32 s72, s56, 3\n"
"s_sub_u32 s72, s72, 2\n"
"s_mul_i32 s72, s72, s57\n"
"s_mul_i32 s34, s51, 12288\n"
"s_add_u32 s72, s72, s58\n"
"s_add_u32 s34, s34, 81024\n"
"v_add3_u32 v130, v117, s72, v115\n"
"s_lshr_b32 s76, 0x12492480, s84\n"
"s_waitcnt lgkmcnt(0)\n"
"s_add_u32 m0, s74, 162768\n"
"s_add_u32 s74, s72, s57\n"
"buffer_load_dword v122, s[20:23], s73 offen lds\n"
"s_mul_i32 s73, s75, 576\n"
"s_add_u32 s73, s73, s85\n"
"s_add_u32 m0, s71, 0\n"
"v_add3_u32 v131, v117, s74, v116\n"
"buffer_load_dwordx4 v27, s[12:15], s73 offen lds\n"
"s_lshr_b32 s77, 0x4924900, s84\n"
"s_and_b32 s60, s2, 7\n"
"s_lshr_b32 s84, s2, 5\n"
"s_and_b32 s76, s76, 0x180\n"
"s_lshl3_add_u32 s60, s60, s84\n"
"s_add_u32 m0, s71, 1024\n"
"s_mul_i32 s84, s76, 576\n"
"s_add_u32 s84, s84, s85\n"
"s_and_b32 s77, s77, 0x180\n"
"s_mul_i32 s86, s77, 576\n"
"s_add_u32 s86, s86, s85\n"
"buffer_load_dwordx4 v28, s[12:15], s73 offen lds\n"
"s_mul_i32 s72, s54, 172032\n"
"s_sub_u32 s36, s58, s72\n"
"s_add_u32 s32, s36, s75\n"
"v_add3_u32 v190, v117, s72, v115\n"
"v_min_u32 v96, 0xdc, v31\n"
"v_and_b32 v134, 3, v112\n"
"v_lshrrev_b32 v137, 1, v112\n"
"v_mul_u32_u24 v97, 0xf1, v96\n"
"s_mul_i32 s85, s54, -52\n"
"s_add_u32 m0, s71, 2048\n"
"v_and_or_b32 v137, v137, 4, v134\n"
"s_sub_u32 s74, 172032, s72\n"
"v_lshrrev_b32 v97, 12, v97\n"
"s_sub_u32 s85, s85, 8\n"
"v_bfe_u32 v136, v112, 2, 1\n"
"buffer_load_dwordx4 v29, s[12:15], s73 offen lds\n"
"v_lshrrev_b32 v140, 1, v137\n"
"v_add3_u32 v191, v117, s74, v116\n"
"v_lshlrev_b32 v30, 3, v96\n"
"v_mul_i32_i24 v97, s85, v97\n"
"v_lshrrev_b32 v132, 4, v112\n"
"v_lshl_add_u32 v85, v136, 2, v140\n"
"v_lshl_add_u32 v96, v96, 1, v97\n"
"s_add_u32 m0, s50, 0\n"
"v_xor_b32 v85, v85, v132\n"
"v_lshlrev_b32 v85, 4, v85\n"
"s_add_u32 s0, s36, s76\n"
"v_lshl_add_u32 v85, v137, 7, v85\n"
"v_mul_u32_u24 v25, 0xc00, v136\n"
"buffer_load_dwordx4 v118, s[8:11], s75 offen lds\n"
"v_add_u32 v25, v25, v85\n"
"v_add_u32 v188, s34, v25\n"
"v_xad_u32 v189, v25, 64, s34\n"
"s_add_u32 s1, s36, s77\n"
"s_mul_i32 s35, s51, 6144\n"
"s_lshl_b32 s33, s57, 1\n"
"s_add_u32 s35, s35, 130176\n"
"s_add_u32 m0, s50, 3072\n"
"v_lshrrev_b32 v84, 2, v112\n"
"v_mul_u32_u24 v84, 96, v84\n"
"v_lshl_add_u32 v23, v134, 3, v84\n"
"v_xor_b32 v69, 64, v25\n"
"v_add_u32 v30, 160896, v30\n"
"buffer_load_dwordx4 v119, s[8:11], s75 offen lds\n"
"v_add_u32 v72, 54016, v23\n"
"v_add_u32 v68, s50, v25\n"
"v_add_u32 v69, s50, v69\n"
"s_mul_i32 s3, s65, 12\n"
"s_add_u32 s70, s70, s3\n"
"s_mul_i32 s81, s77, 576\n"
"s_add_u32 s81, s81, s70\n"
"s_add_u32 m0, s34, 6144\n"
"s_sub_u32 s3, s76, s75\n"
"s_mul_i32 s3, s3, s66\n"
"s_add_u32 s3, s3, s75\n"
"s_mul_i32 s41, s56, -60768\n"
"s_mul_i32 s43, s66, 6752\n"
"buffer_load_dwordx4 v130, s[8:11], s75 offen lds\n"
"s_mul_i32 s3, s3, 576\n"
"s_add_u32 s41, s41, 60768\n"
"s_add_u32 s43, s43, s65\n"
"s_add_u32 s3, s3, s70\n"
"s_add_u32 s43, s43, s41\n"
"s_mov_b32 s42, 0x80000\n"
"v_add_u32 v142, s61, v137\n"
"v_bfe_u32 v86, v142, 1, 1\n"
"v_mul_u32_u24 v87, 43, v142\n"
"s_add_u32 m0, s34, 9216\n"
"s_movk_i32 s68, 6752\n"
"v_and_b32 v133, 48, v112\n"
"v_xor_b32 v86, v86, v132\n"
"v_lshrrev_b32 v87, 9, v87\n"
"s_movk_i32 s67, 192\n"
"buffer_load_dwordx4 v131, s[8:11], s75 offen lds\n"
"v_lshlrev_b32 v86, 4, v86\n"
"v_mul_u32_u24 v87, 80832, v87\n"
"v_mad_u32_u24 v21, v142, s68, v133\n"
"v_mad_u32_u24 v141, v142, s67, v86\n"
"v_sub_u32 v21, v21, v87\n"
"v_and_b32 v88, 1, v112\n"
"s_mul_i32 s48, s56, 13504\n"
"v_add_u32 v21, 6144, v21\n"
"s_add_u32 m0, s71, 13504\n"
"v_add_u32 v187, 74272, v141\n"
"v_add_u32 v198, 60768, v141\n"
"v_mad_u32_u24 v88, v88, s68, v133\n"
"s_mov_b32 s6, 0xf0f0f0f0\n"
"s_mov_b32 s7, 0xf0f0f0f0\n"
"buffer_load_dwordx4 v27, s[12:15], s84 offen lds\n"
"s_mov_b64 exec, s[6:7]\n"
"v_add_u32 v21, s48, v141\n"
"v_mov_b32 v187, v141\n"
"v_add_u32 v198, 60544, v88\n"
"s_mov_b64 exec, -1\n"
"s_lshl_b32 s85, s66, 10\n"
"v_add_u32 v192, 99456, v120\n"
"v_bfe_u32 v138, v112, 4, 1\n"
"v_lshrrev_b32 v139, 3, v112\n"
"s_add_u32 m0, s71, 14528\n"
"s_add_u32 s62, s59, s61\n"
"v_and_b32 v139, 4, v139\n"
"v_sub_u32 v94, v136, v138\n"
"s_sub_u32 s63, 108, s62\n"
"s_mul_i32 s49, s56, 272\n"
"buffer_load_dwordx4 v28, s[12:15], s84 offen lds\n"
"v_mul_i32_i24 v94, 0x88, v94\n"
"v_sub_u32 v95, v137, v139\n"
"s_sub_u32 s62, 3, s62\n"
"v_add_u32 v150, 1, v139\n"
"v_add_u32 v151, 2, v139\n"
"v_add_u32 v152, 3, v139\n"
"v_lshl_add_u32 v94, v95, 3, v94\n"
"v_max_i32 v149, s62, v139\n"
"v_max_i32 v150, s62, v150\n"
"s_add_u32 m0, s71, 15552\n"
"v_max_i32 v151, s62, v151\n"
"v_max_i32 v152, s62, v152\n"
"v_mov_b32 v153, 0x7f\n"
"v_min_i32 v149, s63, v149\n"
"v_min_i32 v150, s63, v150\n"
"buffer_load_dwordx4 v29, s[12:15], s84 offen lds\n"
"s_add_u32 s84, s49, 161064\n"
"v_add_u32 v20, s84, v94\n"
"v_min_i32 v151, s63, v151\n"
"v_min_i32 v152, s63, v152\n"
"s_lshl_b32 s84, s51, 10\n"
"v_lshlrev_b32 v149, v149, v153\n"
"v_lshlrev_b32 v150, v150, v153\n"
"s_add_u32 m0, s50, 1024\n"
"v_lshlrev_b32 v151, v151, v153\n"
"v_lshlrev_b32 v152, v152, v153\n"
"v_add_u32 v202, s84, v192\n"
"v_lshrrev_b32 v149, v137, v149\n"
"v_lshrrev_b32 v150, v137, v150\n"
"buffer_load_dwordx4 v118, s[8:11], s76 offen lds\n"
"v_lshrrev_b32 v151, v137, v151\n"
"v_lshrrev_b32 v152, v137, v152\n"
"s_sub_u32 s74, 3, s61\n"
"s_mov_b32 s84, 0xc0000007\n"
"s_cmp_eq_u32 s53, 0\n"
"s_cselect_b32 s84, 0xe0000000, s84\n"
"s_cmp_eq_u32 s53, 3\n"
"s_cselect_b32 s84, 7, s84\n"
"s_add_u32 m0, s50, 4096\n"
"s_lshl_b32 s73, s48, 2\n"
"s_mov_b32 s72, 0xffff\n"
"v_bfe_i32 v98, v149, 0, 1\n"
"v_bfe_i32 v99, v150, 0, 1\n"
"buffer_load_dwordx4 v119, s[8:11], s76 offen lds\n"
"v_bfe_i32 v100, v151, 0, 1\n"
"v_bfe_i32 v101, v152, 0, 1\n"
"v_bfe_i32 v102, v149, 8, 1\n"
"v_bfe_i32 v103, v150, 8, 1\n"
"v_bfe_i32 v56, v151, 8, 1\n"
"v_bfe_i32 v57, v152, 8, 1\n"
"s_max_i32 s74, s74, s62\n"
"v_mad_u32_u24 v186, v136, s68, v141\n"
"s_add_u32 s73, s73, 6752\n"
"v_bfi_b32 v0, s72, v98, v99\n"
"v_bfi_b32 v1, s72, v100, v101\n"
"v_bfi_b32 v2, s72, v102, v103\n"
"v_bfi_b32 v3, s72, v56, v57\n"
"v_max_i32 v58, s74, v137\n"
"v_and_b32 v247, 1, v112\n"
"v_add_u32 v22, s73, v186\n"
"s_lshl2_add_u32 s72, s60, s51\n"
"s_mul_i32 s74, s51, 1536\n"
"v_and_b32 v249, 15, v112\n"
"s_sub_u32 s73, 28, s61\n"
"s_mul_i32 s37, s72, 6144\n"
"v_mul_u32_u24 v249, 0x180, v249\n"
"v_and_b32 v250, 7, v112\n"
"s_min_i32 s73, s73, s63\n"
"s_lshl_b32 s72, s51, 6\n"
"v_add_u32 v249, 99456, v249\n"
"v_lshrrev_b32 v252, 5, v112\n"
"v_min_i32 v58, s73, v58\n"
"v_lshlrev_b32 v253, 3, v134\n"
"v_lshl_add_u32 v254, v23, 2, s72\n"
"s_mul_i32 s73, s52, 1536\n"
"v_sub_u32 v254, v254, v253\n"
"s_add_u32 s37, s37, s73\n"
"v_sub_u32 v254, v254, v253\n"
"s_lshl_b32 s73, s51, 5\n"
"v_add_u32 v236, 130176, v254\n"
"s_sub_u32 s73, 256, s73\n"
"v_sub_u32 v255, v236, v253\n"
"v_add_u32 v235, s73, v255\n"
"v_add_u32 v70, s35, v25\n"
"v_xor_b32 v71, 64, v25\n"
"v_add_u32 v71, s35, v71\n"
"v_lshlrev_b32 v58, v58, -1\n"
"v_lshrrev_b32 v58, v139, v58\n"
"v_not_b32 v58, v58\n"
"v_and_b32 v59, 5, v58\n"
"v_sub_u32 v61, v136, v138\n"
"v_and_b32 v60, 10, v58\n"
"v_lshlrev_b32 v59, 1, v59\n"
"v_ashrrev_i32 v149, 31, v61\n"
"v_add_u32 v150, -1, v61\n"
"v_mov_b32 v160, 0x40de\n"
"v_mov_b32 v161, 0\n"
"v_lshlrev_b32 v63, 4, v134\n"
"v_mov_b64 v[156:157], 0\n"
"s_mov_b32 s6, 0x00f0000f\n"
"s_mov_b32 s7, 0xf0000f00\n"
"v_lshl_or_b32 v59, v60, 4, v59\n"
"v_not_b32 v149, v149\n"
"v_ashrrev_i32 v150, 31, v150\n"
"v_or_b32 v62, s56, v138\n"
"s_mov_b64 exec, s[6:7]\n"
"v_lshlrev_b64 v[156:157], v63, v[160:161]\n"
"s_mov_b64 exec, -1\n"
"v_or_b32 v59, 0x44, v59\n"
"s_mov_b64 exec, 0\n"
"s_cmp_eq_u32 s51, 0\n"
"s_cbranch_scc1 .Lic0%=\n"
"s_cmp_eq_u32 s51, 1\n"
"s_cbranch_scc1 .Lic1%=\n"
"s_cmp_eq_u32 s51, 2\n"
"s_cbranch_scc1 .Lic2%=\n"
"s_branch .Lic3%=\n"
".Lib0%=:\n"
"s_mov_b64 exec, -1\n"
"v_and_b32 v12, v149, v0\n"
"v_and_b32 v16, v150, v0\n"
"v_and_b32 v13, v149, v1\n"
"v_and_b32 v17, v150, v1\n"
"v_and_b32 v14, v149, v2\n"
"v_and_b32 v18, v150, v2\n"
"v_and_b32 v15, v149, v3\n"
"s_add_u32 m0, s34, 7168\n"
"v_and_b32 v19, v150, v3\n"
"buffer_load_dwordx4 v130, s[8:11], s76 offen lds\n"
"v_sub_u32 v62, v136, v62\n"
"v_mov_b32 v160, 0xffff\n"
"v_mov_b64 v[176:177], 0\n"
"v_lshl_or_b32 v66, v59, 8, v59\n"
"v_ashrrev_i32 v149, 31, v62\n"
"v_add_u32 v150, -1, v62\n"
"s_mov_b64 exec, s[6:7]\n"
"v_lshlrev_b64 v[176:177], v63, v[160:161]\n"
"s_mov_b64 exec, -1\n"
"s_mov_b32 s30, 23724032\n"
"s_mov_b32 s31, 0x20000\n"
"s_add_u32 m0, s34, 10240\n"
"v_and_b32 v146, 8, v112\n"
"buffer_load_dwordx4 v131, s[8:11], s76 offen lds\n"
"v_lshl_add_u32 v146, v136, 1, v146\n"
"v_add3_u32 v146, v146, s61, 1\n"
"v_lshrrev_b32 v145, 5, v112\n"
"v_max_u32 v144, 3, v142\n"
"v_mul_u32_u24 v145, s68, v145\n"
"v_bfe_u32 v92, v146, 1, 1\n"
"v_add_u32 v144, -3, v144\n"
"v_add_u32 v145, -768, v145\n"
"v_xor_b32 v92, v92, v134\n"
"v_mov_b32 v143, s48\n"
"v_bfe_u32 v90, v144, 1, 1\n"
"s_add_u32 m0, s71, 60768\n"
"v_lshl_add_u32 v92, v92, 4, v145\n"
"buffer_load_dwordx4 v27, s[12:15], s86 offen lds\n"
"s_waitcnt vmcnt(12)\n"
"s_barrier\n"
"ds_read_b64_tr_b16 a[0:1], v23\n"
"ds_read_b64_tr_b16 a[2:3], v23 offset:1536\n"
"ds_read_b64_tr_b16 a[4:5], v23 offset:32\n"
"ds_read_b64_tr_b16 a[6:7], v23 offset:1568\n"
"ds_read_b64_tr_b16 a[8:9], v23 offset:64\n"
"v_mad_u32_u24 v143, v136, s68, v143\n"
"ds_read_b64_tr_b16 a[10:11], v23 offset:1600\n"
"s_add_u32 m0, s71, 61792\n"
"ds_read_b64_tr_b16 a[72:73], v23 offset:3072\n"
"buffer_load_dwordx4 v28, s[12:15], s86 offen lds\n"
"ds_read_b64_tr_b16 a[74:75], v23 offset:4608\n"
"ds_read_b64_tr_b16 a[76:77], v23 offset:3104\n"
"ds_read_b64_tr_b16 a[78:79], v23 offset:4640\n"
"v_xor_b32 v90, v90, v132\n"
"ds_read_b64_tr_b16 a[80:81], v23 offset:3136\n"
"ds_read_b64_tr_b16 a[82:83], v23 offset:4672\n"
"ds_read_b64_tr_b16 a[12:13], v23 offset:6752\n"
"v_cmp_eq_u32 s[46:47], 35, v146\n"
"ds_read_b64_tr_b16 a[14:15], v23 offset:8288\n"
"s_add_u32 m0, s71, 62816\n"
"s_waitcnt vmcnt(11)\n"
"buffer_load_dwordx4 v29, s[12:15], s86 offen lds\n"
"ds_read_b128 a[144:147], v68\n"
"ds_read_b128 a[148:151], v69\n"
"s_mul_i32 s71, s54, 312\n"
"s_and_b32 s86, s52, 1\n"
"s_lshl1_add_u32 s71, s86, s71\n"
"s_add_u32 s71, s71, 162760\n"
"v_add_u32 v24, s71, v96\n"
"ds_read_u16 v240, v24 offset:6\n"
"ds_read_u16 v241, v24 offset:4\n"
"s_add_u32 m0, s50, 2048\n"
"ds_read_u16 v242, v24 offset:2\n"
"buffer_load_dwordx4 v118, s[8:11], s77 offen lds\n"
"ds_read_u16 v243, v24\n"
"ds_read_b64_tr_b16 a[16:17], v23 offset:6784\n"
"ds_read_b64_tr_b16 a[18:19], v23 offset:8320\n"
"ds_read_b64_tr_b16 a[20:21], v23 offset:6816\n"
"s_and_b32 s86, s51, 1\n"
"ds_read_b64_tr_b16 a[22:23], v23 offset:8352\n"
"s_lshl3_add_u32 s85, s86, s85\n"
"ds_read_b64_tr_b16 a[84:85], v23 offset:9824\n"
"v_mad_u32_u24 v184, v146, s67, v92\n"
"v_lshl_add_u32 v90, v90, 4, v143\n"
"v_add_u32 v146, 1, v146\n"
"v_mad_u32_u24 v178, v144, s67, v90\n"
"v_bfe_u32 v93, v146, 1, 1\n"
"v_add_u32 v144, 5, v142\n"
"v_xor_b32 v93, v93, v134\n"
"v_min_u32 v144, 32, v144\n"
"v_mul_u32_u24 v147, 13504, v138\n"
"v_lshl_add_u32 v93, v93, 4, v145\n"
"v_bfe_u32 v91, v144, 1, 1\n"
"v_add_u32 v147, s48, v147\n"
"v_mul_i32_i24 v148, -67520, v138\n"
"v_mad_u32_u24 v185, v146, s67, v93\n"
"v_bfe_u32 v135, v112, 2, 2\n"
"v_xor_b32 v91, v91, v132\n"
"v_add_u32 v148, 67520, v148\n"
"v_add_u32 v183, v185, v147\n"
"v_add_u32 v185, v185, v148\n"
"s_mov_b64 exec, s[46:47]\n"
"v_mad_u32_u24 v185, v138, s67, v185\n"
"s_mov_b64 exec, -1\n"
"v_lshl_add_u32 v91, v91, 4, v143\n"
"v_mad_u32_u24 v179, v144, s67, v91\n"
"v_cmp_eq_u32 s[44:45], 32, v144\n"
"v_add_u32 v182, v184, v147\n"
"v_not_b32 v149, v149\n"
"v_add_u32 v184, v184, v148\n"
"v_and_b32 v4, v149, v0\n"
"v_and_b32 v5, v149, v1\n"
"v_and_b32 v6, v149, v2\n"
"v_and_b32 v7, v149, v3\n"
"v_ashrrev_i32 v150, 31, v150\n"
"v_and_b32 v8, v150, v0\n"
"v_and_b32 v9, v150, v1\n"
"v_and_b32 v10, v150, v2\n"
"v_and_b32 v11, v150, v3\n"
"v_lshrrev_b32 v85, 2, v112\n"
"v_sub_u32 v86, 11, v112\n"
"v_and_b32 v86, 0x80000000, v86\n"
"v_and_b32 v89, 1, v114\n"
"v_add_u32 v89, 29, v89\n"
"v_mul_i32_i24 v89, s57, v89\n"
"v_add3_u32 v199, v89, s64, v116\n"
"v_lshlrev_b32 v193, 3, v112\n"
"v_add_u32 v193, 99456, v193\n"
"v_mov_b32 v26, 0xf149f149\n"
"v_mov_b64 v[160:161], v[156:157]\n"
"v_add_u32 v180, 40512, v178\n"
"v_add_u32 v181, 40512, v179\n"
"v_mov_b32 v172, 0x3f803f80\n"
"v_mov_b32 v173, v172\n"
"v_mov_b64 v[164:165], v[156:157]\n"
"v_mov_b64 v[168:169], v[156:157]\n"
"v_mov_b64 v[174:175], v[172:173]\n"
"s_waitcnt vmcnt(11) lgkmcnt(7)\n"
"v_mfma_f32_16x16x32_bf16 v[204:207], a[0:3], a[144:147], 0\n"
"s_add_u32 m0, s50, 5120\n"
"ds_read_b128 a[168:171], v188 offset:6144\n"
"buffer_load_dwordx4 v119, s[8:11], s77 offen lds\n"
"v_mfma_f32_16x16x32_bf16 v[204:207], a[12:15], a[148:151], v[204:207]\n"
"s_add_u32 s85, s85, 4096\n"
"ds_read_b128 a[172:175], v189 offset:6144\n"
"ds_read_b64_tr_b16 a[86:87], v23 offset:11360\n"
"v_mfma_f32_16x16x32_bf16 v[208:211], a[4:7], a[144:147], 0\n"
"s_waitcnt lgkmcnt(6)\n"
"ds_read_b64_tr_b16 a[88:89], v23 offset:9856\n"
"ds_read_b64_tr_b16 a[90:91], v23 offset:11392\n"
"v_mfma_f32_16x16x32_bf16 v[208:211], a[16:19], a[148:151], v[208:211]\n"
"s_add_u32 m0, s34, 8192\n"
"ds_read_b64_tr_b16 a[92:93], v23 offset:9888\n"
"buffer_load_dwordx4 v130, s[8:11], s77 offen lds\n"
"v_mfma_f32_16x16x32_bf16 v[212:215], a[8:11], a[144:147], 0\n"
"s_waitcnt lgkmcnt(7)\n"
"ds_read_b64_tr_b16 a[94:95], v23 offset:11424\n"
"v_lshl_or_b32 v240, v241, 16, v240\n"
"v_mfma_f32_16x16x32_bf16 v[212:215], a[20:23], a[148:151], v[212:215]\n"
"s_lshl_b32 s86, s52, 3\n"
"v_lshl_or_b32 v241, v243, 16, v242\n"
"v_add_u32 v203, s85, v192\n"
"v_mfma_f32_16x16x32_bf16 v[216:219], a[72:75], a[144:147], 0\n"
"s_add_u32 m0, s34, 11264\n"
"ds_write_b64 v30, v[240:241]\n"
"v_mfma_f32_16x16x32_bf16 v[220:223], a[76:79], a[144:147], 0\n"
"s_waitcnt lgkmcnt(7)\n"
"buffer_load_dwordx4 v131, s[8:11], s77 offen lds\n"
"s_mov_b32 s70, 0x3e553b95\n"
"v_mfma_f32_16x16x32_bf16 v[224:227], a[80:83], a[144:147], 0\n"
"s_waitcnt vmcnt(9)\n"
"s_lshl_b32 s85, s51, 6\n"
"v_add_u32 v81, s85, v112\n"
"v_mfma_f32_16x16x32_bf16 v[104:107], a[72:75], a[168:171], 0\n"
"s_waitcnt lgkmcnt(5)\n"
"ds_read_b128 a[152:155], v68 offset:1024\n"
"ds_read_b128 a[156:159], v69 offset:1024\n"
"v_mfma_f32_16x16x32_bf16 v[216:219], a[84:87], a[148:151], v[216:219]\n"
"s_waitcnt lgkmcnt(5)\n"
"v_lshrrev_b32 v81, 1, v81\n"
"v_add_u32 v81, 0x100, v81\n"
"v_mfma_f32_16x16x32_bf16 v[220:223], a[88:91], a[148:151], v[220:223]\n"
"s_waitcnt lgkmcnt(3)\n"
"v_add_u32 v154, s85, v112\n"
"v_mul_u32_u24 v82, 0xaab, v81\n"
"v_mfma_f32_16x16x32_bf16 v[224:227], a[92:95], a[148:151], v[224:227]\n"
"s_add_u32 m0, s34, 0\n"
"v_mul_u32_u24 v155, 0xaab, v154\n"
"v_lshrrev_b32 v82, 16, v82\n"
"v_mfma_f32_16x16x32_bf16 v[108:111], a[76:79], a[168:171], 0\n"
"s_lshl2_add_u32 s71, s60, s52\n"
"buffer_load_dwordx4 v190, s[8:11], s32 offen lds\n"
"v_lshrrev_b32 v155, 16, v155\n"
"v_mfma_f32_16x16x32_bf16 v[108:111], a[88:91], a[172:175], v[108:111]\n"
"s_mul_i32 s38, s71, 6144\n"
"v_mul_i32_i24 v83, -24, v82\n"
"v_mul_i32_i24 v80, -24, v155\n"
"v_mfma_f32_16x16x32_bf16 v[228:231], a[80:83], a[168:171], 0\n"
"s_add_u32 m0, s34, 3072\n"
"v_add_u32 v81, v81, v83\n"
"v_add_u32 v154, v154, v80\n"
"v_mfma_f32_16x16x32_bf16 v[228:231], a[92:95], a[172:175], v[228:231]\n"
"s_waitcnt vmcnt(8)\n"
"buffer_load_dwordx4 v191, s[8:11], s32 offen lds\n"
"v_and_b32 v83, 7, v82\n"
"v_mfma_f32_16x16x32_bf16 v[104:107], a[84:87], a[172:175], v[104:107]\n"
"s_add_u32 s32, s32, s33\n"
"ds_read_b128 a[176:179], v188 offset:7168\n"
"ds_read_b128 a[180:183], v189 offset:7168\n"
"v_and_b32 v80, 7, v155\n"
"v_xor_b32 v81, v81, v83\n"
"v_xor_b32 v154, v154, v80\n"
"v_lshrrev_b32 v83, 1, v82\n"
"s_add_u32 m0, s34, 1024\n"
"v_lshrrev_b32 v80, 1, v155\n"
"v_and_b32 v83, 4, v83\n"
"v_and_b32 v80, 4, v80\n"
"v_and_or_b32 v83, v82, 3, v83\n"
"v_and_or_b32 v80, v155, 3, v80\n"
"buffer_load_dwordx4 v190, s[8:11], s0 offen lds\n"
"v_add_u32 v83, s86, v83\n"
"v_add_u32 v80, s86, v80\n"
"v_bfe_u32 v245, v155, 2, 1\n"
"v_lshrrev_b32 v246, v83, s84\n"
"v_bfe_u32 v248, v82, 2, 1\n"
"v_lshrrev_b32 v244, v80, s84\n"
"v_xor_b32 v245, s54, v245\n"
"s_add_u32 m0, s34, 4096\n"
"v_lshlrev_b32 v246, 31, v246\n"
"v_xor_b32 v248, s54, v248\n"
"s_lshl_b32 s71, s51, 2\n"
"v_lshlrev_b32 v244, 31, v244\n"
"v_mul_u32_u24 v245, 172032, v245\n"
"buffer_load_dwordx4 v191, s[8:11], s0 offen lds\n"
"s_add_u32 s0, s0, s33\n"
"v_mul_u32_u24 v80, 0x300, v80\n"
"v_lshl_add_u32 v246, v247, 3, v246\n"
"v_mul_u32_u24 v248, 172032, v248\n"
"v_mul_u32_u24 v83, 0x300, v83\n"
"s_add_u32 s38, s38, s74\n"
"v_add_u32 v251, s71, v132\n"
"s_add_u32 m0, s34, 2048\n"
"v_add3_u32 v244, v244, v245, v80\n"
"s_mov_b64 exec, 0\n"
"s_cmp_eq_u32 s51, 0\n"
"s_cbranch_scc1 .Lic4%=\n"
"s_cmp_eq_u32 s51, 1\n"
"s_cbranch_scc1 .Lic5%=\n"
"s_cmp_eq_u32 s51, 2\n"
"s_cbranch_scc1 .Lic6%=\n"
"s_branch .Lic7%=\n"
".Lib1%=:\n"
"s_mov_b64 exec, -1\n"
"buffer_load_dwordx4 v190, s[8:11], s1 offen lds\n"
"s_barrier\n"
"ds_read_b64_tr_b16 a[24:25], v23 offset:13504\n"
"ds_read_b64_tr_b16 a[26:27], v23 offset:15040\n"
"s_add_u32 m0, s34, 5120\n"
"ds_read_b64_tr_b16 a[28:29], v23 offset:13536\n"
"ds_read_b64_tr_b16 a[30:31], v23 offset:15072\n"
"ds_read_b64_tr_b16 a[32:33], v23 offset:13568\n"
"ds_read_b64_tr_b16 a[34:35], v23 offset:15104\n"
"s_waitcnt vmcnt(9)\n"
"buffer_load_dwordx4 v191, s[8:11], s1 offen lds\n"
"s_add_u32 s1, s1, s33\n"
"s_barrier\n"
"ds_read_b64_tr_b16 a[96:97], v23 offset:16576\n"
"ds_read_b64_tr_b16 a[98:99], v23 offset:18112\n"
"ds_read_b64_tr_b16 a[100:101], v23 offset:16608\n"
"ds_read_b64_tr_b16 a[102:103], v23 offset:18144\n"
"ds_read_b64_tr_b16 a[104:105], v23 offset:16640\n"
"s_add_u32 m0, s35, 0\n"
"ds_read_b64_tr_b16 a[106:107], v23 offset:18176\n"
"v_add3_u32 v246, v246, v248, v83\n"
"ds_read_b64_tr_b16 a[36:37], v23 offset:20256\n"
"buffer_load_dwordx4 v190, s[8:11], s32 offen lds\n"
"ds_read_b64_tr_b16 a[38:39], v23 offset:21792\n"
"ds_read_b64_tr_b16 a[40:41], v23 offset:20288\n"
"ds_read_b64_tr_b16 a[42:43], v23 offset:21824\n"
"v_xor_b32 v251, v251, v250\n"
"v_lshl_add_u32 v194, v154, 4, v244\n"
"v_lshl_add_u32 v195, v81, 4, v246\n"
"v_mfma_f32_16x16x32_bf16 v[244:247], a[0:3], a[168:171], 0\n"
"s_waitcnt vmcnt(9) lgkmcnt(12)\n"
"ds_read_b128 a[160:163], v68 offset:2048\n"
"ds_read_b128 a[164:167], v69 offset:2048\n"
"v_mfma_f32_16x16x32_bf16 v[204:207], a[24:27], a[152:155], v[204:207]\n"
"s_waitcnt lgkmcnt(12)\n"
"ds_read_b64_tr_b16 a[44:45], v23 offset:20320\n"
"v_lshl_add_u32 v196, v251, 4, v249\n"
"v_mfma_f32_16x16x32_bf16 v[208:211], a[28:31], a[152:155], v[208:211]\n"
"s_add_u32 m0, s35, 3072\n"
"ds_read_b64_tr_b16 a[46:47], v23 offset:21856\n"
"buffer_load_dwordx4 v191, s[8:11], s32 offen lds\n"
"v_mfma_f32_16x16x32_bf16 v[212:215], a[32:35], a[152:155], v[212:215]\n"
"s_waitcnt lgkmcnt(12)\n"
"ds_read_b64_tr_b16 a[108:109], v23 offset:23328\n"
"ds_read_b64_tr_b16 a[110:111], v23 offset:24864\n"
"v_mfma_f32_16x16x32_bf16 v[216:219], a[96:99], a[152:155], v[216:219]\n"
"s_waitcnt lgkmcnt(12)\n"
"ds_read_b64_tr_b16 a[112:113], v23 offset:23360\n"
"ds_read_b64_tr_b16 a[114:115], v23 offset:24896\n"
"v_mfma_f32_16x16x32_bf16 v[220:223], a[100:103], a[152:155], v[220:223]\n"
"s_waitcnt lgkmcnt(12)\n"
"ds_read_b64_tr_b16 a[116:117], v23 offset:23392\n"
"ds_read_b64_tr_b16 a[118:119], v23 offset:24928\n"
"v_mfma_f32_16x16x32_bf16 v[224:227], a[104:107], a[152:155], v[224:227]\n"
"s_waitcnt lgkmcnt(12)\n"
"ds_read_b64_tr_b16 a[48:49], v23 offset:60768\n"
"ds_read_b64_tr_b16 a[50:51], v23 offset:62304\n"
"v_mfma_f32_16x16x32_bf16 v[204:207], a[36:39], a[156:159], v[204:207]\n"
"s_waitcnt vmcnt(8) lgkmcnt(12)\n"
"ds_read_b64_tr_b16 a[52:53], v23 offset:60800\n"
"ds_read_b128 a[184:187], v188 offset:8192\n"
"v_mfma_f32_16x16x32_bf16 v[208:211], a[40:43], a[156:159], v[208:211]\n"
"s_add_u32 m0, s35, 1024\n"
"ds_read_b128 a[188:191], v189 offset:8192\n"
"buffer_load_dwordx4 v190, s[8:11], s0 offen lds\n"
"v_mfma_f32_16x16x32_bf16 v[244:247], a[12:15], a[172:175], v[244:247]\n"
"s_add_u32 s32, s32, s33\n"
"ds_read_b64_tr_b16 a[54:55], v23 offset:62336\n"
"ds_read_b64_tr_b16 a[56:57], v23 offset:60832\n"
"v_mfma_f32_16x16x32_bf16 v[244:247], a[24:27], a[176:179], v[244:247]\n"
"s_waitcnt lgkmcnt(13)\n"
"ds_read_b64_tr_b16 a[58:59], v23 offset:62368\n"
"ds_read_b64_tr_b16 a[120:121], v72 offset:9824\n"
"v_mfma_f32_16x16x32_bf16 v[212:215], a[44:47], a[156:159], v[212:215]\n"
"s_waitcnt lgkmcnt(13)\n"
"ds_read_b64_tr_b16 a[122:123], v72 offset:11360\n"
"ds_read_b64_tr_b16 a[124:125], v72 offset:9856\n"
"v_mfma_f32_16x16x32_bf16 v[216:219], a[108:111], a[156:159], v[216:219]\n"
"s_waitcnt lgkmcnt(13)\n"
"ds_read_b64_tr_b16 a[126:127], v72 offset:11392\n"
"ds_read_b64_tr_b16 a[128:129], v72 offset:9888\n"
"v_mfma_f32_16x16x32_bf16 v[220:223], a[112:115], a[156:159], v[220:223]\n"
"s_waitcnt lgkmcnt(13)\n"
"ds_read_b64_tr_b16 a[130:131], v72 offset:11424\n"
"ds_read_b64_tr_b16 a[60:61], v72 offset:13504\n"
"v_mfma_f32_16x16x32_bf16 v[224:227], a[116:119], a[156:159], v[224:227]\n"
"s_waitcnt lgkmcnt(13)\n"
"ds_read_b64_tr_b16 a[62:63], v72 offset:15040\n"
"ds_read_b64_tr_b16 a[64:65], v72 offset:13536\n"
"v_mfma_f32_16x16x32_bf16 v[204:207], a[48:51], a[160:163], v[204:207]\n"
"s_add_u32 m0, s35, 4096\n"
"ds_read_b64_tr_b16 a[66:67], v72 offset:15072\n"
"buffer_load_dwordx4 v191, s[8:11], s0 offen lds\n"
"v_mfma_f32_16x16x32_bf16 v[104:107], a[96:99], a[176:179], v[104:107]\n"
"s_add_u32 s0, s0, s33\n"
"ds_read_b64_tr_b16 a[68:69], v72 offset:13568\n"
"ds_read_b64_tr_b16 a[70:71], v72 offset:15104\n"
"v_mfma_f32_16x16x32_bf16 v[104:107], a[108:111], a[180:183], v[104:107]\n"
"s_waitcnt lgkmcnt(14)\n"
"ds_read_b64_tr_b16 a[132:133], v72 offset:16576\n"
"ds_read_b64_tr_b16 a[134:135], v72 offset:18112\n"
"v_mfma_f32_16x16x32_bf16 v[208:211], a[52:55], a[160:163], v[208:211]\n"
"s_waitcnt lgkmcnt(14)\n"
"ds_read_b64_tr_b16 a[136:137], v72 offset:16608\n"
"ds_read_b64_tr_b16 a[138:139], v72 offset:18144\n"
"v_mfma_f32_16x16x32_bf16 v[212:215], a[56:59], a[160:163], v[212:215]\n"
"s_waitcnt lgkmcnt(14)\n"
"ds_read_b64_tr_b16 a[140:141], v72 offset:16640\n"
"ds_read_b64_tr_b16 a[142:143], v72 offset:18176\n"
"v_mfma_f32_16x16x32_bf16 v[216:219], a[120:123], a[160:163], v[216:219]\n"
"s_waitcnt lgkmcnt(14)\n"
"v_lshl_add_u32 v249, v138, 3, v249\n"
"v_add_u32 v68, s41, v23\n"
"v_mfma_f32_16x16x32_bf16 v[220:223], a[124:127], a[160:163], v[220:223]\n"
"s_waitcnt lgkmcnt(12)\n"
"v_add_u32 v69, 154752, v23\n"
"s_mov_b32 s71, 0x3e553b95\n"
"v_mfma_f32_16x16x32_bf16 v[224:227], a[128:131], a[160:163], v[224:227]\n"
"s_waitcnt lgkmcnt(10)\n"
"s_add_u32 m0, s35, 2048\n"
"s_mul_i32 s20, s33, 3\n"
"v_mfma_f32_16x16x32_bf16 v[204:207], a[60:63], a[164:167], v[204:207]\n"
"s_waitcnt lgkmcnt(8)\n"
"buffer_load_dwordx4 v190, s[8:11], s1 offen lds\n"
"s_mul_i32 s21, s33, 5\n"
"v_mfma_f32_16x16x32_bf16 v[208:211], a[64:67], a[164:167], v[208:211]\n"
"s_waitcnt lgkmcnt(6)\n"
"s_lshl1_add_u32 s74, s51, 16\n"
"v_add_u32 v252, s74, v252\n"
"v_mfma_f32_16x16x32_bf16 v[212:215], a[68:71], a[164:167], v[212:215]\n"
"s_waitcnt lgkmcnt(4)\n"
"v_cvt_pk_bf16_f32 v204, v204, v205\n"
"v_cvt_pk_bf16_f32 v205, v206, v207\n"
"v_mfma_f32_16x16x32_bf16 v[216:219], a[132:135], a[164:167], v[216:219]\n"
"s_waitcnt lgkmcnt(2)\n"
"v_cvt_pk_bf16_f32 v206, v208, v209\n"
"v_cvt_pk_bf16_f32 v207, v210, v211\n"
"v_mfma_f32_16x16x32_bf16 v[220:223], a[136:139], a[164:167], v[220:223]\n"
"s_waitcnt lgkmcnt(0)\n"
"v_cvt_pk_bf16_f32 v212, v212, v213\n"
"v_cvt_pk_bf16_f32 v213, v214, v215\n"
"v_mfma_f32_16x16x32_bf16 v[224:227], a[140:143], a[164:167], v[224:227]\n"
"s_add_u32 m0, s35, 5120\n"
"v_cvt_pk_bf16_f32 v216, v216, v217\n"
"v_cvt_pk_bf16_f32 v217, v218, v219\n"
"v_mfma_f32_16x16x32_bf16 v[108:111], a[100:103], a[176:179], v[108:111]\n"
"s_barrier\n"
"v_mfma_f32_16x16x32_bf16 v[108:111], a[112:115], a[180:183], v[108:111]\n"
"s_add_u32 s38, s38, 31616\n"
"buffer_load_dwordx4 v191, s[8:11], s1 offen lds\n"
"ds_write_b128 v21, v[204:207]\n"
"v_mfma_f32_16x16x32_bf16 v[108:111], a[124:127], a[184:187], v[108:111]\n"
"s_add_u32 s1, s1, s33\n"
"v_cvt_pk_bf16_f32 v218, v220, v221\n"
"v_cvt_pk_bf16_f32 v219, v222, v223\n"
"v_mfma_f32_16x16x32_bf16 v[108:111], a[136:139], a[188:191], v[108:111]\n"
"s_add_u32 s39, s38, 22119552\n"
"ds_write_b128 v21, v[216:219] offset:128\n"
"v_mfma_f32_16x16x32_bf16 v[228:231], a[104:107], a[176:179], v[228:231]\n"
"s_and_b32 s85, s52, 1\n"
"v_cvt_pk_bf16_f32 v214, v224, v225\n"
"v_cvt_pk_bf16_f32 v215, v226, v227\n"
"v_mfma_f32_16x16x32_bf16 v[228:231], a[116:119], a[180:183], v[228:231]\n"
"s_lshl2_add_u32 s85, s85, s61\n"
"ds_write_b128 v21, v[212:215] offset:64\n"
"v_mfma_f32_16x16x32_bf16 v[228:231], a[128:131], a[184:187], v[228:231]\n"
"s_add_u32 m0, s34, 6144\n"
"v_xor_b32 v252, v252, v250\n"
"v_lshl_add_u32 v197, v252, 4, v249\n"
"v_mfma_f32_16x16x32_bf16 v[248:251], a[4:7], a[168:171], 0\n"
"s_lshl_b32 s5, s51, 9\n"
"buffer_load_dwordx4 v190, s[8:11], s32 offen lds\n"
"v_add_u32 v84, s85, v134\n"
"v_mfma_f32_16x16x32_bf16 v[248:251], a[16:19], a[172:175], v[248:251]\n"
"s_add_u32 s5, s5, 4096\n"
"ds_read_b64 v[80:81], v20 offset:272\n"
"ds_read_b64 v[88:89], v20 offset:544\n"
"v_mfma_f32_16x16x32_bf16 v[248:251], a[28:31], a[176:179], v[248:251]\n"
"s_waitcnt vmcnt(11)\n"
"ds_read_b64 v[94:95], v20 offset:608\n"
"ds_read_b128 v[32:35], v188\n"
"v_mfma_f32_16x16x32_bf16 v[248:251], a[40:43], a[180:183], v[248:251]\n"
"s_add_u32 m0, s34, 9216\n"
"ds_read_b128 v[36:39], v189\n"
"buffer_load_dwordx4 v191, s[8:11], s32 offen lds\n"
"v_mfma_f32_16x16x32_bf16 v[248:251], a[52:55], a[184:187], v[248:251]\n"
"s_add_u32 s32, s32, s33\n"
"ds_read_b64 v[96:97], v20 offset:816\n"
"ds_read_b64 v[102:103], v20 offset:880\n"
"v_mfma_f32_16x16x32_bf16 v[248:251], a[64:67], a[188:191], v[248:251]\n"
"s_lshr_b32 s86, s52, 1\n"
"v_mul_u32_u24 v84, 0x300, v84\n"
"v_lshl_add_u32 v84, v85, 7, v84\n"
"v_mfma_f32_16x16x32_bf16 v[252:255], a[8:11], a[168:171], 0\n"
"s_add_u32 m0, s34, 7168\n"
"v_or_b32 v67, v86, v84\n"
"s_mul_i32 s22, s33, 6\n"
"v_mfma_f32_16x16x32_bf16 v[252:255], a[20:23], a[172:175], v[252:255]\n"
"s_mul_i32 s86, s86, 172032\n"
"buffer_load_dwordx4 v190, s[8:11], s0 offen lds\n"
"ds_read_b64 v[86:87], v20 offset:336\n"
"v_mfma_f32_16x16x32_bf16 v[252:255], a[32:35], a[176:179], v[252:255]\n"
"s_waitcnt lgkmcnt(7)\n"
"s_add_u32 s86, s86, s36\n"
"v_mfma_f32_16x16x32_bf16 v[252:255], a[44:47], a[180:183], v[252:255]\n"
"s_waitcnt lgkmcnt(5)\n"
"v_bfi_b32 v80, v4, v80, v26\n"
"v_bfi_b32 v81, v5, v81, v26\n"
"v_mfma_f32_16x16x32_bf16 v[252:255], a[56:59], a[184:187], v[252:255]\n"
"s_waitcnt vmcnt(11) lgkmcnt(4)\n"
"v_bfi_b32 v88, v0, v88, v26\n"
"v_bfi_b32 v89, v1, v89, v26\n"
"v_mfma_f32_16x16x32_bf16 v[252:255], a[68:71], a[188:191], v[252:255]\n"
"s_add_u32 m0, s34, 10240\n"
"ds_read_b128 v[40:43], v188 offset:1024\n"
"buffer_load_dwordx4 v191, s[8:11], s0 offen lds\n"
"v_mfma_f32_16x16x32_bf16 v[244:247], a[36:39], a[180:183], v[244:247]\n"
"s_waitcnt lgkmcnt(2)\n"
"ds_read_b128 v[44:47], v189 offset:1024\n"
"v_bfi_b32 v94, v2, v94, v26\n"
"v_mfma_f32_16x16x32_bf16 v[244:247], a[48:51], a[184:187], v[244:247]\n"
"s_add_u32 s0, s0, s33\n"
"v_cvt_pk_bf16_f32 v252, v252, v253\n"
"v_cvt_pk_bf16_f32 v253, v254, v255\n"
"v_mfma_f32_16x16x32_bf16 v[244:247], a[60:63], a[188:191], v[244:247]\n"
"s_waitcnt lgkmcnt(2)\n"
"v_bfi_b32 v95, v3, v95, v26\n"
"v_bfi_b32 v96, v0, v96, v26\n"
"v_mfma_f32_16x16x32_bf16 v[104:107], a[120:123], a[184:187], v[104:107]\n"
"s_add_u32 m0, s34, 8192\n"
"v_bfi_b32 v86, v6, v86, v26\n"
"v_bfi_b32 v87, v7, v87, v26\n"
"v_mfma_f32_16x16x32_bf16 v[104:107], a[132:135], a[188:191], v[104:107]\n"
"s_mul_i32 s78, s33, 8\n"
"buffer_load_dwordx4 v190, s[8:11], s1 offen lds\n"
"v_cvt_pk_bf16_f32 v244, v244, v245\n"
"v_mfma_f32_16x16x32_bf16 v[228:231], a[140:143], a[188:191], v[228:231]\n"
"s_add_u32 s78, s78, s86\n"
"v_cvt_pk_bf16_f32 v245, v246, v247\n"
"v_cvt_pk_bf16_f32 v246, v248, v249\n"
"v_mfma_f32_16x16x32_bf16 v[204:207], a[0:3], v[32:35], 0\n"
"s_waitcnt vmcnt(11)\n"
"v_cvt_pk_bf16_f32 v247, v250, v251\n"
"v_cvt_pk_bf16_f32 v104, v104, v105\n"
"v_mfma_f32_16x16x32_bf16 v[204:207], a[12:15], v[36:39], v[204:207]\n"
"s_waitcnt lgkmcnt(1)\n"
"ds_read_b128 v[48:51], v188 offset:2048\n"
"ds_read_b128 v[52:55], v189 offset:2048\n"
"v_mfma_f32_16x16x32_bf16 v[204:207], a[24:27], v[40:43], v[204:207]\n"
"s_waitcnt lgkmcnt(2)\n"
"ds_write_b128 v22, v[244:247]\n"
"v_mfma_f32_16x16x32_bf16 v[204:207], a[36:39], v[44:47], v[204:207]\n"
"s_add_u32 m0, s34, 11264\n"
"v_cvt_pk_bf16_f32 v105, v106, v107\n"
"v_cvt_pk_bf16_f32 v106, v108, v109\n"
"v_mfma_f32_16x16x32_bf16 v[208:211], a[4:7], v[32:35], 0\n"
"s_mov_b32 s18, 73728\n"
"buffer_load_dwordx4 v191, s[8:11], s1 offen lds\n"
"v_cvt_pk_bf16_f32 v107, v110, v111\n"
"v_mfma_f32_16x16x32_bf16 v[208:211], a[16:19], v[36:39], v[208:211]\n"
"s_add_u32 s1, s1, s33\n"
"ds_write_b128 v22, v[104:107] offset:128\n"
"v_mfma_f32_16x16x32_bf16 v[208:211], a[28:31], v[40:43], v[208:211]\n"
"s_add_u32 m0, s65, 154752\n"
"v_cvt_pk_bf16_f32 v254, v228, v229\n"
"v_cvt_pk_bf16_f32 v255, v230, v231\n"
"v_mfma_f32_16x16x32_bf16 v[208:211], a[40:43], v[44:47], v[208:211]\n"
"s_waitcnt vmcnt(10) lgkmcnt(3)\n"
"buffer_load_dwordx4 v27, s[12:15], s81 offen lds\n"
"ds_write_b128 v22, v[252:255] offset:64\n"
"v_mfma_f32_16x16x32_bf16 v[208:211], a[52:55], v[48:51], v[208:211]\n"
"s_waitcnt lgkmcnt(3)\n"
"v_bfi_b32 v97, v1, v97, v26\n"
"v_bfi_b32 v102, v2, v102, v26\n"
"v_mfma_f32_16x16x32_bf16 v[208:211], a[64:67], v[52:55], v[208:211]\n"
"s_add_u32 m0, s65, 155776\n"
"ds_read_b128 a[216:219], v70\n"
"buffer_load_dwordx4 v28, s[12:15], s81 offen lds\n"
"v_mfma_f32_16x16x32_bf16 v[212:215], a[8:11], v[32:35], 0\n"
"s_add_u32 s37, s37, 31616\n"
"ds_read_b128 a[220:223], v71\n"
"v_bfi_b32 v103, v3, v103, v26\n"
"v_mfma_f32_16x16x32_bf16 v[212:215], a[20:23], v[36:39], v[212:215]\n"
"s_mul_i32 s79, s51, 3072\n"
"s_mul_i32 s80, s66, 12288\n"
"v_mfma_f32_16x16x32_bf16 v[212:215], a[32:35], v[40:43], v[212:215]\n"
"s_add_u32 m0, s65, 156800\n"
"s_mov_b32 s26, 19267584\n"
"v_mfma_f32_16x16x32_bf16 v[212:215], a[44:47], v[44:47], v[212:215]\n"
"s_mov_b32 s27, 0x20000\n"
"s_mov_b32 s19, 0x20000\n"
"buffer_load_dwordx4 v29, s[12:15], s81 offen lds\n"
"v_mfma_f32_16x16x32_bf16 v[212:215], a[56:59], v[48:51], v[212:215]\n"
"s_add_u32 s40, s37, 22119552\n"
"s_sub_u32 s16, s16, 99456\n"
"s_subb_u32 s17, s17, 0\n"
"v_mfma_f32_16x16x32_bf16 v[212:215], a[68:71], v[52:55], v[212:215]\n"
"s_mov_b32 s18, 173184\n"
"s_add_u32 s80, s80, s79\n"
"v_mfma_f32_16x16x32_bf16 v[216:219], a[72:75], v[32:35], 0\n"
"v_mfma_f32_16x16x32_bf16 v[216:219], a[84:87], v[36:39], v[216:219]\n"
"s_waitcnt lgkmcnt(1)\n"
"v_mfma_f32_16x16x32_bf16 v[216:219], a[96:99], v[40:43], v[216:219]\n"
"v_mfma_f32_16x16x32_bf16 v[216:219], a[108:111], v[44:47], v[216:219]\n"
"s_nop 0\n"
"v_cvt_pk_bf16_f32 v212, v212, v213\n"
"v_mfma_f32_16x16x32_bf16 v[216:219], a[120:123], v[48:51], v[216:219]\n"
"v_cvt_pk_bf16_f32 v213, v214, v215\n"
"v_mfma_f32_16x16x32_bf16 v[216:219], a[132:135], v[52:55], v[216:219]\n"
"v_mfma_f32_16x16x32_bf16 v[220:223], a[76:79], v[32:35], 0\n"
"v_mfma_f32_16x16x32_bf16 v[220:223], a[88:91], v[36:39], v[220:223]\n"
"v_mfma_f32_16x16x32_bf16 v[220:223], a[100:103], v[40:43], v[220:223]\n"
"v_mfma_f32_16x16x32_bf16 v[220:223], a[112:115], v[44:47], v[220:223]\n"
"v_mfma_f32_16x16x32_bf16 v[220:223], a[124:127], v[48:51], v[220:223]\n"
"v_mfma_f32_16x16x32_bf16 v[220:223], a[136:139], v[52:55], v[220:223]\n"
"v_mfma_f32_16x16x32_bf16 v[224:227], a[80:83], v[32:35], 0\n"
"s_nop 0\n"
"v_cvt_pk_bf16_f32 v216, v216, v217\n"
"v_mfma_f32_16x16x32_bf16 v[224:227], a[92:95], v[36:39], v[224:227]\n"
"v_cvt_pk_bf16_f32 v217, v218, v219\n"
"v_mfma_f32_16x16x32_bf16 v[224:227], a[104:107], v[40:43], v[224:227]\n"
"v_mfma_f32_16x16x32_bf16 v[224:227], a[116:119], v[44:47], v[224:227]\n"
"s_nop 0\n"
"v_cvt_pk_bf16_f32 v218, v220, v221\n"
"v_mfma_f32_16x16x32_bf16 v[224:227], a[128:131], v[48:51], v[224:227]\n"
"v_cvt_pk_bf16_f32 v219, v222, v223\n"
"v_mfma_f32_16x16x32_bf16 v[224:227], a[140:143], v[52:55], v[224:227]\n"
"ds_write_b128 v186, v[216:219] offset:20384\n"
"v_mfma_f32_16x16x32_bf16 v[204:207], a[48:51], v[48:51], v[204:207]\n"
"v_mfma_f32_16x16x32_bf16 v[204:207], a[60:63], v[52:55], v[204:207]\n"
"v_mfma_f32_16x16x32_bf16 v[244:247], a[0:3], a[216:219], 0\n"
"s_waitcnt lgkmcnt(1)\n"
"v_mfma_f32_16x16x32_bf16 v[244:247], a[12:15], a[220:223], v[244:247]\n"
"v_mfma_f32_16x16x32_bf16 v[248:251], a[4:7], a[216:219], 0\n"
"s_nop 0\n"
"v_cvt_pk_bf16_f32 v214, v224, v225\n"
"v_mfma_f32_16x16x32_bf16 v[248:251], a[16:19], a[220:223], v[248:251]\n"
"s_waitcnt vmcnt(11)\n"
"v_cvt_pk_bf16_f32 v204, v204, v205\n"
"v_cvt_pk_bf16_f32 v205, v206, v207\n"
"v_mfma_f32_16x16x32_bf16 v[252:255], a[8:11], a[216:219], 0\n"
"s_add_u32 m0, s43, 0\n"
"ds_read_b128 a[224:227], v70 offset:1024\n"
"buffer_load_dwordx4 v27, s[12:15], s3 offen lds\n"
"v_mfma_f32_16x16x32_bf16 v[252:255], a[20:23], a[220:223], v[252:255]\n"
"ds_read_b128 a[228:231], v71 offset:1024\n"
"v_cvt_pk_bf16_f32 v206, v208, v209\n"
"v_mfma_f32_16x16x32_bf16 v[104:107], a[72:75], a[216:219], 0\n"
"s_add_u32 m0, s43, 1024\n"
"v_cvt_pk_bf16_f32 v207, v210, v211\n"
"v_cvt_pk_bf16_f32 v215, v226, v227\n"
"v_mfma_f32_16x16x32_bf16 v[104:107], a[84:87], a[220:223], v[104:107]\n"
"ds_write_b128 v186, v[204:207] offset:20256\n"
"v_mfma_f32_16x16x32_bf16 v[108:111], a[76:79], a[216:219], 0\n"
"buffer_load_dwordx4 v28, s[12:15], s3 offen lds\n"
"v_mfma_f32_16x16x32_bf16 v[108:111], a[88:91], a[220:223], v[108:111]\n"
"ds_write_b128 v186, v[212:215] offset:20320\n"
"v_mfma_f32_16x16x32_bf16 v[228:231], a[80:83], a[216:219], 0\n"
"s_waitcnt vmcnt(11) lgkmcnt(3)\n"
"s_add_u32 m0, s43, 2048\n"
"s_add_u32 s43, s79, 0\n"
"v_mfma_f32_16x16x32_bf16 v[228:231], a[92:95], a[220:223], v[228:231]\n"
"buffer_load_dwordx4 v29, s[12:15], s3 offen lds\n"
"ds_read_b128 a[232:235], v70 offset:2048\n"
"v_mfma_f32_16x16x32_bf16 v[228:231], a[104:107], a[224:227], v[228:231]\n"
"s_waitcnt lgkmcnt(3)\n"
"ds_read_b128 a[236:239], v71 offset:2048\n"
"v_mfma_f32_16x16x32_bf16 v[228:231], a[116:119], a[228:231], v[228:231]\n"
"s_add_u32 m0, s34, 0\n"
"v_mfma_f32_16x16x32_bf16 v[244:247], a[24:27], a[224:227], v[244:247]\n"
"buffer_load_dwordx4 v190, s[8:11], s32 offen lds\n"
"v_mfma_f32_16x16x32_bf16 v[244:247], a[36:39], a[228:231], v[244:247]\n"
"v_mfma_f32_16x16x32_bf16 v[248:251], a[28:31], a[224:227], v[248:251]\n"
"v_mfma_f32_16x16x32_bf16 v[248:251], a[40:43], a[228:231], v[248:251]\n"
"s_add_u32 m0, s34, 3072\n"
"v_mfma_f32_16x16x32_bf16 v[252:255], a[32:35], a[224:227], v[252:255]\n"
"s_waitcnt vmcnt(11)\n"
"buffer_load_dwordx4 v191, s[8:11], s32 offen lds\n"
"v_mfma_f32_16x16x32_bf16 v[252:255], a[44:47], a[228:231], v[252:255]\n"
"s_waitcnt lgkmcnt(1)\n"
"ds_read_b128 a[240:243], v188 offset:6144\n"
"ds_read_b128 a[244:247], v189 offset:6144\n"
"v_mfma_f32_16x16x32_bf16 v[252:255], a[56:59], a[232:235], v[252:255]\n"
"s_waitcnt lgkmcnt(2)\n"
"s_add_u32 s32, s32, s33\n"
"v_mfma_f32_16x16x32_bf16 v[252:255], a[68:71], a[236:239], v[252:255]\n"
"s_add_u32 m0, s34, 1024\n"
"v_mfma_f32_16x16x32_bf16 v[104:107], a[96:99], a[224:227], v[104:107]\n"
"buffer_load_dwordx4 v190, s[8:11], s0 offen lds\n"
"v_mfma_f32_16x16x32_bf16 v[104:107], a[108:111], a[228:231], v[104:107]\n"
"v_mfma_f32_16x16x32_bf16 v[104:107], a[120:123], a[232:235], v[104:107]\n"
"v_mfma_f32_16x16x32_bf16 v[104:107], a[132:135], a[236:239], v[104:107]\n"
"s_add_u32 m0, s34, 4096\n"
"v_mfma_f32_16x16x32_bf16 v[108:111], a[100:103], a[224:227], v[108:111]\n"
"v_cvt_pk_bf16_f32 v252, v252, v253\n"
"v_cvt_pk_bf16_f32 v253, v254, v255\n"
"v_mfma_f32_16x16x32_bf16 v[108:111], a[112:115], a[228:231], v[108:111]\n"
"s_waitcnt vmcnt(11) lgkmcnt(1)\n"
"buffer_load_dwordx4 v191, s[8:11], s0 offen lds\n"
"ds_read_b128 a[248:251], v188 offset:7168\n"
"v_mfma_f32_16x16x32_bf16 v[108:111], a[124:127], a[232:235], v[108:111]\n"
"s_add_u32 s0, s0, s33\n"
"ds_read_b128 a[252:255], v189 offset:7168\n"
"v_cvt_pk_bf16_f32 v104, v104, v105\n"
"v_mfma_f32_16x16x32_bf16 v[108:111], a[136:139], a[236:239], v[108:111]\n"
"s_add_u32 m0, s34, 2048\n"
"v_cvt_pk_bf16_f32 v105, v106, v107\n"
"v_mfma_f32_16x16x32_bf16 v[244:247], a[48:51], a[232:235], v[244:247]\n"
"buffer_load_dwordx4 v190, s[8:11], s1 offen lds\n"
"v_mfma_f32_16x16x32_bf16 v[244:247], a[60:63], a[236:239], v[244:247]\n"
"v_mfma_f32_16x16x32_bf16 v[248:251], a[52:55], a[232:235], v[248:251]\n"
"v_mfma_f32_16x16x32_bf16 v[248:251], a[64:67], a[236:239], v[248:251]\n"
"s_add_u32 m0, s34, 5120\n"
"v_cvt_pk_bf16_f32 v106, v108, v109\n"
"v_cvt_pk_bf16_f32 v107, v110, v111\n"
"v_mfma_f32_16x16x32_bf16 v[228:231], a[128:131], a[232:235], v[228:231]\n"
"ds_write_b128 v186, v[104:107] offset:33888\n"
"v_mfma_f32_16x16x32_bf16 v[228:231], a[140:143], a[236:239], v[228:231]\n"
"s_waitcnt vmcnt(11)\n"
"buffer_load_dwordx4 v191, s[8:11], s1 offen lds\n"
"v_cvt_pk_bf16_f32 v244, v244, v245\n"
"v_mfma_f32_16x16x32_bf16 v[120:123], a[0:3], a[240:243], 0\n"
"s_waitcnt lgkmcnt(3)\n"
"ds_read_b128 v[56:59], v188 offset:8192\n"
"ds_read_b128 v[60:63], v189 offset:8192\n"
"v_mfma_f32_16x16x32_bf16 v[120:123], a[12:15], a[244:247], v[120:123]\n"
"s_waitcnt lgkmcnt(4)\n"
"v_cvt_pk_bf16_f32 v245, v246, v247\n"
"v_cvt_pk_bf16_f32 v246, v248, v249\n"
"v_mfma_f32_16x16x32_bf16 v[120:123], a[24:27], a[248:251], v[120:123]\n"
"s_waitcnt lgkmcnt(3)\n"
"v_cvt_pk_bf16_f32 v247, v250, v251\n"
"v_cvt_pk_bf16_f32 v254, v228, v229\n"
"v_mfma_f32_16x16x32_bf16 v[120:123], a[36:39], a[252:255], v[120:123]\n"
"s_add_u32 m0, s34, 6144\n"
"ds_write_b128 v186, v[244:247] offset:33760\n"
"v_mfma_f32_16x16x32_bf16 v[124:127], a[4:7], a[240:243], 0\n"
"s_add_u32 s1, s1, s33\n"
"buffer_load_dwordx4 v190, s[8:11], s32 offen lds\n"
"v_cvt_pk_bf16_f32 v255, v230, v231\n"
"v_mfma_f32_16x16x32_bf16 v[124:127], a[16:19], a[244:247], v[124:127]\n"
"ds_write_b128 v186, v[252:255] offset:33824\n"
"v_mfma_f32_16x16x32_bf16 v[124:127], a[28:31], a[248:251], v[124:127]\n"
"s_waitcnt lgkmcnt(3)\n"
"v_mov_b64 v[252:253], 0\n"
"v_mov_b64 v[254:255], 0\n"
"v_mfma_f32_16x16x32_bf16 v[124:127], a[40:43], a[252:255], v[124:127]\n"
"ds_read_b64 v[104:105], v20 offset:1088\n"
"ds_read_b64 v[110:111], v20 offset:1152\n"
"v_mfma_f32_16x16x32_bf16 v[124:127], a[52:55], v[56:59], v[124:127]\n"
"s_waitcnt lgkmcnt(4)\n"
"v_subrev_u32 v20, s49, v20\n"
"v_mfma_f32_16x16x32_bf16 v[124:127], a[64:67], v[60:63], v[124:127]\n"
"v_mfma_f32_16x16x32_bf16 v[128:131], a[8:11], a[240:243], 0\n"
"v_mfma_f32_16x16x32_bf16 v[128:131], a[20:23], a[244:247], v[128:131]\n"
"v_mfma_f32_16x16x32_bf16 v[128:131], a[32:35], a[248:251], v[128:131]\n"
"v_mfma_f32_16x16x32_bf16 v[128:131], a[44:47], a[252:255], v[128:131]\n"
"v_mfma_f32_16x16x32_bf16 v[128:131], a[56:59], v[56:59], v[128:131]\n"
"s_waitcnt lgkmcnt(1)\n"
"v_mfma_f32_16x16x32_bf16 v[128:131], a[68:71], v[60:63], v[128:131]\n"
"s_waitcnt lgkmcnt(0)\n"
"v_bfi_b32 v104, v8, v104, v26\n"
"v_bfi_b32 v105, v9, v105, v26\n"
"v_mfma_f32_16x16x32_bf16 v[132:135], a[72:75], a[240:243], 0\n"
"v_bfi_b32 v110, v10, v110, v26\n"
"v_bfi_b32 v111, v11, v111, v26\n"
"v_mfma_f32_16x16x32_bf16 v[132:135], a[84:87], a[244:247], v[132:135]\n"
"s_nop 0\n"
"v_cvt_pk_bf16_f32 v128, v128, v129\n"
"v_mfma_f32_16x16x32_bf16 v[132:135], a[96:99], a[248:251], v[132:135]\n"
"v_cvt_pk_bf16_f32 v129, v130, v131\n"
"v_mfma_f32_16x16x32_bf16 v[132:135], a[108:111], a[252:255], v[132:135]\n"
"v_mfma_f32_16x16x32_bf16 v[132:135], a[120:123], v[56:59], v[132:135]\n"
"v_mfma_f32_16x16x32_bf16 v[132:135], a[132:135], v[60:63], v[132:135]\n"
"v_mfma_f32_16x16x32_bf16 v[136:139], a[76:79], a[240:243], 0\n"
"v_mfma_f32_16x16x32_bf16 v[136:139], a[88:91], a[244:247], v[136:139]\n"
"v_mfma_f32_16x16x32_bf16 v[136:139], a[100:103], a[248:251], v[136:139]\n"
"v_mfma_f32_16x16x32_bf16 v[136:139], a[112:115], a[252:255], v[136:139]\n"
"v_mfma_f32_16x16x32_bf16 v[136:139], a[124:127], v[56:59], v[136:139]\n"
"s_add_u32 m0, s34, 9216\n"
"s_nop 0\n"
"buffer_load_dwordx4 v191, s[8:11], s32 offen lds\n"
"v_mfma_f32_16x16x32_bf16 v[136:139], a[136:139], v[60:63], v[136:139]\n"
"s_add_u32 s32, s32, s33\n"
"v_cvt_pk_bf16_f32 v132, v132, v133\n"
"v_cvt_pk_bf16_f32 v133, v134, v135\n"
"v_mfma_f32_16x16x32_bf16 v[140:143], a[80:83], a[240:243], 0\n"
"v_mfma_f32_16x16x32_bf16 v[140:143], a[92:95], a[244:247], v[140:143]\n"
"v_mfma_f32_16x16x32_bf16 v[140:143], a[104:107], a[248:251], v[140:143]\n"
"s_add_u32 m0, s34, 7168\n"
"s_nop 0\n"
"buffer_load_dwordx4 v190, s[8:11], s0 offen lds\n"
"v_mfma_f32_16x16x32_bf16 v[140:143], a[116:119], a[252:255], v[140:143]\n"
"v_cvt_pk_bf16_f32 v134, v136, v137\n"
"v_cvt_pk_bf16_f32 v135, v138, v139\n"
"v_mfma_f32_16x16x32_bf16 v[140:143], a[128:131], v[56:59], v[140:143]\n"
"ds_write_b128 v186, v[132:135] offset:47392\n"
"v_mfma_f32_16x16x32_bf16 v[140:143], a[140:143], v[60:63], v[140:143]\n"
"s_add_u32 m0, s34, 10240\n"
"v_mfma_f32_16x16x32_bf16 v[120:123], a[48:51], v[56:59], v[120:123]\n"
"buffer_load_dwordx4 v191, s[8:11], s0 offen lds\n"
"s_add_u32 s0, s0, s33\n"
"v_mfma_f32_16x16x32_bf16 v[120:123], a[60:63], v[60:63], v[120:123]\n"
"s_nop 2\n"
"v_cvt_pk_bf16_f32 v130, v140, v141\n"
"v_cvt_pk_bf16_f32 v131, v142, v143\n"
"ds_write_b128 v186, v[128:131] offset:47328\n"
"s_add_u32 m0, s34, 8192\n"
"s_nop 0\n"
"v_cvt_pk_bf16_f32 v120, v120, v121\n"
"buffer_load_dwordx4 v190, s[8:11], s1 offen lds\n"
"v_cvt_pk_bf16_f32 v121, v122, v123\n"
"v_cvt_pk_bf16_f32 v122, v124, v125\n"
"v_cvt_pk_bf16_f32 v123, v126, v127\n"
"ds_write_b128 v186, v[120:123] offset:47264\n"
"s_add_u32 m0, s34, 11264\n"
"s_waitcnt vmcnt(11)\n"
"buffer_load_dwordx4 v191, s[8:11], s1 offen lds\n"
"s_add_u32 s1, s1, s33\n"
"s_barrier\n"
"ds_read_b64_tr_b16 a[144:145], v68\n"
"ds_read_b64_tr_b16 a[146:147], v68 offset:1536\n"
"ds_read_b64_tr_b16 a[148:149], v68 offset:32\n"
"ds_read_b64_tr_b16 a[150:151], v68 offset:1568\n"
"ds_read_b64_tr_b16 a[152:153], v68 offset:64\n"
"ds_read_b64_tr_b16 a[154:155], v68 offset:1600\n"
"ds_read_b64_tr_b16 a[156:157], v68 offset:3072\n"
"ds_read_b64_tr_b16 a[158:159], v68 offset:4608\n"
"buffer_load_dword v200, off, s[28:31], s39 sc1\n"
"ds_read_b64_tr_b16 a[160:161], v68 offset:3104\n"
"ds_read_b64_tr_b16 a[162:163], v68 offset:4640\n"
"ds_read_b64_tr_b16 a[164:165], v68 offset:3136\n"
"ds_read_b64_tr_b16 a[166:167], v68 offset:4672\n"
"ds_read_b64_tr_b16 a[168:169], v68 offset:6752\n"
"ds_read_b64_tr_b16 a[170:171], v68 offset:8288\n"
"ds_read_b64_tr_b16 a[172:173], v68 offset:6784\n"
"ds_read_b64_tr_b16 a[174:175], v68 offset:8320\n"
"ds_read_b64_tr_b16 a[176:177], v68 offset:6816\n"
"ds_read_b64_tr_b16 a[178:179], v68 offset:8352\n"
"ds_read_b64_tr_b16 a[180:181], v68 offset:9824\n"
"s_waitcnt lgkmcnt(15)\n"
"v_mfma_f32_16x16x32_bf16 v[204:207], a[144:147], v[32:35], 0\n"
"ds_read_b64_tr_b16 a[182:183], v68 offset:11360\n"
"ds_read_b64_tr_b16 a[184:185], v68 offset:9856\n"
"v_mfma_f32_16x16x32_bf16 v[208:211], a[148:151], v[32:35], 0\n"
"s_waitcnt lgkmcnt(15)\n"
"ds_read_b64_tr_b16 a[186:187], v68 offset:11392\n"
"ds_read_b64_tr_b16 a[188:189], v68 offset:9888\n"
"v_mfma_f32_16x16x32_bf16 v[212:215], a[152:155], v[32:35], 0\n"
"s_waitcnt lgkmcnt(15)\n"
"ds_read_b64_tr_b16 a[190:191], v68 offset:11424\n"
"ds_read_b64_tr_b16 a[192:193], v69\n"
"v_mfma_f32_16x16x32_bf16 v[204:207], a[156:159], v[36:39], v[204:207]\n"
"s_waitcnt lgkmcnt(15)\n"
"ds_read_b64_tr_b16 a[194:195], v69 offset:1536\n"
"ds_read_b64_tr_b16 a[196:197], v69 offset:32\n"
"v_mfma_f32_16x16x32_bf16 v[208:211], a[160:163], v[36:39], v[208:211]\n"
"s_waitcnt lgkmcnt(15)\n"
"ds_read_b64_tr_b16 a[198:199], v69 offset:1568\n"
"ds_read_b64_tr_b16 a[200:201], v69 offset:64\n"
"v_mfma_f32_16x16x32_bf16 v[212:215], a[164:167], v[36:39], v[212:215]\n"
"s_waitcnt lgkmcnt(15)\n"
"ds_read_b64_tr_b16 a[202:203], v69 offset:1600\n"
"ds_read_b64_tr_b16 a[204:205], v69 offset:3072\n"
"v_mfma_f32_16x16x32_bf16 v[204:207], a[168:171], v[40:43], v[204:207]\n"
"s_waitcnt lgkmcnt(15)\n"
"ds_read_b64_tr_b16 a[206:207], v69 offset:4608\n"
"ds_read_b64_tr_b16 a[208:209], v69 offset:3104\n"
"v_mfma_f32_16x16x32_bf16 v[208:211], a[172:175], v[40:43], v[208:211]\n"
"s_waitcnt lgkmcnt(15)\n"
"ds_read_b64_tr_b16 a[210:211], v69 offset:4640\n"
"ds_read_b64_tr_b16 a[212:213], v69 offset:3136\n"
"v_mfma_f32_16x16x32_bf16 v[212:215], a[176:179], v[40:43], v[212:215]\n"
"s_waitcnt lgkmcnt(15)\n"
"ds_read_b64_tr_b16 a[214:215], v69 offset:4672\n"
"v_mfma_f32_16x16x32_bf16 v[204:207], a[180:183], v[44:47], v[204:207]\n"
"s_waitcnt lgkmcnt(14)\n"
"v_mfma_f32_16x16x32_bf16 v[208:211], a[184:187], v[44:47], v[208:211]\n"
"s_waitcnt lgkmcnt(12)\n"
"v_mfma_f32_16x16x32_bf16 v[212:215], a[188:191], v[44:47], v[212:215]\n"
"s_waitcnt lgkmcnt(10)\n"
"v_mfma_f32_16x16x32_bf16 v[204:207], a[192:195], v[48:51], v[204:207]\n"
"s_waitcnt lgkmcnt(8)\n"
"v_mfma_f32_16x16x32_bf16 v[208:211], a[196:199], v[48:51], v[208:211]\n"
"s_waitcnt lgkmcnt(6)\n"
"v_mfma_f32_16x16x32_bf16 v[212:215], a[200:203], v[48:51], v[212:215]\n"
"s_waitcnt lgkmcnt(4)\n"
"v_mfma_f32_16x16x32_bf16 v[204:207], a[204:207], v[52:55], v[204:207]\n"
"s_waitcnt lgkmcnt(2)\n"
"v_mfma_f32_16x16x32_bf16 v[208:211], a[208:211], v[52:55], v[208:211]\n"
"s_waitcnt lgkmcnt(0)\n"
"s_barrier\n"
"v_mfma_f32_16x16x32_bf16 v[212:215], a[212:215], v[52:55], v[212:215]\n"
"ds_read_b128 v[112:115], v178\n"
"ds_read_b64 v[82:83], v178 offset:64\n"
"v_mfma_f32_16x16x32_bf16 v[216:219], a[144:147], a[216:219], 0\n"
"ds_read_b128 v[120:123], v179\n"
"buffer_load_dword v237, v67, s[8:11], s78 offen\n"
"v_mfma_f32_16x16x32_bf16 v[216:219], a[156:159], a[220:223], v[216:219]\n"
"s_add_u32 s78, s78, s33\n"
"ds_read_b64 v[84:85], v179 offset:64\n"
"v_cvt_pk_bf16_f32 v144, v204, v205\n"
"v_mfma_f32_16x16x32_bf16 v[216:219], a[168:171], a[224:227], v[216:219]\n"
"s_waitcnt vmcnt(12)\n"
"ds_read_b128 v[116:119], v178 offset:13504\n"
"v_cvt_pk_bf16_f32 v145, v206, v207\n"
"v_mfma_f32_16x16x32_bf16 v[216:219], a[180:183], a[228:231], v[216:219]\n"
"ds_read_b64 v[90:91], v178 offset:13568\n"
"v_cvt_pk_bf16_f32 v146, v208, v209\n"
"v_mfma_f32_16x16x32_bf16 v[216:219], a[192:195], a[232:235], v[216:219]\n"
"ds_read_b128 v[124:127], v179 offset:13504\n"
"v_cvt_pk_bf16_f32 v158, v212, v213\n"
"v_mfma_f32_16x16x32_bf16 v[216:219], a[204:207], a[236:239], v[216:219]\n"
"ds_read_b64 v[92:93], v179 offset:13568\n"
"v_cvt_pk_bf16_f32 v159, v214, v215\n"
"v_mfma_f32_16x16x32_bf16 v[220:223], a[148:151], a[216:219], 0\n"
".Lic0%=:\n"
"s_cbranch_execz .Lib0%=\n"
"ds_read_b128 v[128:131], v178 offset:27008\n"
"v_cvt_pk_bf16_f32 v147, v210, v211\n"
"v_mfma_f32_16x16x32_bf16 v[220:223], a[160:163], a[220:223], v[220:223]\n"
"ds_read_b64 v[98:99], v178 offset:27072\n"
"buffer_load_dword v237, v67, s[8:11], s78 offen\n"
"v_mfma_f32_16x16x32_bf16 v[220:223], a[172:175], a[224:227], v[220:223]\n"
"s_waitcnt lgkmcnt(8)\n"
"ds_read_b128 v[136:139], v179 offset:27008\n"
"v_cvt_pk_bf16_f32 v148, v216, v217\n"
"v_mfma_f32_16x16x32_bf16 v[112:115], v[112:115], v[144:147], 0\n"
"s_add_u32 s78, s78, s33\n"
"ds_read_b64 v[100:101], v179 offset:27072\n"
"v_cvt_pk_bf16_f32 v149, v218, v219\n"
"v_mfma_f32_16x16x32_bf16 v[112:115], v[80:83], v[156:159], v[112:115]\n"
"s_waitcnt vmcnt(11) lgkmcnt(8)\n"
"ds_read_b128 v[32:35], v188\n"
"v_subrev_u32 v178, s48, v178\n"
"v_mfma_f32_16x16x32_bf16 v[120:123], v[120:123], v[144:147], 0\n"
"ds_read_b128 v[36:39], v189\n"
"v_subrev_u32 v179, s48, v179\n"
"v_mfma_f32_16x16x32_bf16 v[120:123], v[84:87], v[158:161], v[120:123]\n"
"s_waitcnt lgkmcnt(8)\n"
"ds_read_b128 v[132:135], v180\n"
"v_mfma_f32_16x16x32_bf16 v[116:119], v[116:119], v[144:147], 0\n"
"ds_read_b64 v[106:107], v180 offset:64\n"
"v_subrev_u32 v180, s48, v180\n"
"v_mfma_f32_16x16x32_bf16 v[116:119], v[88:91], v[156:159], v[116:119]\n"
"s_waitcnt lgkmcnt(8)\n"
"ds_read_b128 v[140:143], v181\n"
"v_max_f32 v112, v112, v120\n"
"v_mfma_f32_16x16x32_bf16 v[124:127], v[124:127], v[144:147], 0\n"
"ds_read_b64 v[108:109], v181 offset:64\n"
"v_subrev_u32 v181, s48, v181\n"
"v_mfma_f32_16x16x32_bf16 v[124:127], v[92:95], v[158:161], v[124:127]\n"
"s_waitcnt lgkmcnt(8)\n"
"ds_read_b128 v[40:43], v188 offset:1024\n"
"v_max_f32 v113, v113, v121\n"
"v_mfma_f32_16x16x32_bf16 v[128:131], v[128:131], v[144:147], 0\n"
"ds_read_b128 v[44:47], v189 offset:1024\n"
"v_max_f32 v114, v114, v122\n"
"v_mfma_f32_16x16x32_bf16 v[128:131], v[96:99], v[156:159], v[128:131]\n"
"s_waitcnt lgkmcnt(8)\n"
"ds_read_b64_tr_b16 v[244:245], v182 offset:128\n"
"v_max_f32 v115, v115, v123\n"
"v_mfma_f32_16x16x32_bf16 v[136:139], v[136:139], v[144:147], 0\n"
"s_waitcnt lgkmcnt(8)\n"
"ds_read_b64_tr_b16 v[246:247], v183 offset:128\n"
"v_max3_f32 v170, v112, v113, v114\n"
"v_mfma_f32_16x16x32_bf16 v[136:139], v[100:103], v[158:161], v[136:139]\n"
"s_waitcnt lgkmcnt(8)\n"
"ds_read_b64_tr_b16 v[248:249], v182 offset:896\n"
"v_max_f32 v116, v116, v124\n"
"v_mfma_f32_16x16x32_bf16 v[212:215], a[0:3], v[32:35], 0\n"
"s_waitcnt vmcnt(9)\n"
"ds_read_b128 v[48:51], v188 offset:2048\n"
"v_max_f32 v117, v117, v125\n"
"v_mfma_f32_16x16x32_bf16 v[212:215], a[12:15], v[36:39], v[212:215]\n"
"s_waitcnt lgkmcnt(8)\n"
"ds_read_b128 v[52:55], v189 offset:2048\n"
"v_max_f32 v118, v118, v126\n"
"v_mfma_f32_16x16x32_bf16 v[132:135], v[132:135], v[144:147], 0\n"
"s_add_u32 m0, s34, 0\n"
"ds_read_b64_tr_b16 v[250:251], v183 offset:896\n"
"buffer_load_dwordx4 v190, s[8:11], s32 offen lds\n"
"v_mfma_f32_16x16x32_bf16 v[132:135], v[104:107], v[156:159], v[132:135]\n"
"s_waitcnt lgkmcnt(8)\n"
"ds_read_b64_tr_b16 v[120:121], v182 offset:136\n"
"v_max_f32 v119, v119, v127\n"
"v_mfma_f32_16x16x32_bf16 v[140:143], v[140:143], v[144:147], 0\n"
"s_waitcnt lgkmcnt(8)\n"
"ds_read_b64_tr_b16 v[122:123], v183 offset:136\n"
"v_max3_f32 v170, v170, v115, v116\n"
"v_mfma_f32_16x16x32_bf16 v[140:143], v[108:111], v[158:161], v[140:143]\n"
"ds_read_b64_tr_b16 v[124:125], v182 offset:904\n"
"v_max3_f32 v170, v170, v117, v118\n"
"v_mfma_f32_16x16x32_bf16 v[212:215], a[24:27], v[40:43], v[212:215]\n"
"s_waitcnt lgkmcnt(9)\n"
"ds_read_b64_tr_b16 v[126:127], v183 offset:904\n"
"v_max_f32 v128, v128, v136\n"
"v_mfma_f32_16x16x32_bf16 v[212:215], a[36:39], v[44:47], v[212:215]\n"
"v_max_f32 v129, v129, v137\n"
"v_max_f32 v130, v130, v138\n"
"v_mfma_f32_16x16x32_bf16 v[216:219], a[4:7], v[32:35], 0\n"
"ds_read_b64_tr_b16 v[136:137], v182 offset:72\n"
"v_max_f32 v131, v131, v139\n"
"v_mfma_f32_16x16x32_bf16 v[216:219], a[16:19], v[36:39], v[216:219]\n"
"s_waitcnt lgkmcnt(7)\n"
"ds_read_b64_tr_b16 v[138:139], v183 offset:72\n"
"v_max3_f32 v170, v170, v119, v128\n"
"v_mfma_f32_16x16x32_bf16 v[216:219], a[28:31], v[40:43], v[216:219]\n"
"v_max3_f32 v170, v170, v129, v130\n"
"v_max_f32 v132, v132, v140\n"
"v_mfma_f32_16x16x32_bf16 v[216:219], a[40:43], v[44:47], v[216:219]\n"
"s_waitcnt lgkmcnt(7)\n"
"v_max_f32 v133, v133, v141\n"
"v_max_f32 v134, v134, v142\n"
"v_mfma_f32_16x16x32_bf16 v[216:219], a[52:55], v[48:51], v[216:219]\n"
"s_add_u32 m0, s34, 3072\n"
"ds_read_b64_tr_b16 v[140:141], v182 offset:840\n"
"buffer_load_dwordx4 v191, s[8:11], s32 offen lds\n"
"v_mfma_f32_16x16x32_bf16 v[216:219], a[64:67], v[52:55], v[216:219]\n"
"s_add_u32 s32, s32, s33\n"
"v_max_f32 v135, v135, v143\n"
"v_max3_f32 v170, v170, v131, v132\n"
"v_mfma_f32_16x16x32_bf16 v[212:215], a[48:51], v[48:51], v[212:215]\n"
"ds_read_b64_tr_b16 v[142:143], v183 offset:840\n"
"v_max3_f32 v170, v170, v133, v134\n"
"v_mfma_f32_16x16x32_bf16 v[212:215], a[60:63], v[52:55], v[212:215]\n"
"v_mov_b64 v[98:99], 0\n"
"v_mov_b64 v[100:101], 0\n"
"v_mfma_f32_16x16x32_bf16 v[220:223], a[184:187], a[228:231], v[220:223]\n"
"v_mov_b64 v[106:107], 0\n"
"v_mov_b64 v[108:109], 0\n"
"v_mfma_f32_16x16x32_bf16 v[220:223], a[196:199], a[232:235], v[220:223]\n"
"v_max_f32 v171, v170, v135\n"
"v_max_f32 v170, v170, v135\n"
"v_mfma_f32_16x16x32_bf16 v[220:223], a[208:211], a[236:239], v[220:223]\n"
"v_cvt_pk_bf16_f32 v212, v212, v213\n"
"v_cvt_pk_bf16_f32 v213, v214, v215\n"
"v_mfma_f32_16x16x32_bf16 v[228:231], a[144:147], a[240:243], 0\n"
"v_cvt_pk_bf16_f32 v214, v216, v217\n"
"v_cvt_pk_bf16_f32 v215, v218, v219\n"
"v_mfma_f32_16x16x32_bf16 v[228:231], a[156:159], a[244:247], v[228:231]\n"
"ds_write_b128 v186, v[212:215] offset:60768\n"
"v_mfma_f32_16x16x32_bf16 v[228:231], a[168:171], a[248:251], v[228:231]\n"
"s_add_u32 m0, s34, 1024\n"
"v_cvt_pk_bf16_f32 v150, v220, v221\n"
"v_cvt_pk_bf16_f32 v151, v222, v223\n"
"v_mfma_f32_16x16x32_bf16 v[228:231], a[180:183], a[252:255], v[228:231]\n"
"buffer_load_dwordx4 v190, s[8:11], s0 offen lds\n"
"ds_read_b64 v[80:81], v20 offset:272\n"
"v_mfma_f32_16x16x32_bf16 v[228:231], a[192:195], v[56:59], v[228:231]\n"
"v_permlane16_swap_b32 v170, v171\n"
"v_mfma_f32_16x16x32_bf16 v[228:231], a[204:207], v[60:63], v[228:231]\n"
"v_max_f32 v170, v170, v171\n"
"v_mov_b32 v171, v170\n"
"v_mfma_f32_16x16x32_bf16 v[220:223], a[8:11], v[32:35], 0\n"
"s_nop 0\n"
"v_permlane32_swap_b32 v170, v171\n"
"v_max_f32 v170, v170, v171\n"
"v_mul_f32 v170, 0xbe553b95, v170\n"
"v_pk_fma_f32 v[112:113], v[112:113], s[70:71], v[170:171] op_sel_hi:[1,0,0]\n"
"v_pk_fma_f32 v[114:115], v[114:115], s[70:71], v[170:171] op_sel_hi:[1,0,0]\n"
"v_pk_fma_f32 v[116:117], v[116:117], s[70:71], v[170:171] op_sel_hi:[1,0,0]\n"
"v_pk_fma_f32 v[118:119], v[118:119], s[70:71], v[170:171] op_sel_hi:[1,0,0]\n"
"v_pk_fma_f32 v[128:129], v[128:129], s[70:71], v[170:171] op_sel_hi:[1,0,0]\n"
"v_pk_fma_f32 v[130:131], v[130:131], s[70:71], v[170:171] op_sel_hi:[1,0,0]\n"
"v_pk_fma_f32 v[132:133], v[132:133], s[70:71], v[170:171] op_sel_hi:[1,0,0]\n"
"v_pk_fma_f32 v[134:135], v[134:135], s[70:71], v[170:171] op_sel_hi:[1,0,0]\n"
"v_mfma_f32_16x16x32_bf16 v[220:223], a[20:23], v[36:39], v[220:223]\n"
"v_cvt_pk_bf16_f32 v152, v228, v229\n"
"v_cvt_pk_bf16_f32 v153, v230, v231\n"
"v_mfma_f32_16x16x32_bf16 v[220:223], a[32:35], v[40:43], v[220:223]\n"
"s_waitcnt lgkmcnt(0)\n"
"v_exp_f32 v112, v112\n"
"v_mfma_f32_16x16x32_bf16 v[220:223], a[44:47], v[44:47], v[220:223]\n"
"s_add_u32 m0, s34, 4096\n"
"v_exp_f32 v113, v113\n"
"v_mfma_f32_16x16x32_bf16 v[220:223], a[56:59], v[48:51], v[220:223]\n"
"buffer_load_dwordx4 v191, s[8:11], s0 offen lds\n"
"ds_read_b64 v[86:87], v20 offset:336\n"
"v_mfma_f32_16x16x32_bf16 v[220:223], a[68:71], v[52:55], v[220:223]\n"
"s_add_u32 s0, s0, s33\n"
"v_exp_f32 v114, v114\n"
"v_mfma_f32_16x16x32_bf16 v[204:207], a[148:151], a[240:243], 0\n"
"v_exp_f32 v115, v115\n"
"v_mfma_f32_16x16x32_bf16 v[204:207], a[160:163], a[244:247], v[204:207]\n"
"ds_read_b64 v[88:89], v20 offset:544\n"
"v_cvt_pk_bf16_f32 v82, v112, v114\n"
"v_mfma_f32_16x16x32_bf16 v[204:207], a[172:175], a[248:251], v[204:207]\n"
"v_cvt_pk_bf16_f32 v220, v220, v221\n"
"v_cvt_pk_bf16_f32 v221, v222, v223\n"
"v_mfma_f32_16x16x32_bf16 v[204:207], a[184:187], a[252:255], v[204:207]\n"
"v_cvt_pk_bf16_f32 v83, v113, v115\n"
"v_exp_f32 v116, v116\n"
"v_mfma_f32_16x16x32_bf16 v[204:207], a[196:199], v[56:59], v[204:207]\n"
"ds_read_b64_tr_b16 v[112:113], v182 offset:27136\n"
"ds_read_b64_tr_b16 v[114:115], v183 offset:27136\n"
"v_mfma_f32_16x16x32_bf16 v[204:207], a[208:211], v[60:63], v[204:207]\n"
"s_waitcnt lgkmcnt(3)\n"
"ds_read_b64 v[94:95], v20 offset:608\n"
"v_exp_f32 v117, v117\n"
"v_mfma_f32_16x16x32_bf16 v[208:211], a[152:155], a[240:243], 0\n"
"s_add_u32 m0, s34, 2048\n"
"ds_read_b64 v[96:97], v20 offset:816\n"
"buffer_load_dwordx4 v190, s[8:11], s1 offen lds\n"
"v_mfma_f32_16x16x32_bf16 v[208:211], a[164:167], a[244:247], v[208:211]\n"
"v_cvt_pk_bf16_f32 v154, v204, v205\n"
"v_cvt_pk_bf16_f32 v155, v206, v207\n"
"v_mfma_f32_16x16x32_bf16 v[208:211], a[176:179], a[248:251], v[208:211]\n"
"s_waitcnt lgkmcnt(4)\n"
"v_exp_f32 v118, v118\n"
"v_mfma_f32_16x16x32_bf16 v[208:211], a[188:191], a[252:255], v[208:211]\n"
"s_waitcnt vmcnt(7)\n"
"ds_read_b64 v[102:103], v20 offset:880\n"
"v_cvt_pk_bf16_f32 v84, v116, v118\n"
"v_mfma_f32_16x16x32_bf16 v[208:211], a[200:203], v[56:59], v[208:211]\n"
"v_and_b32 v200, 0xffffff00, v200\n"
"v_add_u32 v200, 0x100, v200\n"
"v_mfma_f32_16x16x32_bf16 v[208:211], a[212:215], v[60:63], v[208:211]\n"
"s_mov_b64 exec, 1\n"
"buffer_store_dword v200, off, s[28:31], s39 offset:128 sc1\n"
"s_mov_b64 exec, -1\n"
"v_mfma_f32_16x16x32_bf16 v[224:227], a[152:155], a[216:219], 0\n"
"s_waitcnt lgkmcnt(2)\n"
"v_add_u32 v62, s5, v193\n"
"v_sub_u32 v201, 0, v200\n"
"v_mfma_f32_16x16x32_bf16 v[224:227], a[164:167], a[220:223], v[224:227]\n"
"s_waitcnt lgkmcnt(1)\n"
"s_nop 0\n"
"v_readfirstlane_b32 s50, v201\n"
"v_mfma_f32_16x16x32_bf16 v[224:227], a[176:179], a[224:227], v[224:227]\n"
"v_cvt_pk_bf16_f32 v166, v208, v209\n"
"v_cvt_pk_bf16_f32 v167, v210, v211\n"
"v_mfma_f32_16x16x32_bf16 v[224:227], a[188:191], a[228:231], v[224:227]\n"
"s_add_u32 m0, s34, 5120\n"
"v_exp_f32 v119, v119\n"
"v_mfma_f32_16x16x32_bf16 v[224:227], a[200:203], a[232:235], v[224:227]\n"
"s_waitcnt lgkmcnt(0)\n"
"buffer_load_dwordx4 v191, s[8:11], s1 offen lds\n"
"ds_read_b64 v[104:105], v20 offset:1088\n"
"v_mfma_f32_16x16x32_bf16 v[224:227], a[212:215], a[236:239], v[224:227]\n"
"s_add_u32 s1, s1, s33\n"
"v_cvt_pk_bf16_f32 v85, v117, v119\n"
"v_exp_f32 v128, v128\n"
"v_mfma_f32_16x16x32_bf16 v[228:231], a[76:79], v[32:35], 0\n"
"ds_read_b64_tr_b16 v[116:117], v182 offset:27904\n"
"ds_read_b64_tr_b16 v[118:119], v183 offset:27904\n"
"v_mfma_f32_16x16x32_bf16 v[228:231], a[88:91], v[36:39], v[228:231]\n"
"ds_read_b64 v[110:111], v20 offset:1152\n"
"v_cvt_pk_bf16_f32 v162, v224, v225\n"
"v_mfma_f32_16x16x32_bf16 v[228:231], a[100:103], v[40:43], v[228:231]\n"
"v_cvt_pk_bf16_f32 v163, v226, v227\n"
"v_exp_f32 v129, v129\n"
"v_mfma_f32_16x16x32_bf16 v[228:231], a[112:115], v[44:47], v[228:231]\n"
"v_exp_f32 v130, v130\n"
"v_mfma_f32_16x16x32_bf16 v[228:231], a[124:127], v[48:51], v[228:231]\n"
"v_exp_f32 v131, v131\n"
"v_mfma_f32_16x16x32_bf16 v[228:231], a[136:139], v[52:55], v[228:231]\n"
"s_waitcnt lgkmcnt(3)\n"
"v_cvt_pk_bf16_f32 v90, v128, v130\n"
"v_cvt_pk_bf16_f32 v91, v129, v131\n"
"v_mfma_f32_16x16x32_bf16 v[224:227], a[72:75], v[32:35], 0\n"
"ds_read_b128 v[128:131], v179 offset:13504\n"
"v_exp_f32 v132, v132\n"
"v_mfma_f32_16x16x32_bf16 v[224:227], a[84:87], v[36:39], v[224:227]\n"
"v_exp_f32 v133, v133\n"
"v_mfma_f32_16x16x32_bf16 v[224:227], a[96:99], v[40:43], v[224:227]\n"
"v_exp_f32 v134, v134\n"
"v_mfma_f32_16x16x32_bf16 v[224:227], a[108:111], v[44:47], v[224:227]\n"
"s_waitcnt lgkmcnt(1)\n"
"v_exp_f32 v135, v135\n"
"v_mfma_f32_16x16x32_bf16 v[224:227], a[120:123], v[48:51], v[224:227]\n"
"v_cvt_pk_bf16_f32 v92, v132, v134\n"
"v_cvt_pk_bf16_f32 v93, v133, v135\n"
"v_mfma_f32_16x16x32_bf16 v[224:227], a[132:135], v[52:55], v[224:227]\n"
"ds_read_b128 v[132:135], v179 offset:27008\n"
"v_bfi_b32 v80, v4, v80, v26\n"
"v_mfma_f32_16x16x32_bf16 v[204:207], a[80:83], v[32:35], 0\n"
"v_bfi_b32 v81, v5, v81, v26\n"
"v_bfi_b32 v86, v6, v86, v26\n"
"v_mfma_f32_16x16x32_bf16 v[204:207], a[92:95], v[36:39], v[204:207]\n"
"v_bfi_b32 v87, v7, v87, v26\n"
"v_bfi_b32 v88, v0, v88, v26\n"
"v_mfma_f32_16x16x32_bf16 v[204:207], a[104:107], v[40:43], v[204:207]\n"
"v_cvt_pk_bf16_f32 v224, v224, v225\n"
"v_cvt_pk_bf16_f32 v225, v226, v227\n"
"v_mfma_f32_16x16x32_bf16 v[204:207], a[116:119], v[44:47], v[204:207]\n"
"v_cvt_pk_bf16_f32 v226, v228, v229\n"
"v_cvt_pk_bf16_f32 v227, v230, v231\n"
"v_mfma_f32_16x16x32_bf16 v[204:207], a[128:131], v[48:51], v[204:207]\n"
"ds_write_b128 v186, v[224:227] offset:60896\n"
"v_mfma_f32_16x16x32_bf16 v[204:207], a[140:143], v[52:55], v[204:207]\n"
"v_bfi_b32 v89, v1, v89, v26\n"
"v_bfi_b32 v94, v2, v94, v26\n"
"v_mfma_f32_16x16x32_bf16 v[208:211], a[144:147], v[32:35], 0\n"
"v_bfi_b32 v95, v3, v95, v26\n"
"v_bfi_b32 v96, v0, v96, v26\n"
"v_mfma_f32_16x16x32_bf16 v[208:211], a[156:159], v[36:39], v[208:211]\n"
"v_bfi_b32 v97, v1, v97, v26\n"
"v_bfi_b32 v102, v2, v102, v26\n"
"v_mfma_f32_16x16x32_bf16 v[208:211], a[168:171], v[40:43], v[208:211]\n"
"v_cvt_pk_bf16_f32 v222, v204, v205\n"
"v_cvt_pk_bf16_f32 v223, v206, v207\n"
"v_mfma_f32_16x16x32_bf16 v[208:211], a[180:183], v[44:47], v[208:211]\n"
"ds_write_b128 v186, v[220:223] offset:60832\n"
"v_mfma_f32_16x16x32_bf16 v[208:211], a[192:195], v[48:51], v[208:211]\n"
"v_bfi_b32 v103, v3, v103, v26\n"
"v_bfi_b32 v104, v8, v104, v26\n"
"v_mfma_f32_16x16x32_bf16 v[208:211], a[204:207], v[52:55], v[208:211]\n"
"v_bfi_b32 v105, v9, v105, v26\n"
"v_bfi_b32 v110, v10, v110, v26\n"
"v_mfma_f32_16x16x32_bf16 v[216:219], a[148:151], v[32:35], 0\n"
"v_bfi_b32 v111, v11, v111, v26\n"
"v_mfma_f32_16x16x32_bf16 v[216:219], a[160:163], v[36:39], v[216:219]\n"
"v_mfma_f32_16x16x32_bf16 v[216:219], a[172:175], v[40:43], v[216:219]\n"
"v_mfma_f32_16x16x32_bf16 v[216:219], a[184:187], v[44:47], v[216:219]\n"
"s_nop 0\n"
"v_cvt_pk_bf16_f32 v144, v208, v209\n"
"v_mfma_f32_16x16x32_bf16 v[216:219], a[196:199], v[48:51], v[216:219]\n"
"v_cvt_pk_bf16_f32 v145, v210, v211\n"
"v_mfma_f32_16x16x32_bf16 v[216:219], a[208:211], v[52:55], v[216:219]\n"
"s_waitcnt lgkmcnt(0)\n"
"s_barrier\n"
"v_mfma_f32_16x16x32_bf16 v[212:215], a[152:155], v[32:35], 0\n"
"s_add_u32 m0, s79, 130176\n"
"ds_read_b128 v[32:35], v188 offset:6144\n"
"buffer_load_dwordx4 v192, s[16:19], s43 offen lds\n"
"v_mfma_f32_16x16x32_bf16 v[212:215], a[164:167], v[36:39], v[212:215]\n"
"ds_read_b128 v[36:39], v189 offset:6144\n"
"v_cvt_pk_bf16_f32 v146, v216, v217\n"
"v_mfma_f32_16x16x32_bf16 v[212:215], a[176:179], v[40:43], v[212:215]\n"
"ds_read_b128 v[40:43], v188 offset:7168\n"
"v_cvt_pk_bf16_f32 v147, v218, v219\n"
"v_mfma_f32_16x16x32_bf16 v[212:215], a[188:191], v[44:47], v[212:215]\n"
"ds_read_b128 v[44:47], v189 offset:7168\n"
"buffer_load_dword v237, v67, s[8:11], s78 offen\n"
"v_mfma_f32_16x16x32_bf16 v[212:215], a[200:203], v[48:51], v[212:215]\n"
"s_add_u32 s78, s78, s33\n"
"ds_read_b128 v[48:51], v188 offset:8192\n"
"v_mfma_f32_16x16x32_bf16 v[212:215], a[212:215], v[52:55], v[212:215]\n"
"ds_read_b128 v[52:55], v189 offset:8192\n"
"v_smfmac_f32_16x16x64_bf16 v[252:255], v[82:85], v[244:251], v66\n"
"ds_read_b64_tr_b16 v[244:245], v182 offset:27144\n"
"v_smfmac_f32_16x16x64_bf16 v[252:255], v[90:93], v[112:119], v66\n"
"ds_read_b64_tr_b16 v[246:247], v183 offset:27144\n"
"v_smfmac_f32_16x16x64_bf16 v[98:101], v[82:85], v[120:127], v66\n"
"s_waitcnt lgkmcnt(7)\n"
"ds_read_b64_tr_b16 v[248:249], v182 offset:27912\n"
"v_mfma_f32_16x16x32_bf16 v[228:231], a[0:3], v[32:35], 0\n"
"s_waitcnt lgkmcnt(7)\n"
"ds_read_b64_tr_b16 v[250:251], v183 offset:27912\n"
"ds_read_b64_tr_b16 v[120:121], v182 offset:27080\n"
"v_mfma_f32_16x16x32_bf16 v[228:231], a[12:15], v[36:39], v[228:231]\n"
"s_waitcnt lgkmcnt(8)\n"
"v_cvt_pk_bf16_f32 v158, v212, v213\n"
"v_cvt_pk_bf16_f32 v159, v214, v215\n"
"v_mfma_f32_16x16x32_bf16 v[228:231], a[24:27], v[40:43], v[228:231]\n"
"s_waitcnt lgkmcnt(7)\n"
"ds_read_b64_tr_b16 v[122:123], v183 offset:27080\n"
"ds_read_b64_tr_b16 v[124:125], v182 offset:27848\n"
"v_mfma_f32_16x16x32_bf16 v[228:231], a[36:39], v[44:47], v[228:231]\n"
"s_waitcnt lgkmcnt(8)\n"
"v_subrev_u32 v182, s48, v182\n"
"v_cvt_pk_bf16_f32 v252, v252, v253\n"
"v_mfma_f32_16x16x32_bf16 v[228:231], a[48:51], v[48:51], v[228:231]\n"
"s_waitcnt lgkmcnt(7)\n"
"ds_read_b64_tr_b16 v[126:127], v183 offset:27848\n"
"ds_read_b128 v[112:115], v180\n"
"v_mfma_f32_16x16x32_bf16 v[228:231], a[60:63], v[52:55], v[228:231]\n"
"s_add_u32 m0, s79, 130176\n"
"v_subrev_u32 v183, s48, v183\n"
"v_cvt_pk_bf16_f32 v253, v254, v255\n"
"v_smfmac_f32_16x16x64_bf16 v[106:109], v[82:85], v[136:143], v66\n"
"buffer_load_dwordx4 v192, s[16:19], s43 offen offset:1024 lds\n"
"v_mfma_f32_16x16x32_bf16 v[82:85], v[172:175], v[82:85], 0\n"
"ds_read_b128 v[136:139], v178 offset:13504\n"
"ds_read_b128 v[140:143], v178 offset:27008\n"
"v_mfma_f32_16x16x32_bf16 v[204:207], a[4:7], v[32:35], 0\n"
"s_waitcnt lgkmcnt(7)\n"
"ds_read_b128 v[116:119], v180 offset:13504\n"
"v_cvt_pk_bf16_f32 v228, v228, v229\n"
"v_mfma_f32_16x16x32_bf16 v[204:207], a[16:19], v[36:39], v[204:207]\n"
"v_cvt_pk_bf16_f32 v229, v230, v231\n"
"v_mfma_f32_16x16x32_bf16 v[204:207], a[28:31], v[40:43], v[204:207]\n"
"buffer_load_dword v237, v67, s[8:11], s78 offen\n"
"ds_read_b64_tr_b16 a[240:241], v182 offset:40640\n"
"v_mfma_f32_16x16x32_bf16 v[204:207], a[40:43], v[44:47], v[204:207]\n"
"s_add_u32 s78, s78, s33\n"
"ds_read_b64_tr_b16 a[242:243], v183 offset:40640\n"
"ds_read_b64_tr_b16 a[244:245], v182 offset:41408\n"
"v_mfma_f32_16x16x32_bf16 v[204:207], a[52:55], v[48:51], v[204:207]\n"
"s_waitcnt lgkmcnt(7)\n"
"ds_read_b64_tr_b16 a[246:247], v183 offset:41408\n"
"v_mfma_f32_16x16x32_bf16 v[204:207], a[64:67], v[52:55], v[204:207]\n"
"ds_read_b64_tr_b16 a[248:249], v182 offset:40648\n"
"v_smfmac_f32_16x16x64_bf16 v[98:101], v[90:93], v[244:251], v66\n"
"ds_read_b128 v[244:247], v181\n"
"v_smfmac_f32_16x16x64_bf16 v[106:109], v[90:93], v[120:127], v66\n"
"s_add_u32 m0, s79, 130176\n"
"s_nop 0\n"
"buffer_load_dwordx4 v192, s[16:19], s43 offen offset:2048 lds\n"
"v_mfma_f32_16x16x32_bf16 v[90:93], v[172:175], v[90:93], v[82:85]\n"
"s_add_u32 s43, s79, 18432\n"
"ds_read_b128 v[248:251], v181 offset:13504\n"
"ds_read_b64_tr_b16 v[120:121], v182 offset:13632\n"
"v_mfma_f32_16x16x32_bf16 v[220:223], a[8:11], v[32:35], 0\n"
"v_cvt_pk_bf16_f32 v98, v98, v99\n"
"v_cvt_pk_bf16_f32 v99, v100, v101\n"
"v_mfma_f32_16x16x32_bf16 v[220:223], a[20:23], v[36:39], v[220:223]\n"
"ds_read_b64 v[82:83], v178 offset:13568\n"
"ds_read_b64 v[84:85], v179 offset:13568\n"
"v_mfma_f32_16x16x32_bf16 v[220:223], a[32:35], v[40:43], v[220:223]\n"
"v_rcp_f32 v171, v90\n"
"v_mfma_f32_16x16x32_bf16 v[220:223], a[44:47], v[44:47], v[220:223]\n"
"ds_read_b64 v[90:91], v178 offset:27072\n"
"ds_read_b64 v[92:93], v179 offset:27072\n"
"v_mfma_f32_16x16x32_bf16 v[220:223], a[56:59], v[48:51], v[220:223]\n"
"v_cvt_pk_bf16_f32 v171, v171, v171\n"
"v_and_b32 v64, v171, v176\n"
"v_mfma_f32_16x16x32_bf16 v[220:223], a[68:71], v[52:55], v[220:223]\n"
"v_and_b32 v65, v171, v177\n"
"v_cvt_pk_bf16_f32 v106, v106, v107\n"
"v_mfma_f32_16x16x32_bf16 v[224:227], a[72:75], v[32:35], 0\n"
"v_cvt_pk_bf16_f32 v107, v108, v109\n"
"v_cvt_pk_bf16_f32 v230, v204, v205\n"
"v_mfma_f32_16x16x32_bf16 v[224:227], a[84:87], v[36:39], v[224:227]\n"
"s_add_u32 m0, s34, 6144\n"
"ds_read_b64_tr_b16 v[122:123], v183 offset:13632\n"
"buffer_load_dwordx4 v190, s[8:11], s32 offen lds\n"
"v_mfma_f32_16x16x32_bf16 v[224:227], a[96:99], v[40:43], v[224:227]\n"
"ds_read_b64_tr_b16 v[124:125], v182 offset:14400\n"
"ds_read_b64_tr_b16 v[126:127], v183 offset:14400\n"
"v_mfma_f32_16x16x32_bf16 v[224:227], a[108:111], v[44:47], v[224:227]\n"
"s_waitcnt lgkmcnt(6)\n"
"v_cvt_pk_bf16_f32 v231, v206, v207\n"
"v_cvt_pk_bf16_f32 v220, v220, v221\n"
"v_mfma_f32_16x16x32_bf16 v[224:227], a[120:123], v[48:51], v[224:227]\n"
"s_waitcnt lgkmcnt(5)\n"
"ds_write_b128 v187, v[228:231]\n"
"v_mfma_f32_16x16x16_bf16 v[252:255], v[252:253], v[64:65], 0\n"
"s_waitcnt lgkmcnt(4)\n"
"v_cvt_pk_bf16_f32 v221, v222, v223\n"
"v_mfma_f32_16x16x16_bf16 v[98:101], v[98:99], v[64:65], 0\n"
"ds_read_b64_tr_b16 a[250:251], v183 offset:40648\n"
"ds_read_b64_tr_b16 a[252:253], v182 offset:41416\n"
"v_mfma_f32_16x16x32_bf16 v[136:139], v[136:139], v[148:151], 0\n"
"ds_read_b64_tr_b16 a[254:255], v183 offset:41416\n"
"v_mfma_f32_16x16x32_bf16 v[136:139], v[80:83], v[160:163], v[136:139]\n"
"v_cvt_pk_bf16_f32 v252, v252, v253\n"
"v_cvt_pk_bf16_f32 v253, v254, v255\n"
"v_mfma_f32_16x16x16_bf16 v[106:109], v[106:107], v[64:65], 0\n"
"s_add_u32 m0, s34, 9216\n"
"v_cvt_pk_bf16_f32 v254, v98, v99\n"
"v_cvt_pk_bf16_f32 v255, v100, v101\n"
"v_mfma_f32_16x16x32_bf16 v[128:131], v[128:131], v[148:151], 0\n"
"buffer_load_dwordx4 v191, s[8:11], s32 offen lds\n"
"ds_read_b64 v[98:99], v180 offset:64\n"
"v_mfma_f32_16x16x32_bf16 v[128:131], v[84:87], v[162:165], v[128:131]\n"
"s_add_u32 s32, s32, s33\n"
"ds_read_b64 v[100:101], v181 offset:64\n"
"v_cvt_pk_bf16_f32 v106, v106, v107\n"
"v_mfma_f32_16x16x32_bf16 v[140:143], v[140:143], v[148:151], 0\n"
"v_cvt_pk_bf16_f32 v107, v108, v109\n"
"v_mfma_f32_16x16x32_bf16 v[140:143], v[88:91], v[160:163], v[140:143]\n"
"buffer_store_dwordx4 v[252:255], v192, s[28:31], s37 offen sc1\n"
"v_mfma_f32_16x16x32_bf16 v[132:135], v[132:135], v[148:151], 0\n"
"ds_read_b64 v[108:109], v181 offset:13568\n"
"ds_read_b64_tr_b16 v[68:69], v182 offset:40584\n"
"v_mfma_f32_16x16x32_bf16 v[132:135], v[92:95], v[162:165], v[132:135]\n"
"ds_read_b64_tr_b16 v[70:71], v183 offset:40584\n"
"v_max_f32 v136, v136, v128\n"
"v_mfma_f32_16x16x32_bf16 v[224:227], a[132:135], v[52:55], v[224:227]\n"
"ds_read_b64_tr_b16 v[72:73], v182 offset:41352\n"
"buffer_store_dwordx2 v[106:107], v193, s[28:31], s37 offen offset:1024 sc1\n"
"v_mfma_f32_16x16x32_bf16 v[208:211], a[76:79], v[32:35], 0\n"
"s_waitcnt lgkmcnt(5)\n"
"ds_read_b64 v[106:107], v180 offset:13568\n"
"v_max_f32 v137, v137, v129\n"
"v_mfma_f32_16x16x32_bf16 v[208:211], a[88:91], v[36:39], v[208:211]\n"
"s_waitcnt lgkmcnt(5)\n"
"ds_read_b64_tr_b16 v[128:129], v182 offset:13640\n"
"v_cvt_pk_bf16_f32 v224, v224, v225\n"
"v_mfma_f32_16x16x32_bf16 v[208:211], a[100:103], v[40:43], v[208:211]\n"
"s_add_u32 m0, s34, 7168\n"
"ds_read_b64_tr_b16 v[74:75], v183 offset:41352\n"
"v_cvt_pk_bf16_f32 v225, v226, v227\n"
"v_mfma_f32_16x16x32_bf16 v[208:211], a[112:115], v[44:47], v[208:211]\n"
"s_add_u32 s37, s37, 1572864\n"
"buffer_load_dwordx4 v190, s[8:11], s0 offen lds\n"
"v_max_f32 v138, v138, v130\n"
"v_mfma_f32_16x16x32_bf16 v[208:211], a[124:127], v[48:51], v[208:211]\n"
"s_waitcnt lgkmcnt(6)\n"
"v_max_f32 v139, v139, v131\n"
"v_max3_f32 v170, v136, v137, v138\n"
"v_mfma_f32_16x16x32_bf16 v[208:211], a[136:139], v[52:55], v[208:211]\n"
"ds_read_b64_tr_b16 v[130:131], v183 offset:13640\n"
"v_max_f32 v140, v140, v132\n"
"v_mfma_f32_16x16x32_bf16 v[112:115], v[112:115], v[148:151], 0\n"
"v_max_f32 v141, v141, v133\n"
"v_max_f32 v142, v142, v134\n"
"v_mfma_f32_16x16x32_bf16 v[112:115], v[96:99], v[160:163], v[112:115]\n"
"ds_read_b64_tr_b16 v[132:133], v182 offset:14408\n"
"v_max_f32 v143, v143, v135\n"
"v_mfma_f32_16x16x32_bf16 v[244:247], v[244:247], v[148:151], 0\n"
"s_waitcnt lgkmcnt(4)\n"
"ds_read_b64_tr_b16 v[134:135], v183 offset:14408\n"
"v_cvt_pk_bf16_f32 v226, v208, v209\n"
"v_mfma_f32_16x16x32_bf16 v[244:247], v[100:103], v[162:165], v[244:247]\n"
"v_cvt_pk_bf16_f32 v227, v210, v211\n"
"v_max3_f32 v170, v170, v139, v140\n"
"v_mfma_f32_16x16x32_bf16 v[116:119], v[116:119], v[148:151], 0\n"
"ds_write_b128 v187, v[224:227] offset:128\n"
"v_mfma_f32_16x16x32_bf16 v[116:119], v[104:107], v[160:163], v[116:119]\n"
"s_add_u32 m0, s34, 10240\n"
"v_max3_f32 v170, v170, v141, v142\n"
"v_mov_b64 v[252:253], 0\n"
"v_mfma_f32_16x16x32_bf16 v[248:251], v[248:251], v[148:151], 0\n"
"buffer_load_dwordx4 v191, s[8:11], s0 offen lds\n"
"v_max_f32 v112, v112, v244\n"
"v_mfma_f32_16x16x32_bf16 v[248:251], v[108:111], v[162:165], v[248:251]\n"
"s_add_u32 s0, s0, s33\n"
"v_max_f32 v113, v113, v245\n"
"v_max_f32 v114, v114, v246\n"
"v_mfma_f32_16x16x32_bf16 v[216:219], a[80:83], v[32:35], 0\n"
"ds_read_b64_tr_b16 v[244:245], v182 offset:13576\n"
"v_max_f32 v115, v115, v247\n"
"v_mfma_f32_16x16x32_bf16 v[216:219], a[92:95], v[36:39], v[216:219]\n"
"ds_read_b64_tr_b16 v[246:247], v183 offset:13576\n"
"v_max3_f32 v170, v170, v143, v112\n"
"v_mfma_f32_16x16x32_bf16 v[216:219], a[104:107], v[40:43], v[216:219]\n"
"v_max3_f32 v170, v170, v113, v114\n"
"v_max_f32 v116, v116, v248\n"
"v_mfma_f32_16x16x32_bf16 v[216:219], a[116:119], v[44:47], v[216:219]\n"
"v_max_f32 v117, v117, v249\n"
"v_max_f32 v118, v118, v250\n"
"v_mfma_f32_16x16x32_bf16 v[216:219], a[128:131], v[48:51], v[216:219]\n"
"ds_read_b64_tr_b16 v[248:249], v182 offset:14344\n"
"v_max_f32 v119, v119, v251\n"
"v_mfma_f32_16x16x32_bf16 v[216:219], a[140:143], v[52:55], v[216:219]\n"
"ds_read_b64_tr_b16 v[250:251], v183 offset:14344\n"
"v_max3_f32 v170, v170, v115, v116\n"
"v_mfma_f32_16x16x32_bf16 v[212:215], a[144:147], v[32:35], 0\n"
"v_max3_f32 v170, v170, v117, v118\n"
"v_mov_b64 v[254:255], 0\n"
"v_mfma_f32_16x16x32_bf16 v[212:215], a[156:159], v[36:39], v[212:215]\n"
"s_add_u32 m0, s34, 8192\n"
"v_mov_b64 v[98:99], 0\n"
"v_cvt_pk_bf16_f32 v222, v216, v217\n"
"v_mfma_f32_16x16x32_bf16 v[212:215], a[168:171], v[40:43], v[212:215]\n"
"buffer_load_dwordx4 v190, s[8:11], s1 offen lds\n"
"v_cvt_pk_bf16_f32 v223, v218, v219\n"
"v_mfma_f32_16x16x32_bf16 v[212:215], a[180:183], v[44:47], v[212:215]\n"
"ds_write_b128 v187, v[220:223] offset:64\n"
"v_mfma_f32_16x16x32_bf16 v[212:215], a[192:195], v[48:51], v[212:215]\n"
"v_mov_b64 v[100:101], 0\n"
"v_mov_b64 v[106:107], 0\n"
"v_mfma_f32_16x16x32_bf16 v[212:215], a[204:207], v[52:55], v[212:215]\n"
"v_mov_b64 v[108:109], 0\n"
"v_max_f32 v171, v170, v119\n"
"v_mfma_f32_16x16x32_bf16 v[204:207], a[148:151], v[32:35], 0\n"
"v_max_f32 v170, v170, v119\n"
"v_mfma_f32_16x16x32_bf16 v[204:207], a[160:163], v[36:39], v[204:207]\n"
"s_nop 0\n"
"v_permlane16_swap_b32 v170, v171\n"
"v_mfma_f32_16x16x32_bf16 v[204:207], a[172:175], v[40:43], v[204:207]\n"
"v_max_f32 v170, v170, v171\n"
"v_mov_b32 v171, v170\n"
"v_mfma_f32_16x16x32_bf16 v[204:207], a[184:187], v[44:47], v[204:207]\n"
"s_nop 0\n"
"v_permlane32_swap_b32 v170, v171\n"
"v_max_f32 v170, v170, v171\n"
"v_mul_f32 v170, 0xbe553b95, v170\n"
"v_pk_fma_f32 v[136:137], v[136:137], s[70:71], v[170:171] op_sel_hi:[1,0,0]\n"
"v_pk_fma_f32 v[138:139], v[138:139], s[70:71], v[170:171] op_sel_hi:[1,0,0]\n"
"v_pk_fma_f32 v[140:141], v[140:141], s[70:71], v[170:171] op_sel_hi:[1,0,0]\n"
"v_pk_fma_f32 v[142:143], v[142:143], s[70:71], v[170:171] op_sel_hi:[1,0,0]\n"
"v_pk_fma_f32 v[112:113], v[112:113], s[70:71], v[170:171] op_sel_hi:[1,0,0]\n"
"v_pk_fma_f32 v[114:115], v[114:115], s[70:71], v[170:171] op_sel_hi:[1,0,0]\n"
"v_pk_fma_f32 v[116:117], v[116:117], s[70:71], v[170:171] op_sel_hi:[1,0,0]\n"
"v_pk_fma_f32 v[118:119], v[118:119], s[70:71], v[170:171] op_sel_hi:[1,0,0]\n"
"v_mfma_f32_16x16x32_bf16 v[204:207], a[196:199], v[48:51], v[204:207]\n"
"s_waitcnt vmcnt(7) lgkmcnt(0)\n"
"ds_read_b64 v[80:81], v20 offset:272\n"
"ds_read_b64 v[86:87], v20 offset:336\n"
"v_mfma_f32_16x16x32_bf16 v[204:207], a[208:211], v[52:55], v[204:207]\n"
"s_add_u32 m0, s34, 11264\n"
"ds_read_b64 v[104:105], v20 offset:1088\n"
"buffer_load_dwordx4 v191, s[8:11], s1 offen lds\n"
"v_mfma_f32_16x16x32_bf16 v[228:231], a[152:155], v[32:35], 0\n"
"s_add_u32 s1, s1, s33\n"
"ds_read_b128 v[32:35], v188\n"
"ds_read_b64 v[110:111], v20 offset:1152\n"
"v_mfma_f32_16x16x32_bf16 v[228:231], a[164:167], v[36:39], v[228:231]\n"
"s_barrier\n"
"v_mfma_f32_16x16x32_bf16 v[228:231], a[176:179], v[40:43], v[228:231]\n"
"ds_read_b128 v[36:39], v189\n"
"buffer_load_dword v201, off, s[28:31], s40 offset:128 sc1\n"
"v_mfma_f32_16x16x32_bf16 v[228:231], a[188:191], v[44:47], v[228:231]\n"
"ds_read_b128 v[40:43], v188 offset:1024\n"
"ds_read_b128 v[44:47], v189 offset:1024\n"
"v_mfma_f32_16x16x32_bf16 v[228:231], a[200:203], v[48:51], v[228:231]\n"
"ds_read_b128 v[48:51], v188 offset:2048\n"
"v_cvt_pk_bf16_f32 v148, v212, v213\n"
"v_mfma_f32_16x16x32_bf16 v[228:231], a[212:215], v[52:55], v[228:231]\n"
"s_waitcnt lgkmcnt(8)\n"
"ds_read_b128 v[52:55], v189 offset:2048\n"
"v_exp_f32 v136, v136\n"
"v_exp_f32 v137, v137\n"
"v_exp_f32 v138, v138\n"
"v_exp_f32 v139, v139\n"
".Lic1%=:\n"
"s_cbranch_execz .Lib0%=\n"
"v_cvt_pk_bf16_f32 v82, v136, v138\n"
"v_cvt_pk_bf16_f32 v83, v137, v139\n"
"v_cvt_pk_bf16_f32 v149, v214, v215\n"
"v_exp_f32 v140, v140\n"
"v_exp_f32 v141, v141\n"
"v_exp_f32 v142, v142\n"
"v_cvt_pk_bf16_f32 v150, v204, v205\n"
"v_exp_f32 v143, v143\n"
"v_cvt_pk_bf16_f32 v151, v206, v207\n"
"v_cvt_pk_bf16_f32 v84, v140, v142\n"
"v_cvt_pk_bf16_f32 v85, v141, v143\n"
"v_cvt_pk_bf16_f32 v162, v228, v229\n"
"v_cvt_pk_bf16_f32 v163, v230, v231\n"
"v_smfmac_f32_16x16x64_bf16 v[252:255], v[82:85], v[120:127], v66\n"
"s_waitcnt lgkmcnt(2)\n"
"ds_read_b128 v[136:139], v178 offset:27008\n"
"v_mfma_f32_16x16x32_bf16 v[208:211], a[0:3], v[32:35], 0\n"
"s_add_u32 m0, s79, 142464\n"
"ds_read_b128 v[120:123], v179 offset:27008\n"
"buffer_load_dwordx4 v192, s[16:19], s43 offen lds\n"
"v_mfma_f32_16x16x32_bf16 v[208:211], a[12:15], v[36:39], v[208:211]\n"
"ds_read_b128 v[140:143], v180\n"
"ds_read_b128 v[124:127], v181\n"
"v_mfma_f32_16x16x32_bf16 v[208:211], a[24:27], v[40:43], v[208:211]\n"
"v_exp_f32 v112, v112\n"
"v_mfma_f32_16x16x32_bf16 v[208:211], a[36:39], v[44:47], v[208:211]\n"
"s_waitcnt lgkmcnt(5)\n"
"ds_read_b64_tr_b16 a[216:217], v236\n"
"v_exp_f32 v113, v113\n"
"v_mfma_f32_16x16x32_bf16 v[208:211], a[48:51], v[48:51], v[208:211]\n"
"s_waitcnt lgkmcnt(5)\n"
"v_exp_f32 v114, v114\n"
"v_mfma_f32_16x16x32_bf16 v[208:211], a[60:63], v[52:55], v[208:211]\n"
"v_exp_f32 v115, v115\n"
"v_smfmac_f32_16x16x64_bf16 v[98:101], v[82:85], v[128:135], v66\n"
"ds_read_b128 v[128:131], v180 offset:13504\n"
"v_smfmac_f32_16x16x64_bf16 v[106:109], v[82:85], v[244:251], v66\n"
"ds_read_b128 v[244:247], v181 offset:13504\n"
"v_mfma_f32_16x16x32_bf16 v[82:85], v[172:175], v[82:85], 0\n"
"ds_read_b128 v[132:135], v180 offset:27008\n"
"ds_read_b128 v[248:251], v181 offset:27008\n"
"v_mfma_f32_16x16x32_bf16 v[216:219], a[4:7], v[32:35], 0\n"
"s_add_u32 m0, s79, 142464\n"
"v_cvt_pk_bf16_f32 v90, v112, v114\n"
"v_cvt_pk_bf16_f32 v91, v113, v115\n"
"v_mfma_f32_16x16x32_bf16 v[216:219], a[16:19], v[36:39], v[216:219]\n"
"buffer_load_dwordx4 v192, s[16:19], s43 offen offset:1024 lds\n"
"v_exp_f32 v116, v116\n"
"v_mfma_f32_16x16x32_bf16 v[216:219], a[28:31], v[40:43], v[216:219]\n"
"v_exp_f32 v117, v117\n"
"v_mfma_f32_16x16x32_bf16 v[216:219], a[40:43], v[44:47], v[216:219]\n"
"v_exp_f32 v118, v118\n"
"v_mfma_f32_16x16x32_bf16 v[216:219], a[52:55], v[48:51], v[216:219]\n"
"v_exp_f32 v119, v119\n"
"v_mfma_f32_16x16x32_bf16 v[216:219], a[64:67], v[52:55], v[216:219]\n"
"v_cvt_pk_bf16_f32 v92, v116, v118\n"
"v_cvt_pk_bf16_f32 v93, v117, v119\n"
"v_mfma_f32_16x16x32_bf16 v[220:223], a[8:11], v[32:35], 0\n"
"ds_read_b64_tr_b16 v[112:113], v182 offset:27136\n"
"ds_read_b64_tr_b16 v[114:115], v183 offset:27136\n"
"v_mfma_f32_16x16x32_bf16 v[220:223], a[20:23], v[36:39], v[220:223]\n"
"ds_read_b64_tr_b16 v[116:117], v182 offset:27904\n"
"v_cvt_pk_bf16_f32 v208, v208, v209\n"
"v_mfma_f32_16x16x32_bf16 v[220:223], a[32:35], v[40:43], v[220:223]\n"
"ds_read_b64_tr_b16 v[118:119], v183 offset:27904\n"
"v_cvt_pk_bf16_f32 v209, v210, v211\n"
"v_mfma_f32_16x16x32_bf16 v[220:223], a[44:47], v[44:47], v[220:223]\n"
"ds_read_b64_tr_b16 a[218:219], v236 offset:6144\n"
"v_cvt_pk_bf16_f32 v210, v216, v217\n"
"v_mfma_f32_16x16x32_bf16 v[220:223], a[56:59], v[48:51], v[220:223]\n"
"s_add_u32 m0, s79, 142464\n"
"v_cvt_pk_bf16_f32 v211, v218, v219\n"
"v_bfi_b32 v80, v12, v80, v26\n"
"v_mfma_f32_16x16x32_bf16 v[220:223], a[68:71], v[52:55], v[220:223]\n"
"buffer_load_dwordx4 v192, s[16:19], s43 offen offset:2048 lds\n"
"ds_write_b128 v186, v[208:211] offset:6752\n"
"v_smfmac_f32_16x16x64_bf16 v[252:255], v[90:93], a[240:247], v66\n"
"s_add_u32 s43, s79, 36864\n"
"v_bfi_b32 v81, v13, v81, v26\n"
"v_smfmac_f32_16x16x64_bf16 v[98:101], v[90:93], a[248:255], v66\n"
"buffer_load_dword v237, v67, s[8:11], s78 offen\n"
"v_smfmac_f32_16x16x64_bf16 v[106:109], v[90:93], v[68:75], v66\n"
"s_add_u32 s78, s78, s33\n"
"v_cvt_pk_bf16_f32 v220, v220, v221\n"
"v_mfma_f32_16x16x32_bf16 v[90:93], v[172:175], v[90:93], v[82:85]\n"
"v_cvt_pk_bf16_f32 v252, v252, v253\n"
"v_cvt_pk_bf16_f32 v253, v254, v255\n"
"v_mfma_f32_16x16x32_bf16 v[224:227], a[72:75], v[32:35], 0\n"
"ds_read_b64 v[82:83], v178 offset:27072\n"
"ds_read_b64 v[84:85], v179 offset:27072\n"
"v_mfma_f32_16x16x32_bf16 v[224:227], a[84:87], v[36:39], v[224:227]\n"
"v_cvt_pk_bf16_f32 v98, v98, v99\n"
"v_cvt_pk_bf16_f32 v99, v100, v101\n"
"v_mfma_f32_16x16x32_bf16 v[224:227], a[96:99], v[40:43], v[224:227]\n"
"ds_read_b64 v[92:93], v181 offset:64\n"
"v_cvt_pk_bf16_f32 v106, v106, v107\n"
"v_mfma_f32_16x16x32_bf16 v[224:227], a[108:111], v[44:47], v[224:227]\n"
"s_add_u32 m0, s34, 0\n"
"v_rcp_f32 v171, v90\n"
"v_mfma_f32_16x16x32_bf16 v[224:227], a[120:123], v[48:51], v[224:227]\n"
"buffer_load_dwordx4 v190, s[8:11], s32 offen lds\n"
"ds_read_b64 v[90:91], v180 offset:64\n"
"v_mfma_f32_16x16x32_bf16 v[224:227], a[132:135], v[52:55], v[224:227]\n"
"v_cvt_pk_bf16_f32 v171, v171, v171\n"
"v_and_b32 v64, v171, v176\n"
"v_mfma_f32_16x16x32_bf16 v[212:215], a[76:79], v[32:35], 0\n"
"v_and_b32 v65, v171, v177\n"
"v_cvt_pk_bf16_f32 v107, v108, v109\n"
"v_mfma_f32_16x16x32_bf16 v[212:215], a[88:91], v[36:39], v[212:215]\n"
"v_cvt_pk_bf16_f32 v221, v222, v223\n"
"v_bfi_b32 v86, v14, v86, v26\n"
"v_mfma_f32_16x16x16_bf16 v[252:255], v[252:253], v[64:65], 0\n"
"s_waitcnt lgkmcnt(3)\n"
"v_bfi_b32 v87, v15, v87, v26\n"
"v_bfi_b32 v104, v16, v104, v26\n"
"v_mfma_f32_16x16x32_bf16 v[136:139], v[136:139], v[152:155], 0\n"
"s_waitcnt lgkmcnt(2)\n"
"v_bfi_b32 v105, v17, v105, v26\n"
"v_bfi_b32 v110, v18, v110, v26\n"
"v_mfma_f32_16x16x32_bf16 v[136:139], v[80:83], v[164:167], v[136:139]\n"
"s_waitcnt lgkmcnt(1)\n"
"v_cvt_pk_bf16_f32 v252, v252, v253\n"
"v_cvt_pk_bf16_f32 v253, v254, v255\n"
"v_mfma_f32_16x16x32_bf16 v[120:123], v[120:123], v[152:155], 0\n"
"v_bfi_b32 v111, v19, v111, v26\n"
"v_cvt_pk_bf16_f32 v224, v224, v225\n"
"v_mfma_f32_16x16x32_bf16 v[120:123], v[84:87], v[166:169], v[120:123]\n"
"s_waitcnt lgkmcnt(0)\n"
"ds_read_b64_tr_b16 v[8:9], v182 offset:54144\n"
"ds_read_b64_tr_b16 v[10:11], v183 offset:54144\n"
"v_mfma_f32_16x16x16_bf16 v[98:101], v[98:99], v[64:65], 0\n"
"s_add_u32 m0, s34, 3072\n"
"ds_read_b64_tr_b16 v[12:13], v182 offset:54912\n"
"buffer_load_dwordx4 v191, s[8:11], s32 offen lds\n"
"v_mfma_f32_16x16x16_bf16 v[106:109], v[106:107], v[64:65], 0\n"
"s_add_u32 s32, s32, s33\n"
"ds_read_b64_tr_b16 v[14:15], v183 offset:54912\n"
"v_max_f32 v136, v136, v120\n"
"v_mfma_f32_16x16x32_bf16 v[140:143], v[140:143], v[152:155], 0\n"
"v_cvt_pk_bf16_f32 v254, v98, v99\n"
"v_cvt_pk_bf16_f32 v255, v100, v101\n"
"v_mfma_f32_16x16x32_bf16 v[140:143], v[88:91], v[164:167], v[140:143]\n"
"ds_read_b64 v[98:99], v180 offset:13568\n"
"buffer_store_dwordx4 v[252:255], v192, s[28:31], s37 offen sc1\n"
"v_mfma_f32_16x16x32_bf16 v[124:127], v[124:127], v[152:155], 0\n"
"ds_read_b64 v[100:101], v181 offset:13568\n"
"v_cvt_pk_bf16_f32 v106, v106, v107\n"
"v_mfma_f32_16x16x32_bf16 v[124:127], v[92:95], v[166:169], v[124:127]\n"
"v_cvt_pk_bf16_f32 v107, v108, v109\n"
"v_max_f32 v137, v137, v121\n"
"v_mfma_f32_16x16x32_bf16 v[212:215], a[100:103], v[40:43], v[212:215]\n"
"ds_read_b64 v[108:109], v181 offset:27072\n"
"buffer_store_dwordx2 v[106:107], v193, s[28:31], s37 offen offset:1024 sc1\n"
"v_mfma_f32_16x16x32_bf16 v[212:215], a[112:115], v[44:47], v[212:215]\n"
"s_add_u32 s37, s37, 1572864\n"
"ds_read_b64 v[106:107], v180 offset:27072\n"
"ds_read_b64_tr_b16 v[120:121], v182 offset:27144\n"
"v_mfma_f32_16x16x32_bf16 v[212:215], a[124:127], v[48:51], v[212:215]\n"
"ds_read_b64_tr_b16 v[16:17], v182 offset:54152\n"
"v_cvt_pk_bf16_f32 v225, v226, v227\n"
"v_mfma_f32_16x16x32_bf16 v[212:215], a[136:139], v[52:55], v[212:215]\n"
"s_waitcnt lgkmcnt(5)\n"
"ds_read_b64_tr_b16 v[18:19], v183 offset:54152\n"
"v_max_f32 v138, v138, v122\n"
"v_mfma_f32_16x16x32_bf16 v[128:131], v[128:131], v[152:155], 0\n"
"s_add_u32 m0, s34, 1024\n"
"ds_read_b64_tr_b16 v[20:21], v182 offset:54920\n"
"buffer_load_dwordx4 v190, s[8:11], s0 offen lds\n"
"v_mfma_f32_16x16x32_bf16 v[128:131], v[96:99], v[164:167], v[128:131]\n"
"s_waitcnt lgkmcnt(6)\n"
"ds_read_b64_tr_b16 v[22:23], v183 offset:54920\n"
"v_cvt_pk_bf16_f32 v226, v212, v213\n"
"v_mfma_f32_16x16x32_bf16 v[244:247], v[244:247], v[152:155], 0\n"
"ds_read_b64_tr_b16 v[24:25], v182 offset:54088\n"
"v_max_f32 v139, v139, v123\n"
"v_mfma_f32_16x16x32_bf16 v[244:247], v[100:103], v[166:169], v[244:247]\n"
"s_waitcnt lgkmcnt(7)\n"
"ds_read_b64_tr_b16 v[122:123], v183 offset:27144\n"
"v_max3_f32 v170, v136, v137, v138\n"
"v_mfma_f32_16x16x32_bf16 v[248:251], v[248:251], v[152:155], 0\n"
"ds_read_b64_tr_b16 v[26:27], v183 offset:54088\n"
"v_cvt_pk_bf16_f32 v227, v214, v215\n"
"v_mfma_f32_16x16x32_bf16 v[248:251], v[108:111], v[166:169], v[248:251]\n"
"s_waitcnt lgkmcnt(8)\n"
"ds_write_b128 v186, v[224:227] offset:6880\n"
"v_mfma_f32_16x16x32_bf16 v[132:135], v[132:135], v[152:155], 0\n"
"v_max_f32 v140, v140, v124\n"
"v_max_f32 v141, v141, v125\n"
"v_mfma_f32_16x16x32_bf16 v[132:135], v[104:107], v[164:167], v[132:135]\n"
"ds_read_b64_tr_b16 v[124:125], v182 offset:27912\n"
"ds_read_b64_tr_b16 a[220:221], v236 offset:8\n"
"v_mfma_f32_16x16x32_bf16 v[204:207], a[80:83], v[32:35], 0\n"
"s_add_u32 m0, s34, 4096\n"
"v_max_f32 v142, v142, v126\n"
"v_max_f32 v143, v143, v127\n"
"v_mfma_f32_16x16x32_bf16 v[204:207], a[92:95], v[36:39], v[204:207]\n"
"buffer_load_dwordx4 v191, s[8:11], s0 offen lds\n"
"ds_read_b64_tr_b16 v[126:127], v183 offset:27912\n"
"v_mfma_f32_16x16x32_bf16 v[204:207], a[104:107], v[40:43], v[204:207]\n"
"s_add_u32 s0, s0, s33\n"
"v_max3_f32 v170, v170, v139, v140\n"
"v_max3_f32 v170, v170, v141, v142\n"
"v_mfma_f32_16x16x32_bf16 v[204:207], a[116:119], v[44:47], v[204:207]\n"
"ds_read_b64_tr_b16 v[28:29], v182 offset:54856\n"
"ds_read_b64_tr_b16 v[30:31], v183 offset:54856\n"
"v_mfma_f32_16x16x32_bf16 v[204:207], a[128:131], v[48:51], v[204:207]\n"
"v_max_f32 v128, v128, v244\n"
"v_max_f32 v129, v129, v245\n"
"v_mfma_f32_16x16x32_bf16 v[204:207], a[140:143], v[52:55], v[204:207]\n"
"ds_read_b64_tr_b16 v[244:245], v182 offset:27080\n"
"ds_read_b64_tr_b16 a[222:223], v236 offset:6152\n"
"v_mfma_f32_16x16x32_bf16 v[228:231], a[144:147], v[32:35], 0\n"
"v_max_f32 v130, v130, v246\n"
"v_max_f32 v131, v131, v247\n"
"v_mfma_f32_16x16x32_bf16 v[228:231], a[156:159], v[36:39], v[228:231]\n"
"v_max3_f32 v170, v170, v143, v128\n"
"v_max3_f32 v170, v170, v129, v130\n"
"v_mfma_f32_16x16x32_bf16 v[228:231], a[168:171], v[40:43], v[228:231]\n"
"ds_read_b64_tr_b16 v[246:247], v183 offset:27080\n"
"ds_read_b64_tr_b16 a[224:225], v235\n"
"v_mfma_f32_16x16x32_bf16 v[228:231], a[180:183], v[44:47], v[228:231]\n"
"v_cvt_pk_bf16_f32 v222, v204, v205\n"
"v_cvt_pk_bf16_f32 v223, v206, v207\n"
"v_mfma_f32_16x16x32_bf16 v[228:231], a[192:195], v[48:51], v[228:231]\n"
"s_add_u32 m0, s34, 2048\n"
"v_max_f32 v132, v132, v248\n"
"v_max_f32 v133, v133, v249\n"
"v_mfma_f32_16x16x32_bf16 v[228:231], a[204:207], v[52:55], v[228:231]\n"
"buffer_load_dwordx4 v190, s[8:11], s1 offen lds\n"
"ds_write_b128 v186, v[220:223] offset:6816\n"
"v_mfma_f32_16x16x32_bf16 v[216:219], a[148:151], v[32:35], 0\n"
"v_max_f32 v134, v134, v250\n"
"v_max_f32 v135, v135, v251\n"
"v_mfma_f32_16x16x32_bf16 v[216:219], a[160:163], v[36:39], v[216:219]\n"
"ds_read_b64_tr_b16 v[248:249], v182 offset:27848\n"
"ds_read_b64_tr_b16 v[250:251], v183 offset:27848\n"
"v_mfma_f32_16x16x32_bf16 v[216:219], a[172:175], v[40:43], v[216:219]\n"
"ds_read_b64_tr_b16 a[226:227], v235 offset:6144\n"
"v_max3_f32 v170, v170, v131, v132\n"
"v_mfma_f32_16x16x32_bf16 v[216:219], a[184:187], v[44:47], v[216:219]\n"
"v_max3_f32 v170, v170, v133, v134\n"
"v_mov_b64 v[252:253], 0\n"
"v_mfma_f32_16x16x32_bf16 v[216:219], a[196:199], v[48:51], v[216:219]\n"
"v_mov_b64 v[254:255], 0\n"
"v_mov_b64 v[98:99], 0\n"
"v_mfma_f32_16x16x32_bf16 v[216:219], a[208:211], v[52:55], v[216:219]\n"
"v_mov_b64 v[100:101], 0\n"
"v_mov_b64 v[106:107], 0\n"
"v_mfma_f32_16x16x32_bf16 v[208:211], a[152:155], v[32:35], 0\n"
"s_waitcnt vmcnt(18)\n"
"ds_read_b128 v[32:35], v188 offset:6144\n"
"v_mov_b64 v[108:109], 0\n"
"v_mfma_f32_16x16x32_bf16 v[208:211], a[164:167], v[36:39], v[208:211]\n"
"s_add_u32 m0, s34, 5120\n"
"ds_read_b128 v[36:39], v189 offset:6144\n"
"buffer_load_dwordx4 v191, s[8:11], s1 offen lds\n"
"v_mfma_f32_16x16x32_bf16 v[208:211], a[176:179], v[40:43], v[208:211]\n"
"s_waitcnt vmcnt(15)\n"
"ds_read_b128 v[40:43], v188 offset:7168\n"
"v_max_f32 v171, v170, v135\n"
"v_mfma_f32_16x16x32_bf16 v[208:211], a[188:191], v[44:47], v[208:211]\n"
"s_add_u32 s1, s1, s33\n"
"ds_read_b128 v[44:47], v189 offset:7168\n"
"v_max_f32 v170, v170, v135\n"
"v_mfma_f32_16x16x32_bf16 v[208:211], a[200:203], v[48:51], v[208:211]\n"
"s_nop 0\n"
"v_permlane16_swap_b32 v170, v171\n"
"v_mfma_f32_16x16x32_bf16 v[208:211], a[212:215], v[52:55], v[208:211]\n"
"v_max_f32 v170, v170, v171\n"
"v_mov_b32 v171, v170\n"
"v_cvt_pk_bf16_f32 v152, v228, v229\n"
"v_cvt_pk_bf16_f32 v153, v230, v231\n"
"v_permlane32_swap_b32 v170, v171\n"
"v_max_f32 v170, v170, v171\n"
"v_mul_f32 v170, 0xbe553b95, v170\n"
"v_pk_fma_f32 v[136:137], v[136:137], s[70:71], v[170:171] op_sel_hi:[1,0,0]\n"
"v_pk_fma_f32 v[138:139], v[138:139], s[70:71], v[170:171] op_sel_hi:[1,0,0]\n"
"v_pk_fma_f32 v[140:141], v[140:141], s[70:71], v[170:171] op_sel_hi:[1,0,0]\n"
"v_pk_fma_f32 v[142:143], v[142:143], s[70:71], v[170:171] op_sel_hi:[1,0,0]\n"
"v_pk_fma_f32 v[128:129], v[128:129], s[70:71], v[170:171] op_sel_hi:[1,0,0]\n"
"v_pk_fma_f32 v[130:131], v[130:131], s[70:71], v[170:171] op_sel_hi:[1,0,0]\n"
"v_pk_fma_f32 v[132:133], v[132:133], s[70:71], v[170:171] op_sel_hi:[1,0,0]\n"
"v_pk_fma_f32 v[134:135], v[134:135], s[70:71], v[170:171] op_sel_hi:[1,0,0]\n"
"v_exp_f32 v136, v136\n"
"v_exp_f32 v137, v137\n"
"v_exp_f32 v138, v138\n"
"v_exp_f32 v139, v139\n"
"v_cvt_pk_bf16_f32 v82, v136, v138\n"
"v_cvt_pk_bf16_f32 v83, v137, v139\n"
"v_exp_f32 v140, v140\n"
"v_exp_f32 v141, v141\n"
"v_exp_f32 v142, v142\n"
"v_exp_f32 v143, v143\n"
"v_cvt_pk_bf16_f32 v84, v140, v142\n"
"v_cvt_pk_bf16_f32 v85, v141, v143\n"
"v_cvt_pk_bf16_f32 v154, v216, v217\n"
"v_cvt_pk_bf16_f32 v155, v218, v219\n"
"v_smfmac_f32_16x16x64_bf16 v[252:255], v[82:85], v[112:119], v66\n"
"s_waitcnt vmcnt(9) lgkmcnt(0)\n"
"v_readfirstlane_b32 s6, v201\n"
"v_mfma_f32_16x16x32_bf16 v[212:215], a[0:3], v[32:35], 0\n"
"s_nop 0\n"
"s_and_b32 s6, s6, 0xffffff00\n"
"ds_read_b128 v[48:51], v188 offset:8192\n"
"ds_read_b128 v[52:55], v189 offset:8192\n"
"v_mfma_f32_16x16x32_bf16 v[212:215], a[12:15], v[36:39], v[212:215]\n"
"s_add_u32 m0, s34, 6144\n"
"ds_read_b128 v[136:139], v180\n"
"buffer_load_dwordx4 v190, s[8:11], s32 offen lds\n"
"v_mfma_f32_16x16x32_bf16 v[212:215], a[24:27], v[40:43], v[212:215]\n"
"s_barrier\n"
"v_mfma_f32_16x16x32_bf16 v[212:215], a[36:39], v[44:47], v[212:215]\n"
"ds_read_b128 v[112:115], v181\n"
"ds_read_b128 v[140:143], v180 offset:13504\n"
"v_smfmac_f32_16x16x64_bf16 v[98:101], v[82:85], v[120:127], v66\n"
"ds_read_b128 v[116:119], v181 offset:13504\n"
"v_smfmac_f32_16x16x64_bf16 v[106:109], v[82:85], v[244:251], v66\n"
"ds_read_b128 v[120:123], v180 offset:27008\n"
"v_mfma_f32_16x16x32_bf16 v[82:85], v[172:175], v[82:85], 0\n"
"ds_read_b128 v[244:247], v181 offset:27008\n"
"v_cvt_pk_bf16_f32 v166, v208, v209\n"
"v_mfma_f32_16x16x32_bf16 v[204:207], a[4:7], v[32:35], 0\n"
"ds_read_b128 v[124:127], v178\n"
"v_cvt_pk_bf16_f32 v167, v210, v211\n"
"v_mfma_f32_16x16x32_bf16 v[204:207], a[16:19], v[36:39], v[204:207]\n"
"s_waitcnt lgkmcnt(8)\n"
"v_exp_f32 v128, v128\n"
"v_mfma_f32_16x16x32_bf16 v[204:207], a[28:31], v[40:43], v[204:207]\n"
"ds_read_b64_tr_b16 a[228:229], v236 offset:12288\n"
"v_exp_f32 v129, v129\n"
"v_mfma_f32_16x16x32_bf16 v[204:207], a[40:43], v[44:47], v[204:207]\n"
"s_waitcnt lgkmcnt(8)\n"
"v_exp_f32 v130, v130\n"
"v_mfma_f32_16x16x32_bf16 v[204:207], a[52:55], v[48:51], v[204:207]\n"
"s_add_u32 m0, s79, 130176\n"
"v_exp_f32 v131, v131\n"
"v_mfma_f32_16x16x32_bf16 v[204:207], a[64:67], v[52:55], v[204:207]\n"
"buffer_load_dwordx4 v192, s[16:19], s43 offen lds\n"
"v_cvt_pk_bf16_f32 v90, v128, v130\n"
"v_mfma_f32_16x16x32_bf16 v[212:215], a[48:51], v[48:51], v[212:215]\n"
"v_cvt_pk_bf16_f32 v91, v129, v131\n"
"v_exp_f32 v132, v132\n"
"v_mfma_f32_16x16x32_bf16 v[212:215], a[60:63], v[52:55], v[212:215]\n"
"v_exp_f32 v133, v133\n"
"v_mfma_f32_16x16x32_bf16 v[220:223], a[8:11], v[32:35], 0\n"
"v_exp_f32 v134, v134\n"
"v_mfma_f32_16x16x32_bf16 v[220:223], a[20:23], v[36:39], v[220:223]\n"
"v_exp_f32 v135, v135\n"
"v_mfma_f32_16x16x32_bf16 v[220:223], a[32:35], v[40:43], v[220:223]\n"
"v_cvt_pk_bf16_f32 v92, v132, v134\n"
"v_cvt_pk_bf16_f32 v93, v133, v135\n"
"v_mfma_f32_16x16x32_bf16 v[220:223], a[44:47], v[44:47], v[220:223]\n"
"ds_read_b64_tr_b16 a[230:231], v236 offset:18432\n"
"v_cvt_pk_bf16_f32 v212, v212, v213\n"
"v_mfma_f32_16x16x32_bf16 v[220:223], a[56:59], v[48:51], v[220:223]\n"
"v_cvt_pk_bf16_f32 v213, v214, v215\n"
"v_cvt_pk_bf16_f32 v214, v204, v205\n"
"v_mfma_f32_16x16x32_bf16 v[220:223], a[68:71], v[52:55], v[220:223]\n"
"v_cvt_pk_bf16_f32 v215, v206, v207\n"
"s_mov_b64 exec, s[44:45]\n"
"v_add_u32 v179, 0xc0, v179\n"
"s_mov_b64 exec, -1\n"
"v_smfmac_f32_16x16x64_bf16 v[252:255], v[90:93], v[8:15], v66\n"
"s_add_u32 m0, s79, 130176\n"
"s_nop 0\n"
"buffer_load_dwordx4 v192, s[16:19], s43 offen offset:1024 lds\n"
"v_smfmac_f32_16x16x64_bf16 v[98:101], v[90:93], v[16:23], v66\n"
"ds_read_b128 v[248:251], v179\n"
"v_smfmac_f32_16x16x64_bf16 v[106:109], v[90:93], v[24:31], v66\n"
"ds_write_b128 v186, v[212:215] offset:20256\n"
"v_mfma_f32_16x16x32_bf16 v[90:93], v[172:175], v[90:93], v[82:85]\n"
"v_cvt_pk_bf16_f32 v220, v220, v221\n"
"v_cvt_pk_bf16_f32 v252, v252, v253\n"
"v_mfma_f32_16x16x32_bf16 v[224:227], a[72:75], v[32:35], 0\n"
"ds_read_b64 v[82:83], v180 offset:64\n"
"ds_read_b64 v[84:85], v181 offset:64\n"
"v_mfma_f32_16x16x32_bf16 v[224:227], a[84:87], v[36:39], v[224:227]\n"
"v_cvt_pk_bf16_f32 v253, v254, v255\n"
"v_cvt_pk_bf16_f32 v98, v98, v99\n"
"v_mfma_f32_16x16x32_bf16 v[224:227], a[96:99], v[40:43], v[224:227]\n"
"ds_read_b64 v[92:93], v181 offset:13568\n"
"v_cvt_pk_bf16_f32 v99, v100, v101\n"
"v_mfma_f32_16x16x32_bf16 v[224:227], a[108:111], v[44:47], v[224:227]\n"
"v_rcp_f32 v171, v90\n"
"v_mfma_f32_16x16x32_bf16 v[224:227], a[120:123], v[48:51], v[224:227]\n"
"s_add_u32 m0, s79, 130176\n"
"ds_read_b64 v[90:91], v180 offset:13568\n"
"buffer_load_dwordx4 v192, s[16:19], s43 offen offset:2048 lds\n"
"v_mfma_f32_16x16x32_bf16 v[224:227], a[132:135], v[52:55], v[224:227]\n"
"s_add_u32 s43, s79, 55296\n"
"v_cvt_pk_bf16_f32 v171, v171, v171\n"
"v_and_b32 v64, v171, v176\n"
"v_mfma_f32_16x16x32_bf16 v[228:231], a[76:79], v[32:35], 0\n"
"v_and_b32 v65, v171, v177\n"
"v_cvt_pk_bf16_f32 v106, v106, v107\n"
"v_mfma_f32_16x16x32_bf16 v[228:231], a[88:91], v[36:39], v[228:231]\n"
"v_cvt_pk_bf16_f32 v107, v108, v109\n"
"v_cvt_pk_bf16_f32 v221, v222, v223\n"
"v_mfma_f32_16x16x16_bf16 v[252:255], v[252:253], v[64:65], 0\n"
"s_waitcnt lgkmcnt(3)\n"
"ds_read_b64_tr_b16 v[128:129], v184 offset:128\n"
"ds_read_b64_tr_b16 v[130:131], v185 offset:128\n"
"v_mfma_f32_16x16x32_bf16 v[136:139], v[136:139], v[144:147], 0\n"
"s_waitcnt lgkmcnt(4)\n"
"ds_read_b64_tr_b16 v[132:133], v184 offset:896\n"
"buffer_load_dword v237, v67, s[8:11], s78 offen\n"
"v_mfma_f32_16x16x32_bf16 v[136:139], v[80:83], v[156:159], v[136:139]\n"
"s_waitcnt lgkmcnt(4)\n"
"v_cvt_pk_bf16_f32 v252, v252, v253\n"
"v_cvt_pk_bf16_f32 v253, v254, v255\n"
"v_mfma_f32_16x16x32_bf16 v[112:115], v[112:115], v[144:147], 0\n"
"s_add_u32 s78, s78, s33\n"
"ds_read_b64_tr_b16 v[134:135], v185 offset:896\n"
"ds_read_b64_tr_b16 a[232:233], v236 offset:12296\n"
"v_mfma_f32_16x16x32_bf16 v[112:115], v[84:87], v[158:161], v[112:115]\n"
"s_waitcnt lgkmcnt(5)\n"
"v_cvt_pk_bf16_f32 v224, v224, v225\n"
"v_cvt_pk_bf16_f32 v225, v226, v227\n"
"v_mfma_f32_16x16x32_bf16 v[140:143], v[140:143], v[144:147], 0\n"
"s_add_u32 m0, s34, 9216\n"
"ds_read_b64_tr_b16 a[234:235], v236 offset:18440\n"
"buffer_load_dwordx4 v191, s[8:11], s32 offen lds\n"
"v_mfma_f32_16x16x32_bf16 v[140:143], v[88:91], v[156:159], v[140:143]\n"
"s_add_u32 s32, s32, s33\n"
"v_max_f32 v136, v136, v112\n"
"v_max_f32 v137, v137, v113\n"
"v_mfma_f32_16x16x16_bf16 v[98:101], v[98:99], v[64:65], 0\n"
"ds_read_b64_tr_b16 v[112:113], v184 offset:136\n"
"v_max_f32 v138, v138, v114\n"
"v_mfma_f32_16x16x32_bf16 v[116:119], v[116:119], v[144:147], 0\n"
"ds_read_b64_tr_b16 a[236:237], v235 offset:12288\n"
"v_max_f32 v139, v139, v115\n"
"v_mfma_f32_16x16x32_bf16 v[116:119], v[92:95], v[158:161], v[116:119]\n"
"v_max3_f32 v170, v136, v137, v138\n"
"s_mov_b64 exec, s[46:47]\n"
"v_add_u32 v183, 0xc0, v183\n"
"s_mov_b64 exec, -1\n"
"v_mfma_f32_16x16x16_bf16 v[106:109], v[106:107], v[64:65], 0\n"
"s_waitcnt vmcnt(10)\n"
"v_cvt_pk_bf16_f32 v254, v98, v99\n"
"v_or_b32 v201, 2, s6\n"
"v_mfma_f32_16x16x32_bf16 v[228:231], a[100:103], v[40:43], v[228:231]\n"
"s_mov_b64 exec, 1\n"
"buffer_store_dword v201, off, s[28:31], s40 sc1\n"
"s_mov_b64 exec, -1\n"
"v_mfma_f32_16x16x32_bf16 v[228:231], a[112:115], v[44:47], v[228:231]\n"
"ds_read_b64 v[98:99], v180 offset:27072\n"
"v_cvt_pk_bf16_f32 v255, v100, v101\n"
"v_mfma_f32_16x16x32_bf16 v[228:231], a[124:127], v[48:51], v[228:231]\n"
"s_add_u32 m0, s34, 7168\n"
"buffer_store_dwordx4 v[252:255], v192, s[28:31], s37 offen sc1\n"
"v_mfma_f32_16x16x32_bf16 v[228:231], a[136:139], v[52:55], v[228:231]\n"
"ds_read_b64 v[100:101], v181 offset:27072\n"
"v_cvt_pk_bf16_f32 v106, v106, v107\n"
"v_mfma_f32_16x16x32_bf16 v[216:219], a[80:83], v[32:35], 0\n"
"v_cvt_pk_bf16_f32 v107, v108, v109\n"
"v_max_f32 v140, v140, v116\n"
"v_mfma_f32_16x16x32_bf16 v[216:219], a[92:95], v[36:39], v[216:219]\n"
"buffer_load_dwordx4 v190, s[8:11], s0 offen lds\n"
"ds_read_b64 v[108:109], v179 offset:64\n"
"v_mfma_f32_16x16x32_bf16 v[216:219], a[104:107], v[40:43], v[216:219]\n"
"ds_read_b64_tr_b16 v[114:115], v185 offset:136\n"
"ds_read_b64_tr_b16 a[238:239], v235 offset:18432\n"
"v_mfma_f32_16x16x32_bf16 v[216:219], a[116:119], v[44:47], v[216:219]\n"
"v_max_f32 v141, v141, v117\n"
"v_max_f32 v142, v142, v118\n"
"v_mfma_f32_16x16x32_bf16 v[216:219], a[128:131], v[48:51], v[216:219]\n"
"s_waitcnt lgkmcnt(4)\n"
"v_max_f32 v143, v143, v119\n"
"v_max3_f32 v170, v170, v139, v140\n"
"v_mfma_f32_16x16x32_bf16 v[120:123], v[120:123], v[144:147], 0\n"
"buffer_store_dwordx2 v[106:107], v193, s[28:31], s37 offen offset:1024 sc1\n"
"v_mfma_f32_16x16x32_bf16 v[120:123], v[96:99], v[156:159], v[120:123]\n"
"s_waitcnt lgkmcnt(3)\n"
"ds_read_b64 v[106:107], v178 offset:64\n"
"ds_read_b64_tr_b16 v[116:117], v184 offset:904\n"
"v_mfma_f32_16x16x32_bf16 v[216:219], a[140:143], v[52:55], v[216:219]\n"
"s_add_u32 s37, s37, 1572864\n"
"ds_read_b64_tr_b16 v[118:119], v185 offset:904\n"
"v_cvt_pk_bf16_f32 v226, v228, v229\n"
"v_mfma_f32_16x16x32_bf16 v[244:247], v[244:247], v[144:147], 0\n"
"v_max3_f32 v170, v170, v141, v142\n"
"v_cvt_pk_bf16_f32 v227, v230, v231\n"
"v_mfma_f32_16x16x32_bf16 v[244:247], v[100:103], v[158:161], v[244:247]\n"
"s_waitcnt lgkmcnt(5)\n"
"ds_write_b128 v186, v[224:227] offset:20384\n"
"v_mfma_f32_16x16x32_bf16 v[248:251], v[248:251], v[144:147], 0\n"
"s_add_u32 m0, s34, 10240\n"
"v_cvt_pk_bf16_f32 v222, v216, v217\n"
"v_cvt_pk_bf16_f32 v223, v218, v219\n"
"v_mfma_f32_16x16x32_bf16 v[248:251], v[108:111], v[158:161], v[248:251]\n"
"buffer_load_dwordx4 v191, s[8:11], s0 offen lds\n"
"ds_write_b128 v186, v[220:223] offset:20320\n"
"v_mfma_f32_16x16x32_bf16 v[208:211], a[144:147], v[32:35], 0\n"
"s_waitcnt lgkmcnt(4)\n"
"v_max_f32 v120, v120, v244\n"
"v_max_f32 v121, v121, v245\n"
"v_mfma_f32_16x16x32_bf16 v[124:127], v[124:127], v[144:147], 0\n"
"s_add_u32 s0, s0, s33\n"
"ds_read_b64_tr_b16 v[244:245], v184 offset:72\n"
"v_max_f32 v122, v122, v246\n"
"v_mfma_f32_16x16x32_bf16 v[124:127], v[104:107], v[156:159], v[124:127]\n"
"v_max_f32 v123, v123, v247\n"
"v_max3_f32 v170, v170, v143, v120\n"
"v_mfma_f32_16x16x32_bf16 v[208:211], a[156:159], v[36:39], v[208:211]\n"
"ds_read_b64_tr_b16 v[246:247], v185 offset:72\n"
"v_max3_f32 v170, v170, v121, v122\n"
"v_mfma_f32_16x16x32_bf16 v[208:211], a[168:171], v[40:43], v[208:211]\n"
"v_mov_b64 v[252:253], 0\n"
"v_mov_b64 v[254:255], 0\n"
"v_mfma_f32_16x16x32_bf16 v[208:211], a[180:183], v[44:47], v[208:211]\n"
"v_max_f32 v124, v124, v248\n"
"v_max_f32 v125, v125, v249\n"
"v_mfma_f32_16x16x32_bf16 v[208:211], a[192:195], v[48:51], v[208:211]\n"
"ds_read_b64_tr_b16 v[248:249], v184 offset:840\n"
"v_max_f32 v126, v126, v250\n"
"v_mfma_f32_16x16x32_bf16 v[208:211], a[204:207], v[52:55], v[208:211]\n"
"s_add_u32 m0, s34, 8192\n"
"v_max_f32 v127, v127, v251\n"
"v_max3_f32 v170, v170, v123, v124\n"
"v_mfma_f32_16x16x32_bf16 v[204:207], a[148:151], v[32:35], 0\n"
"buffer_load_dwordx4 v190, s[8:11], s1 offen lds\n"
"ds_read_b64_tr_b16 v[250:251], v185 offset:840\n"
"v_mfma_f32_16x16x32_bf16 v[204:207], a[160:163], v[36:39], v[204:207]\n"
"v_max3_f32 v170, v170, v125, v126\n"
"v_mov_b64 v[98:99], 0\n"
"v_mfma_f32_16x16x32_bf16 v[204:207], a[172:175], v[40:43], v[204:207]\n"
"v_mov_b64 v[100:101], 0\n"
"v_mov_b64 v[106:107], 0\n"
"v_mfma_f32_16x16x32_bf16 v[204:207], a[184:187], v[44:47], v[204:207]\n"
"v_mov_b64 v[108:109], 0\n"
"v_max_f32 v171, v170, v127\n"
"v_mfma_f32_16x16x32_bf16 v[204:207], a[196:199], v[48:51], v[204:207]\n"
"v_max_f32 v170, v170, v127\n"
"v_cvt_pk_bf16_f32 v144, v208, v209\n"
"v_mfma_f32_16x16x32_bf16 v[204:207], a[208:211], v[52:55], v[204:207]\n"
"v_permlane16_swap_b32 v170, v171\n"
"v_mfma_f32_16x16x32_bf16 v[212:215], a[152:155], v[32:35], 0\n"
"ds_read_b128 v[32:35], v188\n"
"v_max_f32 v170, v170, v171\n"
"v_mfma_f32_16x16x32_bf16 v[212:215], a[164:167], v[36:39], v[212:215]\n"
"ds_read_b128 v[36:39], v189\n"
"v_mov_b32 v171, v170\n"
"v_mfma_f32_16x16x32_bf16 v[212:215], a[176:179], v[40:43], v[212:215]\n"
"s_waitcnt vmcnt(14)\n"
"ds_read_b128 v[40:43], v188 offset:1024\n"
"v_permlane32_swap_b32 v170, v171\n"
"v_max_f32 v170, v170, v171\n"
"v_mul_f32 v170, 0xbe553b95, v170\n"
"v_pk_fma_f32 v[136:137], v[136:137], s[70:71], v[170:171] op_sel_hi:[1,0,0]\n"
"v_pk_fma_f32 v[138:139], v[138:139], s[70:71], v[170:171] op_sel_hi:[1,0,0]\n"
"v_pk_fma_f32 v[140:141], v[140:141], s[70:71], v[170:171] op_sel_hi:[1,0,0]\n"
"v_pk_fma_f32 v[142:143], v[142:143], s[70:71], v[170:171] op_sel_hi:[1,0,0]\n"
"v_pk_fma_f32 v[120:121], v[120:121], s[70:71], v[170:171] op_sel_hi:[1,0,0]\n"
"v_pk_fma_f32 v[122:123], v[122:123], s[70:71], v[170:171] op_sel_hi:[1,0,0]\n"
"v_pk_fma_f32 v[124:125], v[124:125], s[70:71], v[170:171] op_sel_hi:[1,0,0]\n"
"v_pk_fma_f32 v[126:127], v[126:127], s[70:71], v[170:171] op_sel_hi:[1,0,0]\n"
"v_mfma_f32_16x16x32_bf16 v[212:215], a[188:191], v[44:47], v[212:215]\n"
"s_add_u32 m0, s34, 11264\n"
"ds_read_b128 v[44:47], v189 offset:1024\n"
"buffer_load_dwordx4 v191, s[8:11], s1 offen lds\n"
"v_mfma_f32_16x16x32_bf16 v[212:215], a[200:203], v[48:51], v[212:215]\n"
"s_waitcnt vmcnt(13)\n"
"ds_read_b128 v[48:51], v188 offset:2048\n"
"v_exp_f32 v136, v136\n"
"v_mfma_f32_16x16x32_bf16 v[212:215], a[212:215], v[52:55], v[212:215]\n"
"s_waitcnt lgkmcnt(4)\n"
"ds_read_b128 v[52:55], v189 offset:2048\n"
"v_exp_f32 v137, v137\n"
"v_mfma_f32_16x16x32_bf16 v[228:231], a[0:3], v[32:35], 0\n"
"s_waitcnt lgkmcnt(4)\n"
"v_exp_f32 v138, v138\n"
"v_mfma_f32_16x16x32_bf16 v[228:231], a[12:15], v[36:39], v[228:231]\n"
"s_waitcnt lgkmcnt(3)\n"
"v_exp_f32 v139, v139\n"
"v_mfma_f32_16x16x32_bf16 v[228:231], a[24:27], v[40:43], v[228:231]\n"
"s_add_u32 s1, s1, s33\n"
"v_cvt_pk_bf16_f32 v82, v136, v138\n"
"v_cvt_pk_bf16_f32 v83, v137, v139\n"
"v_mfma_f32_16x16x32_bf16 v[216:219], a[4:7], v[32:35], 0\n"
".Lic2%=:\n"
"s_cbranch_execz .Lib0%=\n"
"ds_read_b128 v[136:139], v180 offset:13504\n"
"v_cvt_pk_bf16_f32 v145, v210, v211\n"
"v_mfma_f32_16x16x32_bf16 v[216:219], a[16:19], v[36:39], v[216:219]\n"
"s_waitcnt lgkmcnt(3)\n"
"v_exp_f32 v140, v140\n"
"v_mfma_f32_16x16x32_bf16 v[216:219], a[28:31], v[40:43], v[216:219]\n"
"v_exp_f32 v141, v141\n"
"v_mfma_f32_16x16x32_bf16 v[216:219], a[40:43], v[44:47], v[216:219]\n"
"s_waitcnt lgkmcnt(1)\n"
"v_exp_f32 v142, v142\n"
"v_mfma_f32_16x16x32_bf16 v[216:219], a[52:55], v[48:51], v[216:219]\n"
"s_add_u32 m0, s34, 0\n"
"v_exp_f32 v143, v143\n"
"v_mfma_f32_16x16x32_bf16 v[216:219], a[64:67], v[52:55], v[216:219]\n"
"buffer_load_dwordx4 v190, s[8:11], s32 offen lds\n"
"v_cvt_pk_bf16_f32 v84, v140, v142\n"
"v_mfma_f32_16x16x32_bf16 v[228:231], a[36:39], v[44:47], v[228:231]\n"
"v_cvt_pk_bf16_f32 v85, v141, v143\n"
"v_cvt_pk_bf16_f32 v146, v204, v205\n"
"v_mfma_f32_16x16x32_bf16 v[228:231], a[48:51], v[48:51], v[228:231]\n"
"ds_read_b128 v[140:143], v180 offset:27008\n"
"v_cvt_pk_bf16_f32 v147, v206, v207\n"
"v_mfma_f32_16x16x32_bf16 v[228:231], a[60:63], v[52:55], v[228:231]\n"
"v_cvt_pk_bf16_f32 v158, v212, v213\n"
"v_cvt_pk_bf16_f32 v159, v214, v215\n"
"v_smfmac_f32_16x16x64_bf16 v[252:255], v[82:85], a[240:247], v66\n"
"s_waitcnt vmcnt(10)\n"
"v_exp_f32 v120, v120\n"
"v_smfmac_f32_16x16x64_bf16 v[98:101], v[82:85], a[248:255], v66\n"
"s_barrier\n"
"v_smfmac_f32_16x16x64_bf16 v[106:109], v[82:85], v[68:75], v66\n"
"ds_read_b64_tr_b16 a[240:241], v236\n"
"v_mfma_f32_16x16x32_bf16 v[82:85], v[172:175], v[82:85], 0\n"
"v_exp_f32 v121, v121\n"
"v_mfma_f32_16x16x32_bf16 v[220:223], a[8:11], v[32:35], 0\n"
"v_exp_f32 v122, v122\n"
"v_mfma_f32_16x16x32_bf16 v[220:223], a[20:23], v[36:39], v[220:223]\n"
"s_add_u32 m0, s79, 142464\n"
"v_exp_f32 v123, v123\n"
"v_mfma_f32_16x16x32_bf16 v[220:223], a[32:35], v[40:43], v[220:223]\n"
"buffer_load_dwordx4 v192, s[16:19], s43 offen lds\n"
"v_cvt_pk_bf16_f32 v90, v120, v122\n"
"v_mfma_f32_16x16x32_bf16 v[220:223], a[44:47], v[44:47], v[220:223]\n"
"v_cvt_pk_bf16_f32 v91, v121, v123\n"
"v_exp_f32 v124, v124\n"
"v_mfma_f32_16x16x32_bf16 v[220:223], a[56:59], v[48:51], v[220:223]\n"
"ds_read_b128 v[120:123], v181 offset:13504\n"
"v_exp_f32 v125, v125\n"
"v_mfma_f32_16x16x32_bf16 v[220:223], a[68:71], v[52:55], v[220:223]\n"
"v_exp_f32 v126, v126\n"
"v_mfma_f32_16x16x32_bf16 v[224:227], a[72:75], v[32:35], 0\n"
"v_exp_f32 v127, v127\n"
"v_mfma_f32_16x16x32_bf16 v[224:227], a[84:87], v[36:39], v[224:227]\n"
"v_cvt_pk_bf16_f32 v92, v124, v126\n"
"v_cvt_pk_bf16_f32 v93, v125, v127\n"
"v_mfma_f32_16x16x32_bf16 v[224:227], a[96:99], v[40:43], v[224:227]\n"
"ds_read_b128 v[124:127], v181 offset:27008\n"
"ds_read_b64_tr_b16 a[242:243], v236 offset:6144\n"
"v_smfmac_f32_16x16x64_bf16 v[252:255], v[90:93], v[128:135], v66\n"
"v_cvt_pk_bf16_f32 v228, v228, v229\n"
"v_smfmac_f32_16x16x64_bf16 v[98:101], v[90:93], v[112:119], v66\n"
"s_add_u32 m0, s79, 142464\n"
"s_nop 0\n"
"buffer_load_dwordx4 v192, s[16:19], s43 offen offset:1024 lds\n"
"v_smfmac_f32_16x16x64_bf16 v[106:109], v[90:93], v[244:251], v66\n"
"ds_read_b128 v[128:131], v178\n"
"v_mfma_f32_16x16x32_bf16 v[90:93], v[172:175], v[90:93], v[82:85]\n"
"ds_read_b128 v[112:115], v179\n"
"ds_read_b128 v[132:135], v178 offset:13504\n"
"v_mfma_f32_16x16x32_bf16 v[224:227], a[108:111], v[44:47], v[224:227]\n"
"v_cvt_pk_bf16_f32 v252, v252, v253\n"
"v_cvt_pk_bf16_f32 v253, v254, v255\n"
"v_mfma_f32_16x16x32_bf16 v[224:227], a[120:123], v[48:51], v[224:227]\n"
"ds_read_b64 v[82:83], v180 offset:13568\n"
"ds_read_b64 v[84:85], v181 offset:13568\n"
"v_mfma_f32_16x16x32_bf16 v[224:227], a[132:135], v[52:55], v[224:227]\n"
"v_rcp_f32 v171, v90\n"
"v_mfma_f32_16x16x32_bf16 v[208:211], a[76:79], v[32:35], 0\n"
"ds_read_b64 v[90:91], v180 offset:27072\n"
"ds_read_b64 v[92:93], v181 offset:27072\n"
"v_mfma_f32_16x16x32_bf16 v[208:211], a[88:91], v[36:39], v[208:211]\n"
"v_cvt_pk_bf16_f32 v171, v171, v171\n"
"v_and_b32 v64, v171, v176\n"
"v_mfma_f32_16x16x32_bf16 v[208:211], a[100:103], v[40:43], v[208:211]\n"
"ds_read_b128 v[116:119], v179 offset:13504\n"
"v_and_b32 v65, v171, v177\n"
"v_mfma_f32_16x16x32_bf16 v[208:211], a[112:115], v[44:47], v[208:211]\n"
"s_add_u32 m0, s79, 142464\n"
"v_cvt_pk_bf16_f32 v98, v98, v99\n"
"v_cvt_pk_bf16_f32 v99, v100, v101\n"
"v_mfma_f32_16x16x16_bf16 v[252:255], v[252:253], v[64:65], 0\n"
"buffer_load_dwordx4 v192, s[16:19], s43 offen offset:2048 lds\n"
"v_cvt_pk_bf16_f32 v106, v106, v107\n"
"v_mfma_f32_16x16x16_bf16 v[98:101], v[98:99], v[64:65], 0\n"
"s_add_u32 s43, s80, 12288\n"
"v_cvt_pk_bf16_f32 v107, v108, v109\n"
"v_cvt_pk_bf16_f32 v229, v230, v231\n"
"v_mfma_f32_16x16x32_bf16 v[208:211], a[124:127], v[48:51], v[208:211]\n"
"s_waitcnt lgkmcnt(4)\n"
"v_cvt_pk_bf16_f32 v252, v252, v253\n"
"v_cvt_pk_bf16_f32 v253, v254, v255\n"
"v_mfma_f32_16x16x32_bf16 v[136:139], v[136:139], v[148:151], 0\n"
"s_waitcnt lgkmcnt(3)\n"
"v_cvt_pk_bf16_f32 v254, v98, v99\n"
"v_cvt_pk_bf16_f32 v255, v100, v101\n"
"v_mfma_f32_16x16x32_bf16 v[136:139], v[80:83], v[160:163], v[136:139]\n"
"s_waitcnt lgkmcnt(2)\n"
"buffer_store_dwordx4 v[252:255], v192, s[28:31], s37 offen sc1\n"
"v_mfma_f32_16x16x32_bf16 v[120:123], v[120:123], v[148:151], 0\n"
"s_waitcnt lgkmcnt(1)\n"
"ds_read_b64 v[98:99], v178 offset:64\n"
"ds_read_b64 v[100:101], v179 offset:64\n"
"v_mfma_f32_16x16x32_bf16 v[120:123], v[84:87], v[162:165], v[120:123]\n"
"v_cvt_pk_bf16_f32 v230, v216, v217\n"
"v_cvt_pk_bf16_f32 v231, v218, v219\n"
"v_mfma_f32_16x16x32_bf16 v[140:143], v[140:143], v[148:151], 0\n"
"ds_write_b128 v186, v[228:231] offset:33760\n"
"v_mfma_f32_16x16x32_bf16 v[140:143], v[88:91], v[160:163], v[140:143]\n"
"s_add_u32 m0, s34, 3072\n"
"v_cvt_pk_bf16_f32 v220, v220, v221\n"
"v_cvt_pk_bf16_f32 v221, v222, v223\n"
"v_mfma_f32_16x16x32_bf16 v[124:127], v[124:127], v[148:151], 0\n"
"buffer_load_dwordx4 v191, s[8:11], s32 offen lds\n"
"ds_read_b64_tr_b16 a[244:245], v236 offset:8\n"
"v_mfma_f32_16x16x32_bf16 v[124:127], v[92:95], v[162:165], v[124:127]\n"
"s_add_u32 s32, s32, s33\n"
"v_max_f32 v136, v136, v120\n"
"v_max_f32 v137, v137, v121\n"
"v_mfma_f32_16x16x16_bf16 v[106:109], v[106:107], v[64:65], 0\n"
"v_max_f32 v138, v138, v122\n"
"v_max_f32 v139, v139, v123\n"
"v_mfma_f32_16x16x32_bf16 v[208:211], a[136:139], v[52:55], v[208:211]\n"
"s_waitcnt lgkmcnt(3)\n"
"buffer_load_dword v237, v67, s[8:11], s78 offen\n"
"v_max3_f32 v170, v136, v137, v138\n"
"v_mfma_f32_16x16x32_bf16 v[128:131], v[128:131], v[148:151], 0\n"
"s_waitcnt lgkmcnt(2)\n"
"v_cvt_pk_bf16_f32 v106, v106, v107\n"
"v_cvt_pk_bf16_f32 v107, v108, v109\n"
"v_mfma_f32_16x16x32_bf16 v[128:131], v[96:99], v[160:163], v[128:131]\n"
"s_add_u32 s78, s78, s33\n"
"ds_read_b64 v[108:109], v179 offset:13568\n"
"ds_read_b64_tr_b16 a[246:247], v236 offset:6152\n"
"v_mfma_f32_16x16x32_bf16 v[112:115], v[112:115], v[148:151], 0\n"
"v_max_f32 v140, v140, v124\n"
"v_max_f32 v141, v141, v125\n"
"v_mfma_f32_16x16x32_bf16 v[112:115], v[100:103], v[162:165], v[112:115]\n"
"buffer_load_dword v200, off, s[28:31], s39 sc1\n"
"ds_read_b64_tr_b16 a[248:249], v235\n"
"v_mfma_f32_16x16x32_bf16 v[204:207], a[80:83], v[32:35], 0\n"
"v_max_f32 v142, v142, v126\n"
"v_max_f32 v143, v143, v127\n"
"v_mfma_f32_16x16x32_bf16 v[204:207], a[92:95], v[36:39], v[204:207]\n"
"s_add_u32 m0, s34, 1024\n"
"buffer_store_dwordx2 v[106:107], v193, s[28:31], s37 offen offset:1024 sc1\n"
"v_mfma_f32_16x16x32_bf16 v[204:207], a[104:107], v[40:43], v[204:207]\n"
"s_add_u32 s37, s37, 1572864\n"
"ds_read_b64 v[106:107], v178 offset:13568\n"
"ds_read_b64_tr_b16 a[250:251], v235 offset:6144\n"
"v_mfma_f32_16x16x32_bf16 v[204:207], a[116:119], v[44:47], v[204:207]\n"
"v_max3_f32 v170, v170, v139, v140\n"
"v_cvt_pk_bf16_f32 v224, v224, v225\n"
"v_mfma_f32_16x16x32_bf16 v[204:207], a[128:131], v[48:51], v[204:207]\n"
"s_waitcnt lgkmcnt(4)\n"
"v_max3_f32 v170, v170, v141, v142\n"
"v_cvt_pk_bf16_f32 v225, v226, v227\n"
"v_mfma_f32_16x16x32_bf16 v[204:207], a[140:143], v[52:55], v[204:207]\n"
"buffer_load_dwordx4 v190, s[8:11], s0 offen lds\n"
"v_max_f32 v128, v128, v112\n"
"v_mfma_f32_16x16x32_bf16 v[116:119], v[116:119], v[148:151], 0\n"
"v_max_f32 v129, v129, v113\n"
"v_cvt_pk_bf16_f32 v226, v208, v209\n"
"v_mfma_f32_16x16x32_bf16 v[116:119], v[108:111], v[162:165], v[116:119]\n"
"v_max_f32 v130, v130, v114\n"
"v_cvt_pk_bf16_f32 v227, v210, v211\n"
"v_mfma_f32_16x16x32_bf16 v[212:215], a[144:147], v[32:35], 0\n"
"ds_write_b128 v186, v[224:227] offset:33888\n"
"v_mfma_f32_16x16x32_bf16 v[212:215], a[156:159], v[36:39], v[212:215]\n"
"s_waitcnt lgkmcnt(2)\n"
"v_max_f32 v131, v131, v115\n"
"v_max3_f32 v170, v170, v143, v128\n"
"v_mfma_f32_16x16x32_bf16 v[132:135], v[132:135], v[148:151], 0\n"
"v_cvt_pk_bf16_f32 v222, v204, v205\n"
"v_max3_f32 v170, v170, v129, v130\n"
"v_mfma_f32_16x16x32_bf16 v[132:135], v[104:107], v[160:163], v[132:135]\n"
"v_cvt_pk_bf16_f32 v223, v206, v207\n"
"v_mov_b64 v[252:253], 0\n"
"v_mfma_f32_16x16x32_bf16 v[212:215], a[168:171], v[40:43], v[212:215]\n"
"ds_write_b128 v186, v[220:223] offset:33824\n"
"v_mfma_f32_16x16x32_bf16 v[212:215], a[180:183], v[44:47], v[212:215]\n"
"s_add_u32 m0, s34, 4096\n"
"v_mov_b64 v[254:255], 0\n"
"v_mov_b64 v[98:99], 0\n"
"v_mfma_f32_16x16x32_bf16 v[212:215], a[192:195], v[48:51], v[212:215]\n"
"buffer_load_dwordx4 v191, s[8:11], s0 offen lds\n"
"v_max_f32 v132, v132, v116\n"
"v_mfma_f32_16x16x32_bf16 v[212:215], a[204:207], v[52:55], v[212:215]\n"
"s_add_u32 s0, s0, s33\n"
"v_max_f32 v133, v133, v117\n"
"v_max_f32 v134, v134, v118\n"
"v_mfma_f32_16x16x32_bf16 v[216:219], a[148:151], v[32:35], 0\n"
"v_max_f32 v135, v135, v119\n"
"v_max3_f32 v170, v170, v131, v132\n"
"v_mfma_f32_16x16x32_bf16 v[216:219], a[160:163], v[36:39], v[216:219]\n"
"v_max3_f32 v170, v170, v133, v134\n"
"v_mov_b64 v[100:101], 0\n"
"v_mfma_f32_16x16x32_bf16 v[216:219], a[172:175], v[40:43], v[216:219]\n"
"v_mov_b64 v[106:107], 0\n"
"v_mov_b64 v[108:109], 0\n"
"v_mfma_f32_16x16x32_bf16 v[216:219], a[184:187], v[44:47], v[216:219]\n"
"ds_read_b128 v[244:247], v180 offset:27008\n"
"v_max_f32 v171, v170, v135\n"
"v_mfma_f32_16x16x32_bf16 v[216:219], a[196:199], v[48:51], v[216:219]\n"
"v_max_f32 v170, v170, v135\n"
"v_cvt_pk_bf16_f32 v148, v212, v213\n"
"v_mfma_f32_16x16x32_bf16 v[216:219], a[208:211], v[52:55], v[216:219]\n"
"ds_read_b128 v[120:123], v181 offset:27008\n"
"ds_read_b128 v[248:251], v178\n"
"v_mfma_f32_16x16x32_bf16 v[228:231], a[152:155], v[32:35], 0\n"
"s_waitcnt vmcnt(18)\n"
"ds_read_b128 v[32:35], v188 offset:6144\n"
"v_permlane16_swap_b32 v170, v171\n"
"v_mfma_f32_16x16x32_bf16 v[228:231], a[164:167], v[36:39], v[228:231]\n"
"s_add_u32 m0, s34, 2048\n"
"ds_read_b128 v[36:39], v189 offset:6144\n"
"buffer_load_dwordx4 v190, s[8:11], s1 offen lds\n"
"v_mfma_f32_16x16x32_bf16 v[228:231], a[176:179], v[40:43], v[228:231]\n"
"s_waitcnt vmcnt(14)\n"
"ds_read_b128 v[40:43], v188 offset:7168\n"
"v_max_f32 v170, v170, v171\n"
"v_mfma_f32_16x16x32_bf16 v[228:231], a[188:191], v[44:47], v[228:231]\n"
"ds_read_b128 v[44:47], v189 offset:7168\n"
"v_mov_b32 v171, v170\n"
"v_mfma_f32_16x16x32_bf16 v[228:231], a[200:203], v[48:51], v[228:231]\n"
"s_nop 0\n"
"v_permlane32_swap_b32 v170, v171\n"
"v_max_f32 v170, v170, v171\n"
"v_mul_f32 v170, 0xbe553b95, v170\n"
"v_pk_fma_f32 v[136:137], v[136:137], s[70:71], v[170:171] op_sel_hi:[1,0,0]\n"
"v_pk_fma_f32 v[138:139], v[138:139], s[70:71], v[170:171] op_sel_hi:[1,0,0]\n"
"v_pk_fma_f32 v[140:141], v[140:141], s[70:71], v[170:171] op_sel_hi:[1,0,0]\n"
"v_pk_fma_f32 v[142:143], v[142:143], s[70:71], v[170:171] op_sel_hi:[1,0,0]\n"
"v_pk_fma_f32 v[128:129], v[128:129], s[70:71], v[170:171] op_sel_hi:[1,0,0]\n"
"v_pk_fma_f32 v[130:131], v[130:131], s[70:71], v[170:171] op_sel_hi:[1,0,0]\n"
"v_pk_fma_f32 v[132:133], v[132:133], s[70:71], v[170:171] op_sel_hi:[1,0,0]\n"
"v_pk_fma_f32 v[134:135], v[134:135], s[70:71], v[170:171] op_sel_hi:[1,0,0]\n"
"v_mfma_f32_16x16x32_bf16 v[228:231], a[212:215], v[52:55], v[228:231]\n"
"ds_read_b128 v[124:127], v179\n"
"v_exp_f32 v136, v136\n"
"v_exp_f32 v137, v137\n"
"v_exp_f32 v138, v138\n"
"v_exp_f32 v139, v139\n"
"v_cvt_pk_bf16_f32 v82, v136, v138\n"
"v_cvt_pk_bf16_f32 v83, v137, v139\n"
"v_exp_f32 v140, v140\n"
"v_exp_f32 v141, v141\n"
"v_exp_f32 v142, v142\n"
"v_exp_f32 v143, v143\n"
"v_cvt_pk_bf16_f32 v84, v140, v142\n"
"v_cvt_pk_bf16_f32 v85, v141, v143\n"
"v_exp_f32 v128, v128\n"
"s_nop 0\n"
"v_smfmac_f32_16x16x64_bf16 v[252:255], v[82:85], v[8:15], v66\n"
"s_waitcnt lgkmcnt(1)\n"
"ds_read_b64_tr_b16 v[8:9], v182 offset:128\n"
"v_smfmac_f32_16x16x64_bf16 v[98:101], v[82:85], v[16:23], v66\n"
"s_add_u32 m0, s34, 5120\n"
"s_nop 0\n"
"buffer_load_dwordx4 v191, s[8:11], s1 offen lds\n"
"v_smfmac_f32_16x16x64_bf16 v[106:109], v[82:85], v[24:31], v66\n"
"s_add_u32 s1, s1, s33\n"
"ds_read_b64_tr_b16 v[10:11], v183 offset:128\n"
"v_mfma_f32_16x16x32_bf16 v[82:85], v[172:175], v[82:85], 0\n"
"s_waitcnt vmcnt(13)\n"
"ds_read_b64_tr_b16 v[12:13], v182 offset:896\n"
"ds_read_b64_tr_b16 v[14:15], v183 offset:896\n"
"v_mfma_f32_16x16x32_bf16 v[208:211], a[0:3], v[32:35], 0\n"
"v_exp_f32 v129, v129\n"
"v_mfma_f32_16x16x32_bf16 v[208:211], a[12:15], v[36:39], v[208:211]\n"
"ds_read_b128 v[48:51], v188 offset:8192\n"
"ds_read_b128 v[52:55], v189 offset:8192\n"
"v_mfma_f32_16x16x32_bf16 v[208:211], a[24:27], v[40:43], v[208:211]\n"
"v_exp_f32 v130, v130\n"
"v_mfma_f32_16x16x32_bf16 v[208:211], a[36:39], v[44:47], v[208:211]\n"
"ds_read_b64_tr_b16 v[16:17], v182 offset:136\n"
"ds_read_b64_tr_b16 v[18:19], v183 offset:136\n"
"v_mfma_f32_16x16x32_bf16 v[204:207], a[4:7], v[32:35], 0\n"
"v_exp_f32 v131, v131\n"
"v_mfma_f32_16x16x32_bf16 v[204:207], a[16:19], v[36:39], v[204:207]\n"
"ds_read_b64_tr_b16 v[20:21], v182 offset:904\n"
"ds_read_b64_tr_b16 v[22:23], v183 offset:904\n"
"v_mfma_f32_16x16x32_bf16 v[204:207], a[28:31], v[40:43], v[204:207]\n"
"s_add_u32 m0, s34, 6144\n"
"v_exp_f32 v132, v132\n"
"v_mfma_f32_16x16x32_bf16 v[204:207], a[40:43], v[44:47], v[204:207]\n"
"buffer_load_dwordx4 v190, s[8:11], s32 offen lds\n"
"ds_read_b64_tr_b16 v[24:25], v182 offset:72\n"
"v_mfma_f32_16x16x32_bf16 v[220:223], a[8:11], v[32:35], 0\n"
"ds_read_b64_tr_b16 v[26:27], v183 offset:72\n"
"ds_read_b64_tr_b16 v[28:29], v182 offset:840\n"
"v_mfma_f32_16x16x32_bf16 v[220:223], a[20:23], v[36:39], v[220:223]\n"
"s_waitcnt lgkmcnt(8)\n"
"v_exp_f32 v133, v133\n"
"v_mfma_f32_16x16x32_bf16 v[220:223], a[32:35], v[40:43], v[220:223]\n"
"ds_read_b64_tr_b16 v[30:31], v183 offset:840\n"
"ds_read_b128 v[112:115], v178 offset:13504\n"
"v_mfma_f32_16x16x32_bf16 v[220:223], a[44:47], v[44:47], v[220:223]\n"
"s_waitcnt vmcnt(10)\n"
"v_exp_f32 v134, v134\n"
"v_mfma_f32_16x16x32_bf16 v[220:223], a[56:59], v[48:51], v[220:223]\n"
"s_waitcnt lgkmcnt(9)\n"
"ds_read_b128 v[136:139], v179 offset:13504\n"
"v_cvt_pk_bf16_f32 v90, v128, v130\n"
"v_mfma_f32_16x16x32_bf16 v[220:223], a[68:71], v[52:55], v[220:223]\n"
"s_barrier\n"
"v_mfma_f32_16x16x32_bf16 v[208:211], a[48:51], v[48:51], v[208:211]\n"
"ds_read_b128 v[116:119], v178 offset:27008\n"
"ds_read_b128 v[140:143], v179 offset:27008\n"
"v_mfma_f32_16x16x32_bf16 v[208:211], a[60:63], v[52:55], v[208:211]\n"
"v_exp_f32 v135, v135\n"
"v_mfma_f32_16x16x32_bf16 v[204:207], a[52:55], v[48:51], v[204:207]\n"
"s_add_u32 m0, s79, 130176\n"
"v_cvt_pk_bf16_f32 v91, v129, v131\n"
"v_cvt_pk_bf16_f32 v92, v132, v134\n"
"v_mfma_f32_16x16x32_bf16 v[204:207], a[64:67], v[52:55], v[204:207]\n"
"buffer_load_dwordx4 v192, s[16:19], s43 offen lds\n"
"v_cvt_pk_bf16_f32 v93, v133, v135\n"
"v_mfma_f32_16x16x32_bf16 v[224:227], a[72:75], v[32:35], 0\n"
"s_waitcnt lgkmcnt(4)\n"
"v_cvt_pk_bf16_f32 v149, v214, v215\n"
"v_cvt_pk_bf16_f32 v150, v216, v217\n"
"v_smfmac_f32_16x16x64_bf16 v[252:255], v[90:93], v[8:15], v66\n"
"v_cvt_pk_bf16_f32 v151, v218, v219\n"
"v_smfmac_f32_16x16x64_bf16 v[98:101], v[90:93], v[16:23], v66\n"
"v_cvt_pk_bf16_f32 v162, v228, v229\n"
"v_smfmac_f32_16x16x64_bf16 v[106:109], v[90:93], v[24:31], v66\n"
"v_cvt_pk_bf16_f32 v163, v230, v231\n"
"v_mfma_f32_16x16x32_bf16 v[90:93], v[172:175], v[90:93], v[82:85]\n"
"ds_read_b64_tr_b16 a[252:253], v236 offset:12288\n"
"v_cvt_pk_bf16_f32 v208, v208, v209\n"
"v_mfma_f32_16x16x32_bf16 v[224:227], a[84:87], v[36:39], v[224:227]\n"
"ds_read_b64 v[82:83], v180 offset:27072\n"
"v_cvt_pk_bf16_f32 v252, v252, v253\n"
"v_mfma_f32_16x16x32_bf16 v[224:227], a[96:99], v[40:43], v[224:227]\n"
"ds_read_b64 v[84:85], v181 offset:27072\n"
"v_cvt_pk_bf16_f32 v253, v254, v255\n"
"v_mfma_f32_16x16x32_bf16 v[224:227], a[108:111], v[44:47], v[224:227]\n"
"ds_read_b64 v[92:93], v179 offset:64\n"
"v_cvt_pk_bf16_f32 v106, v106, v107\n"
"v_mfma_f32_16x16x32_bf16 v[224:227], a[120:123], v[48:51], v[224:227]\n"
"s_add_u32 m0, s79, 130176\n"
"v_rcp_f32 v171, v90\n"
"v_mfma_f32_16x16x32_bf16 v[224:227], a[132:135], v[52:55], v[224:227]\n"
"buffer_load_dwordx4 v192, s[16:19], s43 offen offset:1024 lds\n"
"ds_read_b64 v[90:91], v178 offset:64\n"
"v_mfma_f32_16x16x32_bf16 v[212:215], a[76:79], v[32:35], 0\n"
"v_cvt_pk_bf16_f32 v171, v171, v171\n"
"v_and_b32 v64, v171, v176\n"
"v_mfma_f32_16x16x32_bf16 v[212:215], a[88:91], v[36:39], v[212:215]\n"
"v_and_b32 v65, v171, v177\n"
"v_cvt_pk_bf16_f32 v98, v98, v99\n"
"v_mfma_f32_16x16x32_bf16 v[212:215], a[100:103], v[40:43], v[212:215]\n"
"v_cvt_pk_bf16_f32 v107, v108, v109\n"
"v_cvt_pk_bf16_f32 v99, v100, v101\n"
"v_mfma_f32_16x16x16_bf16 v[252:255], v[252:253], v[64:65], 0\n"
"ds_read_b64_tr_b16 v[128:129], v184 offset:128\n"
"ds_read_b64_tr_b16 v[130:131], v185 offset:128\n"
"v_mfma_f32_16x16x16_bf16 v[106:109], v[106:107], v[64:65], 0\n"
"s_waitcnt lgkmcnt(5)\n"
"ds_read_b64_tr_b16 v[132:133], v184 offset:896\n"
"v_cvt_pk_bf16_f32 v209, v210, v211\n"
"v_mfma_f32_16x16x16_bf16 v[98:101], v[98:99], v[64:65], 0\n"
"s_waitcnt lgkmcnt(5)\n"
"v_cvt_pk_bf16_f32 v252, v252, v253\n"
"v_cvt_pk_bf16_f32 v253, v254, v255\n"
"v_mfma_f32_16x16x32_bf16 v[244:247], v[244:247], v[152:155], 0\n"
"s_waitcnt lgkmcnt(4)\n"
"v_cvt_pk_bf16_f32 v106, v106, v107\n"
"v_cvt_pk_bf16_f32 v107, v108, v109\n"
"v_mfma_f32_16x16x32_bf16 v[244:247], v[80:83], v[164:167], v[244:247]\n"
"s_waitcnt lgkmcnt(3)\n"
"buffer_store_dwordx2 v[106:107], v193, s[28:31], s37 offen offset:1024 sc1\n"
"v_mfma_f32_16x16x32_bf16 v[120:123], v[120:123], v[152:155], 0\n"
"ds_read_b64 v[106:107], v178 offset:27072\n"
"ds_read_b64 v[108:109], v179 offset:27072\n"
"v_mfma_f32_16x16x32_bf16 v[120:123], v[84:87], v[166:169], v[120:123]\n"
"s_add_u32 m0, s79, 130176\n"
"v_cvt_pk_bf16_f32 v254, v98, v99\n"
"v_cvt_pk_bf16_f32 v255, v100, v101\n"
"v_mfma_f32_16x16x32_bf16 v[248:251], v[248:251], v[152:155], 0\n"
"buffer_load_dwordx4 v192, s[16:19], s43 offen offset:2048 lds\n"
"ds_read_b64 v[98:99], v178 offset:13568\n"
"v_mfma_f32_16x16x32_bf16 v[248:251], v[88:91], v[164:167], v[248:251]\n"
"s_add_u32 s43, s80, 49152\n"
"ds_read_b64 v[100:101], v179 offset:13568\n"
"ds_read_b64_tr_b16 v[134:135], v185 offset:896\n"
"v_mfma_f32_16x16x32_bf16 v[124:127], v[124:127], v[152:155], 0\n"
"v_cvt_pk_bf16_f32 v210, v204, v205\n"
"v_cvt_pk_bf16_f32 v211, v206, v207\n"
"v_mfma_f32_16x16x32_bf16 v[124:127], v[92:95], v[166:169], v[124:127]\n"
"ds_write_b128 v186, v[208:211] offset:47264\n"
"v_mfma_f32_16x16x32_bf16 v[212:215], a[112:115], v[44:47], v[212:215]\n"
"buffer_store_dwordx4 v[252:255], v192, s[28:31], s37 offen sc1\n"
"v_mfma_f32_16x16x32_bf16 v[212:215], a[124:127], v[48:51], v[212:215]\n"
"s_waitcnt lgkmcnt(4)\n"
"ds_read_b64_tr_b16 a[254:255], v236 offset:18432\n"
"v_cvt_pk_bf16_f32 v220, v220, v221\n"
"v_mfma_f32_16x16x32_bf16 v[212:215], a[136:139], v[52:55], v[212:215]\n"
"s_waitcnt lgkmcnt(4)\n"
"v_cvt_pk_bf16_f32 v221, v222, v223\n"
"v_max_f32 v244, v244, v120\n"
"v_mfma_f32_16x16x32_bf16 v[116:119], v[116:119], v[152:155], 0\n"
"s_waitcnt lgkmcnt(3)\n"
"buffer_load_dword v237, v67, s[8:11], s78 offen\n"
"ds_read_b64_tr_b16 v[0:1], v236 offset:12296\n"
"v_mfma_f32_16x16x32_bf16 v[116:119], v[104:107], v[164:167], v[116:119]\n"
"s_add_u32 s37, s37, 1572864\n"
"v_max_f32 v245, v245, v121\n"
"v_max_f32 v246, v246, v122\n"
"v_mfma_f32_16x16x32_bf16 v[112:115], v[112:115], v[152:155], 0\n"
"s_add_u32 m0, s34, 9216\n"
"ds_read_b64_tr_b16 v[120:121], v184 offset:136\n"
"ds_read_b64_tr_b16 v[2:3], v236 offset:18440\n"
"v_mfma_f32_16x16x32_bf16 v[112:115], v[96:99], v[164:167], v[112:115]\n"
"s_waitcnt vmcnt(12)\n"
"v_max_f32 v247, v247, v123\n"
"v_max3_f32 v170, v244, v245, v246\n"
"v_mfma_f32_16x16x32_bf16 v[136:139], v[136:139], v[152:155], 0\n"
"s_add_u32 s78, s78, s33\n"
"buffer_load_dwordx4 v191, s[8:11], s32 offen lds\n"
"v_add3_u32 v200, v200, s50, -2\n"
"v_mfma_f32_16x16x32_bf16 v[136:139], v[100:103], v[166:169], v[136:139]\n"
"s_add_u32 s32, s32, s33\n"
"v_cmp_lt_u32 vcc, 64, v200\n"
"v_max_f32 v248, v248, v124\n"
"v_mfma_f32_16x16x32_bf16 v[140:143], v[140:143], v[152:155], 0\n"
"ds_read_b64_tr_b16 v[122:123], v185 offset:136\n"
"ds_read_b64_tr_b16 v[4:5], v235 offset:12288\n"
"v_mfma_f32_16x16x32_bf16 v[140:143], v[108:111], v[166:169], v[140:143]\n"
"s_cbranch_vccnz .Lp1%=\n"
".Lr1%=:\n"
"v_mfma_f32_16x16x32_bf16 v[216:219], a[80:83], v[32:35], 0\n"
"buffer_load_dword v201, off, s[28:31], s40 offset:128 sc1\n"
"ds_read_b64_tr_b16 v[6:7], v235 offset:18432\n"
"v_mfma_f32_16x16x32_bf16 v[216:219], a[92:95], v[36:39], v[216:219]\n"
"buffer_load_dwordx4 v[56:59], v192, s[28:31], s38 offen sc1\n"
"v_max_f32 v249, v249, v125\n"
"v_mfma_f32_16x16x32_bf16 v[216:219], a[104:107], v[40:43], v[216:219]\n"
"ds_read_b64_tr_b16 v[124:125], v184 offset:904\n"
"v_max_f32 v250, v250, v126\n"
"v_mfma_f32_16x16x32_bf16 v[216:219], a[116:119], v[44:47], v[216:219]\n"
"v_cvt_pk_bf16_f32 v224, v224, v225\n"
"v_max_f32 v251, v251, v127\n"
"v_mfma_f32_16x16x32_bf16 v[216:219], a[128:131], v[48:51], v[216:219]\n"
"ds_read_b64_tr_b16 v[126:127], v185 offset:904\n"
"v_max3_f32 v170, v170, v247, v248\n"
"v_mfma_f32_16x16x32_bf16 v[216:219], a[140:143], v[52:55], v[216:219]\n"
"s_add_u32 m0, s34, 7168\n"
"buffer_load_dwordx2 v[60:61], v193, s[28:31], s38 offen offset:1024 sc1\n"
"v_cvt_pk_bf16_f32 v225, v226, v227\n"
"v_mfma_f32_16x16x32_bf16 v[228:231], a[144:147], v[32:35], 0\n"
"s_add_u32 s38, s38, 1572864\n"
"v_max3_f32 v170, v170, v249, v250\n"
"v_cvt_pk_bf16_f32 v226, v212, v213\n"
"v_mfma_f32_16x16x32_bf16 v[228:231], a[156:159], v[36:39], v[228:231]\n"
"v_cvt_pk_bf16_f32 v227, v214, v215\n"
"v_cvt_pk_bf16_f32 v222, v216, v217\n"
"v_mfma_f32_16x16x32_bf16 v[228:231], a[168:171], v[40:43], v[228:231]\n"
"ds_write_b128 v186, v[224:227] offset:47392\n"
"v_mfma_f32_16x16x32_bf16 v[228:231], a[180:183], v[44:47], v[228:231]\n"
"buffer_load_dwordx4 v190, s[8:11], s0 offen lds\n"
"v_max_f32 v112, v112, v136\n"
"v_mfma_f32_16x16x32_bf16 v[228:231], a[192:195], v[48:51], v[228:231]\n"
"v_cvt_pk_bf16_f32 v223, v218, v219\n"
"v_max_f32 v113, v113, v137\n"
"v_mfma_f32_16x16x32_bf16 v[228:231], a[204:207], v[52:55], v[228:231]\n"
"ds_write_b128 v186, v[220:223] offset:47328\n"
"v_mfma_f32_16x16x32_bf16 v[204:207], a[148:151], v[32:35], 0\n"
"v_max_f32 v114, v114, v138\n"
"v_max_f32 v115, v115, v139\n"
"v_mfma_f32_16x16x32_bf16 v[204:207], a[160:163], v[36:39], v[204:207]\n"
"ds_read_b64_tr_b16 v[136:137], v184 offset:72\n"
"ds_read_b64_tr_b16 v[138:139], v185 offset:72\n"
"v_mfma_f32_16x16x32_bf16 v[204:207], a[172:175], v[40:43], v[204:207]\n"
"v_max3_f32 v170, v170, v251, v112\n"
"v_max3_f32 v170, v170, v113, v114\n"
"v_mfma_f32_16x16x32_bf16 v[204:207], a[184:187], v[44:47], v[204:207]\n"
"v_max_f32 v116, v116, v140\n"
"v_max_f32 v117, v117, v141\n"
"v_mfma_f32_16x16x32_bf16 v[204:207], a[196:199], v[48:51], v[204:207]\n"
"ds_read_b64_tr_b16 v[140:141], v184 offset:840\n"
"v_max_f32 v118, v118, v142\n"
"v_mfma_f32_16x16x32_bf16 v[204:207], a[208:211], v[52:55], v[204:207]\n"
"s_add_u32 m0, s34, 10240\n"
"v_max_f32 v119, v119, v143\n"
"v_max3_f32 v170, v170, v115, v116\n"
"v_mfma_f32_16x16x32_bf16 v[208:211], a[152:155], v[32:35], 0\n"
"buffer_load_dwordx4 v191, s[8:11], s0 offen lds\n"
"ds_read_b128 v[32:35], v188\n"
"v_mfma_f32_16x16x32_bf16 v[208:211], a[164:167], v[36:39], v[208:211]\n"
"s_add_u32 s0, s0, s33\n"
"ds_read_b128 v[36:39], v189\n"
"ds_read_b64_tr_b16 v[142:143], v185 offset:840\n"
"v_mfma_f32_16x16x32_bf16 v[208:211], a[176:179], v[40:43], v[208:211]\n"
"s_waitcnt vmcnt(15)\n"
"v_max3_f32 v170, v170, v117, v118\n"
"v_mov_b64 v[252:253], 0\n"
"v_mfma_f32_16x16x32_bf16 v[208:211], a[188:191], v[44:47], v[208:211]\n"
"ds_read_b128 v[40:43], v188 offset:1024\n"
"ds_read_b128 v[44:47], v189 offset:1024\n"
"v_mfma_f32_16x16x32_bf16 v[208:211], a[200:203], v[48:51], v[208:211]\n"
"v_mov_b64 v[254:255], 0\n"
"v_mov_b64 v[98:99], 0\n"
"v_mfma_f32_16x16x32_bf16 v[208:211], a[212:215], v[52:55], v[208:211]\n"
"v_mov_b64 v[100:101], 0\n"
"v_mov_b64 v[106:107], 0\n"
"v_mov_b64 v[108:109], 0\n"
"v_max_f32 v171, v170, v119\n"
"v_max_f32 v170, v170, v119\n"
"v_cvt_pk_bf16_f32 v152, v228, v229\n"
"v_cvt_pk_bf16_f32 v153, v230, v231\n"
"v_permlane16_swap_b32 v170, v171\n"
"v_max_f32 v170, v170, v171\n"
"v_mov_b32 v171, v170\n"
"v_cvt_pk_bf16_f32 v154, v204, v205\n"
"v_cvt_pk_bf16_f32 v155, v206, v207\n"
"v_permlane32_swap_b32 v170, v171\n"
"v_max_f32 v170, v170, v171\n"
"v_mul_f32 v170, 0xbe553b95, v170\n"
"v_pk_fma_f32 v[244:245], v[244:245], s[70:71], v[170:171] op_sel_hi:[1,0,0]\n"
"v_pk_fma_f32 v[246:247], v[246:247], s[70:71], v[170:171] op_sel_hi:[1,0,0]\n"
"v_pk_fma_f32 v[248:249], v[248:249], s[70:71], v[170:171] op_sel_hi:[1,0,0]\n"
"v_pk_fma_f32 v[250:251], v[250:251], s[70:71], v[170:171] op_sel_hi:[1,0,0]\n"
"v_pk_fma_f32 v[112:113], v[112:113], s[70:71], v[170:171] op_sel_hi:[1,0,0]\n"
"v_pk_fma_f32 v[114:115], v[114:115], s[70:71], v[170:171] op_sel_hi:[1,0,0]\n"
"v_pk_fma_f32 v[116:117], v[116:117], s[70:71], v[170:171] op_sel_hi:[1,0,0]\n"
"v_pk_fma_f32 v[118:119], v[118:119], s[70:71], v[170:171] op_sel_hi:[1,0,0]\n"
"v_exp_f32 v244, v244\n"
"v_exp_f32 v245, v245\n"
"v_exp_f32 v246, v246\n"
"v_exp_f32 v247, v247\n"
"v_cvt_pk_bf16_f32 v82, v244, v246\n"
"v_cvt_pk_bf16_f32 v83, v245, v247\n"
"v_exp_f32 v248, v248\n"
"v_exp_f32 v249, v249\n"
"v_exp_f32 v250, v250\n"
"v_exp_f32 v251, v251\n"
"v_cvt_pk_bf16_f32 v84, v248, v250\n"
"v_cvt_pk_bf16_f32 v85, v249, v251\n"
"v_cvt_pk_bf16_f32 v166, v208, v209\n"
"v_cvt_pk_bf16_f32 v167, v210, v211\n"
"v_exp_f32 v112, v112\n"
"v_exp_f32 v113, v113\n"
"v_exp_f32 v114, v114\n"
"v_exp_f32 v115, v115\n"
"v_cvt_pk_bf16_f32 v90, v112, v114\n"
"v_cvt_pk_bf16_f32 v91, v113, v115\n"
"v_exp_f32 v116, v116\n"
"v_exp_f32 v117, v117\n"
"v_exp_f32 v118, v118\n"
"v_exp_f32 v119, v119\n"
"v_cvt_pk_bf16_f32 v92, v116, v118\n"
"v_cvt_pk_bf16_f32 v93, v117, v119\n"
"s_mov_b64 exec, s[44:45]\n"
"v_add_u32 v181, 0xc0, v181\n"
"s_mov_b64 exec, -1\n"
"s_mov_b64 exec, s[46:47]\n"
"v_add_u32 v185, 0xc0, v185\n"
"s_mov_b64 exec, -1\n"
"s_waitcnt vmcnt(13) lgkmcnt(0)\n"
"v_smfmac_f32_16x16x64_bf16 v[252:255], v[82:85], v[128:135], v66\n"
"s_add_u32 m0, s34, 8192\n"
"s_nop 0\n"
"buffer_load_dwordx4 v190, s[8:11], s1 offen lds\n"
"v_smfmac_f32_16x16x64_bf16 v[98:101], v[82:85], v[120:127], v66\n"
"ds_read_b128 v[48:51], v188 offset:2048\n"
"v_smfmac_f32_16x16x64_bf16 v[106:109], v[82:85], v[136:143], v66\n"
"ds_read_b128 v[52:55], v189 offset:2048\n"
"v_mfma_f32_16x16x32_bf16 v[212:215], a[0:3], v[32:35], 0\n"
"ds_read_b64_tr_b16 v[244:245], v182 offset:13632\n"
"ds_read_b64_tr_b16 v[246:247], v183 offset:13632\n"
"v_mfma_f32_16x16x32_bf16 v[212:215], a[12:15], v[36:39], v[212:215]\n"
"ds_read_b64_tr_b16 v[248:249], v182 offset:14400\n"
"v_mfma_f32_16x16x32_bf16 v[212:215], a[24:27], v[40:43], v[212:215]\n"
"ds_read_b64_tr_b16 v[250:251], v183 offset:14400\n"
"v_mfma_f32_16x16x32_bf16 v[212:215], a[36:39], v[44:47], v[212:215]\n"
"ds_read_b64_tr_b16 v[128:129], v182 offset:13640\n"
"v_mfma_f32_16x16x32_bf16 v[82:85], v[172:175], v[82:85], 0\n"
"ds_read_b64_tr_b16 v[130:131], v183 offset:13640\n"
"v_mfma_f32_16x16x32_bf16 v[216:219], a[4:7], v[32:35], 0\n"
"ds_read_b64_tr_b16 v[132:133], v182 offset:14408\n"
"v_mfma_f32_16x16x32_bf16 v[216:219], a[16:19], v[36:39], v[216:219]\n"
"s_waitcnt lgkmcnt(8)\n"
"s_add_u32 m0, s34, 11264\n"
"ds_read_b64_tr_b16 v[134:135], v183 offset:14408\n"
"v_mfma_f32_16x16x32_bf16 v[216:219], a[28:31], v[40:43], v[216:219]\n"
".Lic3%=:\n"
"s_cbranch_execz .Lib0%=\n"
"buffer_load_dwordx4 v191, s[8:11], s1 offen lds\n"
"ds_read_b64_tr_b16 v[120:121], v182 offset:13576\n"
"v_mfma_f32_16x16x32_bf16 v[216:219], a[40:43], v[44:47], v[216:219]\n"
"s_add_u32 s1, s1, s33\n"
"ds_read_b64_tr_b16 v[122:123], v183 offset:13576\n"
"v_mfma_f32_16x16x32_bf16 v[216:219], a[52:55], v[48:51], v[216:219]\n"
"s_waitcnt lgkmcnt(10)\n"
"ds_read_b64_tr_b16 v[124:125], v182 offset:14344\n"
"v_mfma_f32_16x16x32_bf16 v[216:219], a[64:67], v[52:55], v[216:219]\n"
"ds_read_b64_tr_b16 v[126:127], v183 offset:14344\n"
"v_mfma_f32_16x16x32_bf16 v[212:215], a[48:51], v[48:51], v[212:215]\n"
"s_waitcnt lgkmcnt(8)\n"
"ds_read_b128 v[136:139], v178\n"
"v_mfma_f32_16x16x32_bf16 v[212:215], a[60:63], v[52:55], v[212:215]\n"
"ds_read_b128 v[112:115], v179\n"
"v_smfmac_f32_16x16x64_bf16 v[252:255], v[90:93], v[244:251], v66\n"
"ds_read_b128 v[140:143], v178 offset:13504\n"
"v_mfma_f32_16x16x32_bf16 v[220:223], a[8:11], v[32:35], 0\n"
"ds_read_b128 v[116:119], v179 offset:13504\n"
"v_mfma_f32_16x16x32_bf16 v[220:223], a[20:23], v[36:39], v[220:223]\n"
"s_waitcnt lgkmcnt(8)\n"
"s_nop 0\n"
"v_cvt_pk_bf16_f32 v212, v212, v213\n"
"v_mfma_f32_16x16x32_bf16 v[220:223], a[32:35], v[40:43], v[220:223]\n"
"s_add_u32 m0, s34, 0\n"
"ds_read_b128 v[244:247], v178 offset:27008\n"
"buffer_load_dwordx4 v190, s[8:11], s32 offen lds\n"
"v_mfma_f32_16x16x32_bf16 v[220:223], a[44:47], v[44:47], v[220:223]\n"
"v_cvt_pk_bf16_f32 v252, v252, v253\n"
"v_cvt_pk_bf16_f32 v253, v254, v255\n"
"v_mfma_f32_16x16x32_bf16 v[220:223], a[56:59], v[48:51], v[220:223]\n"
"v_cvt_pk_bf16_f32 v213, v214, v215\n"
"v_cvt_pk_bf16_f32 v214, v216, v217\n"
"v_mfma_f32_16x16x32_bf16 v[220:223], a[68:71], v[52:55], v[220:223]\n"
"s_waitcnt lgkmcnt(5)\n"
"v_cvt_pk_bf16_f32 v215, v218, v219\n"
"v_smfmac_f32_16x16x64_bf16 v[98:101], v[90:93], v[128:135], v66\n"
"ds_read_b128 v[128:131], v179 offset:27008\n"
"v_smfmac_f32_16x16x64_bf16 v[106:109], v[90:93], v[120:127], v66\n"
"v_mfma_f32_16x16x32_bf16 v[90:93], v[172:175], v[90:93], v[82:85]\n"
"v_mfma_f32_16x16x32_bf16 v[224:227], a[72:75], v[32:35], 0\n"
"s_nop 1\n"
"ds_read_b64 v[82:83], v178 offset:64\n"
"ds_read_b64 v[84:85], v179 offset:64\n"
"v_mfma_f32_16x16x32_bf16 v[224:227], a[84:87], v[36:39], v[224:227]\n"
"v_cvt_pk_bf16_f32 v220, v220, v221\n"
"v_cvt_pk_bf16_f32 v98, v98, v99\n"
"v_mfma_f32_16x16x32_bf16 v[224:227], a[96:99], v[40:43], v[224:227]\n"
"s_add_u32 m0, s34, 3072\n"
"ds_read_b64 v[92:93], v179 offset:13568\n"
"buffer_load_dwordx4 v191, s[8:11], s32 offen lds\n"
"v_mfma_f32_16x16x32_bf16 v[224:227], a[108:111], v[44:47], v[224:227]\n"
"s_add_u32 s32, s32, s33\n"
"v_rcp_f32 v171, v90\n"
"v_mfma_f32_16x16x32_bf16 v[224:227], a[120:123], v[48:51], v[224:227]\n"
"s_waitcnt vmcnt(12)\n"
"ds_read_b64 v[90:91], v178 offset:13568\n"
"v_cvt_pk_bf16_f32 v171, v171, v171\n"
"v_mfma_f32_16x16x32_bf16 v[224:227], a[132:135], v[52:55], v[224:227]\n"
"s_barrier\n"
"v_mfma_f32_16x16x32_bf16 v[228:231], a[76:79], v[32:35], 0\n"
"ds_read_b128 v[248:251], v180\n"
"ds_read_b128 v[132:135], v181\n"
"v_mfma_f32_16x16x32_bf16 v[228:231], a[88:91], v[36:39], v[228:231]\n"
"ds_write_b128 v186, v[212:215] offset:60768\n"
"v_mfma_f32_16x16x32_bf16 v[228:231], a[100:103], v[40:43], v[228:231]\n"
"s_waitcnt lgkmcnt(5)\n"
"v_and_b32 v64, v171, v176\n"
"v_and_b32 v65, v171, v177\n"
"v_mfma_f32_16x16x32_bf16 v[136:139], v[136:139], v[144:147], 0\n"
"s_waitcnt lgkmcnt(4)\n"
"v_cvt_pk_bf16_f32 v106, v106, v107\n"
"v_cvt_pk_bf16_f32 v99, v100, v101\n"
"v_mfma_f32_16x16x32_bf16 v[136:139], v[80:83], v[156:159], v[136:139]\n"
"s_add_u32 m0, s79, 142464\n"
"v_cvt_pk_bf16_f32 v107, v108, v109\n"
"v_cvt_pk_bf16_f32 v221, v222, v223\n"
"v_mfma_f32_16x16x32_bf16 v[112:115], v[112:115], v[144:147], 0\n"
"s_waitcnt lgkmcnt(3)\n"
"buffer_load_dwordx4 v192, s[16:19], s43 offen lds\n"
"ds_read_b64_tr_b16 v[120:121], v182 offset:27136\n"
"v_mfma_f32_16x16x32_bf16 v[112:115], v[84:87], v[158:161], v[112:115]\n"
"ds_read_b64_tr_b16 v[122:123], v183 offset:27136\n"
"ds_read_b64_tr_b16 v[124:125], v182 offset:27904\n"
"v_mfma_f32_16x16x32_bf16 v[140:143], v[140:143], v[144:147], 0\n"
"v_cvt_pk_bf16_f32 v224, v224, v225\n"
"v_cvt_pk_bf16_f32 v225, v226, v227\n"
"v_mfma_f32_16x16x32_bf16 v[140:143], v[88:91], v[156:159], v[140:143]\n"
"ds_read_b64_tr_b16 v[126:127], v183 offset:27904\n"
"v_mfma_f32_16x16x32_bf16 v[116:119], v[116:119], v[144:147], 0\n"
"v_max_f32 v136, v136, v112\n"
"v_max_f32 v137, v137, v113\n"
"v_mfma_f32_16x16x32_bf16 v[116:119], v[92:95], v[158:161], v[116:119]\n"
"v_max_f32 v138, v138, v114\n"
"v_max_f32 v139, v139, v115\n"
"v_mfma_f32_16x16x16_bf16 v[252:255], v[252:253], v[64:65], 0\n"
"ds_read_b64_tr_b16 v[112:113], v182 offset:27144\n"
"v_max3_f32 v170, v136, v137, v138\n"
"v_mfma_f32_16x16x16_bf16 v[98:101], v[98:99], v[64:65], 0\n"
"ds_read_b64_tr_b16 v[114:115], v183 offset:27144\n"
"v_mfma_f32_16x16x16_bf16 v[106:109], v[106:107], v[64:65], 0\n"
"v_max_f32 v140, v140, v116\n"
"v_max_f32 v141, v141, v117\n"
"v_mfma_f32_16x16x32_bf16 v[228:231], a[112:115], v[44:47], v[228:231]\n"
"s_add_u32 m0, s79, 142464\n"
"v_cvt_pk_bf16_f32 v252, v252, v253\n"
"v_cvt_pk_bf16_f32 v253, v254, v255\n"
"v_mfma_f32_16x16x32_bf16 v[228:231], a[124:127], v[48:51], v[228:231]\n"
"buffer_load_dwordx4 v192, s[16:19], s43 offen offset:1024 lds\n"
"v_cvt_pk_bf16_f32 v254, v98, v99\n"
"v_mfma_f32_16x16x32_bf16 v[228:231], a[136:139], v[52:55], v[228:231]\n"
"ds_read_b64 v[98:99], v178 offset:27072\n"
"v_cvt_pk_bf16_f32 v255, v100, v101\n"
"v_mfma_f32_16x16x32_bf16 v[204:207], a[80:83], v[32:35], 0\n"
"ds_read_b64 v[100:101], v179 offset:27072\n"
"v_cvt_pk_bf16_f32 v106, v106, v107\n"
"v_mfma_f32_16x16x32_bf16 v[204:207], a[92:95], v[36:39], v[204:207]\n"
"buffer_store_dwordx4 v[252:255], v192, s[28:31], s37 offen sc1\n"
"v_mfma_f32_16x16x32_bf16 v[204:207], a[104:107], v[40:43], v[204:207]\n"
"s_waitcnt vmcnt(10)\n"
"v_readfirstlane_b32 s6, v201\n"
"v_cvt_pk_bf16_f32 v107, v108, v109\n"
"v_mfma_f32_16x16x32_bf16 v[204:207], a[116:119], v[44:47], v[204:207]\n"
"s_and_b32 s6, s6, 0xffffff00\n"
"v_or_b32 v201, 5, s6\n"
"v_max_f32 v142, v142, v118\n"
"v_mfma_f32_16x16x32_bf16 v[204:207], a[128:131], v[48:51], v[204:207]\n"
"s_waitcnt vmcnt(9)\n"
"ds_read_b64 v[108:109], v181 offset:64\n"
"s_mov_b64 exec, 1\n"
"buffer_store_dword v201, off, s[28:31], s40 sc1\n"
"s_mov_b64 exec, -1\n"
"v_mfma_f32_16x16x32_bf16 v[204:207], a[140:143], v[52:55], v[204:207]\n"
"s_waitcnt lgkmcnt(2)\n"
"ds_write_b128 v202, v[56:59] offset:55296\n"
"v_mfma_f32_16x16x32_bf16 v[244:247], v[244:247], v[144:147], 0\n"
"s_waitcnt lgkmcnt(2)\n"
"buffer_load_dwordx4 v[56:59], v192, s[28:31], s38 offen sc1\n"
"v_max3_f32 v170, v170, v139, v140\n"
"v_mfma_f32_16x16x32_bf16 v[244:247], v[96:99], v[156:159], v[244:247]\n"
"ds_write_b64 v203, v[60:61] offset:55296\n"
"v_mfma_f32_16x16x32_bf16 v[128:131], v[128:131], v[144:147], 0\n"
"s_add_u32 m0, s79, 142464\n"
"v_max_f32 v143, v143, v119\n"
"v_max3_f32 v170, v170, v141, v142\n"
"v_mfma_f32_16x16x32_bf16 v[128:131], v[100:103], v[158:161], v[128:131]\n"
"buffer_load_dwordx4 v192, s[16:19], s43 offen offset:2048 lds\n"
"ds_read_b64_tr_b16 v[116:117], v182 offset:27912\n"
"v_mfma_f32_16x16x32_bf16 v[208:211], a[144:147], v[32:35], 0\n"
"s_waitcnt lgkmcnt(3)\n"
"ds_read_b64_tr_b16 v[118:119], v183 offset:27912\n"
"v_mov_b64 v[252:253], 0\n"
"v_mfma_f32_16x16x32_bf16 v[132:135], v[132:135], v[144:147], 0\n"
"v_mov_b64 v[254:255], 0\n"
"v_max_f32 v244, v244, v128\n"
"v_mfma_f32_16x16x32_bf16 v[132:135], v[108:111], v[158:161], v[132:135]\n"
"v_max_f32 v245, v245, v129\n"
"v_max_f32 v246, v246, v130\n"
"v_mfma_f32_16x16x32_bf16 v[208:211], a[156:159], v[36:39], v[208:211]\n"
"buffer_load_dwordx2 v[60:61], v193, s[28:31], s38 offen offset:1024 sc1\n"
"v_max3_f32 v170, v170, v143, v244\n"
"v_mfma_f32_16x16x32_bf16 v[208:211], a[168:171], v[40:43], v[208:211]\n"
"s_add_u32 s38, s38, 1572864\n"
"v_max_f32 v247, v247, v131\n"
"v_max3_f32 v170, v170, v245, v246\n"
"v_mfma_f32_16x16x32_bf16 v[208:211], a[180:183], v[44:47], v[208:211]\n"
"ds_read_b64_tr_b16 v[128:129], v182 offset:27080\n"
"ds_read_b64_tr_b16 v[130:131], v183 offset:27080\n"
"v_mfma_f32_16x16x32_bf16 v[208:211], a[192:195], v[48:51], v[208:211]\n"
"v_cvt_pk_bf16_f32 v226, v228, v229\n"
"v_cvt_pk_bf16_f32 v227, v230, v231\n"
"v_mfma_f32_16x16x32_bf16 v[208:211], a[204:207], v[52:55], v[208:211]\n"
"buffer_store_dwordx2 v[106:107], v193, s[28:31], s37 offen offset:1024 sc1\n"
"v_mfma_f32_16x16x32_bf16 v[216:219], a[148:151], v[32:35], 0\n"
"s_add_u32 s37, s37, 1572864\n"
"ds_read_b64 v[106:107], v180 offset:64\n"
"ds_write_b128 v186, v[224:227] offset:60896\n"
"v_mfma_f32_16x16x32_bf16 v[216:219], a[160:163], v[36:39], v[216:219]\n"
"s_add_u32 m0, s34, 1024\n"
"v_cvt_pk_bf16_f32 v222, v204, v205\n"
"v_cvt_pk_bf16_f32 v223, v206, v207\n"
"v_mfma_f32_16x16x32_bf16 v[216:219], a[172:175], v[40:43], v[216:219]\n"
"buffer_load_dwordx4 v190, s[8:11], s0 offen lds\n"
"ds_write_b128 v186, v[220:223] offset:60832\n"
"v_mfma_f32_16x16x32_bf16 v[216:219], a[184:187], v[44:47], v[216:219]\n"
"v_mov_b64 v[98:99], 0\n"
"v_mov_b64 v[100:101], 0\n"
"v_mfma_f32_16x16x32_bf16 v[216:219], a[196:199], v[48:51], v[216:219]\n"
"v_mov_b64 v[108:109], 0\n"
"v_mfma_f32_16x16x32_bf16 v[216:219], a[208:211], v[52:55], v[216:219]\n"
"v_mfma_f32_16x16x32_bf16 v[212:215], a[152:155], v[32:35], 0\n"
"s_waitcnt lgkmcnt(2)\n"
"ds_read_b128 v[32:35], v188 offset:6144\n"
"v_mfma_f32_16x16x32_bf16 v[248:251], v[248:251], v[144:147], 0\n"
"v_cvt_pk_bf16_f32 v144, v208, v209\n"
"v_cvt_pk_bf16_f32 v145, v210, v211\n"
"v_mfma_f32_16x16x32_bf16 v[248:251], v[104:107], v[156:159], v[248:251]\n"
"v_mov_b64 v[106:107], 0\n"
"v_cvt_pk_bf16_f32 v146, v216, v217\n"
"v_mfma_f32_16x16x32_bf16 v[212:215], a[164:167], v[36:39], v[212:215]\n"
"ds_read_b128 v[36:39], v189 offset:6144\n"
"v_cvt_pk_bf16_f32 v147, v218, v219\n"
"v_mfma_f32_16x16x32_bf16 v[212:215], a[176:179], v[40:43], v[212:215]\n"
"s_waitcnt vmcnt(13)\n"
"ds_read_b128 v[40:43], v188 offset:7168\n"
"v_max_f32 v248, v248, v132\n"
"v_mfma_f32_16x16x32_bf16 v[212:215], a[188:191], v[44:47], v[212:215]\n"
"s_add_u32 m0, s34, 4096\n"
"ds_read_b128 v[44:47], v189 offset:7168\n"
"buffer_load_dwordx4 v191, s[8:11], s0 offen lds\n"
"v_mfma_f32_16x16x32_bf16 v[212:215], a[200:203], v[48:51], v[212:215]\n"
"s_add_u32 s0, s0, s33\n"
"v_max_f32 v249, v249, v133\n"
"v_max_f32 v250, v250, v134\n"
"v_mfma_f32_16x16x32_bf16 v[212:215], a[212:215], v[52:55], v[212:215]\n"
"v_max3_f32 v170, v170, v247, v248\n"
"v_max_f32 v251, v251, v135\n"
"v_max3_f32 v170, v170, v249, v250\n"
"v_max_f32 v171, v170, v251\n"
"v_max_f32 v170, v170, v251\n"
"s_waitcnt lgkmcnt(3)\n"
"v_mfma_f32_16x16x32_bf16 v[228:231], a[0:3], v[32:35], 0\n"
"v_permlane16_swap_b32 v170, v171\n"
"v_mfma_f32_16x16x32_bf16 v[204:207], a[4:7], v[32:35], 0\n"
"ds_read_b64_tr_b16 v[132:133], v182 offset:27848\n"
"v_max_f32 v170, v170, v171\n"
"v_mfma_f32_16x16x32_bf16 v[220:223], a[8:11], v[32:35], 0\n"
"s_waitcnt lgkmcnt(3)\n"
"ds_read_b64_tr_b16 v[134:135], v183 offset:27848\n"
"v_mov_b32 v171, v170\n"
"v_mfma_f32_16x16x32_bf16 v[228:231], a[12:15], v[36:39], v[228:231]\n"
"s_waitcnt lgkmcnt(3)\n"
"v_permlane32_swap_b32 v170, v171\n"
"v_max_f32 v170, v170, v171\n"
"v_mul_f32 v170, 0xbe553b95, v170\n"
"v_pk_fma_f32 v[136:137], v[136:137], s[70:71], v[170:171] op_sel_hi:[1,0,0]\n"
"v_pk_fma_f32 v[138:139], v[138:139], s[70:71], v[170:171] op_sel_hi:[1,0,0]\n"
"v_pk_fma_f32 v[140:141], v[140:141], s[70:71], v[170:171] op_sel_hi:[1,0,0]\n"
"v_pk_fma_f32 v[142:143], v[142:143], s[70:71], v[170:171] op_sel_hi:[1,0,0]\n"
"v_pk_fma_f32 v[244:245], v[244:245], s[70:71], v[170:171] op_sel_hi:[1,0,0]\n"
"v_pk_fma_f32 v[246:247], v[246:247], s[70:71], v[170:171] op_sel_hi:[1,0,0]\n"
"v_pk_fma_f32 v[248:249], v[248:249], s[70:71], v[170:171] op_sel_hi:[1,0,0]\n"
"v_pk_fma_f32 v[250:251], v[250:251], s[70:71], v[170:171] op_sel_hi:[1,0,0]\n"
"v_mfma_f32_16x16x32_bf16 v[228:231], a[24:27], v[40:43], v[228:231]\n"
"s_waitcnt lgkmcnt(2)\n"
"v_exp_f32 v136, v136\n"
"v_mfma_f32_16x16x32_bf16 v[228:231], a[36:39], v[44:47], v[228:231]\n"
"s_add_u32 m0, s34, 2048\n"
"v_exp_f32 v137, v137\n"
"v_mfma_f32_16x16x32_bf16 v[204:207], a[16:19], v[36:39], v[204:207]\n"
"buffer_load_dwordx4 v190, s[8:11], s1 offen lds\n"
"v_exp_f32 v138, v138\n"
"v_mfma_f32_16x16x32_bf16 v[204:207], a[28:31], v[40:43], v[204:207]\n"
"v_exp_f32 v139, v139\n"
"v_mfma_f32_16x16x32_bf16 v[204:207], a[40:43], v[44:47], v[204:207]\n"
"v_exp_f32 v140, v140\n"
"v_mfma_f32_16x16x32_bf16 v[220:223], a[20:23], v[36:39], v[220:223]\n"
"v_exp_f32 v141, v141\n"
"v_mfma_f32_16x16x32_bf16 v[220:223], a[32:35], v[40:43], v[220:223]\n"
"v_exp_f32 v142, v142\n"
"v_mfma_f32_16x16x32_bf16 v[220:223], a[44:47], v[44:47], v[220:223]\n"
"v_exp_f32 v143, v143\n"
"v_mfma_f32_16x16x32_bf16 v[224:227], a[72:75], v[32:35], 0\n"
"v_cvt_pk_bf16_f32 v82, v136, v138\n"
"v_cvt_pk_bf16_f32 v83, v137, v139\n"
"v_mfma_f32_16x16x32_bf16 v[224:227], a[84:87], v[36:39], v[224:227]\n"
"v_cvt_pk_bf16_f32 v84, v140, v142\n"
"v_cvt_pk_bf16_f32 v85, v141, v143\n"
"v_mfma_f32_16x16x32_bf16 v[224:227], a[96:99], v[40:43], v[224:227]\n"
"ds_read_b128 v[136:139], v178 offset:13504\n"
"ds_read_b128 v[140:143], v178 offset:27008\n"
"v_smfmac_f32_16x16x64_bf16 v[252:255], v[82:85], v[8:15], v66\n"
"s_add_u32 m0, s34, 5120\n"
"s_nop 0\n"
"buffer_load_dwordx4 v191, s[8:11], s1 offen lds\n"
"v_smfmac_f32_16x16x64_bf16 v[98:101], v[82:85], v[16:23], v66\n"
"s_add_u32 s1, s1, s33\n"
"ds_read_b64_tr_b16 v[8:9], v236\n"
"v_smfmac_f32_16x16x64_bf16 v[106:109], v[82:85], v[24:31], v66\n"
"ds_read_b64_tr_b16 v[10:11], v236 offset:6144\n"
"v_mfma_f32_16x16x32_bf16 v[82:85], v[172:175], v[82:85], 0\n"
"v_exp_f32 v244, v244\n"
"v_mfma_f32_16x16x32_bf16 v[224:227], a[108:111], v[44:47], v[224:227]\n"
"s_waitcnt vmcnt(14)\n"
"v_exp_f32 v245, v245\n"
"v_mfma_f32_16x16x32_bf16 v[208:211], a[76:79], v[32:35], 0\n"
"ds_read_b128 v[48:51], v188 offset:8192\n"
"ds_read_b128 v[52:55], v189 offset:8192\n"
"v_mfma_f32_16x16x32_bf16 v[208:211], a[88:91], v[36:39], v[208:211]\n"
"v_exp_f32 v246, v246\n"
"v_mfma_f32_16x16x32_bf16 v[208:211], a[100:103], v[40:43], v[208:211]\n"
"ds_read_b64_tr_b16 v[12:13], v236 offset:8\n"
"v_cvt_pk_bf16_f32 v90, v244, v246\n"
"v_mfma_f32_16x16x32_bf16 v[208:211], a[112:115], v[44:47], v[208:211]\n"
"v_exp_f32 v247, v247\n"
"v_mfma_f32_16x16x32_bf16 v[216:219], a[80:83], v[32:35], 0\n"
"s_add_u32 m0, s34, 6144\n"
"ds_read_b64_tr_b16 v[14:15], v236 offset:6152\n"
"buffer_load_dwordx4 v190, s[8:11], s32 offen lds\n"
"v_mfma_f32_16x16x32_bf16 v[216:219], a[92:95], v[36:39], v[216:219]\n"
"v_exp_f32 v248, v248\n"
"v_mfma_f32_16x16x32_bf16 v[216:219], a[104:107], v[40:43], v[216:219]\n"
"ds_read_b64_tr_b16 v[16:17], v235\n"
"v_cvt_pk_bf16_f32 v91, v245, v247\n"
"v_mfma_f32_16x16x32_bf16 v[216:219], a[116:119], v[44:47], v[216:219]\n"
"v_exp_f32 v249, v249\n"
"v_exp_f32 v250, v250\n"
"v_cvt_pk_bf16_f32 v158, v212, v213\n"
"v_exp_f32 v251, v251\n"
"v_cvt_pk_bf16_f32 v92, v248, v250\n"
"v_cvt_pk_bf16_f32 v93, v249, v251\n"
"v_cvt_pk_bf16_f32 v159, v214, v215\n"
"v_mfma_f32_16x16x32_bf16 v[212:215], a[144:147], v[32:35], 0\n"
"s_waitcnt lgkmcnt(4)\n"
"ds_read_b64_tr_b16 v[18:19], v235 offset:6144\n"
"v_smfmac_f32_16x16x64_bf16 v[252:255], v[90:93], v[120:127], v66\n"
"ds_read_b128 v[244:247], v179 offset:13504\n"
"v_smfmac_f32_16x16x64_bf16 v[98:101], v[90:93], v[112:119], v66\n"
"ds_read_b128 v[248:251], v179 offset:27008\n"
"v_smfmac_f32_16x16x64_bf16 v[106:109], v[90:93], v[128:135], v66\n"
"s_waitcnt lgkmcnt(6)\n"
"ds_read_b128 v[120:123], v180\n"
"v_mfma_f32_16x16x32_bf16 v[90:93], v[172:175], v[90:93], v[82:85]\n"
"s_add_u32 m0, s34, 9216\n"
"ds_read_b128 v[112:115], v181\n"
"buffer_load_dwordx4 v191, s[8:11], s32 offen lds\n"
"v_mfma_f32_16x16x32_bf16 v[228:231], a[48:51], v[48:51], v[228:231]\n"
"s_add_u32 s32, s32, s33\n"
"ds_read_b64 v[82:83], v178 offset:13568\n"
"v_cvt_pk_bf16_f32 v252, v252, v253\n"
"v_mfma_f32_16x16x32_bf16 v[228:231], a[60:63], v[52:55], v[228:231]\n"
"ds_read_b64 v[84:85], v179 offset:13568\n"
"v_cvt_pk_bf16_f32 v253, v254, v255\n"
"v_mfma_f32_16x16x32_bf16 v[204:207], a[52:55], v[48:51], v[204:207]\n"
"ds_read_b64 v[92:93], v179 offset:27072\n"
"v_cvt_pk_bf16_f32 v98, v98, v99\n"
"v_mfma_f32_16x16x32_bf16 v[204:207], a[64:67], v[52:55], v[204:207]\n"
"v_rcp_f32 v171, v90\n"
"v_mfma_f32_16x16x32_bf16 v[220:223], a[56:59], v[48:51], v[220:223]\n"
"ds_read_b64 v[90:91], v178 offset:27072\n"
"v_cvt_pk_bf16_f32 v171, v171, v171\n"
"v_mfma_f32_16x16x32_bf16 v[220:223], a[68:71], v[52:55], v[220:223]\n"
"v_and_b32 v64, v171, v176\n"
"v_and_b32 v65, v171, v177\n"
"v_mfma_f32_16x16x32_bf16 v[224:227], a[120:123], v[48:51], v[224:227]\n"
"v_cvt_pk_bf16_f32 v99, v100, v101\n"
"v_cvt_pk_bf16_f32 v106, v106, v107\n"
"v_mfma_f32_16x16x16_bf16 v[252:255], v[252:253], v[64:65], 0\n"
"v_cvt_pk_bf16_f32 v107, v108, v109\n"
"v_cvt_pk_bf16_f32 v228, v228, v229\n"
"v_mfma_f32_16x16x16_bf16 v[98:101], v[98:99], v[64:65], 0\n"
"s_add_u32 m0, s34, 7168\n"
"ds_read_b64_tr_b16 v[128:129], v182 offset:13632\n"
"buffer_load_dwordx4 v190, s[8:11], s0 offen lds\n"
"v_mfma_f32_16x16x16_bf16 v[106:109], v[106:107], v[64:65], 0\n"
"s_waitcnt lgkmcnt(4)\n"
"v_cvt_pk_bf16_f32 v252, v252, v253\n"
"v_cvt_pk_bf16_f32 v253, v254, v255\n"
"v_mfma_f32_16x16x32_bf16 v[136:139], v[136:139], v[148:151], 0\n"
"s_waitcnt lgkmcnt(3)\n"
"v_cvt_pk_bf16_f32 v254, v98, v99\n"
"v_cvt_pk_bf16_f32 v255, v100, v101\n"
"v_mfma_f32_16x16x32_bf16 v[136:139], v[80:83], v[160:163], v[136:139]\n"
"s_waitcnt lgkmcnt(2)\n"
"ds_read_b64 v[98:99], v180 offset:64\n"
"buffer_store_dwordx4 v[252:255], v192, s[28:31], s37 offen sc1\n"
"v_mfma_f32_16x16x32_bf16 v[244:247], v[244:247], v[148:151], 0\n"
"s_waitcnt lgkmcnt(2)\n"
"ds_read_b64 v[100:101], v181 offset:64\n"
"v_cvt_pk_bf16_f32 v106, v106, v107\n"
"v_mfma_f32_16x16x32_bf16 v[244:247], v[84:87], v[162:165], v[244:247]\n"
"v_cvt_pk_bf16_f32 v107, v108, v109\n"
"v_cvt_pk_bf16_f32 v229, v230, v231\n"
"v_mfma_f32_16x16x32_bf16 v[140:143], v[140:143], v[148:151], 0\n"
"ds_read_b64_tr_b16 v[130:131], v183 offset:13632\n"
"buffer_store_dwordx2 v[106:107], v193, s[28:31], s37 offen offset:1024 sc1\n"
"v_mfma_f32_16x16x32_bf16 v[140:143], v[88:91], v[160:163], v[140:143]\n"
"s_add_u32 s37, s37, 1572864\n"
"ds_read_b64_tr_b16 v[132:133], v182 offset:14400\n"
"ds_read_b64_tr_b16 v[134:135], v183 offset:14400\n"
"v_mfma_f32_16x16x32_bf16 v[248:251], v[248:251], v[148:151], 0\n"
"s_add_u32 s4, s37, 6291456\n"
"v_cvt_pk_bf16_f32 v230, v204, v205\n"
"v_cvt_pk_bf16_f32 v231, v206, v207\n"
"v_mfma_f32_16x16x32_bf16 v[248:251], v[92:95], v[162:165], v[248:251]\n"
"s_waitcnt lgkmcnt(4)\n"
"v_cvt_pk_bf16_f32 v220, v220, v221\n"
"v_cvt_pk_bf16_f32 v221, v222, v223\n"
"v_mfma_f32_16x16x32_bf16 v[120:123], v[120:123], v[148:151], 0\n"
"s_add_u32 m0, s34, 10240\n"
"v_max_f32 v136, v136, v244\n"
"v_max_f32 v137, v137, v245\n"
"v_mfma_f32_16x16x32_bf16 v[120:123], v[96:99], v[160:163], v[120:123]\n"
"s_waitcnt lgkmcnt(3)\n"
"buffer_load_dwordx4 v191, s[8:11], s0 offen lds\n"
"ds_read_b64_tr_b16 v[244:245], v182 offset:13640\n"
"v_mfma_f32_16x16x32_bf16 v[112:115], v[112:115], v[148:151], 0\n"
"s_add_u32 s0, s0, s33\n"
"v_max_f32 v138, v138, v246\n"
"v_max_f32 v139, v139, v247\n"
"v_mfma_f32_16x16x32_bf16 v[112:115], v[100:103], v[162:165], v[112:115]\n"
"ds_read_b64_tr_b16 v[246:247], v183 offset:13640\n"
"v_max3_f32 v170, v136, v137, v138\n"
"v_mfma_f32_16x16x32_bf16 v[224:227], a[132:135], v[52:55], v[224:227]\n"
"buffer_load_dword v200, off, s[28:31], s39 sc1\n"
"v_max_f32 v140, v140, v248\n"
"v_mfma_f32_16x16x32_bf16 v[208:211], a[124:127], v[48:51], v[208:211]\n"
"v_max_f32 v141, v141, v249\n"
"v_max_f32 v142, v142, v250\n"
"v_mfma_f32_16x16x32_bf16 v[208:211], a[136:139], v[52:55], v[208:211]\n"
"ds_read_b64_tr_b16 v[248:249], v182 offset:14408\n"
"v_max_f32 v143, v143, v251\n"
"v_mfma_f32_16x16x32_bf16 v[216:219], a[128:131], v[48:51], v[216:219]\n"
"s_waitcnt vmcnt(13)\n"
"ds_read_b64_tr_b16 v[250:251], v183 offset:14408\n"
"v_cvt_pk_bf16_f32 v224, v224, v225\n"
"v_mfma_f32_16x16x32_bf16 v[216:219], a[140:143], v[52:55], v[216:219]\n"
"s_barrier\n"
"v_mfma_f32_16x16x32_bf16 v[212:215], a[156:159], v[36:39], v[212:215]\n"
"ds_read_b128 v[76:79], v192 offset:55296\n"
"ds_read_b128 v[124:127], v180 offset:13504\n"
"v_mfma_f32_16x16x32_bf16 v[212:215], a[168:171], v[40:43], v[212:215]\n"
"s_add_u32 m0, s34, 8192\n"
"ds_read_b64 v[106:107], v180 offset:13568\n"
"buffer_load_dwordx4 v190, s[8:11], s1 offen lds\n"
"v_mfma_f32_16x16x32_bf16 v[212:215], a[180:183], v[44:47], v[212:215]\n"
"ds_write_b128 v187, v[228:231]\n"
"v_mfma_f32_16x16x32_bf16 v[212:215], a[192:195], v[48:51], v[212:215]\n"
"v_cvt_pk_bf16_f32 v225, v226, v227\n"
"v_cvt_pk_bf16_f32 v226, v208, v209\n"
"v_mfma_f32_16x16x32_bf16 v[212:215], a[204:207], v[52:55], v[212:215]\n"
"s_waitcnt vmcnt(13)\n"
"ds_read_b128 v[116:119], v181 offset:13504\n"
"ds_read_b64 v[108:109], v181 offset:13568\n"
"v_mfma_f32_16x16x32_bf16 v[204:207], a[148:151], v[32:35], 0\n"
"v_max3_f32 v170, v170, v139, v140\n"
"v_max3_f32 v170, v170, v141, v142\n"
"v_mfma_f32_16x16x32_bf16 v[204:207], a[160:163], v[36:39], v[204:207]\n"
"ds_read_b64_tr_b16 v[20:21], v236 offset:12288\n"
"v_cvt_pk_bf16_f32 v227, v210, v211\n"
"v_mfma_f32_16x16x32_bf16 v[204:207], a[172:175], v[40:43], v[204:207]\n"
"s_waitcnt lgkmcnt(6)\n"
"v_mov_b64 v[68:69], -1\n"
"v_mov_b64 v[70:71], -1\n"
"v_mfma_f32_16x16x32_bf16 v[240:243], a[224:227], v[76:79], 0\n"
"buffer_store_dwordx4 v[68:71], v192, s[28:31], s4 offen sc1\n"
"v_mfma_f32_16x16x32_bf16 v[204:207], a[184:187], v[44:47], v[204:207]\n"
"s_waitcnt lgkmcnt(4)\n"
"ds_read_b128 v[68:71], v192 offset:56320\n"
"ds_write_b128 v202, v[56:59] offset:36864\n"
"v_mfma_f32_16x16x32_bf16 v[124:127], v[124:127], v[148:151], 0\n"
"s_add_u32 m0, s34, 11264\n"
"v_cvt_pk_bf16_f32 v222, v216, v217\n"
"v_cvt_pk_bf16_f32 v223, v218, v219\n"
"v_mfma_f32_16x16x32_bf16 v[124:127], v[104:107], v[160:163], v[124:127]\n"
"s_waitcnt lgkmcnt(3)\n"
"buffer_load_dwordx4 v191, s[8:11], s1 offen lds\n"
"ds_read_b64_tr_b16 v[22:23], v236 offset:18432\n"
"v_mfma_f32_16x16x32_bf16 v[116:119], v[116:119], v[148:151], 0\n"
"s_add_u32 s1, s1, s33\n"
"v_max_f32 v120, v120, v112\n"
"v_max_f32 v121, v121, v113\n"
"v_mfma_f32_16x16x32_bf16 v[116:119], v[108:111], v[162:165], v[116:119]\n"
"s_add_u32 s4, s37, 6291456\n"
"ds_read_b64_tr_b16 v[24:25], v236 offset:12296\n"
"v_max_f32 v122, v122, v114\n"
"v_mfma_f32_16x16x32_bf16 v[204:207], a[196:199], v[48:51], v[204:207]\n"
"v_max_f32 v123, v123, v115\n"
"v_max3_f32 v170, v170, v143, v120\n"
"v_mfma_f32_16x16x32_bf16 v[204:207], a[208:211], v[52:55], v[204:207]\n"
"ds_read_b64_tr_b16 v[26:27], v236 offset:18440\n"
"v_max3_f32 v170, v170, v121, v122\n"
"v_mfma_f32_16x16x32_bf16 v[236:239], a[220:223], v[76:79], 0\n"
"s_waitcnt lgkmcnt(4)\n"
"v_max_f32 v124, v124, v116\n"
"v_max_f32 v125, v125, v117\n"
"v_mfma_f32_16x16x32_bf16 v[236:239], a[232:235], v[68:71], v[236:239]\n"
"ds_read_b64_tr_b16 v[28:29], v235 offset:12288\n"
"v_max_f32 v126, v126, v118\n"
"v_mfma_f32_16x16x32_bf16 v[240:243], a[236:239], v[68:71], v[240:243]\n"
"v_max_f32 v127, v127, v119\n"
"v_max3_f32 v170, v170, v123, v124\n"
"v_mfma_f32_16x16x32_bf16 v[228:231], a[152:155], v[32:35], 0\n"
"ds_read_b128 v[32:35], v188\n"
"ds_read_b64_tr_b16 v[30:31], v235 offset:18432\n"
"v_mfma_f32_16x16x32_bf16 v[232:235], a[216:219], v[76:79], 0\n"
"v_max3_f32 v170, v170, v125, v126\n"
"v_mov_b64 v[252:253], 0\n"
"v_mfma_f32_16x16x32_bf16 v[232:235], a[228:231], v[68:71], v[232:235]\n"
"v_mov_b64 v[254:255], 0\n"
"v_mov_b64 v[98:99], 0\n"
"v_mfma_f32_16x16x32_bf16 v[228:231], a[164:167], v[36:39], v[228:231]\n"
"ds_read_b128 v[76:79], v192 offset:58368\n"
"ds_read_b128 v[68:71], v192 offset:59392\n"
"v_mfma_f32_16x16x32_bf16 v[228:231], a[176:179], v[40:43], v[228:231]\n"
"v_mov_b64 v[100:101], 0\n"
"v_mov_b64 v[106:107], 0\n"
"v_mfma_f32_16x16x32_bf16 v[228:231], a[188:191], v[44:47], v[228:231]\n"
"ds_read_b128 v[36:39], v189\n"
"ds_write_b128 v187, v[220:223] offset:64\n"
"v_mfma_f32_16x16x32_bf16 v[228:231], a[200:203], v[48:51], v[228:231]\n"
"v_mov_b64 v[108:109], 0\n"
"v_max_f32 v171, v170, v127\n"
"v_mfma_f32_16x16x32_bf16 v[228:231], a[212:215], v[52:55], v[228:231]\n"
"s_waitcnt vmcnt(12) lgkmcnt(5)\n"
"ds_write_b64 v203, v[60:61] offset:36864\n"
"v_mfma_f32_16x16x32_bf16 v[208:211], a[0:3], v[32:35], 0\n"
"v_max_f32 v170, v170, v127\n"
"v_cvt_pk_bf16_f32 v148, v212, v213\n"
"v_mfma_f32_16x16x32_bf16 v[216:219], a[4:7], v[32:35], 0\n"
"ds_read_b128 v[40:43], v188 offset:1024\n"
"ds_read_b128 v[44:47], v189 offset:1024\n"
"v_mfma_f32_16x16x32_bf16 v[220:223], a[8:11], v[32:35], 0\n"
"ds_write_b128 v187, v[224:227] offset:128\n"
"v_mfma_f32_16x16x32_bf16 v[224:227], a[72:75], v[32:35], 0\n"
"s_waitcnt lgkmcnt(5)\n"
"v_permlane16_swap_b32 v170, v171\n"
"v_mfma_f32_16x16x32_bf16 v[208:211], a[12:15], v[36:39], v[208:211]\n"
"s_add_u32 m0, s34, 0\n"
"ds_read_b64_tr_b16 v[112:113], v182 offset:13576\n"
"buffer_load_dwordx4 v190, s[8:11], s32 offen lds\n"
"v_mfma_f32_16x16x32_bf16 v[216:219], a[16:19], v[36:39], v[216:219]\n"
"ds_read_b64_tr_b16 v[114:115], v183 offset:13576\n"
"ds_read_b64_tr_b16 v[116:117], v182 offset:14344\n"
"v_mfma_f32_16x16x32_bf16 v[220:223], a[20:23], v[36:39], v[220:223]\n"
"v_max_f32 v170, v170, v171\n"
"v_mov_b32 v171, v170\n"
"v_mfma_f32_16x16x32_bf16 v[224:227], a[84:87], v[36:39], v[224:227]\n"
"ds_read_b64_tr_b16 v[118:119], v183 offset:14344\n"
"v_permlane32_swap_b32 v170, v171\n"
"v_max_f32 v170, v170, v171\n"
"v_mul_f32 v170, 0xbe553b95, v170\n"
"v_pk_fma_f32 v[136:137], v[136:137], s[70:71], v[170:171] op_sel_hi:[1,0,0]\n"
"v_pk_fma_f32 v[138:139], v[138:139], s[70:71], v[170:171] op_sel_hi:[1,0,0]\n"
"v_pk_fma_f32 v[140:141], v[140:141], s[70:71], v[170:171] op_sel_hi:[1,0,0]\n"
"v_pk_fma_f32 v[142:143], v[142:143], s[70:71], v[170:171] op_sel_hi:[1,0,0]\n"
"v_pk_fma_f32 v[120:121], v[120:121], s[70:71], v[170:171] op_sel_hi:[1,0,0]\n"
"v_pk_fma_f32 v[122:123], v[122:123], s[70:71], v[170:171] op_sel_hi:[1,0,0]\n"
"v_pk_fma_f32 v[124:125], v[124:125], s[70:71], v[170:171] op_sel_hi:[1,0,0]\n"
"v_pk_fma_f32 v[126:127], v[126:127], s[70:71], v[170:171] op_sel_hi:[1,0,0]\n"
"v_exp_f32 v136, v136\n"
"v_exp_f32 v137, v137\n"
"v_exp_f32 v138, v138\n"
"v_exp_f32 v139, v139\n"
"v_cvt_pk_bf16_f32 v82, v136, v138\n"
"v_cvt_pk_bf16_f32 v83, v137, v139\n"
"v_exp_f32 v140, v140\n"
"v_exp_f32 v141, v141\n"
"v_exp_f32 v142, v142\n"
"v_exp_f32 v143, v143\n"
"v_cvt_pk_bf16_f32 v84, v140, v142\n"
"v_cvt_pk_bf16_f32 v85, v141, v143\n"
"v_exp_f32 v120, v120\n"
"s_nop 0\n"
"v_smfmac_f32_16x16x64_bf16 v[252:255], v[82:85], v[128:135], v66\n"
"s_waitcnt lgkmcnt(0)\n"
"ds_read_b64_tr_b16 v[136:137], v182 offset:40640\n"
"v_smfmac_f32_16x16x64_bf16 v[98:101], v[82:85], v[244:251], v66\n"
"s_add_u32 m0, s34, 3072\n"
"s_nop 0\n"
"buffer_load_dwordx4 v191, s[8:11], s32 offen lds\n"
"v_smfmac_f32_16x16x64_bf16 v[106:109], v[82:85], v[112:119], v66\n"
"s_add_u32 s32, s32, s33\n"
"ds_read_b64_tr_b16 v[138:139], v183 offset:40640\n"
"v_mfma_f32_16x16x32_bf16 v[82:85], v[172:175], v[82:85], 0\n"
"ds_read_b64_tr_b16 v[140:141], v182 offset:41408\n"
"ds_read_b64_tr_b16 v[142:143], v183 offset:41408\n"
"v_mfma_f32_16x16x32_bf16 v[208:211], a[24:27], v[40:43], v[208:211]\n"
"s_waitcnt vmcnt(12)\n"
"v_exp_f32 v121, v121\n"
"v_mfma_f32_16x16x32_bf16 v[208:211], a[36:39], v[44:47], v[208:211]\n"
"ds_read_b128 v[48:51], v188 offset:2048\n"
"ds_read_b128 v[52:55], v189 offset:2048\n"
"v_mfma_f32_16x16x32_bf16 v[216:219], a[28:31], v[40:43], v[216:219]\n"
"v_mov_b64 v[72:73], -1\n"
"v_cvt_pk_bf16_f32 v149, v214, v215\n"
"v_mfma_f32_16x16x32_bf16 v[216:219], a[40:43], v[44:47], v[216:219]\n"
"buffer_store_dwordx2 v[72:73], v193, s[28:31], s4 offen offset:1024 sc1\n"
"v_mfma_f32_16x16x32_bf16 v[220:223], a[32:35], v[40:43], v[220:223]\n"
"ds_read_b128 v[72:75], v192 offset:57344\n"
"ds_read_b64_tr_b16 v[128:129], v182 offset:40648\n"
"v_mfma_f32_16x16x32_bf16 v[220:223], a[44:47], v[44:47], v[220:223]\n"
"ds_read_b64_tr_b16 v[130:131], v183 offset:40648\n"
"v_cvt_pk_bf16_f32 v150, v204, v205\n"
"v_mfma_f32_16x16x32_bf16 v[224:227], a[96:99], v[40:43], v[224:227]\n"
"s_add_u32 m0, s34, 1024\n"
"ds_read_b64_tr_b16 v[132:133], v182 offset:41416\n"
"buffer_load_dwordx4 v190, s[8:11], s0 offen lds\n"
".Lic4%=:\n"
"s_cbranch_execz .Lib1%=\n"
"v_mfma_f32_16x16x32_bf16 v[224:227], a[108:111], v[44:47], v[224:227]\n"
"ds_read_b64_tr_b16 v[134:135], v183 offset:41416\n"
"v_cvt_pk_bf16_f32 v151, v206, v207\n"
"v_mfma_f32_16x16x32_bf16 v[212:215], a[76:79], v[32:35], 0\n"
"ds_read_b64_tr_b16 v[244:245], v182 offset:40584\n"
"v_cvt_pk_bf16_f32 v162, v228, v229\n"
"v_mfma_f32_16x16x32_bf16 v[212:215], a[88:91], v[36:39], v[212:215]\n"
"s_waitcnt lgkmcnt(7)\n"
"ds_read_b64_tr_b16 v[246:247], v183 offset:40584\n"
"v_cvt_pk_bf16_f32 v163, v230, v231\n"
"v_mfma_f32_16x16x32_bf16 v[208:211], a[48:51], v[48:51], v[208:211]\n"
"s_waitcnt lgkmcnt(7)\n"
"ds_read_b64_tr_b16 v[248:249], v182 offset:41352\n"
"v_exp_f32 v122, v122\n"
"v_mfma_f32_16x16x32_bf16 v[208:211], a[60:63], v[52:55], v[208:211]\n"
"s_waitcnt lgkmcnt(7)\n"
"ds_read_b64_tr_b16 v[250:251], v183 offset:41352\n"
"v_cvt_pk_bf16_f32 v90, v120, v122\n"
"v_mfma_f32_16x16x32_bf16 v[216:219], a[52:55], v[48:51], v[216:219]\n"
"ds_read_b128 v[112:115], v178 offset:27008\n"
"v_exp_f32 v123, v123\n"
"v_mfma_f32_16x16x32_bf16 v[232:235], a[240:243], v[72:75], v[232:235]\n"
"ds_read_b128 v[116:119], v180\n"
"v_cvt_pk_bf16_f32 v91, v121, v123\n"
"v_mfma_f32_16x16x32_bf16 v[232:235], a[252:255], v[76:79], v[232:235]\n"
"ds_read_b128 v[120:123], v179 offset:27008\n"
"v_exp_f32 v124, v124\n"
"v_mfma_f32_16x16x32_bf16 v[232:235], v[8:11], v[68:71], v[232:235]\n"
"s_add_u32 m0, s34, 4096\n"
"v_exp_f32 v125, v125\n"
"v_mfma_f32_16x16x32_bf16 v[236:239], a[244:247], v[72:75], v[236:239]\n"
"buffer_load_dwordx4 v191, s[8:11], s0 offen lds\n"
"v_exp_f32 v126, v126\n"
"v_mfma_f32_16x16x32_bf16 v[236:239], v[0:3], v[76:79], v[236:239]\n"
"s_add_u32 s0, s0, s33\n"
"v_exp_f32 v127, v127\n"
"v_mfma_f32_16x16x32_bf16 v[236:239], v[12:15], v[68:71], v[236:239]\n"
"v_cvt_pk_bf16_f32 v92, v124, v126\n"
"v_cvt_pk_bf16_f32 v93, v125, v127\n"
"v_mfma_f32_16x16x32_bf16 v[240:243], a[248:251], v[72:75], v[240:243]\n"
"s_waitcnt lgkmcnt(3)\n"
"ds_read_b128 v[72:75], v192 offset:60416\n"
"ds_read_b128 v[124:127], v181\n"
"v_mfma_f32_16x16x32_bf16 v[240:243], v[4:7], v[76:79], v[240:243]\n"
"v_cvt_pk_bf16_f32 v208, v208, v209\n"
"v_cvt_pk_bf16_f32 v209, v210, v211\n"
"v_mfma_f32_16x16x32_bf16 v[240:243], v[16:19], v[68:71], v[240:243]\n"
"v_smfmac_f32_16x16x64_bf16 v[252:255], v[90:93], v[136:143], v66\n"
"ds_read_b128 v[136:139], v180 offset:13504\n"
"v_smfmac_f32_16x16x64_bf16 v[98:101], v[90:93], v[128:135], v66\n"
"ds_read_b128 v[128:131], v181 offset:13504\n"
"v_smfmac_f32_16x16x64_bf16 v[106:109], v[90:93], v[244:251], v66\n"
"ds_read_b64_tr_b16 v[244:245], v182 offset:27136\n"
"v_mfma_f32_16x16x32_bf16 v[90:93], v[172:175], v[90:93], v[82:85]\n"
"s_add_u32 m0, s34, 2048\n"
"ds_read_b64_tr_b16 v[246:247], v183 offset:27136\n"
"buffer_load_dwordx4 v190, s[8:11], s1 offen lds\n"
"v_mfma_f32_16x16x32_bf16 v[216:219], a[64:67], v[52:55], v[216:219]\n"
"ds_read_b64 v[82:83], v178 offset:27072\n"
"ds_read_b64 v[84:85], v179 offset:27072\n"
"v_mfma_f32_16x16x32_bf16 v[220:223], a[56:59], v[48:51], v[220:223]\n"
"s_waitcnt lgkmcnt(7)\n"
"v_rcp_f32 v171, v90\n"
"v_mfma_f32_16x16x32_bf16 v[232:235], v[20:23], v[72:75], v[232:235]\n"
"ds_read_b64 v[90:91], v180 offset:64\n"
"ds_read_b64 v[92:93], v181 offset:64\n"
"v_mfma_f32_16x16x32_bf16 v[240:243], v[28:31], v[72:75], v[240:243]\n"
"v_cvt_pk_bf16_f32 v171, v171, v171\n"
"v_cvt_pk_bf16_f32 v252, v252, v253\n"
"v_mfma_f32_16x16x32_bf16 v[236:239], v[24:27], v[72:75], v[236:239]\n"
"v_and_b32 v64, v171, v176\n"
"v_and_b32 v65, v171, v177\n"
"v_mfma_f32_16x16x32_bf16 v[220:223], a[68:71], v[52:55], v[220:223]\n"
"v_cvt_pk_bf16_f32 v232, v232, v233\n"
"v_cvt_pk_bf16_f32 v233, v234, v235\n"
"v_mfma_f32_16x16x32_bf16 v[224:227], a[120:123], v[48:51], v[224:227]\n"
"v_cvt_pk_bf16_f32 v240, v240, v241\n"
"v_cvt_pk_bf16_f32 v241, v242, v243\n"
"v_mfma_f32_16x16x32_bf16 v[224:227], a[132:135], v[52:55], v[224:227]\n"
"ds_write_b64 v197, v[240:241] offset:30720\n"
"v_mfma_f32_16x16x32_bf16 v[212:215], a[100:103], v[40:43], v[212:215]\n"
"s_waitcnt lgkmcnt(4)\n"
"v_cvt_pk_bf16_f32 v234, v236, v237\n"
"v_cvt_pk_bf16_f32 v235, v238, v239\n"
"v_mfma_f32_16x16x32_bf16 v[112:115], v[112:115], v[152:155], 0\n"
"s_waitcnt lgkmcnt(3)\n"
"ds_write_b128 v196, v[232:235] offset:30720\n"
"v_mfma_f32_16x16x32_bf16 v[112:115], v[80:83], v[164:167], v[112:115]\n"
"s_waitcnt lgkmcnt(2)\n"
"v_cvt_pk_bf16_f32 v253, v254, v255\n"
"v_cvt_pk_bf16_f32 v98, v98, v99\n"
"v_mfma_f32_16x16x32_bf16 v[120:123], v[120:123], v[152:155], 0\n"
"s_add_u32 m0, s34, 5120\n"
"v_cvt_pk_bf16_f32 v99, v100, v101\n"
"v_cvt_pk_bf16_f32 v106, v106, v107\n"
"v_mfma_f32_16x16x16_bf16 v[252:255], v[252:253], v[64:65], 0\n"
"s_waitcnt vmcnt(9)\n"
"buffer_load_dwordx4 v191, s[8:11], s1 offen lds\n"
"v_add3_u32 v200, v200, s50, -5\n"
"v_mfma_f32_16x16x16_bf16 v[98:101], v[98:99], v[64:65], 0\n"
"s_add_u32 s1, s1, s33\n"
"v_cmp_lt_u32 vcc, 64, v200\n"
"v_cvt_pk_bf16_f32 v107, v108, v109\n"
"v_mfma_f32_16x16x32_bf16 v[120:123], v[84:87], v[166:169], v[120:123]\n"
"v_cvt_pk_bf16_f32 v252, v252, v253\n"
"v_cvt_pk_bf16_f32 v253, v254, v255\n"
"v_mfma_f32_16x16x16_bf16 v[106:109], v[106:107], v[64:65], 0\n"
"s_cbranch_vccnz .Lp4%=\n"
".Lr4%=:\n"
"v_mfma_f32_16x16x32_bf16 v[116:119], v[116:119], v[152:155], 0\n"
"buffer_load_dwordx4 v[56:59], v192, s[28:31], s38 offen sc1\n"
"v_cvt_pk_bf16_f32 v254, v98, v99\n"
"v_mfma_f32_16x16x32_bf16 v[116:119], v[88:91], v[164:167], v[116:119]\n"
"s_waitcnt lgkmcnt(0)\n"
"ds_read_b64 v[98:99], v180 offset:13568\n"
"v_cvt_pk_bf16_f32 v255, v100, v101\n"
"v_mfma_f32_16x16x32_bf16 v[124:127], v[124:127], v[152:155], 0\n"
"s_barrier\n"
"v_mfma_f32_16x16x32_bf16 v[124:127], v[92:95], v[166:169], v[124:127]\n"
"buffer_load_dwordx2 v[60:61], v193, s[28:31], s38 offen offset:1024 sc1\n"
"ds_read_b64 v[100:101], v181 offset:13568\n"
"v_mfma_f32_16x16x32_bf16 v[212:215], a[112:115], v[44:47], v[212:215]\n"
"s_add_u32 s38, s38, 1572864\n"
"ds_read_b128 v[140:143], v180 offset:27008\n"
"ds_read_b128 v[132:135], v181 offset:27008\n"
"v_mfma_f32_16x16x32_bf16 v[212:215], a[124:127], v[48:51], v[212:215]\n"
"v_cvt_pk_bf16_f32 v106, v106, v107\n"
"v_cvt_pk_bf16_f32 v107, v108, v109\n"
"v_mfma_f32_16x16x32_bf16 v[212:215], a[136:139], v[52:55], v[212:215]\n"
"ds_read_b64 v[108:109], v181 offset:27072\n"
"ds_read_b128 v[72:75], v202 offset:30720\n"
"v_mfma_f32_16x16x32_bf16 v[204:207], a[80:83], v[32:35], 0\n"
"buffer_store_dwordx4 v[252:255], v192, s[28:31], s37 offen sc1\n"
"v_mfma_f32_16x16x32_bf16 v[204:207], a[92:95], v[36:39], v[204:207]\n"
"s_waitcnt lgkmcnt(5)\n"
"ds_read_b64_tr_b16 v[248:249], v182 offset:27904\n"
"ds_read_b64_tr_b16 v[250:251], v183 offset:27904\n"
"v_mfma_f32_16x16x32_bf16 v[136:139], v[136:139], v[152:155], 0\n"
"v_cvt_pk_bf16_f32 v210, v216, v217\n"
"v_cvt_pk_bf16_f32 v211, v218, v219\n"
"v_mfma_f32_16x16x32_bf16 v[136:139], v[96:99], v[164:167], v[136:139]\n"
"s_waitcnt lgkmcnt(6)\n"
"ds_write_b128 v186, v[208:211] offset:6752\n"
"v_mfma_f32_16x16x32_bf16 v[128:131], v[128:131], v[152:155], 0\n"
"buffer_store_dwordx2 v[106:107], v193, s[28:31], s37 offen offset:1024 sc1\n"
"v_mfma_f32_16x16x32_bf16 v[128:131], v[100:103], v[166:169], v[128:131]\n"
"s_waitcnt lgkmcnt(4)\n"
"ds_read_b64 v[106:107], v180 offset:27072\n"
"v_cvt_pk_bf16_f32 v220, v220, v221\n"
"v_mfma_f32_16x16x32_bf16 v[132:135], v[132:135], v[152:155], 0\n"
"s_waitcnt lgkmcnt(4)\n"
"v_cvt_pk_bf16_f32 v221, v222, v223\n"
"v_max_f32 v112, v112, v120\n"
"v_mfma_f32_16x16x32_bf16 v[132:135], v[108:111], v[166:169], v[132:135]\n"
"s_add_u32 s37, s37, 1572864\n"
"buffer_store_dwordx4 v[72:75], v194, s[24:27], s36 offen sc1\n"
"v_mfma_f32_16x16x32_bf16 v[204:207], a[104:107], v[40:43], v[204:207]\n"
"s_add_u32 s4, s37, 6291456\n"
"ds_read_b128 v[72:75], v192 offset:37888\n"
"v_max_f32 v113, v113, v121\n"
"v_mfma_f32_16x16x32_bf16 v[204:207], a[116:119], v[44:47], v[204:207]\n"
"ds_read_b64_tr_b16 v[120:121], v182 offset:27144\n"
"v_max_f32 v114, v114, v122\n"
"v_mfma_f32_16x16x32_bf16 v[204:207], a[128:131], v[48:51], v[204:207]\n"
"v_max_f32 v115, v115, v123\n"
"v_max3_f32 v170, v112, v113, v114\n"
"v_mfma_f32_16x16x32_bf16 v[204:207], a[140:143], v[52:55], v[204:207]\n"
"ds_read_b64_tr_b16 v[122:123], v183 offset:27144\n"
"v_max_f32 v116, v116, v124\n"
"v_mfma_f32_16x16x32_bf16 v[228:231], a[144:147], v[32:35], 0\n"
"s_waitcnt lgkmcnt(3)\n"
"v_max_f32 v117, v117, v125\n"
"v_max_f32 v118, v118, v126\n"
"v_mfma_f32_16x16x32_bf16 v[140:143], v[140:143], v[152:155], 0\n"
"ds_read_b64_tr_b16 v[124:125], v182 offset:27912\n"
"v_cvt_pk_bf16_f32 v224, v224, v225\n"
"v_mfma_f32_16x16x32_bf16 v[140:143], v[104:107], v[164:167], v[140:143]\n"
"v_max_f32 v119, v119, v127\n"
"v_max3_f32 v170, v170, v115, v116\n"
"v_mfma_f32_16x16x32_bf16 v[228:231], a[156:159], v[36:39], v[228:231]\n"
"ds_read_b64_tr_b16 v[126:127], v183 offset:27912\n"
"v_cvt_pk_bf16_f32 v225, v226, v227\n"
"v_mfma_f32_16x16x32_bf16 v[228:231], a[168:171], v[40:43], v[228:231]\n"
"v_max3_f32 v170, v170, v117, v118\n"
"v_cvt_pk_bf16_f32 v226, v212, v213\n"
"v_mfma_f32_16x16x32_bf16 v[228:231], a[180:183], v[44:47], v[228:231]\n"
"v_cvt_pk_bf16_f32 v227, v214, v215\n"
"v_cvt_pk_bf16_f32 v222, v204, v205\n"
"v_mfma_f32_16x16x32_bf16 v[228:231], a[192:195], v[48:51], v[228:231]\n"
"ds_write_b128 v186, v[224:227] offset:6880\n"
"v_mfma_f32_16x16x32_bf16 v[228:231], a[204:207], v[52:55], v[228:231]\n"
"v_max_f32 v136, v136, v128\n"
"v_cvt_pk_bf16_f32 v223, v206, v207\n"
"v_mfma_f32_16x16x32_bf16 v[216:219], a[148:151], v[32:35], 0\n"
"ds_write_b128 v186, v[220:223] offset:6816\n"
"v_mfma_f32_16x16x32_bf16 v[216:219], a[160:163], v[36:39], v[216:219]\n"
"v_max_f32 v137, v137, v129\n"
"v_max_f32 v138, v138, v130\n"
"v_mfma_f32_16x16x32_bf16 v[216:219], a[172:175], v[40:43], v[216:219]\n"
"ds_read_b64_tr_b16 v[128:129], v182 offset:27080\n"
"v_max_f32 v139, v139, v131\n"
"v_mfma_f32_16x16x32_bf16 v[216:219], a[184:187], v[44:47], v[216:219]\n"
"ds_read_b64_tr_b16 v[130:131], v183 offset:27080\n"
"v_max3_f32 v170, v170, v119, v136\n"
"v_mfma_f32_16x16x32_bf16 v[216:219], a[196:199], v[48:51], v[216:219]\n"
"v_max3_f32 v170, v170, v137, v138\n"
"v_max_f32 v140, v140, v132\n"
"v_mfma_f32_16x16x32_bf16 v[216:219], a[208:211], v[52:55], v[216:219]\n"
"v_mov_b64 v[76:77], -1\n"
"v_mov_b64 v[78:79], -1\n"
"v_mfma_f32_16x16x32_bf16 v[208:211], a[152:155], v[32:35], 0\n"
"buffer_store_dwordx4 v[76:79], v192, s[28:31], s4 offen sc1\n"
"v_mfma_f32_16x16x32_bf16 v[208:211], a[164:167], v[36:39], v[208:211]\n"
"s_add_u32 s4, s37, 6291456\n"
"ds_read_b128 v[32:35], v188 offset:6144\n"
"ds_read_b128 v[36:39], v189 offset:6144\n"
"v_mfma_f32_16x16x32_bf16 v[208:211], a[176:179], v[40:43], v[208:211]\n"
"ds_read_b128 v[40:43], v188 offset:7168\n"
"v_max_f32 v141, v141, v133\n"
"v_mfma_f32_16x16x32_bf16 v[208:211], a[188:191], v[44:47], v[208:211]\n"
"ds_read_b128 v[44:47], v189 offset:7168\n"
"v_max_f32 v142, v142, v134\n"
"v_mfma_f32_16x16x32_bf16 v[208:211], a[200:203], v[48:51], v[208:211]\n"
"s_waitcnt vmcnt(13)\n"
"ds_read_b128 v[48:51], v188 offset:8192\n"
"v_max_f32 v143, v143, v135\n"
"v_mfma_f32_16x16x32_bf16 v[208:211], a[212:215], v[52:55], v[208:211]\n"
"ds_read_b128 v[52:55], v189 offset:8192\n"
"v_max3_f32 v170, v170, v139, v140\n"
"v_max3_f32 v170, v170, v141, v142\n"
"v_mov_b64 v[252:253], 0\n"
"ds_read_b64 v[76:77], v62 offset:30720\n"
"ds_read_b64_tr_b16 v[132:133], v182 offset:27848\n"
"v_mov_b64 v[254:255], 0\n"
"v_mov_b64 v[98:99], 0\n"
"v_mov_b64 v[100:101], 0\n"
"v_mov_b64 v[106:107], 0\n"
"v_mov_b64 v[108:109], 0\n"
"v_max_f32 v171, v170, v143\n"
"v_max_f32 v170, v170, v143\n"
"v_cvt_pk_bf16_f32 v152, v228, v229\n"
"v_cvt_pk_bf16_f32 v153, v230, v231\n"
"v_permlane16_swap_b32 v170, v171\n"
"v_max_f32 v170, v170, v171\n"
"v_mov_b32 v171, v170\n"
"v_cvt_pk_bf16_f32 v154, v216, v217\n"
"v_cvt_pk_bf16_f32 v155, v218, v219\n"
"v_permlane32_swap_b32 v170, v171\n"
"v_max_f32 v170, v170, v171\n"
"v_mul_f32 v170, 0xbe553b95, v170\n"
"v_pk_fma_f32 v[112:113], v[112:113], s[70:71], v[170:171] op_sel_hi:[1,0,0]\n"
"v_pk_fma_f32 v[114:115], v[114:115], s[70:71], v[170:171] op_sel_hi:[1,0,0]\n"
"v_pk_fma_f32 v[116:117], v[116:117], s[70:71], v[170:171] op_sel_hi:[1,0,0]\n"
"v_pk_fma_f32 v[118:119], v[118:119], s[70:71], v[170:171] op_sel_hi:[1,0,0]\n"
"v_pk_fma_f32 v[136:137], v[136:137], s[70:71], v[170:171] op_sel_hi:[1,0,0]\n"
"v_pk_fma_f32 v[138:139], v[138:139], s[70:71], v[170:171] op_sel_hi:[1,0,0]\n"
"v_pk_fma_f32 v[140:141], v[140:141], s[70:71], v[170:171] op_sel_hi:[1,0,0]\n"
"v_pk_fma_f32 v[142:143], v[142:143], s[70:71], v[170:171] op_sel_hi:[1,0,0]\n"
"v_exp_f32 v112, v112\n"
"v_exp_f32 v113, v113\n"
"v_exp_f32 v114, v114\n"
"v_exp_f32 v115, v115\n"
"v_cvt_pk_bf16_f32 v82, v112, v114\n"
"v_cvt_pk_bf16_f32 v83, v113, v115\n"
"v_exp_f32 v116, v116\n"
"v_exp_f32 v117, v117\n"
"v_exp_f32 v118, v118\n"
"v_exp_f32 v119, v119\n"
"v_cvt_pk_bf16_f32 v84, v116, v118\n"
"v_cvt_pk_bf16_f32 v85, v117, v119\n"
"v_exp_f32 v136, v136\n"
"s_nop 0\n"
"v_smfmac_f32_16x16x64_bf16 v[252:255], v[82:85], v[244:251], v66\n"
"s_waitcnt lgkmcnt(1)\n"
"buffer_store_dwordx2 v[76:77], v195, s[24:27], s36 offen sc1\n"
"v_smfmac_f32_16x16x64_bf16 v[98:101], v[82:85], v[120:127], v66\n"
"s_add_u32 s36, s36, s33\n"
"ds_read_b128 v[76:79], v192 offset:38912\n"
"v_mfma_f32_16x16x32_bf16 v[212:215], a[0:3], v[32:35], 0\n"
"s_add_u32 m0, s34, 6144\n"
"ds_read_b64_tr_b16 v[134:135], v183 offset:27848\n"
"ds_read_b64_tr_b16 v[112:113], v182 offset:54144\n"
"v_mfma_f32_16x16x32_bf16 v[212:215], a[12:15], v[36:39], v[212:215]\n"
"v_exp_f32 v137, v137\n"
"v_mfma_f32_16x16x32_bf16 v[212:215], a[24:27], v[40:43], v[212:215]\n"
"buffer_load_dwordx4 v190, s[8:11], s32 offen lds\n"
"ds_read_b64_tr_b16 v[114:115], v183 offset:54144\n"
"v_mfma_f32_16x16x32_bf16 v[212:215], a[36:39], v[44:47], v[212:215]\n"
"ds_read_b64_tr_b16 v[116:117], v182 offset:54912\n"
"ds_read_b64_tr_b16 v[118:119], v183 offset:54912\n"
"v_mfma_f32_16x16x32_bf16 v[212:215], a[48:51], v[48:51], v[212:215]\n"
"v_exp_f32 v138, v138\n"
"v_mfma_f32_16x16x32_bf16 v[212:215], a[60:63], v[52:55], v[212:215]\n"
"ds_read_b64_tr_b16 v[244:245], v182 offset:54152\n"
"ds_read_b64_tr_b16 v[246:247], v183 offset:54152\n"
"v_mfma_f32_16x16x32_bf16 v[204:207], a[4:7], v[32:35], 0\n"
"v_exp_f32 v139, v139\n"
"v_mfma_f32_16x16x32_bf16 v[204:207], a[16:19], v[36:39], v[204:207]\n"
"ds_read_b64_tr_b16 v[248:249], v182 offset:54920\n"
"ds_read_b64_tr_b16 v[250:251], v183 offset:54920\n"
"v_mfma_f32_16x16x32_bf16 v[204:207], a[28:31], v[40:43], v[204:207]\n"
"s_waitcnt lgkmcnt(8)\n"
"v_exp_f32 v140, v140\n"
"v_smfmac_f32_16x16x64_bf16 v[106:109], v[82:85], v[128:135], v66\n"
"ds_read_b64_tr_b16 v[120:121], v182 offset:54088\n"
"v_mfma_f32_16x16x32_bf16 v[82:85], v[172:175], v[82:85], 0\n"
"ds_read_b64_tr_b16 v[122:123], v183 offset:54088\n"
"ds_read_b64_tr_b16 v[124:125], v182 offset:54856\n"
"v_mfma_f32_16x16x32_bf16 v[204:207], a[40:43], v[44:47], v[204:207]\n"
"s_add_u32 m0, s34, 9216\n"
"v_exp_f32 v141, v141\n"
"v_mfma_f32_16x16x32_bf16 v[204:207], a[52:55], v[48:51], v[204:207]\n"
"buffer_load_dwordx4 v191, s[8:11], s32 offen lds\n"
"ds_read_b64_tr_b16 v[126:127], v183 offset:54856\n"
"v_mfma_f32_16x16x32_bf16 v[204:207], a[64:67], v[52:55], v[204:207]\n"
"s_add_u32 s32, s32, s33\n"
"ds_read_b128 v[128:131], v180\n"
"ds_read_b128 v[132:135], v180 offset:13504\n"
"v_mfma_f32_16x16x32_bf16 v[220:223], a[8:11], v[32:35], 0\n"
"v_exp_f32 v142, v142\n"
"v_mfma_f32_16x16x32_bf16 v[220:223], a[20:23], v[36:39], v[220:223]\n"
"v_exp_f32 v143, v143\n"
"v_mfma_f32_16x16x32_bf16 v[220:223], a[32:35], v[40:43], v[220:223]\n"
"v_mov_b64 v[68:69], -1\n"
"v_cvt_pk_bf16_f32 v90, v136, v138\n"
"v_mfma_f32_16x16x32_bf16 v[220:223], a[44:47], v[44:47], v[220:223]\n"
"buffer_store_dwordx2 v[68:69], v193, s[28:31], s4 offen offset:1024 sc1\n"
"v_mfma_f32_16x16x32_bf16 v[220:223], a[56:59], v[48:51], v[220:223]\n"
"ds_read_b128 v[68:71], v192 offset:36864\n"
"v_cvt_pk_bf16_f32 v91, v137, v139\n"
"v_mfma_f32_16x16x32_bf16 v[220:223], a[68:71], v[52:55], v[220:223]\n"
"ds_read_b128 v[136:139], v181\n"
"v_cvt_pk_bf16_f32 v92, v140, v142\n"
"v_mfma_f32_16x16x32_bf16 v[224:227], a[72:75], v[32:35], 0\n"
"v_cvt_pk_bf16_f32 v93, v141, v143\n"
"v_cvt_pk_bf16_f32 v166, v208, v209\n"
"v_mfma_f32_16x16x32_bf16 v[224:227], a[84:87], v[36:39], v[224:227]\n"
"s_waitcnt lgkmcnt(4)\n"
"ds_read_b128 v[140:143], v181 offset:13504\n"
"v_cvt_pk_bf16_f32 v167, v210, v211\n"
"v_smfmac_f32_16x16x64_bf16 v[252:255], v[90:93], v[112:119], v66\n"
"s_add_u32 m0, s34, 7168\n"
"s_nop 0\n"
"buffer_load_dwordx4 v190, s[8:11], s0 offen lds\n"
"v_smfmac_f32_16x16x64_bf16 v[98:101], v[90:93], v[244:251], v66\n"
"ds_read_b128 v[112:115], v180 offset:27008\n"
"v_smfmac_f32_16x16x64_bf16 v[106:109], v[90:93], v[120:127], v66\n"
"ds_read_b128 v[244:247], v181 offset:27008\n"
"v_mfma_f32_16x16x32_bf16 v[90:93], v[172:175], v[90:93], v[82:85]\n"
"ds_read_b64_tr_b16 v[120:121], v182 offset:40640\n"
"v_cvt_pk_bf16_f32 v252, v252, v253\n"
"v_mfma_f32_16x16x32_bf16 v[224:227], a[96:99], v[40:43], v[224:227]\n"
"s_waitcnt lgkmcnt(5)\n"
"ds_read_b64 v[82:83], v180 offset:64\n"
"ds_read_b64 v[84:85], v181 offset:64\n"
"v_mfma_f32_16x16x32_bf16 v[232:235], a[216:219], v[68:71], 0\n"
"v_cvt_pk_bf16_f32 v253, v254, v255\n"
"v_cvt_pk_bf16_f32 v98, v98, v99\n"
"v_mfma_f32_16x16x32_bf16 v[232:235], a[228:231], v[72:75], v[232:235]\n"
"ds_read_b64 v[92:93], v181 offset:13568\n"
"v_cvt_pk_bf16_f32 v99, v100, v101\n"
"v_mfma_f32_16x16x32_bf16 v[236:239], a[220:223], v[68:71], 0\n"
"v_rcp_f32 v171, v90\n"
"v_mfma_f32_16x16x32_bf16 v[236:239], a[232:235], v[72:75], v[236:239]\n"
"ds_read_b64 v[90:91], v180 offset:13568\n"
"v_cvt_pk_bf16_f32 v171, v171, v171\n"
"v_mfma_f32_16x16x32_bf16 v[240:243], a[224:227], v[68:71], 0\n"
"s_add_u32 m0, s34, 10240\n"
"ds_read_b128 v[68:71], v192 offset:39936\n"
"buffer_load_dwordx4 v191, s[8:11], s0 offen lds\n"
"v_mfma_f32_16x16x32_bf16 v[240:243], a[236:239], v[72:75], v[240:243]\n"
"s_add_u32 s0, s0, s33\n"
"ds_read_b128 v[72:75], v192 offset:40960\n"
"v_and_b32 v64, v171, v176\n"
"v_mfma_f32_16x16x32_bf16 v[240:243], a[248:251], v[76:79], v[240:243]\n"
"v_and_b32 v65, v171, v177\n"
"v_cvt_pk_bf16_f32 v106, v106, v107\n"
"v_mfma_f32_16x16x32_bf16 v[232:235], a[240:243], v[76:79], v[232:235]\n"
"s_waitcnt lgkmcnt(5)\n"
"v_cvt_pk_bf16_f32 v107, v108, v109\n"
"v_cvt_pk_bf16_f32 v212, v212, v213\n"
"v_mfma_f32_16x16x32_bf16 v[236:239], a[244:247], v[76:79], v[236:239]\n"
"s_waitcnt vmcnt(11) lgkmcnt(4)\n"
"ds_read_b128 v[76:79], v192 offset:41984\n"
"ds_read_b64_tr_b16 v[122:123], v183 offset:40640\n"
"v_mfma_f32_16x16x16_bf16 v[252:255], v[252:253], v[64:65], 0\n"
"s_waitcnt vmcnt(10) lgkmcnt(5)\n"
"ds_write_b128 v202, v[56:59] offset:43008\n"
"v_mfma_f32_16x16x16_bf16 v[98:101], v[98:99], v[64:65], 0\n"
"s_waitcnt lgkmcnt(5)\n"
"buffer_load_dwordx4 v[56:59], v192, s[28:31], s38 offen sc1\n"
"v_cvt_pk_bf16_f32 v213, v214, v215\n"
"v_mfma_f32_16x16x16_bf16 v[106:109], v[106:107], v[64:65], 0\n"
"s_waitcnt lgkmcnt(4)\n"
"ds_write_b64 v203, v[60:61] offset:43008\n"
"v_mfma_f32_16x16x32_bf16 v[232:235], a[252:255], v[68:71], v[232:235]\n"
"s_waitcnt lgkmcnt(4)\n"
"v_cvt_pk_bf16_f32 v252, v252, v253\n"
"v_cvt_pk_bf16_f32 v253, v254, v255\n"
"v_mfma_f32_16x16x32_bf16 v[232:235], v[8:11], v[72:75], v[232:235]\n"
"s_add_u32 m0, s34, 8192\n"
"buffer_load_dwordx2 v[60:61], v193, s[28:31], s38 offen offset:1024 sc1\n"
"v_cvt_pk_bf16_f32 v254, v98, v99\n"
"v_mfma_f32_16x16x32_bf16 v[240:243], v[4:7], v[68:71], v[240:243]\n"
"s_add_u32 s38, s38, 1572864\n"
"ds_read_b64 v[98:99], v180 offset:27072\n"
"v_cvt_pk_bf16_f32 v255, v100, v101\n"
"v_mfma_f32_16x16x32_bf16 v[240:243], v[16:19], v[72:75], v[240:243]\n"
"s_waitcnt lgkmcnt(4)\n"
"ds_read_b64 v[100:101], v181 offset:27072\n"
"v_cvt_pk_bf16_f32 v106, v106, v107\n"
"v_mfma_f32_16x16x32_bf16 v[240:243], v[28:31], v[76:79], v[240:243]\n"
"v_cvt_pk_bf16_f32 v107, v108, v109\n"
"v_cvt_pk_bf16_f32 v214, v204, v205\n"
"v_mfma_f32_16x16x32_bf16 v[236:239], v[0:3], v[68:71], v[236:239]\n"
"buffer_load_dwordx4 v190, s[8:11], s1 offen lds\n"
"ds_read_b64_tr_b16 v[124:125], v182 offset:41408\n"
"v_mfma_f32_16x16x32_bf16 v[236:239], v[12:15], v[72:75], v[236:239]\n"
"s_waitcnt vmcnt(11)\n"
"v_or_b32 v201, 8, s6\n"
"v_cvt_pk_bf16_f32 v240, v240, v241\n"
"v_mfma_f32_16x16x32_bf16 v[236:239], v[24:27], v[76:79], v[236:239]\n"
"v_cvt_pk_bf16_f32 v241, v242, v243\n"
"v_cvt_pk_bf16_f32 v215, v206, v207\n"
"v_mfma_f32_16x16x32_bf16 v[232:235], v[20:23], v[76:79], v[232:235]\n"
"ds_write_b64 v197, v[240:241] offset:55296\n"
"v_mfma_f32_16x16x32_bf16 v[128:131], v[128:131], v[144:147], 0\n"
"s_waitcnt lgkmcnt(3)\n"
"s_mov_b64 exec, 1\n"
"buffer_store_dword v201, off, s[28:31], s40 sc1\n"
"s_mov_b64 exec, -1\n"
"v_mfma_f32_16x16x32_bf16 v[128:131], v[80:83], v[156:159], v[128:131]\n"
"s_waitcnt lgkmcnt(2)\n"
"v_cvt_pk_bf16_f32 v232, v232, v233\n"
"v_cvt_pk_bf16_f32 v233, v234, v235\n"
"v_mfma_f32_16x16x32_bf16 v[136:139], v[136:139], v[144:147], 0\n"
"buffer_store_dwordx4 v[252:255], v192, s[28:31], s37 offen sc1\n"
"v_mfma_f32_16x16x32_bf16 v[136:139], v[84:87], v[158:161], v[136:139]\n"
"v_cvt_pk_bf16_f32 v234, v236, v237\n"
"v_cvt_pk_bf16_f32 v235, v238, v239\n"
"v_mfma_f32_16x16x32_bf16 v[132:135], v[132:135], v[144:147], 0\n"
"s_add_u32 m0, s34, 11264\n"
"ds_write_b128 v196, v[232:235] offset:55296\n"
"v_mfma_f32_16x16x32_bf16 v[132:135], v[88:91], v[156:159], v[132:135]\n"
"buffer_load_dwordx4 v191, s[8:11], s1 offen lds\n"
"v_cvt_pk_bf16_f32 v220, v220, v221\n"
"v_mfma_f32_16x16x32_bf16 v[140:143], v[140:143], v[144:147], 0\n"
"s_add_u32 s1, s1, s33\n"
"ds_read_b64_tr_b16 v[126:127], v183 offset:41408\n"
"v_cvt_pk_bf16_f32 v221, v222, v223\n"
"v_mfma_f32_16x16x32_bf16 v[140:143], v[92:95], v[158:161], v[140:143]\n"
"s_mov_b64 exec, s[44:45]\n"
"v_add_u32 v179, 0xc0, v179\n"
"s_mov_b64 exec, -1\n"
"v_mfma_f32_16x16x32_bf16 v[112:115], v[112:115], v[144:147], 0\n"
"buffer_store_dwordx2 v[106:107], v193, s[28:31], s37 offen offset:1024 sc1\n"
"v_mfma_f32_16x16x32_bf16 v[112:115], v[96:99], v[156:159], v[112:115]\n"
"s_add_u32 s37, s37, 1572864\n"
"v_max_f32 v128, v128, v136\n"
"v_max_f32 v129, v129, v137\n"
"v_mfma_f32_16x16x32_bf16 v[244:247], v[244:247], v[144:147], 0\n"
"s_add_u32 s4, s37, 6291456\n"
"ds_read_b64_tr_b16 v[136:137], v182 offset:40648\n"
"v_max_f32 v130, v130, v138\n"
"v_mfma_f32_16x16x32_bf16 v[244:247], v[100:103], v[158:161], v[244:247]\n"
"s_waitcnt lgkmcnt(2)\n"
"v_max_f32 v131, v131, v139\n"
"v_max3_f32 v170, v128, v129, v130\n"
"v_mfma_f32_16x16x32_bf16 v[224:227], a[108:111], v[44:47], v[224:227]\n"
"s_barrier\n"
"v_mfma_f32_16x16x32_bf16 v[224:227], a[120:123], v[48:51], v[224:227]\n"
"ds_read_b128 v[116:119], v178\n"
"ds_read_b64 v[106:107], v178 offset:64\n"
"v_mfma_f32_16x16x32_bf16 v[224:227], a[132:135], v[52:55], v[224:227]\n"
"ds_write_b128 v186, v[212:215] offset:20256\n"
"v_mfma_f32_16x16x32_bf16 v[228:231], a[76:79], v[32:35], 0\n"
"v_max_f32 v132, v132, v140\n"
"v_max_f32 v133, v133, v141\n"
"v_mfma_f32_16x16x32_bf16 v[228:231], a[88:91], v[36:39], v[228:231]\n"
"ds_read_b128 v[248:251], v179\n"
"ds_read_b64 v[108:109], v179 offset:64\n"
"v_mfma_f32_16x16x32_bf16 v[228:231], a[100:103], v[40:43], v[228:231]\n"
"v_cvt_pk_bf16_f32 v224, v224, v225\n"
"v_max_f32 v134, v134, v142\n"
"v_mfma_f32_16x16x32_bf16 v[228:231], a[112:115], v[44:47], v[228:231]\n"
"ds_read_b128 v[76:79], v202 offset:55296\n"
"ds_read_b64_tr_b16 v[138:139], v183 offset:40648\n"
"v_mfma_f32_16x16x32_bf16 v[228:231], a[124:127], v[48:51], v[228:231]\n"
"v_cvt_pk_bf16_f32 v225, v226, v227\n"
"v_max_f32 v135, v135, v143\n"
"v_mfma_f32_16x16x32_bf16 v[228:231], a[136:139], v[52:55], v[228:231]\n"
"ds_read_b64_tr_b16 v[140:141], v182 offset:41416\n"
"ds_read_b64_tr_b16 v[142:143], v183 offset:41416\n"
"v_mfma_f32_16x16x32_bf16 v[216:219], a[80:83], v[32:35], 0\n"
"s_waitcnt lgkmcnt(7)\n"
"v_max3_f32 v170, v170, v131, v132\n"
"v_max3_f32 v170, v170, v133, v134\n"
"v_mfma_f32_16x16x32_bf16 v[116:119], v[116:119], v[144:147], 0\n"
"v_max_f32 v112, v112, v244\n"
"v_cvt_pk_bf16_f32 v226, v228, v229\n"
"v_mfma_f32_16x16x32_bf16 v[116:119], v[104:107], v[156:159], v[116:119]\n"
"v_cvt_pk_bf16_f32 v227, v230, v231\n"
"v_max_f32 v113, v113, v245\n"
"v_mfma_f32_16x16x32_bf16 v[216:219], a[92:95], v[36:39], v[216:219]\n"
"ds_write_b128 v186, v[224:227] offset:20384\n"
"v_mfma_f32_16x16x32_bf16 v[216:219], a[104:107], v[40:43], v[216:219]\n"
"s_waitcnt lgkmcnt(4)\n"
"v_max_f32 v114, v114, v246\n"
"v_max_f32 v115, v115, v247\n"
"v_mfma_f32_16x16x32_bf16 v[248:251], v[248:251], v[144:147], 0\n"
"buffer_store_dwordx4 v[76:79], v194, s[24:27], s36 offen sc1\n"
"v_mfma_f32_16x16x32_bf16 v[248:251], v[108:111], v[158:161], v[248:251]\n"
"s_nop 0\n"
"ds_read_b128 v[76:79], v192 offset:44032\n"
"v_mov_b64 v[68:69], -1\n"
"v_mfma_f32_16x16x32_bf16 v[216:219], a[116:119], v[44:47], v[216:219]\n"
"v_mov_b64 v[70:71], -1\n"
"v_max3_f32 v170, v170, v135, v112\n"
"v_mfma_f32_16x16x32_bf16 v[216:219], a[128:131], v[48:51], v[216:219]\n"
"buffer_store_dwordx4 v[68:71], v192, s[28:31], s4 offen sc1\n"
"v_mfma_f32_16x16x32_bf16 v[216:219], a[140:143], v[52:55], v[216:219]\n"
"s_add_u32 s4, s37, 6291456\n"
"ds_read_b64 v[68:69], v62 offset:55296\n"
"ds_read_b64_tr_b16 v[244:245], v182 offset:40584\n"
"v_mfma_f32_16x16x32_bf16 v[208:211], a[144:147], v[32:35], 0\n"
"ds_read_b64_tr_b16 v[246:247], v183 offset:40584\n"
"v_max3_f32 v170, v170, v113, v114\n"
"v_mfma_f32_16x16x32_bf16 v[208:211], a[156:159], v[36:39], v[208:211]\n"
"v_max_f32 v116, v116, v248\n"
"v_cvt_pk_bf16_f32 v222, v216, v217\n"
"v_mfma_f32_16x16x32_bf16 v[208:211], a[168:171], v[40:43], v[208:211]\n"
"v_cvt_pk_bf16_f32 v223, v218, v219\n"
"v_max_f32 v117, v117, v249\n"
"v_mfma_f32_16x16x32_bf16 v[208:211], a[180:183], v[44:47], v[208:211]\n"
"ds_write_b128 v186, v[220:223] offset:20320\n"
"v_mfma_f32_16x16x32_bf16 v[208:211], a[192:195], v[48:51], v[208:211]\n"
"v_max_f32 v118, v118, v250\n"
"v_max_f32 v119, v119, v251\n"
"v_mfma_f32_16x16x32_bf16 v[208:211], a[204:207], v[52:55], v[208:211]\n"
"s_waitcnt lgkmcnt(3)\n"
"ds_read_b64_tr_b16 v[248:249], v182 offset:41352\n"
"ds_read_b64_tr_b16 v[250:251], v183 offset:41352\n"
"v_mfma_f32_16x16x32_bf16 v[204:207], a[148:151], v[32:35], 0\n"
"buffer_store_dwordx2 v[68:69], v195, s[24:27], s36 offen sc1\n"
"v_mfma_f32_16x16x32_bf16 v[204:207], a[160:163], v[36:39], v[204:207]\n"
"s_add_u32 s36, s36, s33\n"
"ds_read_b128 v[68:71], v192 offset:45056\n"
"v_max3_f32 v170, v170, v115, v116\n"
"v_mfma_f32_16x16x32_bf16 v[204:207], a[172:175], v[40:43], v[204:207]\n"
"v_max3_f32 v170, v170, v117, v118\n"
"v_mov_b64 v[252:253], 0\n"
"v_mfma_f32_16x16x32_bf16 v[204:207], a[184:187], v[44:47], v[204:207]\n"
"v_mov_b64 v[254:255], 0\n"
"v_mov_b64 v[98:99], 0\n"
"v_mfma_f32_16x16x32_bf16 v[204:207], a[196:199], v[48:51], v[204:207]\n"
"v_mov_b64 v[100:101], 0\n"
"v_mov_b64 v[106:107], 0\n"
"v_mfma_f32_16x16x32_bf16 v[204:207], a[208:211], v[52:55], v[204:207]\n"
"v_mov_b64 v[108:109], 0\n"
"v_max_f32 v171, v170, v119\n"
"v_mfma_f32_16x16x32_bf16 v[212:215], a[152:155], v[32:35], 0\n"
"ds_read_b128 v[32:35], v188\n"
"v_max_f32 v170, v170, v119\n"
"v_mfma_f32_16x16x32_bf16 v[212:215], a[164:167], v[36:39], v[212:215]\n"
"ds_read_b128 v[36:39], v189\n"
"v_permlane16_swap_b32 v170, v171\n"
"v_mfma_f32_16x16x32_bf16 v[212:215], a[176:179], v[40:43], v[212:215]\n"
"ds_read_b128 v[40:43], v188 offset:1024\n"
"v_max_f32 v170, v170, v171\n"
"v_mfma_f32_16x16x32_bf16 v[212:215], a[188:191], v[44:47], v[212:215]\n"
"ds_read_b128 v[44:47], v189 offset:1024\n"
"v_mov_b32 v171, v170\n"
"v_mfma_f32_16x16x32_bf16 v[212:215], a[200:203], v[48:51], v[212:215]\n"
"ds_read_b128 v[48:51], v188 offset:2048\n"
"v_permlane32_swap_b32 v170, v171\n"
"v_max_f32 v170, v170, v171\n"
"v_mul_f32 v170, 0xbe553b95, v170\n"
"v_pk_fma_f32 v[128:129], v[128:129], s[70:71], v[170:171] op_sel_hi:[1,0,0]\n"
"v_pk_fma_f32 v[130:131], v[130:131], s[70:71], v[170:171] op_sel_hi:[1,0,0]\n"
"v_pk_fma_f32 v[132:133], v[132:133], s[70:71], v[170:171] op_sel_hi:[1,0,0]\n"
"v_pk_fma_f32 v[134:135], v[134:135], s[70:71], v[170:171] op_sel_hi:[1,0,0]\n"
"v_pk_fma_f32 v[112:113], v[112:113], s[70:71], v[170:171] op_sel_hi:[1,0,0]\n"
"v_pk_fma_f32 v[114:115], v[114:115], s[70:71], v[170:171] op_sel_hi:[1,0,0]\n"
"v_pk_fma_f32 v[116:117], v[116:117], s[70:71], v[170:171] op_sel_hi:[1,0,0]\n"
".Lic5%=:\n"
"s_cbranch_execz .Lib1%=\n"
"v_pk_fma_f32 v[118:119], v[118:119], s[70:71], v[170:171] op_sel_hi:[1,0,0]\n"
"v_mfma_f32_16x16x32_bf16 v[212:215], a[212:215], v[52:55], v[212:215]\n"
"s_waitcnt lgkmcnt(4)\n"
"ds_read_b128 v[52:55], v189 offset:2048\n"
"v_exp_f32 v128, v128\n"
"v_mfma_f32_16x16x32_bf16 v[228:231], a[0:3], v[32:35], 0\n"
"s_waitcnt lgkmcnt(4)\n"
"v_exp_f32 v129, v129\n"
"v_mfma_f32_16x16x32_bf16 v[228:231], a[12:15], v[36:39], v[228:231]\n"
"s_waitcnt lgkmcnt(3)\n"
"v_exp_f32 v130, v130\n"
"v_mfma_f32_16x16x32_bf16 v[228:231], a[24:27], v[40:43], v[228:231]\n"
"s_waitcnt lgkmcnt(2)\n"
"v_exp_f32 v131, v131\n"
"v_mfma_f32_16x16x32_bf16 v[228:231], a[36:39], v[44:47], v[228:231]\n"
"s_waitcnt lgkmcnt(1)\n"
"v_cvt_pk_bf16_f32 v82, v128, v130\n"
"v_cvt_pk_bf16_f32 v83, v129, v131\n"
"v_mfma_f32_16x16x32_bf16 v[228:231], a[48:51], v[48:51], v[228:231]\n"
"s_add_u32 m0, s34, 0\n"
"ds_read_b64_tr_b16 v[128:129], v184 offset:128\n"
"buffer_load_dwordx4 v190, s[8:11], s32 offen lds\n"
"v_mfma_f32_16x16x32_bf16 v[216:219], a[4:7], v[32:35], 0\n"
"s_add_u32 s32, s32, s33\n"
"ds_read_b64_tr_b16 v[130:131], v185 offset:128\n"
"v_exp_f32 v132, v132\n"
"v_mfma_f32_16x16x32_bf16 v[216:219], a[16:19], v[36:39], v[216:219]\n"
"s_waitcnt lgkmcnt(2)\n"
"v_exp_f32 v133, v133\n"
"v_mfma_f32_16x16x32_bf16 v[216:219], a[28:31], v[40:43], v[216:219]\n"
"v_exp_f32 v134, v134\n"
"v_mfma_f32_16x16x32_bf16 v[216:219], a[40:43], v[44:47], v[216:219]\n"
"v_exp_f32 v135, v135\n"
"v_mfma_f32_16x16x32_bf16 v[216:219], a[52:55], v[48:51], v[216:219]\n"
"v_cvt_pk_bf16_f32 v84, v132, v134\n"
"v_cvt_pk_bf16_f32 v85, v133, v135\n"
"v_mfma_f32_16x16x32_bf16 v[216:219], a[64:67], v[52:55], v[216:219]\n"
"ds_read_b64_tr_b16 v[132:133], v184 offset:896\n"
"ds_read_b64_tr_b16 v[134:135], v185 offset:896\n"
"v_smfmac_f32_16x16x64_bf16 v[252:255], v[82:85], v[120:127], v66\n"
"ds_read_b64_tr_b16 v[120:121], v184 offset:136\n"
"v_smfmac_f32_16x16x64_bf16 v[98:101], v[82:85], v[136:143], v66\n"
"ds_read_b64_tr_b16 v[122:123], v185 offset:136\n"
"v_smfmac_f32_16x16x64_bf16 v[106:109], v[82:85], v[244:251], v66\n"
"s_add_u32 m0, s34, 3072\n"
"s_nop 0\n"
"buffer_load_dwordx4 v199, s[8:11], s75 offen lds\n"
"v_mfma_f32_16x16x32_bf16 v[82:85], v[172:175], v[82:85], 0\n"
"v_mov_b64 v[72:73], -1\n"
"v_cvt_pk_bf16_f32 v144, v208, v209\n"
"v_mfma_f32_16x16x32_bf16 v[228:231], a[60:63], v[52:55], v[228:231]\n"
"ds_read_b64_tr_b16 v[124:125], v184 offset:904\n"
"ds_read_b64_tr_b16 v[126:127], v185 offset:904\n"
"v_mfma_f32_16x16x32_bf16 v[220:223], a[8:11], v[32:35], 0\n"
"ds_read_b64_tr_b16 v[136:137], v184 offset:72\n"
"buffer_store_dwordx2 v[72:73], v193, s[28:31], s4 offen offset:1024 sc1\n"
"v_mfma_f32_16x16x32_bf16 v[220:223], a[20:23], v[36:39], v[220:223]\n"
"ds_read_b128 v[72:75], v192 offset:43008\n"
"v_cvt_pk_bf16_f32 v145, v210, v211\n"
"v_mfma_f32_16x16x32_bf16 v[220:223], a[32:35], v[40:43], v[220:223]\n"
"ds_read_b64_tr_b16 v[138:139], v185 offset:72\n"
"v_cvt_pk_bf16_f32 v146, v204, v205\n"
"v_mfma_f32_16x16x32_bf16 v[220:223], a[44:47], v[44:47], v[220:223]\n"
"ds_read_b64_tr_b16 v[140:141], v184 offset:840\n"
"v_cvt_pk_bf16_f32 v147, v206, v207\n"
"v_mfma_f32_16x16x32_bf16 v[220:223], a[56:59], v[48:51], v[220:223]\n"
"ds_read_b64_tr_b16 v[142:143], v185 offset:840\n"
"v_cvt_pk_bf16_f32 v158, v212, v213\n"
"v_mfma_f32_16x16x32_bf16 v[220:223], a[68:71], v[52:55], v[220:223]\n"
"ds_read_b128 v[244:247], v180 offset:13504\n"
"v_cvt_pk_bf16_f32 v159, v214, v215\n"
"v_mfma_f32_16x16x32_bf16 v[224:227], a[72:75], v[32:35], 0\n"
"s_waitcnt vmcnt(12)\n"
"ds_read_b128 v[248:251], v180 offset:27008\n"
"v_exp_f32 v112, v112\n"
"v_mfma_f32_16x16x32_bf16 v[224:227], a[84:87], v[36:39], v[224:227]\n"
"s_add_u32 m0, s34, 1024\n"
"ds_write_b128 v202, v[56:59] offset:30720\n"
"v_mfma_f32_16x16x32_bf16 v[224:227], a[96:99], v[40:43], v[224:227]\n"
"s_waitcnt lgkmcnt(6)\n"
"buffer_load_dwordx4 v190, s[8:11], s0 offen lds\n"
"v_exp_f32 v113, v113\n"
"v_mfma_f32_16x16x32_bf16 v[232:235], a[216:219], v[72:75], 0\n"
"s_waitcnt vmcnt(12)\n"
"ds_write_b64 v203, v[60:61] offset:30720\n"
"v_mfma_f32_16x16x32_bf16 v[232:235], a[228:231], v[76:79], v[232:235]\n"
"s_add_u32 s0, s0, s33\n"
"v_exp_f32 v114, v114\n"
"v_mfma_f32_16x16x32_bf16 v[236:239], a[220:223], v[72:75], 0\n"
"buffer_load_dwordx4 v[56:59], v192, s[28:31], s38 offen sc1\n"
"v_cvt_pk_bf16_f32 v90, v112, v114\n"
"v_mfma_f32_16x16x32_bf16 v[236:239], a[232:235], v[76:79], v[236:239]\n"
"v_exp_f32 v115, v115\n"
"v_mfma_f32_16x16x32_bf16 v[240:243], a[224:227], v[72:75], 0\n"
"ds_read_b128 v[72:75], v192 offset:46080\n"
"v_cvt_pk_bf16_f32 v91, v113, v115\n"
"v_mfma_f32_16x16x32_bf16 v[240:243], a[236:239], v[76:79], v[240:243]\n"
"ds_read_b128 v[76:79], v192 offset:47104\n"
"ds_read_b128 v[112:115], v181 offset:13504\n"
"v_mfma_f32_16x16x32_bf16 v[240:243], a[248:251], v[68:71], v[240:243]\n"
"buffer_load_dwordx2 v[60:61], v193, s[28:31], s38 offen offset:1024 sc1\n"
"v_exp_f32 v116, v116\n"
"v_mfma_f32_16x16x32_bf16 v[232:235], a[240:243], v[68:71], v[232:235]\n"
"s_add_u32 s38, s38, 1572864\n"
"v_exp_f32 v117, v117\n"
"v_mfma_f32_16x16x32_bf16 v[236:239], a[244:247], v[68:71], v[236:239]\n"
"s_add_u32 m0, s34, 4096\n"
"ds_read_b128 v[68:71], v192 offset:48128\n"
"v_exp_f32 v118, v118\n"
"v_mfma_f32_16x16x32_bf16 v[224:227], a[108:111], v[44:47], v[224:227]\n"
"buffer_load_dwordx4 v199, s[8:11], s76 offen lds\n"
"v_cvt_pk_bf16_f32 v92, v116, v118\n"
"v_mfma_f32_16x16x32_bf16 v[224:227], a[120:123], v[48:51], v[224:227]\n"
"v_exp_f32 v119, v119\n"
"v_mfma_f32_16x16x32_bf16 v[224:227], a[132:135], v[52:55], v[224:227]\n"
"s_waitcnt lgkmcnt(3)\n"
"v_cvt_pk_bf16_f32 v93, v117, v119\n"
"v_cvt_pk_bf16_f32 v228, v228, v229\n"
"v_mfma_f32_16x16x32_bf16 v[232:235], a[252:255], v[72:75], v[232:235]\n"
"s_waitcnt lgkmcnt(2)\n"
"ds_read_b128 v[116:119], v181 offset:27008\n"
"v_cvt_pk_bf16_f32 v229, v230, v231\n"
"v_mfma_f32_16x16x32_bf16 v[232:235], v[8:11], v[76:79], v[232:235]\n"
"buffer_load_dword v200, off, s[28:31], s39 sc1\n"
"v_cvt_pk_bf16_f32 v230, v216, v217\n"
"v_mfma_f32_16x16x32_bf16 v[240:243], v[4:7], v[72:75], v[240:243]\n"
"v_cvt_pk_bf16_f32 v231, v218, v219\n"
"v_cvt_pk_bf16_f32 v220, v220, v221\n"
"v_mfma_f32_16x16x32_bf16 v[240:243], v[16:19], v[76:79], v[240:243]\n"
"s_waitcnt lgkmcnt(1)\n"
"v_cvt_pk_bf16_f32 v221, v222, v223\n"
"v_cvt_pk_bf16_f32 v224, v224, v225\n"
"v_mfma_f32_16x16x32_bf16 v[236:239], v[0:3], v[72:75], v[236:239]\n"
"v_cvt_pk_bf16_f32 v225, v226, v227\n"
"v_mfma_f32_16x16x32_bf16 v[236:239], v[12:15], v[76:79], v[236:239]\n"
"s_add_u32 m0, s34, 2048\n"
"v_mfma_f32_16x16x32_bf16 v[236:239], v[24:27], v[68:71], v[236:239]\n"
"buffer_load_dwordx4 v190, s[8:11], s1 offen lds\n"
"s_add_u32 s1, s1, s33\n"
"v_mfma_f32_16x16x32_bf16 v[232:235], v[20:23], v[68:71], v[232:235]\n"
"v_mfma_f32_16x16x32_bf16 v[240:243], v[28:31], v[68:71], v[240:243]\n"
"v_smfmac_f32_16x16x64_bf16 v[252:255], v[90:93], v[128:135], v66\n"
"ds_read_b128 v[128:131], v178\n"
"v_smfmac_f32_16x16x64_bf16 v[98:101], v[90:93], v[120:127], v66\n"
"ds_read_b128 v[120:123], v179\n"
"v_smfmac_f32_16x16x64_bf16 v[106:109], v[90:93], v[136:143], v66\n"
"ds_read_b64_tr_b16 v[136:137], v182 offset:54144\n"
"v_mfma_f32_16x16x32_bf16 v[90:93], v[172:175], v[90:93], v[82:85]\n"
"v_cvt_pk_bf16_f32 v232, v232, v233\n"
"v_cvt_pk_bf16_f32 v233, v234, v235\n"
"v_mfma_f32_16x16x32_bf16 v[208:211], a[76:79], v[32:35], 0\n"
"ds_read_b64 v[82:83], v180 offset:13568\n"
"ds_read_b64 v[84:85], v181 offset:13568\n"
"v_mfma_f32_16x16x32_bf16 v[208:211], a[88:91], v[36:39], v[208:211]\n"
"v_cvt_pk_bf16_f32 v240, v240, v241\n"
"v_cvt_pk_bf16_f32 v234, v236, v237\n"
"v_mfma_f32_16x16x32_bf16 v[208:211], a[100:103], v[40:43], v[208:211]\n"
"s_add_u32 m0, s34, 5120\n"
"ds_read_b64 v[92:93], v181 offset:27072\n"
"buffer_load_dwordx4 v199, s[8:11], s77 offen lds\n"
"v_mfma_f32_16x16x32_bf16 v[208:211], a[112:115], v[44:47], v[208:211]\n"
"s_mul_i32 s8, s33, 0\n"
"v_cvt_pk_bf16_f32 v235, v238, v239\n"
"v_cvt_pk_bf16_f32 v241, v242, v243\n"
"v_mfma_f32_16x16x32_bf16 v[208:211], a[124:127], v[48:51], v[208:211]\n"
"s_mul_i32 s9, s33, 1\n"
"ds_write_b128 v196, v[232:235] offset:49152\n"
"v_mfma_f32_16x16x32_bf16 v[208:211], a[136:139], v[52:55], v[208:211]\n"
"s_mul_i32 s10, s33, 2\n"
"v_rcp_f32 v171, v90\n"
"v_mfma_f32_16x16x32_bf16 v[204:207], a[80:83], v[32:35], 0\n"
"s_mul_i32 s11, s33, 4\n"
"ds_write_b64 v197, v[240:241] offset:49152\n"
"v_mfma_f32_16x16x32_bf16 v[204:207], a[92:95], v[36:39], v[204:207]\n"
"s_waitcnt lgkmcnt(4)\n"
"v_cvt_pk_bf16_f32 v171, v171, v171\n"
"v_and_b32 v64, v171, v176\n"
"v_mfma_f32_16x16x32_bf16 v[244:247], v[244:247], v[148:151], 0\n"
"s_waitcnt lgkmcnt(3)\n"
"ds_read_b64 v[90:91], v180 offset:27072\n"
"v_and_b32 v65, v171, v177\n"
"v_mfma_f32_16x16x32_bf16 v[244:247], v[80:83], v[160:163], v[244:247]\n"
"s_waitcnt lgkmcnt(3)\n"
"v_cvt_pk_bf16_f32 v252, v252, v253\n"
"v_cvt_pk_bf16_f32 v253, v254, v255\n"
"v_mfma_f32_16x16x32_bf16 v[112:115], v[112:115], v[148:151], 0\n"
"v_cvt_pk_bf16_f32 v98, v98, v99\n"
"v_cvt_pk_bf16_f32 v99, v100, v101\n"
"v_mfma_f32_16x16x32_bf16 v[112:115], v[84:87], v[162:165], v[112:115]\n"
"ds_read_b64_tr_b16 v[138:139], v183 offset:54144\n"
"v_cvt_pk_bf16_f32 v106, v106, v107\n"
"v_mfma_f32_16x16x16_bf16 v[252:255], v[252:253], v[64:65], 0\n"
"ds_read_b64_tr_b16 v[140:141], v182 offset:54912\n"
"v_cvt_pk_bf16_f32 v107, v108, v109\n"
"v_mfma_f32_16x16x16_bf16 v[98:101], v[98:99], v[64:65], 0\n"
"s_waitcnt lgkmcnt(3)\n"
"ds_read_b64_tr_b16 v[142:143], v183 offset:54912\n"
"v_max_f32 v244, v244, v112\n"
"v_mfma_f32_16x16x16_bf16 v[106:109], v[106:107], v[64:65], 0\n"
"s_barrier\n"
"v_mfma_f32_16x16x32_bf16 v[116:119], v[116:119], v[148:151], 0\n"
"ds_read_b128 v[68:71], v192 offset:30720\n"
"ds_read_b128 v[132:135], v178 offset:13504\n"
"v_mfma_f32_16x16x32_bf16 v[116:119], v[92:95], v[162:165], v[116:119]\n"
"s_waitcnt lgkmcnt(5)\n"
"ds_read_b128 v[124:127], v179 offset:13504\n"
"v_cvt_pk_bf16_f32 v252, v252, v253\n"
"v_mfma_f32_16x16x32_bf16 v[248:251], v[248:251], v[148:151], 0\n"
"ds_write_b128 v186, v[228:231] offset:33760\n"
"v_mfma_f32_16x16x32_bf16 v[248:251], v[88:91], v[160:163], v[248:251]\n"
"v_cvt_pk_bf16_f32 v253, v254, v255\n"
"v_cvt_pk_bf16_f32 v254, v98, v99\n"
"v_mfma_f32_16x16x32_bf16 v[204:207], a[104:107], v[40:43], v[204:207]\n"
"ds_read_b64 v[98:99], v178 offset:64\n"
"ds_read_b128 v[72:75], v202 offset:49152\n"
"v_mfma_f32_16x16x32_bf16 v[204:207], a[116:119], v[44:47], v[204:207]\n"
"v_cvt_pk_bf16_f32 v255, v100, v101\n"
"v_cvt_pk_bf16_f32 v106, v106, v107\n"
"v_mfma_f32_16x16x32_bf16 v[204:207], a[128:131], v[48:51], v[204:207]\n"
"buffer_store_dwordx4 v[252:255], v192, s[28:31], s37 offen sc1\n"
"v_mfma_f32_16x16x32_bf16 v[204:207], a[140:143], v[52:55], v[204:207]\n"
"s_waitcnt lgkmcnt(5)\n"
"ds_read_b64 v[100:101], v179 offset:64\n"
"ds_read_b64 v[76:77], v62 offset:49152\n"
"v_mfma_f32_16x16x32_bf16 v[216:219], a[148:151], v[32:35], 0\n"
"v_cvt_pk_bf16_f32 v107, v108, v109\n"
"v_max_f32 v245, v245, v113\n"
"v_mfma_f32_16x16x32_bf16 v[216:219], a[160:163], v[36:39], v[216:219]\n"
"buffer_store_dwordx2 v[106:107], v193, s[28:31], s37 offen offset:1024 sc1\n"
"v_mfma_f32_16x16x32_bf16 v[216:219], a[172:175], v[40:43], v[216:219]\n"
"s_add_u32 s37, s37, 1572864\n"
"ds_read_b64 v[106:107], v178 offset:13568\n"
"ds_read_b64 v[108:109], v179 offset:13568\n"
"v_mfma_f32_16x16x32_bf16 v[216:219], a[184:187], v[44:47], v[216:219]\n"
"s_waitcnt lgkmcnt(5)\n"
"ds_read_b64_tr_b16 v[112:113], v182 offset:54152\n"
"v_max_f32 v246, v246, v114\n"
"v_mfma_f32_16x16x32_bf16 v[128:131], v[128:131], v[148:151], 0\n"
"s_waitcnt lgkmcnt(5)\n"
"v_max_f32 v247, v247, v115\n"
"v_max3_f32 v170, v244, v245, v246\n"
"v_mfma_f32_16x16x32_bf16 v[128:131], v[96:99], v[160:163], v[128:131]\n"
"buffer_store_dwordx4 v[72:75], v194, s[24:27], s36 offen sc1\n"
"v_mfma_f32_16x16x32_bf16 v[228:231], a[152:155], v[32:35], 0\n"
"s_waitcnt lgkmcnt(3)\n"
"ds_read_b128 v[72:75], v192 offset:31744\n"
"ds_read_b64_tr_b16 v[114:115], v183 offset:54152\n"
"v_mfma_f32_16x16x32_bf16 v[120:123], v[120:123], v[148:151], 0\n"
"v_max_f32 v248, v248, v116\n"
"v_max_f32 v249, v249, v117\n"
"v_mfma_f32_16x16x32_bf16 v[120:123], v[100:103], v[162:165], v[120:123]\n"
"buffer_store_dwordx2 v[76:77], v195, s[24:27], s36 offen sc1\n"
"v_mfma_f32_16x16x32_bf16 v[228:231], a[164:167], v[36:39], v[228:231]\n"
"s_waitcnt lgkmcnt(4)\n"
"ds_read_b128 v[76:79], v192 offset:32768\n"
"ds_read_b64_tr_b16 v[116:117], v182 offset:54920\n"
"v_mfma_f32_16x16x32_bf16 v[132:135], v[132:135], v[148:151], 0\n"
"s_waitcnt lgkmcnt(5)\n"
"v_max_f32 v250, v250, v118\n"
"v_max_f32 v251, v251, v119\n"
"v_mfma_f32_16x16x32_bf16 v[132:135], v[104:107], v[160:163], v[132:135]\n"
"s_add_u32 s36, s36, s33\n"
"ds_read_b64_tr_b16 v[118:119], v183 offset:54920\n"
"v_max3_f32 v170, v170, v247, v248\n"
"v_mfma_f32_16x16x32_bf16 v[124:127], v[124:127], v[148:151], 0\n"
"v_max3_f32 v170, v170, v249, v250\n"
"v_cvt_pk_bf16_f32 v226, v208, v209\n"
"v_mfma_f32_16x16x32_bf16 v[124:127], v[108:111], v[162:165], v[124:127]\n"
"v_cvt_pk_bf16_f32 v227, v210, v211\n"
"v_max_f32 v128, v128, v120\n"
"v_mfma_f32_16x16x32_bf16 v[232:235], a[216:219], v[68:71], 0\n"
"s_waitcnt lgkmcnt(4)\n"
"ds_write_b128 v186, v[224:227] offset:33888\n"
"v_mfma_f32_16x16x32_bf16 v[232:235], a[228:231], v[72:75], v[232:235]\n"
"v_cvt_pk_bf16_f32 v222, v204, v205\n"
"v_max_f32 v129, v129, v121\n"
"v_mfma_f32_16x16x32_bf16 v[228:231], a[176:179], v[40:43], v[228:231]\n"
"s_waitcnt lgkmcnt(3)\n"
"ds_read_b64_tr_b16 v[120:121], v182 offset:54088\n"
"v_cvt_pk_bf16_f32 v223, v206, v207\n"
"v_mfma_f32_16x16x32_bf16 v[228:231], a[188:191], v[44:47], v[228:231]\n"
"ds_write_b128 v186, v[220:223] offset:33824\n"
"v_mfma_f32_16x16x32_bf16 v[228:231], a[200:203], v[48:51], v[228:231]\n"
"v_max_f32 v130, v130, v122\n"
"v_max_f32 v131, v131, v123\n"
"v_mfma_f32_16x16x32_bf16 v[216:219], a[196:199], v[48:51], v[216:219]\n"
"ds_read_b64_tr_b16 v[122:123], v183 offset:54088\n"
"v_max3_f32 v170, v170, v251, v128\n"
"v_mfma_f32_16x16x32_bf16 v[236:239], a[220:223], v[68:71], 0\n"
"v_max3_f32 v170, v170, v129, v130\n"
"v_max_f32 v132, v132, v124\n"
"v_mfma_f32_16x16x32_bf16 v[236:239], a[232:235], v[72:75], v[236:239]\n"
"v_max_f32 v133, v133, v125\n"
"v_max_f32 v134, v134, v126\n"
"v_mfma_f32_16x16x32_bf16 v[240:243], a[224:227], v[68:71], 0\n"
"ds_read_b128 v[68:71], v192 offset:33792\n"
"ds_read_b64_tr_b16 v[124:125], v182 offset:54856\n"
"v_mfma_f32_16x16x32_bf16 v[240:243], a[236:239], v[72:75], v[240:243]\n"
"ds_read_b128 v[72:75], v192 offset:34816\n"
"v_max_f32 v135, v135, v127\n"
"v_mfma_f32_16x16x32_bf16 v[240:243], a[248:251], v[76:79], v[240:243]\n"
"ds_read_b64_tr_b16 v[126:127], v183 offset:54856\n"
"v_max3_f32 v170, v170, v131, v132\n"
"v_mfma_f32_16x16x32_bf16 v[212:215], a[144:147], v[32:35], 0\n"
"ds_read_b128 v[32:35], v188 offset:6144\n"
"v_max3_f32 v170, v170, v133, v134\n"
"v_mfma_f32_16x16x32_bf16 v[212:215], a[156:159], v[36:39], v[212:215]\n"
"ds_read_b128 v[36:39], v189 offset:6144\n"
"s_mov_b64 exec, s[46:47]\n"
"v_add_u32 v183, 0xc0, v183\n"
"s_mov_b64 exec, -1\n"
"v_mfma_f32_16x16x32_bf16 v[212:215], a[168:171], v[40:43], v[212:215]\n"
"ds_read_b128 v[40:43], v188 offset:7168\n"
"v_mov_b64 v[252:253], 0\n"
"v_mfma_f32_16x16x32_bf16 v[212:215], a[180:183], v[44:47], v[212:215]\n"
"ds_read_b128 v[44:47], v189 offset:7168\n"
"v_mov_b64 v[254:255], 0\n"
"v_mfma_f32_16x16x32_bf16 v[212:215], a[192:195], v[48:51], v[212:215]\n"
"s_waitcnt vmcnt(18) lgkmcnt(7)\n"
"ds_read_b128 v[48:51], v188 offset:8192\n"
"v_mov_b64 v[98:99], 0\n"
"v_mfma_f32_16x16x32_bf16 v[212:215], a[204:207], v[52:55], v[212:215]\n"
"v_mov_b64 v[100:101], 0\n"
"v_mov_b64 v[106:107], 0\n"
"v_mfma_f32_16x16x32_bf16 v[232:235], a[240:243], v[76:79], v[232:235]\n"
"v_mov_b64 v[108:109], 0\n"
"v_max_f32 v171, v170, v135\n"
"v_mfma_f32_16x16x32_bf16 v[232:235], a[252:255], v[68:71], v[232:235]\n"
"s_waitcnt lgkmcnt(6)\n"
"v_max_f32 v170, v170, v135\n"
"v_cvt_pk_bf16_f32 v148, v212, v213\n"
"v_mfma_f32_16x16x32_bf16 v[232:235], v[8:11], v[72:75], v[232:235]\n"
"s_waitcnt lgkmcnt(4)\n"
"v_permlane16_swap_b32 v170, v171\n"
"v_mfma_f32_16x16x32_bf16 v[236:239], a[244:247], v[76:79], v[236:239]\n"
"ds_read_b128 v[76:79], v192 offset:35840\n"
"v_max_f32 v170, v170, v171\n"
"v_mfma_f32_16x16x32_bf16 v[236:239], v[0:3], v[68:71], v[236:239]\n"
"v_mov_b32 v171, v170\n"
"v_cvt_pk_bf16_f32 v149, v214, v215\n"
"v_mfma_f32_16x16x32_bf16 v[236:239], v[12:15], v[72:75], v[236:239]\n"
"v_permlane32_swap_b32 v170, v171\n"
"v_max_f32 v170, v170, v171\n"
"v_mul_f32 v170, 0xbe553b95, v170\n"
"v_pk_fma_f32 v[244:245], v[244:245], s[70:71], v[170:171] op_sel_hi:[1,0,0]\n"
"v_pk_fma_f32 v[246:247], v[246:247], s[70:71], v[170:171] op_sel_hi:[1,0,0]\n"
"v_pk_fma_f32 v[248:249], v[248:249], s[70:71], v[170:171] op_sel_hi:[1,0,0]\n"
"v_pk_fma_f32 v[250:251], v[250:251], s[70:71], v[170:171] op_sel_hi:[1,0,0]\n"
"v_pk_fma_f32 v[128:129], v[128:129], s[70:71], v[170:171] op_sel_hi:[1,0,0]\n"
"v_pk_fma_f32 v[130:131], v[130:131], s[70:71], v[170:171] op_sel_hi:[1,0,0]\n"
"v_pk_fma_f32 v[132:133], v[132:133], s[70:71], v[170:171] op_sel_hi:[1,0,0]\n"
"v_pk_fma_f32 v[134:135], v[134:135], s[70:71], v[170:171] op_sel_hi:[1,0,0]\n"
"v_mfma_f32_16x16x32_bf16 v[216:219], a[208:211], v[52:55], v[216:219]\n"
"v_exp_f32 v244, v244\n"
"v_mfma_f32_16x16x32_bf16 v[240:243], v[4:7], v[68:71], v[240:243]\n"
"v_exp_f32 v245, v245\n"
"v_mfma_f32_16x16x32_bf16 v[240:243], v[16:19], v[72:75], v[240:243]\n"
"s_waitcnt lgkmcnt(0)\n"
"v_exp_f32 v246, v246\n"
"v_mfma_f32_16x16x32_bf16 v[240:243], v[28:31], v[76:79], v[240:243]\n"
"v_exp_f32 v247, v247\n"
"v_mfma_f32_16x16x32_bf16 v[232:235], v[20:23], v[76:79], v[232:235]\n"
"s_waitcnt vmcnt(9)\n"
"v_cvt_pk_bf16_f32 v82, v244, v246\n"
"v_cvt_pk_bf16_f32 v83, v245, v247\n"
"v_mfma_f32_16x16x32_bf16 v[236:239], v[24:27], v[76:79], v[236:239]\n"
"ds_write_b128 v202, v[56:59] offset:55296\n"
"v_mfma_f32_16x16x32_bf16 v[228:231], a[212:215], v[52:55], v[228:231]\n"
"s_waitcnt vmcnt(8)\n"
"v_exp_f32 v248, v248\n"
"v_mfma_f32_16x16x32_bf16 v[208:211], a[0:3], v[32:35], 0\n"
"ds_read_b128 v[52:55], v189 offset:8192\n"
"ds_write_b64 v203, v[60:61] offset:55296\n"
"v_mfma_f32_16x16x32_bf16 v[208:211], a[12:15], v[36:39], v[208:211]\n"
"v_exp_f32 v249, v249\n"
"v_mfma_f32_16x16x32_bf16 v[208:211], a[24:27], v[40:43], v[208:211]\n"
"ds_read_b64_tr_b16 v[244:245], v182 offset:128\n"
"ds_read_b64_tr_b16 v[246:247], v183 offset:128\n"
"v_mfma_f32_16x16x32_bf16 v[208:211], a[36:39], v[44:47], v[208:211]\n"
"v_exp_f32 v250, v250\n"
"v_mfma_f32_16x16x32_bf16 v[208:211], a[48:51], v[48:51], v[208:211]\n"
"v_exp_f32 v251, v251\n"
"v_mfma_f32_16x16x32_bf16 v[204:207], a[4:7], v[32:35], 0\n"
"v_cvt_pk_bf16_f32 v84, v248, v250\n"
"v_cvt_pk_bf16_f32 v85, v249, v251\n"
"v_mfma_f32_16x16x32_bf16 v[204:207], a[16:19], v[36:39], v[204:207]\n"
"ds_read_b64_tr_b16 v[248:249], v182 offset:896\n"
"ds_read_b64_tr_b16 v[250:251], v183 offset:896\n"
"v_smfmac_f32_16x16x64_bf16 v[252:255], v[82:85], v[136:143], v66\n"
"s_waitcnt vmcnt(6) lgkmcnt(5)\n"
"ds_read_b64_tr_b16 v[136:137], v182 offset:136\n"
"v_smfmac_f32_16x16x64_bf16 v[98:101], v[82:85], v[112:119], v66\n"
"v_add3_u32 v200, v200, s50, -8\n"
"v_smfmac_f32_16x16x64_bf16 v[106:109], v[82:85], v[120:127], v66\n"
"v_cmp_lt_u32 vcc, 64, v200\n"
"v_mfma_f32_16x16x32_bf16 v[82:85], v[172:175], v[82:85], 0\n"
"ds_read_b64_tr_b16 v[138:139], v183 offset:136\n"
"ds_read_b64_tr_b16 v[140:141], v182 offset:904\n"
"v_mfma_f32_16x16x32_bf16 v[208:211], a[60:63], v[52:55], v[208:211]\n"
"s_nop 0\n"
"s_cbranch_vccnz .Lp7%=\n"
".Lr7%=:\n"
"v_mfma_f32_16x16x32_bf16 v[204:207], a[28:31], v[40:43], v[204:207]\n"
"buffer_load_dwordx4 a[198:201], v192, s[28:31], s38 offen sc1\n"
"ds_read_b64_tr_b16 v[142:143], v183 offset:904\n"
"v_mfma_f32_16x16x32_bf16 v[204:207], a[40:43], v[44:47], v[204:207]\n"
"ds_read_b64_tr_b16 v[112:113], v182 offset:72\n"
"ds_read_b64_tr_b16 v[114:115], v183 offset:72\n"
"v_mfma_f32_16x16x32_bf16 v[204:207], a[52:55], v[48:51], v[204:207]\n"
"v_cvt_pk_bf16_f32 v232, v232, v233\n"
"v_cvt_pk_bf16_f32 v233, v234, v235\n"
"v_mfma_f32_16x16x32_bf16 v[204:207], a[64:67], v[52:55], v[204:207]\n"
"buffer_load_dwordx2 a[202:203], v193, s[28:31], s38 offen offset:1024 sc1\n"
"ds_read_b64_tr_b16 v[116:117], v182 offset:840\n"
"v_mfma_f32_16x16x32_bf16 v[220:223], a[8:11], v[32:35], 0\n"
"s_add_u32 s38, s38, 1572864\n"
"ds_read_b64_tr_b16 v[118:119], v183 offset:840\n"
"ds_read_b128 v[120:123], v180 offset:27008\n"
"v_mfma_f32_16x16x32_bf16 v[220:223], a[20:23], v[36:39], v[220:223]\n"
"v_cvt_pk_bf16_f32 v240, v240, v241\n"
"v_cvt_pk_bf16_f32 v234, v236, v237\n"
"v_mfma_f32_16x16x32_bf16 v[220:223], a[32:35], v[40:43], v[220:223]\n"
"ds_read_b128 v[124:127], v178\n"
"v_cvt_pk_bf16_f32 v235, v238, v239\n"
"v_mfma_f32_16x16x32_bf16 v[220:223], a[44:47], v[44:47], v[220:223]\n"
"buffer_load_dwordx4 a[204:207], v192, s[28:31], s38 offen sc1\n"
"ds_write_b128 v196, v[232:235] offset:36864\n"
"v_mfma_f32_16x16x32_bf16 v[220:223], a[56:59], v[48:51], v[220:223]\n"
"v_cvt_pk_bf16_f32 v241, v242, v243\n"
"v_cvt_pk_bf16_f32 v150, v216, v217\n"
"v_mfma_f32_16x16x32_bf16 v[220:223], a[68:71], v[52:55], v[220:223]\n"
"ds_write_b64 v197, v[240:241] offset:36864\n"
"v_mfma_f32_16x16x32_bf16 v[224:227], a[72:75], v[32:35], 0\n"
"buffer_load_dwordx2 a[208:209], v193, s[28:31], s38 offen offset:1024 sc1\n"
"v_cvt_pk_bf16_f32 v151, v218, v219\n"
"v_mfma_f32_16x16x32_bf16 v[224:227], a[84:87], v[36:39], v[224:227]\n"
"s_add_u32 s38, s38, 1572864\n"
"v_cvt_pk_bf16_f32 v162, v228, v229\n"
"v_cvt_pk_bf16_f32 v163, v230, v231\n"
"v_mfma_f32_16x16x32_bf16 v[224:227], a[96:99], v[40:43], v[224:227]\n"
"s_waitcnt vmcnt(6)\n"
"v_exp_f32 v128, v128\n"
"v_mfma_f32_16x16x32_bf16 v[224:227], a[108:111], v[44:47], v[224:227]\n"
"s_add_u32 s12, s38, 0\n"
"v_or_b32 v201, 10, s6\n"
"v_exp_f32 v129, v129\n"
"v_mfma_f32_16x16x32_bf16 v[224:227], a[120:123], v[48:51], v[224:227]\n"
"s_add_u32 s13, s38, 1572864\n"
"s_mov_b64 exec, 1\n"
"buffer_store_dword v201, off, s[28:31], s40 sc1\n"
"s_mov_b64 exec, -1\n"
"v_mfma_f32_16x16x32_bf16 v[224:227], a[132:135], v[52:55], v[224:227]\n"
"s_add_u32 s14, s38, 3145728\n"
"v_exp_f32 v130, v130\n"
"v_mfma_f32_16x16x32_bf16 v[212:215], a[76:79], v[32:35], 0\n"
"s_waitcnt lgkmcnt(0)\n"
"buffer_load_dwordx4 a[144:147], v192, s[28:31], s12 offen sc1\n"
"v_cvt_pk_bf16_f32 v90, v128, v130\n"
"v_mfma_f32_16x16x32_bf16 v[212:215], a[88:91], v[36:39], v[212:215]\n"
"s_barrier\n"
"v_mfma_f32_16x16x32_bf16 v[212:215], a[100:103], v[40:43], v[212:215]\n"
"s_add_u32 s16, s38, 4718592\n"
"ds_read_b128 v[76:79], v192 offset:55296\n"
"ds_read_b128 v[68:71], v202 offset:36864\n"
"v_mfma_f32_16x16x32_bf16 v[212:215], a[112:115], v[44:47], v[212:215]\n"
"s_add_u32 s15, s38, 6291456\n"
"v_exp_f32 v131, v131\n"
"v_mfma_f32_16x16x32_bf16 v[212:215], a[124:127], v[48:51], v[212:215]\n"
"s_add_u32 s17, s38, 7864320\n"
"buffer_load_dwordx2 a[148:149], v193, s[28:31], s12 offen offset:1024 sc1\n"
"v_cvt_pk_bf16_f32 v91, v129, v131\n"
"v_mfma_f32_16x16x32_bf16 v[212:215], a[136:139], v[52:55], v[212:215]\n"
"s_add_u32 s18, s38, 9437184\n"
"ds_read_b128 v[128:131], v181 offset:27008\n"
"v_exp_f32 v132, v132\n"
"v_mfma_f32_16x16x32_bf16 v[216:219], a[80:83], v[32:35], 0\n"
"ds_read_b128 v[32:35], v188\n"
"ds_read_b64 v[72:73], v62 offset:36864\n"
"v_mfma_f32_16x16x32_bf16 v[216:219], a[92:95], v[36:39], v[216:219]\n"
"v_exp_f32 v133, v133\n"
"v_mfma_f32_16x16x32_bf16 v[216:219], a[104:107], v[40:43], v[216:219]\n"
"ds_read_b128 v[36:39], v189\n"
"ds_read_b128 v[40:43], v188 offset:1024\n"
"v_mfma_f32_16x16x32_bf16 v[216:219], a[116:119], v[44:47], v[216:219]\n"
"s_waitcnt lgkmcnt(6)\n"
"v_exp_f32 v134, v134\n"
"v_mfma_f32_16x16x32_bf16 v[216:219], a[128:131], v[48:51], v[216:219]\n"
"s_waitcnt lgkmcnt(5)\n"
"v_exp_f32 v135, v135\n"
"v_mfma_f32_16x16x32_bf16 v[232:235], a[216:219], v[76:79], 0\n"
"ds_read_b128 v[44:47], v189 offset:1024\n"
"v_cvt_pk_bf16_f32 v92, v132, v134\n"
"v_mfma_f32_16x16x32_bf16 v[236:239], a[220:223], v[76:79], 0\n"
"v_cvt_pk_bf16_f32 v93, v133, v135\n"
"v_cvt_pk_bf16_f32 v208, v208, v209\n"
"v_mfma_f32_16x16x32_bf16 v[240:243], a[224:227], v[76:79], 0\n"
"ds_read_b128 v[132:135], v179\n"
"ds_read_b128 v[76:79], v192 offset:58368\n"
"v_smfmac_f32_16x16x64_bf16 v[252:255], v[90:93], v[244:251], v66\n"
"s_waitcnt lgkmcnt(5)\n"
"ds_read_b128 v[244:247], v178 offset:13504\n"
"v_smfmac_f32_16x16x64_bf16 v[98:101], v[90:93], v[136:143], v66\n"
"ds_read_b128 v[136:139], v179 offset:13504\n"
"v_smfmac_f32_16x16x64_bf16 v[106:109], v[90:93], v[112:119], v66\n"
"ds_read_b128 v[248:251], v178 offset:27008\n"
"v_mfma_f32_16x16x32_bf16 v[90:93], v[172:175], v[90:93], v[82:85]\n"
"ds_read_b128 v[140:143], v179 offset:27008\n"
"buffer_store_dwordx4 v[68:71], v194, s[24:27], s36 offen sc1\n"
"v_mfma_f32_16x16x32_bf16 v[216:219], a[140:143], v[52:55], v[216:219]\n"
"ds_read_b64 v[82:83], v180 offset:27072\n"
"ds_read_b64 v[84:85], v181 offset:27072\n"
"v_mfma_f32_16x16x32_bf16 v[228:231], a[0:3], v[32:35], 0\n"
"s_waitcnt lgkmcnt(10)\n"
"ds_read_b128 v[68:71], v192 offset:56320\n"
"v_cvt_pk_bf16_f32 v252, v252, v253\n"
"v_mfma_f32_16x16x32_bf16 v[228:231], a[12:15], v[36:39], v[228:231]\n"
"s_waitcnt lgkmcnt(10)\n"
"ds_read_b64 v[92:93], v179 offset:64\n"
"v_cvt_pk_bf16_f32 v253, v254, v255\n"
"v_mfma_f32_16x16x32_bf16 v[228:231], a[24:27], v[40:43], v[228:231]\n"
"s_waitcnt lgkmcnt(10)\n"
"ds_read_b128 v[48:51], v188 offset:2048\n"
"v_cvt_pk_bf16_f32 v98, v98, v99\n"
"v_mfma_f32_16x16x32_bf16 v[228:231], a[36:39], v[44:47], v[228:231]\n"
"v_rcp_f32 v171, v90\n"
"ds_read_b64 v[90:91], v178 offset:64\n"
"v_cvt_pk_bf16_f32 v171, v171, v171\n"
"v_and_b32 v64, v171, v176\n"
"v_and_b32 v65, v171, v177\n"
"v_cvt_pk_bf16_f32 v99, v100, v101\n"
"v_cvt_pk_bf16_f32 v106, v106, v107\n"
"v_mfma_f32_16x16x16_bf16 v[252:255], v[252:253], v[64:65], 0\n"
"ds_read_b128 v[52:55], v189 offset:2048\n"
"buffer_store_dwordx2 v[72:73], v195, s[24:27], s36 offen sc1\n"
"v_mfma_f32_16x16x16_bf16 v[98:101], v[98:99], v[64:65], 0\n"
"s_waitcnt lgkmcnt(5)\n"
"ds_read_b128 v[72:75], v192 offset:57344\n"
"ds_read_b64_tr_b16 v[112:113], v184 offset:128\n"
"v_mfma_f32_16x16x32_bf16 v[120:123], v[120:123], v[152:155], 0\n"
"s_waitcnt lgkmcnt(6)\n"
"ds_read_b64_tr_b16 v[114:115], v185 offset:128\n"
"v_cvt_pk_bf16_f32 v107, v108, v109\n"
"v_mfma_f32_16x16x32_bf16 v[120:123], v[80:83], v[164:167], v[120:123]\n"
"s_waitcnt lgkmcnt(6)\n"
"ds_read_b64_tr_b16 v[116:117], v184 offset:896\n"
"v_cvt_pk_bf16_f32 v252, v252, v253\n"
"v_mfma_f32_16x16x32_bf16 v[128:131], v[128:131], v[152:155], 0\n"
"s_waitcnt lgkmcnt(6)\n"
"ds_read_b64_tr_b16 v[118:119], v185 offset:896\n"
"v_cvt_pk_bf16_f32 v253, v254, v255\n"
"v_mfma_f32_16x16x32_bf16 v[128:131], v[84:87], v[166:169], v[128:131]\n"
"s_waitcnt lgkmcnt(6)\n"
"v_cvt_pk_bf16_f32 v254, v98, v99\n"
"v_cvt_pk_bf16_f32 v255, v100, v101\n"
"v_mfma_f32_16x16x32_bf16 v[124:127], v[124:127], v[152:155], 0\n"
"s_add_u32 s36, s36, s33\n"
"buffer_store_dwordx4 v[252:255], v192, s[28:31], s37 offen sc1\n"
"v_mfma_f32_16x16x32_bf16 v[124:127], v[88:91], v[164:167], v[124:127]\n"
"ds_read_b64 v[98:99], v178 offset:13568\n"
"ds_read_b64 v[100:101], v179 offset:13568\n"
"v_mfma_f32_16x16x32_bf16 v[132:135], v[132:135], v[152:155], 0\n"
"v_cvt_pk_bf16_f32 v209, v210, v211\n"
"v_cvt_pk_bf16_f32 v210, v204, v205\n"
"v_mfma_f32_16x16x32_bf16 v[132:135], v[92:95], v[166:169], v[132:135]\n"
"ds_read_b64_tr_b16 a[168:169], v182 offset:13632\n"
"ds_read_b64_tr_b16 a[170:171], v183 offset:13632\n"
"v_mfma_f32_16x16x16_bf16 v[106:109], v[106:107], v[64:65], 0\n"
"v_cvt_pk_bf16_f32 v211, v206, v207\n"
"v_cvt_pk_bf16_f32 v220, v220, v221\n"
"v_mfma_f32_16x16x32_bf16 v[232:235], a[228:231], v[68:71], v[232:235]\n"
"s_waitcnt lgkmcnt(8)\n"
"ds_write_b128 v186, v[208:211] offset:47264\n"
"v_mfma_f32_16x16x32_bf16 v[232:235], a[240:243], v[72:75], v[232:235]\n"
"v_cvt_pk_bf16_f32 v221, v222, v223\n"
"v_max_f32 v120, v120, v128\n"
"v_mfma_f32_16x16x32_bf16 v[236:239], a[232:235], v[68:71], v[236:239]\n"
".Lic6%=:\n"
"s_cbranch_execz .Lib1%=\n"
"v_cvt_pk_bf16_f32 v106, v106, v107\n"
"v_cvt_pk_bf16_f32 v107, v108, v109\n"
"v_mfma_f32_16x16x32_bf16 v[236:239], a[244:247], v[72:75], v[236:239]\n"
"s_waitcnt lgkmcnt(4)\n"
"buffer_store_dwordx2 v[106:107], v193, s[28:31], s37 offen offset:1024 sc1\n"
"v_mfma_f32_16x16x32_bf16 v[244:247], v[244:247], v[152:155], 0\n"
"s_waitcnt lgkmcnt(3)\n"
"ds_read_b64 v[106:107], v178 offset:27072\n"
"ds_read_b64 v[108:109], v179 offset:27072\n"
"v_mfma_f32_16x16x32_bf16 v[244:247], v[96:99], v[164:167], v[244:247]\n"
"s_add_u32 s37, s37, 1572864\n"
"ds_read_b64_tr_b16 a[172:173], v182 offset:14400\n"
"v_max_f32 v121, v121, v129\n"
"v_mfma_f32_16x16x32_bf16 v[136:139], v[136:139], v[152:155], 0\n"
"ds_read_b64_tr_b16 v[128:129], v184 offset:136\n"
"v_max_f32 v122, v122, v130\n"
"v_mfma_f32_16x16x32_bf16 v[136:139], v[100:103], v[166:169], v[136:139]\n"
"ds_read_b64_tr_b16 a[174:175], v183 offset:14400\n"
"v_max_f32 v123, v123, v131\n"
"v_mfma_f32_16x16x32_bf16 v[240:243], a[236:239], v[68:71], v[240:243]\n"
"ds_read_b128 v[68:71], v192 offset:59392\n"
"v_max3_f32 v170, v120, v121, v122\n"
"v_mfma_f32_16x16x32_bf16 v[240:243], a[248:251], v[72:75], v[240:243]\n"
"ds_read_b128 v[72:75], v192 offset:60416\n"
"v_max_f32 v124, v124, v132\n"
"v_mfma_f32_16x16x32_bf16 v[240:243], v[4:7], v[76:79], v[240:243]\n"
"ds_read_b64_tr_b16 v[130:131], v185 offset:136\n"
"v_max_f32 v125, v125, v133\n"
"v_mfma_f32_16x16x32_bf16 v[232:235], a[252:255], v[76:79], v[232:235]\n"
"ds_read_b64_tr_b16 v[132:133], v184 offset:904\n"
"v_max_f32 v126, v126, v134\n"
"v_mfma_f32_16x16x32_bf16 v[236:239], v[0:3], v[76:79], v[236:239]\n"
"s_waitcnt lgkmcnt(8)\n"
"ds_read_b64_tr_b16 a[176:177], v182 offset:13640\n"
"v_max_f32 v127, v127, v135\n"
"v_mfma_f32_16x16x32_bf16 v[248:251], v[248:251], v[152:155], 0\n"
"s_waitcnt lgkmcnt(8)\n"
"ds_read_b64_tr_b16 v[134:135], v185 offset:904\n"
"v_max3_f32 v170, v170, v123, v124\n"
"v_mfma_f32_16x16x32_bf16 v[248:251], v[104:107], v[164:167], v[248:251]\n"
"ds_read_b64_tr_b16 a[178:179], v183 offset:13640\n"
"v_max3_f32 v170, v170, v125, v126\n"
"v_mfma_f32_16x16x32_bf16 v[140:143], v[140:143], v[152:155], 0\n"
"ds_read_b64_tr_b16 a[180:181], v182 offset:14408\n"
"v_max_f32 v244, v244, v136\n"
"v_mfma_f32_16x16x32_bf16 v[140:143], v[108:111], v[166:169], v[140:143]\n"
"ds_read_b64_tr_b16 a[182:183], v183 offset:14408\n"
"v_max_f32 v245, v245, v137\n"
"v_mfma_f32_16x16x32_bf16 v[228:231], a[48:51], v[48:51], v[228:231]\n"
"s_waitcnt lgkmcnt(8)\n"
"ds_read_b64_tr_b16 v[136:137], v184 offset:72\n"
"v_max_f32 v246, v246, v138\n"
"v_mfma_f32_16x16x32_bf16 v[232:235], v[8:11], v[68:71], v[232:235]\n"
"s_waitcnt lgkmcnt(8)\n"
"ds_read_b64_tr_b16 a[184:185], v182 offset:13576\n"
"v_max_f32 v247, v247, v139\n"
"v_mfma_f32_16x16x32_bf16 v[232:235], v[20:23], v[72:75], v[232:235]\n"
"ds_read_b64_tr_b16 v[138:139], v185 offset:72\n"
"v_max3_f32 v170, v170, v127, v244\n"
"v_mfma_f32_16x16x32_bf16 v[240:243], v[16:19], v[68:71], v[240:243]\n"
"ds_read_b64_tr_b16 a[186:187], v183 offset:13576\n"
"v_max3_f32 v170, v170, v245, v246\n"
"v_mfma_f32_16x16x32_bf16 v[240:243], v[28:31], v[72:75], v[240:243]\n"
"ds_read_b64_tr_b16 a[188:189], v182 offset:14344\n"
"v_max_f32 v248, v248, v140\n"
"v_mfma_f32_16x16x32_bf16 v[236:239], v[12:15], v[68:71], v[236:239]\n"
"ds_read_b64_tr_b16 a[190:191], v183 offset:14344\n"
"v_max_f32 v249, v249, v141\n"
"v_mfma_f32_16x16x32_bf16 v[236:239], v[24:27], v[72:75], v[236:239]\n"
"ds_read_b64_tr_b16 v[140:141], v184 offset:840\n"
"v_max_f32 v250, v250, v142\n"
"v_mfma_f32_16x16x32_bf16 v[228:231], a[60:63], v[52:55], v[228:231]\n"
"v_max_f32 v251, v251, v143\n"
"v_max3_f32 v170, v170, v247, v248\n"
"v_mfma_f32_16x16x32_bf16 v[204:207], a[4:7], v[32:35], 0\n"
"ds_read_b64_tr_b16 v[142:143], v185 offset:840\n"
"v_max3_f32 v170, v170, v249, v250\n"
"v_mfma_f32_16x16x32_bf16 v[204:207], a[16:19], v[36:39], v[204:207]\n"
"v_mov_b64 v[252:253], 0\n"
"v_mov_b64 v[254:255], 0\n"
"v_mfma_f32_16x16x32_bf16 v[204:207], a[28:31], v[40:43], v[204:207]\n"
"v_mov_b64 v[98:99], 0\n"
"v_mov_b64 v[100:101], 0\n"
"v_mfma_f32_16x16x32_bf16 v[204:207], a[40:43], v[44:47], v[204:207]\n"
"v_mov_b64 v[106:107], 0\n"
"v_mov_b64 v[108:109], 0\n"
"v_mfma_f32_16x16x32_bf16 v[204:207], a[52:55], v[48:51], v[204:207]\n"
"v_max_f32 v171, v170, v251\n"
"v_max_f32 v170, v170, v251\n"
"v_mfma_f32_16x16x32_bf16 v[204:207], a[64:67], v[52:55], v[204:207]\n"
"s_nop 0\n"
"v_permlane16_swap_b32 v170, v171\n"
"v_mfma_f32_16x16x32_bf16 v[208:211], a[8:11], v[32:35], 0\n"
"v_max_f32 v170, v170, v171\n"
"v_mov_b32 v171, v170\n"
"v_mfma_f32_16x16x32_bf16 v[208:211], a[20:23], v[36:39], v[208:211]\n"
"s_nop 0\n"
"v_permlane32_swap_b32 v170, v171\n"
"v_max_f32 v170, v170, v171\n"
"v_mul_f32 v170, 0xbe553b95, v170\n"
"v_pk_fma_f32 v[120:121], v[120:121], s[70:71], v[170:171] op_sel_hi:[1,0,0]\n"
"v_pk_fma_f32 v[122:123], v[122:123], s[70:71], v[170:171] op_sel_hi:[1,0,0]\n"
"v_pk_fma_f32 v[124:125], v[124:125], s[70:71], v[170:171] op_sel_hi:[1,0,0]\n"
"v_pk_fma_f32 v[126:127], v[126:127], s[70:71], v[170:171] op_sel_hi:[1,0,0]\n"
"v_pk_fma_f32 v[244:245], v[244:245], s[70:71], v[170:171] op_sel_hi:[1,0,0]\n"
"v_pk_fma_f32 v[246:247], v[246:247], s[70:71], v[170:171] op_sel_hi:[1,0,0]\n"
"v_pk_fma_f32 v[248:249], v[248:249], s[70:71], v[170:171] op_sel_hi:[1,0,0]\n"
"v_pk_fma_f32 v[250:251], v[250:251], s[70:71], v[170:171] op_sel_hi:[1,0,0]\n"
"v_mfma_f32_16x16x32_bf16 v[208:211], a[32:35], v[40:43], v[208:211]\n"
"s_waitcnt vmcnt(10)\n"
"v_cvt_pk_bf16_f32 v224, v224, v225\n"
"v_cvt_pk_bf16_f32 v225, v226, v227\n"
"v_mfma_f32_16x16x32_bf16 v[208:211], a[44:47], v[44:47], v[208:211]\n"
"ds_write_b128 v202, a[198:201] offset:30720\n"
"v_mfma_f32_16x16x32_bf16 v[208:211], a[56:59], v[48:51], v[208:211]\n"
"v_exp_f32 v120, v120\n"
"v_mfma_f32_16x16x32_bf16 v[208:211], a[68:71], v[52:55], v[208:211]\n"
"s_waitcnt vmcnt(9)\n"
"ds_write_b64 v203, a[202:203] offset:30720\n"
"buffer_load_dword v200, off, s[28:31], s39 sc1\n"
"v_cvt_pk_bf16_f32 v226, v212, v213\n"
"v_exp_f32 v121, v121\n"
"v_cvt_pk_bf16_f32 v227, v214, v215\n"
"v_mfma_f32_16x16x32_bf16 v[212:215], a[72:75], v[32:35], 0\n"
"ds_write_b128 v186, v[224:227] offset:47392\n"
"v_mfma_f32_16x16x32_bf16 v[212:215], a[84:87], v[36:39], v[212:215]\n"
"v_exp_f32 v122, v122\n"
"v_mfma_f32_16x16x32_bf16 v[212:215], a[96:99], v[40:43], v[212:215]\n"
"v_exp_f32 v123, v123\n"
"v_mfma_f32_16x16x32_bf16 v[212:215], a[108:111], v[44:47], v[212:215]\n"
"v_cvt_pk_bf16_f32 v222, v216, v217\n"
"v_cvt_pk_bf16_f32 v82, v120, v122\n"
"v_mfma_f32_16x16x32_bf16 v[212:215], a[120:123], v[48:51], v[212:215]\n"
"v_cvt_pk_bf16_f32 v223, v218, v219\n"
"v_cvt_pk_bf16_f32 v83, v121, v123\n"
"v_mfma_f32_16x16x32_bf16 v[212:215], a[132:135], v[52:55], v[212:215]\n"
"ds_write_b128 v186, v[220:223] offset:47328\n"
"v_mfma_f32_16x16x32_bf16 v[216:219], a[76:79], v[32:35], 0\n"
"v_exp_f32 v124, v124\n"
"v_mfma_f32_16x16x32_bf16 v[216:219], a[88:91], v[36:39], v[216:219]\n"
"ds_read_b128 v[120:123], v178\n"
"v_cvt_pk_bf16_f32 v232, v232, v233\n"
"v_mfma_f32_16x16x32_bf16 v[216:219], a[100:103], v[40:43], v[216:219]\n"
"v_exp_f32 v125, v125\n"
"v_mfma_f32_16x16x32_bf16 v[216:219], a[112:115], v[44:47], v[216:219]\n"
"v_exp_f32 v126, v126\n"
"v_mfma_f32_16x16x32_bf16 v[216:219], a[124:127], v[48:51], v[216:219]\n"
"v_exp_f32 v127, v127\n"
"v_mfma_f32_16x16x32_bf16 v[216:219], a[136:139], v[52:55], v[216:219]\n"
"v_cvt_pk_bf16_f32 v84, v124, v126\n"
"v_cvt_pk_bf16_f32 v85, v125, v127\n"
"v_mfma_f32_16x16x32_bf16 v[220:223], a[80:83], v[32:35], 0\n"
"s_waitcnt lgkmcnt(5)\n"
"ds_read_b128 v[124:127], v178 offset:13504\n"
"v_cvt_pk_bf16_f32 v233, v234, v235\n"
"v_smfmac_f32_16x16x64_bf16 v[252:255], v[82:85], v[112:119], v66\n"
"ds_read_b128 v[112:115], v179\n"
"v_smfmac_f32_16x16x64_bf16 v[98:101], v[82:85], v[128:135], v66\n"
"ds_read_b128 v[116:119], v179 offset:13504\n"
"v_smfmac_f32_16x16x64_bf16 v[106:109], v[82:85], v[136:143], v66\n"
"ds_read_b128 v[128:131], v178 offset:27008\n"
"v_mfma_f32_16x16x32_bf16 v[82:85], v[172:175], v[82:85], 0\n"
"ds_read_b128 v[136:139], v179 offset:27008\n"
"v_cvt_pk_bf16_f32 v240, v240, v241\n"
"v_mfma_f32_16x16x32_bf16 v[220:223], a[92:95], v[36:39], v[220:223]\n"
"v_cvt_pk_bf16_f32 v234, v236, v237\n"
"v_cvt_pk_bf16_f32 v235, v238, v239\n"
"v_mfma_f32_16x16x32_bf16 v[220:223], a[104:107], v[40:43], v[220:223]\n"
"ds_write_b128 v196, v[232:235] offset:43008\n"
"v_mfma_f32_16x16x32_bf16 v[220:223], a[116:119], v[44:47], v[220:223]\n"
"v_cvt_pk_bf16_f32 v241, v242, v243\n"
"v_exp_f32 v244, v244\n"
"v_mfma_f32_16x16x32_bf16 v[220:223], a[128:131], v[48:51], v[220:223]\n"
"ds_write_b64 v197, v[240:241] offset:43008\n"
"v_mfma_f32_16x16x32_bf16 v[220:223], a[140:143], v[52:55], v[220:223]\n"
"v_exp_f32 v245, v245\n"
"v_exp_f32 v246, v246\n"
"v_exp_f32 v247, v247\n"
"v_cvt_pk_bf16_f32 v90, v244, v246\n"
"v_cvt_pk_bf16_f32 v91, v245, v247\n"
"v_exp_f32 v248, v248\n"
"v_exp_f32 v249, v249\n"
"v_exp_f32 v250, v250\n"
"v_exp_f32 v251, v251\n"
"v_cvt_pk_bf16_f32 v92, v248, v250\n"
"v_cvt_pk_bf16_f32 v93, v249, v251\n"
"s_mov_b64 exec, s[44:45]\n"
"v_add_u32 v181, 0xc0, v181\n"
"s_mov_b64 exec, -1\n"
"v_smfmac_f32_16x16x64_bf16 v[252:255], v[90:93], a[168:175], v66\n"
"ds_read_b64_tr_b16 v[244:245], v182 offset:128\n"
"v_smfmac_f32_16x16x64_bf16 v[98:101], v[90:93], a[176:183], v66\n"
"s_waitcnt lgkmcnt(1)\n"
"ds_read_b64_tr_b16 v[246:247], v183 offset:128\n"
"v_smfmac_f32_16x16x64_bf16 v[106:109], v[90:93], a[184:191], v66\n"
"s_barrier\n"
"v_mfma_f32_16x16x32_bf16 v[90:93], v[172:175], v[90:93], v[82:85]\n"
"s_waitcnt vmcnt(5)\n"
"ds_write_b128 v202, a[144:147]\n"
"v_cvt_pk_bf16_f32 v252, v252, v253\n"
"v_cvt_pk_bf16_f32 v253, v254, v255\n"
"v_cvt_pk_bf16_f32 v98, v98, v99\n"
"v_cvt_pk_bf16_f32 v99, v100, v101\n"
"s_nop 1\n"
"v_rcp_f32 v171, v90\n"
"ds_write_b64 v203, a[148:149]\n"
"v_cvt_pk_bf16_f32 v171, v171, v171\n"
"v_and_b32 v64, v171, v176\n"
"v_and_b32 v65, v171, v177\n"
"v_cvt_pk_bf16_f32 v106, v106, v107\n"
"v_cvt_pk_bf16_f32 v107, v108, v109\n"
"v_mfma_f32_16x16x16_bf16 v[252:255], v[252:253], v[64:65], 0\n"
"s_waitcnt vmcnt(1)\n"
"ds_read_b128 v[72:75], v192 offset:30720\n"
"v_or_b32 v201, 11, s6\n"
"v_mfma_f32_16x16x16_bf16 v[98:101], v[98:99], v[64:65], 0\n"
"s_mov_b64 exec, 1\n"
"buffer_store_dword v201, off, s[28:31], s40 sc1\n"
"s_mov_b64 exec, -1\n"
"v_mfma_f32_16x16x16_bf16 v[106:109], v[106:107], v[64:65], 0\n"
"ds_read_b64 v[82:83], v178 offset:64\n"
"ds_read_b64 v[84:85], v179 offset:64\n"
"v_cvt_pk_bf16_f32 v252, v252, v253\n"
"v_cvt_pk_bf16_f32 v253, v254, v255\n"
"ds_read_b64 v[90:91], v178 offset:13568\n"
"v_cvt_pk_bf16_f32 v254, v98, v99\n"
"ds_read_b64 v[92:93], v179 offset:13568\n"
"v_cvt_pk_bf16_f32 v255, v100, v101\n"
"v_cvt_pk_bf16_f32 v106, v106, v107\n"
"v_cvt_pk_bf16_f32 v107, v108, v109\n"
"v_min_u32 v252, -2, v252\n"
"v_min_u32 v253, -2, v253\n"
"v_min_u32 v254, -2, v254\n"
"v_min_u32 v255, -2, v255\n"
"v_min_u32 v106, -2, v106\n"
"v_min_u32 v107, -2, v107\n"
"v_cvt_pk_bf16_f32 v228, v228, v229\n"
"v_cvt_pk_bf16_f32 v229, v230, v231\n"
"v_cvt_pk_bf16_f32 v230, v204, v205\n"
"v_cvt_pk_bf16_f32 v231, v206, v207\n"
"v_cvt_pk_bf16_f32 v208, v208, v209\n"
"v_cvt_pk_bf16_f32 v209, v210, v211\n"
"v_cvt_pk_bf16_f32 v212, v212, v213\n"
"v_cvt_pk_bf16_f32 v213, v214, v215\n"
"v_cvt_pk_bf16_f32 v214, v216, v217\n"
"v_cvt_pk_bf16_f32 v215, v218, v219\n"
"v_cvt_pk_bf16_f32 v210, v220, v221\n"
"v_cvt_pk_bf16_f32 v211, v222, v223\n"
"s_waitcnt lgkmcnt(3)\n"
"v_mfma_f32_16x16x32_bf16 v[120:123], v[120:123], v[144:147], 0\n"
"s_waitcnt lgkmcnt(2)\n"
"buffer_store_dwordx4 v[252:255], v192, s[28:31], s37 offen sc1\n"
"v_mfma_f32_16x16x32_bf16 v[120:123], v[80:83], v[156:159], v[120:123]\n"
"s_waitcnt lgkmcnt(0)\n"
"ds_read_b64 v[98:99], v178 offset:27072\n"
"ds_read_b64 v[100:101], v179 offset:27072\n"
"v_mfma_f32_16x16x32_bf16 v[112:115], v[112:115], v[144:147], 0\n"
"ds_read_b128 v[132:135], v180\n"
"buffer_store_dwordx2 v[106:107], v193, s[28:31], s37 offen offset:1024 sc1\n"
"v_mfma_f32_16x16x32_bf16 v[112:115], v[84:87], v[158:161], v[112:115]\n"
"s_waitcnt vmcnt(3)\n"
"ds_read_b64 v[106:107], v180 offset:64\n"
"v_add3_u32 v200, v200, s50, -10\n"
"v_mfma_f32_16x16x32_bf16 v[124:127], v[124:127], v[144:147], 0\n"
"s_add_u32 s37, s37, 1572864\n"
"v_cmp_lt_u32 vcc, 64, v200\n"
"v_mov_b64 v[252:253], 0\n"
"v_mfma_f32_16x16x32_bf16 v[124:127], v[88:91], v[156:159], v[124:127]\n"
"ds_read_b128 v[140:143], v181\n"
"ds_read_b64 v[108:109], v181 offset:64\n"
"v_mfma_f32_16x16x32_bf16 v[116:119], v[116:119], v[144:147], 0\n"
"s_cbranch_vccnz .Lp9%=\n"
".Lr9%=:\n"
"v_mfma_f32_16x16x32_bf16 v[116:119], v[92:95], v[158:161], v[116:119]\n"
"buffer_load_dwordx4 a[150:153], v192, s[28:31], s13 offen sc1\n"
"ds_read_b128 v[76:79], v202 offset:43008\n"
"v_mfma_f32_16x16x32_bf16 v[232:235], a[216:219], v[72:75], 0\n"
"s_waitcnt lgkmcnt(6)\n"
"ds_write_b128 v202, a[204:207] offset:55296\n"
"v_mfma_f32_16x16x32_bf16 v[128:131], v[128:131], v[144:147], 0\n"
"s_waitcnt lgkmcnt(6)\n"
"v_max_f32 v120, v120, v112\n"
"v_max_f32 v121, v121, v113\n"
"v_mfma_f32_16x16x32_bf16 v[128:131], v[96:99], v[156:159], v[128:131]\n"
"s_waitcnt lgkmcnt(4)\n"
"buffer_load_dwordx2 a[154:155], v193, s[28:31], s13 offen offset:1024 sc1\n"
"ds_write_b128 v198, v[228:231]\n"
"v_mfma_f32_16x16x32_bf16 v[136:139], v[136:139], v[144:147], 0\n"
"v_max_f32 v122, v122, v114\n"
"v_max_f32 v123, v123, v115\n"
"v_mfma_f32_16x16x32_bf16 v[136:139], v[100:103], v[158:161], v[136:139]\n"
"s_waitcnt lgkmcnt(3)\n"
"ds_read_b64 v[68:69], v62 offset:43008\n"
"buffer_load_dwordx4 a[156:159], v192, s[28:31], s14 offen sc1\n"
"v_mfma_f32_16x16x32_bf16 v[132:135], v[132:135], v[144:147], 0\n"
"s_waitcnt lgkmcnt(3)\n"
"ds_read_b64_tr_b16 v[248:249], v182 offset:896\n"
"ds_read_b64_tr_b16 v[250:251], v183 offset:896\n"
"v_mfma_f32_16x16x32_bf16 v[132:135], v[104:107], v[156:159], v[132:135]\n"
"ds_read_b64_tr_b16 v[112:113], v182 offset:136\n"
"v_max3_f32 v170, v120, v121, v122\n"
"v_mfma_f32_16x16x32_bf16 v[140:143], v[140:143], v[144:147], 0\n"
"ds_read_b64_tr_b16 v[114:115], v183 offset:136\n"
"v_max_f32 v124, v124, v116\n"
"v_mfma_f32_16x16x32_bf16 v[140:143], v[108:111], v[158:161], v[140:143]\n"
"ds_write_b64 v203, a[208:209] offset:55296\n"
"v_mfma_f32_16x16x32_bf16 v[236:239], a[220:223], v[72:75], 0\n"
"buffer_load_dwordx2 a[160:161], v193, s[28:31], s14 offen offset:1024 sc1\n"
"v_max_f32 v125, v125, v117\n"
"v_mfma_f32_16x16x32_bf16 v[240:243], a[224:227], v[72:75], 0\n"
"ds_read_b128 v[72:75], v192 offset:33792\n"
"v_max_f32 v126, v126, v118\n"
"v_max_f32 v127, v127, v119\n"
"v_max3_f32 v170, v170, v123, v124\n"
"v_max3_f32 v170, v170, v125, v126\n"
"v_max_f32 v128, v128, v136\n"
"v_max_f32 v129, v129, v137\n"
"v_max_f32 v130, v130, v138\n"
"buffer_store_dwordx4 v[76:79], v194, s[24:27], s36 offen sc1\n"
"s_nop 1\n"
"ds_read_b128 v[76:79], v192 offset:31744\n"
"ds_read_b64_tr_b16 v[116:117], v182 offset:904\n"
"v_max_f32 v131, v131, v139\n"
"v_max3_f32 v170, v170, v127, v128\n"
"v_max3_f32 v170, v170, v129, v130\n"
"v_max_f32 v132, v132, v140\n"
"ds_read_b64_tr_b16 v[118:119], v183 offset:904\n"
"v_max_f32 v133, v133, v141\n"
"v_max_f32 v134, v134, v142\n"
"s_waitcnt lgkmcnt(9)\n"
"buffer_store_dwordx2 v[68:69], v195, s[24:27], s36 offen sc1\n"
"ds_read_b128 v[68:71], v192 offset:32768\n"
"s_add_u32 s36, s36, s33\n"
"v_max_f32 v135, v135, v143\n"
"v_max3_f32 v170, v170, v131, v132\n"
"v_max3_f32 v170, v170, v133, v134\n"
"v_mov_b64 v[254:255], 0\n"
"v_mov_b64 v[98:99], 0\n"
"v_mov_b64 v[100:101], 0\n"
"v_mov_b64 v[106:107], 0\n"
"v_mov_b64 v[108:109], 0\n"
"v_max_f32 v171, v170, v135\n"
"v_max_f32 v170, v170, v135\n"
"ds_read_b64_tr_b16 v[136:137], v182 offset:72\n"
"ds_read_b64_tr_b16 v[138:139], v183 offset:72\n"
"v_permlane16_swap_b32 v170, v171\n"
"v_max_f32 v170, v170, v171\n"
"v_mov_b32 v171, v170\n"
"s_waitcnt lgkmcnt(5)\n"
"v_mfma_f32_16x16x32_bf16 v[240:243], a[236:239], v[76:79], v[240:243]\n"
"ds_read_b64_tr_b16 v[140:141], v182 offset:840\n"
"ds_read_b64_tr_b16 v[142:143], v183 offset:840\n"
"v_mfma_f32_16x16x32_bf16 v[232:235], a[228:231], v[76:79], v[232:235]\n"
"ds_write_b128 v198, v[208:211] offset:64\n"
"v_mfma_f32_16x16x32_bf16 v[236:239], a[232:235], v[76:79], v[236:239]\n"
"s_waitcnt lgkmcnt(5)\n"
"v_permlane32_swap_b32 v170, v171\n"
"v_max_f32 v170, v170, v171\n"
"v_mul_f32 v170, 0xbe553b95, v170\n"
"v_pk_fma_f32 v[120:121], v[120:121], s[70:71], v[170:171] op_sel_hi:[1,0,0]\n"
"v_pk_fma_f32 v[122:123], v[122:123], s[70:71], v[170:171] op_sel_hi:[1,0,0]\n"
"v_pk_fma_f32 v[124:125], v[124:125], s[70:71], v[170:171] op_sel_hi:[1,0,0]\n"
"v_pk_fma_f32 v[126:127], v[126:127], s[70:71], v[170:171] op_sel_hi:[1,0,0]\n"
"v_pk_fma_f32 v[128:129], v[128:129], s[70:71], v[170:171] op_sel_hi:[1,0,0]\n"
"v_pk_fma_f32 v[130:131], v[130:131], s[70:71], v[170:171] op_sel_hi:[1,0,0]\n"
"v_pk_fma_f32 v[132:133], v[132:133], s[70:71], v[170:171] op_sel_hi:[1,0,0]\n"
"v_pk_fma_f32 v[134:135], v[134:135], s[70:71], v[170:171] op_sel_hi:[1,0,0]\n"
"v_mfma_f32_16x16x32_bf16 v[236:239], a[244:247], v[68:71], v[236:239]\n"
"ds_read_b128 v[76:79], v192 offset:34816\n"
"ds_write_b128 v198, v[212:215] offset:128\n"
"v_mfma_f32_16x16x32_bf16 v[236:239], v[0:3], v[72:75], v[236:239]\n"
"v_exp_f32 v120, v120\n"
"v_mfma_f32_16x16x32_bf16 v[232:235], a[240:243], v[68:71], v[232:235]\n"
"v_exp_f32 v121, v121\n"
"v_mfma_f32_16x16x32_bf16 v[232:235], a[252:255], v[72:75], v[232:235]\n"
"v_exp_f32 v122, v122\n"
"v_mfma_f32_16x16x32_bf16 v[240:243], a[248:251], v[68:71], v[240:243]\n"
"ds_read_b128 v[68:71], v192 offset:35840\n"
"v_cvt_pk_bf16_f32 v82, v120, v122\n"
"v_mfma_f32_16x16x32_bf16 v[240:243], v[4:7], v[72:75], v[240:243]\n"
"v_exp_f32 v123, v123\n"
"v_exp_f32 v124, v124\n"
"v_cvt_pk_bf16_f32 v83, v121, v123\n"
"v_exp_f32 v125, v125\n"
"v_exp_f32 v126, v126\n"
"v_exp_f32 v127, v127\n"
"v_cvt_pk_bf16_f32 v84, v124, v126\n"
"v_cvt_pk_bf16_f32 v85, v125, v127\n"
"v_exp_f32 v128, v128\n"
"s_nop 0\n"
"v_smfmac_f32_16x16x64_bf16 v[252:255], v[82:85], v[244:251], v66\n"
"s_waitcnt lgkmcnt(2)\n"
"ds_read_b64_tr_b16 v[120:121], v182 offset:27136\n"
"v_smfmac_f32_16x16x64_bf16 v[98:101], v[82:85], v[112:119], v66\n"
"ds_read_b64_tr_b16 v[122:123], v183 offset:27136\n"
"v_smfmac_f32_16x16x64_bf16 v[106:109], v[82:85], v[136:143], v66\n"
"s_waitcnt lgkmcnt(2)\n"
"ds_read_b64_tr_b16 v[124:125], v182 offset:27904\n"
"v_mfma_f32_16x16x32_bf16 v[82:85], v[172:175], v[82:85], 0\n"
"s_barrier\n"
"v_mfma_f32_16x16x32_bf16 v[232:235], v[8:11], v[76:79], v[232:235]\n"
"ds_read_b128 v[72:75], v192 offset:55296\n"
"ds_read_b64_tr_b16 v[126:127], v183 offset:27904\n"
"v_mfma_f32_16x16x32_bf16 v[232:235], v[20:23], v[68:71], v[232:235]\n"
"ds_read_b64_tr_b16 v[244:245], v182 offset:27144\n"
"v_exp_f32 v129, v129\n"
"v_mfma_f32_16x16x32_bf16 v[236:239], v[12:15], v[76:79], v[236:239]\n"
"ds_read_b64_tr_b16 v[246:247], v183 offset:27144\n"
"v_exp_f32 v130, v130\n"
"v_mfma_f32_16x16x32_bf16 v[236:239], v[24:27], v[68:71], v[236:239]\n"
"ds_read_b64_tr_b16 v[248:249], v182 offset:27912\n"
"v_cvt_pk_bf16_f32 v90, v128, v130\n"
"v_mfma_f32_16x16x32_bf16 v[240:243], v[16:19], v[76:79], v[240:243]\n"
"ds_read_b128 v[76:79], v192 offset:56320\n"
"v_exp_f32 v131, v131\n"
"v_mfma_f32_16x16x32_bf16 v[240:243], v[28:31], v[68:71], v[240:243]\n"
"ds_read_b128 v[68:71], v192 offset:57344\n"
"ds_read_b64_tr_b16 v[250:251], v183 offset:27912\n"
"v_cvt_pk_bf16_f32 v91, v129, v131\n"
"v_exp_f32 v132, v132\n"
"ds_read_b64_tr_b16 v[112:113], v182 offset:27080\n"
"v_exp_f32 v133, v133\n"
"ds_read_b64_tr_b16 v[114:115], v183 offset:27080\n"
"ds_read_b64_tr_b16 v[116:117], v182 offset:27848\n"
"v_exp_f32 v134, v134\n"
"v_exp_f32 v135, v135\n"
"ds_read_b64_tr_b16 v[118:119], v183 offset:27848\n"
"v_cvt_pk_bf16_f32 v92, v132, v134\n"
"v_cvt_pk_bf16_f32 v93, v133, v135\n"
"v_cvt_pk_bf16_f32 v232, v232, v233\n"
"v_cvt_pk_bf16_f32 v233, v234, v235\n"
"v_cvt_pk_bf16_f32 v234, v236, v237\n"
"v_cvt_pk_bf16_f32 v235, v238, v239\n"
"v_cvt_pk_bf16_f32 v240, v240, v241\n"
"v_cvt_pk_bf16_f32 v241, v242, v243\n"
"s_waitcnt lgkmcnt(10)\n"
"v_smfmac_f32_16x16x64_bf16 v[252:255], v[90:93], v[120:127], v66\n"
"ds_read_b128 v[136:139], v178 offset:13504\n"
"v_mfma_f32_16x16x32_bf16 v[236:239], a[220:223], v[72:75], 0\n"
"s_waitcnt lgkmcnt(5)\n"
"ds_read_b128 v[128:131], v179 offset:13504\n"
"ds_read_b128 v[140:143], v178 offset:27008\n"
"v_smfmac_f32_16x16x64_bf16 v[98:101], v[90:93], v[244:251], v66\n"
"buffer_load_dword v201, off, s[28:31], s39 sc1\n"
"v_mfma_f32_16x16x32_bf16 v[236:239], a[232:235], v[76:79], v[236:239]\n"
"ds_read_b128 v[132:135], v179 offset:27008\n"
"ds_read_b128 v[120:123], v180\n"
"v_mfma_f32_16x16x32_bf16 v[236:239], a[244:247], v[68:71], v[236:239]\n"
"v_cvt_pk_bf16_f32 v252, v252, v253\n"
"v_cvt_pk_bf16_f32 v253, v254, v255\n"
"s_nop 0\n"
"v_cvt_pk_bf16_f32 v98, v98, v99\n"
"v_cvt_pk_bf16_f32 v99, v100, v101\n"
"s_waitcnt lgkmcnt(5)\n"
"v_smfmac_f32_16x16x64_bf16 v[106:109], v[90:93], v[112:119], v66\n"
"ds_read_b128 v[244:247], v181\n"
"v_mfma_f32_16x16x32_bf16 v[90:93], v[172:175], v[90:93], v[82:85]\n"
"ds_read_b128 v[124:127], v180 offset:13504\n"
"ds_read_b128 v[248:251], v181 offset:13504\n"
"s_nop 3\n"
"v_cvt_pk_bf16_f32 v106, v106, v107\n"
"ds_read_b64 v[82:83], v178 offset:13568\n"
"ds_read_b64 v[84:85], v179 offset:13568\n"
"v_rcp_f32 v171, v90\n"
"v_cvt_pk_bf16_f32 v107, v108, v109\n"
"v_cvt_pk_bf16_f32 v171, v171, v171\n"
"v_and_b32 v64, v171, v176\n"
"v_and_b32 v65, v171, v177\n"
"s_nop 1\n"
"v_mfma_f32_16x16x16_bf16 v[252:255], v[252:253], v[64:65], 0\n"
"ds_read_b64 v[90:91], v178 offset:27072\n"
"ds_read_b64 v[92:93], v179 offset:27072\n"
"v_mfma_f32_16x16x16_bf16 v[98:101], v[98:99], v[64:65], 0\n"
"ds_write_b128 v196, v[232:235] offset:49152\n"
"v_mfma_f32_16x16x16_bf16 v[106:109], v[106:107], v[64:65], 0\n"
"buffer_load_dwordx4 v[228:231], v192, s[28:31], s15 offen sc1\n"
"v_mfma_f32_16x16x32_bf16 v[232:235], a[216:219], v[72:75], 0\n"
"ds_write_b64 v197, v[240:241] offset:49152\n"
"v_mfma_f32_16x16x32_bf16 v[232:235], a[228:231], v[76:79], v[232:235]\n"
"s_waitcnt lgkmcnt(5)\n"
"v_cvt_pk_bf16_f32 v252, v252, v253\n"
"v_cvt_pk_bf16_f32 v253, v254, v255\n"
"v_mfma_f32_16x16x32_bf16 v[136:139], v[136:139], v[148:151], 0\n"
"s_waitcnt lgkmcnt(4)\n"
"buffer_load_dwordx2 v[152:153], v193, s[28:31], s15 offen offset:1024 sc1\n"
"ds_read_b64_tr_b16 v[112:113], v182 offset:40640\n"
"v_mfma_f32_16x16x32_bf16 v[136:139], v[80:83], v[160:163], v[136:139]\n"
"ds_read_b64_tr_b16 v[114:115], v183 offset:40640\n"
"ds_read_b64_tr_b16 v[116:117], v182 offset:41408\n"
"v_mfma_f32_16x16x32_bf16 v[128:131], v[128:131], v[148:151], 0\n"
"s_waitcnt lgkmcnt(6)\n"
"v_cvt_pk_bf16_f32 v254, v98, v99\n"
"v_cvt_pk_bf16_f32 v255, v100, v101\n"
"v_mfma_f32_16x16x32_bf16 v[128:131], v[84:87], v[162:165], v[128:131]\n"
"s_waitcnt lgkmcnt(5)\n"
"ds_read_b64 v[98:99], v180 offset:64\n"
"ds_read_b64 v[100:101], v181 offset:64\n"
"v_mfma_f32_16x16x32_bf16 v[140:143], v[140:143], v[148:151], 0\n"
"v_cvt_pk_bf16_f32 v106, v106, v107\n"
"v_cvt_pk_bf16_f32 v107, v108, v109\n"
"v_mfma_f32_16x16x32_bf16 v[140:143], v[88:91], v[160:163], v[140:143]\n"
"ds_read_b64 v[108:109], v181 offset:13568\n"
"ds_read_b64_tr_b16 v[118:119], v183 offset:41408\n"
"v_mfma_f32_16x16x32_bf16 v[132:135], v[132:135], v[148:151], 0\n"
"v_min_u32 v252, -2, v252\n"
"v_min_u32 v253, -2, v253\n"
"v_mfma_f32_16x16x32_bf16 v[132:135], v[92:95], v[162:165], v[132:135]\n"
"v_min_u32 v254, -2, v254\n"
"v_min_u32 v255, -2, v255\n"
"v_mfma_f32_16x16x32_bf16 v[240:243], a[224:227], v[72:75], 0\n"
"buffer_store_dwordx4 v[252:255], v192, s[28:31], s37 offen sc1\n"
"v_mfma_f32_16x16x32_bf16 v[240:243], a[236:239], v[76:79], v[240:243]\n"
"ds_read_b128 v[72:75], v192 offset:58368\n"
"ds_read_b128 v[76:79], v192 offset:59392\n"
"v_mfma_f32_16x16x32_bf16 v[240:243], a[248:251], v[68:71], v[240:243]\n"
"s_waitcnt lgkmcnt(5)\n"
"v_min_u32 v106, -2, v106\n"
"v_min_u32 v107, -2, v107\n"
"v_mfma_f32_16x16x32_bf16 v[120:123], v[120:123], v[148:151], 0\n"
"s_waitcnt lgkmcnt(4)\n"
"buffer_store_dwordx2 v[106:107], v193, s[28:31], s37 offen offset:1024 sc1\n"
"v_mfma_f32_16x16x32_bf16 v[120:123], v[96:99], v[160:163], v[120:123]\n"
"s_waitcnt lgkmcnt(3)\n"
"ds_read_b64 v[106:107], v180 offset:13568\n"
"v_max_f32 v136, v136, v128\n"
"v_mfma_f32_16x16x32_bf16 v[244:247], v[244:247], v[148:151], 0\n"
"s_waitcnt vmcnt(10)\n"
"v_max_f32 v137, v137, v129\n"
"v_max_f32 v138, v138, v130\n"
"v_mfma_f32_16x16x32_bf16 v[244:247], v[100:103], v[162:165], v[244:247]\n"
"s_add_u32 s37, s37, 1572864\n"
"ds_write_b128 v202, a[150:153] offset:24576\n"
"v_mfma_f32_16x16x32_bf16 v[248:251], v[248:251], v[148:151], 0\n"
"v_max_f32 v139, v139, v131\n"
"v_max3_f32 v170, v136, v137, v138\n"
"v_mfma_f32_16x16x32_bf16 v[248:251], v[108:111], v[162:165], v[248:251]\n"
"s_waitcnt vmcnt(7) lgkmcnt(3)\n"
"ds_read_b64_tr_b16 v[128:129], v182 offset:40648\n"
"ds_write_b64 v203, a[154:155] offset:24576\n"
"v_mfma_f32_16x16x32_bf16 v[232:235], a[240:243], v[68:71], v[232:235]\n"
"v_max_f32 v140, v140, v132\n"
"v_max_f32 v141, v141, v133\n"
"v_mfma_f32_16x16x32_bf16 v[232:235], a[252:255], v[72:75], v[232:235]\n"
"s_waitcnt lgkmcnt(3)\n"
"ds_write_b128 v202, a[156:159] offset:30720\n"
"v_mfma_f32_16x16x32_bf16 v[124:127], v[124:127], v[148:151], 0\n"
"v_max_f32 v142, v142, v134\n"
"v_max3_f32 v170, v170, v139, v140\n"
"v_mfma_f32_16x16x32_bf16 v[124:127], v[104:107], v[160:163], v[124:127]\n"
"ds_write_b64 v203, a[160:161] offset:30720\n"
"v_mfma_f32_16x16x32_bf16 v[240:243], v[4:7], v[72:75], v[240:243]\n"
"v_max_f32 v143, v143, v135\n"
"v_max3_f32 v170, v170, v141, v142\n"
"v_mfma_f32_16x16x32_bf16 v[240:243], v[16:19], v[76:79], v[240:243]\n"
"ds_read_b128 v[68:71], v192 offset:60416\n"
"ds_read_b64_tr_b16 v[130:131], v183 offset:40648\n"
"v_mfma_f32_16x16x32_bf16 v[236:239], v[0:3], v[72:75], v[236:239]\n"
"ds_read_b64_tr_b16 v[132:133], v182 offset:41416\n"
"v_max_f32 v120, v120, v244\n"
"v_mfma_f32_16x16x32_bf16 v[236:239], v[12:15], v[76:79], v[236:239]\n"
"ds_read_b64_tr_b16 v[134:135], v183 offset:41416\n"
"v_max_f32 v121, v121, v245\n"
"v_mfma_f32_16x16x32_bf16 v[232:235], v[8:11], v[76:79], v[232:235]\n"
"v_max_f32 v122, v122, v246\n"
"v_max3_f32 v170, v170, v143, v120\n"
"v_max_f32 v123, v123, v247\n"
"v_max3_f32 v170, v170, v121, v122\n"
"v_max_f32 v124, v124, v248\n"
"v_max_f32 v125, v125, v249\n"
"v_max_f32 v126, v126, v250\n"
"v_max3_f32 v170, v170, v123, v124\n"
"v_max_f32 v127, v127, v251\n"
"v_max3_f32 v170, v170, v125, v126\n"
"v_max_f32 v171, v170, v127\n"
"v_max_f32 v170, v170, v127\n"
"v_mov_b64 v[252:253], 0\n"
"v_mov_b64 v[254:255], 0\n"
"v_permlane16_swap_b32 v170, v171\n"
"v_max_f32 v170, v170, v171\n"
"v_mov_b32 v171, v170\n"
"v_mov_b64 v[98:99], 0\n"
"v_mov_b64 v[100:101], 0\n"
"v_permlane32_swap_b32 v170, v171\n"
"v_max_f32 v170, v170, v171\n"
"v_mul_f32 v170, 0xbe553b95, v170\n"
"v_pk_fma_f32 v[136:137], v[136:137], s[70:71], v[170:171] op_sel_hi:[1,0,0]\n"
"v_pk_fma_f32 v[138:139], v[138:139], s[70:71], v[170:171] op_sel_hi:[1,0,0]\n"
"v_pk_fma_f32 v[140:141], v[140:141], s[70:71], v[170:171] op_sel_hi:[1,0,0]\n"
"v_pk_fma_f32 v[142:143], v[142:143], s[70:71], v[170:171] op_sel_hi:[1,0,0]\n"
"v_pk_fma_f32 v[120:121], v[120:121], s[70:71], v[170:171] op_sel_hi:[1,0,0]\n"
"v_pk_fma_f32 v[122:123], v[122:123], s[70:71], v[170:171] op_sel_hi:[1,0,0]\n"
"v_pk_fma_f32 v[124:125], v[124:125], s[70:71], v[170:171] op_sel_hi:[1,0,0]\n"
"v_pk_fma_f32 v[126:127], v[126:127], s[70:71], v[170:171] op_sel_hi:[1,0,0]\n"
"v_exp_f32 v136, v136\n"
"v_exp_f32 v137, v137\n"
"v_exp_f32 v138, v138\n"
"v_exp_f32 v139, v139\n"
"v_exp_f32 v140, v140\n"
"v_exp_f32 v141, v141\n"
"v_exp_f32 v142, v142\n"
"v_exp_f32 v143, v143\n"
"v_mov_b64 v[106:107], 0\n"
"v_mov_b64 v[108:109], 0\n"
"v_cvt_pk_bf16_f32 v82, v136, v138\n"
"v_cvt_pk_bf16_f32 v83, v137, v139\n"
"v_cvt_pk_bf16_f32 v84, v140, v142\n"
"v_cvt_pk_bf16_f32 v85, v141, v143\n"
"v_exp_f32 v120, v120\n"
"s_nop 0\n"
"v_smfmac_f32_16x16x64_bf16 v[252:255], v[82:85], a[168:175], v66\n"
"s_waitcnt vmcnt(4) lgkmcnt(3)\n"
"v_add3_u32 v201, v201, s50, -11\n"
"v_mfma_f32_16x16x32_bf16 v[232:235], v[20:23], v[68:71], v[232:235]\n"
"s_barrier\n"
"v_mfma_f32_16x16x32_bf16 v[240:243], v[28:31], v[68:71], v[240:243]\n"
".Lic7%=:\n"
"s_cbranch_execz .Lib1%=\n"
"v_cmp_lt_u32 vcc, 64, v201\n"
"v_exp_f32 v121, v121\n"
"v_mfma_f32_16x16x32_bf16 v[236:239], v[24:27], v[68:71], v[236:239]\n"
"ds_read_b128 v[68:71], v192\n"
"ds_read_b128 v[32:35], v192 offset:1024\n"
"v_smfmac_f32_16x16x64_bf16 v[98:101], v[82:85], a[176:183], v66\n"
"s_cbranch_vccnz .Lp10%=\n"
".Lr10%=:\n"
"v_smfmac_f32_16x16x64_bf16 v[106:109], v[82:85], a[184:191], v66\n"
"buffer_load_dwordx4 a[162:165], v192, s[28:31], s16 offen sc1\n"
"v_mfma_f32_16x16x32_bf16 v[82:85], v[172:175], v[82:85], 0\n"
"ds_read_b128 v[36:39], v192 offset:2048\n"
"ds_read_b128 v[40:43], v192 offset:3072\n"
"v_cvt_pk_bf16_f32 v232, v232, v233\n"
"v_cvt_pk_bf16_f32 v233, v234, v235\n"
"ds_read_b128 v[44:47], v192 offset:4096\n"
"v_cvt_pk_bf16_f32 v240, v240, v241\n"
"ds_read_b128 v[48:51], v192 offset:5120\n"
"buffer_load_dwordx2 a[166:167], v193, s[28:31], s16 offen offset:1024 sc1\n"
"ds_read_b64_tr_b16 v[244:245], v182 offset:40584\n"
"v_cvt_pk_bf16_f32 v234, v236, v237\n"
"v_cvt_pk_bf16_f32 v235, v238, v239\n"
"v_cvt_pk_bf16_f32 v241, v242, v243\n"
"v_exp_f32 v122, v122\n"
"v_exp_f32 v123, v123\n"
"v_exp_f32 v124, v124\n"
"v_exp_f32 v125, v125\n"
"v_exp_f32 v126, v126\n"
"v_exp_f32 v127, v127\n"
"v_cvt_pk_bf16_f32 v90, v120, v122\n"
"v_cvt_pk_bf16_f32 v91, v121, v123\n"
"v_cvt_pk_bf16_f32 v92, v124, v126\n"
"v_cvt_pk_bf16_f32 v93, v125, v127\n"
"s_waitcnt lgkmcnt(6)\n"
"v_mfma_f32_16x16x32_bf16 v[216:219], a[216:219], v[68:71], 0\n"
"s_waitcnt lgkmcnt(5)\n"
"ds_read_b64_tr_b16 v[246:247], v183 offset:40584\n"
"ds_read_b64_tr_b16 v[248:249], v182 offset:41352\n"
"v_smfmac_f32_16x16x64_bf16 v[252:255], v[90:93], v[112:119], v66\n"
"ds_read_b64_tr_b16 v[250:251], v183 offset:41352\n"
"v_smfmac_f32_16x16x64_bf16 v[98:101], v[90:93], v[128:135], v66\n"
"ds_write_b128 v196, v[232:235] offset:36864\n"
"v_mfma_f32_16x16x32_bf16 v[220:223], a[220:223], v[68:71], 0\n"
"v_mfma_f32_16x16x32_bf16 v[220:223], a[232:235], v[32:35], v[220:223]\n"
"s_waitcnt lgkmcnt(8)\n"
"ds_write_b64 v197, v[240:241] offset:36864\n"
"v_mfma_f32_16x16x32_bf16 v[220:223], a[244:247], v[36:39], v[220:223]\n"
"s_waitcnt lgkmcnt(8)\n"
"v_cvt_pk_bf16_f32 v252, v252, v253\n"
"v_cvt_pk_bf16_f32 v253, v254, v255\n"
"v_mfma_f32_16x16x32_bf16 v[220:223], v[0:3], v[40:43], v[220:223]\n"
"s_waitcnt lgkmcnt(7)\n"
"ds_read_b128 v[72:75], v202 offset:49152\n"
"ds_read_b64 v[76:77], v62 offset:49152\n"
"v_mfma_f32_16x16x32_bf16 v[220:223], v[12:15], v[44:47], v[220:223]\n"
"s_waitcnt lgkmcnt(8)\n"
"v_cvt_pk_bf16_f32 v98, v98, v99\n"
"v_cvt_pk_bf16_f32 v99, v100, v101\n"
"v_mfma_f32_16x16x32_bf16 v[220:223], v[24:27], v[48:51], v[220:223]\n"
"s_waitcnt lgkmcnt(4)\n"
"v_mfma_f32_16x16x32_bf16 v[224:227], a[224:227], v[68:71], 0\n"
"v_smfmac_f32_16x16x64_bf16 v[106:109], v[90:93], v[244:251], v66\n"
"v_mfma_f32_16x16x32_bf16 v[90:93], v[172:175], v[90:93], v[82:85]\n"
"v_mfma_f32_16x16x32_bf16 v[216:219], a[228:231], v[32:35], v[216:219]\n"
"v_mfma_f32_16x16x32_bf16 v[216:219], a[240:243], v[36:39], v[216:219]\n"
"v_mfma_f32_16x16x32_bf16 v[216:219], a[252:255], v[40:43], v[216:219]\n"
"s_waitcnt lgkmcnt(0)\n"
"buffer_store_dwordx4 v[72:75], v194, s[24:27], s36 offen sc1\n"
"v_mfma_f32_16x16x32_bf16 v[216:219], v[8:11], v[44:47], v[216:219]\n"
"s_barrier\n"
"v_mfma_f32_16x16x32_bf16 v[216:219], v[20:23], v[48:51], v[216:219]\n"
"ds_read_b128 v[68:71], v192 offset:25600\n"
"v_cvt_pk_bf16_f32 v106, v106, v107\n"
"v_mfma_f32_16x16x32_bf16 v[224:227], a[236:239], v[32:35], v[224:227]\n"
"ds_read_b128 v[32:35], v192 offset:26624\n"
"buffer_store_dwordx2 v[76:77], v195, s[24:27], s36 offen sc1\n"
"v_mfma_f32_16x16x32_bf16 v[224:227], a[248:251], v[36:39], v[224:227]\n"
"s_add_u32 s36, s36, s33\n"
"ds_read_b128 v[76:79], v192 offset:24576\n"
"ds_read_b128 v[36:39], v192 offset:27648\n"
"v_mfma_f32_16x16x32_bf16 v[224:227], v[4:7], v[40:43], v[224:227]\n"
"ds_read_b128 v[40:43], v192 offset:28672\n"
"v_cvt_pk_bf16_f32 v107, v108, v109\n"
"v_mfma_f32_16x16x32_bf16 v[224:227], v[16:19], v[44:47], v[224:227]\n"
"ds_read_b128 v[44:47], v192 offset:29696\n"
"v_rcp_f32 v171, v90\n"
"v_mfma_f32_16x16x32_bf16 v[224:227], v[28:31], v[48:51], v[224:227]\n"
"v_cvt_pk_bf16_f32 v171, v171, v171\n"
"v_and_b32 v64, v171, v176\n"
"v_and_b32 v65, v171, v177\n"
"v_cvt_pk_bf16_f32 v216, v216, v217\n"
"v_cvt_pk_bf16_f32 v217, v218, v219\n"
"v_mfma_f32_16x16x16_bf16 v[252:255], v[252:253], v[64:65], 0\n"
"ds_read_b128 v[48:51], v192 offset:30720\n"
"ds_read_b128 v[52:55], v202 offset:36864\n"
"v_mfma_f32_16x16x16_bf16 v[98:101], v[98:99], v[64:65], 0\n"
"ds_read_b64 v[72:73], v62 offset:36864\n"
"v_cvt_pk_bf16_f32 v218, v220, v221\n"
"v_mfma_f32_16x16x16_bf16 v[106:109], v[106:107], v[64:65], 0\n"
"v_cvt_pk_bf16_f32 v219, v222, v223\n"
"v_cvt_pk_bf16_f32 v224, v224, v225\n"
"v_cvt_pk_bf16_f32 v252, v252, v253\n"
"v_cvt_pk_bf16_f32 v253, v254, v255\n"
"v_min_u32 v252, -2, v252\n"
"v_cvt_pk_bf16_f32 v254, v98, v99\n"
"v_cvt_pk_bf16_f32 v255, v100, v101\n"
"v_min_u32 v253, -2, v253\n"
"v_cvt_pk_bf16_f32 v106, v106, v107\n"
"v_min_u32 v254, -2, v254\n"
"v_min_u32 v255, -2, v255\n"
"v_cvt_pk_bf16_f32 v107, v108, v109\n"
"v_min_u32 v106, -2, v106\n"
"v_min_u32 v107, -2, v107\n"
"v_cvt_pk_bf16_f32 v225, v226, v227\n"
"s_waitcnt lgkmcnt(6)\n"
"v_mfma_f32_16x16x32_bf16 v[232:235], a[216:219], v[76:79], 0\n"
"buffer_store_dwordx4 v[252:255], v192, s[28:31], s37 offen sc1\n"
"v_mfma_f32_16x16x32_bf16 v[232:235], a[228:231], v[68:71], v[232:235]\n"
"s_waitcnt lgkmcnt(1)\n"
"ds_write_b128 v196, v[216:219] offset:12288\n"
"v_mfma_f32_16x16x32_bf16 v[232:235], a[240:243], v[32:35], v[232:235]\n"
"s_waitcnt lgkmcnt(1)\n"
"buffer_store_dwordx2 v[106:107], v193, s[28:31], s37 offen offset:1024 sc1\n"
"v_mfma_f32_16x16x32_bf16 v[232:235], a[252:255], v[36:39], v[232:235]\n"
"s_add_u32 s37, s37, 1572864\n"
"ds_write_b64 v197, v[224:225] offset:12288\n"
"v_mfma_f32_16x16x32_bf16 v[232:235], v[8:11], v[40:43], v[232:235]\n"
"v_mfma_f32_16x16x32_bf16 v[232:235], v[20:23], v[44:47], v[232:235]\n"
"buffer_load_dwordx4 v[144:147], v192, s[28:31], s17 offen sc1\n"
"v_mfma_f32_16x16x32_bf16 v[236:239], a[220:223], v[76:79], 0\n"
"v_mfma_f32_16x16x32_bf16 v[236:239], a[232:235], v[68:71], v[236:239]\n"
"v_mfma_f32_16x16x32_bf16 v[236:239], a[244:247], v[32:35], v[236:239]\n"
"v_mfma_f32_16x16x32_bf16 v[236:239], v[0:3], v[36:39], v[236:239]\n"
"buffer_load_dwordx2 v[154:155], v193, s[28:31], s17 offen offset:1024 sc1\n"
"v_mfma_f32_16x16x32_bf16 v[236:239], v[12:15], v[40:43], v[236:239]\n"
"s_nop 0\n"
"v_cvt_pk_bf16_f32 v232, v232, v233\n"
"v_mfma_f32_16x16x32_bf16 v[236:239], v[24:27], v[44:47], v[236:239]\n"
"v_cvt_pk_bf16_f32 v233, v234, v235\n"
"v_mfma_f32_16x16x32_bf16 v[240:243], a[224:227], v[76:79], 0\n"
"s_waitcnt vmcnt(7)\n"
"ds_read_b128 v[76:79], v192 offset:33792\n"
"v_mfma_f32_16x16x32_bf16 v[240:243], a[236:239], v[68:71], v[240:243]\n"
"ds_write_b128 v202, a[162:165] offset:49152\n"
"v_mfma_f32_16x16x32_bf16 v[240:243], a[248:251], v[32:35], v[240:243]\n"
"buffer_store_dwordx4 v[52:55], v194, s[24:27], s36 offen sc1\n"
"v_mfma_f32_16x16x32_bf16 v[240:243], v[4:7], v[36:39], v[240:243]\n"
"s_waitcnt vmcnt(7)\n"
"ds_write_b64 v203, a[166:167] offset:49152\n"
"v_mfma_f32_16x16x32_bf16 v[240:243], v[16:19], v[40:43], v[240:243]\n"
"v_cvt_pk_bf16_f32 v234, v236, v237\n"
"v_cvt_pk_bf16_f32 v235, v238, v239\n"
"v_mfma_f32_16x16x32_bf16 v[240:243], v[28:31], v[44:47], v[240:243]\n"
"ds_read_b128 v[52:55], v192 offset:31744\n"
"ds_read_b128 v[68:71], v192 offset:34816\n"
"v_mfma_f32_16x16x32_bf16 v[204:207], a[216:219], v[48:51], 0\n"
"ds_read_b128 v[32:35], v192 offset:35840\n"
"buffer_store_dwordx2 v[72:73], v195, s[24:27], s36 offen sc1\n"
"v_mfma_f32_16x16x32_bf16 v[208:211], a[220:223], v[48:51], 0\n"
"s_add_u32 s36, s36, s33\n"
"ds_read_b128 v[72:75], v192 offset:32768\n"
"v_cvt_pk_bf16_f32 v240, v240, v241\n"
"v_mfma_f32_16x16x32_bf16 v[212:215], a[224:227], v[48:51], 0\n"
"s_add_u32 s8, s8, s36\n"
"v_cvt_pk_bf16_f32 v241, v242, v243\n"
"ds_write_b128 v196, v[232:235] offset:43008\n"
"v_max3_u32 v200, v228, v229, v230\n"
"v_max3_u32 v200, v200, v231, v152\n"
"v_max_u32 v200, v200, v153\n"
"v_cmp_eq_u32 vcc, -1, v200\n"
"s_add_u32 s9, s9, s36\n"
"s_add_u32 s10, s10, s36\n"
"ds_write_b64 v197, v[240:241] offset:43008\n"
"s_nop 1\n"
"s_cbranch_vccnz .Lp11%=\n"
".Lr11%=:\n"
"s_waitcnt lgkmcnt(5)\n"
"v_mfma_f32_16x16x32_bf16 v[212:215], a[236:239], v[52:55], v[212:215]\n"
"s_add_u32 s11, s11, s36\n"
"ds_write_b128 v202, v[228:231] offset:18432\n"
"v_mfma_f32_16x16x32_bf16 v[204:207], a[228:231], v[52:55], v[204:207]\n"
"s_waitcnt lgkmcnt(3)\n"
"s_add_u32 s20, s20, s36\n"
"s_add_u32 s21, s21, s36\n"
"v_mfma_f32_16x16x32_bf16 v[204:207], a[240:243], v[72:75], v[204:207]\n"
"s_add_u32 s22, s22, s36\n"
"ds_write_b64 v203, v[152:153] offset:18432\n"
"v_mfma_f32_16x16x32_bf16 v[204:207], a[252:255], v[76:79], v[204:207]\n"
"v_mfma_f32_16x16x32_bf16 v[204:207], v[8:11], v[68:71], v[204:207]\n"
"v_mfma_f32_16x16x32_bf16 v[204:207], v[20:23], v[32:35], v[204:207]\n"
"v_mfma_f32_16x16x32_bf16 v[208:211], a[232:235], v[52:55], v[208:211]\n"
"v_mfma_f32_16x16x32_bf16 v[208:211], a[244:247], v[72:75], v[208:211]\n"
"v_mfma_f32_16x16x32_bf16 v[208:211], v[0:3], v[76:79], v[208:211]\n"
"v_mfma_f32_16x16x32_bf16 v[208:211], v[12:15], v[68:71], v[208:211]\n"
"s_waitcnt lgkmcnt(0)\n"
"v_mfma_f32_16x16x32_bf16 v[208:211], v[24:27], v[32:35], v[208:211]\n"
"s_barrier\n"
"v_mfma_f32_16x16x32_bf16 v[212:215], a[248:251], v[72:75], v[212:215]\n"
"ds_read_b128 v[44:47], v192 offset:18432\n"
"ds_read_b128 v[48:51], v192 offset:19456\n"
"v_mfma_f32_16x16x32_bf16 v[212:215], v[4:7], v[76:79], v[212:215]\n"
"ds_read_b128 v[52:55], v192 offset:20480\n"
"v_cvt_pk_bf16_f32 v204, v204, v205\n"
"v_mfma_f32_16x16x32_bf16 v[212:215], v[16:19], v[68:71], v[212:215]\n"
"ds_read_b128 v[72:75], v192 offset:21504\n"
"v_cvt_pk_bf16_f32 v205, v206, v207\n"
"v_mfma_f32_16x16x32_bf16 v[212:215], v[28:31], v[32:35], v[212:215]\n"
"ds_read_b128 v[76:79], v192 offset:22528\n"
"v_cvt_pk_bf16_f32 v206, v208, v209\n"
"v_cvt_pk_bf16_f32 v207, v210, v211\n"
"ds_read_b128 v[68:71], v192 offset:23552\n"
"ds_write_b128 v196, v[204:207] offset:6144\n"
"s_nop 2\n"
"v_cvt_pk_bf16_f32 v212, v212, v213\n"
"v_cvt_pk_bf16_f32 v213, v214, v215\n"
"ds_write_b64 v197, v[212:213] offset:6144\n"
"s_waitcnt lgkmcnt(7)\n"
"v_mfma_f32_16x16x32_bf16 v[216:219], a[216:219], v[44:47], 0\n"
"s_waitcnt lgkmcnt(6)\n"
"ds_read_b128 v[36:39], v202 offset:12288\n"
"v_mfma_f32_16x16x32_bf16 v[216:219], a[228:231], v[48:51], v[216:219]\n"
"s_waitcnt lgkmcnt(6)\n"
"ds_read_b64 v[40:41], v62 offset:12288\n"
"v_mfma_f32_16x16x32_bf16 v[216:219], a[240:243], v[52:55], v[216:219]\n"
"s_waitcnt lgkmcnt(6)\n"
"v_mfma_f32_16x16x32_bf16 v[216:219], a[252:255], v[72:75], v[216:219]\n"
"s_waitcnt lgkmcnt(5)\n"
"v_mfma_f32_16x16x32_bf16 v[216:219], v[8:11], v[76:79], v[216:219]\n"
"s_waitcnt lgkmcnt(4)\n"
"v_mfma_f32_16x16x32_bf16 v[216:219], v[20:23], v[68:71], v[216:219]\n"
"v_mfma_f32_16x16x32_bf16 v[220:223], a[220:223], v[44:47], 0\n"
"v_mfma_f32_16x16x32_bf16 v[220:223], a[232:235], v[48:51], v[220:223]\n"
"v_mfma_f32_16x16x32_bf16 v[220:223], a[244:247], v[52:55], v[220:223]\n"
"s_waitcnt lgkmcnt(0)\n"
"buffer_store_dwordx4 v[36:39], v194, s[24:27], s8 offen sc1\n"
"v_mfma_f32_16x16x32_bf16 v[220:223], v[0:3], v[72:75], v[220:223]\n"
"s_barrier\n"
"v_mfma_f32_16x16x32_bf16 v[220:223], v[12:15], v[76:79], v[220:223]\n"
"ds_read_b128 v[32:35], v202 offset:43008\n"
"ds_read_b64 v[36:37], v62 offset:43008\n"
"v_mfma_f32_16x16x32_bf16 v[220:223], v[24:27], v[68:71], v[220:223]\n"
"buffer_store_dwordx2 v[40:41], v195, s[24:27], s8 offen sc1\n"
"v_mfma_f32_16x16x32_bf16 v[224:227], a[224:227], v[44:47], 0\n"
"ds_read_b128 v[40:43], v202 offset:6144\n"
"ds_read_b64 v[44:45], v62 offset:6144\n"
"v_mfma_f32_16x16x32_bf16 v[224:227], a[236:239], v[48:51], v[224:227]\n"
"ds_read_b128 v[48:51], v192 offset:49152\n"
"v_cvt_pk_bf16_f32 v216, v216, v217\n"
"v_mfma_f32_16x16x32_bf16 v[224:227], a[248:251], v[52:55], v[224:227]\n"
"ds_read_b128 v[52:55], v192 offset:50176\n"
"v_cvt_pk_bf16_f32 v217, v218, v219\n"
"v_mfma_f32_16x16x32_bf16 v[224:227], v[4:7], v[72:75], v[224:227]\n"
"ds_read_b128 v[72:75], v192 offset:51200\n"
"v_cvt_pk_bf16_f32 v218, v220, v221\n"
"v_mfma_f32_16x16x32_bf16 v[224:227], v[16:19], v[76:79], v[224:227]\n"
"ds_read_b128 v[76:79], v192 offset:52224\n"
"v_cvt_pk_bf16_f32 v219, v222, v223\n"
"v_mfma_f32_16x16x32_bf16 v[224:227], v[28:31], v[68:71], v[224:227]\n"
"s_waitcnt lgkmcnt(7)\n"
"ds_read_b128 v[68:71], v192 offset:53248\n"
"ds_write_b128 v196, v[216:219] offset:55296\n"
"buffer_store_dwordx4 v[32:35], v194, s[24:27], s9 offen sc1\n"
"s_nop 3\n"
"v_cvt_pk_bf16_f32 v224, v224, v225\n"
"v_cvt_pk_bf16_f32 v225, v226, v227\n"
"s_waitcnt lgkmcnt(5)\n"
"v_mfma_f32_16x16x32_bf16 v[232:235], a[216:219], v[48:51], 0\n"
"s_waitcnt lgkmcnt(4)\n"
"ds_read_b128 v[32:35], v192 offset:54272\n"
"ds_write_b64 v197, v[224:225] offset:55296\n"
"v_mfma_f32_16x16x32_bf16 v[232:235], a[228:231], v[52:55], v[232:235]\n"
"s_waitcnt lgkmcnt(5)\n"
"buffer_store_dwordx2 v[36:37], v195, s[24:27], s9 offen sc1\n"
"v_mfma_f32_16x16x32_bf16 v[232:235], a[240:243], v[72:75], v[232:235]\n"
"s_waitcnt vmcnt(7) lgkmcnt(4)\n"
"v_mfma_f32_16x16x32_bf16 v[232:235], a[252:255], v[76:79], v[232:235]\n"
"s_waitcnt lgkmcnt(3)\n"
"v_max3_u32 v200, v144, v145, v146\n"
"s_waitcnt vmcnt(6)\n"
"v_mfma_f32_16x16x32_bf16 v[232:235], v[8:11], v[68:71], v[232:235]\n"
"v_max3_u32 v200, v200, v147, v154\n"
"v_max_u32 v200, v200, v155\n"
"v_mfma_f32_16x16x32_bf16 v[236:239], a[220:223], v[48:51], 0\n"
"buffer_store_dwordx4 v[40:43], v194, s[24:27], s10 offen sc1\n"
"v_mfma_f32_16x16x32_bf16 v[236:239], a[232:235], v[52:55], v[236:239]\n"
"s_waitcnt lgkmcnt(1)\n"
"v_cmp_eq_u32 vcc, -1, v200\n"
"v_mfma_f32_16x16x32_bf16 v[236:239], a[244:247], v[72:75], v[236:239]\n"
"v_mfma_f32_16x16x32_bf16 v[236:239], v[0:3], v[76:79], v[236:239]\n"
"s_nop 2\n"
"s_cbranch_vccnz .Lp12%=\n"
".Lr12%=:\n"
"v_mfma_f32_16x16x32_bf16 v[236:239], v[12:15], v[68:71], v[236:239]\n"
"ds_write_b128 v202, v[144:147] offset:30720\n"
"v_mfma_f32_16x16x32_bf16 v[236:239], v[24:27], v[32:35], v[236:239]\n"
"buffer_store_dwordx2 v[44:45], v195, s[24:27], s10 offen sc1\n"
"v_mfma_f32_16x16x32_bf16 v[240:243], a[224:227], v[48:51], 0\n"
"ds_write_b64 v203, v[154:155] offset:30720\n"
"v_mfma_f32_16x16x32_bf16 v[240:243], a[236:239], v[52:55], v[240:243]\n"
"v_mfma_f32_16x16x32_bf16 v[240:243], a[248:251], v[72:75], v[240:243]\n"
"v_mfma_f32_16x16x32_bf16 v[240:243], v[4:7], v[76:79], v[240:243]\n"
"buffer_load_dwordx4 v[56:59], v192, s[28:31], s18 offen sc1\n"
"v_mfma_f32_16x16x32_bf16 v[240:243], v[16:19], v[68:71], v[240:243]\n"
"v_mfma_f32_16x16x32_bf16 v[240:243], v[28:31], v[32:35], v[240:243]\n"
"v_mfma_f32_16x16x32_bf16 v[232:235], v[20:23], v[32:35], v[232:235]\n"
"buffer_load_dwordx2 v[60:61], v193, s[28:31], s18 offen offset:1024 sc1\n"
"s_nop 5\n"
"v_cvt_pk_bf16_f32 v240, v240, v241\n"
"s_waitcnt lgkmcnt(0)\n"
"s_barrier\n"
"ds_read_b128 v[44:47], v192 offset:30720\n"
"ds_read_b128 v[48:51], v192 offset:31744\n"
"v_cvt_pk_bf16_f32 v232, v232, v233\n"
"v_cvt_pk_bf16_f32 v233, v234, v235\n"
"ds_read_b128 v[52:55], v192 offset:32768\n"
"v_cvt_pk_bf16_f32 v234, v236, v237\n"
"ds_read_b128 v[72:75], v192 offset:33792\n"
"v_cvt_pk_bf16_f32 v235, v238, v239\n"
"ds_read_b128 v[76:79], v192 offset:34816\n"
"v_cvt_pk_bf16_f32 v241, v242, v243\n"
"ds_read_b128 v[68:71], v192 offset:35840\n"
"ds_write_b128 v196, v[232:235]\n"
"ds_read_b128 v[36:39], v202 offset:55296\n"
"ds_write_b64 v197, v[240:241]\n"
"s_waitcnt lgkmcnt(8)\n"
"v_mfma_f32_16x16x32_bf16 v[204:207], a[216:219], v[44:47], 0\n"
"s_waitcnt lgkmcnt(7)\n"
"ds_read_b64 v[40:41], v62 offset:55296\n"
"v_mfma_f32_16x16x32_bf16 v[204:207], a[228:231], v[48:51], v[204:207]\n"
"s_waitcnt lgkmcnt(7)\n"
"v_mfma_f32_16x16x32_bf16 v[204:207], a[240:243], v[52:55], v[204:207]\n"
"s_waitcnt lgkmcnt(6)\n"
"v_mfma_f32_16x16x32_bf16 v[204:207], a[252:255], v[72:75], v[204:207]\n"
"s_waitcnt lgkmcnt(5)\n"
"v_mfma_f32_16x16x32_bf16 v[204:207], v[8:11], v[76:79], v[204:207]\n"
"s_waitcnt lgkmcnt(4)\n"
"v_mfma_f32_16x16x32_bf16 v[204:207], v[20:23], v[68:71], v[204:207]\n"
"v_mfma_f32_16x16x32_bf16 v[212:215], a[224:227], v[44:47], 0\n"
"s_waitcnt lgkmcnt(2)\n"
"v_mfma_f32_16x16x32_bf16 v[212:215], a[236:239], v[48:51], v[212:215]\n"
"buffer_store_dwordx4 v[36:39], v194, s[24:27], s11 offen sc1\n"
"v_mfma_f32_16x16x32_bf16 v[212:215], a[248:251], v[52:55], v[212:215]\n"
"s_waitcnt lgkmcnt(0)\n"
"v_mfma_f32_16x16x32_bf16 v[212:215], v[4:7], v[72:75], v[212:215]\n"
"s_nop 0\n"
"v_cvt_pk_bf16_f32 v204, v204, v205\n"
"v_mfma_f32_16x16x32_bf16 v[212:215], v[16:19], v[76:79], v[212:215]\n"
"buffer_store_dwordx2 v[40:41], v195, s[24:27], s11 offen sc1\n"
"v_mfma_f32_16x16x32_bf16 v[212:215], v[28:31], v[68:71], v[212:215]\n"
"v_cvt_pk_bf16_f32 v205, v206, v207\n"
"v_mfma_f32_16x16x32_bf16 v[208:211], a[220:223], v[44:47], 0\n"
"v_mfma_f32_16x16x32_bf16 v[208:211], a[232:235], v[48:51], v[208:211]\n"
"v_mfma_f32_16x16x32_bf16 v[208:211], a[244:247], v[52:55], v[208:211]\n"
"v_mfma_f32_16x16x32_bf16 v[208:211], v[0:3], v[72:75], v[208:211]\n"
"v_mfma_f32_16x16x32_bf16 v[208:211], v[12:15], v[76:79], v[208:211]\n"
"v_mfma_f32_16x16x32_bf16 v[208:211], v[24:27], v[68:71], v[208:211]\n"
"s_nop 0\n"
"v_cvt_pk_bf16_f32 v212, v212, v213\n"
"v_cvt_pk_bf16_f32 v213, v214, v215\n"
"ds_write_b64 v197, v[212:213] offset:36864\n"
"s_nop 3\n"
"v_cvt_pk_bf16_f32 v206, v208, v209\n"
"v_cvt_pk_bf16_f32 v207, v210, v211\n"
"ds_write_b128 v196, v[204:207] offset:36864\n"
"s_waitcnt vmcnt(3)\n"
"v_max3_u32 v200, v56, v57, v58\n"
"s_waitcnt vmcnt(2)\n"
"v_max3_u32 v200, v200, v59, v60\n"
"v_max_u32 v200, v200, v61\n"
"v_cmp_eq_u32 vcc, -1, v200\n"
"s_nop 4\n"
"s_cbranch_vccnz .Lp13%=\n"
".Lr13%=:\n"
"ds_write_b128 v202, v[56:59] offset:24576\n"
"ds_write_b64 v203, v[60:61] offset:24576\n"
"s_waitcnt lgkmcnt(0)\n"
"s_barrier\n"
"ds_read_b128 v[48:51], v192 offset:24576\n"
"ds_read_b128 v[52:55], v192 offset:25600\n"
"ds_read_b128 v[72:75], v192 offset:26624\n"
"ds_read_b128 v[76:79], v192 offset:27648\n"
"ds_read_b128 v[68:71], v192 offset:28672\n"
"ds_read_b128 v[32:35], v202\n"
"ds_read_b64 v[36:37], v62\n"
"ds_read_b128 v[40:43], v202 offset:36864\n"
"ds_read_b64 v[44:45], v62 offset:36864\n"
"s_waitcnt lgkmcnt(8)\n"
"v_mfma_f32_16x16x32_bf16 v[216:219], a[216:219], v[48:51], 0\n"
"s_waitcnt lgkmcnt(7)\n"
"v_mfma_f32_16x16x32_bf16 v[216:219], a[228:231], v[52:55], v[216:219]\n"
"s_waitcnt lgkmcnt(6)\n"
"v_mfma_f32_16x16x32_bf16 v[216:219], a[240:243], v[72:75], v[216:219]\n"
"s_waitcnt lgkmcnt(5)\n"
"v_mfma_f32_16x16x32_bf16 v[216:219], a[252:255], v[76:79], v[216:219]\n"
"s_waitcnt lgkmcnt(4)\n"
"v_mfma_f32_16x16x32_bf16 v[216:219], v[8:11], v[68:71], v[216:219]\n"
"s_waitcnt lgkmcnt(2)\n"
"buffer_store_dwordx4 v[32:35], v194, s[24:27], s20 offen sc1\n"
"v_mfma_f32_16x16x32_bf16 v[224:227], a[224:227], v[48:51], 0\n"
"s_waitcnt lgkmcnt(0)\n"
"ds_read_b128 v[32:35], v192 offset:29696\n"
"v_mfma_f32_16x16x32_bf16 v[224:227], a[236:239], v[52:55], v[224:227]\n"
"v_mfma_f32_16x16x32_bf16 v[224:227], a[248:251], v[72:75], v[224:227]\n"
"buffer_store_dwordx2 v[36:37], v195, s[24:27], s20 offen sc1\n"
"v_mfma_f32_16x16x32_bf16 v[224:227], v[4:7], v[76:79], v[224:227]\n"
"v_mfma_f32_16x16x32_bf16 v[224:227], v[16:19], v[68:71], v[224:227]\n"
"v_mfma_f32_16x16x32_bf16 v[220:223], a[220:223], v[48:51], 0\n"
"v_mfma_f32_16x16x32_bf16 v[220:223], a[232:235], v[52:55], v[220:223]\n"
"buffer_store_dwordx4 v[40:43], v194, s[24:27], s21 offen sc1\n"
"v_mfma_f32_16x16x32_bf16 v[220:223], a[244:247], v[72:75], v[220:223]\n"
"s_waitcnt lgkmcnt(0)\n"
"v_mfma_f32_16x16x32_bf16 v[220:223], v[0:3], v[76:79], v[220:223]\n"
"v_mfma_f32_16x16x32_bf16 v[220:223], v[12:15], v[68:71], v[220:223]\n"
"v_mfma_f32_16x16x32_bf16 v[220:223], v[24:27], v[32:35], v[220:223]\n"
"buffer_store_dwordx2 v[44:45], v195, s[24:27], s21 offen sc1\n"
"v_mfma_f32_16x16x32_bf16 v[216:219], v[20:23], v[32:35], v[216:219]\n"
"v_mfma_f32_16x16x32_bf16 v[224:227], v[28:31], v[32:35], v[224:227]\n"
"s_nop 6\n"
"v_cvt_pk_bf16_f32 v216, v216, v217\n"
"v_cvt_pk_bf16_f32 v217, v218, v219\n"
"v_cvt_pk_bf16_f32 v224, v224, v225\n"
"v_cvt_pk_bf16_f32 v218, v220, v221\n"
"v_cvt_pk_bf16_f32 v219, v222, v223\n"
"v_cvt_pk_bf16_f32 v225, v226, v227\n"
"ds_write_b128 v196, v[216:219] offset:12288\n"
"ds_write_b64 v197, v[224:225] offset:12288\n"
"s_waitcnt lgkmcnt(0)\n"
"s_barrier\n"
"ds_read_b128 v[36:39], v202 offset:12288\n"
"ds_read_b64 v[40:41], v62 offset:12288\n"
"s_waitcnt lgkmcnt(1)\n"
"buffer_store_dwordx4 v[36:39], v194, s[24:27], s22 offen sc1\n"
"s_waitcnt lgkmcnt(0)\n"
"buffer_store_dwordx2 v[40:41], v195, s[24:27], s22 offen sc1\n"
"s_branch .Lend%=\n"
".Lp1%=:\n"
"s_sub_u32 s42, s42, 1\n"
"s_cbranch_scc1 .Lt1%=\n"
"s_sleep 1\n"
"buffer_load_dword v200, off, s[28:31], s39 sc1\n"
"s_waitcnt vmcnt(0)\n"
"v_add3_u32 v200, v200, s50, -2\n"
"v_cmp_lt_u32 vcc, 64, v200\n"
"s_nop 4\n"
"s_cbranch_vccnz .Lp1%=\n"
"s_branch .Lr1%=\n"
".Lt1%=:\n"
"s_mov_b32 s42, 0\n"
"s_branch .Lr1%=\n"
".Lp4%=:\n"
"s_sub_u32 s42, s42, 1\n"
"s_cbranch_scc1 .Lt4%=\n"
"s_sleep 1\n"
"buffer_load_dword v200, off, s[28:31], s39 sc1\n"
"s_waitcnt vmcnt(0)\n"
"v_add3_u32 v200, v200, s50, -5\n"
"v_cmp_lt_u32 vcc, 64, v200\n"
"s_nop 4\n"
"s_cbranch_vccnz .Lp4%=\n"
"s_branch .Lr4%=\n"
".Lt4%=:\n"
"s_mov_b32 s42, 0\n"
"s_branch .Lr4%=\n"
".Lp7%=:\n"
"s_sub_u32 s42, s42, 1\n"
"s_cbranch_scc1 .Lt7%=\n"
"s_sleep 1\n"
"buffer_load_dword v200, off, s[28:31], s39 sc1\n"
"s_waitcnt vmcnt(0)\n"
"v_add3_u32 v200, v200, s50, -8\n"
"v_cmp_lt_u32 vcc, 64, v200\n"
"s_nop 4\n"
"s_cbranch_vccnz .Lp7%=\n"
"s_branch .Lr7%=\n"
".Lt7%=:\n"
"s_mov_b32 s42, 0\n"
"s_branch .Lr7%=\n"
".Lp9%=:\n"
"s_sub_u32 s42, s42, 1\n"
"s_cbranch_scc1 .Lt9%=\n"
"s_sleep 1\n"
"buffer_load_dword v200, off, s[28:31], s39 sc1\n"
"s_waitcnt vmcnt(0)\n"
"v_add3_u32 v200, v200, s50, -10\n"
"v_cmp_lt_u32 vcc, 64, v200\n"
"s_nop 4\n"
"s_cbranch_vccnz .Lp9%=\n"
"s_branch .Lr9%=\n"
".Lt9%=:\n"
"s_mov_b32 s42, 0\n"
"s_branch .Lr9%=\n"
".Lp10%=:\n"
"s_sub_u32 s42, s42, 1\n"
"s_cbranch_scc1 .Lt10%=\n"
"s_sleep 1\n"
"buffer_load_dword v201, off, s[28:31], s39 sc1\n"
"s_waitcnt vmcnt(0)\n"
"v_add3_u32 v201, v201, s50, -11\n"
"v_cmp_lt_u32 vcc, 64, v201\n"
"s_nop 4\n"
"s_cbranch_vccnz .Lp10%=\n"
"s_branch .Lr10%=\n"
".Lt10%=:\n"
"s_mov_b32 s42, 0\n"
"s_branch .Lr10%=\n"
".Lp11%=:\n"
"s_sub_u32 s42, s42, 1\n"
"s_cbranch_scc1 .Lt11%=\n"
"buffer_load_dwordx4 v[228:231], v192, s[28:31], s15 offen sc1\n"
"buffer_load_dwordx2 v[152:153], v193, s[28:31], s15 offen offset:1024 sc1\n"
"s_waitcnt vmcnt(0)\n"
"v_max3_u32 v200, v228, v229, v230\n"
"v_max3_u32 v200, v200, v231, v152\n"
"v_max_u32 v200, v200, v153\n"
"v_cmp_eq_u32 vcc, -1, v200\n"
"s_nop 4\n"
"s_cbranch_vccnz .Lp11%=\n"
"s_branch .Lr11%=\n"
".Lt11%=:\n"
"s_mov_b32 s42, 0\n"
"s_branch .Lr11%=\n"
".Lp12%=:\n"
"s_sub_u32 s42, s42, 1\n"
"s_cbranch_scc1 .Lt12%=\n"
"buffer_load_dwordx4 v[144:147], v192, s[28:31], s17 offen sc1\n"
"buffer_load_dwordx2 v[154:155], v193, s[28:31], s17 offen offset:1024 sc1\n"
"s_waitcnt vmcnt(0)\n"
"v_max3_u32 v200, v144, v145, v146\n"
"v_max3_u32 v200, v200, v147, v154\n"
"v_max_u32 v200, v200, v155\n"
"v_cmp_eq_u32 vcc, -1, v200\n"
"s_nop 4\n"
"s_cbranch_vccnz .Lp12%=\n"
"s_branch .Lr12%=\n"
".Lt12%=:\n"
"s_mov_b32 s42, 0\n"
"s_branch .Lr12%=\n"
".Lp13%=:\n"
"s_sub_u32 s42, s42, 1\n"
"s_cbranch_scc1 .Lt13%=\n"
"buffer_load_dwordx4 v[56:59], v192, s[28:31], s18 offen sc1\n"
"buffer_load_dwordx2 v[60:61], v193, s[28:31], s18 offen offset:1024 sc1\n"
"s_waitcnt vmcnt(0)\n"
"v_max3_u32 v200, v56, v57, v58\n"
"v_max3_u32 v200, v200, v59, v60\n"
"v_max_u32 v200, v200, v61\n"
"v_cmp_eq_u32 vcc, -1, v200\n"
"s_nop 4\n"
"s_cbranch_vccnz .Lp13%=\n"
"s_branch .Lr13%=\n"
".Lt13%=:\n"
"s_mov_b32 s42, 0\n"
"s_branch .Lr13%=\n"
".Lend%=:\n"
      : "+{v31}"(tid), "+{s2}"(bid), "+{s[0:1]}"(ka)
      : "i"((__attribute__((address_space(3))) char*)smem)
      : "memory", "scc", "vcc", VCLOB, ACLOB, SCLOB);
}

extern "C" void kernel_launch(void* const* d_in, const int* in_sizes, int n_in,
                              void* d_out, int out_size, void* d_ws, size_t ws_size,
                              hipStream_t stream) {
  const uint16_t* x = (const uint16_t*)d_in[0];
  const uint16_t* wqkv = (const uint16_t*)d_in[1];
  const uint16_t* wo = (const uint16_t*)d_in[2];
  const uint16_t* rpb = (const uint16_t*)d_in[3];
  dinat_hs<<<256, 256, 0, stream>>>(x, wqkv, wo, rpb, (uint16_t*)d_out, (char*)d_ws + (2 << 20)
  );
}
```

```cpp
#include <hip/hip_runtime.h>
#include <hip/hip_bf16.h>
#include <stdint.h>

#define VCLOB "v0", "v1", "v2", "v3", "v4", "v5", "v6", "v7", "v8", "v9", "v10", "v11", "v12", "v13", "v14", "v15", "v16", "v17", "v18", "v19", "v20", "v21", "v22", "v23", "v24", "v25", "v26", "v27", "v28", "v29", "v30", "v32", "v33", "v34", "v35", "v36", "v37", "v38", "v39", "v40", "v41", "v42", "v43", "v44", "v45", "v46", "v47", "v48", "v49", "v50", "v51", "v52", "v53", "v54", "v55", "v56", "v57", "v58", "v59", "v60", "v61", "v62", "v63", "v64", "v65", "v66", "v67", "v68", "v69", "v70", "v71", "v72", "v73", "v74", "v75", "v76", "v77", "v78", "v79", "v80", "v81", "v82", "v83", "v84", "v85", "v86", "v87", "v88", "v89", "v90", "v91", "v92", "v93", "v94", "v95", "v96", "v97", "v98", "v99", "v100", "v101", "v102", "v103", "v104", "v105", "v106", "v107", "v108", "v109", "v110", "v111", "v112", "v113", "v114", "v115", "v116", "v117", "v118", "v119", "v120", "v121", "v122", "v123", "v124", "v125", "v126", "v127", "v128", "v129", "v130", "v131", "v132", "v133", "v134", "v135", "v136", "v137", "v138", "v139", "v140", "v141", "v142", "v143", "v144", "v145", "v146", "v147", "v148", "v149", "v150", "v151", "v152", "v153", "v154", "v155", "v156", "v157", "v158", "v159", "v160", "v161", "v162", "v163", "v164", "v165", "v166", "v167", "v168", "v169", "v170", "v171", "v172", "v173", "v174", "v175", "v176", "v177", "v178", "v179", "v180", "v181", "v182", "v183", "v184", "v185", "v186", "v187", "v188", "v189", "v190", "v191", "v192", "v193", "v194", "v195", "v196", "v197", "v198", "v199", "v200", "v201", "v202", "v203", "v204", "v205", "v206", "v207", "v208", "v209", "v210", "v211", "v212", "v213", "v214", "v215", "v216", "v217", "v218", "v219", "v220", "v221", "v222", "v223", "v224", "v225", "v226", "v227", "v228", "v229", "v230", "v231", "v232", "v233", "v234", "v235", "v236", "v237", "v238", "v239", "v240", "v241", "v242", "v243", "v244", "v245", "v246", "v247", "v248", "v249", "v250", "v251", "v252", "v253", "v254", "v255"
#define ACLOB "a0", "a1", "a2", "a3", "a4", "a5", "a6", "a7", "a8", "a9", "a10", "a11", "a12", "a13", "a14", "a15", "a16", "a17", "a18", "a19", "a20", "a21", "a22", "a23", "a24", "a25", "a26", "a27", "a28", "a29", "a30", "a31", "a32", "a33", "a34", "a35", "a36", "a37", "a38", "a39", "a40", "a41", "a42", "a43", "a44", "a45", "a46", "a47", "a48", "a49", "a50", "a51", "a52", "a53", "a54", "a55", "a56", "a57", "a58", "a59", "a60", "a61", "a62", "a63", "a64", "a65", "a66", "a67", "a68", "a69", "a70", "a71", "a72", "a73", "a74", "a75", "a76", "a77", "a78", "a79", "a80", "a81", "a82", "a83", "a84", "a85", "a86", "a87", "a88", "a89", "a90", "a91", "a92", "a93", "a94", "a95", "a96", "a97", "a98", "a99", "a100", "a101", "a102", "a103", "a104", "a105", "a106", "a107", "a108", "a109", "a110", "a111", "a112", "a113", "a114", "a115", "a116", "a117", "a118", "a119", "a120", "a121", "a122", "a123", "a124", "a125", "a126", "a127", "a128", "a129", "a130", "a131", "a132", "a133", "a134", "a135", "a136", "a137", "a138", "a139", "a140", "a141", "a142", "a143", "a144", "a145", "a146", "a147", "a148", "a149", "a150", "a151", "a152", "a153", "a154", "a155", "a156", "a157", "a158", "a159", "a160", "a161", "a162", "a163", "a164", "a165", "a166", "a167", "a168", "a169", "a170", "a171", "a172", "a173", "a174", "a175", "a176", "a177", "a178", "a179", "a180", "a181", "a182", "a183", "a184", "a185", "a186", "a187", "a188", "a189", "a190", "a191", "a192", "a193", "a194", "a195", "a196", "a197", "a198", "a199", "a200", "a201", "a202", "a203", "a204", "a205", "a206", "a207", "a208", "a209", "a210", "a211", "a212", "a213", "a214", "a215", "a216", "a217", "a218", "a219", "a220", "a221", "a222", "a223", "a224", "a225", "a226", "a227", "a228", "a229", "a230", "a231", "a232", "a233", "a234", "a235", "a236", "a237", "a238", "a239", "a240", "a241", "a242", "a243", "a244", "a245", "a246", "a247", "a248", "a249", "a250", "a251", "a252", "a253", "a254", "a255"
#define SCLOB "s3", "s4", "s5", "s6", "s7", "s8", "s9", "s10", "s11", "s12", "s13", "s14", "s15", "s16", "s17", "s18", "s19", "s20", "s21", "s22", "s23", "s24", "s25", "s26", "s27", "s28", "s29", "s30", "s31", "s32", "s33", "s34", "s35", "s36", "s37", "s38", "s39", "s40", "s41", "s42", "s43", "s44", "s45", "s46", "s47", "s48", "s49", "s50", "s51", "s52", "s53", "s54", "s55", "s56", "s57", "s58", "s59", "s60", "s61", "s62", "s63", "s64", "s65", "s66", "s67", "s68", "s69", "s70", "s71", "s72", "s73", "s74", "s75", "s76", "s77", "s78", "s79", "s80", "s81", "s82", "s83", "s84", "s85", "s86", "s87", "s88", "s89", "s90", "s91", "s92", "s93", "s94", "s95", "s96", "s97", "s98", "s99", "s100", "s101"
#define LDS_BYTES 163392

__global__ __launch_bounds__(256, 1) __attribute__((aligned(4096))) void dinat_hs(const uint16_t* __restrict__ x, const uint16_t* __restrict__ wqkv,
                                                   const uint16_t* __restrict__ wo, const uint16_t* __restrict__ rpb,
                                                   uint16_t* __restrict__ out, char* __restrict__ ws
                                                   ) {
  __shared__ __attribute__((aligned(16))) char smem[LDS_BYTES];
  unsigned tid = threadIdx.x, bid = blockIdx.x;
  auto ka = __builtin_amdgcn_kernarg_segment_ptr();
  asm volatile(
"s_load_dwordx2 s[8:9], s[0:1], 0\n"
"s_load_dwordx2 s[12:13], s[0:1], 8\n"
"s_load_dwordx2 s[16:17], s[0:1], 16\n"
"s_load_dwordx2 s[20:21], s[0:1], 24\n"
"s_load_dwordx2 s[24:25], s[0:1], 32\n"
"s_load_dwordx2 s[28:29], s[0:1], 40\n"
"s_getpc_b64 s[4:5]\n"
".Lpc%=:\n"
"s_bfe_u32 s53, s2, 0x20005\n"
"s_and_b32 s54, s2, 1\n"
"s_lshr_b32 s55, s2, 7\n"
"s_mul_i32 s59, s53, 27\n"
"v_lshrrev_b32 v113, 6, v31\n"
"s_max_u32 s59, s59, 1\n"
"s_mul_i32 s58, s55, 4816896\n"
"s_mul_i32 s71, s54, 14278656\n"
"s_nop 0\n"
"v_readfirstlane_b32 s51, v113\n"
"s_sub_u32 s59, s59, 1\n"
"s_add_u32 s58, s58, s71\n"
"v_and_b32 v112, 63, v31\n"
"s_mul_i32 s72, s59, 768\n"
"s_lshr_b32 s71, s2, 3\n"
"s_add_u32 s58, s58, s72\n"
"s_and_b32 s73, s2, 4\n"
"s_lshl2_add_u32 s71, s71, s51\n"
"v_min_u32 v121, 2, v112\n"
"s_xnor_b32 s56, s54, s55\n"
"s_mul_i32 s73, s73, 21504\n"
"s_and_b32 s74, s2, 2\n"
"s_mul_i32 s72, s71, 384\n"
"v_lshlrev_b32 v121, 7, v121\n"
"s_and_b32 s56, s56, 1\n"
"s_add_u32 s58, s58, s73\n"
"s_mul_i32 s74, s74, 192\n"
"v_add_u32 v121, s72, v121\n"
"s_bfe_u32 s52, s2, 0x20003\n"
"s_mul_i32 s57, s54, -344064\n"
"s_add_u32 s58, s58, s74\n"
"v_lshlrev_b32 v120, 4, v112\n"
"s_mul_i32 s85, s56, 3\n"
"v_min_u32 v121, .Lend%=-.Lpc%=-4, v121\n"
"s_add_u32 s57, s57, 172032\n"
"s_lshl_b32 s61, s51, 3\n"
"v_lshrrev_b32 v114, 3, v112\n"
"v_xor_b32 v115, v120, v112\n"
"s_sub_u32 s86, s85, 3\n"
"s_mov_b32 m0, 163136\n"
"s_mul_i32 s73, s52, 338\n"
"global_load_lds_dword v121, s[4:5]\n"
"s_and_b32 s74, s51, 1\n"
"s_lshr_b32 s84, s2, 5\n"
"v_and_b32 v115, 0x70, v115\n"
"v_add_u32 v114, s61, v114\n"
"s_mul_i32 s86, s86, s57\n"
"s_and_b32 s73, s73, -4\n"
"s_mul_i32 s74, s74, 88\n"
"v_mul_u32_u24 v123, 0x2aab, v112\n"
"v_add_u32 v124, 64, v112\n"
"s_mov_b32 s22, 1352\n"
"s_mov_b32 s23, 0x20000\n"
"s_add_u32 s84, s84, 0\n"
"v_xor_b32 v116, 64, v115\n"
"v_mul_u32_u24 v117, 0x300, v114\n"
"s_add_u32 s86, s86, s58\n"
"v_max_u32 v118, s85, v114\n"
"s_add_u32 s73, s73, s74\n"
"v_lshlrev_b32 v122, 2, v112\n"
"v_lshrrev_b32 v123, 16, v123\n"
"v_mul_u32_u24 v124, 0x2aab, v124\n"
"s_and_b32 s65, s51, 1\n"
"s_mul_i32 s84, s84, 2\n"
"v_add3_u32 v119, v117, s86, v116\n"
"v_lshlrev_b32 v126, 1, v123\n"
"v_lshrrev_b32 v124, 16, v124\n"
"s_lshr_b32 s66, s51, 1\n"
"s_mul_i32 s70, s52, 96\n"
"s_mul_i32 s85, s65, 384\n"
"s_lshr_b32 s75, 0x9249200, s84\n"
"v_and_b32 v126, 24, v126\n"
"v_lshlrev_b32 v127, 1, v124\n"
"s_add_u32 s85, s85, s70\n"
"s_mul_i32 s86, s66, 36864\n"
"s_movk_i32 s69, 1152\n"
"s_and_b32 s75, s75, 0x180\n"
"v_and_or_b32 v126, v123, 3, v126\n"
"v_and_b32 v127, 24, v127\n"
"v_add_u32 v125, 128, v112\n"
"s_add_u32 s85, s85, s86\n"
"v_mad_u32_u24 v126, v126, s69, v120\n"
"v_mul_u32_u24 v27, 96, v123\n"
"v_and_or_b32 v127, v124, 3, v127\n"
"v_lshrrev_b32 v128, 2, v124\n"
"v_mul_u32_u24 v125, 0x2aab, v125\n"
"s_add_u32 s85, s85, 384\n"
"s_mul_i32 s65, s65, 3072\n"
"s_mul_i32 s71, s66, 6752\n"
"s_mov_b32 s14, 221184\n"
"s_mov_b32 s15, 0x20000\n"
"v_sub_u32 v27, v126, v27\n"
"v_and_or_b32 v127, v128, 4, v127\n"
"v_lshrrev_b32 v125, 16, v125\n"
"s_add_u32 s71, s71, s65\n"
"v_mad_u32_u24 v127, v127, s69, v120\n"
"v_mul_u32_u24 v28, 96, v124\n"
"v_lshlrev_b32 v129, 1, v125\n"
"v_sub_u32 v28, v127, v28\n"
"v_and_b32 v129, 24, v129\n"
"v_add_u32 v28, 1024, v28\n"
"v_and_or_b32 v129, v125, 3, v129\n"
"v_mad_u32_u24 v129, v129, s69, v120\n"
"v_mul_u32_u24 v29, 96, v125\n"
"s_sub_u32 s64, 111, s59\n"
"v_sub_u32 v29, v129, v29\n"
"s_min_u32 s64, s64, 32\n"
"v_add_u32 v29, 6656, v29\n"
"s_mul_i32 s64, s64, 768\n"
"v_add_u32 v118, -3, v118\n"
"s_add_u32 s64, s64, s58\n"
"s_mul_i32 s50, s51, 6752\n"
"v_mul_i32_i24 v118, s57, v118\n"
"s_mov_b32 s10, 19267584\n"
"s_mov_b32 s11, 0x20000\n"
"s_add_u32 s50, s50, 27008\n"
"v_add3_u32 v118, v118, s64, v115\n"
"s_lshl_b32 s72, s56, 3\n"
"s_sub_u32 s72, s72, 2\n"
"s_mul_i32 s72, s72, s57\n"
"s_mul_i32 s34, s51, 12288\n"
"s_add_u32 s72, s72, s58\n"
"s_add_u32 s34, s34, 81024\n"
"v_add3_u32 v130, v117, s72, v115\n"
"s_lshr_b32 s76, 0x12492480, s84\n"
"s_waitcnt lgkmcnt(0)\n"
"s_add_u32 m0, s74, 162768\n"
"s_add_u32 s74, s72, s57\n"
"buffer_load_dword v122, s[20:23], s73 offen lds\n"
"s_mul_i32 s73, s75, 576\n"
"s_add_u32 s73, s73, s85\n"
"s_add_u32 m0, s71, 0\n"
"v_add3_u32 v131, v117, s74, v116\n"
"buffer_load_dwordx4 v27, s[12:15], s73 offen lds\n"
"s_lshr_b32 s77, 0x4924900, s84\n"
"s_and_b32 s60, s2, 7\n"
"s_lshr_b32 s84, s2, 5\n"
"s_and_b32 s76, s76, 0x180\n"
"s_lshl3_add_u32 s60, s60, s84\n"
"s_add_u32 m0, s71, 1024\n"
"s_mul_i32 s84, s76, 576\n"
"s_add_u32 s84, s84, s85\n"
"s_and_b32 s77, s77, 0x180\n"
"s_mul_i32 s86, s77, 576\n"
"s_add_u32 s86, s86, s85\n"
"buffer_load_dwordx4 v28, s[12:15], s73 offen lds\n"
"s_mul_i32 s72, s54, 172032\n"
"s_sub_u32 s36, s58, s72\n"
"s_add_u32 s32, s36, s75\n"
"v_add3_u32 v190, v117, s72, v115\n"
"s_sub_u32 s74, 172032, s72\n"
"v_add3_u32 v191, v117, s74, v116\n"
"v_and_b32 v134, 3, v112\n"
"v_lshrrev_b32 v137, 1, v112\n"
"v_and_or_b32 v137, v137, 4, v134\n"
"s_add_u32 m0, s71, 2048\n"
"v_bfe_u32 v136, v112, 2, 1\n"
"v_lshrrev_b32 v140, 1, v137\n"
"v_min_u32 v96, 0xdc, v31\n"
"s_add_u32 s0, s36, s76\n"
"v_lshrrev_b32 v132, 4, v112\n"
"buffer_load_dwordx4 v29, s[12:15], s73 offen lds\n"
"v_lshl_add_u32 v85, v136, 2, v140\n"
"v_mul_u32_u24 v97, 0xf1, v96\n"
"s_mul_i32 s85, s54, -52\n"
"v_xor_b32 v85, v85, v132\n"
"v_lshrrev_b32 v97, 12, v97\n"
"s_sub_u32 s85, s85, 8\n"
"v_lshlrev_b32 v85, 4, v85\n"
"s_add_u32 m0, s50, 0\n"
"v_lshlrev_b32 v30, 3, v96\n"
"v_mul_i32_i24 v97, s85, v97\n"
"v_lshl_add_u32 v85, v137, 7, v85\n"
"v_mul_u32_u24 v25, 0xc00, v136\n"
"v_lshl_add_u32 v96, v96, 1, v97\n"
"buffer_load_dwordx4 v118, s[8:11], s75 offen lds\n"
"v_add_u32 v25, v25, v85\n"
"v_add_u32 v188, s34, v25\n"
"v_xad_u32 v189, v25, 64, s34\n"
"s_add_u32 s1, s36, s77\n"
"s_mul_i32 s35, s51, 6144\n"
"s_lshl_b32 s33, s57, 1\n"
"s_add_u32 s35, s35, 130176\n"
"s_add_u32 m0, s50, 3072\n"
"v_lshrrev_b32 v84, 2, v112\n"
"v_mul_u32_u24 v84, 96, v84\n"
"v_lshl_add_u32 v23, v134, 3, v84\n"
"v_xor_b32 v69, 64, v25\n"
"v_add_u32 v30, 160896, v30\n"
"buffer_load_dwordx4 v119, s[8:11], s75 offen lds\n"
"v_add_u32 v72, 54016, v23\n"
"v_add_u32 v68, s50, v25\n"
"v_add_u32 v69, s50, v69\n"
"s_mul_i32 s3, s65, 12\n"
"s_add_u32 s70, s70, s3\n"
"s_mul_i32 s81, s77, 576\n"
"s_add_u32 s81, s81, s70\n"
"s_add_u32 m0, s34, 6144\n"
"s_sub_u32 s3, s76, s75\n"
"s_mul_i32 s3, s3, s66\n"
"s_add_u32 s3, s3, s75\n"
"s_mul_i32 s41, s56, -60768\n"
"s_mul_i32 s43, s66, 6752\n"
"buffer_load_dwordx4 v130, s[8:11], s75 offen lds\n"
"s_mul_i32 s3, s3, 576\n"
"s_add_u32 s41, s41, 60768\n"
"s_add_u32 s43, s43, s65\n"
"s_add_u32 s3, s3, s70\n"
"s_add_u32 s43, s43, s41\n"
"s_mov_b32 s42, 0x80000\n"
"v_add_u32 v142, s61, v137\n"
"v_bfe_u32 v86, v142, 1, 1\n"
"v_mul_u32_u24 v87, 43, v142\n"
"s_add_u32 m0, s34, 9216\n"
"s_movk_i32 s68, 6752\n"
"v_and_b32 v133, 48, v112\n"
"v_xor_b32 v86, v86, v132\n"
"v_lshrrev_b32 v87, 9, v87\n"
"s_movk_i32 s67, 192\n"
"buffer_load_dwordx4 v131, s[8:11], s75 offen lds\n"
"v_lshlrev_b32 v86, 4, v86\n"
"v_mul_u32_u24 v87, 80832, v87\n"
"v_mad_u32_u24 v21, v142, s68, v133\n"
"v_mad_u32_u24 v141, v142, s67, v86\n"
"v_sub_u32 v21, v21, v87\n"
"v_and_b32 v88, 1, v112\n"
"s_mul_i32 s48, s56, 13504\n"
"v_add_u32 v21, 6144, v21\n"
"s_add_u32 m0, s71, 13504\n"
"v_add_u32 v187, 74272, v141\n"
"v_add_u32 v198, 60768, v141\n"
"v_mad_u32_u24 v88, v88, s68, v133\n"
"s_mov_b32 s6, 0xf0f0f0f0\n"
"s_mov_b32 s7, 0xf0f0f0f0\n"
"buffer_load_dwordx4 v27, s[12:15], s84 offen lds\n"
"s_mov_b64 exec, s[6:7]\n"
"v_add_u32 v21, s48, v141\n"
"v_mov_b32 v187, v141\n"
"v_add_u32 v198, 60544, v88\n"
"s_mov_b64 exec, -1\n"
"s_lshl_b32 s85, s66, 10\n"
"v_add_u32 v192, 99456, v120\n"
"v_bfe_u32 v138, v112, 4, 1\n"
"v_lshrrev_b32 v139, 3, v112\n"
"s_add_u32 m0, s71, 14528\n"
"s_add_u32 s62, s59, s61\n"
"v_and_b32 v139, 4, v139\n"
"v_sub_u32 v94, v136, v138\n"
"s_sub_u32 s63, 108, s62\n"
"s_mul_i32 s49, s56, 272\n"
"buffer_load_dwordx4 v28, s[12:15], s84 offen lds\n"
"v_mul_i32_i24 v94, 0x88, v94\n"
"v_sub_u32 v95, v137, v139\n"
"s_sub_u32 s62, 3, s62\n"
"v_add_u32 v150, 1, v139\n"
"v_add_u32 v151, 2, v139\n"
"v_add_u32 v152, 3, v139\n"
"v_lshl_add_u32 v94, v95, 3, v94\n"
"v_max_i32 v149, s62, v139\n"
"v_max_i32 v150, s62, v150\n"
"s_add_u32 m0, s71, 15552\n"
"v_max_i32 v151, s62, v151\n"
"v_max_i32 v152, s62, v152\n"
"v_mov_b32 v153, 0x7f\n"
"v_min_i32 v149, s63, v149\n"
"v_min_i32 v150, s63, v150\n"
"buffer_load_dwordx4 v29, s[12:15], s84 offen lds\n"
"s_add_u32 s84, s49, 161064\n"
"v_add_u32 v20, s84, v94\n"
"v_min_i32 v151, s63, v151\n"
"v_min_i32 v152, s63, v152\n"
"v_lshlrev_b32 v149, v149, v153\n"
"v_lshlrev_b32 v150, v150, v153\n"
"s_lshl_b32 s84, s51, 10\n"
"s_add_u32 m0, s50, 1024\n"
"v_lshlrev_b32 v151, v151, v153\n"
"v_lshlrev_b32 v152, v152, v153\n"
"s_sub_u32 s74, 3, s61\n"
"v_lshrrev_b32 v149, v137, v149\n"
"v_lshrrev_b32 v150, v137, v150\n"
"buffer_load_dwordx4 v118, s[8:11], s76 offen lds\n"
"v_add_u32 v202, s84, v192\n"
"v_lshrrev_b32 v151, v137, v151\n"
"v_lshrrev_b32 v152, v137, v152\n"
"s_max_i32 s74, s74, s62\n"
"v_bfe_i32 v98, v149, 0, 1\n"
"v_bfe_i32 v99, v150, 0, 1\n"
"v_bfe_i32 v102, v149, 8, 1\n"
"s_add_u32 m0, s50, 4096\n"
"v_bfe_i32 v103, v150, 8, 1\n"
"v_sub_u32 v61, v136, v138\n"
"s_mov_b32 s84, 0xc0000007\n"
"s_cmp_eq_u32 s53, 0\n"
"s_cselect_b32 s84, 0xe0000000, s84\n"
"s_cmp_eq_u32 s53, 3\n"
"s_cselect_b32 s84, 7, s84\n"
"buffer_load_dwordx4 v119, s[8:11], s76 offen lds\n"
"s_mov_b32 s72, 0xffff\n"
"v_bfe_i32 v100, v151, 0, 1\n"
"v_bfe_i32 v101, v152, 0, 1\n"
"v_bfe_i32 v56, v151, 8, 1\n"
"v_bfe_i32 v57, v152, 8, 1\n"
"v_max_i32 v58, s74, v137\n"
"v_ashrrev_i32 v149, 31, v61\n"
"v_add_u32 v150, -1, v61\n"
"v_mov_b32 v160, 0x40de\n"
"v_mov_b32 v161, 0\n"
"v_lshlrev_b32 v245, 4, v134\n"
"v_mov_b64 v[156:157], 0\n"
"s_mov_b32 s6, 0x00f0000f\n"
"s_mov_b32 s7, 0xf0000f00\n"
"v_bfi_b32 v0, s72, v98, v99\n"
"v_bfi_b32 v1, s72, v100, v101\n"
"v_bfi_b32 v2, s72, v102, v103\n"
"v_bfi_b32 v3, s72, v56, v57\n"
"s_mul_i32 s74, s51, 1536\n"
"v_and_b32 v251, 15, v112\n"
"v_not_b32 v149, v149\n"
"v_ashrrev_i32 v150, 31, v150\n"
"v_or_b32 v244, s56, v138\n"
"s_mov_b64 exec, s[6:7]\n"
"v_lshlrev_b64 v[156:157], v245, v[160:161]\n"
"s_mov_b64 exec, -1\n"
"s_lshl2_add_u32 s72, s60, s51\n"
"v_mul_u32_u24 v251, 0x180, v251\n"
"v_and_b32 v252, 7, v112\n"
"v_and_b32 v12, v149, v0\n"
"v_and_b32 v16, v150, v0\n"
"v_and_b32 v13, v149, v1\n"
"v_and_b32 v17, v150, v1\n"
"v_and_b32 v14, v149, v2\n"
"v_and_b32 v18, v150, v2\n"
"v_and_b32 v15, v149, v3\n"
"v_and_b32 v19, v150, v3\n"
"v_sub_u32 v244, v136, v244\n"
"v_mov_b32 v160, 0xffff\n"
"v_mov_b64 v[176:177], 0\n"
"s_mul_i32 s37, s72, 6144\n"
"v_and_b32 v249, 1, v112\n"
"v_add_u32 v251, 99456, v251\n"
"v_lshrrev_b32 v254, 5, v112\n"
"v_ashrrev_i32 v149, 31, v244\n"
"v_add_u32 v150, -1, v244\n"
"s_mov_b64 exec, s[6:7]\n"
"v_lshlrev_b64 v[176:177], v245, v[160:161]\n"
"s_mov_b64 exec, -1\n"
"s_lshl_b32 s72, s51, 6\n"
"v_lshlrev_b32 v255, 3, v134\n"
"v_lshl_add_u32 v84, v23, 2, s72\n"
"v_sub_u32 v84, v84, v255\n"
"v_sub_u32 v84, v84, v255\n"
"v_add_u32 v236, 130176, v84\n"
"v_sub_u32 v85, v236, v255\n"
"s_lshl_b32 s73, s48, 2\n"
"v_mad_u32_u24 v186, v136, s68, v141\n"
"s_add_u32 s73, s73, 6752\n"
"v_add_u32 v22, s73, v186\n"
"v_add_u32 v70, s35, v25\n"
"v_xor_b32 v71, 64, v25\n"
"v_add_u32 v71, s35, v71\n"
"s_sub_u32 s73, 28, s61\n"
"s_min_i32 s73, s73, s63\n"
"s_mov_b64 exec, 0\n"
"s_cmp_eq_u32 s51, 0\n"
"s_cbranch_scc1 .Lic0%=\n"
"s_cmp_eq_u32 s51, 1\n"
"s_cbranch_scc1 .Lic1%=\n"
"s_cmp_eq_u32 s51, 2\n"
"s_cbranch_scc1 .Lic2%=\n"
"s_branch .Lic3%=\n"
".Lib0%=:\n"
"s_mov_b64 exec, -1\n"
"v_min_i32 v58, s73, v58\n"
"v_lshlrev_b32 v58, v58, -1\n"
"v_lshrrev_b32 v58, v139, v58\n"
"v_not_b32 v58, v58\n"
"v_and_b32 v59, 5, v58\n"
"v_and_b32 v60, 10, v58\n"
"v_lshlrev_b32 v59, 1, v59\n"
"s_add_u32 m0, s34, 7168\n"
"v_lshl_or_b32 v59, v60, 4, v59\n"
"buffer_load_dwordx4 v130, s[8:11], s76 offen lds\n"
"v_or_b32 v59, 0x44, v59\n"
"v_lshl_or_b32 v63, v59, 8, v59\n"
"s_mov_b32 s30, 23724032\n"
"s_mov_b32 s31, 0x20000\n"
"v_and_b32 v146, 8, v112\n"
"v_lshl_add_u32 v146, v136, 1, v146\n"
"v_add3_u32 v146, v146, s61, 1\n"
"v_lshrrev_b32 v145, 5, v112\n"
"v_max_u32 v144, 3, v142\n"
"v_mul_u32_u24 v145, s68, v145\n"
"v_bfe_u32 v92, v146, 1, 1\n"
"s_add_u32 m0, s34, 10240\n"
"v_add_u32 v144, -3, v144\n"
"buffer_load_dwordx4 v131, s[8:11], s76 offen lds\n"
"v_add_u32 v145, -768, v145\n"
"v_xor_b32 v92, v92, v134\n"
"v_mov_b32 v143, s48\n"
"v_bfe_u32 v90, v144, 1, 1\n"
"v_lshl_add_u32 v92, v92, 4, v145\n"
"v_mad_u32_u24 v143, v136, s68, v143\n"
"v_xor_b32 v90, v90, v132\n"
"v_cmp_eq_u32 s[46:47], 35, v146\n"
"v_mad_u32_u24 v184, v146, s67, v92\n"
"v_lshl_add_u32 v90, v90, 4, v143\n"
"v_add_u32 v146, 1, v146\n"
"s_add_u32 m0, s71, 60768\n"
"v_mad_u32_u24 v178, v144, s67, v90\n"
"buffer_load_dwordx4 v27, s[12:15], s86 offen lds\n"
"s_waitcnt vmcnt(12)\n"
"s_barrier\n"
"ds_read_b64_tr_b16 a[0:1], v23\n"
"ds_read_b64_tr_b16 a[2:3], v23 offset:1536\n"
"ds_read_b64_tr_b16 a[4:5], v23 offset:32\n"
"ds_read_b64_tr_b16 a[6:7], v23 offset:1568\n"
"ds_read_b64_tr_b16 a[8:9], v23 offset:64\n"
"ds_read_b64_tr_b16 a[10:11], v23 offset:1600\n"
"ds_read_b64_tr_b16 a[72:73], v23 offset:3072\n"
"s_add_u32 m0, s71, 61792\n"
"ds_read_b64_tr_b16 a[74:75], v23 offset:4608\n"
"buffer_load_dwordx4 v28, s[12:15], s86 offen lds\n"
"ds_read_b64_tr_b16 a[76:77], v23 offset:3104\n"
"v_bfe_u32 v93, v146, 1, 1\n"
"ds_read_b64_tr_b16 a[78:79], v23 offset:4640\n"
"ds_read_b64_tr_b16 a[80:81], v23 offset:3136\n"
"ds_read_b64_tr_b16 a[82:83], v23 offset:4672\n"
"v_add_u32 v144, 5, v142\n"
"ds_read_b64_tr_b16 a[12:13], v23 offset:6752\n"
"ds_read_b64_tr_b16 a[14:15], v23 offset:8288\n"
"ds_read_b64_tr_b16 a[16:17], v23 offset:6784\n"
"s_add_u32 m0, s71, 62816\n"
"s_mul_i32 s71, s54, 312\n"
"buffer_load_dwordx4 v29, s[12:15], s86 offen lds\n"
"s_waitcnt vmcnt(12)\n"
"ds_read_b128 a[144:147], v68\n"
"ds_read_b128 a[148:151], v69\n"
"s_and_b32 s86, s52, 1\n"
"s_lshl1_add_u32 s71, s86, s71\n"
"s_add_u32 s71, s71, 162760\n"
"v_add_u32 v24, s71, v96\n"
"ds_read_u16 v240, v24 offset:6\n"
"ds_read_u16 v241, v24 offset:4\n"
"ds_read_u16 v242, v24 offset:2\n"
"ds_read_u16 v243, v24\n"
"s_add_u32 m0, s50, 2048\n"
"ds_read_b64_tr_b16 a[18:19], v23 offset:8320\n"
"buffer_load_dwordx4 v118, s[8:11], s77 offen lds\n"
"ds_read_b64_tr_b16 a[20:21], v23 offset:6816\n"
"s_and_b32 s86, s51, 1\n"
"ds_read_b64_tr_b16 a[22:23], v23 offset:8352\n"
"s_lshl3_add_u32 s85, s86, s85\n"
"ds_read_b64_tr_b16 a[84:85], v23 offset:9824\n"
"s_add_u32 s85, s85, 4096\n"
"ds_read_b64_tr_b16 a[86:87], v23 offset:11360\n"
"v_add_u32 v203, s85, v192\n"
"v_xor_b32 v93, v93, v134\n"
"v_min_u32 v144, 32, v144\n"
"v_mul_u32_u24 v147, 13504, v138\n"
"v_lshl_add_u32 v93, v93, 4, v145\n"
"v_bfe_u32 v91, v144, 1, 1\n"
"v_add_u32 v147, s48, v147\n"
"v_mul_i32_i24 v148, -67520, v138\n"
"v_mad_u32_u24 v185, v146, s67, v93\n"
"v_bfe_u32 v135, v112, 2, 2\n"
"v_xor_b32 v91, v91, v132\n"
"v_add_u32 v148, 67520, v148\n"
"v_add_u32 v183, v185, v147\n"
"v_add_u32 v185, v185, v148\n"
"s_mov_b64 exec, s[46:47]\n"
"v_mad_u32_u24 v185, v138, s67, v185\n"
"s_mov_b64 exec, -1\n"
"v_lshl_add_u32 v91, v91, 4, v143\n"
"v_mad_u32_u24 v179, v144, s67, v91\n"
"v_cmp_eq_u32 s[44:45], 32, v144\n"
"v_add_u32 v182, v184, v147\n"
"v_not_b32 v149, v149\n"
"v_add_u32 v184, v184, v148\n"
"v_and_b32 v4, v149, v0\n"
"v_and_b32 v5, v149, v1\n"
"v_and_b32 v6, v149, v2\n"
"v_and_b32 v7, v149, v3\n"
"v_ashrrev_i32 v150, 31, v150\n"
"v_and_b32 v8, v150, v0\n"
"v_and_b32 v9, v150, v1\n"
"v_and_b32 v10, v150, v2\n"
"v_and_b32 v11, v150, v3\n"
"v_lshrrev_b32 v87, 2, v112\n"
"v_sub_u32 v88, 11, v112\n"
"v_and_b32 v88, 0x80000000, v88\n"
"v_and_b32 v89, 1, v114\n"
"v_lshlrev_b32 v193, 3, v112\n"
"v_add_u32 v89, 29, v89\n"
"v_add_u32 v193, 99456, v193\n"
"v_mul_i32_i24 v89, s57, v89\n"
"v_add3_u32 v199, v89, s64, v116\n"
"v_mov_b32 v26, 0xf149f149\n"
"v_mov_b64 v[160:161], v[156:157]\n"
"v_add_u32 v180, 40512, v178\n"
"v_add_u32 v181, 40512, v179\n"
"v_mov_b32 v172, 0x3f803f80\n"
"v_mov_b32 v173, v172\n"
"v_mov_b64 v[164:165], v[156:157]\n"
"v_mov_b64 v[168:169], v[156:157]\n"
"v_mov_b64 v[174:175], v[172:173]\n"
"s_waitcnt vmcnt(11) lgkmcnt(7)\n"
"v_mfma_f32_16x16x32_bf16 v[204:207], a[0:3], a[144:147], 0\n"
"s_add_u32 m0, s50, 5120\n"
"ds_read_b128 a[168:171], v188 offset:6144\n"
"buffer_load_dwordx4 v119, s[8:11], s77 offen lds\n"
"v_mfma_f32_16x16x32_bf16 v[204:207], a[12:15], a[148:151], v[204:207]\n"
"s_lshl_b32 s85, s51, 6\n"
"ds_read_b128 a[172:175], v189 offset:6144\n"
"ds_read_b64_tr_b16 a[88:89], v23 offset:9856\n"
"v_mfma_f32_16x16x32_bf16 v[208:211], a[4:7], a[144:147], 0\n"
"s_waitcnt lgkmcnt(7)\n"
"ds_read_b64_tr_b16 a[90:91], v23 offset:11392\n"
"ds_read_b64_tr_b16 a[92:93], v23 offset:9888\n"
"v_mfma_f32_16x16x32_bf16 v[208:211], a[16:19], a[148:151], v[208:211]\n"
"s_add_u32 m0, s34, 8192\n"
"ds_read_b64_tr_b16 a[94:95], v23 offset:11424\n"
"buffer_load_dwordx4 v130, s[8:11], s77 offen lds\n"
"v_mfma_f32_16x16x32_bf16 v[212:215], a[8:11], a[144:147], 0\n"
"s_waitcnt lgkmcnt(8)\n"
"v_lshl_or_b32 v240, v241, 16, v240\n"
"v_lshl_or_b32 v241, v243, 16, v242\n"
"v_mfma_f32_16x16x32_bf16 v[212:215], a[20:23], a[148:151], v[212:215]\n"
"s_lshl_b32 s86, s52, 3\n"
"ds_write_b64 v30, v[240:241]\n"
"v_mfma_f32_16x16x32_bf16 v[216:219], a[72:75], a[144:147], 0\n"
"s_waitcnt lgkmcnt(7)\n"
"v_add_u32 v81, s85, v112\n"
"v_add_u32 v154, s85, v112\n"
"v_mfma_f32_16x16x32_bf16 v[216:219], a[84:87], a[148:151], v[216:219]\n"
"s_waitcnt lgkmcnt(6)\n"
"v_lshrrev_b32 v81, 1, v81\n"
"v_mul_u32_u24 v155, 0xaab, v154\n"
"v_mfma_f32_16x16x32_bf16 v[220:223], a[76:79], a[144:147], 0\n"
"s_add_u32 m0, s34, 11264\n"
"v_add_u32 v81, 0x100, v81\n"
"v_lshrrev_b32 v155, 16, v155\n"
"v_mfma_f32_16x16x32_bf16 v[224:227], a[80:83], a[144:147], 0\n"
"s_waitcnt lgkmcnt(3)\n"
"buffer_load_dwordx4 v131, s[8:11], s77 offen lds\n"
"v_mul_u32_u24 v82, 0xaab, v81\n"
"v_mfma_f32_16x16x32_bf16 v[220:223], a[88:91], a[148:151], v[220:223]\n"
"s_waitcnt vmcnt(9) lgkmcnt(1)\n"
"ds_read_b128 a[152:155], v68 offset:1024\n"
"ds_read_b128 a[156:159], v69 offset:1024\n"
"v_mfma_f32_16x16x32_bf16 v[224:227], a[92:95], a[148:151], v[224:227]\n"
"s_add_u32 m0, s34, 0\n"
"v_mul_i32_i24 v80, -24, v155\n"
"v_lshrrev_b32 v82, 16, v82\n"
"v_mfma_f32_16x16x32_bf16 v[104:107], a[72:75], a[168:171], 0\n"
"s_lshl2_add_u32 s71, s60, s52\n"
"buffer_load_dwordx4 v190, s[8:11], s32 offen lds\n"
"v_add_u32 v154, v154, v80\n"
"v_mfma_f32_16x16x32_bf16 v[104:107], a[84:87], a[172:175], v[104:107]\n"
"s_mul_i32 s38, s71, 6144\n"
"v_mul_i32_i24 v83, -24, v82\n"
"v_and_b32 v80, 7, v155\n"
"v_mfma_f32_16x16x32_bf16 v[108:111], a[76:79], a[168:171], 0\n"
"s_lshl_b32 s71, s51, 2\n"
"v_add_u32 v81, v81, v83\n"
"v_xor_b32 v154, v154, v80\n"
"v_mfma_f32_16x16x32_bf16 v[108:111], a[88:91], a[172:175], v[108:111]\n"
"s_add_u32 m0, s34, 3072\n"
"v_and_b32 v83, 7, v82\n"
"v_lshrrev_b32 v80, 1, v155\n"
"v_mfma_f32_16x16x32_bf16 v[228:231], a[80:83], a[168:171], 0\n"
"s_add_u32 s38, s38, s74\n"
"buffer_load_dwordx4 v191, s[8:11], s32 offen lds\n"
"v_xor_b32 v81, v81, v83\n"
"v_mfma_f32_16x16x32_bf16 v[228:231], a[92:95], a[172:175], v[228:231]\n"
"s_add_u32 s32, s32, s33\n"
"v_and_b32 v80, 4, v80\n"
"v_lshrrev_b32 v83, 1, v82\n"
"v_and_or_b32 v80, v155, 3, v80\n"
"v_and_b32 v83, 4, v83\n"
"s_waitcnt vmcnt(9)\n"
"ds_read_b128 a[176:179], v188 offset:7168\n"
"s_add_u32 m0, s34, 1024\n"
"ds_read_b128 a[180:183], v189 offset:7168\n"
"v_add_u32 v80, s86, v80\n"
"v_bfe_u32 v247, v155, 2, 1\n"
"v_and_or_b32 v83, v82, 3, v83\n"
"buffer_load_dwordx4 v190, s[8:11], s0 offen lds\n"
"v_lshrrev_b32 v246, v80, s84\n"
"v_xor_b32 v247, s54, v247\n"
"v_add_u32 v83, s86, v83\n"
"v_lshlrev_b32 v246, 31, v246\n"
"v_mul_u32_u24 v247, 172032, v247\n"
"v_mul_u32_u24 v80, 0x300, v80\n"
"v_lshrrev_b32 v248, v83, s84\n"
"s_add_u32 m0, s34, 4096\n"
"v_bfe_u32 v250, v82, 2, 1\n"
"v_add_u32 v253, s71, v132\n"
"v_add3_u32 v246, v246, v247, v80\n"
"v_lshlrev_b32 v248, 31, v248\n"
"v_xor_b32 v250, s54, v250\n"
"buffer_load_dwordx4 v191, s[8:11], s0 offen lds\n"
"s_add_u32 s0, s0, s33\n"
"v_xor_b32 v253, v253, v252\n"
"s_lshl1_add_u32 s74, s51, 16\n"
"v_lshl_add_u32 v194, v154, 4, v246\n"
"v_mfma_f32_16x16x32_bf16 v[244:247], a[0:3], a[168:171], 0\n"
"s_add_u32 s38, s38, 31616\n"
"v_lshl_add_u32 v248, v249, 3, v248\n"
"v_mul_u32_u24 v250, 172032, v250\n"
"v_mfma_f32_16x16x32_bf16 v[244:247], a[12:15], a[172:175], v[244:247]\n"
"s_add_u32 m0, s34, 2048\n"
"v_mul_u32_u24 v83, 0x300, v83\n"
"v_lshl_add_u32 v196, v253, 4, v251\n"
"v_add_u32 v254, s74, v254\n"
"buffer_load_dwordx4 v190, s[8:11], s1 offen lds\n"
"s_mov_b64 exec, 0\n"
"s_cmp_eq_u32 s51, 0\n"
"s_cbranch_scc1 .Lic4%=\n"
"s_cmp_eq_u32 s51, 1\n"
"s_cbranch_scc1 .Lic5%=\n"
"s_cmp_eq_u32 s51, 2\n"
"s_cbranch_scc1 .Lic6%=\n"
"s_branch .Lic7%=\n"
".Lib1%=:\n"
"s_mov_b64 exec, -1\n"
"s_add_u32 m0, s34, 5120\n"
"s_barrier\n"
"buffer_load_dwordx4 v191, s[8:11], s1 offen lds\n"
"s_add_u32 s1, s1, s33\n"
"ds_read_b64_tr_b16 a[24:25], v23 offset:13504\n"
"ds_read_b64_tr_b16 a[26:27], v23 offset:15040\n"
"ds_read_b64_tr_b16 a[28:29], v23 offset:13536\n"
"ds_read_b64_tr_b16 a[30:31], v23 offset:15072\n"
"ds_read_b64_tr_b16 a[32:33], v23 offset:13568\n"
"ds_read_b64_tr_b16 a[34:35], v23 offset:15104\n"
"ds_read_b64_tr_b16 a[96:97], v23 offset:16576\n"
"s_add_u32 m0, s35, 0\n"
"ds_read_b64_tr_b16 a[98:99], v23 offset:18112\n"
"s_waitcnt vmcnt(10)\n"
"s_barrier\n"
"buffer_load_dwordx4 v190, s[8:11], s32 offen lds\n"
"ds_read_b64_tr_b16 a[100:101], v23 offset:16608\n"
"ds_read_b64_tr_b16 a[102:103], v23 offset:18144\n"
"ds_read_b64_tr_b16 a[104:105], v23 offset:16640\n"
"ds_read_b64_tr_b16 a[106:107], v23 offset:18176\n"
"ds_read_b64_tr_b16 a[36:37], v23 offset:20256\n"
"v_add3_u32 v248, v248, v250, v83\n"
"v_lshl_add_u32 v251, v138, 3, v251\n"
"v_xor_b32 v254, v254, v252\n"
"v_lshl_add_u32 v195, v81, 4, v248\n"
"v_lshl_add_u32 v197, v254, 4, v251\n"
"v_mfma_f32_16x16x32_bf16 v[248:251], a[4:7], a[168:171], 0\n"
"s_waitcnt vmcnt(9)\n"
"ds_read_b64_tr_b16 a[38:39], v23 offset:21792\n"
"ds_read_b128 a[160:163], v68 offset:2048\n"
"v_mfma_f32_16x16x32_bf16 v[248:251], a[16:19], a[172:175], v[248:251]\n"
"s_waitcnt lgkmcnt(13)\n"
"ds_read_b128 a[164:167], v69 offset:2048\n"
"ds_read_b64_tr_b16 a[40:41], v23 offset:20288\n"
"v_mfma_f32_16x16x32_bf16 v[204:207], a[24:27], a[152:155], v[204:207]\n"
"s_waitcnt lgkmcnt(13)\n"
"ds_read_b64_tr_b16 a[42:43], v23 offset:21824\n"
"ds_read_b64_tr_b16 a[44:45], v23 offset:20320\n"
"v_mfma_f32_16x16x32_bf16 v[208:211], a[28:31], a[152:155], v[208:211]\n"
"s_waitcnt lgkmcnt(13)\n"
"ds_read_b64_tr_b16 a[46:47], v23 offset:21856\n"
"ds_read_b64_tr_b16 a[108:109], v23 offset:23328\n"
"v_mfma_f32_16x16x32_bf16 v[212:215], a[32:35], a[152:155], v[212:215]\n"
"s_waitcnt lgkmcnt(13)\n"
"ds_read_b64_tr_b16 a[110:111], v23 offset:24864\n"
"ds_read_b64_tr_b16 a[112:113], v23 offset:23360\n"
"v_mfma_f32_16x16x32_bf16 v[216:219], a[96:99], a[152:155], v[216:219]\n"
"s_waitcnt lgkmcnt(13)\n"
"ds_read_b64_tr_b16 a[114:115], v23 offset:24896\n"
"ds_read_b64_tr_b16 a[116:117], v23 offset:23392\n"
"v_mfma_f32_16x16x32_bf16 v[220:223], a[100:103], a[152:155], v[220:223]\n"
"s_waitcnt lgkmcnt(13)\n"
"ds_read_b64_tr_b16 a[118:119], v23 offset:24928\n"
"ds_read_b64_tr_b16 a[48:49], v23 offset:60768\n"
"v_mfma_f32_16x16x32_bf16 v[224:227], a[104:107], a[152:155], v[224:227]\n"
"s_waitcnt lgkmcnt(13)\n"
"ds_read_b64_tr_b16 a[50:51], v23 offset:62304\n"
"ds_read_b64_tr_b16 a[52:53], v23 offset:60800\n"
"v_mfma_f32_16x16x32_bf16 v[204:207], a[36:39], a[156:159], v[204:207]\n"
"s_waitcnt vmcnt(7)\n"
"ds_read_b64_tr_b16 a[54:55], v23 offset:62336\n"
"ds_read_b128 a[184:187], v188 offset:8192\n"
"v_mfma_f32_16x16x32_bf16 v[252:255], a[8:11], a[168:171], 0\n"
"s_add_u32 m0, s35, 3072\n"
"ds_read_b128 a[188:191], v189 offset:8192\n"
"buffer_load_dwordx4 v191, s[8:11], s32 offen lds\n"
"v_mfma_f32_16x16x32_bf16 v[252:255], a[20:23], a[172:175], v[252:255]\n"
"s_waitcnt lgkmcnt(14)\n"
"ds_read_b64_tr_b16 a[56:57], v23 offset:60832\n"
"v_add_u32 v68, s41, v23\n"
"v_mfma_f32_16x16x32_bf16 v[208:211], a[40:43], a[156:159], v[208:211]\n"
"s_waitcnt lgkmcnt(13)\n"
"ds_read_b64_tr_b16 a[58:59], v23 offset:62368\n"
"ds_read_b64_tr_b16 a[120:121], v72 offset:9824\n"
"v_mfma_f32_16x16x32_bf16 v[212:215], a[44:47], a[156:159], v[212:215]\n"
"s_waitcnt lgkmcnt(13)\n"
"ds_read_b64_tr_b16 a[122:123], v72 offset:11360\n"
"ds_read_b64_tr_b16 a[124:125], v72 offset:9856\n"
"v_mfma_f32_16x16x32_bf16 v[216:219], a[108:111], a[156:159], v[216:219]\n"
"s_waitcnt lgkmcnt(13)\n"
"ds_read_b64_tr_b16 a[126:127], v72 offset:11392\n"
"ds_read_b64_tr_b16 a[128:129], v72 offset:9888\n"
"v_mfma_f32_16x16x32_bf16 v[220:223], a[112:115], a[156:159], v[220:223]\n"
"s_waitcnt lgkmcnt(13)\n"
"ds_read_b64_tr_b16 a[130:131], v72 offset:11424\n"
"ds_read_b64_tr_b16 a[60:61], v72 offset:13504\n"
"v_mfma_f32_16x16x32_bf16 v[224:227], a[116:119], a[156:159], v[224:227]\n"
"s_waitcnt lgkmcnt(13)\n"
"ds_read_b64_tr_b16 a[62:63], v72 offset:15040\n"
"ds_read_b64_tr_b16 a[64:65], v72 offset:13536\n"
"v_mfma_f32_16x16x32_bf16 v[204:207], a[48:51], a[160:163], v[204:207]\n"
"s_waitcnt lgkmcnt(13)\n"
"ds_read_b64_tr_b16 a[66:67], v72 offset:15072\n"
"ds_read_b64_tr_b16 a[68:69], v72 offset:13568\n"
"v_mfma_f32_16x16x32_bf16 v[208:211], a[52:55], a[160:163], v[208:211]\n"
"s_add_u32 m0, s35, 1024\n"
"ds_read_b64_tr_b16 a[70:71], v72 offset:15104\n"
"buffer_load_dwordx4 v190, s[8:11], s0 offen lds\n"
"v_mfma_f32_16x16x32_bf16 v[244:247], a[24:27], a[176:179], v[244:247]\n"
"s_add_u32 s32, s32, s33\n"
"ds_read_b64_tr_b16 a[132:133], v72 offset:16576\n"
"ds_read_b64_tr_b16 a[134:135], v72 offset:18112\n"
"v_mfma_f32_16x16x32_bf16 v[244:247], a[36:39], a[180:183], v[244:247]\n"
"s_waitcnt lgkmcnt(14)\n"
"ds_read_b64_tr_b16 a[136:137], v72 offset:16608\n"
"ds_read_b64_tr_b16 a[138:139], v72 offset:18144\n"
"v_mfma_f32_16x16x32_bf16 v[212:215], a[56:59], a[160:163], v[212:215]\n"
"s_waitcnt lgkmcnt(14)\n"
"ds_read_b64_tr_b16 a[140:141], v72 offset:16640\n"
"ds_read_b64_tr_b16 a[142:143], v72 offset:18176\n"
"v_mfma_f32_16x16x32_bf16 v[216:219], a[120:123], a[160:163], v[216:219]\n"
"s_waitcnt lgkmcnt(14)\n"
"v_add_u32 v69, 154752, v23\n"
"s_mov_b32 s70, 0x3e553b95\n"
"v_mfma_f32_16x16x32_bf16 v[220:223], a[124:127], a[160:163], v[220:223]\n"
"s_waitcnt lgkmcnt(12)\n"
"s_add_u32 m0, s35, 4096\n"
"s_mov_b32 s71, 0x3e553b95\n"
"v_mfma_f32_16x16x32_bf16 v[224:227], a[128:131], a[160:163], v[224:227]\n"
"s_waitcnt lgkmcnt(10)\n"
"buffer_load_dwordx4 v191, s[8:11], s0 offen lds\n"
"s_mul_i32 s20, s33, 3\n"
"v_mfma_f32_16x16x32_bf16 v[204:207], a[60:63], a[164:167], v[204:207]\n"
"s_waitcnt lgkmcnt(8)\n"
"s_add_u32 s0, s0, s33\n"
"s_mul_i32 s21, s33, 5\n"
"v_mfma_f32_16x16x32_bf16 v[208:211], a[64:67], a[164:167], v[208:211]\n"
"s_waitcnt lgkmcnt(6)\n"
"s_add_u32 m0, s35, 2048\n"
"s_mul_i32 s22, s33, 6\n"
"v_mfma_f32_16x16x32_bf16 v[212:215], a[68:71], a[164:167], v[212:215]\n"
"s_waitcnt lgkmcnt(4)\n"
"buffer_load_dwordx4 v190, s[8:11], s1 offen lds\n"
"v_cvt_pk_bf16_f32 v204, v204, v205\n"
"v_mfma_f32_16x16x32_bf16 v[216:219], a[132:135], a[164:167], v[216:219]\n"
"s_waitcnt lgkmcnt(2)\n"
"v_cvt_pk_bf16_f32 v205, v206, v207\n"
"v_cvt_pk_bf16_f32 v206, v208, v209\n"
"v_mfma_f32_16x16x32_bf16 v[220:223], a[136:139], a[164:167], v[220:223]\n"
"s_waitcnt lgkmcnt(0)\n"
"v_cvt_pk_bf16_f32 v207, v210, v211\n"
"v_cvt_pk_bf16_f32 v212, v212, v213\n"
"v_mfma_f32_16x16x32_bf16 v[224:227], a[140:143], a[164:167], v[224:227]\n"
"s_add_u32 m0, s35, 5120\n"
"v_cvt_pk_bf16_f32 v213, v214, v215\n"
"v_cvt_pk_bf16_f32 v216, v216, v217\n"
"v_mfma_f32_16x16x32_bf16 v[248:251], a[28:31], a[176:179], v[248:251]\n"
"s_barrier\n"
"v_mfma_f32_16x16x32_bf16 v[248:251], a[40:43], a[180:183], v[248:251]\n"
"s_add_u32 s39, s38, 22119552\n"
"buffer_load_dwordx4 v191, s[8:11], s1 offen lds\n"
"ds_write_b128 v21, v[204:207]\n"
"v_mfma_f32_16x16x32_bf16 v[248:251], a[52:55], a[184:187], v[248:251]\n"
"s_add_u32 s1, s1, s33\n"
"v_cvt_pk_bf16_f32 v214, v224, v225\n"
"v_cvt_pk_bf16_f32 v215, v226, v227\n"
"v_mfma_f32_16x16x32_bf16 v[248:251], a[64:67], a[188:191], v[248:251]\n"
"s_and_b32 s85, s52, 1\n"
"v_cvt_pk_bf16_f32 v217, v218, v219\n"
"v_cvt_pk_bf16_f32 v218, v220, v221\n"
"v_mfma_f32_16x16x32_bf16 v[252:255], a[32:35], a[176:179], v[252:255]\n"
"s_lshl2_add_u32 s85, s85, s61\n"
"ds_write_b128 v21, v[212:215] offset:64\n"
"v_mfma_f32_16x16x32_bf16 v[252:255], a[44:47], a[180:183], v[252:255]\n"
"s_lshl_b32 s5, s51, 9\n"
"v_cvt_pk_bf16_f32 v219, v222, v223\n"
"v_add_u32 v86, s85, v134\n"
"v_mfma_f32_16x16x32_bf16 v[252:255], a[56:59], a[184:187], v[252:255]\n"
"s_add_u32 m0, s34, 6144\n"
"v_mul_u32_u24 v86, 0x300, v86\n"
"v_lshl_add_u32 v86, v87, 7, v86\n"
"v_mfma_f32_16x16x32_bf16 v[252:255], a[68:71], a[188:191], v[252:255]\n"
"s_add_u32 s5, s5, 4096\n"
"buffer_load_dwordx4 v190, s[8:11], s32 offen lds\n"
"ds_write_b128 v21, v[216:219] offset:128\n"
"v_mfma_f32_16x16x32_bf16 v[104:107], a[96:99], a[176:179], v[104:107]\n"
"s_waitcnt vmcnt(11)\n"
"v_or_b32 v238, v88, v86\n"
"v_add_u32 v62, s5, v193\n"
"v_mfma_f32_16x16x32_bf16 v[104:107], a[108:111], a[180:183], v[104:107]\n"
"s_add_u32 m0, s34, 9216\n"
"v_cvt_pk_bf16_f32 v252, v252, v253\n"
"v_cvt_pk_bf16_f32 v253, v254, v255\n"
"v_mfma_f32_16x16x32_bf16 v[104:107], a[120:123], a[184:187], v[104:107]\n"
"s_lshr_b32 s86, s52, 1\n"
"buffer_load_dwordx4 v191, s[8:11], s32 offen lds\n"
"ds_read_b128 v[32:35], v188\n"
"v_mfma_f32_16x16x32_bf16 v[104:107], a[132:135], a[188:191], v[104:107]\n"
"s_add_u32 s32, s32, s33\n"
"ds_read_b128 v[36:39], v189\n"
"ds_read_b64 v[80:81], v20 offset:272\n"
"v_mfma_f32_16x16x32_bf16 v[108:111], a[100:103], a[176:179], v[108:111]\n"
"s_mul_i32 s86, s86, 172032\n"
"ds_read_b64 v[86:87], v20 offset:336\n"
"ds_read_b64 v[88:89], v20 offset:544\n"
"v_mfma_f32_16x16x32_bf16 v[108:111], a[112:115], a[180:183], v[108:111]\n"
"s_add_u32 m0, s34, 7168\n"
"v_cvt_pk_bf16_f32 v104, v104, v105\n"
"v_cvt_pk_bf16_f32 v105, v106, v107\n"
"v_mfma_f32_16x16x32_bf16 v[108:111], a[124:127], a[184:187], v[108:111]\n"
"s_add_u32 s86, s86, s36\n"
"buffer_load_dwordx4 v190, s[8:11], s0 offen lds\n"
"ds_read_b64 v[94:95], v20 offset:608\n"
"v_mfma_f32_16x16x32_bf16 v[108:111], a[136:139], a[188:191], v[108:111]\n"
"s_mul_i32 s78, s33, 8\n"
"ds_read_b64 v[96:97], v20 offset:816\n"
"ds_read_b64 v[102:103], v20 offset:880\n"
"v_mfma_f32_16x16x32_bf16 v[228:231], a[104:107], a[176:179], v[228:231]\n"
"s_waitcnt vmcnt(11)\n"
"s_add_u32 m0, s34, 10240\n"
"v_mfma_f32_16x16x32_bf16 v[228:231], a[116:119], a[180:183], v[228:231]\n"
"s_waitcnt lgkmcnt(7)\n"
"buffer_load_dwordx4 v191, s[8:11], s0 offen lds\n"
"ds_read_b128 v[40:43], v188 offset:1024\n"
"v_mfma_f32_16x16x32_bf16 v[228:231], a[128:131], a[184:187], v[228:231]\n"
"s_add_u32 s0, s0, s33\n"
"ds_read_b128 v[44:47], v189 offset:1024\n"
"v_cvt_pk_bf16_f32 v106, v108, v109\n"
"v_mfma_f32_16x16x32_bf16 v[228:231], a[140:143], a[188:191], v[228:231]\n"
"s_waitcnt lgkmcnt(6)\n"
"v_cvt_pk_bf16_f32 v107, v110, v111\n"
"v_bfi_b32 v80, v4, v80, v26\n"
"v_mfma_f32_16x16x32_bf16 v[244:247], a[48:51], a[184:187], v[244:247]\n"
"s_waitcnt lgkmcnt(5)\n"
"ds_write_b128 v22, v[104:107] offset:128\n"
"v_mfma_f32_16x16x32_bf16 v[244:247], a[60:63], a[188:191], v[244:247]\n"
"s_waitcnt lgkmcnt(3)\n"
"v_cvt_pk_bf16_f32 v254, v228, v229\n"
"v_cvt_pk_bf16_f32 v255, v230, v231\n"
"v_mfma_f32_16x16x32_bf16 v[204:207], a[0:3], v[32:35], 0\n"
"s_add_u32 m0, s34, 8192\n"
"v_bfi_b32 v81, v5, v81, v26\n"
"v_bfi_b32 v86, v6, v86, v26\n"
"v_mfma_f32_16x16x32_bf16 v[204:207], a[12:15], v[36:39], v[204:207]\n"
"s_add_u32 s78, s78, s86\n"
"buffer_load_dwordx4 v190, s[8:11], s1 offen lds\n"
"ds_write_b128 v22, v[252:255] offset:64\n"
"v_mfma_f32_16x16x32_bf16 v[208:211], a[4:7], v[32:35], 0\n"
"s_waitcnt vmcnt(11) lgkmcnt(3)\n"
"v_cvt_pk_bf16_f32 v244, v244, v245\n"
"v_cvt_pk_bf16_f32 v245, v246, v247\n"
"v_mfma_f32_16x16x32_bf16 v[208:211], a[16:19], v[36:39], v[208:211]\n"
"s_add_u32 m0, s34, 11264\n"
"v_cvt_pk_bf16_f32 v246, v248, v249\n"
"v_cvt_pk_bf16_f32 v247, v250, v251\n"
"v_mfma_f32_16x16x32_bf16 v[208:211], a[28:31], v[40:43], v[208:211]\n"
"s_waitcnt lgkmcnt(2)\n"
"buffer_load_dwordx4 v191, s[8:11], s1 offen lds\n"
"ds_read_b128 v[48:51], v188 offset:2048\n"
"v_mfma_f32_16x16x32_bf16 v[208:211], a[40:43], v[44:47], v[208:211]\n"
"s_add_u32 s1, s1, s33\n"
"ds_read_b128 v[52:55], v189 offset:2048\n"
"ds_write_b128 v22, v[244:247]\n"
"v_mfma_f32_16x16x32_bf16 v[212:215], a[8:11], v[32:35], 0\n"
"s_add_u32 m0, s65, 154752\n"
"v_bfi_b32 v87, v7, v87, v26\n"
"v_bfi_b32 v88, v0, v88, v26\n"
"v_mfma_f32_16x16x32_bf16 v[212:215], a[20:23], v[36:39], v[212:215]\n"
"s_mul_i32 s73, s52, 1536\n"
"buffer_load_dwordx4 v27, s[12:15], s81 offen lds\n"
"v_bfi_b32 v89, v1, v89, v26\n"
"v_mfma_f32_16x16x32_bf16 v[212:215], a[32:35], v[40:43], v[212:215]\n"
"s_add_u32 s37, s37, s73\n"
"v_bfi_b32 v94, v2, v94, v26\n"
"v_bfi_b32 v95, v3, v95, v26\n"
"v_mfma_f32_16x16x32_bf16 v[212:215], a[44:47], v[44:47], v[212:215]\n"
"s_lshl_b32 s73, s51, 5\n"
"v_bfi_b32 v96, v0, v96, v26\n"
"v_bfi_b32 v97, v1, v97, v26\n"
"v_mfma_f32_16x16x32_bf16 v[216:219], a[72:75], v[32:35], 0\n"
"s_waitcnt lgkmcnt(2)\n"
"v_bfi_b32 v102, v2, v102, v26\n"
"v_bfi_b32 v103, v3, v103, v26\n"
"v_mfma_f32_16x16x32_bf16 v[216:219], a[84:87], v[36:39], v[216:219]\n"
"s_sub_u32 s73, 256, s73\n"
"v_add_u32 v235, s73, v85\n"
"v_mfma_f32_16x16x32_bf16 v[216:219], a[96:99], v[40:43], v[216:219]\n"
"s_mov_b32 s18, 73728\n"
"s_add_u32 s37, s37, 31616\n"
"v_mfma_f32_16x16x32_bf16 v[216:219], a[108:111], v[44:47], v[216:219]\n"
"s_mul_i32 s79, s51, 3072\n"
"s_mul_i32 s80, s66, 12288\n"
"v_mfma_f32_16x16x32_bf16 v[216:219], a[120:123], v[48:51], v[216:219]\n"
"s_waitcnt vmcnt(11) lgkmcnt(1)\n"
"s_add_u32 m0, s65, 155776\n"
"ds_read_b128 a[216:219], v70\n"
"v_mfma_f32_16x16x32_bf16 v[216:219], a[132:135], v[52:55], v[216:219]\n"
"s_mov_b32 s26, 19267584\n"
"buffer_load_dwordx4 v28, s[12:15], s81 offen lds\n"
"ds_read_b128 a[220:223], v71\n"
"v_mfma_f32_16x16x32_bf16 v[220:223], a[76:79], v[32:35], 0\n"
"s_mov_b32 s27, 0x20000\n"
"s_mov_b32 s19, 0x20000\n"
"v_mfma_f32_16x16x32_bf16 v[220:223], a[88:91], v[36:39], v[220:223]\n"
"s_add_u32 s40, s37, 22119552\n"
"v_cvt_pk_bf16_f32 v216, v216, v217\n"
"v_cvt_pk_bf16_f32 v217, v218, v219\n"
"v_mfma_f32_16x16x32_bf16 v[220:223], a[100:103], v[40:43], v[220:223]\n"
"s_sub_u32 s16, s16, 99456\n"
"s_subb_u32 s17, s17, 0\n"
"v_mfma_f32_16x16x32_bf16 v[220:223], a[112:115], v[44:47], v[220:223]\n"
"s_mov_b32 s18, 173184\n"
"s_add_u32 s80, s80, s79\n"
"v_mfma_f32_16x16x32_bf16 v[220:223], a[124:127], v[48:51], v[220:223]\n"
"v_mfma_f32_16x16x32_bf16 v[220:223], a[136:139], v[52:55], v[220:223]\n"
"s_waitcnt lgkmcnt(1)\n"
"v_mfma_f32_16x16x32_bf16 v[224:227], a[80:83], v[32:35], 0\n"
"s_add_u32 m0, s65, 156800\n"
"s_nop 0\n"
"buffer_load_dwordx4 v29, s[12:15], s81 offen lds\n"
"v_mfma_f32_16x16x32_bf16 v[224:227], a[92:95], v[36:39], v[224:227]\n"
"v_mfma_f32_16x16x32_bf16 v[224:227], a[104:107], v[40:43], v[224:227]\n"
"v_mfma_f32_16x16x32_bf16 v[224:227], a[116:119], v[44:47], v[224:227]\n"
"v_cvt_pk_bf16_f32 v218, v220, v221\n"
"v_mfma_f32_16x16x32_bf16 v[224:227], a[128:131], v[48:51], v[224:227]\n"
"v_cvt_pk_bf16_f32 v219, v222, v223\n"
"v_mfma_f32_16x16x32_bf16 v[224:227], a[140:143], v[52:55], v[224:227]\n"
"ds_write_b128 v186, v[216:219] offset:20384\n"
"v_mfma_f32_16x16x32_bf16 v[204:207], a[24:27], v[40:43], v[204:207]\n"
"v_mfma_f32_16x16x32_bf16 v[204:207], a[36:39], v[44:47], v[204:207]\n"
"v_mfma_f32_16x16x32_bf16 v[204:207], a[48:51], v[48:51], v[204:207]\n"
"v_mfma_f32_16x16x32_bf16 v[204:207], a[60:63], v[52:55], v[204:207]\n"
"v_mfma_f32_16x16x32_bf16 v[208:211], a[52:55], v[48:51], v[208:211]\n"
"v_mfma_f32_16x16x32_bf16 v[208:211], a[64:67], v[52:55], v[208:211]\n"
"v_mfma_f32_16x16x32_bf16 v[212:215], a[56:59], v[48:51], v[212:215]\n"
"v_mfma_f32_16x16x32_bf16 v[212:215], a[68:71], v[52:55], v[212:215]\n"
"v_mfma_f32_16x16x32_bf16 v[244:247], a[0:3], a[216:219], 0\n"
"s_waitcnt lgkmcnt(1)\n"
"v_mfma_f32_16x16x32_bf16 v[244:247], a[12:15], a[220:223], v[244:247]\n"
"s_nop 0\n"
"v_cvt_pk_bf16_f32 v204, v204, v205\n"
"v_mfma_f32_16x16x32_bf16 v[248:251], a[4:7], a[216:219], 0\n"
"v_cvt_pk_bf16_f32 v205, v206, v207\n"
"v_cvt_pk_bf16_f32 v206, v208, v209\n"
"v_mfma_f32_16x16x32_bf16 v[248:251], a[16:19], a[220:223], v[248:251]\n"
"v_cvt_pk_bf16_f32 v207, v210, v211\n"
"v_cvt_pk_bf16_f32 v212, v212, v213\n"
"v_mfma_f32_16x16x32_bf16 v[252:255], a[8:11], a[216:219], 0\n"
"ds_write_b128 v186, v[204:207] offset:20256\n"
"v_mfma_f32_16x16x32_bf16 v[252:255], a[20:23], a[220:223], v[252:255]\n"
"s_waitcnt vmcnt(11)\n"
"v_cvt_pk_bf16_f32 v213, v214, v215\n"
"v_cvt_pk_bf16_f32 v214, v224, v225\n"
"v_mfma_f32_16x16x32_bf16 v[104:107], a[72:75], a[216:219], 0\n"
"s_add_u32 m0, s43, 0\n"
"v_cvt_pk_bf16_f32 v215, v226, v227\n"
"v_mfma_f32_16x16x32_bf16 v[104:107], a[84:87], a[220:223], v[104:107]\n"
"buffer_load_dwordx4 v27, s[12:15], s3 offen lds\n"
"ds_read_b128 a[224:227], v70 offset:1024\n"
"v_mfma_f32_16x16x32_bf16 v[108:111], a[76:79], a[216:219], 0\n"
"ds_read_b128 a[228:231], v71 offset:1024\n"
"ds_write_b128 v186, v[212:215] offset:20320\n"
"v_mfma_f32_16x16x32_bf16 v[108:111], a[88:91], a[220:223], v[108:111]\n"
"s_add_u32 m0, s43, 1024\n"
"s_nop 0\n"
"buffer_load_dwordx4 v28, s[12:15], s3 offen lds\n"
"v_mfma_f32_16x16x32_bf16 v[228:231], a[80:83], a[216:219], 0\n"
"v_mfma_f32_16x16x32_bf16 v[228:231], a[92:95], a[220:223], v[228:231]\n"
"s_add_u32 m0, s43, 2048\n"
"s_waitcnt vmcnt(11)\n"
"ds_read_b128 a[232:235], v70 offset:2048\n"
"ds_read_b128 a[236:239], v71 offset:2048\n"
"s_add_u32 s43, s79, 0\n"
"buffer_load_dwordx4 v29, s[12:15], s3 offen lds\n"
"s_waitcnt lgkmcnt(4)\n"
"v_mfma_f32_16x16x32_bf16 v[228:231], a[104:107], a[224:227], v[228:231]\n"
"s_waitcnt lgkmcnt(3)\n"
"v_mfma_f32_16x16x32_bf16 v[228:231], a[116:119], a[228:231], v[228:231]\n"
"s_add_u32 m0, s34, 0\n"
"v_mfma_f32_16x16x32_bf16 v[244:247], a[24:27], a[224:227], v[244:247]\n"
"v_mfma_f32_16x16x32_bf16 v[244:247], a[36:39], a[228:231], v[244:247]\n"
"buffer_load_dwordx4 v190, s[8:11], s32 offen lds\n"
"v_mfma_f32_16x16x32_bf16 v[248:251], a[28:31], a[224:227], v[248:251]\n"
"v_mfma_f32_16x16x32_bf16 v[248:251], a[40:43], a[228:231], v[248:251]\n"
"s_waitcnt vmcnt(11) lgkmcnt(1)\n"
"s_add_u32 m0, s34, 3072\n"
"v_mfma_f32_16x16x32_bf16 v[248:251], a[52:55], a[232:235], v[248:251]\n"
"s_waitcnt lgkmcnt(0)\n"
"ds_read_b128 a[240:243], v188 offset:6144\n"
"ds_read_b128 a[244:247], v189 offset:6144\n"
"v_mfma_f32_16x16x32_bf16 v[248:251], a[64:67], a[236:239], v[248:251]\n"
"buffer_load_dwordx4 v191, s[8:11], s32 offen lds\n"
"s_add_u32 s32, s32, s33\n"
"v_mfma_f32_16x16x32_bf16 v[252:255], a[32:35], a[224:227], v[252:255]\n"
"v_mfma_f32_16x16x32_bf16 v[252:255], a[44:47], a[228:231], v[252:255]\n"
"s_add_u32 m0, s34, 1024\n"
"v_mfma_f32_16x16x32_bf16 v[252:255], a[56:59], a[232:235], v[252:255]\n"
"v_mfma_f32_16x16x32_bf16 v[252:255], a[68:71], a[236:239], v[252:255]\n"
"buffer_load_dwordx4 v190, s[8:11], s0 offen lds\n"
"v_mfma_f32_16x16x32_bf16 v[104:107], a[96:99], a[224:227], v[104:107]\n"
"v_mfma_f32_16x16x32_bf16 v[104:107], a[108:111], a[228:231], v[104:107]\n"
"s_waitcnt lgkmcnt(1)\n"
"s_add_u32 m0, s34, 4096\n"
"v_mfma_f32_16x16x32_bf16 v[104:107], a[120:123], a[232:235], v[104:107]\n"
"v_mfma_f32_16x16x32_bf16 v[104:107], a[132:135], a[236:239], v[104:107]\n"
"s_waitcnt vmcnt(11)\n"
"buffer_load_dwordx4 v191, s[8:11], s0 offen lds\n"
"ds_read_b128 a[248:251], v188 offset:7168\n"
"v_mfma_f32_16x16x32_bf16 v[108:111], a[100:103], a[224:227], v[108:111]\n"
"s_add_u32 s0, s0, s33\n"
"ds_read_b128 a[252:255], v189 offset:7168\n"
"v_cvt_pk_bf16_f32 v252, v252, v253\n"
"v_mfma_f32_16x16x32_bf16 v[108:111], a[112:115], a[228:231], v[108:111]\n"
"v_cvt_pk_bf16_f32 v253, v254, v255\n"
"v_cvt_pk_bf16_f32 v104, v104, v105\n"
"v_mfma_f32_16x16x32_bf16 v[108:111], a[124:127], a[232:235], v[108:111]\n"
"s_add_u32 m0, s34, 2048\n"
"v_cvt_pk_bf16_f32 v105, v106, v107\n"
"v_mfma_f32_16x16x32_bf16 v[108:111], a[136:139], a[236:239], v[108:111]\n"
"buffer_load_dwordx4 v190, s[8:11], s1 offen lds\n"
"v_mfma_f32_16x16x32_bf16 v[244:247], a[48:51], a[232:235], v[244:247]\n"
"v_mfma_f32_16x16x32_bf16 v[244:247], a[60:63], a[236:239], v[244:247]\n"
"v_mfma_f32_16x16x32_bf16 v[228:231], a[128:131], a[232:235], v[228:231]\n"
"v_mfma_f32_16x16x32_bf16 v[228:231], a[140:143], a[236:239], v[228:231]\n"
"v_mfma_f32_16x16x32_bf16 v[120:123], a[0:3], a[240:243], 0\n"
"s_waitcnt lgkmcnt(2)\n"
"s_add_u32 m0, s34, 5120\n"
"v_cvt_pk_bf16_f32 v106, v108, v109\n"
"v_mfma_f32_16x16x32_bf16 v[120:123], a[12:15], a[244:247], v[120:123]\n"
"s_waitcnt lgkmcnt(1)\n"
"buffer_load_dwordx4 v191, s[8:11], s1 offen lds\n"
"v_cvt_pk_bf16_f32 v244, v244, v245\n"
"v_mfma_f32_16x16x32_bf16 v[120:123], a[24:27], a[248:251], v[120:123]\n"
"s_waitcnt lgkmcnt(0)\n"
"v_cvt_pk_bf16_f32 v245, v246, v247\n"
"v_cvt_pk_bf16_f32 v246, v248, v249\n"
"v_mfma_f32_16x16x32_bf16 v[120:123], a[36:39], a[252:255], v[120:123]\n"
"s_add_u32 s1, s1, s33\n"
"v_cvt_pk_bf16_f32 v247, v250, v251\n"
"v_cvt_pk_bf16_f32 v254, v228, v229\n"
"v_mfma_f32_16x16x32_bf16 v[124:127], a[4:7], a[240:243], 0\n"
"ds_write_b128 v186, v[244:247] offset:33760\n"
"v_mfma_f32_16x16x32_bf16 v[124:127], a[16:19], a[244:247], v[124:127]\n"
"v_cvt_pk_bf16_f32 v255, v230, v231\n"
"v_cvt_pk_bf16_f32 v107, v110, v111\n"
"v_mfma_f32_16x16x32_bf16 v[124:127], a[28:31], a[248:251], v[124:127]\n"
"ds_write_b128 v186, v[252:255] offset:33824\n"
"v_mfma_f32_16x16x32_bf16 v[124:127], a[40:43], a[252:255], v[124:127]\n"
"s_waitcnt vmcnt(12)\n"
"v_mov_b64 v[252:253], 0\n"
"v_mov_b64 v[254:255], 0\n"
"v_mfma_f32_16x16x32_bf16 v[128:131], a[8:11], a[240:243], 0\n"
"s_add_u32 m0, s34, 6144\n"
"s_nop 0\n"
"buffer_load_dwordx4 v190, s[8:11], s32 offen lds\n"
"v_mfma_f32_16x16x32_bf16 v[128:131], a[20:23], a[244:247], v[128:131]\n"
"ds_read_b128 v[56:59], v188 offset:8192\n"
"ds_read_b128 v[64:67], v189 offset:8192\n"
"v_mfma_f32_16x16x32_bf16 v[128:131], a[32:35], a[248:251], v[128:131]\n"
"ds_write_b128 v186, v[104:107] offset:33888\n"
"v_mfma_f32_16x16x32_bf16 v[128:131], a[44:47], a[252:255], v[128:131]\n"
"v_mfma_f32_16x16x32_bf16 v[132:135], a[72:75], a[240:243], 0\n"
"ds_read_b64 v[104:105], v20 offset:1088\n"
"v_mfma_f32_16x16x32_bf16 v[132:135], a[84:87], a[244:247], v[132:135]\n"
"ds_read_b64 v[110:111], v20 offset:1152\n"
"v_subrev_u32 v20, s49, v20\n"
"v_mfma_f32_16x16x32_bf16 v[132:135], a[96:99], a[248:251], v[132:135]\n"
"v_mfma_f32_16x16x32_bf16 v[132:135], a[108:111], a[252:255], v[132:135]\n"
"s_waitcnt lgkmcnt(4)\n"
"v_mfma_f32_16x16x32_bf16 v[132:135], a[120:123], v[56:59], v[132:135]\n"
"s_waitcnt lgkmcnt(3)\n"
"v_mfma_f32_16x16x32_bf16 v[132:135], a[132:135], v[64:67], v[132:135]\n"
"v_mfma_f32_16x16x32_bf16 v[136:139], a[76:79], a[240:243], 0\n"
"v_mfma_f32_16x16x32_bf16 v[136:139], a[88:91], a[244:247], v[136:139]\n"
"s_waitcnt lgkmcnt(1)\n"
"v_bfi_b32 v104, v8, v104, v26\n"
"v_bfi_b32 v105, v9, v105, v26\n"
"v_mfma_f32_16x16x32_bf16 v[136:139], a[100:103], a[248:251], v[136:139]\n"
"s_waitcnt lgkmcnt(0)\n"
"v_bfi_b32 v110, v10, v110, v26\n"
"v_cvt_pk_bf16_f32 v132, v132, v133\n"
"v_mfma_f32_16x16x32_bf16 v[136:139], a[112:115], a[252:255], v[136:139]\n"
"v_cvt_pk_bf16_f32 v133, v134, v135\n"
"v_bfi_b32 v111, v11, v111, v26\n"
"v_mfma_f32_16x16x32_bf16 v[136:139], a[124:127], v[56:59], v[136:139]\n"
"v_mfma_f32_16x16x32_bf16 v[136:139], a[136:139], v[64:67], v[136:139]\n"
"v_mfma_f32_16x16x32_bf16 v[140:143], a[80:83], a[240:243], 0\n"
"v_mfma_f32_16x16x32_bf16 v[140:143], a[92:95], a[244:247], v[140:143]\n"
"s_add_u32 m0, s34, 9216\n"
"v_mfma_f32_16x16x32_bf16 v[140:143], a[104:107], a[248:251], v[140:143]\n"
"buffer_load_dwordx4 v191, s[8:11], s32 offen lds\n"
"s_add_u32 s32, s32, s33\n"
"v_mfma_f32_16x16x32_bf16 v[140:143], a[116:119], a[252:255], v[140:143]\n"
"s_nop 0\n"
"v_cvt_pk_bf16_f32 v134, v136, v137\n"
"v_mfma_f32_16x16x32_bf16 v[140:143], a[128:131], v[56:59], v[140:143]\n"
"v_cvt_pk_bf16_f32 v135, v138, v139\n"
"v_mfma_f32_16x16x32_bf16 v[140:143], a[140:143], v[64:67], v[140:143]\n"
"ds_write_b128 v186, v[132:135] offset:47392\n"
"v_mfma_f32_16x16x32_bf16 v[120:123], a[48:51], v[56:59], v[120:123]\n"
"s_add_u32 m0, s34, 7168\n"
"s_nop 0\n"
"buffer_load_dwordx4 v190, s[8:11], s0 offen lds\n"
"v_mfma_f32_16x16x32_bf16 v[120:123], a[60:63], v[64:67], v[120:123]\n"
"v_mfma_f32_16x16x32_bf16 v[124:127], a[52:55], v[56:59], v[124:127]\n"
"s_add_u32 m0, s34, 10240\n"
"v_mfma_f32_16x16x32_bf16 v[124:127], a[64:67], v[64:67], v[124:127]\n"
"buffer_load_dwordx4 v191, s[8:11], s0 offen lds\n"
"v_mfma_f32_16x16x32_bf16 v[128:131], a[56:59], v[56:59], v[128:131]\n"
"s_add_u32 s0, s0, s33\n"
"v_mfma_f32_16x16x32_bf16 v[128:131], a[68:71], v[64:67], v[128:131]\n"
"s_nop 0\n"
"v_cvt_pk_bf16_f32 v120, v120, v121\n"
"v_cvt_pk_bf16_f32 v121, v122, v123\n"
"s_nop 0\n"
"v_cvt_pk_bf16_f32 v122, v124, v125\n"
"s_add_u32 m0, s34, 8192\n"
"v_cvt_pk_bf16_f32 v123, v126, v127\n"
"ds_write_b128 v186, v[120:123] offset:47264\n"
"buffer_load_dwordx4 v190, s[8:11], s1 offen lds\n"
"v_cvt_pk_bf16_f32 v128, v128, v129\n"
"v_cvt_pk_bf16_f32 v129, v130, v131\n"
"v_cvt_pk_bf16_f32 v130, v140, v141\n"
"v_cvt_pk_bf16_f32 v131, v142, v143\n"
"s_add_u32 m0, s34, 11264\n"
"ds_write_b128 v186, v[128:131] offset:47328\n"
"buffer_load_dwordx4 v191, s[8:11], s1 offen lds\n"
"s_add_u32 s1, s1, s33\n"
"s_waitcnt vmcnt(12)\n"
"s_barrier\n"
"ds_read_b64_tr_b16 a[144:145], v68\n"
"ds_read_b64_tr_b16 a[146:147], v68 offset:1536\n"
"ds_read_b64_tr_b16 a[148:149], v68 offset:32\n"
"ds_read_b64_tr_b16 a[150:151], v68 offset:1568\n"
"ds_read_b64_tr_b16 a[152:153], v68 offset:64\n"
"ds_read_b64_tr_b16 a[154:155], v68 offset:1600\n"
"ds_read_b64_tr_b16 a[156:157], v68 offset:3072\n"
"ds_read_b64_tr_b16 a[158:159], v68 offset:4608\n"
"buffer_load_dword v200, off, s[28:31], s39 sc1\n"
"ds_read_b64_tr_b16 a[160:161], v68 offset:3104\n"
"ds_read_b64_tr_b16 a[162:163], v68 offset:4640\n"
"ds_read_b64_tr_b16 a[164:165], v68 offset:3136\n"
"ds_read_b64_tr_b16 a[166:167], v68 offset:4672\n"
"ds_read_b64_tr_b16 a[168:169], v68 offset:6752\n"
"ds_read_b64_tr_b16 a[170:171], v68 offset:8288\n"
"ds_read_b64_tr_b16 a[172:173], v68 offset:6784\n"
"ds_read_b64_tr_b16 a[174:175], v68 offset:8320\n"
"ds_read_b64_tr_b16 a[176:177], v68 offset:6816\n"
"ds_read_b64_tr_b16 a[178:179], v68 offset:8352\n"
"s_waitcnt lgkmcnt(15)\n"
"v_mfma_f32_16x16x32_bf16 v[204:207], a[144:147], v[32:35], 0\n"
"s_waitcnt lgkmcnt(14)\n"
"ds_read_b64_tr_b16 a[180:181], v68 offset:9824\n"
"ds_read_b64_tr_b16 a[182:183], v68 offset:11360\n"
"v_mfma_f32_16x16x32_bf16 v[208:211], a[148:151], v[32:35], 0\n"
"s_waitcnt lgkmcnt(14)\n"
"ds_read_b64_tr_b16 a[184:185], v68 offset:9856\n"
"ds_read_b64_tr_b16 a[186:187], v68 offset:11392\n"
"v_mfma_f32_16x16x32_bf16 v[212:215], a[152:155], v[32:35], 0\n"
"s_waitcnt lgkmcnt(14)\n"
"ds_read_b64_tr_b16 a[188:189], v68 offset:9888\n"
"ds_read_b64_tr_b16 a[190:191], v68 offset:11424\n"
"v_mfma_f32_16x16x32_bf16 v[204:207], a[156:159], v[36:39], v[204:207]\n"
"s_waitcnt lgkmcnt(14)\n"
"ds_read_b64_tr_b16 a[192:193], v69\n"
"ds_read_b64_tr_b16 a[194:195], v69 offset:1536\n"
"v_mfma_f32_16x16x32_bf16 v[208:211], a[160:163], v[36:39], v[208:211]\n"
"s_waitcnt lgkmcnt(14)\n"
"ds_read_b64_tr_b16 a[196:197], v69 offset:32\n"
"ds_read_b64_tr_b16 a[198:199], v69 offset:1568\n"
"v_mfma_f32_16x16x32_bf16 v[212:215], a[164:167], v[36:39], v[212:215]\n"
"s_waitcnt lgkmcnt(14)\n"
"ds_read_b64_tr_b16 a[200:201], v69 offset:64\n"
"ds_read_b64_tr_b16 a[202:203], v69 offset:1600\n"
"v_mfma_f32_16x16x32_bf16 v[204:207], a[168:171], v[40:43], v[204:207]\n"
"s_waitcnt lgkmcnt(14)\n"
"ds_read_b64_tr_b16 a[204:205], v69 offset:3072\n"
"ds_read_b64_tr_b16 a[206:207], v69 offset:4608\n"
"v_mfma_f32_16x16x32_bf16 v[208:211], a[172:175], v[40:43], v[208:211]\n"
"s_waitcnt lgkmcnt(14)\n"
"ds_read_b64_tr_b16 a[208:209], v69 offset:3104\n"
"ds_read_b64_tr_b16 a[210:211], v69 offset:4640\n"
"v_mfma_f32_16x16x32_bf16 v[212:215], a[176:179], v[40:43], v[212:215]\n"
"s_waitcnt lgkmcnt(14)\n"
"ds_read_b64_tr_b16 a[212:213], v69 offset:3136\n"
"ds_read_b64_tr_b16 a[214:215], v69 offset:4672\n"
"v_mfma_f32_16x16x32_bf16 v[204:207], a[180:183], v[44:47], v[204:207]\n"
"s_waitcnt lgkmcnt(14)\n"
"v_mfma_f32_16x16x32_bf16 v[208:211], a[184:187], v[44:47], v[208:211]\n"
"s_waitcnt lgkmcnt(12)\n"
"v_mfma_f32_16x16x32_bf16 v[212:215], a[188:191], v[44:47], v[212:215]\n"
"s_waitcnt lgkmcnt(10)\n"
"v_mfma_f32_16x16x32_bf16 v[204:207], a[192:195], v[48:51], v[204:207]\n"
"s_waitcnt lgkmcnt(8)\n"
"v_mfma_f32_16x16x32_bf16 v[208:211], a[196:199], v[48:51], v[208:211]\n"
"s_waitcnt lgkmcnt(6)\n"
"v_mfma_f32_16x16x32_bf16 v[212:215], a[200:203], v[48:51], v[212:215]\n"
"s_waitcnt lgkmcnt(4)\n"
"v_mfma_f32_16x16x32_bf16 v[204:207], a[204:207], v[52:55], v[204:207]\n"
"s_waitcnt lgkmcnt(2)\n"
"v_mfma_f32_16x16x32_bf16 v[208:211], a[208:211], v[52:55], v[208:211]\n"
"s_waitcnt lgkmcnt(0)\n"
"s_barrier\n"
"v_mfma_f32_16x16x32_bf16 v[212:215], a[212:215], v[52:55], v[212:215]\n"
"ds_read_b128 v[112:115], v178\n"
"ds_read_b64 v[82:83], v178 offset:64\n"
"ds_read_b128 v[120:123], v179\n"
"v_cvt_pk_bf16_f32 v144, v204, v205\n"
"v_cvt_pk_bf16_f32 v145, v206, v207\n"
"v_mfma_f32_16x16x32_bf16 v[204:207], a[144:147], a[216:219], 0\n"
"ds_read_b64 v[84:85], v179 offset:64\n"
"buffer_load_dword v237, v238, s[8:11], s78 offen\n"
"v_mfma_f32_16x16x32_bf16 v[204:207], a[156:159], a[220:223], v[204:207]\n"
"s_add_u32 s78, s78, s33\n"
"ds_read_b128 v[116:119], v178 offset:13504\n"
"v_cvt_pk_bf16_f32 v146, v208, v209\n"
"v_mfma_f32_16x16x32_bf16 v[204:207], a[168:171], a[224:227], v[204:207]\n"
"ds_read_b64 v[90:91], v178 offset:13568\n"
"v_cvt_pk_bf16_f32 v147, v210, v211\n"
"v_mfma_f32_16x16x32_bf16 v[204:207], a[180:183], a[228:231], v[204:207]\n"
"s_waitcnt vmcnt(12)\n"
"ds_read_b128 v[124:127], v179 offset:13504\n"
"v_cvt_pk_bf16_f32 v158, v212, v213\n"
"v_mfma_f32_16x16x32_bf16 v[204:207], a[192:195], a[232:235], v[204:207]\n"
"ds_read_b64 v[92:93], v179 offset:13568\n"
"v_cvt_pk_bf16_f32 v159, v214, v215\n"
"v_mfma_f32_16x16x32_bf16 v[204:207], a[204:207], a[236:239], v[204:207]\n"
".Lic0%=:\n"
"s_cbranch_execz .Lib0%=\n"
"ds_read_b128 v[128:131], v178 offset:27008\n"
"buffer_load_dword v237, v238, s[8:11], s78 offen\n"
"v_mfma_f32_16x16x32_bf16 v[208:211], a[148:151], a[216:219], 0\n"
"s_waitcnt lgkmcnt(7)\n"
"ds_read_b64 v[98:99], v178 offset:27072\n"
"v_subrev_u32 v178, s48, v178\n"
"v_mfma_f32_16x16x32_bf16 v[112:115], v[112:115], v[144:147], 0\n"
"s_add_u32 s78, s78, s33\n"
"ds_read_b128 v[136:139], v179 offset:27008\n"
"v_cvt_pk_bf16_f32 v148, v204, v205\n"
"v_mfma_f32_16x16x32_bf16 v[112:115], v[80:83], v[156:159], v[112:115]\n"
"s_waitcnt lgkmcnt(7)\n"
"ds_read_b64 v[100:101], v179 offset:27072\n"
"v_cvt_pk_bf16_f32 v149, v206, v207\n"
"v_mfma_f32_16x16x32_bf16 v[120:123], v[120:123], v[144:147], 0\n"
"ds_read_b128 v[132:135], v180\n"
"v_subrev_u32 v179, s48, v179\n"
"v_mfma_f32_16x16x32_bf16 v[120:123], v[84:87], v[158:161], v[120:123]\n"
"s_waitcnt lgkmcnt(7)\n"
"ds_read_b64 v[106:107], v180 offset:64\n"
"v_subrev_u32 v180, s48, v180\n"
"v_mfma_f32_16x16x32_bf16 v[116:119], v[116:119], v[144:147], 0\n"
"s_waitcnt vmcnt(11)\n"
"ds_read_b128 v[140:143], v181\n"
"v_mfma_f32_16x16x32_bf16 v[116:119], v[88:91], v[156:159], v[116:119]\n"
"s_waitcnt lgkmcnt(7)\n"
"ds_read_b64 v[108:109], v181 offset:64\n"
"v_subrev_u32 v181, s48, v181\n"
"v_mfma_f32_16x16x32_bf16 v[124:127], v[124:127], v[144:147], 0\n"
"ds_read_b128 v[32:35], v188\n"
"v_max_f32 v112, v112, v120\n"
"v_mfma_f32_16x16x32_bf16 v[124:127], v[92:95], v[158:161], v[124:127]\n"
"s_waitcnt lgkmcnt(7)\n"
"ds_read_b128 v[36:39], v189\n"
"v_max_f32 v113, v113, v121\n"
"v_mfma_f32_16x16x32_bf16 v[128:131], v[128:131], v[144:147], 0\n"
"ds_read_b128 v[40:43], v188 offset:1024\n"
"v_max_f32 v114, v114, v122\n"
"v_mfma_f32_16x16x32_bf16 v[128:131], v[96:99], v[156:159], v[128:131]\n"
"s_waitcnt lgkmcnt(7)\n"
"ds_read_b128 v[44:47], v189 offset:1024\n"
"v_max_f32 v115, v115, v123\n"
"v_mfma_f32_16x16x32_bf16 v[136:139], v[136:139], v[144:147], 0\n"
"ds_read_b64_tr_b16 v[224:225], v182 offset:128\n"
"v_max3_f32 v170, v112, v113, v114\n"
"v_mfma_f32_16x16x32_bf16 v[136:139], v[100:103], v[158:161], v[136:139]\n"
"s_waitcnt lgkmcnt(7)\n"
"ds_read_b64_tr_b16 v[226:227], v183 offset:128\n"
"v_max_f32 v116, v116, v124\n"
"v_mfma_f32_16x16x32_bf16 v[132:135], v[132:135], v[144:147], 0\n"
"ds_read_b64_tr_b16 v[228:229], v182 offset:896\n"
"v_max_f32 v117, v117, v125\n"
"v_mfma_f32_16x16x32_bf16 v[132:135], v[104:107], v[156:159], v[132:135]\n"
"s_waitcnt lgkmcnt(7)\n"
"ds_read_b64_tr_b16 v[230:231], v183 offset:896\n"
"v_max_f32 v118, v118, v126\n"
"v_mfma_f32_16x16x32_bf16 v[140:143], v[140:143], v[144:147], 0\n"
"s_waitcnt vmcnt(9)\n"
"ds_read_b128 v[48:51], v188 offset:2048\n"
"v_max_f32 v119, v119, v127\n"
"v_mfma_f32_16x16x32_bf16 v[140:143], v[108:111], v[158:161], v[140:143]\n"
"s_waitcnt lgkmcnt(7)\n"
"ds_read_b128 v[52:55], v189 offset:2048\n"
"v_max3_f32 v170, v170, v115, v116\n"
"v_mfma_f32_16x16x32_bf16 v[204:207], a[144:147], a[240:243], 0\n"
"s_add_u32 m0, s34, 0\n"
"ds_read_b64_tr_b16 v[244:245], v182 offset:136\n"
"buffer_load_dwordx4 v190, s[8:11], s32 offen lds\n"
"v_mfma_f32_16x16x32_bf16 v[204:207], a[156:159], a[244:247], v[204:207]\n"
"ds_read_b64_tr_b16 v[246:247], v183 offset:136\n"
"v_max3_f32 v170, v170, v117, v118\n"
"v_mfma_f32_16x16x32_bf16 v[204:207], a[168:171], a[248:251], v[204:207]\n"
"ds_read_b64_tr_b16 v[248:249], v182 offset:904\n"
"v_max_f32 v128, v128, v136\n"
"v_mfma_f32_16x16x32_bf16 v[204:207], a[180:183], a[252:255], v[204:207]\n"
"ds_read_b64_tr_b16 v[250:251], v183 offset:904\n"
"v_max_f32 v129, v129, v137\n"
"v_mfma_f32_16x16x32_bf16 v[204:207], a[192:195], v[56:59], v[204:207]\n"
"ds_read_b64_tr_b16 v[120:121], v182 offset:72\n"
"v_max_f32 v130, v130, v138\n"
"v_mfma_f32_16x16x32_bf16 v[204:207], a[204:207], v[64:67], v[204:207]\n"
"ds_read_b64_tr_b16 v[122:123], v183 offset:72\n"
"v_max_f32 v131, v131, v139\n"
"v_mfma_f32_16x16x32_bf16 v[208:211], a[160:163], a[220:223], v[208:211]\n"
"ds_read_b64_tr_b16 v[124:125], v182 offset:840\n"
"v_max3_f32 v170, v170, v119, v128\n"
"v_mfma_f32_16x16x32_bf16 v[208:211], a[172:175], a[224:227], v[208:211]\n"
"ds_read_b64_tr_b16 v[126:127], v183 offset:840\n"
"v_max3_f32 v170, v170, v129, v130\n"
"v_mfma_f32_16x16x32_bf16 v[208:211], a[184:187], a[228:231], v[208:211]\n"
"s_waitcnt lgkmcnt(8)\n"
"v_cvt_pk_bf16_f32 v152, v204, v205\n"
"v_cvt_pk_bf16_f32 v153, v206, v207\n"
"v_mfma_f32_16x16x32_bf16 v[208:211], a[196:199], a[232:235], v[208:211]\n"
"s_add_u32 m0, s34, 3072\n"
"ds_read_b64_tr_b16 v[216:217], v182 offset:27136\n"
"buffer_load_dwordx4 v191, s[8:11], s32 offen lds\n"
"v_mfma_f32_16x16x32_bf16 v[208:211], a[208:211], a[236:239], v[208:211]\n"
"s_add_u32 s32, s32, s33\n"
"ds_read_b64_tr_b16 v[218:219], v183 offset:27136\n"
"ds_read_b64_tr_b16 v[220:221], v182 offset:27904\n"
"v_mfma_f32_16x16x32_bf16 v[204:207], a[0:3], v[32:35], 0\n"
"ds_read_b64_tr_b16 v[222:223], v183 offset:27904\n"
"v_max_f32 v132, v132, v140\n"
"v_mfma_f32_16x16x32_bf16 v[204:207], a[12:15], v[36:39], v[204:207]\n"
"ds_read_b64_tr_b16 v[72:73], v182 offset:27144\n"
"v_cvt_pk_bf16_f32 v150, v208, v209\n"
"v_mfma_f32_16x16x32_bf16 v[204:207], a[24:27], v[40:43], v[204:207]\n"
"v_cvt_pk_bf16_f32 v151, v210, v211\n"
"v_max_f32 v133, v133, v141\n"
"v_mfma_f32_16x16x32_bf16 v[204:207], a[36:39], v[44:47], v[204:207]\n"
"ds_read_b64_tr_b16 v[74:75], v183 offset:27144\n"
"ds_read_b64_tr_b16 v[76:77], v182 offset:27912\n"
"v_mfma_f32_16x16x32_bf16 v[204:207], a[48:51], v[48:51], v[204:207]\n"
"ds_read_b64_tr_b16 v[78:79], v183 offset:27912\n"
"v_max_f32 v134, v134, v142\n"
"v_mfma_f32_16x16x32_bf16 v[204:207], a[60:63], v[52:55], v[204:207]\n"
"ds_read_b64_tr_b16 v[136:137], v182 offset:27080\n"
"v_max_f32 v135, v135, v143\n"
"v_mfma_f32_16x16x32_bf16 v[208:211], a[148:151], a[240:243], 0\n"
"s_add_u32 m0, s34, 1024\n"
"ds_read_b64_tr_b16 v[138:139], v183 offset:27080\n"
"buffer_load_dwordx4 v190, s[8:11], s0 offen lds\n"
"v_mfma_f32_16x16x32_bf16 v[208:211], a[160:163], a[244:247], v[208:211]\n"
"ds_read_b64_tr_b16 v[140:141], v182 offset:27848\n"
"v_cvt_pk_bf16_f32 v204, v204, v205\n"
"v_mfma_f32_16x16x32_bf16 v[208:211], a[172:175], a[248:251], v[208:211]\n"
"v_cvt_pk_bf16_f32 v205, v206, v207\n"
"v_max3_f32 v170, v170, v131, v132\n"
"v_mfma_f32_16x16x32_bf16 v[208:211], a[184:187], a[252:255], v[208:211]\n"
"ds_read_b64_tr_b16 v[142:143], v183 offset:27848\n"
"v_max3_f32 v170, v170, v133, v134\n"
"v_mfma_f32_16x16x32_bf16 v[208:211], a[196:199], v[56:59], v[208:211]\n"
"ds_read_b64 v[80:81], v20 offset:272\n"
"ds_read_b64 v[86:87], v20 offset:336\n"
"v_mfma_f32_16x16x32_bf16 v[208:211], a[208:211], v[64:67], v[208:211]\n"
"ds_read_b64 v[88:89], v20 offset:544\n"
"ds_read_b64 v[94:95], v20 offset:608\n"
"v_mfma_f32_16x16x32_bf16 v[212:215], a[152:155], a[216:219], 0\n"
"v_mov_b64 v[98:99], 0\n"
"v_mov_b64 v[100:101], 0\n"
"v_mfma_f32_16x16x32_bf16 v[212:215], a[164:167], a[220:223], v[212:215]\n"
"ds_read_b64 v[96:97], v20 offset:816\n"
"v_mov_b64 v[106:107], 0\n"
"v_mfma_f32_16x16x32_bf16 v[212:215], a[176:179], a[224:227], v[212:215]\n"
"s_add_u32 m0, s34, 4096\n"
"v_cvt_pk_bf16_f32 v154, v208, v209\n"
"v_cvt_pk_bf16_f32 v155, v210, v211\n"
"v_mfma_f32_16x16x32_bf16 v[212:215], a[188:191], a[228:231], v[212:215]\n"
"buffer_load_dwordx4 v191, s[8:11], s0 offen lds\n"
"v_mov_b64 v[108:109], 0\n"
"v_mfma_f32_16x16x32_bf16 v[212:215], a[200:203], a[232:235], v[212:215]\n"
"s_add_u32 s0, s0, s33\n"
"v_max_f32 v171, v170, v135\n"
"v_max_f32 v170, v170, v135\n"
"v_mfma_f32_16x16x32_bf16 v[212:215], a[212:215], a[236:239], v[212:215]\n"
"s_waitcnt lgkmcnt(3)\n"
"ds_read_b64 v[102:103], v20 offset:880\n"
"ds_read_b64 v[104:105], v20 offset:1088\n"
"v_mfma_f32_16x16x32_bf16 v[208:211], a[4:7], v[32:35], 0\n"
"s_waitcnt lgkmcnt(3)\n"
"v_permlane16_swap_b32 v170, v171\n"
"v_mfma_f32_16x16x32_bf16 v[208:211], a[16:19], v[36:39], v[208:211]\n"
"v_max_f32 v170, v170, v171\n"
"v_cvt_pk_bf16_f32 v162, v212, v213\n"
"v_mfma_f32_16x16x32_bf16 v[208:211], a[28:31], v[40:43], v[208:211]\n"
"s_waitcnt lgkmcnt(2)\n"
"v_cvt_pk_bf16_f32 v163, v214, v215\n"
"v_mov_b32 v171, v170\n"
"v_mfma_f32_16x16x32_bf16 v[208:211], a[40:43], v[44:47], v[208:211]\n"
"s_nop 0\n"
"v_permlane32_swap_b32 v170, v171\n"
"v_max_f32 v170, v170, v171\n"
"v_mul_f32 v170, 0xbe553b95, v170\n"
"v_pk_fma_f32 v[112:113], v[112:113], s[70:71], v[170:171] op_sel_hi:[1,0,0]\n"
"v_pk_fma_f32 v[114:115], v[114:115], s[70:71], v[170:171] op_sel_hi:[1,0,0]\n"
"v_pk_fma_f32 v[116:117], v[116:117], s[70:71], v[170:171] op_sel_hi:[1,0,0]\n"
"v_pk_fma_f32 v[118:119], v[118:119], s[70:71], v[170:171] op_sel_hi:[1,0,0]\n"
"v_pk_fma_f32 v[128:129], v[128:129], s[70:71], v[170:171] op_sel_hi:[1,0,0]\n"
"v_pk_fma_f32 v[130:131], v[130:131], s[70:71], v[170:171] op_sel_hi:[1,0,0]\n"
"v_pk_fma_f32 v[132:133], v[132:133], s[70:71], v[170:171] op_sel_hi:[1,0,0]\n"
"v_pk_fma_f32 v[134:135], v[134:135], s[70:71], v[170:171] op_sel_hi:[1,0,0]\n"
"v_mfma_f32_16x16x32_bf16 v[208:211], a[52:55], v[48:51], v[208:211]\n"
"s_waitcnt lgkmcnt(0)\n"
"ds_read_b64 v[110:111], v20 offset:1152\n"
"v_exp_f32 v112, v112\n"
"v_mfma_f32_16x16x32_bf16 v[208:211], a[64:67], v[52:55], v[208:211]\n"
"s_add_u32 m0, s34, 2048\n"
"v_exp_f32 v113, v113\n"
"v_mfma_f32_16x16x32_bf16 v[212:215], a[152:155], a[240:243], 0\n"
"buffer_load_dwordx4 v190, s[8:11], s1 offen lds\n"
"v_exp_f32 v114, v114\n"
"v_mfma_f32_16x16x32_bf16 v[212:215], a[164:167], a[244:247], v[212:215]\n"
"v_exp_f32 v115, v115\n"
"v_mfma_f32_16x16x32_bf16 v[212:215], a[176:179], a[248:251], v[212:215]\n"
"v_cvt_pk_bf16_f32 v206, v208, v209\n"
"v_cvt_pk_bf16_f32 v207, v210, v211\n"
"v_mfma_f32_16x16x32_bf16 v[212:215], a[188:191], a[252:255], v[212:215]\n"
"ds_write_b128 v186, v[204:207] offset:60768\n"
"v_mfma_f32_16x16x32_bf16 v[212:215], a[200:203], v[56:59], v[212:215]\n"
"s_waitcnt vmcnt(7) lgkmcnt(1)\n"
"v_and_b32 v200, 0xffffff00, v200\n"
"v_add_u32 v200, 0x100, v200\n"
"v_mfma_f32_16x16x32_bf16 v[212:215], a[212:215], v[64:67], v[212:215]\n"
"s_mov_b64 exec, 1\n"
"buffer_store_dword v200, off, s[28:31], s39 offset:128 sc1\n"
"s_mov_b64 exec, -1\n"
"v_mfma_f32_16x16x32_bf16 v[208:211], a[80:83], v[32:35], 0\n"
"s_add_u32 m0, s34, 5120\n"
"v_sub_u32 v201, 0, v200\n"
"s_nop 0\n"
"v_readfirstlane_b32 s50, v201\n"
"v_mfma_f32_16x16x32_bf16 v[208:211], a[92:95], v[36:39], v[208:211]\n"
"buffer_load_dwordx4 v191, s[8:11], s1 offen lds\n"
"v_cvt_pk_bf16_f32 v166, v212, v213\n"
"v_mfma_f32_16x16x32_bf16 v[208:211], a[104:107], v[40:43], v[208:211]\n"
"s_add_u32 s1, s1, s33\n"
"v_cvt_pk_bf16_f32 v167, v214, v215\n"
"v_cvt_pk_bf16_f32 v82, v112, v114\n"
"v_mfma_f32_16x16x32_bf16 v[208:211], a[116:119], v[44:47], v[208:211]\n"
"v_cvt_pk_bf16_f32 v83, v113, v115\n"
"v_exp_f32 v116, v116\n"
"v_mfma_f32_16x16x32_bf16 v[208:211], a[128:131], v[48:51], v[208:211]\n"
"ds_read_b128 v[112:115], v178 offset:13504\n"
"v_exp_f32 v117, v117\n"
"v_mfma_f32_16x16x32_bf16 v[208:211], a[140:143], v[52:55], v[208:211]\n"
"v_exp_f32 v118, v118\n"
"v_mfma_f32_16x16x32_bf16 v[212:215], a[8:11], v[32:35], 0\n"
"v_exp_f32 v119, v119\n"
"v_mfma_f32_16x16x32_bf16 v[212:215], a[20:23], v[36:39], v[212:215]\n"
"v_cvt_pk_bf16_f32 v84, v116, v118\n"
"v_cvt_pk_bf16_f32 v85, v117, v119\n"
"v_mfma_f32_16x16x32_bf16 v[212:215], a[32:35], v[40:43], v[212:215]\n"
"ds_read_b128 v[116:119], v178 offset:27008\n"
"v_exp_f32 v128, v128\n"
"v_mfma_f32_16x16x32_bf16 v[212:215], a[44:47], v[44:47], v[212:215]\n"
"v_exp_f32 v129, v129\n"
"v_mfma_f32_16x16x32_bf16 v[212:215], a[56:59], v[48:51], v[212:215]\n"
"v_exp_f32 v130, v130\n"
"v_mfma_f32_16x16x32_bf16 v[212:215], a[68:71], v[52:55], v[212:215]\n"
"v_exp_f32 v131, v131\n"
"v_mfma_f32_16x16x32_bf16 v[204:207], a[72:75], v[32:35], 0\n"
"v_cvt_pk_bf16_f32 v90, v128, v130\n"
"v_cvt_pk_bf16_f32 v91, v129, v131\n"
"v_mfma_f32_16x16x32_bf16 v[204:207], a[84:87], v[36:39], v[204:207]\n"
"ds_read_b128 v[128:131], v180\n"
"v_subrev_u32 v182, s48, v182\n"
"v_mfma_f32_16x16x32_bf16 v[204:207], a[96:99], v[40:43], v[204:207]\n"
"v_cvt_pk_bf16_f32 v212, v212, v213\n"
"v_cvt_pk_bf16_f32 v213, v214, v215\n"
"v_mfma_f32_16x16x32_bf16 v[204:207], a[108:111], v[44:47], v[204:207]\n"
"v_cvt_pk_bf16_f32 v214, v208, v209\n"
"v_cvt_pk_bf16_f32 v215, v210, v211\n"
"v_mfma_f32_16x16x32_bf16 v[204:207], a[120:123], v[48:51], v[204:207]\n"
"ds_write_b128 v186, v[212:215] offset:60832\n"
"v_mfma_f32_16x16x32_bf16 v[204:207], a[132:135], v[52:55], v[204:207]\n"
"v_exp_f32 v132, v132\n"
"v_mfma_f32_16x16x32_bf16 v[208:211], a[76:79], v[32:35], 0\n"
"v_exp_f32 v133, v133\n"
"v_mfma_f32_16x16x32_bf16 v[208:211], a[88:91], v[36:39], v[208:211]\n"
"v_exp_f32 v134, v134\n"
"v_mfma_f32_16x16x32_bf16 v[208:211], a[100:103], v[40:43], v[208:211]\n"
"v_exp_f32 v135, v135\n"
"v_mfma_f32_16x16x32_bf16 v[208:211], a[112:115], v[44:47], v[208:211]\n"
"v_cvt_pk_bf16_f32 v204, v204, v205\n"
"v_cvt_pk_bf16_f32 v205, v206, v207\n"
"v_mfma_f32_16x16x32_bf16 v[208:211], a[124:127], v[48:51], v[208:211]\n"
"v_cvt_pk_bf16_f32 v92, v132, v134\n"
"v_cvt_pk_bf16_f32 v93, v133, v135\n"
"v_mfma_f32_16x16x32_bf16 v[208:211], a[136:139], v[52:55], v[208:211]\n"
"ds_read_b64_tr_b16 a[240:241], v182 offset:13632\n"
"ds_read_b64_tr_b16 a[244:245], v182 offset:14400\n"
"v_mfma_f32_16x16x32_bf16 v[212:215], a[144:147], v[32:35], 0\n"
"v_subrev_u32 v183, s48, v183\n"
"v_bfi_b32 v80, v4, v80, v26\n"
"v_mfma_f32_16x16x32_bf16 v[212:215], a[156:159], v[36:39], v[212:215]\n"
"ds_read_b64_tr_b16 a[242:243], v183 offset:13632\n"
"ds_read_b64_tr_b16 a[246:247], v183 offset:14400\n"
"v_mfma_f32_16x16x32_bf16 v[212:215], a[168:171], v[40:43], v[212:215]\n"
"v_cvt_pk_bf16_f32 v206, v208, v209\n"
"v_cvt_pk_bf16_f32 v207, v210, v211\n"
"v_mfma_f32_16x16x32_bf16 v[212:215], a[180:183], v[44:47], v[212:215]\n"
"ds_write_b128 v186, v[204:207] offset:60896\n"
"v_mfma_f32_16x16x32_bf16 v[212:215], a[192:195], v[48:51], v[212:215]\n"
"v_bfi_b32 v81, v5, v81, v26\n"
"v_bfi_b32 v86, v6, v86, v26\n"
"v_mfma_f32_16x16x32_bf16 v[212:215], a[204:207], v[52:55], v[212:215]\n"
"v_bfi_b32 v87, v7, v87, v26\n"
"v_bfi_b32 v88, v0, v88, v26\n"
"v_mfma_f32_16x16x32_bf16 v[208:211], a[148:151], v[32:35], 0\n"
"v_bfi_b32 v89, v1, v89, v26\n"
"v_bfi_b32 v94, v2, v94, v26\n"
"v_mfma_f32_16x16x32_bf16 v[208:211], a[160:163], v[36:39], v[208:211]\n"
"v_bfi_b32 v95, v3, v95, v26\n"
"v_bfi_b32 v96, v0, v96, v26\n"
"v_mfma_f32_16x16x32_bf16 v[208:211], a[172:175], v[40:43], v[208:211]\n"
"v_cvt_pk_bf16_f32 v144, v212, v213\n"
"v_cvt_pk_bf16_f32 v145, v214, v215\n"
"v_mfma_f32_16x16x32_bf16 v[208:211], a[184:187], v[44:47], v[208:211]\n"
"v_bfi_b32 v97, v1, v97, v26\n"
"v_bfi_b32 v102, v2, v102, v26\n"
"v_mfma_f32_16x16x32_bf16 v[208:211], a[196:199], v[48:51], v[208:211]\n"
"s_waitcnt lgkmcnt(0)\n"
"v_bfi_b32 v103, v3, v103, v26\n"
"v_bfi_b32 v104, v8, v104, v26\n"
"v_mfma_f32_16x16x32_bf16 v[208:211], a[208:211], v[52:55], v[208:211]\n"
"s_barrier\n"
"v_smfmac_f32_16x16x64_bf16 v[252:255], v[82:85], v[224:231], v63\n"
"s_add_u32 m0, s79, 130176\n"
"s_nop 0\n"
"buffer_load_dwordx4 v192, s[16:19], s43 offen lds\n"
"v_smfmac_f32_16x16x64_bf16 v[252:255], v[90:93], v[216:223], v63\n"
"ds_read_b128 v[132:135], v180 offset:13504\n"
"v_smfmac_f32_16x16x64_bf16 v[98:101], v[82:85], v[244:251], v63\n"
"ds_read_b64_tr_b16 v[224:225], v182 offset:40640\n"
"v_smfmac_f32_16x16x64_bf16 v[98:101], v[90:93], v[72:79], v63\n"
"v_cvt_pk_bf16_f32 v146, v208, v209\n"
"v_mfma_f32_16x16x32_bf16 v[204:207], a[152:155], v[32:35], 0\n"
"ds_read_b128 v[32:35], v188 offset:6144\n"
"v_cvt_pk_bf16_f32 v147, v210, v211\n"
"v_mfma_f32_16x16x32_bf16 v[204:207], a[164:167], v[36:39], v[204:207]\n"
"ds_read_b128 v[36:39], v189 offset:6144\n"
"buffer_load_dword v237, v238, s[8:11], s78 offen\n"
"v_mfma_f32_16x16x32_bf16 v[204:207], a[176:179], v[40:43], v[204:207]\n"
"s_add_u32 s78, s78, s33\n"
"ds_read_b128 v[40:43], v188 offset:7168\n"
"v_cvt_pk_bf16_f32 v252, v252, v253\n"
"v_mfma_f32_16x16x32_bf16 v[204:207], a[188:191], v[44:47], v[204:207]\n"
"ds_read_b128 v[44:47], v189 offset:7168\n"
"v_cvt_pk_bf16_f32 v253, v254, v255\n"
"v_mfma_f32_16x16x32_bf16 v[204:207], a[200:203], v[48:51], v[204:207]\n"
"s_add_u32 m0, s79, 130176\n"
"ds_read_b128 v[48:51], v188 offset:8192\n"
"buffer_load_dwordx4 v192, s[16:19], s43 offen offset:1024 lds\n"
"v_mfma_f32_16x16x32_bf16 v[204:207], a[212:215], v[52:55], v[204:207]\n"
"ds_read_b128 v[52:55], v189 offset:8192\n"
"v_cvt_pk_bf16_f32 v98, v98, v99\n"
"v_smfmac_f32_16x16x64_bf16 v[106:109], v[82:85], v[120:127], v63\n"
"ds_read_b128 v[120:123], v179 offset:13504\n"
"v_smfmac_f32_16x16x64_bf16 v[106:109], v[90:93], v[136:143], v63\n"
"s_waitcnt lgkmcnt(6)\n"
"ds_read_b128 v[124:127], v179 offset:27008\n"
"v_mfma_f32_16x16x32_bf16 v[82:85], v[172:175], v[82:85], 0\n"
"ds_read_b128 v[136:139], v181\n"
"ds_read_b128 v[140:143], v181 offset:13504\n"
"v_mfma_f32_16x16x32_bf16 v[90:93], v[172:175], v[90:93], v[82:85]\n"
"v_cvt_pk_bf16_f32 v158, v204, v205\n"
"v_cvt_pk_bf16_f32 v159, v206, v207\n"
"v_mfma_f32_16x16x32_bf16 v[212:215], a[0:3], v[32:35], 0\n"
"s_waitcnt lgkmcnt(8)\n"
"v_cvt_pk_bf16_f32 v99, v100, v101\n"
"v_cvt_pk_bf16_f32 v106, v106, v107\n"
"v_mfma_f32_16x16x32_bf16 v[212:215], a[12:15], v[36:39], v[212:215]\n"
"s_waitcnt lgkmcnt(7)\n"
"ds_read_b64 v[82:83], v178 offset:13568\n"
"ds_read_b64 v[84:85], v179 offset:13568\n"
"v_mfma_f32_16x16x32_bf16 v[212:215], a[24:27], v[40:43], v[212:215]\n"
"s_waitcnt lgkmcnt(6)\n"
"v_rcp_f32 v171, v90\n"
"v_mfma_f32_16x16x32_bf16 v[212:215], a[36:39], v[44:47], v[212:215]\n"
"s_add_u32 m0, s79, 130176\n"
"ds_read_b64 v[90:91], v178 offset:27072\n"
"buffer_load_dwordx4 v192, s[16:19], s43 offen offset:2048 lds\n"
"v_mfma_f32_16x16x32_bf16 v[212:215], a[48:51], v[48:51], v[212:215]\n"
"s_add_u32 s43, s79, 18432\n"
"v_cvt_pk_bf16_f32 v171, v171, v171\n"
"v_and_b32 v92, v171, v176\n"
"v_mfma_f32_16x16x32_bf16 v[212:215], a[60:63], v[52:55], v[212:215]\n"
"v_and_b32 v93, v171, v177\n"
"v_cvt_pk_bf16_f32 v107, v108, v109\n"
"v_mfma_f32_16x16x32_bf16 v[208:211], a[4:7], v[32:35], 0\n"
"ds_read_b64_tr_b16 v[226:227], v183 offset:40640\n"
"buffer_load_dword v237, v238, s[8:11], s78 offen\n"
"v_mfma_f32_16x16x32_bf16 v[208:211], a[16:19], v[36:39], v[208:211]\n"
"s_add_u32 s78, s78, s33\n"
"ds_read_b64_tr_b16 v[228:229], v182 offset:41408\n"
"v_cvt_pk_bf16_f32 v212, v212, v213\n"
"v_mfma_f32_16x16x32_bf16 v[208:211], a[28:31], v[40:43], v[208:211]\n"
"s_waitcnt lgkmcnt(4)\n"
"v_cvt_pk_bf16_f32 v213, v214, v215\n"
"v_bfi_b32 v105, v9, v105, v26\n"
"v_mfma_f32_16x16x32_bf16 v[208:211], a[40:43], v[44:47], v[208:211]\n"
"s_waitcnt lgkmcnt(3)\n"
"ds_read_b64_tr_b16 v[230:231], v183 offset:41408\n"
"ds_read_b64_tr_b16 v[244:245], v182 offset:40648\n"
"v_mfma_f32_16x16x32_bf16 v[208:211], a[52:55], v[48:51], v[208:211]\n"
"s_waitcnt lgkmcnt(4)\n"
"ds_read_b64_tr_b16 v[246:247], v183 offset:40648\n"
"v_bfi_b32 v110, v10, v110, v26\n"
"v_mfma_f32_16x16x32_bf16 v[208:211], a[64:67], v[52:55], v[208:211]\n"
"s_add_u32 m0, s34, 6144\n"
"ds_read_b64_tr_b16 v[248:249], v182 offset:41416\n"
"buffer_load_dwordx4 v190, s[8:11], s32 offen lds\n"
"v_mfma_f32_16x16x16_bf16 v[252:255], v[252:253], v[92:93], 0\n"
"ds_read_b64_tr_b16 v[250:251], v183 offset:41416\n"
"v_bfi_b32 v111, v11, v111, v26\n"
"v_mfma_f32_16x16x16_bf16 v[98:101], v[98:99], v[92:93], 0\n"
"ds_read_b64_tr_b16 a[248:249], v182 offset:40584\n"
"v_cvt_pk_bf16_f32 v214, v208, v209\n"
"v_mfma_f32_16x16x16_bf16 v[106:109], v[106:107], v[92:93], 0\n"
"ds_read_b64 v[92:93], v179 offset:27072\n"
"v_cvt_pk_bf16_f32 v215, v210, v211\n"
"v_mfma_f32_16x16x32_bf16 v[112:115], v[112:115], v[148:151], 0\n"
"ds_write_b128 v187, v[212:215]\n"
"v_mfma_f32_16x16x32_bf16 v[112:115], v[80:83], v[160:163], v[112:115]\n"
"v_cvt_pk_bf16_f32 v252, v252, v253\n"
"v_cvt_pk_bf16_f32 v253, v254, v255\n"
"v_mfma_f32_16x16x32_bf16 v[120:123], v[120:123], v[148:151], 0\n"
"v_cvt_pk_bf16_f32 v254, v98, v99\n"
"v_cvt_pk_bf16_f32 v255, v100, v101\n"
"v_mfma_f32_16x16x32_bf16 v[120:123], v[84:87], v[162:165], v[120:123]\n"
"s_add_u32 m0, s34, 9216\n"
"buffer_store_dwordx4 v[252:255], v192, s[28:31], s37 offen sc1\n"
"v_mfma_f32_16x16x32_bf16 v[116:119], v[116:119], v[148:151], 0\n"
"ds_read_b64 v[98:99], v180 offset:64\n"
"ds_read_b64 v[100:101], v181 offset:64\n"
"v_mfma_f32_16x16x32_bf16 v[116:119], v[88:91], v[160:163], v[116:119]\n"
"v_cvt_pk_bf16_f32 v106, v106, v107\n"
"v_cvt_pk_bf16_f32 v107, v108, v109\n"
"v_mfma_f32_16x16x32_bf16 v[204:207], a[8:11], v[32:35], 0\n"
"s_waitcnt lgkmcnt(3)\n"
"buffer_load_dwordx4 v191, s[8:11], s32 offen lds\n"
"ds_read_b64 v[108:109], v181 offset:13568\n"
"v_mfma_f32_16x16x32_bf16 v[204:207], a[20:23], v[36:39], v[204:207]\n"
"s_add_u32 s32, s32, s33\n"
"ds_read_b64_tr_b16 a[250:251], v183 offset:40584\n"
"ds_read_b64_tr_b16 a[252:253], v182 offset:41352\n"
"v_mfma_f32_16x16x32_bf16 v[204:207], a[32:35], v[40:43], v[204:207]\n"
"ds_read_b64_tr_b16 a[254:255], v183 offset:41352\n"
"v_max_f32 v112, v112, v120\n"
"v_mfma_f32_16x16x32_bf16 v[204:207], a[44:47], v[44:47], v[204:207]\n"
"v_max_f32 v113, v113, v121\n"
"v_max_f32 v114, v114, v122\n"
"v_mfma_f32_16x16x32_bf16 v[204:207], a[56:59], v[48:51], v[204:207]\n"
"buffer_store_dwordx2 v[106:107], v193, s[28:31], s37 offen offset:1024 sc1\n"
"v_mfma_f32_16x16x32_bf16 v[204:207], a[68:71], v[52:55], v[204:207]\n"
"s_waitcnt lgkmcnt(5)\n"
"ds_read_b64 v[106:107], v180 offset:13568\n"
"ds_read_b64_tr_b16 v[120:121], v182 offset:13640\n"
"v_mfma_f32_16x16x32_bf16 v[124:127], v[124:127], v[148:151], 0\n"
"s_waitcnt lgkmcnt(6)\n"
"v_max_f32 v115, v115, v123\n"
"v_max3_f32 v170, v112, v113, v114\n"
"v_mfma_f32_16x16x32_bf16 v[124:127], v[92:95], v[162:165], v[124:127]\n"
"s_waitcnt lgkmcnt(5)\n"
"ds_read_b64_tr_b16 v[122:123], v183 offset:13640\n"
"v_cvt_pk_bf16_f32 v204, v204, v205\n"
"v_mfma_f32_16x16x32_bf16 v[128:131], v[128:131], v[148:151], 0\n"
"s_add_u32 m0, s34, 7168\n"
"v_cvt_pk_bf16_f32 v205, v206, v207\n"
"v_mov_b64 v[252:253], 0\n"
"v_mfma_f32_16x16x32_bf16 v[128:131], v[96:99], v[160:163], v[128:131]\n"
"s_add_u32 s37, s37, 1572864\n"
"buffer_load_dwordx4 v190, s[8:11], s0 offen lds\n"
"v_max_f32 v116, v116, v124\n"
"v_mfma_f32_16x16x32_bf16 v[136:139], v[136:139], v[148:151], 0\n"
"v_max_f32 v117, v117, v125\n"
"v_max_f32 v118, v118, v126\n"
"v_mfma_f32_16x16x32_bf16 v[136:139], v[100:103], v[162:165], v[136:139]\n"
"ds_read_b64_tr_b16 v[124:125], v182 offset:14408\n"
"v_max_f32 v119, v119, v127\n"
"v_mfma_f32_16x16x32_bf16 v[208:211], a[80:83], v[32:35], 0\n"
"s_waitcnt lgkmcnt(3)\n"
"ds_read_b64_tr_b16 v[126:127], v183 offset:14408\n"
"v_max3_f32 v170, v170, v115, v116\n"
"v_mfma_f32_16x16x32_bf16 v[208:211], a[92:95], v[36:39], v[208:211]\n"
"v_max3_f32 v170, v170, v117, v118\n"
"v_max_f32 v128, v128, v136\n"
"v_mfma_f32_16x16x32_bf16 v[208:211], a[104:107], v[40:43], v[208:211]\n"
"v_max_f32 v129, v129, v137\n"
"v_max_f32 v130, v130, v138\n"
"v_mfma_f32_16x16x32_bf16 v[208:211], a[116:119], v[44:47], v[208:211]\n"
"ds_read_b64_tr_b16 v[136:137], v182 offset:13576\n"
"v_max_f32 v131, v131, v139\n"
"v_mfma_f32_16x16x32_bf16 v[208:211], a[128:131], v[48:51], v[208:211]\n"
"ds_read_b64_tr_b16 v[138:139], v183 offset:13576\n"
"v_max3_f32 v170, v170, v119, v128\n"
"v_mfma_f32_16x16x32_bf16 v[208:211], a[140:143], v[52:55], v[208:211]\n"
"s_add_u32 m0, s34, 10240\n"
"v_max3_f32 v170, v170, v129, v130\n"
"v_mov_b64 v[254:255], 0\n"
"v_mfma_f32_16x16x32_bf16 v[132:135], v[132:135], v[148:151], 0\n"
"buffer_load_dwordx4 v191, s[8:11], s0 offen lds\n"
"ds_read_b64 v[80:81], v20 offset:272\n"
"v_mfma_f32_16x16x32_bf16 v[132:135], v[104:107], v[160:163], v[132:135]\n"
"s_add_u32 s0, s0, s33\n"
"v_cvt_pk_bf16_f32 v206, v208, v209\n"
"v_cvt_pk_bf16_f32 v207, v210, v211\n"
"v_mfma_f32_16x16x32_bf16 v[140:143], v[140:143], v[148:151], 0\n"
"ds_write_b128 v187, v[204:207] offset:64\n"
"v_mfma_f32_16x16x32_bf16 v[140:143], v[108:111], v[162:165], v[140:143]\n"
"v_mov_b64 v[98:99], 0\n"
"v_mov_b64 v[100:101], 0\n"
"v_mfma_f32_16x16x32_bf16 v[212:215], a[72:75], v[32:35], 0\n"
"v_mov_b64 v[106:107], 0\n"
"v_mov_b64 v[108:109], 0\n"
"v_mfma_f32_16x16x32_bf16 v[212:215], a[84:87], v[36:39], v[212:215]\n"
"ds_read_b64 v[86:87], v20 offset:336\n"
"ds_read_b64 v[104:105], v20 offset:1088\n"
"v_mfma_f32_16x16x32_bf16 v[212:215], a[96:99], v[40:43], v[212:215]\n"
"s_waitcnt lgkmcnt(3)\n"
"v_max_f32 v132, v132, v140\n"
"v_max_f32 v133, v133, v141\n"
"v_mfma_f32_16x16x32_bf16 v[212:215], a[108:111], v[44:47], v[212:215]\n"
"s_add_u32 m0, s34, 8192\n"
"ds_read_b64_tr_b16 v[140:141], v182 offset:14344\n"
"buffer_load_dwordx4 v190, s[8:11], s1 offen lds\n"
"v_mfma_f32_16x16x32_bf16 v[212:215], a[120:123], v[48:51], v[212:215]\n"
"v_max_f32 v134, v134, v142\n"
"v_max_f32 v135, v135, v143\n"
"v_mfma_f32_16x16x32_bf16 v[212:215], a[132:135], v[52:55], v[212:215]\n"
"ds_read_b64_tr_b16 v[142:143], v183 offset:14344\n"
"v_max3_f32 v170, v170, v131, v132\n"
"v_mfma_f32_16x16x32_bf16 v[208:211], a[76:79], v[32:35], 0\n"
"v_max3_f32 v170, v170, v133, v134\n"
"v_max_f32 v171, v170, v135\n"
"v_mfma_f32_16x16x32_bf16 v[208:211], a[88:91], v[36:39], v[208:211]\n"
"ds_read_b64 v[110:111], v20 offset:1152\n"
"v_max_f32 v170, v170, v135\n"
"v_mfma_f32_16x16x32_bf16 v[208:211], a[100:103], v[40:43], v[208:211]\n"
"s_waitcnt lgkmcnt(3)\n"
"v_cvt_pk_bf16_f32 v212, v212, v213\n"
"v_cvt_pk_bf16_f32 v213, v214, v215\n"
"v_mfma_f32_16x16x32_bf16 v[208:211], a[112:115], v[44:47], v[208:211]\n"
"v_permlane16_swap_b32 v170, v171\n"
"v_mfma_f32_16x16x32_bf16 v[208:211], a[124:127], v[48:51], v[208:211]\n"
"v_max_f32 v170, v170, v171\n"
"v_mov_b32 v171, v170\n"
"v_mfma_f32_16x16x32_bf16 v[208:211], a[136:139], v[52:55], v[208:211]\n"
"s_add_u32 m0, s34, 11264\n"
"v_permlane32_swap_b32 v170, v171\n"
"v_max_f32 v170, v170, v171\n"
"v_mul_f32 v170, 0xbe553b95, v170\n"
"v_pk_fma_f32 v[112:113], v[112:113], s[70:71], v[170:171] op_sel_hi:[1,0,0]\n"
"v_pk_fma_f32 v[114:115], v[114:115], s[70:71], v[170:171] op_sel_hi:[1,0,0]\n"
"v_pk_fma_f32 v[116:117], v[116:117], s[70:71], v[170:171] op_sel_hi:[1,0,0]\n"
"v_pk_fma_f32 v[118:119], v[118:119], s[70:71], v[170:171] op_sel_hi:[1,0,0]\n"
"v_pk_fma_f32 v[128:129], v[128:129], s[70:71], v[170:171] op_sel_hi:[1,0,0]\n"
"v_pk_fma_f32 v[130:131], v[130:131], s[70:71], v[170:171] op_sel_hi:[1,0,0]\n"
"v_pk_fma_f32 v[132:133], v[132:133], s[70:71], v[170:171] op_sel_hi:[1,0,0]\n"
"v_pk_fma_f32 v[134:135], v[134:135], s[70:71], v[170:171] op_sel_hi:[1,0,0]\n"
"v_mfma_f32_16x16x32_bf16 v[204:207], a[144:147], v[32:35], 0\n"
"s_waitcnt lgkmcnt(0)\n"
"buffer_load_dwordx4 v191, s[8:11], s1 offen lds\n"
"v_cvt_pk_bf16_f32 v214, v208, v209\n"
"v_mfma_f32_16x16x32_bf16 v[204:207], a[156:159], v[36:39], v[204:207]\n"
"s_add_u32 s1, s1, s33\n"
"v_cvt_pk_bf16_f32 v215, v210, v211\n"
"v_exp_f32 v112, v112\n"
"v_mfma_f32_16x16x32_bf16 v[204:207], a[168:171], v[40:43], v[204:207]\n"
"ds_write_b128 v187, v[212:215] offset:128\n"
"v_mfma_f32_16x16x32_bf16 v[204:207], a[180:183], v[44:47], v[204:207]\n"
"v_exp_f32 v113, v113\n"
"v_mfma_f32_16x16x32_bf16 v[204:207], a[192:195], v[48:51], v[204:207]\n"
"v_exp_f32 v114, v114\n"
"v_mfma_f32_16x16x32_bf16 v[204:207], a[204:207], v[52:55], v[204:207]\n"
"v_exp_f32 v115, v115\n"
"v_mfma_f32_16x16x32_bf16 v[208:211], a[148:151], v[32:35], 0\n"
"v_cvt_pk_bf16_f32 v82, v112, v114\n"
"v_cvt_pk_bf16_f32 v83, v113, v115\n"
"v_mfma_f32_16x16x32_bf16 v[208:211], a[160:163], v[36:39], v[208:211]\n"
"ds_read_b128 v[112:115], v178 offset:27008\n"
"v_exp_f32 v116, v116\n"
"v_mfma_f32_16x16x32_bf16 v[208:211], a[172:175], v[40:43], v[208:211]\n"
"v_cvt_pk_bf16_f32 v148, v204, v205\n"
"v_cvt_pk_bf16_f32 v149, v206, v207\n"
"v_mfma_f32_16x16x32_bf16 v[208:211], a[184:187], v[44:47], v[208:211]\n"
"s_waitcnt vmcnt(9) lgkmcnt(1)\n"
"v_exp_f32 v117, v117\n"
"v_mfma_f32_16x16x32_bf16 v[208:211], a[196:199], v[48:51], v[208:211]\n"
"s_barrier\n"
"v_mfma_f32_16x16x32_bf16 v[208:211], a[208:211], v[52:55], v[208:211]\n"
"s_add_u32 m0, s79, 142464\n"
"buffer_load_dword v201, off, s[28:31], s40 offset:128 sc1\n"
"ds_read_b64_tr_b16 a[216:217], v236\n"
"v_mfma_f32_16x16x32_bf16 v[212:215], a[152:155], v[32:35], 0\n"
"buffer_load_dwordx4 v192, s[16:19], s43 offen lds\n"
"ds_read_b128 v[32:35], v188\n"
"v_mfma_f32_16x16x32_bf16 v[212:215], a[164:167], v[36:39], v[212:215]\n"
"ds_read_b128 v[36:39], v189\n"
"v_cvt_pk_bf16_f32 v150, v208, v209\n"
"v_mfma_f32_16x16x32_bf16 v[212:215], a[176:179], v[40:43], v[212:215]\n"
".Lic1%=:\n"
"s_cbranch_execz .Lib0%=\n"
"ds_read_b128 v[40:43], v188 offset:1024\n"
"v_cvt_pk_bf16_f32 v151, v210, v211\n"
"v_mfma_f32_16x16x32_bf16 v[212:215], a[188:191], v[44:47], v[212:215]\n"
"ds_read_b128 v[44:47], v189 offset:1024\n"
"v_exp_f32 v118, v118\n"
"v_mfma_f32_16x16x32_bf16 v[212:215], a[200:203], v[48:51], v[212:215]\n"
"ds_read_b128 v[48:51], v188 offset:2048\n"
"v_cvt_pk_bf16_f32 v84, v116, v118\n"
"v_mfma_f32_16x16x32_bf16 v[212:215], a[212:215], v[52:55], v[212:215]\n"
"ds_read_b128 v[52:55], v189 offset:2048\n"
"v_exp_f32 v119, v119\n"
"v_exp_f32 v128, v128\n"
"v_cvt_pk_bf16_f32 v85, v117, v119\n"
"v_exp_f32 v129, v129\n"
"s_nop 0\n"
"v_smfmac_f32_16x16x64_bf16 v[252:255], v[82:85], a[240:247], v63\n"
"s_add_u32 m0, s79, 142464\n"
"s_nop 0\n"
"buffer_load_dwordx4 v192, s[16:19], s43 offen offset:1024 lds\n"
"v_smfmac_f32_16x16x64_bf16 v[98:101], v[82:85], v[120:127], v63\n"
"s_waitcnt lgkmcnt(5)\n"
"ds_read_b128 v[120:123], v179 offset:27008\n"
"v_smfmac_f32_16x16x64_bf16 v[106:109], v[82:85], v[136:143], v63\n"
"ds_read_b128 v[116:119], v180\n"
"v_mfma_f32_16x16x32_bf16 v[82:85], v[172:175], v[82:85], 0\n"
"ds_read_b128 v[124:127], v181\n"
"ds_read_b128 v[136:139], v180 offset:13504\n"
"v_mfma_f32_16x16x32_bf16 v[204:207], a[0:3], v[32:35], 0\n"
"s_waitcnt lgkmcnt(8)\n"
"ds_read_b128 v[140:143], v180 offset:27008\n"
"v_cvt_pk_bf16_f32 v162, v212, v213\n"
"v_mfma_f32_16x16x32_bf16 v[204:207], a[12:15], v[36:39], v[204:207]\n"
"s_waitcnt lgkmcnt(8)\n"
"v_exp_f32 v130, v130\n"
"v_mfma_f32_16x16x32_bf16 v[204:207], a[24:27], v[40:43], v[204:207]\n"
"s_waitcnt lgkmcnt(7)\n"
"v_exp_f32 v131, v131\n"
"v_mfma_f32_16x16x32_bf16 v[204:207], a[36:39], v[44:47], v[204:207]\n"
"s_waitcnt lgkmcnt(6)\n"
"v_cvt_pk_bf16_f32 v90, v128, v130\n"
"v_cvt_pk_bf16_f32 v91, v129, v131\n"
"v_mfma_f32_16x16x32_bf16 v[204:207], a[48:51], v[48:51], v[204:207]\n"
"s_waitcnt lgkmcnt(5)\n"
"ds_read_b128 v[128:131], v181 offset:13504\n"
"v_cvt_pk_bf16_f32 v163, v214, v215\n"
"v_mfma_f32_16x16x32_bf16 v[204:207], a[60:63], v[52:55], v[204:207]\n"
"s_add_u32 m0, s79, 142464\n"
"v_exp_f32 v132, v132\n"
"v_mfma_f32_16x16x32_bf16 v[208:211], a[4:7], v[32:35], 0\n"
"buffer_load_dwordx4 v192, s[16:19], s43 offen offset:2048 lds\n"
"ds_read_b64_tr_b16 a[240:241], v182 offset:27136\n"
"v_mfma_f32_16x16x32_bf16 v[208:211], a[16:19], v[36:39], v[208:211]\n"
"s_add_u32 s43, s79, 36864\n"
"v_exp_f32 v133, v133\n"
"v_mfma_f32_16x16x32_bf16 v[208:211], a[28:31], v[40:43], v[208:211]\n"
"v_exp_f32 v134, v134\n"
"v_mfma_f32_16x16x32_bf16 v[208:211], a[40:43], v[44:47], v[208:211]\n"
"ds_read_b64_tr_b16 a[242:243], v183 offset:27136\n"
"ds_read_b64_tr_b16 a[244:245], v182 offset:27904\n"
"v_mfma_f32_16x16x32_bf16 v[208:211], a[52:55], v[48:51], v[208:211]\n"
"v_exp_f32 v135, v135\n"
"v_mfma_f32_16x16x32_bf16 v[208:211], a[64:67], v[52:55], v[208:211]\n"
"ds_read_b64_tr_b16 a[246:247], v183 offset:27904\n"
"v_cvt_pk_bf16_f32 v92, v132, v134\n"
"v_mfma_f32_16x16x32_bf16 v[212:215], a[8:11], v[32:35], 0\n"
"v_cvt_pk_bf16_f32 v93, v133, v135\n"
"v_cvt_pk_bf16_f32 v204, v204, v205\n"
"v_mfma_f32_16x16x32_bf16 v[212:215], a[20:23], v[36:39], v[212:215]\n"
"ds_read_b128 v[132:135], v181 offset:27008\n"
"ds_read_b64_tr_b16 a[218:219], v236 offset:6144\n"
"v_smfmac_f32_16x16x64_bf16 v[252:255], v[90:93], v[224:231], v63\n"
"s_add_u32 m0, s34, 0\n"
"s_nop 0\n"
"buffer_load_dwordx4 v190, s[8:11], s32 offen lds\n"
"v_smfmac_f32_16x16x64_bf16 v[98:101], v[90:93], v[244:251], v63\n"
"ds_read_b64_tr_b16 v[216:217], v182 offset:54144\n"
"v_smfmac_f32_16x16x64_bf16 v[106:109], v[90:93], a[248:255], v63\n"
"ds_read_b64_tr_b16 v[218:219], v183 offset:54144\n"
"v_mfma_f32_16x16x32_bf16 v[90:93], v[172:175], v[90:93], v[82:85]\n"
"ds_read_b64_tr_b16 v[220:221], v182 offset:54912\n"
"v_cvt_pk_bf16_f32 v252, v252, v253\n"
"v_mfma_f32_16x16x32_bf16 v[212:215], a[32:35], v[40:43], v[212:215]\n"
"ds_read_b64 v[82:83], v178 offset:27072\n"
"v_cvt_pk_bf16_f32 v253, v254, v255\n"
"v_mfma_f32_16x16x32_bf16 v[212:215], a[44:47], v[44:47], v[212:215]\n"
"ds_read_b64 v[84:85], v179 offset:27072\n"
"buffer_load_dword v237, v238, s[8:11], s78 offen\n"
"v_mfma_f32_16x16x32_bf16 v[212:215], a[56:59], v[48:51], v[212:215]\n"
"s_add_u32 s78, s78, s33\n"
"v_rcp_f32 v171, v90\n"
"v_mfma_f32_16x16x32_bf16 v[212:215], a[68:71], v[52:55], v[212:215]\n"
"v_cvt_pk_bf16_f32 v171, v171, v171\n"
"s_add_u32 m0, s34, 3072\n"
"v_and_b32 v92, v171, v176\n"
"v_and_b32 v93, v171, v177\n"
"v_cvt_pk_bf16_f32 v98, v98, v99\n"
"v_cvt_pk_bf16_f32 v99, v100, v101\n"
"v_mfma_f32_16x16x16_bf16 v[252:255], v[252:253], v[92:93], 0\n"
"buffer_load_dwordx4 v191, s[8:11], s32 offen lds\n"
"ds_read_b64 v[90:91], v180 offset:64\n"
"v_mfma_f32_16x16x16_bf16 v[98:101], v[98:99], v[92:93], 0\n"
"s_add_u32 s32, s32, s33\n"
"v_cvt_pk_bf16_f32 v106, v106, v107\n"
"v_cvt_pk_bf16_f32 v107, v108, v109\n"
"v_cvt_pk_bf16_f32 v205, v206, v207\n"
"v_bfi_b32 v80, v12, v80, v26\n"
"v_mfma_f32_16x16x16_bf16 v[106:109], v[106:107], v[92:93], 0\n"
"ds_read_b64 v[92:93], v181 offset:64\n"
"v_cvt_pk_bf16_f32 v252, v252, v253\n"
"v_cvt_pk_bf16_f32 v253, v254, v255\n"
"v_cvt_pk_bf16_f32 v254, v98, v99\n"
"v_cvt_pk_bf16_f32 v255, v100, v101\n"
"v_bfi_b32 v81, v13, v81, v26\n"
"v_bfi_b32 v86, v14, v86, v26\n"
"v_bfi_b32 v87, v15, v87, v26\n"
"v_cvt_pk_bf16_f32 v106, v106, v107\n"
"v_cvt_pk_bf16_f32 v107, v108, v109\n"
"v_bfi_b32 v104, v16, v104, v26\n"
"v_bfi_b32 v105, v17, v105, v26\n"
"v_bfi_b32 v110, v18, v110, v26\n"
"v_cvt_pk_bf16_f32 v206, v208, v209\n"
"v_bfi_b32 v111, v19, v111, v26\n"
"v_cvt_pk_bf16_f32 v207, v210, v211\n"
"v_mfma_f32_16x16x32_bf16 v[208:211], a[80:83], v[32:35], 0\n"
"s_waitcnt lgkmcnt(1)\n"
"buffer_store_dwordx4 v[252:255], v192, s[28:31], s37 offen sc1\n"
"v_mfma_f32_16x16x32_bf16 v[112:115], v[112:115], v[152:155], 0\n"
"s_add_u32 m0, s34, 1024\n"
"ds_read_b64 v[98:99], v180 offset:13568\n"
"ds_read_b64 v[100:101], v181 offset:13568\n"
"v_mfma_f32_16x16x32_bf16 v[112:115], v[80:83], v[164:167], v[112:115]\n"
"s_waitcnt lgkmcnt(2)\n"
"ds_write_b128 v186, v[204:207] offset:6752\n"
"v_mfma_f32_16x16x32_bf16 v[120:123], v[120:123], v[152:155], 0\n"
"buffer_load_dwordx4 v190, s[8:11], s0 offen lds\n"
"v_cvt_pk_bf16_f32 v212, v212, v213\n"
"v_mfma_f32_16x16x32_bf16 v[120:123], v[84:87], v[166:169], v[120:123]\n"
"v_cvt_pk_bf16_f32 v213, v214, v215\n"
"v_mov_b64 v[252:253], 0\n"
"v_mfma_f32_16x16x32_bf16 v[116:119], v[116:119], v[152:155], 0\n"
"ds_read_b64 v[108:109], v181 offset:27072\n"
"ds_read_b64_tr_b16 v[222:223], v183 offset:54912\n"
"v_mfma_f32_16x16x32_bf16 v[116:119], v[88:91], v[164:167], v[116:119]\n"
"buffer_store_dwordx2 v[106:107], v193, s[28:31], s37 offen offset:1024 sc1\n"
"v_mfma_f32_16x16x32_bf16 v[124:127], v[124:127], v[152:155], 0\n"
"s_waitcnt lgkmcnt(4)\n"
"ds_read_b64 v[106:107], v180 offset:27072\n"
"ds_read_b64_tr_b16 v[72:73], v182 offset:54152\n"
"v_mfma_f32_16x16x32_bf16 v[124:127], v[92:95], v[166:169], v[124:127]\n"
"s_waitcnt lgkmcnt(5)\n"
"ds_read_b64_tr_b16 v[74:75], v183 offset:54152\n"
"v_max_f32 v112, v112, v120\n"
"v_mfma_f32_16x16x32_bf16 v[136:139], v[136:139], v[152:155], 0\n"
"s_add_u32 s37, s37, 1572864\n"
"ds_read_b64_tr_b16 v[76:77], v182 offset:54920\n"
"v_max_f32 v113, v113, v121\n"
"v_mfma_f32_16x16x32_bf16 v[136:139], v[96:99], v[164:167], v[136:139]\n"
"s_add_u32 m0, s34, 4096\n"
"ds_read_b64_tr_b16 v[120:121], v182 offset:27144\n"
"buffer_load_dwordx4 v191, s[8:11], s0 offen lds\n"
"v_mfma_f32_16x16x32_bf16 v[128:131], v[128:131], v[152:155], 0\n"
"s_add_u32 s0, s0, s33\n"
"ds_read_b64_tr_b16 v[78:79], v183 offset:54920\n"
"v_max_f32 v114, v114, v122\n"
"v_mfma_f32_16x16x32_bf16 v[128:131], v[100:103], v[166:169], v[128:131]\n"
"s_waitcnt lgkmcnt(7)\n"
"ds_read_b64_tr_b16 v[8:9], v182 offset:54088\n"
"v_max_f32 v115, v115, v123\n"
"v_mfma_f32_16x16x32_bf16 v[132:135], v[132:135], v[152:155], 0\n"
"ds_read_b64_tr_b16 v[122:123], v183 offset:27144\n"
"v_max3_f32 v170, v112, v113, v114\n"
"v_mfma_f32_16x16x32_bf16 v[132:135], v[108:111], v[166:169], v[132:135]\n"
"s_waitcnt lgkmcnt(7)\n"
"ds_read_b64_tr_b16 v[10:11], v183 offset:54088\n"
"v_max_f32 v116, v116, v124\n"
"v_mfma_f32_16x16x32_bf16 v[140:143], v[140:143], v[152:155], 0\n"
"ds_read_b64_tr_b16 v[12:13], v182 offset:54856\n"
"v_max_f32 v117, v117, v125\n"
"v_mfma_f32_16x16x32_bf16 v[140:143], v[104:107], v[164:167], v[140:143]\n"
"ds_read_b64_tr_b16 v[124:125], v182 offset:27912\n"
"v_max_f32 v118, v118, v126\n"
"v_mfma_f32_16x16x32_bf16 v[208:211], a[92:95], v[36:39], v[208:211]\n"
"ds_read_b64_tr_b16 v[14:15], v183 offset:54856\n"
"v_max_f32 v119, v119, v127\n"
"v_mfma_f32_16x16x32_bf16 v[208:211], a[104:107], v[40:43], v[208:211]\n"
"s_add_u32 m0, s34, 2048\n"
"ds_read_b64_tr_b16 v[126:127], v183 offset:27912\n"
"buffer_load_dwordx4 v190, s[8:11], s1 offen lds\n"
"v_mfma_f32_16x16x32_bf16 v[208:211], a[116:119], v[44:47], v[208:211]\n"
"ds_read_b64_tr_b16 a[220:221], v236 offset:8\n"
"v_max3_f32 v170, v170, v115, v116\n"
"v_mfma_f32_16x16x32_bf16 v[208:211], a[128:131], v[48:51], v[208:211]\n"
"v_max3_f32 v170, v170, v117, v118\n"
"v_max_f32 v136, v136, v128\n"
"v_mfma_f32_16x16x32_bf16 v[208:211], a[140:143], v[52:55], v[208:211]\n"
"ds_read_b64_tr_b16 a[222:223], v236 offset:6152\n"
"v_max_f32 v137, v137, v129\n"
"v_mfma_f32_16x16x32_bf16 v[204:207], a[72:75], v[32:35], 0\n"
"v_max_f32 v138, v138, v130\n"
"v_max_f32 v139, v139, v131\n"
"v_mfma_f32_16x16x32_bf16 v[204:207], a[84:87], v[36:39], v[204:207]\n"
"ds_read_b64_tr_b16 v[128:129], v182 offset:27080\n"
"v_max3_f32 v170, v170, v119, v136\n"
"v_mfma_f32_16x16x32_bf16 v[204:207], a[96:99], v[40:43], v[204:207]\n"
"ds_read_b64_tr_b16 v[130:131], v183 offset:27080\n"
"v_cvt_pk_bf16_f32 v214, v208, v209\n"
"v_mfma_f32_16x16x32_bf16 v[204:207], a[108:111], v[44:47], v[204:207]\n"
"ds_read_b64_tr_b16 a[224:225], v235\n"
"v_cvt_pk_bf16_f32 v215, v210, v211\n"
"v_mfma_f32_16x16x32_bf16 v[204:207], a[120:123], v[48:51], v[204:207]\n"
"s_add_u32 m0, s34, 5120\n"
"v_max3_f32 v170, v170, v137, v138\n"
"v_max_f32 v140, v140, v132\n"
"v_mfma_f32_16x16x32_bf16 v[204:207], a[132:135], v[52:55], v[204:207]\n"
"buffer_load_dwordx4 v191, s[8:11], s1 offen lds\n"
"ds_write_b128 v186, v[212:215] offset:6816\n"
"v_mfma_f32_16x16x32_bf16 v[208:211], a[76:79], v[32:35], 0\n"
"s_add_u32 s1, s1, s33\n"
"v_max_f32 v141, v141, v133\n"
"v_max_f32 v142, v142, v134\n"
"v_mfma_f32_16x16x32_bf16 v[208:211], a[88:91], v[36:39], v[208:211]\n"
"v_max_f32 v143, v143, v135\n"
"v_cvt_pk_bf16_f32 v204, v204, v205\n"
"v_mfma_f32_16x16x32_bf16 v[208:211], a[100:103], v[40:43], v[208:211]\n"
"ds_read_b64_tr_b16 v[132:133], v182 offset:27848\n"
"ds_read_b64_tr_b16 v[134:135], v183 offset:27848\n"
"v_mfma_f32_16x16x32_bf16 v[208:211], a[112:115], v[44:47], v[208:211]\n"
"ds_read_b64_tr_b16 a[226:227], v235 offset:6144\n"
"v_cvt_pk_bf16_f32 v205, v206, v207\n"
"v_mfma_f32_16x16x32_bf16 v[208:211], a[124:127], v[48:51], v[208:211]\n"
"v_max3_f32 v170, v170, v139, v140\n"
"v_max3_f32 v170, v170, v141, v142\n"
"v_mfma_f32_16x16x32_bf16 v[208:211], a[136:139], v[52:55], v[208:211]\n"
"v_mov_b64 v[254:255], 0\n"
"v_mov_b64 v[98:99], 0\n"
"v_mfma_f32_16x16x32_bf16 v[212:215], a[144:147], v[32:35], 0\n"
"v_mov_b64 v[100:101], 0\n"
"v_mov_b64 v[106:107], 0\n"
"v_mfma_f32_16x16x32_bf16 v[212:215], a[156:159], v[36:39], v[212:215]\n"
"v_mov_b64 v[108:109], 0\n"
"v_max_f32 v171, v170, v143\n"
"v_mfma_f32_16x16x32_bf16 v[212:215], a[168:171], v[40:43], v[212:215]\n"
"v_cvt_pk_bf16_f32 v206, v208, v209\n"
"v_cvt_pk_bf16_f32 v207, v210, v211\n"
"v_mfma_f32_16x16x32_bf16 v[212:215], a[180:183], v[44:47], v[212:215]\n"
"ds_write_b128 v186, v[204:207] offset:6880\n"
"v_mfma_f32_16x16x32_bf16 v[212:215], a[192:195], v[48:51], v[212:215]\n"
"v_max_f32 v170, v170, v143\n"
"s_mov_b64 exec, s[44:45]\n"
"v_add_u32 v179, 0xc0, v179\n"
"s_mov_b64 exec, -1\n"
"v_mfma_f32_16x16x32_bf16 v[212:215], a[204:207], v[52:55], v[212:215]\n"
"v_permlane16_swap_b32 v170, v171\n"
"v_mfma_f32_16x16x32_bf16 v[208:211], a[148:151], v[32:35], 0\n"
"v_max_f32 v170, v170, v171\n"
"v_mov_b32 v171, v170\n"
"v_mfma_f32_16x16x32_bf16 v[208:211], a[160:163], v[36:39], v[208:211]\n"
"s_waitcnt vmcnt(12)\n"
"v_readfirstlane_b32 s6, v201\n"
"v_permlane32_swap_b32 v170, v171\n"
"v_max_f32 v170, v170, v171\n"
"v_mul_f32 v170, 0xbe553b95, v170\n"
"v_pk_fma_f32 v[112:113], v[112:113], s[70:71], v[170:171] op_sel_hi:[1,0,0]\n"
"v_pk_fma_f32 v[114:115], v[114:115], s[70:71], v[170:171] op_sel_hi:[1,0,0]\n"
"v_pk_fma_f32 v[116:117], v[116:117], s[70:71], v[170:171] op_sel_hi:[1,0,0]\n"
"v_pk_fma_f32 v[118:119], v[118:119], s[70:71], v[170:171] op_sel_hi:[1,0,0]\n"
"v_pk_fma_f32 v[136:137], v[136:137], s[70:71], v[170:171] op_sel_hi:[1,0,0]\n"
"v_pk_fma_f32 v[138:139], v[138:139], s[70:71], v[170:171] op_sel_hi:[1,0,0]\n"
"v_pk_fma_f32 v[140:141], v[140:141], s[70:71], v[170:171] op_sel_hi:[1,0,0]\n"
"v_pk_fma_f32 v[142:143], v[142:143], s[70:71], v[170:171] op_sel_hi:[1,0,0]\n"
"v_mfma_f32_16x16x32_bf16 v[208:211], a[172:175], v[40:43], v[208:211]\n"
"s_waitcnt vmcnt(9) lgkmcnt(0)\n"
"v_exp_f32 v112, v112\n"
"v_mfma_f32_16x16x32_bf16 v[208:211], a[184:187], v[44:47], v[208:211]\n"
"s_and_b32 s6, s6, 0xffffff00\n"
"v_exp_f32 v113, v113\n"
"v_mfma_f32_16x16x32_bf16 v[208:211], a[196:199], v[48:51], v[208:211]\n"
"s_barrier\n"
"v_mfma_f32_16x16x32_bf16 v[208:211], a[208:211], v[52:55], v[208:211]\n"
"s_add_u32 m0, s79, 130176\n"
"v_exp_f32 v114, v114\n"
"v_mfma_f32_16x16x32_bf16 v[204:207], a[152:155], v[32:35], 0\n"
"buffer_load_dwordx4 v192, s[16:19], s43 offen lds\n"
"ds_read_b128 v[32:35], v188 offset:6144\n"
"v_mfma_f32_16x16x32_bf16 v[204:207], a[164:167], v[36:39], v[204:207]\n"
"ds_read_b128 v[36:39], v189 offset:6144\n"
"v_cvt_pk_bf16_f32 v152, v212, v213\n"
"v_mfma_f32_16x16x32_bf16 v[204:207], a[176:179], v[40:43], v[204:207]\n"
"ds_read_b128 v[40:43], v188 offset:7168\n"
"v_cvt_pk_bf16_f32 v153, v214, v215\n"
"v_mfma_f32_16x16x32_bf16 v[204:207], a[188:191], v[44:47], v[204:207]\n"
"ds_read_b128 v[44:47], v189 offset:7168\n"
"v_cvt_pk_bf16_f32 v82, v112, v114\n"
"v_mfma_f32_16x16x32_bf16 v[204:207], a[200:203], v[48:51], v[204:207]\n"
"ds_read_b128 v[48:51], v188 offset:8192\n"
"v_cvt_pk_bf16_f32 v154, v208, v209\n"
"v_mfma_f32_16x16x32_bf16 v[204:207], a[212:215], v[52:55], v[204:207]\n"
"ds_read_b128 v[52:55], v189 offset:8192\n"
"v_exp_f32 v115, v115\n"
"v_exp_f32 v116, v116\n"
"v_cvt_pk_bf16_f32 v83, v113, v115\n"
"v_exp_f32 v117, v117\n"
"v_exp_f32 v118, v118\n"
"v_exp_f32 v119, v119\n"
"v_cvt_pk_bf16_f32 v84, v116, v118\n"
"v_cvt_pk_bf16_f32 v85, v117, v119\n"
"v_cvt_pk_bf16_f32 v155, v210, v211\n"
"v_exp_f32 v136, v136\n"
"v_smfmac_f32_16x16x64_bf16 v[252:255], v[82:85], a[240:247], v63\n"
"s_waitcnt lgkmcnt(4)\n"
"ds_read_b128 v[112:115], v180\n"
"v_smfmac_f32_16x16x64_bf16 v[98:101], v[82:85], v[120:127], v63\n"
"s_waitcnt lgkmcnt(3)\n"
"ds_read_b128 v[120:123], v181\n"
"v_smfmac_f32_16x16x64_bf16 v[106:109], v[82:85], v[128:135], v63\n"
"s_add_u32 m0, s79, 130176\n"
"s_nop 0\n"
"buffer_load_dwordx4 v192, s[16:19], s43 offen offset:1024 lds\n"
"v_mfma_f32_16x16x32_bf16 v[82:85], v[172:175], v[82:85], 0\n"
"ds_read_b128 v[116:119], v180 offset:13504\n"
"ds_read_b128 v[124:127], v181 offset:13504\n"
"v_mfma_f32_16x16x32_bf16 v[212:215], a[0:3], v[32:35], 0\n"
"ds_read_b128 v[128:131], v180 offset:27008\n"
"v_cvt_pk_bf16_f32 v166, v204, v205\n"
"v_mfma_f32_16x16x32_bf16 v[212:215], a[12:15], v[36:39], v[212:215]\n"
"ds_read_b128 v[132:135], v178\n"
"v_cvt_pk_bf16_f32 v167, v206, v207\n"
"v_mfma_f32_16x16x32_bf16 v[212:215], a[24:27], v[40:43], v[212:215]\n"
"ds_read_b64_tr_b16 a[228:229], v236 offset:12288\n"
"v_exp_f32 v137, v137\n"
"v_mfma_f32_16x16x32_bf16 v[212:215], a[36:39], v[44:47], v[212:215]\n"
"s_waitcnt lgkmcnt(8)\n"
"v_exp_f32 v138, v138\n"
"v_mfma_f32_16x16x32_bf16 v[212:215], a[48:51], v[48:51], v[212:215]\n"
"s_waitcnt lgkmcnt(7)\n"
"v_exp_f32 v139, v139\n"
"v_mfma_f32_16x16x32_bf16 v[212:215], a[60:63], v[52:55], v[212:215]\n"
"v_cvt_pk_bf16_f32 v90, v136, v138\n"
"v_cvt_pk_bf16_f32 v91, v137, v139\n"
"v_mfma_f32_16x16x32_bf16 v[208:211], a[4:7], v[32:35], 0\n"
"s_add_u32 m0, s79, 130176\n"
"ds_read_b128 v[136:139], v181 offset:27008\n"
"buffer_load_dwordx4 v192, s[16:19], s43 offen offset:2048 lds\n"
"v_mfma_f32_16x16x32_bf16 v[208:211], a[16:19], v[36:39], v[208:211]\n"
"s_add_u32 s43, s79, 55296\n"
"v_exp_f32 v140, v140\n"
"v_mfma_f32_16x16x32_bf16 v[208:211], a[28:31], v[40:43], v[208:211]\n"
"v_exp_f32 v141, v141\n"
"v_mfma_f32_16x16x32_bf16 v[208:211], a[40:43], v[44:47], v[208:211]\n"
"v_exp_f32 v142, v142\n"
"v_mfma_f32_16x16x32_bf16 v[208:211], a[52:55], v[48:51], v[208:211]\n"
"v_exp_f32 v143, v143\n"
"v_mfma_f32_16x16x32_bf16 v[208:211], a[64:67], v[52:55], v[208:211]\n"
"v_cvt_pk_bf16_f32 v92, v140, v142\n"
"v_cvt_pk_bf16_f32 v93, v141, v143\n"
"v_mfma_f32_16x16x32_bf16 v[204:207], a[8:11], v[32:35], 0\n"
"ds_read_b128 v[140:143], v179\n"
"ds_read_b64_tr_b16 v[24:25], v184 offset:72\n"
"v_smfmac_f32_16x16x64_bf16 v[252:255], v[90:93], v[216:223], v63\n"
"ds_read_b64_tr_b16 a[230:231], v236 offset:18432\n"
"v_smfmac_f32_16x16x64_bf16 v[98:101], v[90:93], v[72:79], v63\n"
"s_add_u32 m0, s34, 6144\n"
"s_nop 0\n"
"buffer_load_dwordx4 v190, s[8:11], s32 offen lds\n"
"v_smfmac_f32_16x16x64_bf16 v[106:109], v[90:93], v[8:15], v63\n"
"ds_read_b64_tr_b16 v[26:27], v185 offset:72\n"
"v_mfma_f32_16x16x32_bf16 v[90:93], v[172:175], v[90:93], v[82:85]\n"
"ds_read_b64_tr_b16 v[28:29], v184 offset:840\n"
"v_cvt_pk_bf16_f32 v252, v252, v253\n"
"v_mfma_f32_16x16x32_bf16 v[204:207], a[20:23], v[36:39], v[204:207]\n"
"ds_read_b64 v[82:83], v180 offset:64\n"
"v_cvt_pk_bf16_f32 v253, v254, v255\n"
"v_mfma_f32_16x16x32_bf16 v[204:207], a[32:35], v[40:43], v[204:207]\n"
"ds_read_b64 v[84:85], v181 offset:64\n"
"v_cvt_pk_bf16_f32 v98, v98, v99\n"
"v_mfma_f32_16x16x32_bf16 v[204:207], a[44:47], v[44:47], v[204:207]\n"
"v_rcp_f32 v171, v90\n"
"v_mfma_f32_16x16x32_bf16 v[204:207], a[56:59], v[48:51], v[204:207]\n"
"ds_read_b64 v[90:91], v180 offset:13568\n"
"v_cvt_pk_bf16_f32 v171, v171, v171\n"
"v_mfma_f32_16x16x32_bf16 v[204:207], a[68:71], v[52:55], v[204:207]\n"
"v_and_b32 v92, v171, v176\n"
"s_add_u32 m0, s34, 9216\n"
"v_and_b32 v93, v171, v177\n"
"v_cvt_pk_bf16_f32 v99, v100, v101\n"
"v_cvt_pk_bf16_f32 v106, v106, v107\n"
"v_mfma_f32_16x16x16_bf16 v[252:255], v[252:253], v[92:93], 0\n"
"buffer_load_dwordx4 v191, s[8:11], s32 offen lds\n"
"ds_read_b64_tr_b16 v[30:31], v185 offset:840\n"
"v_mfma_f32_16x16x16_bf16 v[98:101], v[98:99], v[92:93], 0\n"
"s_add_u32 s32, s32, s33\n"
"v_cvt_pk_bf16_f32 v107, v108, v109\n"
"v_cvt_pk_bf16_f32 v212, v212, v213\n"
"v_cvt_pk_bf16_f32 v213, v214, v215\n"
"v_mfma_f32_16x16x16_bf16 v[106:109], v[106:107], v[92:93], 0\n"
"s_waitcnt lgkmcnt(3)\n"
"ds_read_b64 v[92:93], v181 offset:13568\n"
"v_cvt_pk_bf16_f32 v252, v252, v253\n"
"v_mfma_f32_16x16x32_bf16 v[112:115], v[112:115], v[144:147], 0\n"
"s_waitcnt lgkmcnt(3)\n"
"buffer_load_dword v237, v238, s[8:11], s78 offen\n"
"v_cvt_pk_bf16_f32 v253, v254, v255\n"
"v_mfma_f32_16x16x32_bf16 v[112:115], v[80:83], v[156:159], v[112:115]\n"
"s_waitcnt lgkmcnt(2)\n"
"v_cvt_pk_bf16_f32 v254, v98, v99\n"
"v_cvt_pk_bf16_f32 v255, v100, v101\n"
"v_mfma_f32_16x16x32_bf16 v[120:123], v[120:123], v[144:147], 0\n"
"s_add_u32 s78, s78, s33\n"
"ds_read_b64 v[98:99], v180 offset:27072\n"
"ds_read_b64 v[100:101], v181 offset:27072\n"
"v_mfma_f32_16x16x32_bf16 v[120:123], v[84:87], v[158:161], v[120:123]\n"
"v_cvt_pk_bf16_f32 v106, v106, v107\n"
"v_cvt_pk_bf16_f32 v107, v108, v109\n"
"v_mfma_f32_16x16x32_bf16 v[116:119], v[116:119], v[144:147], 0\n"
"s_add_u32 m0, s34, 7168\n"
"buffer_store_dwordx4 v[252:255], v192, s[28:31], s37 offen sc1\n"
"v_mfma_f32_16x16x32_bf16 v[116:119], v[88:91], v[156:159], v[116:119]\n"
"s_waitcnt lgkmcnt(2)\n"
"ds_read_b64 v[108:109], v179 offset:64\n"
"v_cvt_pk_bf16_f32 v214, v208, v209\n"
"v_mfma_f32_16x16x32_bf16 v[124:127], v[124:127], v[144:147], 0\n"
"v_cvt_pk_bf16_f32 v215, v210, v211\n"
"v_cvt_pk_bf16_f32 v204, v204, v205\n"
"v_mfma_f32_16x16x32_bf16 v[124:127], v[92:95], v[158:161], v[124:127]\n"
"buffer_load_dwordx4 v190, s[8:11], s0 offen lds\n"
"ds_write_b128 v186, v[212:215] offset:20256\n"
"v_mfma_f32_16x16x32_bf16 v[208:211], a[80:83], v[32:35], 0\n"
"s_waitcnt lgkmcnt(2)\n"
"v_cvt_pk_bf16_f32 v205, v206, v207\n"
"v_max_f32 v112, v112, v120\n"
"v_mfma_f32_16x16x32_bf16 v[208:211], a[92:95], v[36:39], v[208:211]\n"
"v_max_f32 v113, v113, v121\n"
"v_max_f32 v114, v114, v122\n"
"v_mfma_f32_16x16x32_bf16 v[208:211], a[104:107], v[40:43], v[208:211]\n"
"s_waitcnt vmcnt(11)\n"
"v_or_b32 v201, 2, s6\n"
"v_max_f32 v115, v115, v123\n"
"v_mfma_f32_16x16x32_bf16 v[208:211], a[116:119], v[44:47], v[208:211]\n"
"s_waitcnt lgkmcnt(1)\n"
"s_mov_b64 exec, 1\n"
"buffer_store_dword v201, off, s[28:31], s40 sc1\n"
"s_mov_b64 exec, -1\n"
"v_mfma_f32_16x16x32_bf16 v[208:211], a[128:131], v[48:51], v[208:211]\n"
"buffer_store_dwordx2 v[106:107], v193, s[28:31], s37 offen offset:1024 sc1\n"
"v_mfma_f32_16x16x32_bf16 v[208:211], a[140:143], v[52:55], v[208:211]\n"
"s_add_u32 s37, s37, 1572864\n"
"ds_read_b64 v[106:107], v178 offset:64\n"
"ds_read_b64_tr_b16 a[232:233], v236 offset:12296\n"
"v_mfma_f32_16x16x32_bf16 v[128:131], v[128:131], v[144:147], 0\n"
"v_max3_f32 v170, v112, v113, v114\n"
"v_max_f32 v116, v116, v124\n"
"v_mfma_f32_16x16x32_bf16 v[128:131], v[96:99], v[156:159], v[128:131]\n"
"s_add_u32 m0, s34, 10240\n"
"ds_read_b64_tr_b16 a[234:235], v236 offset:18440\n"
"v_cvt_pk_bf16_f32 v206, v208, v209\n"
"v_mfma_f32_16x16x32_bf16 v[136:139], v[136:139], v[144:147], 0\n"
"buffer_load_dwordx4 v191, s[8:11], s0 offen lds\n"
"v_cvt_pk_bf16_f32 v207, v210, v211\n"
"v_mfma_f32_16x16x32_bf16 v[136:139], v[100:103], v[158:161], v[136:139]\n"
"s_add_u32 s0, s0, s33\n"
"ds_write_b128 v186, v[204:207] offset:20320\n"
"v_mfma_f32_16x16x32_bf16 v[140:143], v[140:143], v[144:147], 0\n"
"v_max_f32 v117, v117, v125\n"
"v_max_f32 v118, v118, v126\n"
"v_mfma_f32_16x16x32_bf16 v[140:143], v[108:111], v[158:161], v[140:143]\n"
"s_waitcnt lgkmcnt(3)\n"
"v_max_f32 v119, v119, v127\n"
"v_max3_f32 v170, v170, v115, v116\n"
"v_mfma_f32_16x16x32_bf16 v[132:135], v[132:135], v[144:147], 0\n"
"ds_read_b64_tr_b16 a[236:237], v235 offset:12288\n"
"v_max3_f32 v170, v170, v117, v118\n"
"v_mfma_f32_16x16x32_bf16 v[132:135], v[104:107], v[156:159], v[132:135]\n"
"ds_read_b64_tr_b16 a[238:239], v235 offset:18432\n"
"v_max_f32 v128, v128, v136\n"
"v_mfma_f32_16x16x32_bf16 v[212:215], a[72:75], v[32:35], 0\n"
"v_max_f32 v129, v129, v137\n"
"v_max_f32 v130, v130, v138\n"
"v_mfma_f32_16x16x32_bf16 v[212:215], a[84:87], v[36:39], v[212:215]\n"
"s_add_u32 m0, s34, 8192\n"
"v_max_f32 v131, v131, v139\n"
"v_max3_f32 v170, v170, v119, v128\n"
"v_mfma_f32_16x16x32_bf16 v[212:215], a[96:99], v[40:43], v[212:215]\n"
"buffer_load_dwordx4 v190, s[8:11], s1 offen lds\n"
"v_max3_f32 v170, v170, v129, v130\n"
"v_mfma_f32_16x16x32_bf16 v[212:215], a[108:111], v[44:47], v[212:215]\n"
"v_max_f32 v132, v132, v140\n"
"v_max_f32 v133, v133, v141\n"
"v_mfma_f32_16x16x32_bf16 v[212:215], a[120:123], v[48:51], v[212:215]\n"
"v_max_f32 v134, v134, v142\n"
"v_max_f32 v135, v135, v143\n"
"v_mfma_f32_16x16x32_bf16 v[212:215], a[132:135], v[52:55], v[212:215]\n"
"v_max3_f32 v170, v170, v131, v132\n"
"v_max3_f32 v170, v170, v133, v134\n"
"v_mfma_f32_16x16x32_bf16 v[208:211], a[76:79], v[32:35], 0\n"
"v_mov_b64 v[252:253], 0\n"
"v_mov_b64 v[254:255], 0\n"
"v_mfma_f32_16x16x32_bf16 v[208:211], a[88:91], v[36:39], v[208:211]\n"
"v_mov_b64 v[98:99], 0\n"
"v_mov_b64 v[100:101], 0\n"
"v_mfma_f32_16x16x32_bf16 v[208:211], a[100:103], v[40:43], v[208:211]\n"
"v_cvt_pk_bf16_f32 v212, v212, v213\n"
"v_cvt_pk_bf16_f32 v213, v214, v215\n"
"v_mfma_f32_16x16x32_bf16 v[208:211], a[112:115], v[44:47], v[208:211]\n"
"ds_read_b128 v[120:123], v180 offset:13504\n"
"v_mov_b64 v[106:107], 0\n"
"v_mfma_f32_16x16x32_bf16 v[208:211], a[124:127], v[48:51], v[208:211]\n"
"s_add_u32 m0, s34, 11264\n"
"v_mov_b64 v[108:109], 0\n"
"v_max_f32 v171, v170, v135\n"
"v_mfma_f32_16x16x32_bf16 v[208:211], a[136:139], v[52:55], v[208:211]\n"
"buffer_load_dwordx4 v191, s[8:11], s1 offen lds\n"
"ds_read_b128 v[136:139], v181 offset:13504\n"
"v_mfma_f32_16x16x32_bf16 v[204:207], a[144:147], v[32:35], 0\n"
"s_add_u32 s1, s1, s33\n"
"v_max_f32 v170, v170, v135\n"
"s_mov_b64 exec, s[46:47]\n"
"v_add_u32 v183, 0xc0, v183\n"
"s_mov_b64 exec, -1\n"
"v_mfma_f32_16x16x32_bf16 v[204:207], a[156:159], v[36:39], v[204:207]\n"
"v_permlane16_swap_b32 v170, v171\n"
"v_mfma_f32_16x16x32_bf16 v[204:207], a[168:171], v[40:43], v[204:207]\n"
"v_max_f32 v170, v170, v171\n"
"v_mov_b32 v171, v170\n"
"v_mfma_f32_16x16x32_bf16 v[204:207], a[180:183], v[44:47], v[204:207]\n"
"s_nop 0\n"
"v_permlane32_swap_b32 v170, v171\n"
"v_max_f32 v170, v170, v171\n"
"v_mul_f32 v170, 0xbe553b95, v170\n"
"v_pk_fma_f32 v[112:113], v[112:113], s[70:71], v[170:171] op_sel_hi:[1,0,0]\n"
"v_pk_fma_f32 v[114:115], v[114:115], s[70:71], v[170:171] op_sel_hi:[1,0,0]\n"
"v_pk_fma_f32 v[116:117], v[116:117], s[70:71], v[170:171] op_sel_hi:[1,0,0]\n"
"v_pk_fma_f32 v[118:119], v[118:119], s[70:71], v[170:171] op_sel_hi:[1,0,0]\n"
"v_pk_fma_f32 v[128:129], v[128:129], s[70:71], v[170:171] op_sel_hi:[1,0,0]\n"
"v_pk_fma_f32 v[130:131], v[130:131], s[70:71], v[170:171] op_sel_hi:[1,0,0]\n"
"v_pk_fma_f32 v[132:133], v[132:133], s[70:71], v[170:171] op_sel_hi:[1,0,0]\n"
"v_pk_fma_f32 v[134:135], v[134:135], s[70:71], v[170:171] op_sel_hi:[1,0,0]\n"
"v_mfma_f32_16x16x32_bf16 v[204:207], a[192:195], v[48:51], v[204:207]\n"
"ds_read_b128 v[124:127], v180 offset:27008\n"
"ds_read_b128 v[140:143], v181 offset:27008\n"
"v_mfma_f32_16x16x32_bf16 v[204:207], a[204:207], v[52:55], v[204:207]\n"
"v_cvt_pk_bf16_f32 v214, v208, v209\n"
"v_cvt_pk_bf16_f32 v215, v210, v211\n"
"v_mfma_f32_16x16x32_bf16 v[208:211], a[148:151], v[32:35], 0\n"
"ds_write_b128 v186, v[212:215] offset:20384\n"
"v_mfma_f32_16x16x32_bf16 v[208:211], a[160:163], v[36:39], v[208:211]\n"
"v_exp_f32 v112, v112\n"
"v_mfma_f32_16x16x32_bf16 v[208:211], a[172:175], v[40:43], v[208:211]\n"
"v_exp_f32 v113, v113\n"
"v_mfma_f32_16x16x32_bf16 v[208:211], a[184:187], v[44:47], v[208:211]\n"
"v_exp_f32 v114, v114\n"
"v_mfma_f32_16x16x32_bf16 v[208:211], a[196:199], v[48:51], v[208:211]\n"
"v_exp_f32 v115, v115\n"
"v_mfma_f32_16x16x32_bf16 v[208:211], a[208:211], v[52:55], v[208:211]\n"
"v_cvt_pk_bf16_f32 v82, v112, v114\n"
"v_cvt_pk_bf16_f32 v83, v113, v115\n"
"v_mfma_f32_16x16x32_bf16 v[212:215], a[152:155], v[32:35], 0\n"
"ds_read_b128 v[32:35], v188\n"
"ds_read_b128 v[112:115], v178\n"
"v_mfma_f32_16x16x32_bf16 v[212:215], a[164:167], v[36:39], v[212:215]\n"
"s_waitcnt vmcnt(10) lgkmcnt(2)\n"
"ds_read_b128 v[36:39], v189\n"
"v_cvt_pk_bf16_f32 v144, v204, v205\n"
"v_mfma_f32_16x16x32_bf16 v[212:215], a[176:179], v[40:43], v[212:215]\n"
"s_barrier\n"
"v_mfma_f32_16x16x32_bf16 v[212:215], a[188:191], v[44:47], v[212:215]\n"
"s_add_u32 m0, s79, 142464\n"
"ds_read_b128 v[40:43], v188 offset:1024\n"
"buffer_load_dwordx4 v192, s[16:19], s43 offen lds\n"
"v_mfma_f32_16x16x32_bf16 v[212:215], a[200:203], v[48:51], v[212:215]\n"
"ds_read_b128 v[44:47], v189 offset:1024\n"
"ds_read_b128 v[48:51], v188 offset:2048\n"
"v_mfma_f32_16x16x32_bf16 v[212:215], a[212:215], v[52:55], v[212:215]\n"
"ds_read_b128 v[52:55], v189 offset:2048\n"
"v_exp_f32 v116, v116\n"
"v_exp_f32 v117, v117\n"
"v_exp_f32 v118, v118\n"
"v_exp_f32 v119, v119\n"
"v_cvt_pk_bf16_f32 v84, v116, v118\n"
"v_cvt_pk_bf16_f32 v85, v117, v119\n"
"v_cvt_pk_bf16_f32 v145, v206, v207\n"
"v_exp_f32 v128, v128\n"
"v_smfmac_f32_16x16x64_bf16 v[252:255], v[82:85], v[224:231], v63\n"
"s_waitcnt lgkmcnt(6)\n"
"ds_read_b64_tr_b16 v[224:225], v184 offset:128\n"
"v_smfmac_f32_16x16x64_bf16 v[98:101], v[82:85], v[244:251], v63\n"
"ds_read_b64_tr_b16 v[226:227], v185 offset:128\n"
"v_smfmac_f32_16x16x64_bf16 v[106:109], v[82:85], a[248:255], v63\n"
"s_waitcnt lgkmcnt(4)\n"
"ds_read_b64_tr_b16 v[228:229], v184 offset:896\n"
"v_mfma_f32_16x16x32_bf16 v[82:85], v[172:175], v[82:85], 0\n"
".Lic2%=:\n"
"s_cbranch_execz .Lib0%=\n"
"s_add_u32 m0, s79, 142464\n"
"ds_read_b64_tr_b16 v[230:231], v185 offset:896\n"
"buffer_load_dwordx4 v192, s[16:19], s43 offen offset:1024 lds\n"
"v_mfma_f32_16x16x32_bf16 v[204:207], a[0:3], v[32:35], 0\n"
"ds_read_b64_tr_b16 v[244:245], v184 offset:136\n"
"ds_read_b64_tr_b16 v[246:247], v185 offset:136\n"
"v_mfma_f32_16x16x32_bf16 v[204:207], a[12:15], v[36:39], v[204:207]\n"
"ds_read_b64_tr_b16 v[248:249], v184 offset:904\n"
"v_cvt_pk_bf16_f32 v146, v208, v209\n"
"v_mfma_f32_16x16x32_bf16 v[204:207], a[24:27], v[40:43], v[204:207]\n"
"ds_read_b64_tr_b16 v[250:251], v185 offset:904\n"
"v_cvt_pk_bf16_f32 v147, v210, v211\n"
"v_mfma_f32_16x16x32_bf16 v[204:207], a[36:39], v[44:47], v[204:207]\n"
"s_waitcnt lgkmcnt(9)\n"
"ds_read_b128 v[116:119], v178 offset:13504\n"
"v_cvt_pk_bf16_f32 v158, v212, v213\n"
"v_mfma_f32_16x16x32_bf16 v[204:207], a[48:51], v[48:51], v[204:207]\n"
"s_waitcnt lgkmcnt(9)\n"
"v_exp_f32 v129, v129\n"
"v_mfma_f32_16x16x32_bf16 v[204:207], a[60:63], v[52:55], v[204:207]\n"
"v_exp_f32 v130, v130\n"
"v_mfma_f32_16x16x32_bf16 v[208:211], a[4:7], v[32:35], 0\n"
"v_exp_f32 v131, v131\n"
"v_mfma_f32_16x16x32_bf16 v[208:211], a[16:19], v[36:39], v[208:211]\n"
"s_add_u32 m0, s79, 142464\n"
"ds_read_b64_tr_b16 a[240:241], v236\n"
"buffer_load_dwordx4 v192, s[16:19], s43 offen offset:2048 lds\n"
"v_mfma_f32_16x16x32_bf16 v[208:211], a[28:31], v[40:43], v[208:211]\n"
"s_add_u32 s43, s80, 12288\n"
"v_cvt_pk_bf16_f32 v90, v128, v130\n"
"v_cvt_pk_bf16_f32 v91, v129, v131\n"
"v_mfma_f32_16x16x32_bf16 v[208:211], a[40:43], v[44:47], v[208:211]\n"
"ds_read_b128 v[128:131], v179\n"
"v_cvt_pk_bf16_f32 v159, v214, v215\n"
"v_mfma_f32_16x16x32_bf16 v[208:211], a[52:55], v[48:51], v[208:211]\n"
"v_exp_f32 v132, v132\n"
"v_mfma_f32_16x16x32_bf16 v[208:211], a[64:67], v[52:55], v[208:211]\n"
"v_exp_f32 v133, v133\n"
"v_mfma_f32_16x16x32_bf16 v[212:215], a[8:11], v[32:35], 0\n"
"v_exp_f32 v134, v134\n"
"v_mfma_f32_16x16x32_bf16 v[212:215], a[20:23], v[36:39], v[212:215]\n"
"v_exp_f32 v135, v135\n"
"v_mfma_f32_16x16x32_bf16 v[212:215], a[32:35], v[40:43], v[212:215]\n"
"v_cvt_pk_bf16_f32 v92, v132, v134\n"
"v_cvt_pk_bf16_f32 v93, v133, v135\n"
"v_mfma_f32_16x16x32_bf16 v[212:215], a[44:47], v[44:47], v[212:215]\n"
"s_waitcnt lgkmcnt(3)\n"
"ds_read_b128 v[132:135], v179 offset:13504\n"
"ds_read_b64_tr_b16 a[242:243], v236 offset:6144\n"
"v_smfmac_f32_16x16x64_bf16 v[252:255], v[90:93], v[224:231], v63\n"
"s_add_u32 m0, s34, 0\n"
"s_nop 0\n"
"buffer_load_dwordx4 v190, s[8:11], s32 offen lds\n"
"v_smfmac_f32_16x16x64_bf16 v[98:101], v[90:93], v[244:251], v63\n"
"v_cvt_pk_bf16_f32 v204, v204, v205\n"
"v_smfmac_f32_16x16x64_bf16 v[106:109], v[90:93], v[24:31], v63\n"
"v_cvt_pk_bf16_f32 v205, v206, v207\n"
"v_mfma_f32_16x16x32_bf16 v[90:93], v[172:175], v[90:93], v[82:85]\n"
"v_cvt_pk_bf16_f32 v206, v208, v209\n"
"v_cvt_pk_bf16_f32 v252, v252, v253\n"
"v_mfma_f32_16x16x32_bf16 v[212:215], a[56:59], v[48:51], v[212:215]\n"
"ds_read_b64 v[82:83], v180 offset:13568\n"
"ds_read_b64 v[84:85], v181 offset:13568\n"
"v_mfma_f32_16x16x32_bf16 v[212:215], a[68:71], v[52:55], v[212:215]\n"
"v_cvt_pk_bf16_f32 v253, v254, v255\n"
"s_nop 0\n"
"v_rcp_f32 v171, v90\n"
"v_cvt_pk_bf16_f32 v98, v98, v99\n"
"v_cvt_pk_bf16_f32 v171, v171, v171\n"
"v_and_b32 v92, v171, v176\n"
"v_and_b32 v93, v171, v177\n"
"v_cvt_pk_bf16_f32 v99, v100, v101\n"
"v_cvt_pk_bf16_f32 v106, v106, v107\n"
"v_mfma_f32_16x16x16_bf16 v[252:255], v[252:253], v[92:93], 0\n"
"s_add_u32 m0, s34, 3072\n"
"ds_read_b64 v[90:91], v180 offset:27072\n"
"buffer_load_dwordx4 v191, s[8:11], s32 offen lds\n"
"v_mfma_f32_16x16x16_bf16 v[98:101], v[98:99], v[92:93], 0\n"
"s_add_u32 s32, s32, s33\n"
"v_cvt_pk_bf16_f32 v107, v108, v109\n"
"v_cvt_pk_bf16_f32 v207, v210, v211\n"
"v_mfma_f32_16x16x32_bf16 v[208:211], a[80:83], v[32:35], 0\n"
"ds_write_b128 v186, v[204:207] offset:33760\n"
"v_mfma_f32_16x16x16_bf16 v[106:109], v[106:107], v[92:93], 0\n"
"s_waitcnt lgkmcnt(2)\n"
"buffer_load_dword v237, v238, s[8:11], s78 offen\n"
"v_cvt_pk_bf16_f32 v252, v252, v253\n"
"v_mfma_f32_16x16x32_bf16 v[120:123], v[120:123], v[148:151], 0\n"
"s_add_u32 s78, s78, s33\n"
"v_cvt_pk_bf16_f32 v253, v254, v255\n"
"v_cvt_pk_bf16_f32 v254, v98, v99\n"
"v_mfma_f32_16x16x32_bf16 v[120:123], v[80:83], v[160:163], v[120:123]\n"
"ds_read_b64 v[92:93], v181 offset:27072\n"
"ds_read_b64 v[98:99], v178 offset:64\n"
"v_mfma_f32_16x16x32_bf16 v[136:139], v[136:139], v[148:151], 0\n"
"s_waitcnt lgkmcnt(3)\n"
"v_cvt_pk_bf16_f32 v255, v100, v101\n"
"v_cvt_pk_bf16_f32 v106, v106, v107\n"
"v_mfma_f32_16x16x32_bf16 v[136:139], v[84:87], v[162:165], v[136:139]\n"
"s_add_u32 m0, s34, 1024\n"
"buffer_store_dwordx4 v[252:255], v192, s[28:31], s37 offen sc1\n"
"v_mfma_f32_16x16x32_bf16 v[124:127], v[124:127], v[148:151], 0\n"
"ds_read_b64 v[100:101], v179 offset:64\n"
"v_cvt_pk_bf16_f32 v107, v108, v109\n"
"v_mfma_f32_16x16x32_bf16 v[124:127], v[88:91], v[160:163], v[124:127]\n"
"ds_read_b64 v[108:109], v179 offset:13568\n"
"ds_read_b64_tr_b16 a[244:245], v236 offset:8\n"
"v_mfma_f32_16x16x32_bf16 v[208:211], a[92:95], v[36:39], v[208:211]\n"
"buffer_load_dwordx4 v190, s[8:11], s0 offen lds\n"
"v_cvt_pk_bf16_f32 v212, v212, v213\n"
"v_mfma_f32_16x16x32_bf16 v[208:211], a[104:107], v[40:43], v[208:211]\n"
"s_waitcnt lgkmcnt(4)\n"
"v_cvt_pk_bf16_f32 v213, v214, v215\n"
"v_max_f32 v120, v120, v136\n"
"v_mfma_f32_16x16x32_bf16 v[140:143], v[140:143], v[148:151], 0\n"
"s_waitcnt lgkmcnt(3)\n"
"ds_read_b64_tr_b16 a[246:247], v236 offset:6152\n"
"v_max_f32 v121, v121, v137\n"
"v_mfma_f32_16x16x32_bf16 v[140:143], v[92:95], v[162:165], v[140:143]\n"
"ds_read_b64_tr_b16 v[136:137], v182 offset:72\n"
"v_max_f32 v122, v122, v138\n"
"v_mfma_f32_16x16x32_bf16 v[112:115], v[112:115], v[148:151], 0\n"
"buffer_store_dwordx2 v[106:107], v193, s[28:31], s37 offen offset:1024 sc1\n"
"v_mfma_f32_16x16x32_bf16 v[112:115], v[96:99], v[160:163], v[112:115]\n"
"s_waitcnt lgkmcnt(4)\n"
"ds_read_b64 v[106:107], v178 offset:13568\n"
"v_max_f32 v123, v123, v139\n"
"v_mfma_f32_16x16x32_bf16 v[128:131], v[128:131], v[148:151], 0\n"
"s_waitcnt lgkmcnt(4)\n"
"ds_read_b64_tr_b16 v[138:139], v183 offset:72\n"
"v_max3_f32 v170, v120, v121, v122\n"
"v_mfma_f32_16x16x32_bf16 v[128:131], v[100:103], v[162:165], v[128:131]\n"
"s_add_u32 m0, s34, 4096\n"
"v_max_f32 v124, v124, v140\n"
"v_max_f32 v125, v125, v141\n"
"v_mfma_f32_16x16x32_bf16 v[132:135], v[132:135], v[148:151], 0\n"
"s_add_u32 s37, s37, 1572864\n"
"buffer_load_dwordx4 v191, s[8:11], s0 offen lds\n"
"ds_read_b64_tr_b16 v[140:141], v182 offset:840\n"
"v_mfma_f32_16x16x32_bf16 v[132:135], v[108:111], v[162:165], v[132:135]\n"
"s_add_u32 s0, s0, s33\n"
"ds_read_b64_tr_b16 a[248:249], v235\n"
"v_max_f32 v126, v126, v142\n"
"v_mfma_f32_16x16x32_bf16 v[208:211], a[116:119], v[44:47], v[208:211]\n"
"ds_read_b64_tr_b16 a[250:251], v235 offset:6144\n"
"v_max_f32 v127, v127, v143\n"
"v_mfma_f32_16x16x32_bf16 v[208:211], a[128:131], v[48:51], v[208:211]\n"
"v_max3_f32 v170, v170, v123, v124\n"
"v_max3_f32 v170, v170, v125, v126\n"
"v_mfma_f32_16x16x32_bf16 v[208:211], a[140:143], v[52:55], v[208:211]\n"
"s_waitcnt lgkmcnt(4)\n"
"buffer_load_dword v200, off, s[28:31], s39 sc1\n"
"ds_read_b64_tr_b16 v[142:143], v183 offset:840\n"
"v_mfma_f32_16x16x32_bf16 v[116:119], v[116:119], v[148:151], 0\n"
"v_max_f32 v112, v112, v128\n"
"v_max_f32 v113, v113, v129\n"
"v_mfma_f32_16x16x32_bf16 v[116:119], v[104:107], v[160:163], v[116:119]\n"
"v_max_f32 v114, v114, v130\n"
"v_cvt_pk_bf16_f32 v214, v208, v209\n"
"v_mfma_f32_16x16x32_bf16 v[204:207], a[72:75], v[32:35], 0\n"
"v_cvt_pk_bf16_f32 v215, v210, v211\n"
"v_max_f32 v115, v115, v131\n"
"v_mfma_f32_16x16x32_bf16 v[204:207], a[84:87], v[36:39], v[204:207]\n"
"s_add_u32 m0, s34, 2048\n"
"ds_write_b128 v186, v[212:215] offset:33824\n"
"v_mfma_f32_16x16x32_bf16 v[204:207], a[96:99], v[40:43], v[204:207]\n"
"buffer_load_dwordx4 v190, s[8:11], s1 offen lds\n"
"v_max3_f32 v170, v170, v127, v112\n"
"v_mfma_f32_16x16x32_bf16 v[204:207], a[108:111], v[44:47], v[204:207]\n"
"v_max3_f32 v170, v170, v113, v114\n"
"v_max_f32 v116, v116, v132\n"
"v_mfma_f32_16x16x32_bf16 v[204:207], a[120:123], v[48:51], v[204:207]\n"
"v_max_f32 v117, v117, v133\n"
"v_max_f32 v118, v118, v134\n"
"v_mfma_f32_16x16x32_bf16 v[204:207], a[132:135], v[52:55], v[204:207]\n"
"v_max_f32 v119, v119, v135\n"
"v_max3_f32 v170, v170, v115, v116\n"
"v_mfma_f32_16x16x32_bf16 v[208:211], a[76:79], v[32:35], 0\n"
"v_max3_f32 v170, v170, v117, v118\n"
"v_mov_b64 v[252:253], 0\n"
"v_mfma_f32_16x16x32_bf16 v[208:211], a[88:91], v[36:39], v[208:211]\n"
"v_mov_b64 v[254:255], 0\n"
"v_mov_b64 v[98:99], 0\n"
"v_mfma_f32_16x16x32_bf16 v[208:211], a[100:103], v[40:43], v[208:211]\n"
"v_cvt_pk_bf16_f32 v204, v204, v205\n"
"v_cvt_pk_bf16_f32 v205, v206, v207\n"
"v_mfma_f32_16x16x32_bf16 v[208:211], a[112:115], v[44:47], v[208:211]\n"
"v_mov_b64 v[100:101], 0\n"
"v_mov_b64 v[106:107], 0\n"
"v_mfma_f32_16x16x32_bf16 v[208:211], a[124:127], v[48:51], v[208:211]\n"
"s_add_u32 m0, s34, 5120\n"
"ds_read_b128 v[128:131], v180 offset:27008\n"
"buffer_load_dwordx4 v191, s[8:11], s1 offen lds\n"
"v_mfma_f32_16x16x32_bf16 v[208:211], a[136:139], v[52:55], v[208:211]\n"
"s_add_u32 s1, s1, s33\n"
"v_mov_b64 v[108:109], 0\n"
"v_max_f32 v171, v170, v119\n"
"v_mfma_f32_16x16x32_bf16 v[212:215], a[144:147], v[32:35], 0\n"
"v_max_f32 v170, v170, v119\n"
"s_mov_b64 exec, s[46:47]\n"
"v_add_u32 v185, 0xc0, v185\n"
"s_mov_b64 exec, -1\n"
"v_mfma_f32_16x16x32_bf16 v[212:215], a[156:159], v[36:39], v[212:215]\n"
"v_permlane16_swap_b32 v170, v171\n"
"v_mfma_f32_16x16x32_bf16 v[212:215], a[168:171], v[40:43], v[212:215]\n"
"v_max_f32 v170, v170, v171\n"
"v_mov_b32 v171, v170\n"
"v_mfma_f32_16x16x32_bf16 v[212:215], a[180:183], v[44:47], v[212:215]\n"
"s_nop 0\n"
"v_permlane32_swap_b32 v170, v171\n"
"v_max_f32 v170, v170, v171\n"
"v_mul_f32 v170, 0xbe553b95, v170\n"
"v_pk_fma_f32 v[120:121], v[120:121], s[70:71], v[170:171] op_sel_hi:[1,0,0]\n"
"v_pk_fma_f32 v[122:123], v[122:123], s[70:71], v[170:171] op_sel_hi:[1,0,0]\n"
"v_pk_fma_f32 v[124:125], v[124:125], s[70:71], v[170:171] op_sel_hi:[1,0,0]\n"
"v_pk_fma_f32 v[126:127], v[126:127], s[70:71], v[170:171] op_sel_hi:[1,0,0]\n"
"v_pk_fma_f32 v[112:113], v[112:113], s[70:71], v[170:171] op_sel_hi:[1,0,0]\n"
"v_pk_fma_f32 v[114:115], v[114:115], s[70:71], v[170:171] op_sel_hi:[1,0,0]\n"
"v_pk_fma_f32 v[116:117], v[116:117], s[70:71], v[170:171] op_sel_hi:[1,0,0]\n"
"v_pk_fma_f32 v[118:119], v[118:119], s[70:71], v[170:171] op_sel_hi:[1,0,0]\n"
"v_mfma_f32_16x16x32_bf16 v[212:215], a[192:195], v[48:51], v[212:215]\n"
"ds_read_b128 v[132:135], v178\n"
"v_cvt_pk_bf16_f32 v206, v208, v209\n"
"v_mfma_f32_16x16x32_bf16 v[212:215], a[204:207], v[52:55], v[212:215]\n"
"v_cvt_pk_bf16_f32 v207, v210, v211\n"
"v_exp_f32 v120, v120\n"
"v_mfma_f32_16x16x32_bf16 v[208:211], a[148:151], v[32:35], 0\n"
"ds_write_b128 v186, v[204:207] offset:33888\n"
"v_mfma_f32_16x16x32_bf16 v[208:211], a[160:163], v[36:39], v[208:211]\n"
"v_exp_f32 v121, v121\n"
"v_mfma_f32_16x16x32_bf16 v[208:211], a[172:175], v[40:43], v[208:211]\n"
"v_exp_f32 v122, v122\n"
"v_mfma_f32_16x16x32_bf16 v[208:211], a[184:187], v[44:47], v[208:211]\n"
"v_exp_f32 v123, v123\n"
"v_mfma_f32_16x16x32_bf16 v[208:211], a[196:199], v[48:51], v[208:211]\n"
"v_cvt_pk_bf16_f32 v82, v120, v122\n"
"v_cvt_pk_bf16_f32 v83, v121, v123\n"
"v_mfma_f32_16x16x32_bf16 v[208:211], a[208:211], v[52:55], v[208:211]\n"
"ds_read_b128 v[120:123], v181 offset:27008\n"
"v_cvt_pk_bf16_f32 v148, v212, v213\n"
"v_mfma_f32_16x16x32_bf16 v[204:207], a[152:155], v[32:35], 0\n"
"s_waitcnt vmcnt(21)\n"
"ds_read_b128 v[32:35], v188 offset:6144\n"
"v_cvt_pk_bf16_f32 v149, v214, v215\n"
"v_mfma_f32_16x16x32_bf16 v[204:207], a[164:167], v[36:39], v[204:207]\n"
"s_waitcnt vmcnt(10) lgkmcnt(2)\n"
"ds_read_b128 v[36:39], v189 offset:6144\n"
"v_exp_f32 v124, v124\n"
"v_mfma_f32_16x16x32_bf16 v[204:207], a[176:179], v[40:43], v[204:207]\n"
"s_barrier\n"
"v_mfma_f32_16x16x32_bf16 v[204:207], a[188:191], v[44:47], v[204:207]\n"
"s_add_u32 m0, s79, 130176\n"
"ds_read_b128 v[40:43], v188 offset:7168\n"
"buffer_load_dwordx4 v192, s[16:19], s43 offen lds\n"
"v_mfma_f32_16x16x32_bf16 v[204:207], a[200:203], v[48:51], v[204:207]\n"
"ds_read_b128 v[44:47], v189 offset:7168\n"
"ds_read_b128 v[48:51], v188 offset:8192\n"
"v_mfma_f32_16x16x32_bf16 v[204:207], a[212:215], v[52:55], v[204:207]\n"
"ds_read_b128 v[52:55], v189 offset:8192\n"
"v_exp_f32 v125, v125\n"
"v_exp_f32 v126, v126\n"
"v_exp_f32 v127, v127\n"
"v_cvt_pk_bf16_f32 v84, v124, v126\n"
"v_cvt_pk_bf16_f32 v85, v125, v127\n"
"v_exp_f32 v112, v112\n"
"s_nop 0\n"
"v_smfmac_f32_16x16x64_bf16 v[252:255], v[82:85], v[216:223], v63\n"
"s_waitcnt lgkmcnt(5)\n"
"ds_read_b64_tr_b16 v[216:217], v182 offset:128\n"
"v_smfmac_f32_16x16x64_bf16 v[98:101], v[82:85], v[72:79], v63\n"
"ds_read_b64_tr_b16 v[218:219], v183 offset:128\n"
"v_smfmac_f32_16x16x64_bf16 v[106:109], v[82:85], v[8:15], v63\n"
"ds_read_b64_tr_b16 v[220:221], v182 offset:896\n"
"v_mfma_f32_16x16x32_bf16 v[82:85], v[172:175], v[82:85], 0\n"
"s_waitcnt lgkmcnt(5)\n"
"ds_read_b64_tr_b16 v[222:223], v183 offset:896\n"
"ds_read_b64_tr_b16 v[72:73], v182 offset:136\n"
"v_mfma_f32_16x16x32_bf16 v[212:215], a[0:3], v[32:35], 0\n"
"s_add_u32 m0, s79, 130176\n"
"ds_read_b64_tr_b16 v[74:75], v183 offset:136\n"
"buffer_load_dwordx4 v192, s[16:19], s43 offen offset:1024 lds\n"
"v_mfma_f32_16x16x32_bf16 v[212:215], a[12:15], v[36:39], v[212:215]\n"
"ds_read_b64_tr_b16 v[76:77], v182 offset:904\n"
"v_cvt_pk_bf16_f32 v150, v208, v209\n"
"v_mfma_f32_16x16x32_bf16 v[212:215], a[24:27], v[40:43], v[212:215]\n"
"ds_read_b64_tr_b16 v[78:79], v183 offset:904\n"
"v_cvt_pk_bf16_f32 v151, v210, v211\n"
"v_mfma_f32_16x16x32_bf16 v[212:215], a[36:39], v[44:47], v[212:215]\n"
"s_waitcnt lgkmcnt(9)\n"
"ds_read_b128 v[124:127], v179\n"
"v_exp_f32 v113, v113\n"
"v_mfma_f32_16x16x32_bf16 v[212:215], a[48:51], v[48:51], v[212:215]\n"
"s_waitcnt lgkmcnt(9)\n"
"v_exp_f32 v114, v114\n"
"v_mfma_f32_16x16x32_bf16 v[212:215], a[60:63], v[52:55], v[212:215]\n"
"v_exp_f32 v115, v115\n"
"v_mfma_f32_16x16x32_bf16 v[208:211], a[4:7], v[32:35], 0\n"
"v_exp_f32 v116, v116\n"
"v_mfma_f32_16x16x32_bf16 v[208:211], a[16:19], v[36:39], v[208:211]\n"
"v_exp_f32 v117, v117\n"
"v_mfma_f32_16x16x32_bf16 v[208:211], a[28:31], v[40:43], v[208:211]\n"
"s_add_u32 m0, s79, 130176\n"
"v_exp_f32 v118, v118\n"
"v_mfma_f32_16x16x32_bf16 v[208:211], a[40:43], v[44:47], v[208:211]\n"
"buffer_load_dwordx4 v192, s[16:19], s43 offen offset:2048 lds\n"
"v_cvt_pk_bf16_f32 v90, v112, v114\n"
"v_mfma_f32_16x16x32_bf16 v[208:211], a[52:55], v[48:51], v[208:211]\n"
"s_add_u32 s43, s80, 49152\n"
"v_exp_f32 v119, v119\n"
"v_mfma_f32_16x16x32_bf16 v[208:211], a[64:67], v[52:55], v[208:211]\n"
"v_cvt_pk_bf16_f32 v91, v113, v115\n"
"v_cvt_pk_bf16_f32 v92, v116, v118\n"
"v_cvt_pk_bf16_f32 v93, v117, v119\n"
"v_cvt_pk_bf16_f32 v162, v204, v205\n"
"v_cvt_pk_bf16_f32 v163, v206, v207\n"
"v_smfmac_f32_16x16x64_bf16 v[106:109], v[90:93], v[136:143], v63\n"
"s_waitcnt lgkmcnt(1)\n"
"ds_read_b128 v[112:115], v178 offset:13504\n"
"v_smfmac_f32_16x16x64_bf16 v[252:255], v[90:93], v[216:223], v63\n"
"ds_read_b128 v[136:139], v179 offset:13504\n"
"v_smfmac_f32_16x16x64_bf16 v[98:101], v[90:93], v[72:79], v63\n"
"ds_read_b128 v[116:119], v178 offset:27008\n"
"v_mfma_f32_16x16x32_bf16 v[90:93], v[172:175], v[90:93], v[82:85]\n"
"ds_read_b128 v[140:143], v179 offset:27008\n"
"v_cvt_pk_bf16_f32 v106, v106, v107\n"
"v_mfma_f32_16x16x32_bf16 v[204:207], a[8:11], v[32:35], 0\n"
"s_add_u32 m0, s34, 6144\n"
"ds_read_b64 v[82:83], v180 offset:27072\n"
"buffer_load_dwordx4 v190, s[8:11], s32 offen lds\n"
"v_mfma_f32_16x16x32_bf16 v[204:207], a[20:23], v[36:39], v[204:207]\n"
"ds_read_b64 v[84:85], v181 offset:27072\n"
"v_cvt_pk_bf16_f32 v252, v252, v253\n"
"v_mfma_f32_16x16x32_bf16 v[204:207], a[32:35], v[40:43], v[204:207]\n"
"v_rcp_f32 v171, v90\n"
"v_mfma_f32_16x16x32_bf16 v[204:207], a[44:47], v[44:47], v[204:207]\n"
"ds_read_b64 v[90:91], v178 offset:64\n"
"v_cvt_pk_bf16_f32 v171, v171, v171\n"
"v_mfma_f32_16x16x32_bf16 v[204:207], a[56:59], v[48:51], v[204:207]\n"
"v_and_b32 v92, v171, v176\n"
"v_and_b32 v93, v171, v177\n"
"v_mfma_f32_16x16x32_bf16 v[204:207], a[68:71], v[52:55], v[204:207]\n"
"v_cvt_pk_bf16_f32 v253, v254, v255\n"
"v_cvt_pk_bf16_f32 v98, v98, v99\n"
"v_cvt_pk_bf16_f32 v107, v108, v109\n"
"v_mfma_f32_16x16x16_bf16 v[252:255], v[252:253], v[92:93], 0\n"
"v_cvt_pk_bf16_f32 v99, v100, v101\n"
"v_cvt_pk_bf16_f32 v212, v212, v213\n"
"v_mfma_f32_16x16x16_bf16 v[106:109], v[106:107], v[92:93], 0\n"
"s_waitcnt lgkmcnt(2)\n"
"ds_read_b64_tr_b16 a[252:253], v236 offset:12288\n"
"buffer_load_dword v237, v238, s[8:11], s78 offen\n"
"v_mfma_f32_16x16x16_bf16 v[98:101], v[98:99], v[92:93], 0\n"
"s_waitcnt lgkmcnt(2)\n"
"ds_read_b64 v[92:93], v179 offset:64\n"
"v_cvt_pk_bf16_f32 v252, v252, v253\n"
"v_mfma_f32_16x16x32_bf16 v[128:131], v[128:131], v[152:155], 0\n"
"s_add_u32 m0, s34, 9216\n"
"v_cvt_pk_bf16_f32 v253, v254, v255\n"
"v_cvt_pk_bf16_f32 v106, v106, v107\n"
"v_mfma_f32_16x16x32_bf16 v[128:131], v[80:83], v[164:167], v[128:131]\n"
"s_waitcnt lgkmcnt(2)\n"
"v_cvt_pk_bf16_f32 v254, v98, v99\n"
"v_cvt_pk_bf16_f32 v255, v100, v101\n"
"v_mfma_f32_16x16x32_bf16 v[120:123], v[120:123], v[152:155], 0\n"
"s_add_u32 s78, s78, s33\n"
"buffer_load_dwordx4 v191, s[8:11], s32 offen lds\n"
"ds_read_b64 v[98:99], v178 offset:13568\n"
"v_mfma_f32_16x16x32_bf16 v[120:123], v[84:87], v[166:169], v[120:123]\n"
"s_add_u32 s32, s32, s33\n"
"ds_read_b64 v[100:101], v179 offset:13568\n"
"v_cvt_pk_bf16_f32 v107, v108, v109\n"
"v_mfma_f32_16x16x32_bf16 v[132:135], v[132:135], v[152:155], 0\n"
"ds_read_b64 v[108:109], v179 offset:27072\n"
"ds_read_b64_tr_b16 a[254:255], v236 offset:18432\n"
"v_mfma_f32_16x16x32_bf16 v[132:135], v[88:91], v[164:167], v[132:135]\n"
"v_cvt_pk_bf16_f32 v213, v214, v215\n"
"v_cvt_pk_bf16_f32 v214, v208, v209\n"
"v_cvt_pk_bf16_f32 v215, v210, v211\n"
"v_mfma_f32_16x16x32_bf16 v[208:211], a[80:83], v[32:35], 0\n"
"buffer_store_dwordx4 v[252:255], v192, s[28:31], s37 offen sc1\n"
"v_mfma_f32_16x16x32_bf16 v[208:211], a[92:95], v[36:39], v[208:211]\n"
"s_waitcnt lgkmcnt(4)\n"
"ds_write_b128 v186, v[212:215] offset:47264\n"
"v_mfma_f32_16x16x32_bf16 v[124:127], v[124:127], v[152:155], 0\n"
"s_waitcnt lgkmcnt(4)\n"
"buffer_store_dwordx2 v[106:107], v193, s[28:31], s37 offen offset:1024 sc1\n"
"v_mfma_f32_16x16x32_bf16 v[124:127], v[92:95], v[166:169], v[124:127]\n"
"s_waitcnt lgkmcnt(2)\n"
"ds_read_b64 v[106:107], v178 offset:27072\n"
"ds_read_b64_tr_b16 v[0:1], v236 offset:12296\n"
"v_mfma_f32_16x16x32_bf16 v[112:115], v[112:115], v[152:155], 0\n"
"s_add_u32 m0, s34, 7168\n"
"v_cvt_pk_bf16_f32 v204, v204, v205\n"
"v_cvt_pk_bf16_f32 v205, v206, v207\n"
"v_mfma_f32_16x16x32_bf16 v[112:115], v[96:99], v[164:167], v[112:115]\n"
"s_add_u32 s37, s37, 1572864\n"
"ds_read_b64_tr_b16 v[2:3], v236 offset:18440\n"
"buffer_load_dwordx4 v190, s[8:11], s0 offen lds\n"
"v_mfma_f32_16x16x32_bf16 v[136:139], v[136:139], v[152:155], 0\n"
"v_max_f32 v128, v128, v120\n"
"v_max_f32 v129, v129, v121\n"
"v_mfma_f32_16x16x32_bf16 v[136:139], v[100:103], v[166:169], v[136:139]\n"
"ds_read_b64_tr_b16 v[120:121], v182 offset:13576\n"
"v_max_f32 v130, v130, v122\n"
"v_mfma_f32_16x16x32_bf16 v[140:143], v[140:143], v[152:155], 0\n"
"v_max_f32 v131, v131, v123\n"
"v_max3_f32 v170, v128, v129, v130\n"
"v_mfma_f32_16x16x32_bf16 v[140:143], v[108:111], v[166:169], v[140:143]\n"
"ds_read_b64_tr_b16 v[122:123], v183 offset:13576\n"
"v_max_f32 v132, v132, v124\n"
"v_mfma_f32_16x16x32_bf16 v[208:211], a[104:107], v[40:43], v[208:211]\n"
"s_waitcnt lgkmcnt(4)\n"
"v_max_f32 v133, v133, v125\n"
"v_max_f32 v134, v134, v126\n"
"v_mfma_f32_16x16x32_bf16 v[208:211], a[116:119], v[44:47], v[208:211]\n"
"ds_read_b64_tr_b16 v[124:125], v182 offset:14344\n"
"ds_read_b64_tr_b16 v[4:5], v235 offset:12288\n"
"v_mfma_f32_16x16x32_bf16 v[208:211], a[128:131], v[48:51], v[208:211]\n"
"v_max_f32 v135, v135, v127\n"
"v_max3_f32 v170, v170, v131, v132\n"
"v_mfma_f32_16x16x32_bf16 v[208:211], a[140:143], v[52:55], v[208:211]\n"
"s_add_u32 m0, s34, 10240\n"
"ds_read_b64_tr_b16 v[126:127], v183 offset:14344\n"
"buffer_load_dwordx4 v191, s[8:11], s0 offen lds\n"
"v_mfma_f32_16x16x32_bf16 v[116:119], v[116:119], v[152:155], 0\n"
"s_add_u32 s0, s0, s33\n"
"ds_read_b64_tr_b16 v[6:7], v235 offset:18432\n"
"v_max3_f32 v170, v170, v133, v134\n"
"v_mfma_f32_16x16x32_bf16 v[116:119], v[104:107], v[164:167], v[116:119]\n"
"v_cvt_pk_bf16_f32 v206, v208, v209\n"
"v_cvt_pk_bf16_f32 v207, v210, v211\n"
"v_mfma_f32_16x16x32_bf16 v[212:215], a[72:75], v[32:35], 0\n"
"ds_write_b128 v186, v[204:207] offset:47328\n"
"v_mfma_f32_16x16x32_bf16 v[212:215], a[84:87], v[36:39], v[212:215]\n"
"s_waitcnt vmcnt(12)\n"
"v_add3_u32 v200, v200, s50, -2\n"
"v_cmp_lt_u32 vcc, 64, v200\n"
"v_mfma_f32_16x16x32_bf16 v[212:215], a[96:99], v[40:43], v[212:215]\n"
"v_max_f32 v112, v112, v136\n"
"v_max_f32 v113, v113, v137\n"
"v_mfma_f32_16x16x32_bf16 v[212:215], a[108:111], v[44:47], v[212:215]\n"
"s_nop 0\n"
"s_cbranch_vccnz .Lp1%=\n"
".Lr1%=:\n"
"v_mfma_f32_16x16x32_bf16 v[212:215], a[120:123], v[48:51], v[212:215]\n"
"s_add_u32 m0, s34, 8192\n"
"buffer_load_dword v201, off, s[28:31], s40 offset:128 sc1\n"
"v_max_f32 v114, v114, v138\n"
"v_mfma_f32_16x16x32_bf16 v[212:215], a[132:135], v[52:55], v[212:215]\n"
"buffer_load_dwordx4 v190, s[8:11], s1 offen lds\n"
"v_max_f32 v115, v115, v139\n"
"v_mfma_f32_16x16x32_bf16 v[208:211], a[76:79], v[32:35], 0\n"
"v_max3_f32 v170, v170, v135, v112\n"
"v_max3_f32 v170, v170, v113, v114\n"
"v_mfma_f32_16x16x32_bf16 v[208:211], a[88:91], v[36:39], v[208:211]\n"
"v_max_f32 v116, v116, v140\n"
"v_max_f32 v117, v117, v141\n"
"v_mfma_f32_16x16x32_bf16 v[208:211], a[100:103], v[40:43], v[208:211]\n"
"v_max_f32 v118, v118, v142\n"
"v_max_f32 v119, v119, v143\n"
"v_mfma_f32_16x16x32_bf16 v[208:211], a[112:115], v[44:47], v[208:211]\n"
"buffer_load_dwordx4 v[56:59], v192, s[28:31], s38 offen sc1\n"
"v_max3_f32 v170, v170, v115, v116\n"
"v_mfma_f32_16x16x32_bf16 v[208:211], a[124:127], v[48:51], v[208:211]\n"
"v_max3_f32 v170, v170, v117, v118\n"
"v_mov_b64 v[252:253], 0\n"
"v_mfma_f32_16x16x32_bf16 v[208:211], a[136:139], v[52:55], v[208:211]\n"
"v_mov_b64 v[254:255], 0\n"
"v_mov_b64 v[98:99], 0\n"
"v_mfma_f32_16x16x32_bf16 v[204:207], a[144:147], v[32:35], 0\n"
"v_cvt_pk_bf16_f32 v212, v212, v213\n"
"v_mov_b64 v[100:101], 0\n"
"v_mfma_f32_16x16x32_bf16 v[204:207], a[156:159], v[36:39], v[204:207]\n"
"s_add_u32 m0, s34, 11264\n"
"buffer_load_dwordx2 v[60:61], v193, s[28:31], s38 offen offset:1024 sc1\n"
"v_cvt_pk_bf16_f32 v213, v214, v215\n"
"v_mfma_f32_16x16x32_bf16 v[204:207], a[168:171], v[40:43], v[204:207]\n"
"s_add_u32 s38, s38, 1572864\n"
"v_mov_b64 v[106:107], 0\n"
"v_mov_b64 v[108:109], 0\n"
"v_mfma_f32_16x16x32_bf16 v[204:207], a[180:183], v[44:47], v[204:207]\n"
"v_max_f32 v171, v170, v119\n"
"v_max_f32 v170, v170, v119\n"
"v_mfma_f32_16x16x32_bf16 v[204:207], a[192:195], v[48:51], v[204:207]\n"
"s_nop 0\n"
"v_permlane16_swap_b32 v170, v171\n"
"v_mfma_f32_16x16x32_bf16 v[204:207], a[204:207], v[52:55], v[204:207]\n"
"buffer_load_dwordx4 v191, s[8:11], s1 offen lds\n"
"s_add_u32 s1, s1, s33\n"
"v_max_f32 v170, v170, v171\n"
"v_mov_b32 v171, v170\n"
"v_cvt_pk_bf16_f32 v214, v208, v209\n"
"v_cvt_pk_bf16_f32 v215, v210, v211\n"
"v_mfma_f32_16x16x32_bf16 v[208:211], a[148:151], v[32:35], 0\n"
"ds_write_b128 v186, v[212:215] offset:47392\n"
"v_mfma_f32_16x16x32_bf16 v[208:211], a[160:163], v[36:39], v[208:211]\n"
"v_permlane32_swap_b32 v170, v171\n"
"v_max_f32 v170, v170, v171\n"
"v_mul_f32 v170, 0xbe553b95, v170\n"
"v_pk_fma_f32 v[128:129], v[128:129], s[70:71], v[170:171] op_sel_hi:[1,0,0]\n"
"v_pk_fma_f32 v[130:131], v[130:131], s[70:71], v[170:171] op_sel_hi:[1,0,0]\n"
"v_pk_fma_f32 v[132:133], v[132:133], s[70:71], v[170:171] op_sel_hi:[1,0,0]\n"
"v_pk_fma_f32 v[134:135], v[134:135], s[70:71], v[170:171] op_sel_hi:[1,0,0]\n"
"v_pk_fma_f32 v[112:113], v[112:113], s[70:71], v[170:171] op_sel_hi:[1,0,0]\n"
"v_pk_fma_f32 v[114:115], v[114:115], s[70:71], v[170:171] op_sel_hi:[1,0,0]\n"
"v_pk_fma_f32 v[116:117], v[116:117], s[70:71], v[170:171] op_sel_hi:[1,0,0]\n"
"v_pk_fma_f32 v[118:119], v[118:119], s[70:71], v[170:171] op_sel_hi:[1,0,0]\n"
"v_mfma_f32_16x16x32_bf16 v[208:211], a[172:175], v[40:43], v[208:211]\n"
"ds_read_b128 v[136:139], v178\n"
"ds_read_b128 v[140:143], v178 offset:13504\n"
"v_mfma_f32_16x16x32_bf16 v[208:211], a[184:187], v[44:47], v[208:211]\n"
"v_exp_f32 v128, v128\n"
"v_mfma_f32_16x16x32_bf16 v[208:211], a[196:199], v[48:51], v[208:211]\n"
"v_exp_f32 v129, v129\n"
"v_mfma_f32_16x16x32_bf16 v[208:211], a[208:211], v[52:55], v[208:211]\n"
"s_waitcnt vmcnt(12) lgkmcnt(2)\n"
"v_exp_f32 v130, v130\n"
"v_mfma_f32_16x16x32_bf16 v[212:215], a[152:155], v[32:35], 0\n"
"s_barrier\n"
"v_mfma_f32_16x16x32_bf16 v[212:215], a[164:167], v[36:39], v[212:215]\n"
"s_add_u32 m0, s79, 142464\n"
"ds_read_b128 v[32:35], v188\n"
"buffer_load_dwordx4 v192, s[16:19], s43 offen lds\n"
"v_mfma_f32_16x16x32_bf16 v[212:215], a[176:179], v[40:43], v[212:215]\n"
"ds_read_b128 v[36:39], v189\n"
"ds_read_b128 v[40:43], v188 offset:1024\n"
"v_mfma_f32_16x16x32_bf16 v[212:215], a[188:191], v[44:47], v[212:215]\n"
"ds_read_b128 v[44:47], v189 offset:1024\n"
"v_cvt_pk_bf16_f32 v82, v128, v130\n"
"v_mfma_f32_16x16x32_bf16 v[212:215], a[200:203], v[48:51], v[212:215]\n"
"ds_read_b128 v[48:51], v188 offset:2048\n"
"v_cvt_pk_bf16_f32 v152, v204, v205\n"
"v_mfma_f32_16x16x32_bf16 v[212:215], a[212:215], v[52:55], v[212:215]\n"
"ds_read_b128 v[52:55], v189 offset:2048\n"
"v_exp_f32 v131, v131\n"
"v_exp_f32 v132, v132\n"
"v_cvt_pk_bf16_f32 v83, v129, v131\n"
"v_exp_f32 v133, v133\n"
"v_exp_f32 v134, v134\n"
"v_exp_f32 v135, v135\n"
"v_cvt_pk_bf16_f32 v84, v132, v134\n"
"s_add_u32 m0, s79, 142464\n"
"v_cvt_pk_bf16_f32 v85, v133, v135\n"
"v_cvt_pk_bf16_f32 v153, v206, v207\n"
"v_exp_f32 v112, v112\n"
"v_smfmac_f32_16x16x64_bf16 v[252:255], v[82:85], v[224:231], v63\n"
"s_waitcnt lgkmcnt(5)\n"
"buffer_load_dwordx4 v192, s[16:19], s43 offen offset:1024 lds\n"
"v_smfmac_f32_16x16x64_bf16 v[98:101], v[82:85], v[244:251], v63\n"
"ds_read_b64_tr_b16 v[224:225], v182 offset:13632\n"
"v_smfmac_f32_16x16x64_bf16 v[106:109], v[82:85], v[24:31], v63\n"
"ds_read_b64_tr_b16 v[226:227], v183 offset:13632\n"
"v_mfma_f32_16x16x32_bf16 v[82:85], v[172:175], v[82:85], 0\n"
"ds_read_b64_tr_b16 v[228:229], v182 offset:14400\n"
"ds_read_b64_tr_b16 v[230:231], v183 offset:14400\n"
"v_mfma_f32_16x16x32_bf16 v[204:207], a[0:3], v[32:35], 0\n"
"s_waitcnt lgkmcnt(8)\n"
"ds_read_b64_tr_b16 v[244:245], v182 offset:13640\n"
"v_cvt_pk_bf16_f32 v154, v208, v209\n"
"v_mfma_f32_16x16x32_bf16 v[204:207], a[12:15], v[36:39], v[204:207]\n"
"s_waitcnt lgkmcnt(8)\n"
"ds_read_b64_tr_b16 v[246:247], v183 offset:13640\n"
"v_cvt_pk_bf16_f32 v155, v210, v211\n"
"v_mfma_f32_16x16x32_bf16 v[204:207], a[24:27], v[40:43], v[204:207]\n"
"s_waitcnt lgkmcnt(8)\n"
"ds_read_b64_tr_b16 v[248:249], v182 offset:14408\n"
"v_cvt_pk_bf16_f32 v166, v212, v213\n"
"v_mfma_f32_16x16x32_bf16 v[204:207], a[36:39], v[44:47], v[204:207]\n"
"s_waitcnt lgkmcnt(8)\n"
"ds_read_b64_tr_b16 v[250:251], v183 offset:14408\n"
"v_cvt_pk_bf16_f32 v167, v214, v215\n"
"v_mfma_f32_16x16x32_bf16 v[204:207], a[48:51], v[48:51], v[204:207]\n"
"s_waitcnt lgkmcnt(8)\n"
"ds_read_b128 v[128:131], v179\n"
"v_exp_f32 v113, v113\n"
"v_mfma_f32_16x16x32_bf16 v[204:207], a[60:63], v[52:55], v[204:207]\n"
"s_add_u32 m0, s79, 142464\n"
"ds_read_b128 v[132:135], v179 offset:13504\n"
"buffer_load_dwordx4 v192, s[16:19], s43 offen offset:2048 lds\n"
"v_mfma_f32_16x16x32_bf16 v[208:211], a[4:7], v[32:35], 0\n"
"v_exp_f32 v114, v114\n"
"v_mfma_f32_16x16x32_bf16 v[208:211], a[16:19], v[36:39], v[208:211]\n"
"v_exp_f32 v115, v115\n"
"v_mfma_f32_16x16x32_bf16 v[208:211], a[28:31], v[40:43], v[208:211]\n"
".Lic3%=:\n"
"s_cbranch_execz .Lib0%=\n"
"ds_read_b64_tr_b16 v[8:9], v236\n"
"v_cvt_pk_bf16_f32 v90, v112, v114\n"
"v_mfma_f32_16x16x32_bf16 v[208:211], a[40:43], v[44:47], v[208:211]\n"
"v_cvt_pk_bf16_f32 v91, v113, v115\n"
"v_exp_f32 v116, v116\n"
"v_mfma_f32_16x16x32_bf16 v[208:211], a[52:55], v[48:51], v[208:211]\n"
"ds_read_b128 v[112:115], v178 offset:27008\n"
"v_exp_f32 v117, v117\n"
"v_mfma_f32_16x16x32_bf16 v[208:211], a[64:67], v[52:55], v[208:211]\n"
"v_exp_f32 v118, v118\n"
"v_mfma_f32_16x16x32_bf16 v[212:215], a[8:11], v[32:35], 0\n"
"s_add_u32 m0, s34, 0\n"
"v_exp_f32 v119, v119\n"
"v_mfma_f32_16x16x32_bf16 v[212:215], a[20:23], v[36:39], v[212:215]\n"
"buffer_load_dwordx4 v190, s[8:11], s32 offen lds\n"
"v_cvt_pk_bf16_f32 v92, v116, v118\n"
"v_mfma_f32_16x16x32_bf16 v[212:215], a[32:35], v[40:43], v[212:215]\n"
"v_cvt_pk_bf16_f32 v93, v117, v119\n"
"v_cvt_pk_bf16_f32 v204, v204, v205\n"
"v_mfma_f32_16x16x32_bf16 v[212:215], a[44:47], v[44:47], v[212:215]\n"
"s_waitcnt lgkmcnt(4)\n"
"ds_read_b128 v[116:119], v180\n"
"ds_read_b64_tr_b16 v[10:11], v236 offset:6144\n"
"v_smfmac_f32_16x16x64_bf16 v[252:255], v[90:93], v[224:231], v63\n"
"v_cvt_pk_bf16_f32 v205, v206, v207\n"
"v_smfmac_f32_16x16x64_bf16 v[98:101], v[90:93], v[244:251], v63\n"
"s_mov_b64 exec, s[44:45]\n"
"v_add_u32 v181, 0xc0, v181\n"
"s_mov_b64 exec, -1\n"
"v_smfmac_f32_16x16x64_bf16 v[106:109], v[90:93], v[120:127], v63\n"
"ds_read_b128 v[120:123], v179 offset:27008\n"
"v_mfma_f32_16x16x32_bf16 v[90:93], v[172:175], v[90:93], v[82:85]\n"
"ds_read_b128 v[124:127], v181\n"
"v_cvt_pk_bf16_f32 v252, v252, v253\n"
"v_mfma_f32_16x16x32_bf16 v[212:215], a[56:59], v[48:51], v[212:215]\n"
"s_add_u32 m0, s34, 3072\n"
"ds_read_b64 v[82:83], v178 offset:64\n"
"buffer_load_dwordx4 v191, s[8:11], s32 offen lds\n"
"v_mfma_f32_16x16x32_bf16 v[212:215], a[68:71], v[52:55], v[212:215]\n"
"s_add_u32 s32, s32, s33\n"
"v_rcp_f32 v171, v90\n"
"v_cvt_pk_bf16_f32 v253, v254, v255\n"
"v_cvt_pk_bf16_f32 v171, v171, v171\n"
"v_and_b32 v92, v171, v176\n"
"v_and_b32 v93, v171, v177\n"
"v_cvt_pk_bf16_f32 v98, v98, v99\n"
"v_cvt_pk_bf16_f32 v99, v100, v101\n"
"v_mfma_f32_16x16x16_bf16 v[252:255], v[252:253], v[92:93], 0\n"
"ds_read_b64 v[84:85], v179 offset:64\n"
"ds_read_b64 v[90:91], v178 offset:13568\n"
"v_mfma_f32_16x16x16_bf16 v[98:101], v[98:99], v[92:93], 0\n"
"v_cvt_pk_bf16_f32 v106, v106, v107\n"
"v_cvt_pk_bf16_f32 v107, v108, v109\n"
"v_cvt_pk_bf16_f32 v206, v208, v209\n"
"v_cvt_pk_bf16_f32 v207, v210, v211\n"
"v_mfma_f32_16x16x16_bf16 v[106:109], v[106:107], v[92:93], 0\n"
"ds_read_b64 v[92:93], v179 offset:13568\n"
"ds_write_b128 v186, v[204:207] offset:60768\n"
"v_mfma_f32_16x16x32_bf16 v[208:211], a[80:83], v[32:35], 0\n"
"s_waitcnt lgkmcnt(4)\n"
"v_cvt_pk_bf16_f32 v252, v252, v253\n"
"v_cvt_pk_bf16_f32 v253, v254, v255\n"
"v_mfma_f32_16x16x32_bf16 v[136:139], v[136:139], v[144:147], 0\n"
"s_add_u32 m0, s34, 1024\n"
"v_cvt_pk_bf16_f32 v254, v98, v99\n"
"v_cvt_pk_bf16_f32 v255, v100, v101\n"
"v_mfma_f32_16x16x32_bf16 v[136:139], v[80:83], v[156:159], v[136:139]\n"
"buffer_load_dwordx4 v190, s[8:11], s0 offen lds\n"
"ds_read_b64 v[98:99], v178 offset:27072\n"
"v_mfma_f32_16x16x32_bf16 v[208:211], a[92:95], v[36:39], v[208:211]\n"
"s_waitcnt lgkmcnt(4)\n"
"ds_read_b64 v[100:101], v179 offset:27072\n"
"v_cvt_pk_bf16_f32 v106, v106, v107\n"
"v_mfma_f32_16x16x32_bf16 v[128:131], v[128:131], v[144:147], 0\n"
"s_waitcnt lgkmcnt(4)\n"
"v_cvt_pk_bf16_f32 v107, v108, v109\n"
"v_cvt_pk_bf16_f32 v212, v212, v213\n"
"v_mfma_f32_16x16x32_bf16 v[128:131], v[84:87], v[158:161], v[128:131]\n"
"s_waitcnt lgkmcnt(3)\n"
"buffer_store_dwordx4 v[252:255], v192, s[28:31], s37 offen sc1\n"
"v_mfma_f32_16x16x32_bf16 v[140:143], v[140:143], v[144:147], 0\n"
"ds_read_b64 v[108:109], v181 offset:64\n"
"ds_read_b64_tr_b16 v[12:13], v236 offset:8\n"
"v_mfma_f32_16x16x32_bf16 v[140:143], v[88:91], v[156:159], v[140:143]\n"
"v_cvt_pk_bf16_f32 v213, v214, v215\n"
"v_mov_b64 v[252:253], 0\n"
"v_mfma_f32_16x16x32_bf16 v[132:135], v[132:135], v[144:147], 0\n"
"ds_read_b64_tr_b16 v[14:15], v236 offset:6152\n"
"buffer_store_dwordx2 v[106:107], v193, s[28:31], s37 offen offset:1024 sc1\n"
"v_mfma_f32_16x16x32_bf16 v[132:135], v[92:95], v[158:161], v[132:135]\n"
"s_waitcnt lgkmcnt(3)\n"
"ds_read_b64 v[106:107], v180 offset:64\n"
"v_max_f32 v136, v136, v128\n"
"v_mfma_f32_16x16x32_bf16 v[112:115], v[112:115], v[144:147], 0\n"
"s_add_u32 s37, s37, 1572864\n"
"v_max_f32 v137, v137, v129\n"
"v_max_f32 v138, v138, v130\n"
"v_mfma_f32_16x16x32_bf16 v[112:115], v[96:99], v[156:159], v[112:115]\n"
"s_add_u32 m0, s34, 4096\n"
"ds_read_b64_tr_b16 v[128:129], v182 offset:72\n"
"v_max_f32 v139, v139, v131\n"
"v_mfma_f32_16x16x32_bf16 v[120:123], v[120:123], v[144:147], 0\n"
"buffer_load_dwordx4 v191, s[8:11], s0 offen lds\n"
"ds_read_b64_tr_b16 v[130:131], v183 offset:72\n"
"v_mfma_f32_16x16x32_bf16 v[120:123], v[100:103], v[158:161], v[120:123]\n"
"s_waitcnt lgkmcnt(5)\n"
"v_max3_f32 v170, v136, v137, v138\n"
"v_max_f32 v140, v140, v132\n"
"v_mfma_f32_16x16x32_bf16 v[124:127], v[124:127], v[144:147], 0\n"
"s_add_u32 s0, s0, s33\n"
"ds_read_b64_tr_b16 v[16:17], v235\n"
"v_max_f32 v141, v141, v133\n"
"v_mfma_f32_16x16x32_bf16 v[124:127], v[108:111], v[158:161], v[124:127]\n"
"ds_read_b64_tr_b16 v[132:133], v182 offset:840\n"
"v_max_f32 v142, v142, v134\n"
"v_mfma_f32_16x16x32_bf16 v[208:211], a[104:107], v[40:43], v[208:211]\n"
"ds_read_b64_tr_b16 v[18:19], v235 offset:6144\n"
"v_max_f32 v143, v143, v135\n"
"v_mfma_f32_16x16x32_bf16 v[208:211], a[116:119], v[44:47], v[208:211]\n"
"v_max3_f32 v170, v170, v139, v140\n"
"v_max3_f32 v170, v170, v141, v142\n"
"v_mfma_f32_16x16x32_bf16 v[208:211], a[128:131], v[48:51], v[208:211]\n"
"s_waitcnt lgkmcnt(5)\n"
"ds_read_b64_tr_b16 v[134:135], v183 offset:840\n"
"v_max_f32 v112, v112, v120\n"
"v_mfma_f32_16x16x32_bf16 v[208:211], a[140:143], v[52:55], v[208:211]\n"
"v_max_f32 v113, v113, v121\n"
"v_max_f32 v114, v114, v122\n"
"v_mfma_f32_16x16x32_bf16 v[116:119], v[116:119], v[144:147], 0\n"
"s_add_u32 m0, s34, 2048\n"
"ds_read_b64_tr_b16 v[120:121], v182 offset:27080\n"
"buffer_load_dwordx4 v190, s[8:11], s1 offen lds\n"
"v_mfma_f32_16x16x32_bf16 v[116:119], v[104:107], v[156:159], v[116:119]\n"
"v_max_f32 v115, v115, v123\n"
"v_cvt_pk_bf16_f32 v214, v208, v209\n"
"v_mfma_f32_16x16x32_bf16 v[204:207], a[72:75], v[32:35], 0\n"
"ds_read_b64_tr_b16 v[122:123], v183 offset:27080\n"
"v_cvt_pk_bf16_f32 v215, v210, v211\n"
"v_mfma_f32_16x16x32_bf16 v[204:207], a[84:87], v[36:39], v[204:207]\n"
"ds_write_b128 v186, v[212:215] offset:60832\n"
"v_mfma_f32_16x16x32_bf16 v[204:207], a[96:99], v[40:43], v[204:207]\n"
"s_waitcnt vmcnt(14)\n"
"v_readfirstlane_b32 s6, v201\n"
"v_max3_f32 v170, v170, v143, v112\n"
"v_mfma_f32_16x16x32_bf16 v[204:207], a[108:111], v[44:47], v[204:207]\n"
"s_and_b32 s6, s6, 0xffffff00\n"
"v_or_b32 v201, 5, s6\n"
"v_max3_f32 v170, v170, v113, v114\n"
"v_mfma_f32_16x16x32_bf16 v[204:207], a[120:123], v[48:51], v[204:207]\n"
"s_mov_b64 exec, 1\n"
"buffer_store_dword v201, off, s[28:31], s40 sc1\n"
"s_mov_b64 exec, -1\n"
"v_mfma_f32_16x16x32_bf16 v[204:207], a[132:135], v[52:55], v[204:207]\n"
"s_add_u32 m0, s34, 5120\n"
"v_max_f32 v116, v116, v124\n"
"v_max_f32 v117, v117, v125\n"
"v_mfma_f32_16x16x32_bf16 v[208:211], a[76:79], v[32:35], 0\n"
"buffer_load_dwordx4 v191, s[8:11], s1 offen lds\n"
"ds_read_b64_tr_b16 v[124:125], v182 offset:27848\n"
"v_mfma_f32_16x16x32_bf16 v[208:211], a[88:91], v[36:39], v[208:211]\n"
"s_add_u32 s1, s1, s33\n"
"v_max_f32 v118, v118, v126\n"
"v_max_f32 v119, v119, v127\n"
"v_mfma_f32_16x16x32_bf16 v[208:211], a[100:103], v[40:43], v[208:211]\n"
"ds_read_b64_tr_b16 v[126:127], v183 offset:27848\n"
"v_max3_f32 v170, v170, v115, v116\n"
"v_mfma_f32_16x16x32_bf16 v[208:211], a[112:115], v[44:47], v[208:211]\n"
"v_max3_f32 v170, v170, v117, v118\n"
"v_cvt_pk_bf16_f32 v204, v204, v205\n"
"v_mfma_f32_16x16x32_bf16 v[208:211], a[124:127], v[48:51], v[208:211]\n"
"v_cvt_pk_bf16_f32 v205, v206, v207\n"
"v_mov_b64 v[254:255], 0\n"
"v_mfma_f32_16x16x32_bf16 v[208:211], a[136:139], v[52:55], v[208:211]\n"
"v_mov_b64 v[98:99], 0\n"
"v_mov_b64 v[100:101], 0\n"
"v_mfma_f32_16x16x32_bf16 v[212:215], a[144:147], v[32:35], 0\n"
"v_mov_b64 v[106:107], 0\n"
"v_mov_b64 v[108:109], 0\n"
"v_mfma_f32_16x16x32_bf16 v[212:215], a[156:159], v[36:39], v[212:215]\n"
"v_max_f32 v171, v170, v119\n"
"v_max_f32 v170, v170, v119\n"
"v_mfma_f32_16x16x32_bf16 v[212:215], a[168:171], v[40:43], v[212:215]\n"
"s_nop 0\n"
"v_permlane16_swap_b32 v170, v171\n"
"v_mfma_f32_16x16x32_bf16 v[212:215], a[180:183], v[44:47], v[212:215]\n"
"v_max_f32 v170, v170, v171\n"
"v_mov_b32 v171, v170\n"
"v_mfma_f32_16x16x32_bf16 v[212:215], a[192:195], v[48:51], v[212:215]\n"
"s_nop 0\n"
"v_permlane32_swap_b32 v170, v171\n"
"v_max_f32 v170, v170, v171\n"
"v_mul_f32 v170, 0xbe553b95, v170\n"
"v_pk_fma_f32 v[136:137], v[136:137], s[70:71], v[170:171] op_sel_hi:[1,0,0]\n"
"v_pk_fma_f32 v[138:139], v[138:139], s[70:71], v[170:171] op_sel_hi:[1,0,0]\n"
"v_pk_fma_f32 v[140:141], v[140:141], s[70:71], v[170:171] op_sel_hi:[1,0,0]\n"
"v_pk_fma_f32 v[142:143], v[142:143], s[70:71], v[170:171] op_sel_hi:[1,0,0]\n"
"v_pk_fma_f32 v[112:113], v[112:113], s[70:71], v[170:171] op_sel_hi:[1,0,0]\n"
"v_pk_fma_f32 v[114:115], v[114:115], s[70:71], v[170:171] op_sel_hi:[1,0,0]\n"
"v_pk_fma_f32 v[116:117], v[116:117], s[70:71], v[170:171] op_sel_hi:[1,0,0]\n"
"v_pk_fma_f32 v[118:119], v[118:119], s[70:71], v[170:171] op_sel_hi:[1,0,0]\n"
"v_mfma_f32_16x16x32_bf16 v[212:215], a[204:207], v[52:55], v[212:215]\n"
"v_cvt_pk_bf16_f32 v206, v208, v209\n"
"v_cvt_pk_bf16_f32 v207, v210, v211\n"
"v_mfma_f32_16x16x32_bf16 v[208:211], a[148:151], v[32:35], 0\n"
"ds_write_b128 v186, v[204:207] offset:60896\n"
"v_mfma_f32_16x16x32_bf16 v[208:211], a[160:163], v[36:39], v[208:211]\n"
"v_exp_f32 v136, v136\n"
"v_mfma_f32_16x16x32_bf16 v[208:211], a[172:175], v[40:43], v[208:211]\n"
"v_exp_f32 v137, v137\n"
"v_mfma_f32_16x16x32_bf16 v[208:211], a[184:187], v[44:47], v[208:211]\n"
"v_exp_f32 v138, v138\n"
"v_mfma_f32_16x16x32_bf16 v[208:211], a[196:199], v[48:51], v[208:211]\n"
"s_waitcnt vmcnt(14)\n"
"v_exp_f32 v139, v139\n"
"v_mfma_f32_16x16x32_bf16 v[208:211], a[208:211], v[52:55], v[208:211]\n"
"ds_write_b128 v202, v[56:59] offset:55296\n"
"v_mfma_f32_16x16x32_bf16 v[204:207], a[152:155], v[32:35], 0\n"
"buffer_load_dwordx4 v[56:59], v192, s[28:31], s38 offen sc1\n"
"v_cvt_pk_bf16_f32 v82, v136, v138\n"
"v_mfma_f32_16x16x32_bf16 v[204:207], a[164:167], v[36:39], v[204:207]\n"
"s_waitcnt vmcnt(14)\n"
"v_cvt_pk_bf16_f32 v83, v137, v139\n"
"v_cvt_pk_bf16_f32 v144, v212, v213\n"
"v_mfma_f32_16x16x32_bf16 v[204:207], a[176:179], v[40:43], v[204:207]\n"
"ds_read_b128 v[32:35], v188 offset:6144\n"
"ds_read_b128 v[36:39], v189 offset:6144\n"
"v_mfma_f32_16x16x32_bf16 v[204:207], a[188:191], v[44:47], v[204:207]\n"
"ds_read_b128 v[40:43], v188 offset:7168\n"
"v_cvt_pk_bf16_f32 v145, v214, v215\n"
"v_mfma_f32_16x16x32_bf16 v[204:207], a[200:203], v[48:51], v[204:207]\n"
"s_waitcnt vmcnt(13)\n"
"ds_read_b128 v[44:47], v189 offset:7168\n"
"v_exp_f32 v140, v140\n"
"v_mfma_f32_16x16x32_bf16 v[204:207], a[212:215], v[52:55], v[204:207]\n"
"ds_read_b128 v[48:51], v188 offset:8192\n"
"ds_read_b128 v[52:55], v189 offset:8192\n"
"v_exp_f32 v141, v141\n"
"v_exp_f32 v142, v142\n"
"v_exp_f32 v143, v143\n"
"v_cvt_pk_bf16_f32 v84, v140, v142\n"
"v_cvt_pk_bf16_f32 v85, v141, v143\n"
"v_cvt_pk_bf16_f32 v146, v208, v209\n"
"v_cvt_pk_bf16_f32 v147, v210, v211\n"
"v_smfmac_f32_16x16x64_bf16 v[252:255], v[82:85], v[216:223], v63\n"
"s_waitcnt lgkmcnt(5)\n"
"ds_write_b64 v203, v[60:61] offset:55296\n"
"v_smfmac_f32_16x16x64_bf16 v[98:101], v[82:85], v[72:79], v63\n"
"s_waitcnt lgkmcnt(5)\n"
"buffer_load_dwordx2 v[60:61], v193, s[28:31], s38 offen offset:1024 sc1\n"
"v_smfmac_f32_16x16x64_bf16 v[106:109], v[82:85], v[128:135], v63\n"
"s_add_u32 s38, s38, 1572864\n"
"ds_read_b64_tr_b16 v[216:217], v182 offset:27136\n"
"v_mfma_f32_16x16x32_bf16 v[82:85], v[172:175], v[82:85], 0\n"
"s_waitcnt lgkmcnt(4)\n"
"ds_read_b64_tr_b16 v[218:219], v183 offset:27136\n"
"ds_read_b64_tr_b16 v[220:221], v182 offset:27904\n"
"v_mfma_f32_16x16x32_bf16 v[212:215], a[0:3], v[32:35], 0\n"
"s_add_u32 m0, s34, 6144\n"
"ds_read_b64_tr_b16 v[222:223], v183 offset:27904\n"
"buffer_load_dwordx4 v190, s[8:11], s32 offen lds\n"
"v_mfma_f32_16x16x32_bf16 v[212:215], a[12:15], v[36:39], v[212:215]\n"
"ds_read_b64_tr_b16 v[72:73], v182 offset:27144\n"
"v_cvt_pk_bf16_f32 v158, v204, v205\n"
"v_mfma_f32_16x16x32_bf16 v[212:215], a[24:27], v[40:43], v[212:215]\n"
"ds_read_b64_tr_b16 v[74:75], v183 offset:27144\n"
"v_cvt_pk_bf16_f32 v159, v206, v207\n"
"v_mfma_f32_16x16x32_bf16 v[212:215], a[36:39], v[44:47], v[212:215]\n"
"s_waitcnt vmcnt(12) lgkmcnt(6)\n"
"ds_read_b64_tr_b16 v[76:77], v182 offset:27912\n"
"v_exp_f32 v112, v112\n"
"v_mfma_f32_16x16x32_bf16 v[212:215], a[48:51], v[48:51], v[212:215]\n"
"s_barrier\n"
"v_mfma_f32_16x16x32_bf16 v[212:215], a[60:63], v[52:55], v[212:215]\n"
"ds_read_b64_tr_b16 v[78:79], v183 offset:27912\n"
"ds_read_b128 v[136:139], v178 offset:13504\n"
"v_mfma_f32_16x16x32_bf16 v[208:211], a[4:7], v[32:35], 0\n"
"ds_read_b128 v[128:131], v179 offset:13504\n"
"v_exp_f32 v113, v113\n"
"v_mfma_f32_16x16x32_bf16 v[208:211], a[16:19], v[36:39], v[208:211]\n"
"s_add_u32 m0, s34, 9216\n"
"ds_read_b128 v[140:143], v178 offset:27008\n"
"buffer_load_dwordx4 v191, s[8:11], s32 offen lds\n"
"v_mfma_f32_16x16x32_bf16 v[208:211], a[28:31], v[40:43], v[208:211]\n"
"s_add_u32 s32, s32, s33\n"
"ds_read_b128 v[132:135], v179 offset:27008\n"
"ds_read_b64_tr_b16 v[20:21], v236 offset:12288\n"
"v_mfma_f32_16x16x32_bf16 v[208:211], a[40:43], v[44:47], v[208:211]\n"
"v_exp_f32 v114, v114\n"
"v_mfma_f32_16x16x32_bf16 v[208:211], a[52:55], v[48:51], v[208:211]\n"
"v_exp_f32 v115, v115\n"
"v_mfma_f32_16x16x32_bf16 v[208:211], a[64:67], v[52:55], v[208:211]\n"
"v_cvt_pk_bf16_f32 v90, v112, v114\n"
"v_cvt_pk_bf16_f32 v91, v113, v115\n"
"v_mfma_f32_16x16x32_bf16 v[204:207], a[8:11], v[32:35], 0\n"
"ds_read_b128 v[112:115], v180\n"
"ds_read_b64_tr_b16 v[22:23], v236 offset:18432\n"
"v_mfma_f32_16x16x32_bf16 v[204:207], a[20:23], v[36:39], v[204:207]\n"
"v_exp_f32 v116, v116\n"
"v_mfma_f32_16x16x32_bf16 v[204:207], a[32:35], v[40:43], v[204:207]\n"
"v_exp_f32 v117, v117\n"
"v_mfma_f32_16x16x32_bf16 v[204:207], a[44:47], v[44:47], v[204:207]\n"
"s_add_u32 m0, s34, 7168\n"
"v_exp_f32 v118, v118\n"
"v_mfma_f32_16x16x32_bf16 v[204:207], a[56:59], v[48:51], v[204:207]\n"
"buffer_load_dwordx4 v190, s[8:11], s0 offen lds\n"
"ds_read_b64_tr_b16 v[24:25], v236 offset:12296\n"
"v_mfma_f32_16x16x32_bf16 v[204:207], a[68:71], v[52:55], v[204:207]\n"
"v_exp_f32 v119, v119\n"
"v_cvt_pk_bf16_f32 v92, v116, v118\n"
"v_cvt_pk_bf16_f32 v93, v117, v119\n"
"v_cvt_pk_bf16_f32 v212, v212, v213\n"
"v_cvt_pk_bf16_f32 v213, v214, v215\n"
"v_smfmac_f32_16x16x64_bf16 v[106:109], v[90:93], v[120:127], v63\n"
"s_waitcnt lgkmcnt(8)\n"
"ds_read_b128 v[120:123], v181\n"
"v_smfmac_f32_16x16x64_bf16 v[252:255], v[90:93], v[216:223], v63\n"
"ds_read_b128 v[116:119], v180 offset:13504\n"
"v_smfmac_f32_16x16x64_bf16 v[98:101], v[90:93], v[72:79], v63\n"
"ds_read_b128 v[124:127], v181 offset:13504\n"
"v_mfma_f32_16x16x32_bf16 v[90:93], v[172:175], v[90:93], v[82:85]\n"
"s_nop 0\n"
"v_cvt_pk_bf16_f32 v106, v106, v107\n"
"s_nop 1\n"
"v_cvt_pk_bf16_f32 v252, v252, v253\n"
"s_add_u32 m0, s34, 10240\n"
"v_cvt_pk_bf16_f32 v253, v254, v255\n"
"buffer_load_dwordx4 v191, s[8:11], s0 offen lds\n"
"s_add_u32 s0, s0, s33\n"
"v_rcp_f32 v171, v90\n"
"v_cvt_pk_bf16_f32 v98, v98, v99\n"
"v_cvt_pk_bf16_f32 v171, v171, v171\n"
"v_and_b32 v92, v171, v176\n"
"v_and_b32 v93, v171, v177\n"
"v_cvt_pk_bf16_f32 v99, v100, v101\n"
"v_cvt_pk_bf16_f32 v107, v108, v109\n"
"v_mfma_f32_16x16x16_bf16 v[252:255], v[252:253], v[92:93], 0\n"
"ds_read_b64 v[82:83], v178 offset:13568\n"
"ds_read_b64 v[84:85], v179 offset:13568\n"
"v_mfma_f32_16x16x16_bf16 v[98:101], v[98:99], v[92:93], 0\n"
"ds_read_b64 v[90:91], v178 offset:27072\n"
"v_cvt_pk_bf16_f32 v214, v208, v209\n"
"v_mfma_f32_16x16x16_bf16 v[106:109], v[106:107], v[92:93], 0\n"
"ds_read_b64 v[92:93], v179 offset:27072\n"
"s_nop 0\n"
"v_cvt_pk_bf16_f32 v252, v252, v253\n"
"v_cvt_pk_bf16_f32 v253, v254, v255\n"
"s_nop 0\n"
"v_cvt_pk_bf16_f32 v254, v98, v99\n"
"v_cvt_pk_bf16_f32 v255, v100, v101\n"
"buffer_store_dwordx4 v[252:255], v192, s[28:31], s37 offen sc1\n"
"ds_read_b64 v[98:99], v180 offset:64\n"
"ds_read_b64 v[100:101], v181 offset:64\n"
"ds_read_b64_tr_b16 v[26:27], v236 offset:18440\n"
"v_cvt_pk_bf16_f32 v106, v106, v107\n"
"v_cvt_pk_bf16_f32 v107, v108, v109\n"
"v_cvt_pk_bf16_f32 v215, v210, v211\n"
"v_mfma_f32_16x16x32_bf16 v[208:211], a[80:83], v[32:35], 0\n"
"s_waitcnt lgkmcnt(6)\n"
"buffer_store_dwordx2 v[106:107], v193, s[28:31], s37 offen offset:1024 sc1\n"
"v_mfma_f32_16x16x32_bf16 v[136:139], v[136:139], v[148:151], 0\n"
"s_waitcnt lgkmcnt(4)\n"
"ds_write_b128 v187, v[212:215]\n"
"v_mfma_f32_16x16x32_bf16 v[136:139], v[80:83], v[160:163], v[136:139]\n"
"s_waitcnt lgkmcnt(4)\n"
"v_cvt_pk_bf16_f32 v204, v204, v205\n"
"v_cvt_pk_bf16_f32 v205, v206, v207\n"
"v_mfma_f32_16x16x32_bf16 v[128:131], v[128:131], v[148:151], 0\n"
"s_add_u32 m0, s34, 8192\n"
"v_mov_b64 v[252:253], 0\n"
"v_mov_b64 v[254:255], 0\n"
"v_mfma_f32_16x16x32_bf16 v[128:131], v[84:87], v[162:165], v[128:131]\n"
"s_waitcnt lgkmcnt(3)\n"
"buffer_load_dwordx4 v190, s[8:11], s1 offen lds\n"
"ds_read_b64 v[106:107], v180 offset:13568\n"
"v_mfma_f32_16x16x32_bf16 v[140:143], v[140:143], v[148:151], 0\n"
"s_waitcnt lgkmcnt(3)\n"
"ds_read_b64 v[108:109], v181 offset:13568\n"
"ds_read_b64_tr_b16 v[28:29], v235 offset:12288\n"
"v_mfma_f32_16x16x32_bf16 v[140:143], v[88:91], v[160:163], v[140:143]\n"
"s_add_u32 s37, s37, 1572864\n"
"v_max_f32 v136, v136, v128\n"
"v_max_f32 v137, v137, v129\n"
"v_mfma_f32_16x16x32_bf16 v[132:135], v[132:135], v[148:151], 0\n"
"s_add_u32 s4, s37, 6291456\n"
"ds_read_b64_tr_b16 v[30:31], v235 offset:18432\n"
"v_max_f32 v138, v138, v130\n"
"v_mfma_f32_16x16x32_bf16 v[132:135], v[92:95], v[162:165], v[132:135]\n"
"v_max_f32 v139, v139, v131\n"
"v_max3_f32 v170, v136, v137, v138\n"
"v_mfma_f32_16x16x32_bf16 v[112:115], v[112:115], v[148:151], 0\n"
"ds_read_b64_tr_b16 v[128:129], v182 offset:13576\n"
"v_mfma_f32_16x16x32_bf16 v[112:115], v[96:99], v[160:163], v[112:115]\n"
"ds_read_b64_tr_b16 v[130:131], v183 offset:13576\n"
"v_mov_b64 v[98:99], 0\n"
"v_mfma_f32_16x16x32_bf16 v[120:123], v[120:123], v[148:151], 0\n"
"s_waitcnt lgkmcnt(5)\n"
"v_max_f32 v140, v140, v132\n"
"v_max_f32 v141, v141, v133\n"
"v_mfma_f32_16x16x32_bf16 v[120:123], v[100:103], v[162:165], v[120:123]\n"
"s_waitcnt lgkmcnt(4)\n"
"ds_read_b64_tr_b16 v[132:133], v182 offset:14344\n"
"v_max_f32 v142, v142, v134\n"
"v_mfma_f32_16x16x32_bf16 v[116:119], v[116:119], v[148:151], 0\n"
"s_add_u32 m0, s34, 11264\n"
"v_max_f32 v143, v143, v135\n"
"v_max3_f32 v170, v170, v139, v140\n"
"v_mfma_f32_16x16x32_bf16 v[116:119], v[104:107], v[160:163], v[116:119]\n"
"buffer_load_dwordx4 v191, s[8:11], s1 offen lds\n"
"ds_read_b64_tr_b16 v[134:135], v183 offset:14344\n"
"v_mfma_f32_16x16x32_bf16 v[124:127], v[124:127], v[148:151], 0\n"
"s_add_u32 s1, s1, s33\n"
"v_max3_f32 v170, v170, v141, v142\n"
"v_max_f32 v112, v112, v120\n"
"v_mfma_f32_16x16x32_bf16 v[124:127], v[108:111], v[162:165], v[124:127]\n"
"v_max_f32 v113, v113, v121\n"
"v_max_f32 v114, v114, v122\n"
"v_mfma_f32_16x16x32_bf16 v[208:211], a[92:95], v[36:39], v[208:211]\n"
"ds_read_b64_tr_b16 v[120:121], v182 offset:40584\n"
"v_max_f32 v115, v115, v123\n"
"v_mfma_f32_16x16x32_bf16 v[208:211], a[104:107], v[40:43], v[208:211]\n"
"ds_read_b64_tr_b16 v[122:123], v183 offset:40584\n"
"v_max3_f32 v170, v170, v143, v112\n"
"v_mfma_f32_16x16x32_bf16 v[208:211], a[116:119], v[44:47], v[208:211]\n"
"v_max3_f32 v170, v170, v113, v114\n"
"v_mov_b64 v[64:65], -1\n"
"v_mfma_f32_16x16x32_bf16 v[208:211], a[128:131], v[48:51], v[208:211]\n"
"buffer_load_dword v200, off, s[28:31], s39 sc1\n"
"v_mov_b64 v[66:67], -1\n"
"v_mfma_f32_16x16x32_bf16 v[208:211], a[140:143], v[52:55], v[208:211]\n"
"buffer_store_dwordx4 v[64:67], v192, s[28:31], s4 offen sc1\n"
"v_mfma_f32_16x16x32_bf16 v[212:215], a[72:75], v[32:35], 0\n"
"s_add_u32 s4, s37, 6291456\n"
"ds_read_b128 v[64:67], v192 offset:55296\n"
"v_max_f32 v116, v116, v124\n"
"v_mfma_f32_16x16x32_bf16 v[212:215], a[84:87], v[36:39], v[212:215]\n"
"v_max_f32 v117, v117, v125\n"
"v_max_f32 v118, v118, v126\n"
"v_mfma_f32_16x16x32_bf16 v[212:215], a[96:99], v[40:43], v[212:215]\n"
"ds_read_b64_tr_b16 v[124:125], v182 offset:41352\n"
"v_cvt_pk_bf16_f32 v206, v208, v209\n"
"v_mfma_f32_16x16x32_bf16 v[212:215], a[108:111], v[44:47], v[212:215]\n"
"v_cvt_pk_bf16_f32 v207, v210, v211\n"
"v_max_f32 v119, v119, v127\n"
"v_mfma_f32_16x16x32_bf16 v[212:215], a[120:123], v[48:51], v[212:215]\n"
"ds_write_b128 v187, v[204:207] offset:64\n"
"v_mfma_f32_16x16x32_bf16 v[212:215], a[132:135], v[52:55], v[212:215]\n"
"v_max3_f32 v170, v170, v115, v116\n"
"v_max3_f32 v170, v170, v117, v118\n"
"v_mfma_f32_16x16x32_bf16 v[208:211], a[76:79], v[32:35], 0\n"
"s_waitcnt lgkmcnt(2)\n"
"v_mov_b64 v[100:101], 0\n"
"v_mov_b64 v[106:107], 0\n"
"v_mfma_f32_16x16x32_bf16 v[208:211], a[88:91], v[36:39], v[208:211]\n"
"ds_read_b64_tr_b16 v[126:127], v183 offset:41352\n"
"v_cvt_pk_bf16_f32 v212, v212, v213\n"
"v_mfma_f32_16x16x32_bf16 v[208:211], a[100:103], v[40:43], v[208:211]\n"
"v_cvt_pk_bf16_f32 v213, v214, v215\n"
"v_mov_b64 v[108:109], 0\n"
"v_mfma_f32_16x16x32_bf16 v[208:211], a[112:115], v[44:47], v[208:211]\n"
"v_max_f32 v171, v170, v119\n"
"v_max_f32 v170, v170, v119\n"
"v_mfma_f32_16x16x32_bf16 v[208:211], a[124:127], v[48:51], v[208:211]\n"
"s_nop 0\n"
"v_permlane16_swap_b32 v170, v171\n"
"v_mfma_f32_16x16x32_bf16 v[208:211], a[136:139], v[52:55], v[208:211]\n"
"v_max_f32 v170, v170, v171\n"
"v_mov_b32 v171, v170\n"
"v_mfma_f32_16x16x32_bf16 v[232:235], a[216:219], v[64:67], 0\n"
"s_nop 0\n"
"v_permlane32_swap_b32 v170, v171\n"
"v_max_f32 v170, v170, v171\n"
"v_mul_f32 v170, 0xbe553b95, v170\n"
"v_pk_fma_f32 v[136:137], v[136:137], s[70:71], v[170:171] op_sel_hi:[1,0,0]\n"
"v_pk_fma_f32 v[138:139], v[138:139], s[70:71], v[170:171] op_sel_hi:[1,0,0]\n"
"v_pk_fma_f32 v[140:141], v[140:141], s[70:71], v[170:171] op_sel_hi:[1,0,0]\n"
"v_pk_fma_f32 v[142:143], v[142:143], s[70:71], v[170:171] op_sel_hi:[1,0,0]\n"
"v_pk_fma_f32 v[112:113], v[112:113], s[70:71], v[170:171] op_sel_hi:[1,0,0]\n"
"v_pk_fma_f32 v[114:115], v[114:115], s[70:71], v[170:171] op_sel_hi:[1,0,0]\n"
"v_pk_fma_f32 v[116:117], v[116:117], s[70:71], v[170:171] op_sel_hi:[1,0,0]\n"
"v_pk_fma_f32 v[118:119], v[118:119], s[70:71], v[170:171] op_sel_hi:[1,0,0]\n"
"v_mfma_f32_16x16x32_bf16 v[236:239], a[220:223], v[64:67], 0\n"
"v_cvt_pk_bf16_f32 v214, v208, v209\n"
"v_cvt_pk_bf16_f32 v215, v210, v211\n"
"v_mfma_f32_16x16x32_bf16 v[240:243], a[224:227], v[64:67], 0\n"
"ds_read_b128 v[64:67], v192 offset:57344\n"
"ds_write_b128 v187, v[212:215] offset:128\n"
"v_mfma_f32_16x16x32_bf16 v[204:207], a[144:147], v[32:35], 0\n"
"v_exp_f32 v136, v136\n"
"v_mfma_f32_16x16x32_bf16 v[204:207], a[156:159], v[36:39], v[204:207]\n"
"v_exp_f32 v137, v137\n"
"v_mfma_f32_16x16x32_bf16 v[204:207], a[168:171], v[40:43], v[204:207]\n"
"v_exp_f32 v138, v138\n"
"v_mfma_f32_16x16x32_bf16 v[204:207], a[180:183], v[44:47], v[204:207]\n"
"v_exp_f32 v139, v139\n"
"v_mfma_f32_16x16x32_bf16 v[204:207], a[192:195], v[48:51], v[204:207]\n"
"v_cvt_pk_bf16_f32 v82, v136, v138\n"
"v_cvt_pk_bf16_f32 v83, v137, v139\n"
"v_mfma_f32_16x16x32_bf16 v[204:207], a[204:207], v[52:55], v[204:207]\n"
"ds_read_b128 v[136:139], v178 offset:27008\n"
"v_exp_f32 v140, v140\n"
"v_mfma_f32_16x16x32_bf16 v[208:211], a[148:151], v[32:35], 0\n"
"v_mov_b64 v[68:69], -1\n"
"v_exp_f32 v141, v141\n"
"v_mfma_f32_16x16x32_bf16 v[208:211], a[160:163], v[36:39], v[208:211]\n"
"buffer_store_dwordx2 v[68:69], v193, s[28:31], s4 offen offset:1024 sc1\n"
"v_mfma_f32_16x16x32_bf16 v[208:211], a[172:175], v[40:43], v[208:211]\n"
"ds_read_b128 v[68:71], v192 offset:56320\n"
"v_cvt_pk_bf16_f32 v148, v204, v205\n"
"v_mfma_f32_16x16x32_bf16 v[208:211], a[184:187], v[44:47], v[208:211]\n"
"v_exp_f32 v142, v142\n"
"v_mfma_f32_16x16x32_bf16 v[208:211], a[196:199], v[48:51], v[208:211]\n"
"v_exp_f32 v143, v143\n"
"v_mfma_f32_16x16x32_bf16 v[208:211], a[208:211], v[52:55], v[208:211]\n"
"v_cvt_pk_bf16_f32 v84, v140, v142\n"
"v_cvt_pk_bf16_f32 v85, v141, v143\n"
"v_mfma_f32_16x16x32_bf16 v[212:215], a[152:155], v[32:35], 0\n"
"s_waitcnt vmcnt(20)\n"
"ds_read_b128 v[32:35], v188\n"
"ds_read_b128 v[140:143], v180\n"
"v_mfma_f32_16x16x32_bf16 v[212:215], a[164:167], v[36:39], v[212:215]\n"
"ds_read_b128 v[36:39], v189\n"
"v_cvt_pk_bf16_f32 v149, v206, v207\n"
"v_mfma_f32_16x16x32_bf16 v[212:215], a[176:179], v[40:43], v[212:215]\n"
"s_waitcnt vmcnt(16)\n"
"ds_read_b128 v[40:43], v188 offset:1024\n"
"v_cvt_pk_bf16_f32 v150, v208, v209\n"
"v_mfma_f32_16x16x32_bf16 v[212:215], a[188:191], v[44:47], v[212:215]\n"
"s_waitcnt lgkmcnt(4)\n"
"ds_read_b128 v[44:47], v189 offset:1024\n"
"v_cvt_pk_bf16_f32 v151, v210, v211\n"
"v_mfma_f32_16x16x32_bf16 v[232:235], a[228:231], v[68:71], v[232:235]\n"
"v_exp_f32 v112, v112\n"
"v_mfma_f32_16x16x32_bf16 v[232:235], a[240:243], v[64:67], v[232:235]\n"
"v_exp_f32 v113, v113\n"
"v_mfma_f32_16x16x32_bf16 v[236:239], a[232:235], v[68:71], v[236:239]\n"
"v_exp_f32 v114, v114\n"
"v_mfma_f32_16x16x32_bf16 v[236:239], a[244:247], v[64:67], v[236:239]\n"
"s_waitcnt lgkmcnt(4)\n"
"v_exp_f32 v115, v115\n"
"v_mfma_f32_16x16x32_bf16 v[240:243], a[236:239], v[68:71], v[240:243]\n"
"s_waitcnt vmcnt(12)\n"
"ds_read_b128 v[68:71], v192 offset:58368\n"
"ds_write_b128 v202, v[56:59] offset:36864\n"
"v_mfma_f32_16x16x32_bf16 v[240:243], a[248:251], v[64:67], v[240:243]\n"
"s_waitcnt lgkmcnt(4)\n"
"v_cvt_pk_bf16_f32 v90, v112, v114\n"
"v_cvt_pk_bf16_f32 v91, v113, v115\n"
"v_smfmac_f32_16x16x64_bf16 v[252:255], v[82:85], v[224:231], v63\n"
"s_waitcnt lgkmcnt(2)\n"
"ds_read_b128 v[64:67], v192 offset:59392\n"
"v_smfmac_f32_16x16x64_bf16 v[98:101], v[82:85], v[244:251], v63\n"
"s_add_u32 m0, s34, 0\n"
"s_nop 0\n"
"buffer_load_dwordx4 v190, s[8:11], s32 offen lds\n"
"v_smfmac_f32_16x16x64_bf16 v[106:109], v[82:85], v[128:135], v63\n"
"ds_read_b64_tr_b16 v[224:225], v182 offset:40640\n"
"v_mfma_f32_16x16x32_bf16 v[82:85], v[172:175], v[82:85], 0\n"
"ds_read_b64_tr_b16 v[226:227], v183 offset:40640\n"
"ds_read_b64_tr_b16 v[228:229], v182 offset:41408\n"
"v_mfma_f32_16x16x32_bf16 v[212:215], a[200:203], v[48:51], v[212:215]\n"
"s_waitcnt lgkmcnt(5)\n"
"ds_read_b128 v[48:51], v188 offset:2048\n"
"v_exp_f32 v116, v116\n"
"v_mfma_f32_16x16x32_bf16 v[212:215], a[212:215], v[52:55], v[212:215]\n"
"ds_read_b128 v[52:55], v189 offset:2048\n"
"v_exp_f32 v117, v117\n"
"v_mfma_f32_16x16x32_bf16 v[232:235], a[252:255], v[68:71], v[232:235]\n"
"ds_read_b64_tr_b16 v[230:231], v183 offset:41408\n"
"ds_read_b64_tr_b16 v[244:245], v182 offset:40648\n"
"v_mfma_f32_16x16x32_bf16 v[236:239], v[0:3], v[68:71], v[236:239]\n"
"s_waitcnt vmcnt(12) lgkmcnt(7)\n"
"v_exp_f32 v118, v118\n"
"v_mfma_f32_16x16x32_bf16 v[236:239], v[12:15], v[64:67], v[236:239]\n"
"s_add_u32 m0, s34, 3072\n"
"ds_write_b64 v203, v[60:61] offset:36864\n"
"v_mfma_f32_16x16x32_bf16 v[240:243], v[4:7], v[68:71], v[240:243]\n"
"buffer_load_dwordx4 v191, s[8:11], s32 offen lds\n"
"v_cvt_pk_bf16_f32 v92, v116, v118\n"
"v_mfma_f32_16x16x32_bf16 v[240:243], v[16:19], v[64:67], v[240:243]\n"
"s_add_u32 s32, s32, s33\n"
"ds_read_b128 v[68:71], v192 offset:60416\n"
"ds_read_b64_tr_b16 v[246:247], v183 offset:40648\n"
"v_mfma_f32_16x16x32_bf16 v[232:235], v[8:11], v[64:67], v[232:235]\n"
"ds_read_b64_tr_b16 v[248:249], v182 offset:41416\n"
"v_cvt_pk_bf16_f32 v162, v212, v213\n"
"v_mfma_f32_16x16x32_bf16 v[204:207], a[0:3], v[32:35], 0\n"
"ds_read_b64_tr_b16 v[250:251], v183 offset:41416\n"
"v_cvt_pk_bf16_f32 v163, v214, v215\n"
"v_mfma_f32_16x16x32_bf16 v[204:207], a[12:15], v[36:39], v[204:207]\n"
"ds_read_b128 v[128:131], v179 offset:27008\n"
"v_exp_f32 v119, v119\n"
"v_mfma_f32_16x16x32_bf16 v[204:207], a[24:27], v[40:43], v[204:207]\n"
".Lic4%=:\n"
"s_cbranch_execz .Lib1%=\n"
"ds_read_b128 v[132:135], v181\n"
"ds_read_b128 v[112:115], v180 offset:13504\n"
"v_mfma_f32_16x16x32_bf16 v[204:207], a[36:39], v[44:47], v[204:207]\n"
"s_waitcnt lgkmcnt(11)\n"
"v_cvt_pk_bf16_f32 v93, v117, v119\n"
"v_mfma_f32_16x16x32_bf16 v[204:207], a[48:51], v[48:51], v[204:207]\n"
"s_waitcnt lgkmcnt(9)\n"
"s_add_u32 m0, s34, 1024\n"
"v_mfma_f32_16x16x32_bf16 v[204:207], a[60:63], v[52:55], v[204:207]\n"
"s_waitcnt lgkmcnt(6)\n"
"buffer_load_dwordx4 v190, s[8:11], s0 offen lds\n"
"v_mfma_f32_16x16x32_bf16 v[232:235], v[20:23], v[68:71], v[232:235]\n"
"v_mfma_f32_16x16x32_bf16 v[240:243], v[28:31], v[68:71], v[240:243]\n"
"v_mfma_f32_16x16x32_bf16 v[236:239], v[24:27], v[68:71], v[236:239]\n"
"s_waitcnt lgkmcnt(3)\n"
"v_smfmac_f32_16x16x64_bf16 v[252:255], v[90:93], v[224:231], v63\n"
"v_smfmac_f32_16x16x64_bf16 v[98:101], v[90:93], v[244:251], v63\n"
"v_cvt_pk_bf16_f32 v204, v204, v205\n"
"v_smfmac_f32_16x16x64_bf16 v[106:109], v[90:93], v[120:127], v63\n"
"ds_read_b128 v[120:123], v181 offset:13504\n"
"v_mfma_f32_16x16x32_bf16 v[90:93], v[172:175], v[90:93], v[82:85]\n"
"v_cvt_pk_bf16_f32 v232, v232, v233\n"
"v_cvt_pk_bf16_f32 v233, v234, v235\n"
"v_mfma_f32_16x16x32_bf16 v[208:211], a[4:7], v[32:35], 0\n"
"s_add_u32 m0, s34, 4096\n"
"ds_read_b64 v[82:83], v178 offset:27072\n"
"buffer_load_dwordx4 v191, s[8:11], s0 offen lds\n"
"v_mfma_f32_16x16x32_bf16 v[208:211], a[16:19], v[36:39], v[208:211]\n"
"s_add_u32 s0, s0, s33\n"
"ds_read_b64 v[84:85], v179 offset:27072\n"
"v_cvt_pk_bf16_f32 v240, v240, v241\n"
"v_mfma_f32_16x16x32_bf16 v[208:211], a[28:31], v[40:43], v[208:211]\n"
"v_cvt_pk_bf16_f32 v234, v236, v237\n"
"v_cvt_pk_bf16_f32 v235, v238, v239\n"
"v_mfma_f32_16x16x32_bf16 v[208:211], a[40:43], v[44:47], v[208:211]\n"
"ds_write_b128 v196, v[232:235] offset:30720\n"
"v_mfma_f32_16x16x32_bf16 v[208:211], a[52:55], v[48:51], v[208:211]\n"
"v_cvt_pk_bf16_f32 v241, v242, v243\n"
"v_cvt_pk_bf16_f32 v252, v252, v253\n"
"v_mfma_f32_16x16x32_bf16 v[208:211], a[64:67], v[52:55], v[208:211]\n"
"v_rcp_f32 v171, v90\n"
"v_mfma_f32_16x16x32_bf16 v[212:215], a[8:11], v[32:35], 0\n"
"s_waitcnt lgkmcnt(2)\n"
"ds_write_b64 v197, v[240:241] offset:30720\n"
"v_mfma_f32_16x16x32_bf16 v[136:139], v[136:139], v[152:155], 0\n"
"s_waitcnt lgkmcnt(2)\n"
"v_cvt_pk_bf16_f32 v171, v171, v171\n"
"v_and_b32 v92, v171, v176\n"
"v_mfma_f32_16x16x32_bf16 v[136:139], v[80:83], v[164:167], v[136:139]\n"
"s_add_u32 m0, s34, 2048\n"
"ds_read_b64 v[90:91], v180 offset:64\n"
"buffer_load_dwordx4 v190, s[8:11], s1 offen lds\n"
"v_mfma_f32_16x16x32_bf16 v[128:131], v[128:131], v[152:155], 0\n"
"v_and_b32 v93, v171, v177\n"
"v_cvt_pk_bf16_f32 v253, v254, v255\n"
"v_mfma_f32_16x16x32_bf16 v[128:131], v[84:87], v[166:169], v[128:131]\n"
"v_cvt_pk_bf16_f32 v106, v106, v107\n"
"v_cvt_pk_bf16_f32 v98, v98, v99\n"
"v_mfma_f32_16x16x16_bf16 v[252:255], v[252:253], v[92:93], 0\n"
"v_cvt_pk_bf16_f32 v107, v108, v109\n"
"v_cvt_pk_bf16_f32 v99, v100, v101\n"
"v_mfma_f32_16x16x32_bf16 v[212:215], a[20:23], v[36:39], v[212:215]\n"
"v_cvt_pk_bf16_f32 v205, v206, v207\n"
"v_cvt_pk_bf16_f32 v206, v208, v209\n"
"v_mfma_f32_16x16x16_bf16 v[106:109], v[106:107], v[92:93], 0\n"
"s_waitcnt lgkmcnt(1)\n"
"v_cvt_pk_bf16_f32 v207, v210, v211\n"
"v_cvt_pk_bf16_f32 v252, v252, v253\n"
"v_mfma_f32_16x16x16_bf16 v[98:101], v[98:99], v[92:93], 0\n"
"s_barrier\n"
"v_mfma_f32_16x16x32_bf16 v[212:215], a[32:35], v[40:43], v[212:215]\n"
"s_waitcnt lgkmcnt(0)\n"
"ds_read_b64 v[92:93], v181 offset:64\n"
"ds_read_b128 v[116:119], v180 offset:27008\n"
"v_mfma_f32_16x16x32_bf16 v[212:215], a[44:47], v[44:47], v[212:215]\n"
"s_add_u32 m0, s34, 5120\n"
"ds_write_b128 v186, v[204:207] offset:6752\n"
"v_mfma_f32_16x16x32_bf16 v[212:215], a[56:59], v[48:51], v[212:215]\n"
"buffer_load_dwordx4 v191, s[8:11], s1 offen lds\n"
"v_cvt_pk_bf16_f32 v253, v254, v255\n"
"v_mfma_f32_16x16x32_bf16 v[212:215], a[68:71], v[52:55], v[212:215]\n"
"s_add_u32 s1, s1, s33\n"
"v_cvt_pk_bf16_f32 v106, v106, v107\n"
"v_cvt_pk_bf16_f32 v254, v98, v99\n"
"v_mfma_f32_16x16x32_bf16 v[140:143], v[140:143], v[152:155], 0\n"
"ds_read_b64 v[98:99], v180 offset:13568\n"
"ds_read_b128 v[124:127], v181 offset:27008\n"
"v_mfma_f32_16x16x32_bf16 v[140:143], v[88:91], v[164:167], v[140:143]\n"
"s_waitcnt vmcnt(8)\n"
"v_cvt_pk_bf16_f32 v255, v100, v101\n"
"v_cvt_pk_bf16_f32 v107, v108, v109\n"
"v_mfma_f32_16x16x32_bf16 v[208:211], a[80:83], v[32:35], 0\n"
"s_waitcnt lgkmcnt(4)\n"
"v_add3_u32 v200, v200, s50, -5\n"
"v_cmp_lt_u32 vcc, 64, v200\n"
"v_mfma_f32_16x16x32_bf16 v[132:135], v[132:135], v[152:155], 0\n"
"buffer_store_dwordx4 v[252:255], v192, s[28:31], s37 offen sc1\n"
"v_mfma_f32_16x16x32_bf16 v[132:135], v[92:95], v[166:169], v[132:135]\n"
"s_nop 1\n"
"s_cbranch_vccnz .Lp4%=\n"
".Lr4%=:\n"
"v_mfma_f32_16x16x32_bf16 v[208:211], a[92:95], v[36:39], v[208:211]\n"
"ds_read_b64 v[100:101], v181 offset:13568\n"
"buffer_load_dwordx4 v[56:59], v192, s[28:31], s38 offen sc1\n"
"v_mfma_f32_16x16x32_bf16 v[208:211], a[104:107], v[40:43], v[208:211]\n"
"s_waitcnt lgkmcnt(2)\n"
"ds_read_b64 v[108:109], v181 offset:27072\n"
"v_cvt_pk_bf16_f32 v212, v212, v213\n"
"v_mfma_f32_16x16x32_bf16 v[112:115], v[112:115], v[152:155], 0\n"
"v_cvt_pk_bf16_f32 v213, v214, v215\n"
"v_max_f32 v136, v136, v128\n"
"v_mfma_f32_16x16x32_bf16 v[112:115], v[96:99], v[164:167], v[112:115]\n"
"v_max_f32 v137, v137, v129\n"
"v_max_f32 v138, v138, v130\n"
"v_mfma_f32_16x16x32_bf16 v[208:211], a[116:119], v[44:47], v[208:211]\n"
"ds_read_b64_tr_b16 v[128:129], v182 offset:27080\n"
"buffer_load_dwordx2 v[60:61], v193, s[28:31], s38 offen offset:1024 sc1\n"
"v_mfma_f32_16x16x32_bf16 v[208:211], a[128:131], v[48:51], v[208:211]\n"
"s_add_u32 s38, s38, 1572864\n"
"v_max_f32 v139, v139, v131\n"
"v_max3_f32 v170, v136, v137, v138\n"
"v_mfma_f32_16x16x32_bf16 v[208:211], a[140:143], v[52:55], v[208:211]\n"
"ds_read_b64_tr_b16 v[130:131], v183 offset:27080\n"
"v_max_f32 v140, v140, v132\n"
"v_mfma_f32_16x16x32_bf16 v[204:207], a[72:75], v[32:35], 0\n"
"s_waitcnt lgkmcnt(3)\n"
"v_max_f32 v141, v141, v133\n"
"v_max_f32 v142, v142, v134\n"
"v_mfma_f32_16x16x32_bf16 v[120:123], v[120:123], v[152:155], 0\n"
"s_waitcnt lgkmcnt(2)\n"
"ds_read_b64_tr_b16 v[132:133], v182 offset:27848\n"
"buffer_store_dwordx2 v[106:107], v193, s[28:31], s37 offen offset:1024 sc1\n"
"v_mfma_f32_16x16x32_bf16 v[120:123], v[100:103], v[166:169], v[120:123]\n"
"s_add_u32 s37, s37, 1572864\n"
"ds_read_b64 v[106:107], v180 offset:27072\n"
"v_cvt_pk_bf16_f32 v214, v208, v209\n"
"v_mfma_f32_16x16x32_bf16 v[124:127], v[124:127], v[152:155], 0\n"
"s_add_u32 s4, s37, 6291456\n"
"v_cvt_pk_bf16_f32 v215, v210, v211\n"
"v_max_f32 v143, v143, v135\n"
"v_mfma_f32_16x16x32_bf16 v[124:127], v[108:111], v[166:169], v[124:127]\n"
"ds_write_b128 v186, v[212:215] offset:6816\n"
"v_mfma_f32_16x16x32_bf16 v[204:207], a[84:87], v[36:39], v[204:207]\n"
"v_max3_f32 v170, v170, v139, v140\n"
"v_max3_f32 v170, v170, v141, v142\n"
"v_mfma_f32_16x16x32_bf16 v[204:207], a[96:99], v[40:43], v[204:207]\n"
"v_max_f32 v112, v112, v120\n"
"v_max_f32 v113, v113, v121\n"
"v_mfma_f32_16x16x32_bf16 v[204:207], a[108:111], v[44:47], v[204:207]\n"
"ds_read_b64_tr_b16 v[134:135], v183 offset:27848\n"
"ds_read_b64_tr_b16 v[120:121], v182 offset:54088\n"
"v_mfma_f32_16x16x32_bf16 v[204:207], a[120:123], v[48:51], v[204:207]\n"
"s_waitcnt lgkmcnt(3)\n"
"v_max_f32 v114, v114, v122\n"
"v_max_f32 v115, v115, v123\n"
"v_mfma_f32_16x16x32_bf16 v[116:119], v[116:119], v[152:155], 0\n"
"ds_read_b64_tr_b16 v[122:123], v183 offset:54088\n"
"v_max3_f32 v170, v170, v143, v112\n"
"v_mfma_f32_16x16x32_bf16 v[116:119], v[104:107], v[164:167], v[116:119]\n"
"v_max3_f32 v170, v170, v113, v114\n"
"v_mov_b64 v[252:253], 0\n"
"v_mfma_f32_16x16x32_bf16 v[204:207], a[132:135], v[52:55], v[204:207]\n"
"v_mov_b64 v[254:255], 0\n"
"v_mov_b64 v[98:99], 0\n"
"v_mfma_f32_16x16x32_bf16 v[208:211], a[76:79], v[32:35], 0\n"
"v_mov_b64 v[100:101], 0\n"
"v_mov_b64 v[106:107], 0\n"
"v_mfma_f32_16x16x32_bf16 v[208:211], a[88:91], v[36:39], v[208:211]\n"
"v_max_f32 v116, v116, v124\n"
"v_max_f32 v117, v117, v125\n"
"v_mfma_f32_16x16x32_bf16 v[208:211], a[100:103], v[40:43], v[208:211]\n"
"ds_read_b64_tr_b16 v[124:125], v182 offset:54856\n"
"v_max_f32 v118, v118, v126\n"
"v_mfma_f32_16x16x32_bf16 v[208:211], a[112:115], v[44:47], v[208:211]\n"
"v_mov_b64 v[64:65], -1\n"
"v_mov_b64 v[66:67], -1\n"
"v_mfma_f32_16x16x32_bf16 v[208:211], a[124:127], v[48:51], v[208:211]\n"
"buffer_store_dwordx4 v[64:67], v192, s[28:31], s4 offen sc1\n"
"v_mfma_f32_16x16x32_bf16 v[208:211], a[136:139], v[52:55], v[208:211]\n"
"s_add_u32 s4, s37, 6291456\n"
"ds_read_b128 v[64:67], v202 offset:30720\n"
"v_max_f32 v119, v119, v127\n"
"v_mfma_f32_16x16x32_bf16 v[212:215], a[144:147], v[32:35], 0\n"
"ds_read_b64_tr_b16 v[126:127], v183 offset:54856\n"
"v_max3_f32 v170, v170, v115, v116\n"
"v_mfma_f32_16x16x32_bf16 v[212:215], a[156:159], v[36:39], v[212:215]\n"
"v_max3_f32 v170, v170, v117, v118\n"
"v_cvt_pk_bf16_f32 v204, v204, v205\n"
"v_mfma_f32_16x16x32_bf16 v[212:215], a[168:171], v[40:43], v[212:215]\n"
"v_cvt_pk_bf16_f32 v205, v206, v207\n"
"v_mov_b64 v[108:109], 0\n"
"v_mfma_f32_16x16x32_bf16 v[212:215], a[180:183], v[44:47], v[212:215]\n"
"v_max_f32 v171, v170, v119\n"
"v_max_f32 v170, v170, v119\n"
"v_mfma_f32_16x16x32_bf16 v[212:215], a[192:195], v[48:51], v[212:215]\n"
"s_nop 0\n"
"v_permlane16_swap_b32 v170, v171\n"
"v_mfma_f32_16x16x32_bf16 v[212:215], a[204:207], v[52:55], v[212:215]\n"
"v_max_f32 v170, v170, v171\n"
"v_mov_b32 v171, v170\n"
"v_cvt_pk_bf16_f32 v206, v208, v209\n"
"v_cvt_pk_bf16_f32 v207, v210, v211\n"
"v_mfma_f32_16x16x32_bf16 v[208:211], a[148:151], v[32:35], 0\n"
"s_waitcnt lgkmcnt(1)\n"
"buffer_store_dwordx4 v[64:67], v194, s[24:27], s36 offen sc1\n"
"v_mfma_f32_16x16x32_bf16 v[208:211], a[160:163], v[36:39], v[208:211]\n"
"s_nop 0\n"
"ds_read_b128 v[64:67], v192 offset:36864\n"
"ds_write_b128 v186, v[204:207] offset:6880\n"
"v_mfma_f32_16x16x32_bf16 v[208:211], a[172:175], v[40:43], v[208:211]\n"
"v_permlane32_swap_b32 v170, v171\n"
"v_max_f32 v170, v170, v171\n"
"v_mul_f32 v170, 0xbe553b95, v170\n"
"v_pk_fma_f32 v[136:137], v[136:137], s[70:71], v[170:171] op_sel_hi:[1,0,0]\n"
"v_pk_fma_f32 v[138:139], v[138:139], s[70:71], v[170:171] op_sel_hi:[1,0,0]\n"
"v_pk_fma_f32 v[140:141], v[140:141], s[70:71], v[170:171] op_sel_hi:[1,0,0]\n"
"v_pk_fma_f32 v[142:143], v[142:143], s[70:71], v[170:171] op_sel_hi:[1,0,0]\n"
"v_pk_fma_f32 v[112:113], v[112:113], s[70:71], v[170:171] op_sel_hi:[1,0,0]\n"
"v_pk_fma_f32 v[114:115], v[114:115], s[70:71], v[170:171] op_sel_hi:[1,0,0]\n"
"v_pk_fma_f32 v[116:117], v[116:117], s[70:71], v[170:171] op_sel_hi:[1,0,0]\n"
"v_pk_fma_f32 v[118:119], v[118:119], s[70:71], v[170:171] op_sel_hi:[1,0,0]\n"
"v_mfma_f32_16x16x32_bf16 v[208:211], a[184:187], v[44:47], v[208:211]\n"
"v_exp_f32 v136, v136\n"
"v_mfma_f32_16x16x32_bf16 v[208:211], a[196:199], v[48:51], v[208:211]\n"
"v_exp_f32 v137, v137\n"
"v_mfma_f32_16x16x32_bf16 v[208:211], a[208:211], v[52:55], v[208:211]\n"
"s_waitcnt lgkmcnt(1)\n"
"v_exp_f32 v138, v138\n"
"v_mfma_f32_16x16x32_bf16 v[232:235], a[216:219], v[64:67], 0\n"
"v_exp_f32 v139, v139\n"
"v_mfma_f32_16x16x32_bf16 v[236:239], a[220:223], v[64:67], 0\n"
"v_cvt_pk_bf16_f32 v82, v136, v138\n"
"v_cvt_pk_bf16_f32 v83, v137, v139\n"
"v_mfma_f32_16x16x32_bf16 v[240:243], a[224:227], v[64:67], 0\n"
"ds_read_b128 v[64:67], v192 offset:38912\n"
"ds_read_b128 v[136:139], v180\n"
"v_mfma_f32_16x16x32_bf16 v[204:207], a[152:155], v[32:35], 0\n"
"ds_read_b128 v[32:35], v188 offset:6144\n"
"v_cvt_pk_bf16_f32 v152, v212, v213\n"
"v_mfma_f32_16x16x32_bf16 v[204:207], a[164:167], v[36:39], v[204:207]\n"
"ds_read_b128 v[36:39], v189 offset:6144\n"
"v_cvt_pk_bf16_f32 v153, v214, v215\n"
"v_mfma_f32_16x16x32_bf16 v[204:207], a[176:179], v[40:43], v[204:207]\n"
"ds_read_b128 v[40:43], v188 offset:7168\n"
"v_exp_f32 v140, v140\n"
"v_mfma_f32_16x16x32_bf16 v[204:207], a[188:191], v[44:47], v[204:207]\n"
"ds_read_b128 v[44:47], v189 offset:7168\n"
"v_exp_f32 v141, v141\n"
"v_mfma_f32_16x16x32_bf16 v[204:207], a[200:203], v[48:51], v[204:207]\n"
"ds_read_b128 v[48:51], v188 offset:8192\n"
"v_mov_b64 v[68:69], -1\n"
"v_mfma_f32_16x16x32_bf16 v[204:207], a[212:215], v[52:55], v[204:207]\n"
"buffer_store_dwordx2 v[68:69], v193, s[28:31], s4 offen offset:1024 sc1\n"
"ds_read_b128 v[52:55], v189 offset:8192\n"
"ds_read_b64 v[68:69], v62 offset:30720\n"
"v_exp_f32 v142, v142\n"
"v_exp_f32 v143, v143\n"
"v_cvt_pk_bf16_f32 v84, v140, v142\n"
"v_cvt_pk_bf16_f32 v85, v141, v143\n"
"v_cvt_pk_bf16_f32 v154, v208, v209\n"
"v_exp_f32 v112, v112\n"
"v_smfmac_f32_16x16x64_bf16 v[252:255], v[82:85], v[216:223], v63\n"
"s_waitcnt lgkmcnt(5)\n"
"ds_read_b64_tr_b16 v[216:217], v182 offset:54144\n"
"v_smfmac_f32_16x16x64_bf16 v[98:101], v[82:85], v[72:79], v63\n"
"s_add_u32 m0, s34, 6144\n"
"s_nop 0\n"
"buffer_load_dwordx4 v190, s[8:11], s32 offen lds\n"
"v_smfmac_f32_16x16x64_bf16 v[106:109], v[82:85], v[128:135], v63\n"
"ds_read_b64_tr_b16 v[218:219], v183 offset:54144\n"
"v_mfma_f32_16x16x32_bf16 v[82:85], v[172:175], v[82:85], 0\n"
"ds_read_b64_tr_b16 v[220:221], v182 offset:54912\n"
"ds_read_b64_tr_b16 v[222:223], v183 offset:54912\n"
"v_mfma_f32_16x16x32_bf16 v[212:215], a[0:3], v[32:35], 0\n"
"s_waitcnt lgkmcnt(4)\n"
"ds_read_b64_tr_b16 v[72:73], v182 offset:54152\n"
"v_cvt_pk_bf16_f32 v155, v210, v211\n"
"v_mfma_f32_16x16x32_bf16 v[212:215], a[12:15], v[36:39], v[212:215]\n"
"buffer_store_dwordx2 v[68:69], v195, s[24:27], s36 offen sc1\n"
"v_mfma_f32_16x16x32_bf16 v[212:215], a[24:27], v[40:43], v[212:215]\n"
"s_add_u32 s36, s36, s33\n"
"ds_read_b128 v[68:71], v192 offset:37888\n"
"ds_read_b64_tr_b16 v[74:75], v183 offset:54152\n"
"v_mfma_f32_16x16x32_bf16 v[212:215], a[36:39], v[44:47], v[212:215]\n"
"ds_read_b64_tr_b16 v[76:77], v182 offset:54920\n"
"v_cvt_pk_bf16_f32 v166, v204, v205\n"
"v_mfma_f32_16x16x32_bf16 v[212:215], a[48:51], v[48:51], v[212:215]\n"
"ds_read_b64_tr_b16 v[78:79], v183 offset:54920\n"
"v_cvt_pk_bf16_f32 v167, v206, v207\n"
"v_mfma_f32_16x16x32_bf16 v[212:215], a[60:63], v[52:55], v[212:215]\n"
"s_add_u32 m0, s34, 9216\n"
"ds_read_b128 v[128:131], v181\n"
"buffer_load_dwordx4 v191, s[8:11], s32 offen lds\n"
"v_mfma_f32_16x16x32_bf16 v[208:211], a[4:7], v[32:35], 0\n"
"s_add_u32 s32, s32, s33\n"
"ds_read_b128 v[140:143], v180 offset:13504\n"
"v_exp_f32 v113, v113\n"
"v_mfma_f32_16x16x32_bf16 v[208:211], a[16:19], v[36:39], v[208:211]\n"
"ds_read_b128 v[132:135], v181 offset:13504\n"
"v_exp_f32 v114, v114\n"
"v_mfma_f32_16x16x32_bf16 v[208:211], a[28:31], v[40:43], v[208:211]\n"
"v_exp_f32 v115, v115\n"
"v_mfma_f32_16x16x32_bf16 v[208:211], a[40:43], v[44:47], v[208:211]\n"
"s_waitcnt lgkmcnt(6)\n"
"v_cvt_pk_bf16_f32 v90, v112, v114\n"
"v_cvt_pk_bf16_f32 v91, v113, v115\n"
"v_mfma_f32_16x16x32_bf16 v[232:235], a[228:231], v[68:71], v[232:235]\n"
"ds_read_b128 v[112:115], v180 offset:27008\n"
"v_exp_f32 v116, v116\n"
"v_mfma_f32_16x16x32_bf16 v[232:235], a[240:243], v[64:67], v[232:235]\n"
"v_exp_f32 v117, v117\n"
"v_mfma_f32_16x16x32_bf16 v[236:239], a[232:235], v[68:71], v[236:239]\n"
"s_add_u32 m0, s34, 7168\n"
"v_exp_f32 v118, v118\n"
"v_mfma_f32_16x16x32_bf16 v[236:239], a[244:247], v[64:67], v[236:239]\n"
"buffer_load_dwordx4 v190, s[8:11], s0 offen lds\n"
"v_cvt_pk_bf16_f32 v92, v116, v118\n"
"v_mfma_f32_16x16x32_bf16 v[240:243], a[236:239], v[68:71], v[240:243]\n"
"ds_read_b128 v[68:71], v192 offset:39936\n"
"v_exp_f32 v119, v119\n"
"v_mfma_f32_16x16x32_bf16 v[240:243], a[248:251], v[64:67], v[240:243]\n"
"ds_read_b128 v[64:67], v192 offset:40960\n"
"v_cvt_pk_bf16_f32 v93, v117, v119\n"
"v_mfma_f32_16x16x32_bf16 v[208:211], a[52:55], v[48:51], v[208:211]\n"
"s_waitcnt lgkmcnt(6)\n"
"v_cvt_pk_bf16_f32 v212, v212, v213\n"
"v_cvt_pk_bf16_f32 v213, v214, v215\n"
"v_smfmac_f32_16x16x64_bf16 v[252:255], v[90:93], v[216:223], v63\n"
"s_mov_b64 exec, s[44:45]\n"
"v_add_u32 v179, 0xc0, v179\n"
"s_mov_b64 exec, -1\n"
"v_smfmac_f32_16x16x64_bf16 v[98:101], v[90:93], v[72:79], v63\n"
"v_smfmac_f32_16x16x64_bf16 v[106:109], v[90:93], v[120:127], v63\n"
"ds_read_b128 v[120:123], v181 offset:27008\n"
"v_mfma_f32_16x16x32_bf16 v[90:93], v[172:175], v[90:93], v[82:85]\n"
"s_waitcnt lgkmcnt(2)\n"
"v_cvt_pk_bf16_f32 v252, v252, v253\n"
"v_cvt_pk_bf16_f32 v253, v254, v255\n"
"v_mfma_f32_16x16x32_bf16 v[232:235], a[252:255], v[68:71], v[232:235]\n"
"s_add_u32 m0, s34, 10240\n"
"ds_read_b64 v[82:83], v180 offset:64\n"
"buffer_load_dwordx4 v191, s[8:11], s0 offen lds\n"
"v_mfma_f32_16x16x32_bf16 v[236:239], v[0:3], v[68:71], v[236:239]\n"
"s_waitcnt lgkmcnt(2)\n"
"ds_read_b64 v[84:85], v181 offset:64\n"
"v_cvt_pk_bf16_f32 v98, v98, v99\n"
"v_mfma_f32_16x16x32_bf16 v[236:239], v[12:15], v[64:67], v[236:239]\n"
"s_add_u32 s0, s0, s33\n"
"v_rcp_f32 v171, v90\n"
"v_mfma_f32_16x16x32_bf16 v[240:243], v[4:7], v[68:71], v[240:243]\n"
"ds_read_b128 v[68:71], v192 offset:41984\n"
"ds_read_b64 v[90:91], v180 offset:13568\n"
"v_mfma_f32_16x16x32_bf16 v[240:243], v[16:19], v[64:67], v[240:243]\n"
"v_cvt_pk_bf16_f32 v171, v171, v171\n"
"v_and_b32 v92, v171, v176\n"
"v_mfma_f32_16x16x32_bf16 v[232:235], v[8:11], v[64:67], v[232:235]\n"
"v_and_b32 v93, v171, v177\n"
"v_cvt_pk_bf16_f32 v99, v100, v101\n"
"v_mfma_f32_16x16x32_bf16 v[208:211], a[64:67], v[52:55], v[208:211]\n"
"v_cvt_pk_bf16_f32 v106, v106, v107\n"
"v_cvt_pk_bf16_f32 v107, v108, v109\n"
"v_mfma_f32_16x16x16_bf16 v[252:255], v[252:253], v[92:93], 0\n"
"s_waitcnt lgkmcnt(3)\n"
"s_add_u32 m0, s34, 8192\n"
"v_mfma_f32_16x16x16_bf16 v[98:101], v[98:99], v[92:93], 0\n"
"s_waitcnt lgkmcnt(2)\n"
"buffer_load_dwordx4 v190, s[8:11], s1 offen lds\n"
"v_cvt_pk_bf16_f32 v214, v208, v209\n"
"v_mfma_f32_16x16x16_bf16 v[106:109], v[106:107], v[92:93], 0\n"
"ds_read_b64 v[92:93], v181 offset:13568\n"
"v_cvt_pk_bf16_f32 v252, v252, v253\n"
"v_mfma_f32_16x16x32_bf16 v[136:139], v[136:139], v[144:147], 0\n"
"s_waitcnt lgkmcnt(2)\n"
"v_cvt_pk_bf16_f32 v253, v254, v255\n"
"v_cvt_pk_bf16_f32 v254, v98, v99\n"
"v_mfma_f32_16x16x32_bf16 v[232:235], v[20:23], v[68:71], v[232:235]\n"
"s_waitcnt lgkmcnt(1)\n"
"ds_read_b64 v[98:99], v180 offset:27072\n"
"v_cvt_pk_bf16_f32 v255, v100, v101\n"
"v_mfma_f32_16x16x32_bf16 v[240:243], v[28:31], v[68:71], v[240:243]\n"
"buffer_store_dwordx4 v[252:255], v192, s[28:31], s37 offen sc1\n"
"v_mfma_f32_16x16x32_bf16 v[236:239], v[24:27], v[68:71], v[236:239]\n"
"ds_read_b64 v[100:101], v181 offset:27072\n"
"v_cvt_pk_bf16_f32 v106, v106, v107\n"
"v_mfma_f32_16x16x32_bf16 v[128:131], v[128:131], v[144:147], 0\n"
"v_cvt_pk_bf16_f32 v232, v232, v233\n"
"v_cvt_pk_bf16_f32 v233, v234, v235\n"
"v_mfma_f32_16x16x32_bf16 v[128:131], v[84:87], v[158:161], v[128:131]\n"
"s_waitcnt lgkmcnt(2)\n"
"v_cvt_pk_bf16_f32 v240, v240, v241\n"
"v_cvt_pk_bf16_f32 v234, v236, v237\n"
"v_mfma_f32_16x16x32_bf16 v[136:139], v[80:83], v[156:159], v[136:139]\n"
"s_add_u32 m0, s34, 11264\n"
"v_cvt_pk_bf16_f32 v235, v238, v239\n"
"v_cvt_pk_bf16_f32 v241, v242, v243\n"
"v_mfma_f32_16x16x32_bf16 v[140:143], v[140:143], v[144:147], 0\n"
"s_waitcnt lgkmcnt(1)\n"
"buffer_load_dwordx4 v191, s[8:11], s1 offen lds\n"
"ds_write_b128 v196, v[232:235] offset:55296\n"
"v_mfma_f32_16x16x32_bf16 v[140:143], v[88:91], v[156:159], v[140:143]\n"
"s_add_u32 s1, s1, s33\n"
"v_cvt_pk_bf16_f32 v107, v108, v109\n"
"v_cvt_pk_bf16_f32 v215, v210, v211\n"
"v_mfma_f32_16x16x32_bf16 v[132:135], v[132:135], v[144:147], 0\n"
"s_waitcnt lgkmcnt(1)\n"
"buffer_store_dwordx2 v[106:107], v193, s[28:31], s37 offen offset:1024 sc1\n"
"v_mfma_f32_16x16x32_bf16 v[132:135], v[92:95], v[158:161], v[132:135]\n"
"s_add_u32 s37, s37, 1572864\n"
"ds_write_b64 v197, v[240:241] offset:55296\n"
"v_mfma_f32_16x16x32_bf16 v[112:115], v[112:115], v[144:147], 0\n"
"s_add_u32 s4, s37, 6291456\n"
"v_max_f32 v136, v136, v128\n"
"v_max_f32 v137, v137, v129\n"
"v_mfma_f32_16x16x32_bf16 v[112:115], v[96:99], v[156:159], v[112:115]\n"
"ds_read_b64_tr_b16 v[128:129], v182 offset:40584\n"
"v_max_f32 v138, v138, v130\n"
"v_mfma_f32_16x16x32_bf16 v[120:123], v[120:123], v[144:147], 0\n"
"v_max_f32 v139, v139, v131\n"
"v_max3_f32 v170, v136, v137, v138\n"
"v_mfma_f32_16x16x32_bf16 v[120:123], v[100:103], v[158:161], v[120:123]\n"
"ds_read_b64_tr_b16 v[130:131], v183 offset:40584\n"
"v_max_f32 v140, v140, v132\n"
"v_mfma_f32_16x16x32_bf16 v[204:207], a[8:11], v[32:35], 0\n"
"v_max_f32 v141, v141, v133\n"
"v_max_f32 v142, v142, v134\n"
"v_mfma_f32_16x16x32_bf16 v[204:207], a[20:23], v[36:39], v[204:207]\n"
"ds_read_b64_tr_b16 v[132:133], v182 offset:41352\n"
"v_max_f32 v143, v143, v135\n"
"v_mfma_f32_16x16x32_bf16 v[204:207], a[32:35], v[40:43], v[204:207]\n"
"s_waitcnt vmcnt(14)\n"
"ds_read_b64_tr_b16 v[134:135], v183 offset:41352\n"
"v_max3_f32 v170, v170, v139, v140\n"
"v_mfma_f32_16x16x32_bf16 v[204:207], a[44:47], v[44:47], v[204:207]\n"
"ds_write_b128 v202, v[56:59] offset:43008\n"
"v_mfma_f32_16x16x32_bf16 v[204:207], a[56:59], v[48:51], v[204:207]\n"
"buffer_load_dwordx4 v[56:59], v192, s[28:31], s38 offen sc1\n"
"v_max3_f32 v170, v170, v141, v142\n"
"v_mfma_f32_16x16x32_bf16 v[204:207], a[68:71], v[52:55], v[204:207]\n"
"v_max_f32 v112, v112, v120\n"
"v_max_f32 v113, v113, v121\n"
"v_mfma_f32_16x16x32_bf16 v[208:211], a[80:83], v[32:35], 0\n"
"s_waitcnt vmcnt(13)\n"
"v_or_b32 v201, 8, s6\n"
"v_max_f32 v114, v114, v122\n"
"v_mfma_f32_16x16x32_bf16 v[208:211], a[92:95], v[36:39], v[208:211]\n"
"s_mov_b64 exec, 1\n"
"buffer_store_dword v201, off, s[28:31], s40 sc1\n"
"s_mov_b64 exec, -1\n"
"v_mfma_f32_16x16x32_bf16 v[208:211], a[104:107], v[40:43], v[208:211]\n"
"ds_write_b64 v203, v[60:61] offset:43008\n"
"v_mfma_f32_16x16x32_bf16 v[208:211], a[116:119], v[44:47], v[208:211]\n"
"buffer_load_dwordx2 v[60:61], v193, s[28:31], s38 offen offset:1024 sc1\n"
"v_mov_b64 v[64:65], -1\n"
"v_mfma_f32_16x16x32_bf16 v[208:211], a[128:131], v[48:51], v[208:211]\n"
"s_add_u32 s38, s38, 1572864\n"
"v_mov_b64 v[66:67], -1\n"
"v_cvt_pk_bf16_f32 v204, v204, v205\n"
"v_mfma_f32_16x16x32_bf16 v[208:211], a[140:143], v[52:55], v[208:211]\n"
"v_cvt_pk_bf16_f32 v205, v206, v207\n"
"v_max_f32 v115, v115, v123\n"
"v_max3_f32 v170, v170, v143, v112\n"
"v_max3_f32 v170, v170, v113, v114\n"
"v_mov_b64 v[252:253], 0\n"
"v_mov_b64 v[254:255], 0\n"
"v_mov_b64 v[98:99], 0\n"
"buffer_store_dwordx4 v[64:67], v192, s[28:31], s4 offen sc1\n"
"s_add_u32 s4, s37, 6291456\n"
"v_cvt_pk_bf16_f32 v206, v208, v209\n"
"s_waitcnt lgkmcnt(0)\n"
"s_barrier\n"
"ds_write_b128 v186, v[212:215] offset:20256\n"
"v_mfma_f32_16x16x32_bf16 v[212:215], a[72:75], v[32:35], 0\n"
"v_cvt_pk_bf16_f32 v207, v210, v211\n"
"v_mov_b64 v[100:101], 0\n"
"v_mfma_f32_16x16x32_bf16 v[212:215], a[84:87], v[36:39], v[212:215]\n"
"ds_read_b128 v[116:119], v178\n"
"v_mfma_f32_16x16x32_bf16 v[212:215], a[96:99], v[40:43], v[212:215]\n"
"ds_read_b64 v[106:107], v178 offset:64\n"
"ds_read_b128 v[124:127], v179\n"
"v_mfma_f32_16x16x32_bf16 v[212:215], a[108:111], v[44:47], v[212:215]\n"
"ds_read_b64 v[108:109], v179 offset:64\n"
"v_mfma_f32_16x16x32_bf16 v[212:215], a[120:123], v[48:51], v[212:215]\n"
"ds_read_b128 v[64:67], v202 offset:55296\n"
"v_mfma_f32_16x16x32_bf16 v[212:215], a[132:135], v[52:55], v[212:215]\n"
"ds_write_b128 v186, v[204:207] offset:20320\n"
"v_mfma_f32_16x16x32_bf16 v[208:211], a[76:79], v[32:35], 0\n"
"v_mfma_f32_16x16x32_bf16 v[208:211], a[88:91], v[36:39], v[208:211]\n"
"ds_read_b64_tr_b16 v[120:121], v184 offset:72\n"
"v_mfma_f32_16x16x32_bf16 v[208:211], a[100:103], v[40:43], v[208:211]\n"
"ds_read_b64_tr_b16 v[122:123], v185 offset:72\n"
"v_mfma_f32_16x16x32_bf16 v[208:211], a[112:115], v[44:47], v[208:211]\n"
"s_waitcnt lgkmcnt(6)\n"
"v_cvt_pk_bf16_f32 v212, v212, v213\n"
"v_cvt_pk_bf16_f32 v213, v214, v215\n"
"v_mfma_f32_16x16x32_bf16 v[116:119], v[116:119], v[144:147], 0\n"
"v_mfma_f32_16x16x32_bf16 v[116:119], v[104:107], v[156:159], v[116:119]\n"
"s_waitcnt lgkmcnt(4)\n"
"v_mov_b64 v[106:107], 0\n"
"v_mfma_f32_16x16x32_bf16 v[124:127], v[124:127], v[144:147], 0\n"
"s_waitcnt lgkmcnt(3)\n"
"buffer_store_dwordx4 v[64:67], v194, s[24:27], s36 offen sc1\n"
"v_mfma_f32_16x16x32_bf16 v[124:127], v[108:111], v[158:161], v[124:127]\n"
"s_nop 0\n"
"ds_read_b128 v[64:67], v192 offset:43008\n"
"v_mov_b64 v[108:109], 0\n"
"v_mfma_f32_16x16x32_bf16 v[208:211], a[124:127], v[48:51], v[208:211]\n"
"v_mfma_f32_16x16x32_bf16 v[208:211], a[136:139], v[52:55], v[208:211]\n"
"v_mfma_f32_16x16x32_bf16 v[204:207], a[144:147], v[32:35], 0\n"
"v_mfma_f32_16x16x32_bf16 v[204:207], a[156:159], v[36:39], v[204:207]\n"
"v_mfma_f32_16x16x32_bf16 v[204:207], a[168:171], v[40:43], v[204:207]\n"
"v_max_f32 v116, v116, v124\n"
"v_mfma_f32_16x16x32_bf16 v[204:207], a[180:183], v[44:47], v[204:207]\n"
"v_max_f32 v117, v117, v125\n"
"v_max_f32 v118, v118, v126\n"
"v_mfma_f32_16x16x32_bf16 v[204:207], a[192:195], v[48:51], v[204:207]\n"
"s_waitcnt lgkmcnt(0)\n"
"ds_read_b64_tr_b16 v[124:125], v184 offset:840\n"
"v_mov_b64 v[68:69], -1\n"
"v_mfma_f32_16x16x32_bf16 v[232:235], a[216:219], v[64:67], 0\n"
"buffer_store_dwordx2 v[68:69], v193, s[28:31], s4 offen offset:1024 sc1\n"
"v_mfma_f32_16x16x32_bf16 v[236:239], a[220:223], v[64:67], 0\n"
"ds_read_b64 v[68:69], v62 offset:55296\n"
"v_max_f32 v119, v119, v127\n"
"v_mfma_f32_16x16x32_bf16 v[240:243], a[224:227], v[64:67], 0\n"
"ds_read_b128 v[64:67], v192 offset:45056\n"
"ds_read_b64_tr_b16 v[126:127], v185 offset:840\n"
"v_mfma_f32_16x16x32_bf16 v[204:207], a[204:207], v[52:55], v[204:207]\n"
"v_max3_f32 v170, v170, v115, v116\n"
"v_max3_f32 v170, v170, v117, v118\n"
"v_max_f32 v171, v170, v119\n"
"v_max_f32 v170, v170, v119\n"
"v_cvt_pk_bf16_f32 v214, v208, v209\n"
"v_cvt_pk_bf16_f32 v215, v210, v211\n"
"v_mfma_f32_16x16x32_bf16 v[208:211], a[148:151], v[32:35], 0\n"
"ds_write_b128 v186, v[212:215] offset:20384\n"
"v_mfma_f32_16x16x32_bf16 v[208:211], a[160:163], v[36:39], v[208:211]\n"
"v_permlane16_swap_b32 v170, v171\n"
"v_mfma_f32_16x16x32_bf16 v[208:211], a[172:175], v[40:43], v[208:211]\n"
"s_waitcnt lgkmcnt(3)\n"
"buffer_store_dwordx2 v[68:69], v195, s[24:27], s36 offen sc1\n"
"v_mfma_f32_16x16x32_bf16 v[208:211], a[184:187], v[44:47], v[208:211]\n"
"s_add_u32 s36, s36, s33\n"
"ds_read_b128 v[68:71], v192 offset:44032\n"
"v_max_f32 v170, v170, v171\n"
"v_mfma_f32_16x16x32_bf16 v[208:211], a[196:199], v[48:51], v[208:211]\n"
"v_mov_b32 v171, v170\n"
"v_cvt_pk_bf16_f32 v144, v204, v205\n"
"v_mfma_f32_16x16x32_bf16 v[208:211], a[208:211], v[52:55], v[208:211]\n"
"v_permlane32_swap_b32 v170, v171\n"
"v_max_f32 v170, v170, v171\n"
"v_mul_f32 v170, 0xbe553b95, v170\n"
"v_pk_fma_f32 v[136:137], v[136:137], s[70:71], v[170:171] op_sel_hi:[1,0,0]\n"
"v_pk_fma_f32 v[138:139], v[138:139], s[70:71], v[170:171] op_sel_hi:[1,0,0]\n"
"v_pk_fma_f32 v[140:141], v[140:141], s[70:71], v[170:171] op_sel_hi:[1,0,0]\n"
"v_pk_fma_f32 v[142:143], v[142:143], s[70:71], v[170:171] op_sel_hi:[1,0,0]\n"
"v_pk_fma_f32 v[112:113], v[112:113], s[70:71], v[170:171] op_sel_hi:[1,0,0]\n"
"v_pk_fma_f32 v[114:115], v[114:115], s[70:71], v[170:171] op_sel_hi:[1,0,0]\n"
"v_pk_fma_f32 v[116:117], v[116:117], s[70:71], v[170:171] op_sel_hi:[1,0,0]\n"
"v_pk_fma_f32 v[118:119], v[118:119], s[70:71], v[170:171] op_sel_hi:[1,0,0]\n"
"v_mfma_f32_16x16x32_bf16 v[212:215], a[152:155], v[32:35], 0\n"
"ds_read_b128 v[32:35], v188\n"
"v_exp_f32 v136, v136\n"
"v_mfma_f32_16x16x32_bf16 v[212:215], a[164:167], v[36:39], v[212:215]\n"
"s_waitcnt lgkmcnt(1)\n"
"ds_read_b128 v[36:39], v189\n"
"v_cvt_pk_bf16_f32 v145, v206, v207\n"
"v_mfma_f32_16x16x32_bf16 v[232:235], a[228:231], v[68:71], v[232:235]\n"
"v_exp_f32 v137, v137\n"
"v_mfma_f32_16x16x32_bf16 v[232:235], a[240:243], v[64:67], v[232:235]\n"
"v_exp_f32 v138, v138\n"
"v_mfma_f32_16x16x32_bf16 v[236:239], a[232:235], v[68:71], v[236:239]\n"
"v_exp_f32 v139, v139\n"
"v_mfma_f32_16x16x32_bf16 v[236:239], a[244:247], v[64:67], v[236:239]\n"
"v_cvt_pk_bf16_f32 v82, v136, v138\n"
"v_cvt_pk_bf16_f32 v83, v137, v139\n"
"v_mfma_f32_16x16x32_bf16 v[240:243], a[236:239], v[68:71], v[240:243]\n"
"ds_read_b128 v[68:71], v192 offset:46080\n"
"ds_read_b128 v[136:139], v180 offset:13504\n"
"v_mfma_f32_16x16x32_bf16 v[240:243], a[248:251], v[64:67], v[240:243]\n"
"s_waitcnt lgkmcnt(3)\n"
"ds_read_b128 v[64:67], v192 offset:47104\n"
"v_exp_f32 v140, v140\n"
"v_mfma_f32_16x16x32_bf16 v[212:215], a[176:179], v[40:43], v[212:215]\n"
"s_waitcnt lgkmcnt(3)\n"
"ds_read_b128 v[40:43], v188 offset:1024\n"
"v_exp_f32 v141, v141\n"
"v_mfma_f32_16x16x32_bf16 v[212:215], a[188:191], v[44:47], v[212:215]\n"
"s_add_u32 m0, s34, 0\n"
"ds_read_b128 v[44:47], v189 offset:1024\n"
"buffer_load_dwordx4 v190, s[8:11], s32 offen lds\n"
"v_mfma_f32_16x16x32_bf16 v[212:215], a[200:203], v[48:51], v[212:215]\n"
"s_add_u32 s32, s32, s33\n"
"ds_read_b128 v[48:51], v188 offset:2048\n"
"v_exp_f32 v142, v142\n"
"v_mfma_f32_16x16x32_bf16 v[212:215], a[212:215], v[52:55], v[212:215]\n"
".Lic5%=:\n"
"s_cbranch_execz .Lib1%=\n"
"ds_read_b128 v[52:55], v189 offset:2048\n"
"v_cvt_pk_bf16_f32 v84, v140, v142\n"
"v_mfma_f32_16x16x32_bf16 v[204:207], a[0:3], v[32:35], 0\n"
"s_waitcnt lgkmcnt(6)\n"
"v_exp_f32 v143, v143\n"
"v_mfma_f32_16x16x32_bf16 v[232:235], a[252:255], v[68:71], v[232:235]\n"
"v_cvt_pk_bf16_f32 v85, v141, v143\n"
"v_cvt_pk_bf16_f32 v146, v208, v209\n"
"v_mfma_f32_16x16x32_bf16 v[236:239], v[0:3], v[68:71], v[236:239]\n"
"s_waitcnt lgkmcnt(4)\n"
"ds_read_b128 v[140:143], v180 offset:27008\n"
"v_cvt_pk_bf16_f32 v147, v210, v211\n"
"v_mfma_f32_16x16x32_bf16 v[236:239], v[12:15], v[64:67], v[236:239]\n"
"v_exp_f32 v112, v112\n"
"v_smfmac_f32_16x16x64_bf16 v[252:255], v[82:85], v[224:231], v63\n"
"ds_read_b64_tr_b16 v[224:225], v184 offset:128\n"
"v_smfmac_f32_16x16x64_bf16 v[98:101], v[82:85], v[244:251], v63\n"
"s_waitcnt lgkmcnt(4)\n"
"ds_read_b64_tr_b16 v[226:227], v185 offset:128\n"
"v_mfma_f32_16x16x32_bf16 v[240:243], v[4:7], v[68:71], v[240:243]\n"
"s_waitcnt lgkmcnt(3)\n"
"ds_read_b128 v[68:71], v192 offset:48128\n"
"ds_read_b64_tr_b16 v[228:229], v184 offset:896\n"
"v_mfma_f32_16x16x32_bf16 v[240:243], v[16:19], v[64:67], v[240:243]\n"
"s_add_u32 m0, s34, 3072\n"
"ds_read_b64_tr_b16 v[230:231], v185 offset:896\n"
"buffer_load_dwordx4 v199, s[8:11], s75 offen lds\n"
"v_smfmac_f32_16x16x64_bf16 v[106:109], v[82:85], v[128:135], v63\n"
"ds_read_b64_tr_b16 v[244:245], v184 offset:136\n"
"v_mfma_f32_16x16x32_bf16 v[82:85], v[172:175], v[82:85], 0\n"
"ds_read_b64_tr_b16 v[246:247], v185 offset:136\n"
"v_cvt_pk_bf16_f32 v158, v212, v213\n"
"v_mfma_f32_16x16x32_bf16 v[232:235], v[8:11], v[64:67], v[232:235]\n"
"ds_read_b64_tr_b16 v[248:249], v184 offset:904\n"
"v_cvt_pk_bf16_f32 v159, v214, v215\n"
"v_mfma_f32_16x16x32_bf16 v[208:211], a[4:7], v[32:35], 0\n"
"ds_read_b64_tr_b16 v[250:251], v185 offset:904\n"
"v_exp_f32 v113, v113\n"
"v_mfma_f32_16x16x32_bf16 v[208:211], a[16:19], v[36:39], v[208:211]\n"
"ds_read_b128 v[128:131], v181 offset:13504\n"
"v_exp_f32 v114, v114\n"
"v_mfma_f32_16x16x32_bf16 v[208:211], a[28:31], v[40:43], v[208:211]\n"
"s_waitcnt lgkmcnt(7)\n"
"ds_read_b128 v[132:135], v181 offset:27008\n"
"v_cvt_pk_bf16_f32 v90, v112, v114\n"
"v_mfma_f32_16x16x32_bf16 v[208:211], a[40:43], v[44:47], v[208:211]\n"
"s_add_u32 m0, s34, 1024\n"
"v_exp_f32 v115, v115\n"
"v_mfma_f32_16x16x32_bf16 v[232:235], v[20:23], v[68:71], v[232:235]\n"
"buffer_load_dwordx4 v190, s[8:11], s0 offen lds\n"
"v_cvt_pk_bf16_f32 v91, v113, v115\n"
"v_mfma_f32_16x16x32_bf16 v[240:243], v[28:31], v[68:71], v[240:243]\n"
"s_add_u32 s0, s0, s33\n"
"ds_read_b128 v[112:115], v178\n"
"v_exp_f32 v116, v116\n"
"v_mfma_f32_16x16x32_bf16 v[236:239], v[24:27], v[68:71], v[236:239]\n"
"v_exp_f32 v117, v117\n"
"v_mfma_f32_16x16x32_bf16 v[204:207], a[12:15], v[36:39], v[204:207]\n"
"v_cvt_pk_bf16_f32 v232, v232, v233\n"
"v_cvt_pk_bf16_f32 v233, v234, v235\n"
"v_mfma_f32_16x16x32_bf16 v[204:207], a[24:27], v[40:43], v[204:207]\n"
"v_cvt_pk_bf16_f32 v240, v240, v241\n"
"v_cvt_pk_bf16_f32 v241, v242, v243\n"
"v_mfma_f32_16x16x32_bf16 v[204:207], a[36:39], v[44:47], v[204:207]\n"
"ds_write_b64 v197, v[240:241] offset:49152\n"
"v_mfma_f32_16x16x32_bf16 v[204:207], a[48:51], v[48:51], v[204:207]\n"
"v_cvt_pk_bf16_f32 v234, v236, v237\n"
"v_cvt_pk_bf16_f32 v235, v238, v239\n"
"v_mfma_f32_16x16x32_bf16 v[204:207], a[60:63], v[52:55], v[204:207]\n"
"s_add_u32 m0, s34, 4096\n"
"ds_write_b128 v196, v[232:235] offset:49152\n"
"v_mfma_f32_16x16x32_bf16 v[208:211], a[52:55], v[48:51], v[208:211]\n"
"buffer_load_dwordx4 v199, s[8:11], s76 offen lds\n"
"v_exp_f32 v118, v118\n"
"v_mfma_f32_16x16x32_bf16 v[208:211], a[64:67], v[52:55], v[208:211]\n"
"v_exp_f32 v119, v119\n"
"v_mfma_f32_16x16x32_bf16 v[212:215], a[8:11], v[32:35], 0\n"
"v_cvt_pk_bf16_f32 v92, v116, v118\n"
"v_cvt_pk_bf16_f32 v93, v117, v119\n"
"v_mfma_f32_16x16x32_bf16 v[212:215], a[20:23], v[36:39], v[212:215]\n"
"s_waitcnt lgkmcnt(5)\n"
"v_cvt_pk_bf16_f32 v204, v204, v205\n"
"v_cvt_pk_bf16_f32 v205, v206, v207\n"
"v_mfma_f32_16x16x32_bf16 v[212:215], a[32:35], v[40:43], v[212:215]\n"
"v_cvt_pk_bf16_f32 v206, v208, v209\n"
"v_cvt_pk_bf16_f32 v207, v210, v211\n"
"v_mfma_f32_16x16x32_bf16 v[212:215], a[44:47], v[44:47], v[212:215]\n"
"v_mfma_f32_16x16x32_bf16 v[212:215], a[56:59], v[48:51], v[212:215]\n"
"v_mfma_f32_16x16x32_bf16 v[212:215], a[68:71], v[52:55], v[212:215]\n"
"s_add_u32 m0, s34, 2048\n"
"s_nop 0\n"
"buffer_load_dwordx4 v190, s[8:11], s1 offen lds\n"
"v_smfmac_f32_16x16x64_bf16 v[252:255], v[90:93], v[224:231], v63\n"
"s_add_u32 s1, s1, s33\n"
"v_smfmac_f32_16x16x64_bf16 v[98:101], v[90:93], v[244:251], v63\n"
"v_smfmac_f32_16x16x64_bf16 v[106:109], v[90:93], v[120:127], v63\n"
"buffer_load_dword v200, off, s[28:31], s39 sc1\n"
"v_mfma_f32_16x16x32_bf16 v[90:93], v[172:175], v[90:93], v[82:85]\n"
"ds_read_b128 v[120:123], v179\n"
"v_cvt_pk_bf16_f32 v212, v212, v213\n"
"v_mfma_f32_16x16x32_bf16 v[208:211], a[80:83], v[32:35], 0\n"
"ds_read_b64 v[82:83], v180 offset:13568\n"
"ds_read_b64 v[84:85], v181 offset:13568\n"
"v_mfma_f32_16x16x32_bf16 v[208:211], a[92:95], v[36:39], v[208:211]\n"
"v_cvt_pk_bf16_f32 v252, v252, v253\n"
"v_cvt_pk_bf16_f32 v253, v254, v255\n"
"v_mfma_f32_16x16x32_bf16 v[208:211], a[104:107], v[40:43], v[208:211]\n"
"v_rcp_f32 v171, v90\n"
"v_mfma_f32_16x16x32_bf16 v[208:211], a[116:119], v[44:47], v[208:211]\n"
"s_add_u32 m0, s34, 5120\n"
"ds_read_b64 v[90:91], v180 offset:27072\n"
"buffer_load_dwordx4 v199, s[8:11], s77 offen lds\n"
"v_mfma_f32_16x16x32_bf16 v[208:211], a[128:131], v[48:51], v[208:211]\n"
"s_mul_i32 s8, s33, 0\n"
"v_cvt_pk_bf16_f32 v171, v171, v171\n"
"v_and_b32 v92, v171, v176\n"
"v_mfma_f32_16x16x32_bf16 v[208:211], a[140:143], v[52:55], v[208:211]\n"
"s_mul_i32 s9, s33, 1\n"
"v_and_b32 v93, v171, v177\n"
"v_cvt_pk_bf16_f32 v98, v98, v99\n"
"v_cvt_pk_bf16_f32 v99, v100, v101\n"
"v_mfma_f32_16x16x16_bf16 v[252:255], v[252:253], v[92:93], 0\n"
"s_mul_i32 s10, s33, 2\n"
"v_cvt_pk_bf16_f32 v106, v106, v107\n"
"v_cvt_pk_bf16_f32 v107, v108, v109\n"
"v_mfma_f32_16x16x16_bf16 v[98:101], v[98:99], v[92:93], 0\n"
"s_waitcnt lgkmcnt(2)\n"
"v_cvt_pk_bf16_f32 v213, v214, v215\n"
"v_cvt_pk_bf16_f32 v214, v208, v209\n"
"v_mfma_f32_16x16x16_bf16 v[106:109], v[106:107], v[92:93], 0\n"
"s_waitcnt lgkmcnt(1)\n"
"ds_read_b64 v[92:93], v181 offset:27072\n"
"v_cvt_pk_bf16_f32 v252, v252, v253\n"
"v_mfma_f32_16x16x32_bf16 v[136:139], v[136:139], v[148:151], 0\n"
"s_mul_i32 s11, s33, 4\n"
"v_cvt_pk_bf16_f32 v253, v254, v255\n"
"v_cvt_pk_bf16_f32 v254, v98, v99\n"
"v_mfma_f32_16x16x32_bf16 v[136:139], v[80:83], v[160:163], v[136:139]\n"
"s_waitcnt vmcnt(13) lgkmcnt(1)\n"
"ds_read_b64 v[98:99], v178 offset:64\n"
"v_cvt_pk_bf16_f32 v255, v100, v101\n"
"v_mfma_f32_16x16x32_bf16 v[128:131], v[128:131], v[148:151], 0\n"
"buffer_store_dwordx4 v[252:255], v192, s[28:31], s37 offen sc1\n"
"v_mfma_f32_16x16x32_bf16 v[128:131], v[84:87], v[162:165], v[128:131]\n"
"ds_write_b128 v202, v[56:59] offset:30720\n"
"v_mfma_f32_16x16x32_bf16 v[140:143], v[140:143], v[148:151], 0\n"
"buffer_load_dwordx4 v[56:59], v192, s[28:31], s38 offen sc1\n"
"v_cvt_pk_bf16_f32 v106, v106, v107\n"
"v_mfma_f32_16x16x32_bf16 v[140:143], v[88:91], v[160:163], v[140:143]\n"
"s_waitcnt lgkmcnt(2)\n"
"v_cvt_pk_bf16_f32 v107, v108, v109\n"
"v_cvt_pk_bf16_f32 v215, v210, v211\n"
"v_mfma_f32_16x16x32_bf16 v[208:211], a[76:79], v[32:35], 0\n"
"s_waitcnt vmcnt(13)\n"
"ds_write_b64 v203, v[60:61] offset:30720\n"
"v_mfma_f32_16x16x32_bf16 v[208:211], a[88:91], v[36:39], v[208:211]\n"
"s_waitcnt lgkmcnt(2)\n"
"v_max_f32 v136, v136, v128\n"
"v_max_f32 v137, v137, v129\n"
"v_mfma_f32_16x16x32_bf16 v[208:211], a[100:103], v[40:43], v[208:211]\n"
"buffer_load_dwordx2 v[60:61], v193, s[28:31], s38 offen offset:1024 sc1\n"
"ds_read_b64 v[100:101], v179 offset:64\n"
"v_mfma_f32_16x16x32_bf16 v[208:211], a[112:115], v[44:47], v[208:211]\n"
"s_add_u32 s38, s38, 1572864\n"
"ds_read_b64_tr_b16 v[128:129], v182 offset:54088\n"
"v_max_f32 v138, v138, v130\n"
"v_mfma_f32_16x16x32_bf16 v[208:211], a[124:127], v[48:51], v[208:211]\n"
"v_max_f32 v139, v139, v131\n"
"v_max3_f32 v170, v136, v137, v138\n"
"v_mfma_f32_16x16x32_bf16 v[208:211], a[136:139], v[52:55], v[208:211]\n"
"ds_read_b64_tr_b16 v[130:131], v183 offset:54088\n"
"buffer_store_dwordx2 v[106:107], v193, s[28:31], s37 offen offset:1024 sc1\n"
"v_mfma_f32_16x16x32_bf16 v[132:135], v[132:135], v[148:151], 0\n"
"s_waitcnt lgkmcnt(3)\n"
"v_mov_b64 v[252:253], 0\n"
"v_mov_b64 v[254:255], 0\n"
"v_mfma_f32_16x16x32_bf16 v[132:135], v[92:95], v[162:165], v[132:135]\n"
"s_barrier\n"
"v_mfma_f32_16x16x32_bf16 v[112:115], v[112:115], v[148:151], 0\n"
"s_add_u32 s37, s37, 1572864\n"
"ds_write_b128 v186, v[204:207] offset:33760\n"
"v_mfma_f32_16x16x32_bf16 v[112:115], v[96:99], v[160:163], v[112:115]\n"
"s_waitcnt lgkmcnt(3)\n"
"v_mov_b64 v[98:99], 0\n"
"v_mfma_f32_16x16x32_bf16 v[204:207], a[72:75], v[32:35], 0\n"
"v_max_f32 v140, v140, v132\n"
"v_max_f32 v141, v141, v133\n"
"v_mfma_f32_16x16x32_bf16 v[204:207], a[84:87], v[36:39], v[204:207]\n"
"ds_read_b128 v[116:119], v178 offset:13504\n"
"ds_read_b64 v[106:107], v178 offset:13568\n"
"v_mfma_f32_16x16x32_bf16 v[204:207], a[96:99], v[40:43], v[204:207]\n"
"ds_read_b128 v[124:127], v179 offset:13504\n"
"v_max_f32 v142, v142, v134\n"
"v_mfma_f32_16x16x32_bf16 v[204:207], a[108:111], v[44:47], v[204:207]\n"
"ds_read_b64 v[108:109], v179 offset:13568\n"
"v_max_f32 v143, v143, v135\n"
"v_mfma_f32_16x16x32_bf16 v[204:207], a[120:123], v[48:51], v[204:207]\n"
"ds_read_b128 v[64:67], v202 offset:49152\n"
"v_max3_f32 v170, v170, v139, v140\n"
"v_mfma_f32_16x16x32_bf16 v[204:207], a[132:135], v[52:55], v[204:207]\n"
"ds_read_b64 v[68:69], v62 offset:49152\n"
"ds_write_b128 v186, v[212:215] offset:33824\n"
"v_mfma_f32_16x16x32_bf16 v[120:123], v[120:123], v[148:151], 0\n"
"v_max3_f32 v170, v170, v141, v142\n"
"v_mfma_f32_16x16x32_bf16 v[120:123], v[100:103], v[162:165], v[120:123]\n"
"v_mov_b64 v[100:101], 0\n"
"v_mfma_f32_16x16x32_bf16 v[212:215], a[144:147], v[32:35], 0\n"
"s_waitcnt lgkmcnt(5)\n"
"ds_read_b64_tr_b16 v[132:133], v182 offset:54856\n"
"ds_read_b64_tr_b16 v[134:135], v183 offset:54856\n"
"v_mfma_f32_16x16x32_bf16 v[116:119], v[116:119], v[148:151], 0\n"
"v_cvt_pk_bf16_f32 v204, v204, v205\n"
"v_cvt_pk_bf16_f32 v205, v206, v207\n"
"v_mfma_f32_16x16x32_bf16 v[116:119], v[104:107], v[160:163], v[116:119]\n"
"s_waitcnt lgkmcnt(5)\n"
"v_cvt_pk_bf16_f32 v206, v208, v209\n"
"v_cvt_pk_bf16_f32 v207, v210, v211\n"
"v_mfma_f32_16x16x32_bf16 v[208:211], a[148:151], v[32:35], 0\n"
"s_waitcnt lgkmcnt(4)\n"
"ds_write_b128 v186, v[204:207] offset:33888\n"
"v_mfma_f32_16x16x32_bf16 v[208:211], a[160:163], v[36:39], v[208:211]\n"
"s_waitcnt lgkmcnt(4)\n"
"buffer_store_dwordx4 v[64:67], v194, s[24:27], s36 offen sc1\n"
"v_mfma_f32_16x16x32_bf16 v[208:211], a[172:175], v[40:43], v[208:211]\n"
"s_nop 0\n"
"ds_read_b128 v[64:67], v192 offset:30720\n"
"v_max_f32 v112, v112, v120\n"
"v_mfma_f32_16x16x32_bf16 v[208:211], a[184:187], v[44:47], v[208:211]\n"
"v_max_f32 v113, v113, v121\n"
"v_max_f32 v114, v114, v122\n"
"v_mfma_f32_16x16x32_bf16 v[208:211], a[196:199], v[48:51], v[208:211]\n"
"buffer_store_dwordx2 v[68:69], v195, s[24:27], s36 offen sc1\n"
"v_mfma_f32_16x16x32_bf16 v[204:207], a[152:155], v[32:35], 0\n"
"s_add_u32 s36, s36, s33\n"
"ds_read_b128 v[68:71], v192 offset:31744\n"
"ds_read_b128 v[32:35], v188 offset:6144\n"
"v_mfma_f32_16x16x32_bf16 v[204:207], a[164:167], v[36:39], v[204:207]\n"
"ds_read_b64_tr_b16 v[120:121], v182 offset:72\n"
"v_max_f32 v115, v115, v123\n"
"v_mfma_f32_16x16x32_bf16 v[204:207], a[176:179], v[40:43], v[204:207]\n"
"v_max3_f32 v170, v170, v143, v112\n"
"v_max3_f32 v170, v170, v113, v114\n"
"v_mfma_f32_16x16x32_bf16 v[204:207], a[188:191], v[44:47], v[204:207]\n"
"s_mov_b64 exec, s[46:47]\n"
"v_add_u32 v183, 0xc0, v183\n"
"s_mov_b64 exec, -1\n"
"v_mfma_f32_16x16x32_bf16 v[204:207], a[200:203], v[48:51], v[204:207]\n"
"s_waitcnt lgkmcnt(3)\n"
"ds_read_b64_tr_b16 v[122:123], v183 offset:72\n"
"v_mov_b64 v[106:107], 0\n"
"v_mfma_f32_16x16x32_bf16 v[124:127], v[124:127], v[148:151], 0\n"
"v_mfma_f32_16x16x32_bf16 v[124:127], v[108:111], v[162:165], v[124:127]\n"
"v_mov_b64 v[108:109], 0\n"
"v_mfma_f32_16x16x32_bf16 v[232:235], a[216:219], v[64:67], 0\n"
"s_waitcnt lgkmcnt(3)\n"
"v_mfma_f32_16x16x32_bf16 v[232:235], a[228:231], v[68:71], v[232:235]\n"
"v_mfma_f32_16x16x32_bf16 v[236:239], a[220:223], v[64:67], 0\n"
"v_mfma_f32_16x16x32_bf16 v[236:239], a[232:235], v[68:71], v[236:239]\n"
"v_mfma_f32_16x16x32_bf16 v[240:243], a[224:227], v[64:67], 0\n"
"ds_read_b128 v[64:67], v192 offset:32768\n"
"v_max_f32 v116, v116, v124\n"
"v_mfma_f32_16x16x32_bf16 v[240:243], a[236:239], v[68:71], v[240:243]\n"
"ds_read_b128 v[68:71], v192 offset:33792\n"
"v_max_f32 v117, v117, v125\n"
"v_mfma_f32_16x16x32_bf16 v[212:215], a[156:159], v[36:39], v[212:215]\n"
"ds_read_b128 v[36:39], v189 offset:6144\n"
"ds_read_b64_tr_b16 v[124:125], v182 offset:840\n"
"v_mfma_f32_16x16x32_bf16 v[212:215], a[168:171], v[40:43], v[212:215]\n"
"ds_read_b128 v[40:43], v188 offset:7168\n"
"v_max_f32 v118, v118, v126\n"
"v_mfma_f32_16x16x32_bf16 v[212:215], a[180:183], v[44:47], v[212:215]\n"
"ds_read_b128 v[44:47], v189 offset:7168\n"
"v_max_f32 v119, v119, v127\n"
"v_mfma_f32_16x16x32_bf16 v[212:215], a[192:195], v[48:51], v[212:215]\n"
"ds_read_b128 v[48:51], v188 offset:8192\n"
"v_max3_f32 v170, v170, v115, v116\n"
"v_mfma_f32_16x16x32_bf16 v[212:215], a[204:207], v[52:55], v[212:215]\n"
"ds_read_b64_tr_b16 v[126:127], v183 offset:840\n"
"v_max3_f32 v170, v170, v117, v118\n"
"v_mfma_f32_16x16x32_bf16 v[208:211], a[208:211], v[52:55], v[208:211]\n"
"s_waitcnt lgkmcnt(7)\n"
"v_max_f32 v171, v170, v119\n"
"v_max_f32 v170, v170, v119\n"
"v_mfma_f32_16x16x32_bf16 v[232:235], a[240:243], v[64:67], v[232:235]\n"
"s_waitcnt lgkmcnt(6)\n"
"v_permlane16_swap_b32 v170, v171\n"
"v_mfma_f32_16x16x32_bf16 v[232:235], a[252:255], v[68:71], v[232:235]\n"
"v_max_f32 v170, v170, v171\n"
"v_mov_b32 v171, v170\n"
"v_mfma_f32_16x16x32_bf16 v[236:239], a[244:247], v[64:67], v[236:239]\n"
"s_nop 0\n"
"v_permlane32_swap_b32 v170, v171\n"
"v_max_f32 v170, v170, v171\n"
"v_mul_f32 v170, 0xbe553b95, v170\n"
"v_pk_fma_f32 v[136:137], v[136:137], s[70:71], v[170:171] op_sel_hi:[1,0,0]\n"
"v_pk_fma_f32 v[138:139], v[138:139], s[70:71], v[170:171] op_sel_hi:[1,0,0]\n"
"v_pk_fma_f32 v[140:141], v[140:141], s[70:71], v[170:171] op_sel_hi:[1,0,0]\n"
"v_pk_fma_f32 v[142:143], v[142:143], s[70:71], v[170:171] op_sel_hi:[1,0,0]\n"
"v_pk_fma_f32 v[112:113], v[112:113], s[70:71], v[170:171] op_sel_hi:[1,0,0]\n"
"v_pk_fma_f32 v[114:115], v[114:115], s[70:71], v[170:171] op_sel_hi:[1,0,0]\n"
"v_pk_fma_f32 v[116:117], v[116:117], s[70:71], v[170:171] op_sel_hi:[1,0,0]\n"
"v_pk_fma_f32 v[118:119], v[118:119], s[70:71], v[170:171] op_sel_hi:[1,0,0]\n"
"v_mfma_f32_16x16x32_bf16 v[236:239], v[0:3], v[68:71], v[236:239]\n"
"v_exp_f32 v136, v136\n"
"v_mfma_f32_16x16x32_bf16 v[240:243], a[248:251], v[64:67], v[240:243]\n"
"ds_read_b128 v[64:67], v192 offset:34816\n"
"v_cvt_pk_bf16_f32 v148, v212, v213\n"
"v_mfma_f32_16x16x32_bf16 v[240:243], v[4:7], v[68:71], v[240:243]\n"
"ds_read_b128 v[68:71], v192 offset:35840\n"
"v_cvt_pk_bf16_f32 v149, v214, v215\n"
"v_mfma_f32_16x16x32_bf16 v[204:207], a[212:215], v[52:55], v[204:207]\n"
"ds_read_b128 v[52:55], v189 offset:8192\n"
"v_exp_f32 v137, v137\n"
"v_mfma_f32_16x16x32_bf16 v[212:215], a[0:3], v[32:35], 0\n"
"s_waitcnt lgkmcnt(8)\n"
"v_exp_f32 v138, v138\n"
"v_mfma_f32_16x16x32_bf16 v[212:215], a[12:15], v[36:39], v[212:215]\n"
"s_waitcnt lgkmcnt(6)\n"
"v_exp_f32 v139, v139\n"
"v_mfma_f32_16x16x32_bf16 v[212:215], a[24:27], v[40:43], v[212:215]\n"
"s_waitcnt lgkmcnt(5)\n"
"v_cvt_pk_bf16_f32 v82, v136, v138\n"
"v_cvt_pk_bf16_f32 v83, v137, v139\n"
"v_mfma_f32_16x16x32_bf16 v[212:215], a[36:39], v[44:47], v[212:215]\n"
"s_waitcnt lgkmcnt(4)\n"
"ds_read_b128 v[136:139], v180 offset:27008\n"
"v_exp_f32 v140, v140\n"
"v_mfma_f32_16x16x32_bf16 v[212:215], a[48:51], v[48:51], v[212:215]\n"
"s_waitcnt lgkmcnt(3)\n"
"v_exp_f32 v141, v141\n"
"v_mfma_f32_16x16x32_bf16 v[232:235], v[8:11], v[64:67], v[232:235]\n"
"s_waitcnt lgkmcnt(2)\n"
"v_exp_f32 v142, v142\n"
"v_mfma_f32_16x16x32_bf16 v[232:235], v[20:23], v[68:71], v[232:235]\n"
"s_waitcnt lgkmcnt(1)\n"
"v_exp_f32 v143, v143\n"
"v_mfma_f32_16x16x32_bf16 v[240:243], v[16:19], v[64:67], v[240:243]\n"
"v_cvt_pk_bf16_f32 v84, v140, v142\n"
"v_cvt_pk_bf16_f32 v85, v141, v143\n"
"v_mfma_f32_16x16x32_bf16 v[240:243], v[28:31], v[68:71], v[240:243]\n"
"ds_read_b128 v[140:143], v178\n"
"v_cvt_pk_bf16_f32 v150, v208, v209\n"
"v_smfmac_f32_16x16x64_bf16 v[252:255], v[82:85], v[216:223], v63\n"
"ds_read_b64_tr_b16 v[216:217], v182 offset:128\n"
"v_smfmac_f32_16x16x64_bf16 v[98:101], v[82:85], v[72:79], v63\n"
"ds_read_b64_tr_b16 v[218:219], v183 offset:128\n"
"v_smfmac_f32_16x16x64_bf16 v[106:109], v[82:85], v[128:135], v63\n"
"ds_read_b64_tr_b16 v[220:221], v182 offset:896\n"
"v_mfma_f32_16x16x32_bf16 v[82:85], v[172:175], v[82:85], 0\n"
"ds_read_b64_tr_b16 v[222:223], v183 offset:896\n"
"ds_read_b64_tr_b16 v[72:73], v182 offset:136\n"
"v_mfma_f32_16x16x32_bf16 v[236:239], v[12:15], v[64:67], v[236:239]\n"
"ds_read_b64_tr_b16 v[74:75], v183 offset:136\n"
"v_cvt_pk_bf16_f32 v151, v210, v211\n"
"v_mfma_f32_16x16x32_bf16 v[236:239], v[24:27], v[68:71], v[236:239]\n"
"ds_read_b64_tr_b16 v[76:77], v182 offset:904\n"
"v_cvt_pk_bf16_f32 v232, v232, v233\n"
"v_mfma_f32_16x16x32_bf16 v[212:215], a[60:63], v[52:55], v[212:215]\n"
"ds_read_b64_tr_b16 v[78:79], v183 offset:904\n"
"v_cvt_pk_bf16_f32 v233, v234, v235\n"
"v_mfma_f32_16x16x32_bf16 v[208:211], a[4:7], v[32:35], 0\n"
"ds_read_b128 v[128:131], v181 offset:27008\n"
"v_cvt_pk_bf16_f32 v240, v240, v241\n"
"v_mfma_f32_16x16x32_bf16 v[208:211], a[16:19], v[36:39], v[208:211]\n"
"ds_read_b128 v[132:135], v179\n"
"v_cvt_pk_bf16_f32 v234, v236, v237\n"
"v_mfma_f32_16x16x32_bf16 v[208:211], a[28:31], v[40:43], v[208:211]\n"
"v_exp_f32 v112, v112\n"
"v_mfma_f32_16x16x32_bf16 v[208:211], a[40:43], v[44:47], v[208:211]\n"
"s_waitcnt vmcnt(7)\n"
"v_add3_u32 v200, v200, s50, -8\n"
"v_cmp_lt_u32 vcc, 64, v200\n"
"v_mfma_f32_16x16x32_bf16 v[208:211], a[52:55], v[48:51], v[208:211]\n"
"v_exp_f32 v113, v113\n"
"v_mfma_f32_16x16x32_bf16 v[208:211], a[64:67], v[52:55], v[208:211]\n"
"s_nop 1\n"
"s_cbranch_vccnz .Lp7%=\n"
".Lr7%=:\n"
"buffer_load_dwordx4 a[198:201], v192, s[28:31], s38 offen sc1\n"
"v_exp_f32 v114, v114\n"
"v_exp_f32 v115, v115\n"
"v_cvt_pk_bf16_f32 v90, v112, v114\n"
"v_cvt_pk_bf16_f32 v235, v238, v239\n"
"v_cvt_pk_bf16_f32 v241, v242, v243\n"
"v_cvt_pk_bf16_f32 v91, v113, v115\n"
"v_exp_f32 v116, v116\n"
"v_exp_f32 v117, v117\n"
"buffer_load_dwordx2 a[202:203], v193, s[28:31], s38 offen offset:1024 sc1\n"
"ds_write_b128 v196, v[232:235] offset:36864\n"
"v_exp_f32 v118, v118\n"
"v_exp_f32 v119, v119\n"
"v_cvt_pk_bf16_f32 v92, v116, v118\n"
"v_cvt_pk_bf16_f32 v93, v117, v119\n"
"v_cvt_pk_bf16_f32 v162, v204, v205\n"
"v_cvt_pk_bf16_f32 v163, v206, v207\n"
"v_smfmac_f32_16x16x64_bf16 v[106:109], v[90:93], v[120:127], v63\n"
"s_waitcnt lgkmcnt(3)\n"
"ds_write_b64 v197, v[240:241] offset:36864\n"
"v_smfmac_f32_16x16x64_bf16 v[252:255], v[90:93], v[216:223], v63\n"
"s_add_u32 s38, s38, 1572864\n"
"buffer_load_dwordx4 a[204:207], v192, s[28:31], s38 offen sc1\n"
"v_smfmac_f32_16x16x64_bf16 v[98:101], v[90:93], v[72:79], v63\n"
"ds_read_b128 v[112:115], v178 offset:13504\n"
"v_mfma_f32_16x16x32_bf16 v[90:93], v[172:175], v[90:93], v[82:85]\n"
"ds_read_b128 v[120:123], v179 offset:13504\n"
"v_cvt_pk_bf16_f32 v106, v106, v107\n"
"v_mfma_f32_16x16x32_bf16 v[204:207], a[8:11], v[32:35], 0\n"
"ds_read_b64 v[82:83], v180 offset:27072\n"
"buffer_load_dwordx2 a[208:209], v193, s[28:31], s38 offen offset:1024 sc1\n"
"v_mfma_f32_16x16x32_bf16 v[204:207], a[20:23], v[36:39], v[204:207]\n"
"s_add_u32 s38, s38, 1572864\n"
"ds_read_b64 v[84:85], v181 offset:27072\n"
"v_cvt_pk_bf16_f32 v252, v252, v253\n"
"v_mfma_f32_16x16x32_bf16 v[204:207], a[32:35], v[40:43], v[204:207]\n"
"s_add_u32 s12, s38, 0\n"
"v_rcp_f32 v171, v90\n"
"v_mfma_f32_16x16x32_bf16 v[204:207], a[44:47], v[44:47], v[204:207]\n"
"s_add_u32 s13, s38, 1572864\n"
"ds_read_b64 v[90:91], v178 offset:64\n"
"v_cvt_pk_bf16_f32 v171, v171, v171\n"
"v_mfma_f32_16x16x32_bf16 v[204:207], a[56:59], v[48:51], v[204:207]\n"
"s_add_u32 s14, s38, 3145728\n"
"v_and_b32 v92, v171, v176\n"
"v_and_b32 v93, v171, v177\n"
"v_mfma_f32_16x16x32_bf16 v[204:207], a[68:71], v[52:55], v[204:207]\n"
"s_add_u32 s16, s38, 4718592\n"
"buffer_load_dwordx4 a[144:147], v192, s[28:31], s12 offen sc1\n"
"v_cvt_pk_bf16_f32 v253, v254, v255\n"
"v_cvt_pk_bf16_f32 v98, v98, v99\n"
"v_cvt_pk_bf16_f32 v107, v108, v109\n"
"v_mfma_f32_16x16x16_bf16 v[252:255], v[252:253], v[92:93], 0\n"
"s_add_u32 s15, s38, 6291456\n"
"v_cvt_pk_bf16_f32 v99, v100, v101\n"
"v_cvt_pk_bf16_f32 v212, v212, v213\n"
"v_mfma_f32_16x16x16_bf16 v[106:109], v[106:107], v[92:93], 0\n"
"s_waitcnt lgkmcnt(2)\n"
"v_cvt_pk_bf16_f32 v213, v214, v215\n"
"v_cvt_pk_bf16_f32 v214, v208, v209\n"
"v_mfma_f32_16x16x16_bf16 v[98:101], v[98:99], v[92:93], 0\n"
"s_waitcnt lgkmcnt(1)\n"
"buffer_load_dwordx2 a[148:149], v193, s[28:31], s12 offen offset:1024 sc1\n"
"ds_read_b64 v[92:93], v179 offset:64\n"
"v_mfma_f32_16x16x32_bf16 v[136:139], v[136:139], v[152:155], 0\n"
"s_add_u32 s17, s38, 7864320\n"
"v_cvt_pk_bf16_f32 v252, v252, v253\n"
"v_cvt_pk_bf16_f32 v253, v254, v255\n"
"v_mfma_f32_16x16x32_bf16 v[136:139], v[80:83], v[164:167], v[136:139]\n"
"s_waitcnt lgkmcnt(1)\n"
"v_cvt_pk_bf16_f32 v106, v106, v107\n"
"v_cvt_pk_bf16_f32 v254, v98, v99\n"
"v_mfma_f32_16x16x32_bf16 v[128:131], v[128:131], v[152:155], 0\n"
"s_add_u32 s18, s38, 9437184\n"
"ds_read_b64 v[98:99], v178 offset:13568\n"
"v_cvt_pk_bf16_f32 v255, v100, v101\n"
"v_mfma_f32_16x16x32_bf16 v[128:131], v[84:87], v[166:169], v[128:131]\n"
"buffer_store_dwordx4 v[252:255], v192, s[28:31], s37 offen sc1\n"
"v_mfma_f32_16x16x32_bf16 v[140:143], v[140:143], v[152:155], 0\n"
"ds_read_b64 v[100:101], v179 offset:13568\n"
"v_cvt_pk_bf16_f32 v107, v108, v109\n"
"v_mfma_f32_16x16x32_bf16 v[140:143], v[88:91], v[164:167], v[140:143]\n"
"v_cvt_pk_bf16_f32 v215, v210, v211\n"
"v_cvt_pk_bf16_f32 v204, v204, v205\n"
"v_mfma_f32_16x16x32_bf16 v[208:211], a[80:83], v[32:35], 0\n"
"s_waitcnt lgkmcnt(2)\n"
"buffer_store_dwordx2 v[106:107], v193, s[28:31], s37 offen offset:1024 sc1\n"
"v_mfma_f32_16x16x32_bf16 v[132:135], v[132:135], v[152:155], 0\n"
"s_waitcnt vmcnt(12)\n"
"ds_write_b128 v202, v[56:59] offset:55296\n"
"v_mfma_f32_16x16x32_bf16 v[132:135], v[92:95], v[166:169], v[132:135]\n"
"s_waitcnt lgkmcnt(2)\n"
"v_max_f32 v136, v136, v128\n"
"v_max_f32 v137, v137, v129\n"
"v_mfma_f32_16x16x32_bf16 v[112:115], v[112:115], v[152:155], 0\n"
"s_add_u32 s37, s37, 1572864\n"
"v_max_f32 v138, v138, v130\n"
"v_max_f32 v139, v139, v131\n"
"v_mfma_f32_16x16x32_bf16 v[112:115], v[96:99], v[164:167], v[112:115]\n"
"s_waitcnt vmcnt(10) lgkmcnt(1)\n"
"ds_read_b64_tr_b16 v[128:129], v184 offset:72\n"
"v_or_b32 v201, 10, s6\n"
"v_mfma_f32_16x16x32_bf16 v[120:123], v[120:123], v[152:155], 0\n"
"s_mov_b64 exec, 1\n"
"buffer_store_dword v201, off, s[28:31], s40 sc1\n"
"s_mov_b64 exec, -1\n"
"v_mfma_f32_16x16x32_bf16 v[120:123], v[100:103], v[166:169], v[120:123]\n"
"ds_write_b64 v203, v[60:61] offset:55296\n"
"v_mfma_f32_16x16x32_bf16 v[208:211], a[92:95], v[36:39], v[208:211]\n"
"v_max3_f32 v170, v136, v137, v138\n"
"v_max_f32 v140, v140, v132\n"
"v_mfma_f32_16x16x32_bf16 v[208:211], a[104:107], v[40:43], v[208:211]\n"
"ds_read_b64_tr_b16 v[130:131], v185 offset:72\n"
"v_max_f32 v141, v141, v133\n"
"v_mfma_f32_16x16x32_bf16 v[208:211], a[116:119], v[44:47], v[208:211]\n"
"ds_read_b64_tr_b16 v[132:133], v184 offset:840\n"
"v_max_f32 v142, v142, v134\n"
"v_mfma_f32_16x16x32_bf16 v[208:211], a[128:131], v[48:51], v[208:211]\n"
"v_max_f32 v143, v143, v135\n"
"v_max3_f32 v170, v170, v139, v140\n"
"v_mfma_f32_16x16x32_bf16 v[208:211], a[140:143], v[52:55], v[208:211]\n"
"v_max3_f32 v170, v170, v141, v142\n"
"v_cvt_pk_bf16_f32 v205, v206, v207\n"
"v_max_f32 v112, v112, v120\n"
"v_max_f32 v113, v113, v121\n"
"v_max_f32 v114, v114, v122\n"
"v_max_f32 v115, v115, v123\n"
"v_max3_f32 v170, v170, v143, v112\n"
"v_max3_f32 v170, v170, v113, v114\n"
"s_waitcnt lgkmcnt(2)\n"
"s_barrier\n"
"ds_read_b128 v[116:119], v178 offset:27008\n"
"ds_read_b64 v[106:107], v178 offset:27072\n"
"ds_read_b128 v[124:127], v179 offset:27008\n"
"v_mov_b64 v[252:253], 0\n"
"v_mov_b64 v[254:255], 0\n"
"v_mov_b64 v[98:99], 0\n"
"ds_read_b64 v[108:109], v179 offset:27072\n"
"v_mov_b64 v[100:101], 0\n"
"ds_write_b128 v186, v[212:215] offset:47264\n"
"v_mfma_f32_16x16x32_bf16 v[212:215], a[72:75], v[32:35], 0\n"
"v_cvt_pk_bf16_f32 v206, v208, v209\n"
"v_cvt_pk_bf16_f32 v207, v210, v211\n"
"v_mfma_f32_16x16x32_bf16 v[212:215], a[84:87], v[36:39], v[212:215]\n"
"s_mov_b64 exec, s[44:45]\n"
"v_add_u32 v181, 0xc0, v181\n"
"s_mov_b64 exec, -1\n"
"v_mfma_f32_16x16x32_bf16 v[212:215], a[96:99], v[40:43], v[212:215]\n"
"ds_read_b128 v[64:67], v202 offset:36864\n"
"ds_read_b64 v[68:69], v62 offset:36864\n"
"v_mfma_f32_16x16x32_bf16 v[212:215], a[108:111], v[44:47], v[212:215]\n"
"s_waitcnt lgkmcnt(5)\n"
"ds_read_b64_tr_b16 v[134:135], v185 offset:840\n"
"ds_read_b64_tr_b16 a[168:169], v182 offset:13576\n"
"v_mfma_f32_16x16x32_bf16 v[116:119], v[116:119], v[152:155], 0\n"
"ds_read_b64_tr_b16 a[170:171], v183 offset:13576\n"
"v_mfma_f32_16x16x32_bf16 v[116:119], v[104:107], v[164:167], v[116:119]\n"
"s_waitcnt lgkmcnt(6)\n"
"ds_read_b64_tr_b16 a[172:173], v182 offset:14344\n"
"v_mov_b64 v[106:107], 0\n"
"v_mfma_f32_16x16x32_bf16 v[124:127], v[124:127], v[152:155], 0\n"
"ds_read_b64_tr_b16 a[174:175], v183 offset:14344\n"
"v_mfma_f32_16x16x32_bf16 v[124:127], v[108:111], v[166:169], v[124:127]\n"
"ds_write_b128 v186, v[204:207] offset:47328\n"
"v_mfma_f32_16x16x32_bf16 v[212:215], a[120:123], v[48:51], v[212:215]\n"
"s_waitcnt lgkmcnt(7)\n"
"v_mov_b64 v[108:109], 0\n"
"v_mfma_f32_16x16x32_bf16 v[212:215], a[132:135], v[52:55], v[212:215]\n"
"s_waitcnt lgkmcnt(6)\n"
"buffer_store_dwordx4 v[64:67], v194, s[24:27], s36 offen sc1\n"
"v_mfma_f32_16x16x32_bf16 v[208:211], a[76:79], v[32:35], 0\n"
"s_nop 0\n"
"ds_read_b128 v[64:67], v192 offset:55296\n"
"ds_read_b128 v[32:35], v188\n"
"v_mfma_f32_16x16x32_bf16 v[208:211], a[88:91], v[36:39], v[208:211]\n"
"ds_read_b128 v[36:39], v189\n"
"v_max_f32 v116, v116, v124\n"
"v_mfma_f32_16x16x32_bf16 v[208:211], a[100:103], v[40:43], v[208:211]\n"
"ds_read_b128 v[40:43], v188 offset:1024\n"
"buffer_store_dwordx2 v[68:69], v195, s[24:27], s36 offen sc1\n"
"v_mfma_f32_16x16x32_bf16 v[208:211], a[112:115], v[44:47], v[208:211]\n"
"s_add_u32 s36, s36, s33\n"
"ds_read_b128 v[68:71], v192 offset:56320\n"
"ds_read_b128 v[44:47], v189 offset:1024\n"
"v_mfma_f32_16x16x32_bf16 v[208:211], a[124:127], v[48:51], v[208:211]\n"
"ds_read_b128 v[48:51], v188 offset:2048\n"
"v_max_f32 v117, v117, v125\n"
"v_mfma_f32_16x16x32_bf16 v[208:211], a[136:139], v[52:55], v[208:211]\n"
"ds_read_b128 v[52:55], v189 offset:2048\n"
"v_max_f32 v118, v118, v126\n"
"v_max_f32 v119, v119, v127\n"
"v_max3_f32 v170, v170, v115, v116\n"
"v_max3_f32 v170, v170, v117, v118\n"
"v_max_f32 v171, v170, v119\n"
"v_max_f32 v170, v170, v119\n"
"v_cvt_pk_bf16_f32 v212, v212, v213\n"
"v_cvt_pk_bf16_f32 v213, v214, v215\n"
"v_permlane16_swap_b32 v170, v171\n"
"v_max_f32 v170, v170, v171\n"
"v_mov_b32 v171, v170\n"
"v_cvt_pk_bf16_f32 v214, v208, v209\n"
"v_cvt_pk_bf16_f32 v215, v210, v211\n"
"v_permlane32_swap_b32 v170, v171\n"
"v_max_f32 v170, v170, v171\n"
"v_mul_f32 v170, 0xbe553b95, v170\n"
"v_pk_fma_f32 v[136:137], v[136:137], s[70:71], v[170:171] op_sel_hi:[1,0,0]\n"
"v_pk_fma_f32 v[138:139], v[138:139], s[70:71], v[170:171] op_sel_hi:[1,0,0]\n"
"v_pk_fma_f32 v[140:141], v[140:141], s[70:71], v[170:171] op_sel_hi:[1,0,0]\n"
"v_pk_fma_f32 v[142:143], v[142:143], s[70:71], v[170:171] op_sel_hi:[1,0,0]\n"
"v_pk_fma_f32 v[112:113], v[112:113], s[70:71], v[170:171] op_sel_hi:[1,0,0]\n"
"v_pk_fma_f32 v[114:115], v[114:115], s[70:71], v[170:171] op_sel_hi:[1,0,0]\n"
"v_pk_fma_f32 v[116:117], v[116:117], s[70:71], v[170:171] op_sel_hi:[1,0,0]\n"
".Lic6%=:\n"
"s_cbranch_execz .Lib1%=\n"
"v_pk_fma_f32 v[118:119], v[118:119], s[70:71], v[170:171] op_sel_hi:[1,0,0]\n"
"v_exp_f32 v136, v136\n"
"v_exp_f32 v137, v137\n"
"v_exp_f32 v138, v138\n"
"v_exp_f32 v139, v139\n"
"v_cvt_pk_bf16_f32 v82, v136, v138\n"
"v_cvt_pk_bf16_f32 v83, v137, v139\n"
"v_exp_f32 v140, v140\n"
"v_exp_f32 v141, v141\n"
"v_exp_f32 v142, v142\n"
"v_exp_f32 v143, v143\n"
"v_cvt_pk_bf16_f32 v84, v140, v142\n"
"v_cvt_pk_bf16_f32 v85, v141, v143\n"
"v_exp_f32 v112, v112\n"
"s_nop 0\n"
"v_smfmac_f32_16x16x64_bf16 v[252:255], v[82:85], v[224:231], v63\n"
"s_waitcnt lgkmcnt(6)\n"
"ds_read_b64_tr_b16 v[224:225], v182 offset:13632\n"
"v_mfma_f32_16x16x32_bf16 v[232:235], a[216:219], v[64:67], 0\n"
"s_waitcnt lgkmcnt(4)\n"
"ds_read_b64_tr_b16 v[226:227], v183 offset:13632\n"
"ds_read_b64_tr_b16 v[228:229], v182 offset:14400\n"
"v_mfma_f32_16x16x32_bf16 v[232:235], a[228:231], v[68:71], v[232:235]\n"
"ds_read_b64_tr_b16 v[230:231], v183 offset:14400\n"
"v_exp_f32 v113, v113\n"
"v_mfma_f32_16x16x32_bf16 v[236:239], a[220:223], v[64:67], 0\n"
"ds_write_b128 v186, v[212:215] offset:47392\n"
"v_mfma_f32_16x16x32_bf16 v[236:239], a[232:235], v[68:71], v[236:239]\n"
"v_exp_f32 v114, v114\n"
"v_mfma_f32_16x16x32_bf16 v[240:243], a[224:227], v[64:67], 0\n"
"v_exp_f32 v115, v115\n"
"v_mfma_f32_16x16x32_bf16 v[240:243], a[236:239], v[68:71], v[240:243]\n"
"ds_read_b128 v[64:67], v192 offset:57344\n"
"ds_read_b128 v[68:71], v192 offset:58368\n"
"v_smfmac_f32_16x16x64_bf16 v[98:101], v[82:85], v[244:251], v63\n"
"ds_read_b64_tr_b16 v[244:245], v182 offset:13640\n"
"v_smfmac_f32_16x16x64_bf16 v[106:109], v[82:85], v[128:135], v63\n"
"ds_read_b64_tr_b16 v[246:247], v183 offset:13640\n"
"v_mfma_f32_16x16x32_bf16 v[82:85], v[172:175], v[82:85], 0\n"
"ds_read_b64_tr_b16 v[248:249], v182 offset:14408\n"
"v_cvt_pk_bf16_f32 v90, v112, v114\n"
"v_mfma_f32_16x16x32_bf16 v[204:207], a[0:3], v[32:35], 0\n"
"ds_read_b64_tr_b16 v[250:251], v183 offset:14408\n"
"v_cvt_pk_bf16_f32 v91, v113, v115\n"
"v_mfma_f32_16x16x32_bf16 v[204:207], a[12:15], v[36:39], v[204:207]\n"
"ds_read_b128 v[120:123], v178\n"
"v_exp_f32 v116, v116\n"
"v_mfma_f32_16x16x32_bf16 v[204:207], a[24:27], v[40:43], v[204:207]\n"
"s_waitcnt lgkmcnt(14)\n"
"ds_read_b128 v[136:139], v179\n"
"ds_read_b128 v[124:127], v178 offset:13504\n"
"v_mfma_f32_16x16x32_bf16 v[204:207], a[36:39], v[44:47], v[204:207]\n"
"s_waitcnt lgkmcnt(8)\n"
"v_exp_f32 v117, v117\n"
"v_mfma_f32_16x16x32_bf16 v[232:235], a[240:243], v[64:67], v[232:235]\n"
"s_waitcnt lgkmcnt(7)\n"
"ds_read_b128 v[140:143], v179 offset:13504\n"
"ds_read_b128 v[128:131], v178 offset:27008\n"
"v_mfma_f32_16x16x32_bf16 v[232:235], a[252:255], v[68:71], v[232:235]\n"
"v_exp_f32 v118, v118\n"
"v_mfma_f32_16x16x32_bf16 v[236:239], a[244:247], v[64:67], v[236:239]\n"
"ds_read_b128 v[112:115], v179 offset:27008\n"
"v_cvt_pk_bf16_f32 v92, v116, v118\n"
"v_mfma_f32_16x16x32_bf16 v[236:239], v[0:3], v[68:71], v[236:239]\n"
"v_exp_f32 v119, v119\n"
"v_mfma_f32_16x16x32_bf16 v[240:243], a[248:251], v[64:67], v[240:243]\n"
"ds_read_b128 v[64:67], v192 offset:59392\n"
"v_cvt_pk_bf16_f32 v93, v117, v119\n"
"v_mfma_f32_16x16x32_bf16 v[240:243], v[4:7], v[68:71], v[240:243]\n"
"s_waitcnt lgkmcnt(7)\n"
"ds_read_b128 v[68:71], v192 offset:60416\n"
"v_smfmac_f32_16x16x64_bf16 v[252:255], v[90:93], v[224:231], v63\n"
"v_smfmac_f32_16x16x64_bf16 v[98:101], v[90:93], v[244:251], v63\n"
"v_smfmac_f32_16x16x64_bf16 v[106:109], v[90:93], a[168:175], v63\n"
"v_mfma_f32_16x16x32_bf16 v[90:93], v[172:175], v[90:93], v[82:85]\n"
"v_mfma_f32_16x16x32_bf16 v[204:207], a[48:51], v[48:51], v[204:207]\n"
"s_nop 1\n"
"ds_read_b64 v[82:83], v178 offset:64\n"
"ds_read_b64 v[84:85], v179 offset:64\n"
"v_mfma_f32_16x16x32_bf16 v[204:207], a[60:63], v[52:55], v[204:207]\n"
"s_waitcnt lgkmcnt(3)\n"
"v_cvt_pk_bf16_f32 v252, v252, v253\n"
"v_cvt_pk_bf16_f32 v253, v254, v255\n"
"v_mfma_f32_16x16x32_bf16 v[232:235], v[8:11], v[64:67], v[232:235]\n"
"s_waitcnt lgkmcnt(2)\n"
"v_rcp_f32 v171, v90\n"
"v_mfma_f32_16x16x32_bf16 v[232:235], v[20:23], v[68:71], v[232:235]\n"
"ds_read_b64 v[90:91], v178 offset:13568\n"
"v_cvt_pk_bf16_f32 v171, v171, v171\n"
"v_mfma_f32_16x16x32_bf16 v[240:243], v[16:19], v[64:67], v[240:243]\n"
"v_and_b32 v92, v171, v176\n"
"v_and_b32 v93, v171, v177\n"
"v_mfma_f32_16x16x32_bf16 v[240:243], v[28:31], v[68:71], v[240:243]\n"
"v_cvt_pk_bf16_f32 v98, v98, v99\n"
"v_cvt_pk_bf16_f32 v99, v100, v101\n"
"v_mfma_f32_16x16x32_bf16 v[236:239], v[12:15], v[64:67], v[236:239]\n"
"v_cvt_pk_bf16_f32 v232, v232, v233\n"
"v_cvt_pk_bf16_f32 v233, v234, v235\n"
"v_mfma_f32_16x16x32_bf16 v[236:239], v[24:27], v[68:71], v[236:239]\n"
"s_waitcnt lgkmcnt(2)\n"
"v_cvt_pk_bf16_f32 v106, v106, v107\n"
"v_cvt_pk_bf16_f32 v240, v240, v241\n"
"v_mfma_f32_16x16x16_bf16 v[252:255], v[252:253], v[92:93], 0\n"
"s_waitcnt vmcnt(10) lgkmcnt(1)\n"
"v_cvt_pk_bf16_f32 v241, v242, v243\n"
"v_cvt_pk_bf16_f32 v107, v108, v109\n"
"v_mfma_f32_16x16x16_bf16 v[98:101], v[98:99], v[92:93], 0\n"
"ds_write_b128 v202, a[198:201] offset:30720\n"
"v_mfma_f32_16x16x16_bf16 v[106:109], v[106:107], v[92:93], 0\n"
"s_waitcnt lgkmcnt(1)\n"
"v_cvt_pk_bf16_f32 v234, v236, v237\n"
"v_cvt_pk_bf16_f32 v235, v238, v239\n"
"v_mfma_f32_16x16x32_bf16 v[120:123], v[120:123], v[144:147], 0\n"
"s_waitcnt vmcnt(9)\n"
"buffer_load_dword v200, off, s[28:31], s39 sc1\n"
"ds_write_b64 v203, a[202:203] offset:30720\n"
"v_mfma_f32_16x16x32_bf16 v[120:123], v[80:83], v[156:159], v[120:123]\n"
"v_cvt_pk_bf16_f32 v252, v252, v253\n"
"v_cvt_pk_bf16_f32 v253, v254, v255\n"
"v_mfma_f32_16x16x32_bf16 v[136:139], v[136:139], v[144:147], 0\n"
"ds_write_b128 v196, v[232:235] offset:43008\n"
"v_mfma_f32_16x16x32_bf16 v[136:139], v[84:87], v[158:161], v[136:139]\n"
"v_cvt_pk_bf16_f32 v254, v98, v99\n"
"v_cvt_pk_bf16_f32 v255, v100, v101\n"
"v_mfma_f32_16x16x32_bf16 v[208:211], a[4:7], v[32:35], 0\n"
"v_cvt_pk_bf16_f32 v106, v106, v107\n"
"v_cvt_pk_bf16_f32 v107, v108, v109\n"
"v_mfma_f32_16x16x32_bf16 v[208:211], a[16:19], v[36:39], v[208:211]\n"
"ds_write_b64 v197, v[240:241] offset:43008\n"
"v_mfma_f32_16x16x32_bf16 v[208:211], a[28:31], v[40:43], v[208:211]\n"
"v_min_u32 v252, -2, v252\n"
"v_min_u32 v253, -2, v253\n"
"v_mfma_f32_16x16x32_bf16 v[208:211], a[40:43], v[44:47], v[208:211]\n"
"ds_read_b64 v[92:93], v179 offset:13568\n"
"ds_read_b64 v[98:99], v178 offset:27072\n"
"v_mfma_f32_16x16x32_bf16 v[208:211], a[52:55], v[48:51], v[208:211]\n"
"ds_read_b64 v[100:101], v179 offset:27072\n"
"v_min_u32 v254, -2, v254\n"
"v_mfma_f32_16x16x32_bf16 v[208:211], a[64:67], v[52:55], v[208:211]\n"
"v_min_u32 v255, -2, v255\n"
"v_min_u32 v106, -2, v106\n"
"v_mfma_f32_16x16x32_bf16 v[124:127], v[124:127], v[144:147], 0\n"
"buffer_store_dwordx4 v[252:255], v192, s[28:31], s37 offen sc1\n"
"v_mfma_f32_16x16x32_bf16 v[124:127], v[88:91], v[156:159], v[124:127]\n"
"s_waitcnt lgkmcnt(3)\n"
"v_min_u32 v107, -2, v107\n"
"v_cvt_pk_bf16_f32 v204, v204, v205\n"
"v_mfma_f32_16x16x32_bf16 v[212:215], a[8:11], v[32:35], 0\n"
"s_barrier\n"
"v_mfma_f32_16x16x32_bf16 v[212:215], a[20:23], v[36:39], v[212:215]\n"
"s_waitcnt vmcnt(8) lgkmcnt(2)\n"
"buffer_store_dwordx2 v[106:107], v193, s[28:31], s37 offen offset:1024 sc1\n"
"v_mfma_f32_16x16x32_bf16 v[140:143], v[140:143], v[144:147], 0\n"
"s_waitcnt lgkmcnt(1)\n"
"ds_write_b128 v202, a[144:147]\n"
"v_mfma_f32_16x16x32_bf16 v[140:143], v[92:95], v[158:161], v[140:143]\n"
"s_waitcnt vmcnt(6) lgkmcnt(1)\n"
"v_or_b32 v201, 11, s6\n"
"v_cvt_pk_bf16_f32 v205, v206, v207\n"
"v_mfma_f32_16x16x32_bf16 v[128:131], v[128:131], v[144:147], 0\n"
"s_add_u32 s37, s37, 1572864\n"
"s_mov_b64 exec, 1\n"
"buffer_store_dword v201, off, s[28:31], s40 sc1\n"
"s_mov_b64 exec, -1\n"
"v_mfma_f32_16x16x32_bf16 v[128:131], v[96:99], v[156:159], v[128:131]\n"
"ds_write_b64 v203, a[148:149]\n"
"v_mfma_f32_16x16x32_bf16 v[112:115], v[112:115], v[144:147], 0\n"
"v_cvt_pk_bf16_f32 v206, v208, v209\n"
"v_cvt_pk_bf16_f32 v207, v210, v211\n"
"v_mfma_f32_16x16x32_bf16 v[112:115], v[100:103], v[158:161], v[112:115]\n"
"ds_read_b128 v[132:135], v180\n"
"ds_read_b64 v[106:107], v180 offset:64\n"
"v_mfma_f32_16x16x32_bf16 v[212:215], a[32:35], v[40:43], v[212:215]\n"
"ds_read_b128 v[116:119], v181\n"
"v_max_f32 v120, v120, v136\n"
"v_mfma_f32_16x16x32_bf16 v[212:215], a[44:47], v[44:47], v[212:215]\n"
"ds_read_b64 v[108:109], v181 offset:64\n"
"v_max_f32 v121, v121, v137\n"
"v_mfma_f32_16x16x32_bf16 v[212:215], a[56:59], v[48:51], v[212:215]\n"
"ds_write_b128 v202, a[204:207] offset:55296\n"
"v_mfma_f32_16x16x32_bf16 v[212:215], a[68:71], v[52:55], v[212:215]\n"
"v_max_f32 v122, v122, v138\n"
"v_max_f32 v123, v123, v139\n"
"v_mfma_f32_16x16x32_bf16 v[208:211], a[80:83], v[32:35], 0\n"
"v_max3_f32 v170, v120, v121, v122\n"
"v_max_f32 v124, v124, v140\n"
"v_mfma_f32_16x16x32_bf16 v[208:211], a[92:95], v[36:39], v[208:211]\n"
"ds_write_b128 v198, v[204:207]\n"
"v_mfma_f32_16x16x32_bf16 v[208:211], a[104:107], v[40:43], v[208:211]\n"
"s_waitcnt lgkmcnt(4)\n"
"v_max_f32 v125, v125, v141\n"
"v_max_f32 v126, v126, v142\n"
"v_mfma_f32_16x16x32_bf16 v[132:135], v[132:135], v[144:147], 0\n"
"v_max_f32 v127, v127, v143\n"
"v_max3_f32 v170, v170, v123, v124\n"
"v_mfma_f32_16x16x32_bf16 v[132:135], v[104:107], v[156:159], v[132:135]\n"
"s_waitcnt lgkmcnt(2)\n"
"ds_read_b128 v[64:67], v202 offset:43008\n"
"ds_read_b64 v[68:69], v62 offset:43008\n"
"v_mfma_f32_16x16x32_bf16 v[116:119], v[116:119], v[144:147], 0\n"
"ds_read_b64_tr_b16 v[136:137], v182 offset:72\n"
"ds_read_b64_tr_b16 v[138:139], v183 offset:72\n"
"v_mfma_f32_16x16x32_bf16 v[116:119], v[108:111], v[158:161], v[116:119]\n"
"ds_read_b64_tr_b16 v[140:141], v182 offset:840\n"
"v_cvt_pk_bf16_f32 v212, v212, v213\n"
"v_mfma_f32_16x16x32_bf16 v[208:211], a[116:119], v[44:47], v[208:211]\n"
"ds_read_b64_tr_b16 v[142:143], v183 offset:840\n"
"v_max3_f32 v170, v170, v125, v126\n"
"v_mfma_f32_16x16x32_bf16 v[208:211], a[128:131], v[48:51], v[208:211]\n"
"ds_write_b64 v203, a[208:209] offset:55296\n"
"v_mfma_f32_16x16x32_bf16 v[208:211], a[140:143], v[52:55], v[208:211]\n"
"v_cvt_pk_bf16_f32 v213, v214, v215\n"
"v_max_f32 v128, v128, v112\n"
"v_mfma_f32_16x16x32_bf16 v[204:207], a[72:75], v[32:35], 0\n"
"v_max_f32 v129, v129, v113\n"
"v_max_f32 v130, v130, v114\n"
"v_mfma_f32_16x16x32_bf16 v[204:207], a[84:87], v[36:39], v[204:207]\n"
"s_waitcnt lgkmcnt(5)\n"
"buffer_store_dwordx4 v[64:67], v194, s[24:27], s36 offen sc1\n"
"v_mfma_f32_16x16x32_bf16 v[204:207], a[96:99], v[40:43], v[204:207]\n"
"s_nop 0\n"
"ds_read_b128 v[64:67], v192 offset:30720\n"
"ds_read_b64_tr_b16 v[112:113], v182 offset:27080\n"
"v_mfma_f32_16x16x32_bf16 v[204:207], a[108:111], v[44:47], v[204:207]\n"
"v_max_f32 v131, v131, v115\n"
"v_max3_f32 v170, v170, v127, v128\n"
"v_mfma_f32_16x16x32_bf16 v[204:207], a[120:123], v[48:51], v[204:207]\n"
"buffer_store_dwordx2 v[68:69], v195, s[24:27], s36 offen sc1\n"
"v_mfma_f32_16x16x32_bf16 v[204:207], a[132:135], v[52:55], v[204:207]\n"
"s_add_u32 s36, s36, s33\n"
"ds_read_b128 v[68:71], v192 offset:31744\n"
"v_max3_f32 v170, v170, v129, v130\n"
"v_max_f32 v132, v132, v116\n"
"v_max_f32 v133, v133, v117\n"
"v_max_f32 v134, v134, v118\n"
"v_max_f32 v135, v135, v119\n"
"v_max3_f32 v170, v170, v131, v132\n"
"v_max3_f32 v170, v170, v133, v134\n"
"v_mov_b64 v[252:253], 0\n"
"v_mov_b64 v[254:255], 0\n"
"v_mov_b64 v[98:99], 0\n"
"v_mov_b64 v[100:101], 0\n"
"v_mov_b64 v[106:107], 0\n"
"v_mov_b64 v[108:109], 0\n"
"v_max_f32 v171, v170, v135\n"
"v_max_f32 v170, v170, v135\n"
"v_cvt_pk_bf16_f32 v214, v208, v209\n"
"v_cvt_pk_bf16_f32 v215, v210, v211\n"
"v_mfma_f32_16x16x32_bf16 v[208:211], a[76:79], v[32:35], 0\n"
"s_waitcnt lgkmcnt(2)\n"
"ds_read_b64_tr_b16 v[114:115], v183 offset:27080\n"
"ds_read_b64_tr_b16 v[116:117], v182 offset:27848\n"
"v_mfma_f32_16x16x32_bf16 v[232:235], a[216:219], v[64:67], 0\n"
"ds_read_b64_tr_b16 v[118:119], v183 offset:27848\n"
"v_permlane16_swap_b32 v170, v171\n"
"v_mfma_f32_16x16x32_bf16 v[236:239], a[220:223], v[64:67], 0\n"
"s_waitcnt lgkmcnt(3)\n"
"ds_write_b128 v198, v[212:215] offset:64\n"
"v_mfma_f32_16x16x32_bf16 v[236:239], a[232:235], v[68:71], v[236:239]\n"
"v_max_f32 v170, v170, v171\n"
"v_mov_b32 v171, v170\n"
"v_mfma_f32_16x16x32_bf16 v[240:243], a[224:227], v[64:67], 0\n"
"s_waitcnt vmcnt(5)\n"
"v_permlane32_swap_b32 v170, v171\n"
"v_max_f32 v170, v170, v171\n"
"v_mul_f32 v170, 0xbe553b95, v170\n"
"v_pk_fma_f32 v[120:121], v[120:121], s[70:71], v[170:171] op_sel_hi:[1,0,0]\n"
"v_pk_fma_f32 v[122:123], v[122:123], s[70:71], v[170:171] op_sel_hi:[1,0,0]\n"
"v_pk_fma_f32 v[124:125], v[124:125], s[70:71], v[170:171] op_sel_hi:[1,0,0]\n"
"v_pk_fma_f32 v[126:127], v[126:127], s[70:71], v[170:171] op_sel_hi:[1,0,0]\n"
"v_pk_fma_f32 v[128:129], v[128:129], s[70:71], v[170:171] op_sel_hi:[1,0,0]\n"
"v_pk_fma_f32 v[130:131], v[130:131], s[70:71], v[170:171] op_sel_hi:[1,0,0]\n"
"v_pk_fma_f32 v[132:133], v[132:133], s[70:71], v[170:171] op_sel_hi:[1,0,0]\n"
"v_pk_fma_f32 v[134:135], v[134:135], s[70:71], v[170:171] op_sel_hi:[1,0,0]\n"
"v_mfma_f32_16x16x32_bf16 v[240:243], a[236:239], v[68:71], v[240:243]\n"
"v_add3_u32 v200, v200, s50, -10\n"
"v_cmp_lt_u32 vcc, 64, v200\n"
"v_mfma_f32_16x16x32_bf16 v[232:235], a[228:231], v[68:71], v[232:235]\n"
"ds_read_b128 v[64:67], v192 offset:32768\n"
"ds_read_b128 v[68:71], v192 offset:33792\n"
"v_mfma_f32_16x16x32_bf16 v[208:211], a[88:91], v[36:39], v[208:211]\n"
"s_nop 0\n"
"s_cbranch_vccnz .Lp9%=\n"
".Lr9%=:\n"
"v_mfma_f32_16x16x32_bf16 v[208:211], a[100:103], v[40:43], v[208:211]\n"
"buffer_load_dwordx4 a[150:153], v192, s[28:31], s13 offen sc1\n"
"v_exp_f32 v120, v120\n"
"v_mfma_f32_16x16x32_bf16 v[208:211], a[112:115], v[44:47], v[208:211]\n"
"v_exp_f32 v121, v121\n"
"v_mfma_f32_16x16x32_bf16 v[208:211], a[124:127], v[48:51], v[208:211]\n"
"v_exp_f32 v122, v122\n"
"v_mfma_f32_16x16x32_bf16 v[208:211], a[136:139], v[52:55], v[208:211]\n"
"buffer_load_dwordx2 a[154:155], v193, s[28:31], s13 offen offset:1024 sc1\n"
"v_exp_f32 v123, v123\n"
"v_cvt_pk_bf16_f32 v82, v120, v122\n"
"v_cvt_pk_bf16_f32 v83, v121, v123\n"
"v_exp_f32 v124, v124\n"
"v_cvt_pk_bf16_f32 v204, v204, v205\n"
"v_exp_f32 v125, v125\n"
"v_exp_f32 v126, v126\n"
"v_exp_f32 v127, v127\n"
"v_cvt_pk_bf16_f32 v84, v124, v126\n"
"v_cvt_pk_bf16_f32 v85, v125, v127\n"
"v_cvt_pk_bf16_f32 v205, v206, v207\n"
"v_exp_f32 v128, v128\n"
"v_smfmac_f32_16x16x64_bf16 v[252:255], v[82:85], v[216:223], v63\n"
"s_waitcnt lgkmcnt(1)\n"
"buffer_load_dwordx4 a[156:159], v192, s[28:31], s14 offen sc1\n"
"v_mfma_f32_16x16x32_bf16 v[232:235], a[240:243], v[64:67], v[232:235]\n"
"s_waitcnt lgkmcnt(0)\n"
"ds_read_b64_tr_b16 v[216:217], v182 offset:27136\n"
"ds_read_b64_tr_b16 v[218:219], v183 offset:27136\n"
"v_mfma_f32_16x16x32_bf16 v[232:235], a[252:255], v[68:71], v[232:235]\n"
"ds_read_b64_tr_b16 v[220:221], v182 offset:27904\n"
"v_cvt_pk_bf16_f32 v206, v208, v209\n"
"v_mfma_f32_16x16x32_bf16 v[236:239], a[244:247], v[64:67], v[236:239]\n"
"ds_read_b64_tr_b16 v[222:223], v183 offset:27904\n"
"buffer_load_dwordx2 a[160:161], v193, s[28:31], s14 offen offset:1024 sc1\n"
"v_mfma_f32_16x16x32_bf16 v[236:239], v[0:3], v[68:71], v[236:239]\n"
"ds_read_b128 v[120:123], v178 offset:13504\n"
"v_cvt_pk_bf16_f32 v207, v210, v211\n"
"v_smfmac_f32_16x16x64_bf16 v[98:101], v[82:85], v[72:79], v63\n"
"ds_read_b64_tr_b16 v[72:73], v182 offset:27144\n"
"v_mfma_f32_16x16x32_bf16 v[240:243], a[248:251], v[64:67], v[240:243]\n"
"ds_read_b128 v[64:67], v192 offset:34816\n"
"v_exp_f32 v129, v129\n"
"v_mfma_f32_16x16x32_bf16 v[240:243], v[4:7], v[68:71], v[240:243]\n"
"ds_read_b128 v[68:71], v192 offset:35840\n"
"ds_read_b64_tr_b16 v[74:75], v183 offset:27144\n"
"v_smfmac_f32_16x16x64_bf16 v[106:109], v[82:85], v[136:143], v63\n"
"ds_read_b64_tr_b16 v[76:77], v182 offset:27912\n"
"v_mfma_f32_16x16x32_bf16 v[82:85], v[172:175], v[82:85], 0\n"
"v_exp_f32 v130, v130\n"
"ds_read_b64_tr_b16 v[78:79], v183 offset:27912\n"
"v_exp_f32 v131, v131\n"
"v_cvt_pk_bf16_f32 v90, v128, v130\n"
"v_cvt_pk_bf16_f32 v91, v129, v131\n"
"v_exp_f32 v132, v132\n"
"v_exp_f32 v133, v133\n"
"v_exp_f32 v134, v134\n"
"v_exp_f32 v135, v135\n"
"v_cvt_pk_bf16_f32 v92, v132, v134\n"
"v_cvt_pk_bf16_f32 v93, v133, v135\n"
"s_waitcnt lgkmcnt(4)\n"
"s_nop 0\n"
"v_smfmac_f32_16x16x64_bf16 v[252:255], v[90:93], v[216:223], v63\n"
"ds_write_b128 v198, v[204:207] offset:128\n"
"v_mfma_f32_16x16x32_bf16 v[232:235], v[8:11], v[64:67], v[232:235]\n"
"s_waitcnt lgkmcnt(4)\n"
"v_mfma_f32_16x16x32_bf16 v[232:235], v[20:23], v[68:71], v[232:235]\n"
"s_waitcnt lgkmcnt(1)\n"
"ds_read_b128 v[136:139], v179 offset:13504\n"
"v_smfmac_f32_16x16x64_bf16 v[98:101], v[90:93], v[72:79], v63\n"
"ds_read_b128 v[124:127], v178 offset:27008\n"
"v_smfmac_f32_16x16x64_bf16 v[106:109], v[90:93], v[112:119], v63\n"
"ds_read_b128 v[140:143], v179 offset:27008\n"
"v_mfma_f32_16x16x32_bf16 v[90:93], v[172:175], v[90:93], v[82:85]\n"
"ds_read_b128 v[128:131], v180\n"
"ds_read_b128 v[112:115], v181\n"
"v_mfma_f32_16x16x32_bf16 v[236:239], v[12:15], v[64:67], v[236:239]\n"
"ds_read_b64 v[82:83], v178 offset:13568\n"
"v_cvt_pk_bf16_f32 v252, v252, v253\n"
"v_mfma_f32_16x16x32_bf16 v[236:239], v[24:27], v[68:71], v[236:239]\n"
"s_waitcnt lgkmcnt(6)\n"
"ds_read_b64 v[84:85], v179 offset:13568\n"
"v_cvt_pk_bf16_f32 v253, v254, v255\n"
"v_mfma_f32_16x16x32_bf16 v[240:243], v[16:19], v[64:67], v[240:243]\n"
"s_barrier\n"
"v_mfma_f32_16x16x32_bf16 v[240:243], v[28:31], v[68:71], v[240:243]\n"
"ds_read_b128 v[64:67], v192 offset:55296\n"
"ds_read_b128 v[68:71], v192 offset:56320\n"
"v_rcp_f32 v171, v90\n"
"buffer_load_dwordx4 v[52:55], v192, s[28:31], s15 offen sc1\n"
"v_cvt_pk_bf16_f32 v171, v171, v171\n"
"v_and_b32 v92, v171, v176\n"
"v_and_b32 v93, v171, v177\n"
"v_cvt_pk_bf16_f32 v98, v98, v99\n"
"v_cvt_pk_bf16_f32 v99, v100, v101\n"
"v_mfma_f32_16x16x16_bf16 v[252:255], v[252:253], v[92:93], 0\n"
"ds_read_b64 v[90:91], v178 offset:27072\n"
"ds_read_b128 v[132:135], v180 offset:13504\n"
"v_mfma_f32_16x16x16_bf16 v[98:101], v[98:99], v[92:93], 0\n"
"buffer_load_dwordx2 v[152:153], v193, s[28:31], s15 offen offset:1024 sc1\n"
"ds_read_b128 v[116:119], v181 offset:13504\n"
"v_cvt_pk_bf16_f32 v106, v106, v107\n"
"v_cvt_pk_bf16_f32 v107, v108, v109\n"
"v_cvt_pk_bf16_f32 v232, v232, v233\n"
"v_cvt_pk_bf16_f32 v252, v252, v253\n"
"v_mfma_f32_16x16x16_bf16 v[106:109], v[106:107], v[92:93], 0\n"
"s_waitcnt lgkmcnt(5)\n"
"ds_read_b64 v[92:93], v179 offset:27072\n"
"v_cvt_pk_bf16_f32 v253, v254, v255\n"
"v_mfma_f32_16x16x32_bf16 v[120:123], v[120:123], v[148:151], 0\n"
"v_cvt_pk_bf16_f32 v254, v98, v99\n"
"v_cvt_pk_bf16_f32 v255, v100, v101\n"
"v_mfma_f32_16x16x32_bf16 v[120:123], v[80:83], v[160:163], v[120:123]\n"
"buffer_load_dword v201, off, s[28:31], s39 sc1\n"
"ds_read_b64 v[98:99], v180 offset:64\n"
"v_mfma_f32_16x16x32_bf16 v[136:139], v[136:139], v[148:151], 0\n"
"ds_read_b64 v[100:101], v181 offset:64\n"
"v_cvt_pk_bf16_f32 v106, v106, v107\n"
"v_mfma_f32_16x16x32_bf16 v[136:139], v[84:87], v[162:165], v[136:139]\n"
"s_waitcnt lgkmcnt(5)\n"
"v_cvt_pk_bf16_f32 v107, v108, v109\n"
"v_min_u32 v252, -2, v252\n"
"v_mfma_f32_16x16x32_bf16 v[124:127], v[124:127], v[148:151], 0\n"
"ds_read_b64 v[108:109], v181 offset:13568\n"
"v_min_u32 v253, -2, v253\n"
"v_mfma_f32_16x16x32_bf16 v[124:127], v[88:91], v[160:163], v[124:127]\n"
"v_min_u32 v254, -2, v254\n"
"v_min_u32 v255, -2, v255\n"
"v_min_u32 v106, -2, v106\n"
"v_min_u32 v107, -2, v107\n"
"v_cvt_pk_bf16_f32 v233, v234, v235\n"
"v_cvt_pk_bf16_f32 v234, v236, v237\n"
"v_cvt_pk_bf16_f32 v235, v238, v239\n"
"v_mfma_f32_16x16x32_bf16 v[236:239], a[220:223], v[64:67], 0\n"
"s_waitcnt lgkmcnt(3)\n"
"buffer_store_dwordx4 v[252:255], v192, s[28:31], s37 offen sc1\n"
"v_mfma_f32_16x16x32_bf16 v[236:239], a[232:235], v[68:71], v[236:239]\n"
"s_waitcnt lgkmcnt(1)\n"
"ds_write_b128 v196, v[232:235] offset:49152\n"
"v_mfma_f32_16x16x32_bf16 v[140:143], v[140:143], v[148:151], 0\n"
"s_waitcnt lgkmcnt(1)\n"
"buffer_store_dwordx2 v[106:107], v193, s[28:31], s37 offen offset:1024 sc1\n"
"v_mfma_f32_16x16x32_bf16 v[140:143], v[92:95], v[162:165], v[140:143]\n"
"s_add_u32 s37, s37, 1572864\n"
"ds_read_b64 v[106:107], v180 offset:13568\n"
"v_cvt_pk_bf16_f32 v240, v240, v241\n"
"v_mfma_f32_16x16x32_bf16 v[128:131], v[128:131], v[148:151], 0\n"
"v_cvt_pk_bf16_f32 v241, v242, v243\n"
"v_max_f32 v120, v120, v136\n"
"v_mfma_f32_16x16x32_bf16 v[128:131], v[96:99], v[160:163], v[128:131]\n"
"ds_write_b64 v197, v[240:241] offset:49152\n"
"v_mfma_f32_16x16x32_bf16 v[112:115], v[112:115], v[148:151], 0\n"
"v_max_f32 v121, v121, v137\n"
"v_max_f32 v122, v122, v138\n"
"v_mfma_f32_16x16x32_bf16 v[112:115], v[100:103], v[162:165], v[112:115]\n"
"ds_read_b64_tr_b16 v[136:137], v182 offset:40584\n"
"v_max_f32 v123, v123, v139\n"
"v_mfma_f32_16x16x32_bf16 v[116:119], v[116:119], v[148:151], 0\n"
"ds_read_b64_tr_b16 v[138:139], v183 offset:40584\n"
"v_max3_f32 v170, v120, v121, v122\n"
"v_mfma_f32_16x16x32_bf16 v[116:119], v[108:111], v[162:165], v[116:119]\n"
"v_max_f32 v124, v124, v140\n"
"v_max_f32 v125, v125, v141\n"
"v_mfma_f32_16x16x32_bf16 v[232:235], a[216:219], v[64:67], 0\n"
"s_waitcnt lgkmcnt(3)\n"
"ds_read_b64_tr_b16 v[140:141], v182 offset:41352\n"
"v_max_f32 v126, v126, v142\n"
"v_mfma_f32_16x16x32_bf16 v[232:235], a[228:231], v[68:71], v[232:235]\n"
"v_max3_f32 v170, v170, v123, v124\n"
"v_max_f32 v127, v127, v143\n"
"v_mfma_f32_16x16x32_bf16 v[132:135], v[132:135], v[148:151], 0\n"
"ds_read_b64_tr_b16 v[142:143], v183 offset:41352\n"
"v_max3_f32 v170, v170, v125, v126\n"
"v_mfma_f32_16x16x32_bf16 v[132:135], v[104:107], v[160:163], v[132:135]\n"
"v_max_f32 v128, v128, v112\n"
"v_max_f32 v129, v129, v113\n"
"v_mfma_f32_16x16x32_bf16 v[240:243], a[224:227], v[64:67], 0\n"
"ds_read_b128 v[64:67], v192 offset:57344\n"
"v_max_f32 v130, v130, v114\n"
"v_mfma_f32_16x16x32_bf16 v[240:243], a[236:239], v[68:71], v[240:243]\n"
"ds_read_b128 v[68:71], v192 offset:58368\n"
"v_max3_f32 v170, v170, v127, v128\n"
"v_max_f32 v131, v131, v115\n"
"v_max3_f32 v170, v170, v129, v130\n"
"v_max_f32 v132, v132, v116\n"
"v_max_f32 v133, v133, v117\n"
"v_max_f32 v134, v134, v118\n"
"v_max3_f32 v170, v170, v131, v132\n"
"v_max_f32 v135, v135, v119\n"
"v_max3_f32 v170, v170, v133, v134\n"
"v_max_f32 v171, v170, v135\n"
"v_max_f32 v170, v170, v135\n"
"v_mov_b64 v[252:253], 0\n"
"v_mov_b64 v[254:255], 0\n"
"v_permlane16_swap_b32 v170, v171\n"
"v_max_f32 v170, v170, v171\n"
"v_mov_b32 v171, v170\n"
"v_mov_b64 v[98:99], 0\n"
"v_mov_b64 v[100:101], 0\n"
"v_permlane32_swap_b32 v170, v171\n"
"v_max_f32 v170, v170, v171\n"
"v_mul_f32 v170, 0xbe553b95, v170\n"
"v_pk_fma_f32 v[120:121], v[120:121], s[70:71], v[170:171] op_sel_hi:[1,0,0]\n"
"v_pk_fma_f32 v[122:123], v[122:123], s[70:71], v[170:171] op_sel_hi:[1,0,0]\n"
"v_pk_fma_f32 v[124:125], v[124:125], s[70:71], v[170:171] op_sel_hi:[1,0,0]\n"
"v_pk_fma_f32 v[126:127], v[126:127], s[70:71], v[170:171] op_sel_hi:[1,0,0]\n"
"v_pk_fma_f32 v[128:129], v[128:129], s[70:71], v[170:171] op_sel_hi:[1,0,0]\n"
"v_pk_fma_f32 v[130:131], v[130:131], s[70:71], v[170:171] op_sel_hi:[1,0,0]\n"
"v_pk_fma_f32 v[132:133], v[132:133], s[70:71], v[170:171] op_sel_hi:[1,0,0]\n"
"v_pk_fma_f32 v[134:135], v[134:135], s[70:71], v[170:171] op_sel_hi:[1,0,0]\n"
"v_exp_f32 v120, v120\n"
"v_exp_f32 v121, v121\n"
"v_exp_f32 v122, v122\n"
"v_exp_f32 v123, v123\n"
"v_exp_f32 v124, v124\n"
"v_exp_f32 v125, v125\n"
"v_exp_f32 v126, v126\n"
"v_exp_f32 v127, v127\n"
"v_cvt_pk_bf16_f32 v82, v120, v122\n"
"v_cvt_pk_bf16_f32 v83, v121, v123\n"
"v_cvt_pk_bf16_f32 v84, v124, v126\n"
"v_cvt_pk_bf16_f32 v85, v125, v127\n"
"v_mov_b64 v[106:107], 0\n"
"v_mov_b64 v[108:109], 0\n"
"v_smfmac_f32_16x16x64_bf16 v[252:255], v[82:85], v[224:231], v63\n"
"s_waitcnt lgkmcnt(1)\n"
"ds_read_b64_tr_b16 v[224:225], v182 offset:40640\n"
"v_mfma_f32_16x16x32_bf16 v[232:235], a[240:243], v[64:67], v[232:235]\n"
"s_waitcnt lgkmcnt(1)\n"
"ds_read_b64_tr_b16 v[226:227], v183 offset:40640\n"
"ds_read_b64_tr_b16 v[228:229], v182 offset:41408\n"
"v_mfma_f32_16x16x32_bf16 v[232:235], a[252:255], v[68:71], v[232:235]\n"
"ds_read_b64_tr_b16 v[230:231], v183 offset:41408\n"
"v_exp_f32 v128, v128\n"
"v_mfma_f32_16x16x32_bf16 v[236:239], a[244:247], v[64:67], v[236:239]\n"
"v_exp_f32 v129, v129\n"
"v_mfma_f32_16x16x32_bf16 v[236:239], v[0:3], v[68:71], v[236:239]\n"
"v_exp_f32 v130, v130\n"
"v_mfma_f32_16x16x32_bf16 v[240:243], a[248:251], v[64:67], v[240:243]\n"
"ds_read_b128 v[64:67], v192 offset:59392\n"
"v_cvt_pk_bf16_f32 v90, v128, v130\n"
"v_mfma_f32_16x16x32_bf16 v[240:243], v[4:7], v[68:71], v[240:243]\n"
"ds_read_b128 v[68:71], v192 offset:60416\n"
"v_exp_f32 v131, v131\n"
"v_smfmac_f32_16x16x64_bf16 v[98:101], v[82:85], v[244:251], v63\n"
"ds_read_b64_tr_b16 v[244:245], v182 offset:40648\n"
"v_smfmac_f32_16x16x64_bf16 v[106:109], v[82:85], a[168:175], v63\n"
"ds_read_b64_tr_b16 v[246:247], v183 offset:40648\n"
"v_mfma_f32_16x16x32_bf16 v[82:85], v[172:175], v[82:85], 0\n"
"ds_read_b64_tr_b16 v[248:249], v182 offset:41416\n"
"ds_read_b64_tr_b16 v[250:251], v183 offset:41416\n"
"v_exp_f32 v132, v132\n"
"v_exp_f32 v133, v133\n"
"v_exp_f32 v134, v134\n"
"v_exp_f32 v135, v135\n"
"v_cvt_pk_bf16_f32 v91, v129, v131\n"
"v_cvt_pk_bf16_f32 v92, v132, v134\n"
"v_cvt_pk_bf16_f32 v93, v133, v135\n"
"s_waitcnt lgkmcnt(5)\n"
"v_mfma_f32_16x16x32_bf16 v[232:235], v[8:11], v[64:67], v[232:235]\n"
"s_waitcnt lgkmcnt(4)\n"
"s_barrier\n"
"v_mfma_f32_16x16x32_bf16 v[232:235], v[20:23], v[68:71], v[232:235]\n"
"ds_read_b128 v[44:47], v192\n"
"ds_read_b128 v[48:51], v192 offset:1024\n"
"v_mfma_f32_16x16x32_bf16 v[240:243], v[16:19], v[64:67], v[240:243]\n"
"ds_read_b128 a[0:3], v192 offset:2048\n"
"v_mfma_f32_16x16x32_bf16 v[240:243], v[28:31], v[68:71], v[240:243]\n"
"ds_read_b128 a[4:7], v192 offset:3072\n"
"v_mfma_f32_16x16x32_bf16 v[236:239], v[12:15], v[64:67], v[236:239]\n"
"s_waitcnt lgkmcnt(4)\n"
"ds_read_b128 a[8:11], v192 offset:4096\n"
"v_cvt_pk_bf16_f32 v232, v232, v233\n"
"v_mfma_f32_16x16x32_bf16 v[236:239], v[24:27], v[68:71], v[236:239]\n"
"ds_read_b128 a[12:15], v192 offset:5120\n"
"v_cvt_pk_bf16_f32 v233, v234, v235\n"
"v_smfmac_f32_16x16x64_bf16 v[252:255], v[90:93], v[224:231], v63\n"
"ds_read_b128 v[64:67], v202 offset:49152\n"
"v_smfmac_f32_16x16x64_bf16 v[98:101], v[90:93], v[244:251], v63\n"
"ds_read_b64 v[68:69], v62 offset:49152\n"
"v_smfmac_f32_16x16x64_bf16 v[106:109], v[90:93], v[136:143], v63\n"
"s_waitcnt lgkmcnt(7)\n"
"v_cvt_pk_bf16_f32 v240, v240, v241\n"
"v_mfma_f32_16x16x32_bf16 v[90:93], v[172:175], v[90:93], v[82:85]\n"
"v_cvt_pk_bf16_f32 v234, v236, v237\n"
"v_cvt_pk_bf16_f32 v235, v238, v239\n"
"v_mfma_f32_16x16x32_bf16 v[204:207], a[216:219], v[44:47], 0\n"
"s_waitcnt lgkmcnt(6)\n"
"ds_write_b128 v196, v[232:235] offset:36864\n"
"v_mfma_f32_16x16x32_bf16 v[204:207], a[228:231], v[48:51], v[204:207]\n"
"s_waitcnt lgkmcnt(6)\n"
"v_cvt_pk_bf16_f32 v241, v242, v243\n"
"v_cvt_pk_bf16_f32 v252, v252, v253\n"
"v_mfma_f32_16x16x32_bf16 v[204:207], a[240:243], a[0:3], v[204:207]\n"
"s_waitcnt lgkmcnt(5)\n"
"v_rcp_f32 v171, v90\n"
"v_mfma_f32_16x16x32_bf16 v[204:207], a[252:255], a[4:7], v[204:207]\n"
"s_waitcnt lgkmcnt(2)\n"
"ds_write_b64 v197, v[240:241] offset:36864\n"
"v_mfma_f32_16x16x32_bf16 v[204:207], v[8:11], a[8:11], v[204:207]\n"
"s_waitcnt lgkmcnt(2)\n"
"buffer_store_dwordx4 v[64:67], v194, s[24:27], s36 offen sc1\n"
"v_mfma_f32_16x16x32_bf16 v[204:207], v[20:23], a[12:15], v[204:207]\n"
"v_cvt_pk_bf16_f32 v171, v171, v171\n"
"v_and_b32 v92, v171, v176\n"
"v_mfma_f32_16x16x32_bf16 v[208:211], a[220:223], v[44:47], 0\n"
"v_and_b32 v93, v171, v177\n"
"v_cvt_pk_bf16_f32 v253, v254, v255\n"
"v_mfma_f32_16x16x32_bf16 v[208:211], a[232:235], v[48:51], v[208:211]\n"
"buffer_store_dwordx2 v[68:69], v195, s[24:27], s36 offen sc1\n"
"v_mfma_f32_16x16x16_bf16 v[252:255], v[252:253], v[92:93], 0\n"
"s_waitcnt vmcnt(10)\n"
"ds_write_b128 v202, a[150:153] offset:24576\n"
"v_mfma_f32_16x16x32_bf16 v[212:215], a[224:227], v[44:47], 0\n"
"s_add_u32 s36, s36, s33\n"
"v_cvt_pk_bf16_f32 v98, v98, v99\n"
"v_cvt_pk_bf16_f32 v99, v100, v101\n"
"v_mfma_f32_16x16x32_bf16 v[212:215], a[236:239], v[48:51], v[212:215]\n"
"s_waitcnt vmcnt(4)\n"
"v_add3_u32 v201, v201, s50, -11\n"
"v_cmp_lt_u32 vcc, 64, v201\n"
"v_mfma_f32_16x16x16_bf16 v[98:101], v[98:99], v[92:93], 0\n"
".Lic7%=:\n"
"s_cbranch_execz .Lib1%=\n"
"ds_write_b64 v203, a[154:155] offset:24576\n"
"v_mfma_f32_16x16x32_bf16 v[208:211], a[244:247], a[0:3], v[208:211]\n"
"s_nop 1\n"
"s_cbranch_vccnz .Lp10%=\n"
".Lr10%=:\n"
"v_mfma_f32_16x16x32_bf16 v[208:211], v[0:3], a[4:7], v[208:211]\n"
"buffer_load_dwordx4 a[162:165], v192, s[28:31], s16 offen sc1\n"
"ds_write_b128 v202, a[156:159] offset:30720\n"
"v_mfma_f32_16x16x32_bf16 v[208:211], v[12:15], a[8:11], v[208:211]\n"
"v_cvt_pk_bf16_f32 v106, v106, v107\n"
"v_cvt_pk_bf16_f32 v107, v108, v109\n"
"v_mfma_f32_16x16x32_bf16 v[208:211], v[24:27], a[12:15], v[208:211]\n"
"buffer_load_dwordx2 a[166:167], v193, s[28:31], s16 offen offset:1024 sc1\n"
"ds_write_b64 v203, a[160:161] offset:30720\n"
"v_mfma_f32_16x16x16_bf16 v[106:109], v[106:107], v[92:93], 0\n"
"v_cvt_pk_bf16_f32 v252, v252, v253\n"
"v_cvt_pk_bf16_f32 v253, v254, v255\n"
"v_mfma_f32_16x16x32_bf16 v[212:215], a[248:251], a[0:3], v[212:215]\n"
"v_cvt_pk_bf16_f32 v254, v98, v99\n"
"v_cvt_pk_bf16_f32 v255, v100, v101\n"
"v_mfma_f32_16x16x32_bf16 v[212:215], v[4:7], a[4:7], v[212:215]\n"
"v_min_u32 v252, -2, v252\n"
"v_min_u32 v253, -2, v253\n"
"v_mfma_f32_16x16x32_bf16 v[212:215], v[16:19], a[8:11], v[212:215]\n"
"v_cvt_pk_bf16_f32 v106, v106, v107\n"
"v_min_u32 v254, -2, v254\n"
"v_mfma_f32_16x16x32_bf16 v[212:215], v[28:31], a[12:15], v[212:215]\n"
"v_min_u32 v255, -2, v255\n"
"buffer_store_dwordx4 v[252:255], v192, s[28:31], s37 offen sc1\n"
"s_waitcnt lgkmcnt(0)\n"
"s_barrier\n"
"ds_read_b128 v[44:47], v192 offset:24576\n"
"ds_read_b128 v[48:51], v192 offset:25600\n"
"ds_read_b128 a[0:3], v192 offset:26624\n"
"v_cvt_pk_bf16_f32 v107, v108, v109\n"
"v_min_u32 v106, -2, v106\n"
"v_min_u32 v107, -2, v107\n"
"buffer_store_dwordx2 v[106:107], v193, s[28:31], s37 offen offset:1024 sc1\n"
"ds_read_b128 a[4:7], v192 offset:27648\n"
"ds_read_b128 a[8:11], v192 offset:28672\n"
"s_add_u32 s37, s37, 1572864\n"
"ds_read_b128 a[12:15], v192 offset:29696\n"
"v_cvt_pk_bf16_f32 v204, v204, v205\n"
"v_cvt_pk_bf16_f32 v205, v206, v207\n"
"v_cvt_pk_bf16_f32 v206, v208, v209\n"
"v_cvt_pk_bf16_f32 v207, v210, v211\n"
"v_cvt_pk_bf16_f32 v212, v212, v213\n"
"v_cvt_pk_bf16_f32 v213, v214, v215\n"
"buffer_load_dwordx4 v[144:147], v192, s[28:31], s17 offen sc1\n"
"ds_write_b128 v196, v[204:207] offset:12288\n"
"s_waitcnt lgkmcnt(6)\n"
"v_mfma_f32_16x16x32_bf16 v[232:235], a[216:219], v[44:47], 0\n"
"s_waitcnt lgkmcnt(5)\n"
"v_mfma_f32_16x16x32_bf16 v[232:235], a[228:231], v[48:51], v[232:235]\n"
"s_waitcnt lgkmcnt(4)\n"
"buffer_load_dwordx2 v[154:155], v193, s[28:31], s17 offen offset:1024 sc1\n"
"ds_write_b64 v197, v[212:213] offset:12288\n"
"v_mfma_f32_16x16x32_bf16 v[232:235], a[240:243], a[0:3], v[232:235]\n"
"s_waitcnt lgkmcnt(4)\n"
"v_mfma_f32_16x16x32_bf16 v[232:235], a[252:255], a[4:7], v[232:235]\n"
"s_waitcnt lgkmcnt(3)\n"
"ds_read_b128 v[64:67], v202 offset:36864\n"
"ds_read_b64 v[68:69], v62 offset:36864\n"
"v_mfma_f32_16x16x32_bf16 v[232:235], v[8:11], a[8:11], v[232:235]\n"
"s_waitcnt lgkmcnt(4)\n"
"v_mfma_f32_16x16x32_bf16 v[232:235], v[20:23], a[12:15], v[232:235]\n"
"v_mfma_f32_16x16x32_bf16 v[236:239], a[220:223], v[44:47], 0\n"
"v_mfma_f32_16x16x32_bf16 v[236:239], a[232:235], v[48:51], v[236:239]\n"
"v_mfma_f32_16x16x32_bf16 v[236:239], a[244:247], a[0:3], v[236:239]\n"
"v_mfma_f32_16x16x32_bf16 v[236:239], v[0:3], a[4:7], v[236:239]\n"
"v_mfma_f32_16x16x32_bf16 v[236:239], v[12:15], a[8:11], v[236:239]\n"
"s_waitcnt lgkmcnt(1)\n"
"buffer_store_dwordx4 v[64:67], v194, s[24:27], s36 offen sc1\n"
"v_mfma_f32_16x16x32_bf16 v[236:239], v[24:27], a[12:15], v[236:239]\n"
"s_waitcnt lgkmcnt(0)\n"
"ds_read_b128 v[64:67], v192 offset:30720\n"
"v_cvt_pk_bf16_f32 v232, v232, v233\n"
"v_mfma_f32_16x16x32_bf16 v[240:243], a[224:227], v[44:47], 0\n"
"ds_read_b128 v[44:47], v192 offset:32768\n"
"v_cvt_pk_bf16_f32 v233, v234, v235\n"
"v_mfma_f32_16x16x32_bf16 v[240:243], a[236:239], v[48:51], v[240:243]\n"
"ds_read_b128 v[48:51], v192 offset:33792\n"
"buffer_store_dwordx2 v[68:69], v195, s[24:27], s36 offen sc1\n"
"v_mfma_f32_16x16x32_bf16 v[240:243], a[248:251], a[0:3], v[240:243]\n"
"s_add_u32 s36, s36, s33\n"
"ds_read_b128 v[68:71], v192 offset:31744\n"
"ds_read_b128 a[0:3], v192 offset:34816\n"
"v_mfma_f32_16x16x32_bf16 v[240:243], v[4:7], a[4:7], v[240:243]\n"
"s_add_u32 s8, s8, s36\n"
"ds_read_b128 a[4:7], v192 offset:35840\n"
"v_cvt_pk_bf16_f32 v234, v236, v237\n"
"v_mfma_f32_16x16x32_bf16 v[240:243], v[16:19], a[8:11], v[240:243]\n"
"s_add_u32 s9, s9, s36\n"
"v_cvt_pk_bf16_f32 v235, v238, v239\n"
"v_max3_u32 v200, v52, v53, v54\n"
"v_mfma_f32_16x16x32_bf16 v[240:243], v[28:31], a[12:15], v[240:243]\n"
"s_add_u32 s10, s10, s36\n"
"ds_write_b128 v196, v[232:235] offset:43008\n"
"v_max3_u32 v200, v200, v55, v152\n"
"v_max_u32 v200, v200, v153\n"
"v_cmp_eq_u32 vcc, -1, v200\n"
"s_waitcnt lgkmcnt(6)\n"
"v_mfma_f32_16x16x32_bf16 v[32:35], a[216:219], v[64:67], 0\n"
"s_add_u32 s11, s11, s36\n"
"v_cvt_pk_bf16_f32 v240, v240, v241\n"
"v_cvt_pk_bf16_f32 v241, v242, v243\n"
"v_mfma_f32_16x16x32_bf16 v[36:39], a[220:223], v[64:67], 0\n"
"s_waitcnt lgkmcnt(3)\n"
"ds_write_b64 v197, v[240:241] offset:43008\n"
"v_mfma_f32_16x16x32_bf16 v[36:39], a[232:235], v[68:71], v[36:39]\n"
"s_cbranch_vccnz .Lp11%=\n"
".Lr11%=:\n"
"v_mfma_f32_16x16x32_bf16 v[36:39], a[244:247], v[44:47], v[36:39]\n"
"s_add_u32 s20, s20, s36\n"
"ds_write_b128 v202, v[52:55] offset:18432\n"
"v_mfma_f32_16x16x32_bf16 v[36:39], v[0:3], v[48:51], v[36:39]\n"
"s_waitcnt lgkmcnt(4)\n"
"s_add_u32 s21, s21, s36\n"
"s_add_u32 s22, s22, s36\n"
"v_mfma_f32_16x16x32_bf16 v[36:39], v[12:15], a[0:3], v[36:39]\n"
"s_waitcnt lgkmcnt(3)\n"
"ds_write_b64 v203, v[152:153] offset:18432\n"
"v_mfma_f32_16x16x32_bf16 v[36:39], v[24:27], a[4:7], v[36:39]\n"
"v_mfma_f32_16x16x32_bf16 v[40:43], a[224:227], v[64:67], 0\n"
"v_mfma_f32_16x16x32_bf16 v[40:43], a[236:239], v[68:71], v[40:43]\n"
"v_mfma_f32_16x16x32_bf16 v[40:43], a[248:251], v[44:47], v[40:43]\n"
"v_mfma_f32_16x16x32_bf16 v[40:43], v[4:7], v[48:51], v[40:43]\n"
"v_mfma_f32_16x16x32_bf16 v[40:43], v[16:19], a[0:3], v[40:43]\n"
"v_mfma_f32_16x16x32_bf16 v[40:43], v[28:31], a[4:7], v[40:43]\n"
"s_waitcnt lgkmcnt(0)\n"
"s_barrier\n"
"v_mfma_f32_16x16x32_bf16 v[32:35], a[228:231], v[68:71], v[32:35]\n"
"s_waitcnt vmcnt(7)\n"
"ds_read_b128 v[64:67], v192 offset:18432\n"
"ds_write_b128 v202, a[162:165] offset:49152\n"
"v_mfma_f32_16x16x32_bf16 v[32:35], a[240:243], v[44:47], v[32:35]\n"
"s_nop 0\n"
"v_cvt_pk_bf16_f32 v40, v40, v41\n"
"v_mfma_f32_16x16x32_bf16 v[32:35], a[252:255], v[48:51], v[32:35]\n"
"s_waitcnt vmcnt(6)\n"
"v_cvt_pk_bf16_f32 v41, v42, v43\n"
"v_mfma_f32_16x16x32_bf16 v[32:35], v[8:11], a[0:3], v[32:35]\n"
"ds_write_b64 v203, a[166:167] offset:49152\n"
"v_mfma_f32_16x16x32_bf16 v[32:35], v[20:23], a[4:7], v[32:35]\n"
"ds_read_b128 v[68:71], v192 offset:19456\n"
"ds_read_b128 v[44:47], v192 offset:20480\n"
"ds_read_b128 v[48:51], v192 offset:21504\n"
"ds_read_b128 a[0:3], v192 offset:22528\n"
"s_waitcnt lgkmcnt(6)\n"
"v_mfma_f32_16x16x32_bf16 v[204:207], a[216:219], v[64:67], 0\n"
"ds_read_b128 a[4:7], v192 offset:23552\n"
"v_mfma_f32_16x16x32_bf16 v[208:211], a[220:223], v[64:67], 0\n"
"ds_read_b128 a[8:11], v202 offset:12288\n"
"v_cvt_pk_bf16_f32 v32, v32, v33\n"
"v_mfma_f32_16x16x32_bf16 v[212:215], a[224:227], v[64:67], 0\n"
"v_cvt_pk_bf16_f32 v33, v34, v35\n"
"v_cvt_pk_bf16_f32 v34, v36, v37\n"
"v_cvt_pk_bf16_f32 v35, v38, v39\n"
"ds_write_b128 v196, v[32:35] offset:6144\n"
"s_waitcnt lgkmcnt(6)\n"
"v_mfma_f32_16x16x32_bf16 v[212:215], a[236:239], v[68:71], v[212:215]\n"
"s_waitcnt lgkmcnt(5)\n"
"v_mfma_f32_16x16x32_bf16 v[212:215], a[248:251], v[44:47], v[212:215]\n"
"s_waitcnt lgkmcnt(4)\n"
"ds_write_b64 v197, v[40:41] offset:6144\n"
"v_mfma_f32_16x16x32_bf16 v[212:215], v[4:7], v[48:51], v[212:215]\n"
"s_waitcnt lgkmcnt(4)\n"
"v_mfma_f32_16x16x32_bf16 v[212:215], v[16:19], a[0:3], v[212:215]\n"
"s_waitcnt lgkmcnt(3)\n"
"ds_read_b64 a[12:13], v62 offset:12288\n"
"v_mfma_f32_16x16x32_bf16 v[212:215], v[28:31], a[4:7], v[212:215]\n"
"s_waitcnt lgkmcnt(3)\n"
"buffer_store_dwordx4 a[8:11], v194, s[24:27], s8 offen sc1\n"
"v_mfma_f32_16x16x32_bf16 v[204:207], a[228:231], v[68:71], v[204:207]\n"
"v_mfma_f32_16x16x32_bf16 v[204:207], a[240:243], v[44:47], v[204:207]\n"
"v_mfma_f32_16x16x32_bf16 v[204:207], a[252:255], v[48:51], v[204:207]\n"
"v_mfma_f32_16x16x32_bf16 v[204:207], v[8:11], a[0:3], v[204:207]\n"
"v_mfma_f32_16x16x32_bf16 v[204:207], v[20:23], a[4:7], v[204:207]\n"
"s_waitcnt lgkmcnt(0)\n"
"buffer_store_dwordx2 a[12:13], v195, s[24:27], s8 offen sc1\n"
"v_mfma_f32_16x16x32_bf16 v[208:211], a[232:235], v[68:71], v[208:211]\n"
"s_barrier\n"
"v_mfma_f32_16x16x32_bf16 v[208:211], a[244:247], v[44:47], v[208:211]\n"
"ds_read_b128 v[44:47], v192 offset:49152\n"
"ds_read_b128 a[8:11], v202 offset:43008\n"
"v_mfma_f32_16x16x32_bf16 v[208:211], v[0:3], v[48:51], v[208:211]\n"
"ds_read_b128 v[48:51], v192 offset:50176\n"
"v_cvt_pk_bf16_f32 v204, v204, v205\n"
"v_mfma_f32_16x16x32_bf16 v[208:211], v[12:15], a[0:3], v[208:211]\n"
"ds_read_b128 a[0:3], v192 offset:51200\n"
"v_cvt_pk_bf16_f32 v205, v206, v207\n"
"v_mfma_f32_16x16x32_bf16 v[208:211], v[24:27], a[4:7], v[208:211]\n"
"ds_read_b128 a[4:7], v192 offset:52224\n"
"v_cvt_pk_bf16_f32 v212, v212, v213\n"
"v_cvt_pk_bf16_f32 v213, v214, v215\n"
"ds_read_b64 a[12:13], v62 offset:43008\n"
"ds_write_b64 v197, v[212:213] offset:55296\n"
"s_nop 2\n"
"v_cvt_pk_bf16_f32 v206, v208, v209\n"
"v_cvt_pk_bf16_f32 v207, v210, v211\n"
"s_waitcnt vmcnt(5)\n"
"v_max3_u32 v200, v144, v145, v146\n"
"s_waitcnt vmcnt(4)\n"
"v_max3_u32 v200, v200, v147, v154\n"
"v_max_u32 v200, v200, v155\n"
"v_cmp_eq_u32 vcc, -1, v200\n"
"s_waitcnt lgkmcnt(6)\n"
"v_mfma_f32_16x16x32_bf16 v[232:235], a[216:219], v[44:47], 0\n"
"s_waitcnt lgkmcnt(4)\n"
"ds_write_b128 v196, v[204:207] offset:55296\n"
"v_mfma_f32_16x16x32_bf16 v[232:235], a[228:231], v[48:51], v[232:235]\n"
"s_waitcnt lgkmcnt(4)\n"
"buffer_store_dwordx4 a[8:11], v194, s[24:27], s9 offen sc1\n"
"v_mfma_f32_16x16x32_bf16 v[232:235], a[240:243], a[0:3], v[232:235]\n"
"s_waitcnt lgkmcnt(2)\n"
"ds_read_b128 a[8:11], v192 offset:53248\n"
"ds_read_b128 v[64:67], v202 offset:6144\n"
"v_mfma_f32_16x16x32_bf16 v[232:235], a[252:255], a[4:7], v[232:235]\n"
"s_cbranch_vccnz .Lp12%=\n"
".Lr12%=:\n"
"v_mfma_f32_16x16x32_bf16 v[236:239], a[220:223], v[44:47], 0\n"
"ds_write_b128 v202, v[144:147] offset:30720\n"
"v_mfma_f32_16x16x32_bf16 v[236:239], a[232:235], v[48:51], v[236:239]\n"
"buffer_store_dwordx2 a[12:13], v195, s[24:27], s9 offen sc1\n"
"v_mfma_f32_16x16x32_bf16 v[236:239], a[244:247], a[0:3], v[236:239]\n"
"ds_write_b64 v203, v[154:155] offset:30720\n"
"v_mfma_f32_16x16x32_bf16 v[236:239], v[0:3], a[4:7], v[236:239]\n"
"v_mfma_f32_16x16x32_bf16 v[240:243], a[224:227], v[44:47], 0\n"
"s_waitcnt lgkmcnt(3)\n"
"buffer_load_dwordx4 v[56:59], v192, s[28:31], s18 offen sc1\n"
"ds_read_b128 a[12:15], v192 offset:54272\n"
"v_mfma_f32_16x16x32_bf16 v[240:243], a[236:239], v[48:51], v[240:243]\n"
"s_waitcnt lgkmcnt(3)\n"
"ds_read_b64 v[68:69], v62 offset:6144\n"
"v_mfma_f32_16x16x32_bf16 v[240:243], a[248:251], a[0:3], v[240:243]\n"
"v_mfma_f32_16x16x32_bf16 v[240:243], v[4:7], a[4:7], v[240:243]\n"
"v_mfma_f32_16x16x32_bf16 v[240:243], v[16:19], a[8:11], v[240:243]\n"
"buffer_load_dwordx2 v[60:61], v193, s[28:31], s18 offen offset:1024 sc1\n"
"v_mfma_f32_16x16x32_bf16 v[232:235], v[8:11], a[8:11], v[232:235]\n"
"s_waitcnt lgkmcnt(2)\n"
"s_barrier\n"
"v_mfma_f32_16x16x32_bf16 v[236:239], v[12:15], a[8:11], v[236:239]\n"
"ds_read_b128 v[44:47], v192 offset:30720\n"
"ds_read_b128 v[48:51], v192 offset:31744\n"
"ds_read_b128 a[0:3], v192 offset:32768\n"
"s_waitcnt lgkmcnt(4)\n"
"v_mfma_f32_16x16x32_bf16 v[236:239], v[24:27], a[12:15], v[236:239]\n"
"s_waitcnt lgkmcnt(3)\n"
"ds_read_b128 a[4:7], v192 offset:33792\n"
"buffer_store_dwordx4 v[64:67], v194, s[24:27], s10 offen sc1\n"
"v_mfma_f32_16x16x32_bf16 v[232:235], v[20:23], a[12:15], v[232:235]\n"
"ds_read_b128 a[8:11], v192 offset:34816\n"
"ds_read_b128 v[64:67], v202 offset:55296\n"
"v_mfma_f32_16x16x32_bf16 v[240:243], v[28:31], a[12:15], v[240:243]\n"
"ds_read_b128 a[12:15], v192 offset:35840\n"
"buffer_store_dwordx2 v[68:69], v195, s[24:27], s10 offen sc1\n"
"ds_read_b64 v[68:69], v62 offset:55296\n"
"s_waitcnt lgkmcnt(7)\n"
"v_mfma_f32_16x16x32_bf16 v[32:35], a[216:219], v[44:47], 0\n"
"s_waitcnt lgkmcnt(6)\n"
"v_cvt_pk_bf16_f32 v232, v232, v233\n"
"v_cvt_pk_bf16_f32 v233, v234, v235\n"
"v_mfma_f32_16x16x32_bf16 v[32:35], a[228:231], v[48:51], v[32:35]\n"
"s_waitcnt lgkmcnt(5)\n"
"v_cvt_pk_bf16_f32 v234, v236, v237\n"
"v_cvt_pk_bf16_f32 v235, v238, v239\n"
"v_mfma_f32_16x16x32_bf16 v[32:35], a[240:243], a[0:3], v[32:35]\n"
"s_waitcnt lgkmcnt(4)\n"
"ds_write_b128 v196, v[232:235]\n"
"v_mfma_f32_16x16x32_bf16 v[32:35], a[252:255], a[4:7], v[32:35]\n"
"s_waitcnt lgkmcnt(3)\n"
"v_cvt_pk_bf16_f32 v240, v240, v241\n"
"v_cvt_pk_bf16_f32 v241, v242, v243\n"
"v_mfma_f32_16x16x32_bf16 v[32:35], v[8:11], a[8:11], v[32:35]\n"
"s_waitcnt lgkmcnt(2)\n"
"buffer_store_dwordx4 v[64:67], v194, s[24:27], s11 offen sc1\n"
"v_mfma_f32_16x16x32_bf16 v[32:35], v[20:23], a[12:15], v[32:35]\n"
"s_waitcnt lgkmcnt(1)\n"
"ds_write_b64 v197, v[240:241]\n"
"v_mfma_f32_16x16x32_bf16 v[40:43], a[224:227], v[44:47], 0\n"
"v_mfma_f32_16x16x32_bf16 v[40:43], a[236:239], v[48:51], v[40:43]\n"
"buffer_store_dwordx2 v[68:69], v195, s[24:27], s11 offen sc1\n"
"v_mfma_f32_16x16x32_bf16 v[40:43], a[248:251], a[0:3], v[40:43]\n"
"v_mfma_f32_16x16x32_bf16 v[40:43], v[4:7], a[4:7], v[40:43]\n"
"s_nop 0\n"
"v_cvt_pk_bf16_f32 v32, v32, v33\n"
"v_mfma_f32_16x16x32_bf16 v[40:43], v[16:19], a[8:11], v[40:43]\n"
"v_cvt_pk_bf16_f32 v33, v34, v35\n"
"v_mfma_f32_16x16x32_bf16 v[40:43], v[28:31], a[12:15], v[40:43]\n"
"v_mfma_f32_16x16x32_bf16 v[36:39], a[220:223], v[44:47], 0\n"
"v_mfma_f32_16x16x32_bf16 v[36:39], a[232:235], v[48:51], v[36:39]\n"
"v_mfma_f32_16x16x32_bf16 v[36:39], a[244:247], a[0:3], v[36:39]\n"
"v_mfma_f32_16x16x32_bf16 v[36:39], v[0:3], a[4:7], v[36:39]\n"
"v_mfma_f32_16x16x32_bf16 v[36:39], v[12:15], a[8:11], v[36:39]\n"
"v_mfma_f32_16x16x32_bf16 v[36:39], v[24:27], a[12:15], v[36:39]\n"
"s_nop 1\n"
"v_cvt_pk_bf16_f32 v40, v40, v41\n"
"v_cvt_pk_bf16_f32 v41, v42, v43\n"
"ds_write_b64 v197, v[40:41] offset:36864\n"
"s_nop 2\n"
"v_cvt_pk_bf16_f32 v34, v36, v37\n"
"v_cvt_pk_bf16_f32 v35, v38, v39\n"
"ds_write_b128 v196, v[32:35] offset:36864\n"
"s_waitcnt vmcnt(5)\n"
"v_max3_u32 v200, v56, v57, v58\n"
"s_waitcnt vmcnt(4)\n"
"v_max3_u32 v200, v200, v59, v60\n"
"v_max_u32 v200, v200, v61\n"
"v_cmp_eq_u32 vcc, -1, v200\n"
"s_nop 4\n"
"s_cbranch_vccnz .Lp13%=\n"
".Lr13%=:\n"
"ds_write_b128 v202, v[56:59] offset:24576\n"
"ds_write_b64 v203, v[60:61] offset:24576\n"
"s_waitcnt lgkmcnt(0)\n"
"s_barrier\n"
"ds_read_b128 a[0:3], v192 offset:24576\n"
"ds_read_b128 a[4:7], v192 offset:25600\n"
"ds_read_b128 a[8:11], v192 offset:26624\n"
"ds_read_b128 a[12:15], v192 offset:27648\n"
"ds_read_b128 v[64:67], v202\n"
"ds_read_b64 v[68:69], v62\n"
"ds_read_b128 v[44:47], v202 offset:36864\n"
"ds_read_b64 v[48:49], v62 offset:36864\n"
"s_waitcnt lgkmcnt(7)\n"
"v_mfma_f32_16x16x32_bf16 v[204:207], a[216:219], a[0:3], 0\n"
"s_waitcnt lgkmcnt(6)\n"
"v_mfma_f32_16x16x32_bf16 v[204:207], a[228:231], a[4:7], v[204:207]\n"
"s_waitcnt lgkmcnt(5)\n"
"v_mfma_f32_16x16x32_bf16 v[204:207], a[240:243], a[8:11], v[204:207]\n"
"s_waitcnt lgkmcnt(4)\n"
"v_mfma_f32_16x16x32_bf16 v[204:207], a[252:255], a[12:15], v[204:207]\n"
"s_waitcnt lgkmcnt(3)\n"
"buffer_store_dwordx4 v[64:67], v194, s[24:27], s20 offen sc1\n"
"v_mfma_f32_16x16x32_bf16 v[212:215], a[224:227], a[0:3], 0\n"
"s_waitcnt lgkmcnt(0)\n"
"ds_read_b128 v[64:67], v192 offset:28672\n"
"v_mfma_f32_16x16x32_bf16 v[212:215], a[236:239], a[4:7], v[212:215]\n"
"v_mfma_f32_16x16x32_bf16 v[212:215], a[248:251], a[8:11], v[212:215]\n"
"buffer_store_dwordx2 v[68:69], v195, s[24:27], s20 offen sc1\n"
"v_mfma_f32_16x16x32_bf16 v[212:215], v[4:7], a[12:15], v[212:215]\n"
"ds_read_b128 v[68:71], v192 offset:29696\n"
"v_mfma_f32_16x16x32_bf16 v[208:211], a[220:223], a[0:3], 0\n"
"v_mfma_f32_16x16x32_bf16 v[208:211], a[232:235], a[4:7], v[208:211]\n"
"v_mfma_f32_16x16x32_bf16 v[208:211], a[244:247], a[8:11], v[208:211]\n"
"buffer_store_dwordx4 v[44:47], v194, s[24:27], s21 offen sc1\n"
"v_mfma_f32_16x16x32_bf16 v[208:211], v[0:3], a[12:15], v[208:211]\n"
"s_waitcnt lgkmcnt(1)\n"
"v_mfma_f32_16x16x32_bf16 v[208:211], v[12:15], v[64:67], v[208:211]\n"
"v_mfma_f32_16x16x32_bf16 v[204:207], v[8:11], v[64:67], v[204:207]\n"
"s_waitcnt lgkmcnt(0)\n"
"v_mfma_f32_16x16x32_bf16 v[204:207], v[20:23], v[68:71], v[204:207]\n"
"buffer_store_dwordx2 v[48:49], v195, s[24:27], s21 offen sc1\n"
"v_mfma_f32_16x16x32_bf16 v[212:215], v[16:19], v[64:67], v[212:215]\n"
"v_mfma_f32_16x16x32_bf16 v[212:215], v[28:31], v[68:71], v[212:215]\n"
"v_mfma_f32_16x16x32_bf16 v[208:211], v[24:27], v[68:71], v[208:211]\n"
"s_nop 3\n"
"v_cvt_pk_bf16_f32 v204, v204, v205\n"
"v_cvt_pk_bf16_f32 v205, v206, v207\n"
"s_nop 0\n"
"v_cvt_pk_bf16_f32 v212, v212, v213\n"
"v_cvt_pk_bf16_f32 v206, v208, v209\n"
"v_cvt_pk_bf16_f32 v207, v210, v211\n"
"v_cvt_pk_bf16_f32 v213, v214, v215\n"
"ds_write_b128 v196, v[204:207] offset:12288\n"
"ds_write_b64 v197, v[212:213] offset:12288\n"
"s_waitcnt lgkmcnt(0)\n"
"s_barrier\n"
"ds_read_b128 v[44:47], v202 offset:12288\n"
"ds_read_b64 v[48:49], v62 offset:12288\n"
"s_waitcnt lgkmcnt(1)\n"
"buffer_store_dwordx4 v[44:47], v194, s[24:27], s22 offen sc1\n"
"s_waitcnt lgkmcnt(0)\n"
"buffer_store_dwordx2 v[48:49], v195, s[24:27], s22 offen sc1\n"
"s_branch .Lend%=\n"
".Lp1%=:\n"
"s_sub_u32 s42, s42, 1\n"
"s_cbranch_scc1 .Lt1%=\n"
"s_sleep 1\n"
"buffer_load_dword v200, off, s[28:31], s39 sc1\n"
"s_waitcnt vmcnt(0)\n"
"v_add3_u32 v200, v200, s50, -2\n"
"v_cmp_lt_u32 vcc, 64, v200\n"
"s_nop 4\n"
"s_cbranch_vccnz .Lp1%=\n"
"s_branch .Lr1%=\n"
".Lt1%=:\n"
"s_mov_b32 s42, 0\n"
"s_branch .Lr1%=\n"
".Lp4%=:\n"
"s_sub_u32 s42, s42, 1\n"
"s_cbranch_scc1 .Lt4%=\n"
"s_sleep 1\n"
"buffer_load_dword v200, off, s[28:31], s39 sc1\n"
"s_waitcnt vmcnt(0)\n"
"v_add3_u32 v200, v200, s50, -5\n"
"v_cmp_lt_u32 vcc, 64, v200\n"
"s_nop 4\n"
"s_cbranch_vccnz .Lp4%=\n"
"s_branch .Lr4%=\n"
".Lt4%=:\n"
"s_mov_b32 s42, 0\n"
"s_branch .Lr4%=\n"
".Lp7%=:\n"
"s_sub_u32 s42, s42, 1\n"
"s_cbranch_scc1 .Lt7%=\n"
"s_sleep 1\n"
"buffer_load_dword v200, off, s[28:31], s39 sc1\n"
"s_waitcnt vmcnt(0)\n"
"v_add3_u32 v200, v200, s50, -8\n"
"v_cmp_lt_u32 vcc, 64, v200\n"
"s_nop 4\n"
"s_cbranch_vccnz .Lp7%=\n"
"s_branch .Lr7%=\n"
".Lt7%=:\n"
"s_mov_b32 s42, 0\n"
"s_branch .Lr7%=\n"
".Lp9%=:\n"
"s_sub_u32 s42, s42, 1\n"
"s_cbranch_scc1 .Lt9%=\n"
"s_sleep 1\n"
"buffer_load_dword v200, off, s[28:31], s39 sc1\n"
"s_waitcnt vmcnt(0)\n"
"v_add3_u32 v200, v200, s50, -10\n"
"v_cmp_lt_u32 vcc, 64, v200\n"
"s_nop 4\n"
"s_cbranch_vccnz .Lp9%=\n"
"s_branch .Lr9%=\n"
".Lt9%=:\n"
"s_mov_b32 s42, 0\n"
"s_branch .Lr9%=\n"
".Lp10%=:\n"
"s_sub_u32 s42, s42, 1\n"
"s_cbranch_scc1 .Lt10%=\n"
"s_sleep 1\n"
"buffer_load_dword v201, off, s[28:31], s39 sc1\n"
"s_waitcnt vmcnt(0)\n"
"v_add3_u32 v201, v201, s50, -11\n"
"v_cmp_lt_u32 vcc, 64, v201\n"
"s_nop 4\n"
"s_cbranch_vccnz .Lp10%=\n"
"s_branch .Lr10%=\n"
".Lt10%=:\n"
"s_mov_b32 s42, 0\n"
"s_branch .Lr10%=\n"
".Lp11%=:\n"
"s_sub_u32 s42, s42, 1\n"
"s_cbranch_scc1 .Lt11%=\n"
"buffer_load_dwordx4 v[52:55], v192, s[28:31], s15 offen sc1\n"
"buffer_load_dwordx2 v[152:153], v193, s[28:31], s15 offen offset:1024 sc1\n"
"s_waitcnt vmcnt(0)\n"
"v_max3_u32 v200, v52, v53, v54\n"
"v_max3_u32 v200, v200, v55, v152\n"
"v_max_u32 v200, v200, v153\n"
"v_cmp_eq_u32 vcc, -1, v200\n"
"s_nop 4\n"
"s_cbranch_vccnz .Lp11%=\n"
"s_branch .Lr11%=\n"
".Lt11%=:\n"
"s_mov_b32 s42, 0\n"
"s_branch .Lr11%=\n"
".Lp12%=:\n"
"s_sub_u32 s42, s42, 1\n"
"s_cbranch_scc1 .Lt12%=\n"
"buffer_load_dwordx4 v[144:147], v192, s[28:31], s17 offen sc1\n"
"buffer_load_dwordx2 v[154:155], v193, s[28:31], s17 offen offset:1024 sc1\n"
"s_waitcnt vmcnt(0)\n"
"v_max3_u32 v200, v144, v145, v146\n"
"v_max3_u32 v200, v200, v147, v154\n"
"v_max_u32 v200, v200, v155\n"
"v_cmp_eq_u32 vcc, -1, v200\n"
"s_nop 4\n"
"s_cbranch_vccnz .Lp12%=\n"
"s_branch .Lr12%=\n"
".Lt12%=:\n"
"s_mov_b32 s42, 0\n"
"s_branch .Lr12%=\n"
".Lp13%=:\n"
"s_sub_u32 s42, s42, 1\n"
"s_cbranch_scc1 .Lt13%=\n"
"buffer_load_dwordx4 v[56:59], v192, s[28:31], s18 offen sc1\n"
"buffer_load_dwordx2 v[60:61], v193, s[28:31], s18 offen offset:1024 sc1\n"
"s_waitcnt vmcnt(0)\n"
"v_max3_u32 v200, v56, v57, v58\n"
"v_max3_u32 v200, v200, v59, v60\n"
"v_max_u32 v200, v200, v61\n"
"v_cmp_eq_u32 vcc, -1, v200\n"
"s_nop 4\n"
"s_cbranch_vccnz .Lp13%=\n"
"s_branch .Lr13%=\n"
".Lt13%=:\n"
"s_mov_b32 s42, 0\n"
"s_branch .Lr13%=\n"
".Lend%=:\n"
      : "+{v31}"(tid), "+{s2}"(bid), "+{s[0:1]}"(ka)
      : "i"((__attribute__((address_space(3))) char*)smem)
      : "memory", "scc", "vcc", VCLOB, ACLOB, SCLOB);
}

extern "C" void kernel_launch(void* const* d_in, const int* in_sizes, int n_in,
                              void* d_out, int out_size, void* d_ws, size_t ws_size,
                              hipStream_t stream) {
  const uint16_t* x = (const uint16_t*)d_in[0];
  const uint16_t* wqkv = (const uint16_t*)d_in[1];
  const uint16_t* wo = (const uint16_t*)d_in[2];
  const uint16_t* rpb = (const uint16_t*)d_in[3];
  dinat_hs<<<256, 256, 0, stream>>>(x, wqkv, wo, rpb, (uint16_t*)d_out, (char*)d_ws + (2 << 20)
  );
}
```

```cpp
#include <hip/hip_runtime.h>
#include <hip/hip_bf16.h>
#include <stdint.h>

#define VCLOB "v0", "v1", "v2", "v3", "v4", "v5", "v6", "v7", "v8", "v9", "v10", "v11", "v12", "v13", "v14", "v15", "v16", "v17", "v18", "v19", "v20", "v21", "v22", "v23", "v24", "v25", "v26", "v27", "v28", "v29", "v30", "v32", "v33", "v34", "v35", "v36", "v37", "v38", "v39", "v40", "v41", "v42", "v43", "v44", "v45", "v46", "v47", "v48", "v49", "v50", "v51", "v52", "v53", "v54", "v55", "v56", "v57", "v58", "v59", "v60", "v61", "v62", "v63", "v64", "v65", "v66", "v67", "v68", "v69", "v70", "v71", "v72", "v73", "v74", "v75", "v76", "v77", "v78", "v79", "v80", "v81", "v82", "v83", "v84", "v85", "v86", "v87", "v88", "v89", "v90", "v91", "v92", "v93", "v94", "v95", "v96", "v97", "v98", "v99", "v100", "v101", "v102", "v103", "v104", "v105", "v106", "v107", "v108", "v109", "v110", "v111", "v112", "v113", "v114", "v115", "v116", "v117", "v118", "v119", "v120", "v121", "v122", "v123", "v124", "v125", "v126", "v127", "v128", "v129", "v130", "v131", "v132", "v133", "v134", "v135", "v136", "v137", "v138", "v139", "v140", "v141", "v142", "v143", "v144", "v145", "v146", "v147", "v148", "v149", "v150", "v151", "v152", "v153", "v154", "v155", "v156", "v157", "v158", "v159", "v160", "v161", "v162", "v163", "v164", "v165", "v166", "v167", "v168", "v169", "v170", "v171", "v172", "v173", "v174", "v175", "v176", "v177", "v178", "v179", "v180", "v181", "v182", "v183", "v184", "v185", "v186", "v187", "v188", "v189", "v190", "v191", "v192", "v193", "v194", "v195", "v196", "v197", "v198", "v199", "v200", "v201", "v202", "v203", "v204", "v205", "v206", "v207", "v208", "v209", "v210", "v211", "v212", "v213", "v214", "v215", "v216", "v217", "v218", "v219", "v220", "v221", "v222", "v223", "v224", "v225", "v226", "v227", "v228", "v229", "v230", "v231", "v232", "v233", "v234", "v235", "v236", "v237", "v238", "v239", "v240", "v241", "v242", "v243", "v244", "v245", "v246", "v247", "v248", "v249", "v250", "v251", "v252", "v253", "v254", "v255"
#define ACLOB "a0", "a1", "a2", "a3", "a4", "a5", "a6", "a7", "a8", "a9", "a10", "a11", "a12", "a13", "a14", "a15", "a16", "a17", "a18", "a19", "a20", "a21", "a22", "a23", "a24", "a25", "a26", "a27", "a28", "a29", "a30", "a31", "a32", "a33", "a34", "a35", "a36", "a37", "a38", "a39", "a40", "a41", "a42", "a43", "a44", "a45", "a46", "a47", "a48", "a49", "a50", "a51", "a52", "a53", "a54", "a55", "a56", "a57", "a58", "a59", "a60", "a61", "a62", "a63", "a64", "a65", "a66", "a67", "a68", "a69", "a70", "a71", "a72", "a73", "a74", "a75", "a76", "a77", "a78", "a79", "a80", "a81", "a82", "a83", "a84", "a85", "a86", "a87", "a88", "a89", "a90", "a91", "a92", "a93", "a94", "a95", "a96", "a97", "a98", "a99", "a100", "a101", "a102", "a103", "a104", "a105", "a106", "a107", "a108", "a109", "a110", "a111", "a112", "a113", "a114", "a115", "a116", "a117", "a118", "a119", "a120", "a121", "a122", "a123", "a124", "a125", "a126", "a127", "a128", "a129", "a130", "a131", "a132", "a133", "a134", "a135", "a136", "a137", "a138", "a139", "a140", "a141", "a142", "a143", "a144", "a145", "a146", "a147", "a148", "a149", "a150", "a151", "a152", "a153", "a154", "a155", "a156", "a157", "a158", "a159", "a160", "a161", "a162", "a163", "a164", "a165", "a166", "a167", "a168", "a169", "a170", "a171", "a172", "a173", "a174", "a175", "a176", "a177", "a178", "a179", "a180", "a181", "a182", "a183", "a184", "a185", "a186", "a187", "a188", "a189", "a190", "a191", "a192", "a193", "a194", "a195", "a196", "a197", "a198", "a199", "a200", "a201", "a202", "a203", "a204", "a205", "a206", "a207", "a208", "a209", "a210", "a211", "a212", "a213", "a214", "a215", "a216", "a217", "a218", "a219", "a220", "a221", "a222", "a223", "a224", "a225", "a226", "a227", "a228", "a229", "a230", "a231", "a232", "a233", "a234", "a235", "a236", "a237", "a238", "a239", "a240", "a241", "a242", "a243", "a244", "a245", "a246", "a247", "a248", "a249", "a250", "a251", "a252", "a253", "a254", "a255"
#define SCLOB "s3", "s4", "s5", "s6", "s7", "s8", "s9", "s10", "s11", "s12", "s13", "s14", "s15", "s16", "s17", "s18", "s19", "s20", "s21", "s22", "s23", "s24", "s25", "s26", "s27", "s28", "s29", "s30", "s31", "s32", "s33", "s34", "s35", "s36", "s37", "s38", "s39", "s40", "s41", "s42", "s43", "s44", "s45", "s46", "s47", "s48", "s49", "s50", "s51", "s52", "s53", "s54", "s55", "s56", "s57", "s58", "s59", "s60", "s61", "s62", "s63", "s64", "s65", "s66", "s67", "s68", "s69", "s70", "s71", "s72", "s73", "s74", "s75", "s76", "s77", "s78", "s79", "s80", "s81", "s82", "s83", "s84", "s85", "s86", "s87", "s88", "s89", "s90", "s91", "s92", "s93", "s94", "s95", "s96", "s97", "s98", "s99", "s100", "s101"
#define LDS_BYTES 163392

__global__ __launch_bounds__(256, 1) __attribute__((aligned(4096))) void dinat_hs(const uint16_t* __restrict__ x, const uint16_t* __restrict__ wqkv,
                                                   const uint16_t* __restrict__ wo, const uint16_t* __restrict__ rpb,
                                                   uint16_t* __restrict__ out, char* __restrict__ ws
                                                   ) {
  __shared__ __attribute__((aligned(16))) char smem[LDS_BYTES];
  unsigned tid = threadIdx.x, bid = blockIdx.x;
  auto ka = __builtin_amdgcn_kernarg_segment_ptr();
  asm volatile(
"s_load_dwordx2 s[8:9], s[0:1], 0\n"
"s_load_dwordx2 s[12:13], s[0:1], 8\n"
"s_load_dwordx2 s[16:17], s[0:1], 16\n"
"s_load_dwordx2 s[20:21], s[0:1], 24\n"
"s_load_dwordx2 s[24:25], s[0:1], 32\n"
"s_load_dwordx2 s[28:29], s[0:1], 40\n"
"s_getpc_b64 s[4:5]\n"
".Lpc%=:\n"
"s_and_b32 s56, s2, 1\n"
"s_mul_i32 s55, s56, 112\n"
"s_bfe_u32 s53, s2, 0x20005\n"
"s_lshr_b32 s54, s2, 7\n"
"s_sub_u32 s55, s55, 1\n"
"s_mul_i32 s55, s55, s54\n"
"s_mul_i32 s71, s56, -56\n"
"s_mul_i32 s59, s53, 27\n"
"v_lshrrev_b32 v113, 6, v31\n"
"s_add_u32 s55, s55, s71\n"
"s_max_u32 s59, s59, 1\n"
"s_nop 0\n"
"v_readfirstlane_b32 s51, v113\n"
"s_add_u32 s55, s55, 56\n"
"s_sub_u32 s59, s59, 1\n"
"v_and_b32 v112, 63, v31\n"
"s_mul_i32 s58, s55, 172032\n"
"s_mul_i32 s72, s59, 768\n"
"s_lshr_b32 s71, s2, 3\n"
"s_add_u32 s58, s58, s72\n"
"s_and_b32 s73, s2, 4\n"
"s_lshl2_add_u32 s71, s71, s51\n"
"v_min_u32 v121, 2, v112\n"
"s_mul_i32 s73, s73, 21504\n"
"s_and_b32 s74, s2, 2\n"
"s_mul_i32 s72, s71, 384\n"
"v_lshlrev_b32 v121, 7, v121\n"
"s_add_u32 s58, s58, s73\n"
"s_mul_i32 s74, s74, 192\n"
"v_add_u32 v121, s72, v121\n"
"s_bfe_u32 s52, s2, 0x20003\n"
"s_mul_i32 s57, s54, -344064\n"
"s_add_u32 s58, s58, s74\n"
"v_lshlrev_b32 v120, 4, v112\n"
"s_mul_i32 s85, s56, 3\n"
"v_min_u32 v121, .Lend%=-.Lpc%=-4, v121\n"
"s_add_u32 s57, s57, 172032\n"
"s_lshl_b32 s61, s51, 3\n"
"v_lshrrev_b32 v114, 3, v112\n"
"v_xor_b32 v115, v120, v112\n"
"s_sub_u32 s86, s85, 3\n"
"s_mov_b32 m0, 163136\n"
"s_mul_i32 s73, s52, 338\n"
"global_load_lds_dword v121, s[4:5]\n"
"s_and_b32 s74, s51, 1\n"
"s_lshr_b32 s84, s2, 5\n"
"v_and_b32 v115, 0x70, v115\n"
"v_add_u32 v114, s61, v114\n"
"s_mul_i32 s86, s86, s57\n"
"s_and_b32 s73, s73, -4\n"
"s_mul_i32 s74, s74, 88\n"
"v_mul_u32_u24 v123, 0x2aab, v112\n"
"v_add_u32 v124, 64, v112\n"
"s_mov_b32 s22, 1352\n"
"s_mov_b32 s23, 0x20000\n"
"s_add_u32 s84, s84, 0\n"
"v_xor_b32 v116, 64, v115\n"
"v_mul_u32_u24 v117, 0x300, v114\n"
"s_add_u32 s86, s86, s58\n"
"v_max_u32 v118, s85, v114\n"
"s_add_u32 s73, s73, s74\n"
"v_lshlrev_b32 v122, 2, v112\n"
"v_lshrrev_b32 v123, 16, v123\n"
"v_mul_u32_u24 v124, 0x2aab, v124\n"
"s_and_b32 s65, s51, 1\n"
"s_mul_i32 s84, s84, 2\n"
"v_add3_u32 v119, v117, s86, v116\n"
"v_lshlrev_b32 v126, 1, v123\n"
"v_lshrrev_b32 v124, 16, v124\n"
"s_lshr_b32 s66, s51, 1\n"
"s_mul_i32 s70, s52, 96\n"
"s_mul_i32 s85, s65, 384\n"
"s_lshr_b32 s75, 0x9249200, s84\n"
"v_and_b32 v126, 24, v126\n"
"v_lshlrev_b32 v127, 1, v124\n"
"s_add_u32 s85, s85, s70\n"
"s_mul_i32 s86, s66, 36864\n"
"s_movk_i32 s69, 1152\n"
"s_and_b32 s75, s75, 0x180\n"
"v_and_or_b32 v126, v123, 3, v126\n"
"v_and_b32 v127, 24, v127\n"
"v_add_u32 v125, 128, v112\n"
"s_add_u32 s85, s85, s86\n"
"v_mad_u32_u24 v126, v126, s69, v120\n"
"v_mul_u32_u24 v27, 96, v123\n"
"v_and_or_b32 v127, v124, 3, v127\n"
"v_lshrrev_b32 v128, 2, v124\n"
"v_mul_u32_u24 v125, 0x2aab, v125\n"
"s_add_u32 s85, s85, 384\n"
"s_mul_i32 s65, s65, 3072\n"
"s_mul_i32 s71, s66, 6752\n"
"s_mov_b32 s14, 221184\n"
"s_mov_b32 s15, 0x20000\n"
"v_sub_u32 v27, v126, v27\n"
"v_and_or_b32 v127, v128, 4, v127\n"
"v_lshrrev_b32 v125, 16, v125\n"
"s_add_u32 s71, s71, s65\n"
"v_mad_u32_u24 v127, v127, s69, v120\n"
"v_mul_u32_u24 v28, 96, v124\n"
"v_lshlrev_b32 v129, 1, v125\n"
"v_sub_u32 v28, v127, v28\n"
"v_and_b32 v129, 24, v129\n"
"v_add_u32 v28, 1024, v28\n"
"v_and_or_b32 v129, v125, 3, v129\n"
"v_mad_u32_u24 v129, v129, s69, v120\n"
"v_mul_u32_u24 v29, 96, v125\n"
"s_sub_u32 s64, 111, s59\n"
"v_sub_u32 v29, v129, v29\n"
"s_min_u32 s64, s64, 32\n"
"v_add_u32 v29, 6656, v29\n"
"s_mul_i32 s64, s64, 768\n"
"v_add_u32 v118, -3, v118\n"
"s_add_u32 s64, s64, s58\n"
"s_mul_i32 s50, s51, 6752\n"
"v_mul_i32_i24 v118, s57, v118\n"
"s_mov_b32 s10, 19267584\n"
"s_mov_b32 s11, 0x20000\n"
"s_add_u32 s50, s50, 27008\n"
"v_add3_u32 v118, v118, s64, v115\n"
"s_lshr_b32 s76, 0x12492480, s84\n"
"s_and_b32 s76, s76, 0x180\n"
"s_lshr_b32 s77, 0x4924900, s84\n"
"s_and_b32 s77, s77, 0x180\n"
"s_lshl_b32 s72, s56, 3\n"
"s_sub_u32 s72, s72, 2\n"
"s_waitcnt lgkmcnt(0)\n"
"s_add_u32 m0, s74, 162768\n"
"s_mul_i32 s72, s72, s57\n"
"buffer_load_dword v122, s[20:23], s73 offen lds\n"
"s_mul_i32 s73, s75, 576\n"
"s_add_u32 s73, s73, s85\n"
"s_add_u32 m0, s71, 0\n"
"s_mul_i32 s34, s51, 12288\n"
"buffer_load_dwordx4 v27, s[12:15], s73 offen lds\n"
"s_add_u32 s72, s72, s58\n"
"s_add_u32 s34, s34, 81024\n"
"v_add3_u32 v130, v117, s72, v115\n"
"s_add_u32 s74, s72, s57\n"
"v_add3_u32 v131, v117, s74, v116\n"
"s_and_b32 s60, s2, 7\n"
"s_add_u32 m0, s71, 1024\n"
"s_lshr_b32 s84, s2, 5\n"
"s_lshl3_add_u32 s60, s60, s84\n"
"s_mul_i32 s84, s76, 576\n"
"s_add_u32 s84, s84, s85\n"
"s_mul_i32 s86, s77, 576\n"
"buffer_load_dwordx4 v28, s[12:15], s73 offen lds\n"
"s_add_u32 s86, s86, s85\n"
"s_mul_i32 s72, s54, 172032\n"
"s_sub_u32 s36, s58, s72\n"
"s_add_u32 s32, s36, s75\n"
"v_add3_u32 v190, v117, s72, v115\n"
"s_sub_u32 s74, 172032, s72\n"
"v_add3_u32 v191, v117, s74, v116\n"
"v_and_b32 v134, 3, v112\n"
"v_lshrrev_b32 v137, 1, v112\n"
"s_add_u32 m0, s71, 2048\n"
"v_and_or_b32 v137, v137, 4, v134\n"
"v_bfe_u32 v136, v112, 2, 1\n"
"v_lshrrev_b32 v140, 1, v137\n"
"v_min_u32 v96, 0xdc, v31\n"
"s_add_u32 s0, s36, s76\n"
"buffer_load_dwordx4 v29, s[12:15], s73 offen lds\n"
"v_lshrrev_b32 v132, 4, v112\n"
"v_lshl_add_u32 v85, v136, 2, v140\n"
"v_mul_u32_u24 v97, 0xf1, v96\n"
"s_mul_i32 s85, s54, -52\n"
"v_xor_b32 v85, v85, v132\n"
"v_lshrrev_b32 v97, 12, v97\n"
"s_sub_u32 s85, s85, 8\n"
"v_lshlrev_b32 v85, 4, v85\n"
"s_add_u32 m0, s50, 0\n"
"v_lshlrev_b32 v30, 3, v96\n"
"v_mul_i32_i24 v97, s85, v97\n"
"v_lshl_add_u32 v85, v137, 7, v85\n"
"v_mul_u32_u24 v25, 0xc00, v136\n"
"v_lshl_add_u32 v96, v96, 1, v97\n"
"buffer_load_dwordx4 v118, s[8:11], s75 offen lds\n"
"v_add_u32 v25, v25, v85\n"
"v_add_u32 v188, s34, v25\n"
"v_xad_u32 v189, v25, 64, s34\n"
"s_add_u32 s1, s36, s77\n"
"s_mul_i32 s35, s51, 6144\n"
"s_lshl_b32 s33, s57, 1\n"
"s_add_u32 s35, s35, 130176\n"
"s_add_u32 m0, s50, 3072\n"
"v_lshrrev_b32 v84, 2, v112\n"
"v_mul_u32_u24 v84, 96, v84\n"
"v_lshl_add_u32 v23, v134, 3, v84\n"
"v_xor_b32 v69, 64, v25\n"
"v_add_u32 v30, 160896, v30\n"
"buffer_load_dwordx4 v119, s[8:11], s75 offen lds\n"
"v_add_u32 v72, 54016, v23\n"
"v_add_u32 v68, s50, v25\n"
"v_add_u32 v69, s50, v69\n"
"s_mul_i32 s3, s65, 12\n"
"s_add_u32 s70, s70, s3\n"
"s_mul_i32 s81, s77, 576\n"
"s_add_u32 s81, s81, s70\n"
"s_add_u32 m0, s50, 1024\n"
"s_sub_u32 s3, s76, s75\n"
"s_mul_i32 s3, s3, s66\n"
"s_add_u32 s3, s3, s75\n"
"s_mul_i32 s41, s56, -60768\n"
"s_mul_i32 s43, s66, 6752\n"
"buffer_load_dwordx4 v118, s[8:11], s76 offen lds\n"
"s_mul_i32 s3, s3, 576\n"
"s_add_u32 s41, s41, 60768\n"
"s_add_u32 s43, s43, s65\n"
"s_add_u32 s3, s3, s70\n"
"s_add_u32 s43, s43, s41\n"
"s_mov_b32 s42, 0x80000\n"
"v_add_u32 v142, s61, v137\n"
"v_bfe_u32 v86, v142, 1, 1\n"
"v_mul_u32_u24 v87, 43, v142\n"
"s_add_u32 m0, s50, 4096\n"
"s_movk_i32 s68, 6752\n"
"v_and_b32 v133, 48, v112\n"
"v_xor_b32 v86, v86, v132\n"
"v_lshrrev_b32 v87, 9, v87\n"
"s_movk_i32 s67, 192\n"
"buffer_load_dwordx4 v119, s[8:11], s76 offen lds\n"
"v_lshlrev_b32 v86, 4, v86\n"
"v_mul_u32_u24 v87, 80832, v87\n"
"v_mad_u32_u24 v21, v142, s68, v133\n"
"v_mad_u32_u24 v141, v142, s67, v86\n"
"v_sub_u32 v21, v21, v87\n"
"v_and_b32 v88, 1, v112\n"
"s_mul_i32 s48, s56, 13504\n"
"v_add_u32 v21, 6144, v21\n"
"s_add_u32 m0, s50, 2048\n"
"v_add_u32 v187, 74272, v141\n"
"v_add_u32 v198, 60768, v141\n"
"v_mad_u32_u24 v88, v88, s68, v133\n"
"s_mov_b32 s6, 0xf0f0f0f0\n"
"s_mov_b32 s7, 0xf0f0f0f0\n"
"buffer_load_dwordx4 v118, s[8:11], s77 offen lds\n"
"s_mov_b64 exec, s[6:7]\n"
"v_add_u32 v21, s48, v141\n"
"v_mov_b32 v187, v141\n"
"v_add_u32 v198, 60544, v88\n"
"s_mov_b64 exec, -1\n"
"s_lshl_b32 s85, s66, 10\n"
"v_add_u32 v192, 99456, v120\n"
"v_bfe_u32 v138, v112, 4, 1\n"
"v_lshrrev_b32 v139, 3, v112\n"
"s_add_u32 m0, s50, 5120\n"
"s_add_u32 s62, s59, s61\n"
"v_and_b32 v139, 4, v139\n"
"v_sub_u32 v94, v136, v138\n"
"s_sub_u32 s63, 108, s62\n"
"s_mul_i32 s49, s56, 272\n"
"buffer_load_dwordx4 v119, s[8:11], s77 offen lds\n"
"v_mul_i32_i24 v94, 0x88, v94\n"
"v_sub_u32 v95, v137, v139\n"
"s_sub_u32 s62, 3, s62\n"
"v_add_u32 v150, 1, v139\n"
"v_add_u32 v151, 2, v139\n"
"v_add_u32 v152, 3, v139\n"
"v_lshl_add_u32 v94, v95, 3, v94\n"
"v_max_i32 v149, s62, v139\n"
"v_max_i32 v150, s62, v150\n"
"s_add_u32 m0, s34, 6144\n"
"v_max_i32 v151, s62, v151\n"
"v_max_i32 v152, s62, v152\n"
"v_mov_b32 v153, 0x7f\n"
"v_min_i32 v149, s63, v149\n"
"v_min_i32 v150, s63, v150\n"
"buffer_load_dwordx4 v130, s[8:11], s75 offen lds\n"
"v_min_i32 v151, s63, v151\n"
"v_min_i32 v152, s63, v152\n"
"v_lshlrev_b32 v149, v149, v153\n"
"v_lshlrev_b32 v150, v150, v153\n"
"v_lshlrev_b32 v151, v151, v153\n"
"v_lshlrev_b32 v152, v152, v153\n"
"s_sub_u32 s74, 3, s61\n"
"s_add_u32 m0, s34, 9216\n"
"v_lshrrev_b32 v149, v137, v149\n"
"v_lshrrev_b32 v150, v137, v150\n"
"v_lshrrev_b32 v151, v137, v151\n"
"v_lshrrev_b32 v152, v137, v152\n"
"s_max_i32 s74, s74, s62\n"
"buffer_load_dwordx4 v131, s[8:11], s75 offen lds\n"
"v_bfe_i32 v98, v149, 0, 1\n"
"v_bfe_i32 v99, v150, 0, 1\n"
"v_bfe_i32 v102, v149, 8, 1\n"
"v_bfe_i32 v103, v150, 8, 1\n"
"v_sub_u32 v61, v136, v138\n"
"s_mov_b32 s72, 0xffff\n"
"v_bfe_i32 v100, v151, 0, 1\n"
"v_bfe_i32 v101, v152, 0, 1\n"
"s_add_u32 m0, s34, 7168\n"
"v_bfe_i32 v56, v151, 8, 1\n"
"v_bfe_i32 v57, v152, 8, 1\n"
"v_max_i32 v58, s74, v137\n"
"v_ashrrev_i32 v149, 31, v61\n"
"v_add_u32 v150, -1, v61\n"
"buffer_load_dwordx4 v130, s[8:11], s76 offen lds\n"
"v_mov_b32 v160, 0x40de\n"
"v_mov_b32 v161, 0\n"
"v_lshlrev_b32 v245, 4, v134\n"
"v_mov_b64 v[156:157], 0\n"
"s_mov_b32 s6, 0x00f0000f\n"
"s_mov_b32 s7, 0xf0000f00\n"
"v_bfi_b32 v0, s72, v98, v99\n"
"v_bfi_b32 v1, s72, v100, v101\n"
"v_bfi_b32 v2, s72, v102, v103\n"
"v_bfi_b32 v3, s72, v56, v57\n"
"s_mul_i32 s74, s51, 1536\n"
"v_and_b32 v251, 15, v112\n"
"v_not_b32 v149, v149\n"
"v_ashrrev_i32 v150, 31, v150\n"
"v_or_b32 v244, s56, v138\n"
"s_mov_b64 exec, s[6:7]\n"
"v_lshlrev_b64 v[156:157], v245, v[160:161]\n"
"s_mov_b64 exec, -1\n"
"s_lshl2_add_u32 s72, s60, s51\n"
"v_mul_u32_u24 v251, 0x180, v251\n"
"v_and_b32 v252, 7, v112\n"
"v_and_b32 v12, v149, v0\n"
"v_and_b32 v16, v150, v0\n"
"v_and_b32 v13, v149, v1\n"
"v_and_b32 v17, v150, v1\n"
"v_and_b32 v14, v149, v2\n"
"v_and_b32 v18, v150, v2\n"
"v_and_b32 v15, v149, v3\n"
"v_and_b32 v19, v150, v3\n"
"v_sub_u32 v244, v136, v244\n"
"v_mov_b32 v160, 0xffff\n"
"v_mov_b64 v[176:177], 0\n"
"s_mul_i32 s37, s72, 6144\n"
"v_and_b32 v249, 1, v112\n"
"v_add_u32 v251, 99456, v251\n"
"v_lshrrev_b32 v254, 5, v112\n"
"v_ashrrev_i32 v149, 31, v244\n"
"v_add_u32 v150, -1, v244\n"
"s_mov_b64 exec, s[6:7]\n"
"v_lshlrev_b64 v[176:177], v245, v[160:161]\n"
"s_mov_b64 exec, -1\n"
"s_lshl_b32 s72, s51, 6\n"
"v_lshlrev_b32 v255, 3, v134\n"
"v_lshl_add_u32 v84, v23, 2, s72\n"
"v_sub_u32 v84, v84, v255\n"
"v_sub_u32 v84, v84, v255\n"
"v_add_u32 v236, 130176, v84\n"
"v_sub_u32 v85, v236, v255\n"
"s_lshl_b32 s73, s48, 2\n"
"v_mad_u32_u24 v186, v136, s68, v141\n"
"s_add_u32 s73, s73, 6752\n"
"v_add_u32 v22, s73, v186\n"
"v_add_u32 v70, s35, v25\n"
"v_xor_b32 v71, 64, v25\n"
"v_add_u32 v71, s35, v71\n"
"s_sub_u32 s73, 28, s61\n"
"s_min_i32 s73, s73, s63\n"
"v_min_i32 v58, s73, v58\n"
"v_lshlrev_b32 v58, v58, -1\n"
"v_lshrrev_b32 v58, v139, v58\n"
"v_not_b32 v58, v58\n"
"v_and_b32 v59, 5, v58\n"
"v_and_b32 v60, 10, v58\n"
"v_lshlrev_b32 v59, 1, v59\n"
"v_lshl_or_b32 v59, v60, 4, v59\n"
"v_or_b32 v59, 0x44, v59\n"
"s_mov_b64 exec, 0\n"
"s_cmp_eq_u32 s51, 0\n"
"s_cbranch_scc1 .Lic0%=\n"
"s_cmp_eq_u32 s51, 1\n"
"s_cbranch_scc1 .Lic1%=\n"
"s_cmp_eq_u32 s51, 2\n"
"s_cbranch_scc1 .Lic2%=\n"
"s_branch .Lic3%=\n"
".Lib0%=:\n"
"s_mov_b64 exec, -1\n"
"v_lshl_or_b32 v63, v59, 8, v59\n"
"s_mov_b32 s30, 23724032\n"
"s_mov_b32 s31, 0x20000\n"
"v_and_b32 v146, 8, v112\n"
"v_lshl_add_u32 v146, v136, 1, v146\n"
"v_add3_u32 v146, v146, s61, 1\n"
"v_lshrrev_b32 v145, 5, v112\n"
"s_add_u32 m0, s34, 10240\n"
"v_max_u32 v144, 3, v142\n"
"buffer_load_dwordx4 v131, s[8:11], s76 offen lds\n"
"v_mul_u32_u24 v145, s68, v145\n"
"v_bfe_u32 v92, v146, 1, 1\n"
"v_add_u32 v144, -3, v144\n"
"v_add_u32 v145, -768, v145\n"
"v_xor_b32 v92, v92, v134\n"
"v_mov_b32 v143, s48\n"
"v_bfe_u32 v90, v144, 1, 1\n"
"v_lshl_add_u32 v92, v92, 4, v145\n"
"v_mad_u32_u24 v143, v136, s68, v143\n"
"v_xor_b32 v90, v90, v132\n"
"v_cmp_eq_u32 s[46:47], 35, v146\n"
"s_add_u32 m0, s34, 8192\n"
"v_mad_u32_u24 v184, v146, s67, v92\n"
"buffer_load_dwordx4 v130, s[8:11], s77 offen lds\n"
"v_lshl_add_u32 v90, v90, 4, v143\n"
"v_add_u32 v146, 1, v146\n"
"v_mad_u32_u24 v178, v144, s67, v90\n"
"v_bfe_u32 v93, v146, 1, 1\n"
"v_add_u32 v144, 5, v142\n"
"v_xor_b32 v93, v93, v134\n"
"v_min_u32 v144, 32, v144\n"
"v_mul_u32_u24 v147, 13504, v138\n"
"v_lshl_add_u32 v93, v93, 4, v145\n"
"v_bfe_u32 v91, v144, 1, 1\n"
"v_add_u32 v147, s48, v147\n"
"s_add_u32 m0, s34, 11264\n"
"v_mul_i32_i24 v148, -67520, v138\n"
"buffer_load_dwordx4 v131, s[8:11], s77 offen lds\n"
"s_waitcnt vmcnt(12)\n"
"s_barrier\n"
"ds_read_b64_tr_b16 a[0:1], v23\n"
"ds_read_b64_tr_b16 a[2:3], v23 offset:1536\n"
"ds_read_b64_tr_b16 a[4:5], v23 offset:32\n"
"ds_read_b64_tr_b16 a[6:7], v23 offset:1568\n"
"ds_read_b64_tr_b16 a[8:9], v23 offset:64\n"
"ds_read_b64_tr_b16 a[10:11], v23 offset:1600\n"
"ds_read_b64_tr_b16 a[72:73], v23 offset:3072\n"
"s_add_u32 m0, s71, 13504\n"
"ds_read_b64_tr_b16 a[74:75], v23 offset:4608\n"
"buffer_load_dwordx4 v27, s[12:15], s84 offen lds\n"
"ds_read_b64_tr_b16 a[76:77], v23 offset:3104\n"
"v_mad_u32_u24 v185, v146, s67, v93\n"
"ds_read_b64_tr_b16 a[78:79], v23 offset:4640\n"
"ds_read_b64_tr_b16 a[80:81], v23 offset:3136\n"
"ds_read_b64_tr_b16 a[82:83], v23 offset:4672\n"
"v_bfe_u32 v135, v112, 2, 2\n"
"ds_read_b64_tr_b16 a[12:13], v23 offset:6752\n"
"ds_read_b64_tr_b16 a[14:15], v23 offset:8288\n"
"ds_read_b64_tr_b16 a[16:17], v23 offset:6784\n"
"s_add_u32 m0, s71, 14528\n"
"ds_read_b64_tr_b16 a[18:19], v23 offset:8320\n"
"buffer_load_dwordx4 v28, s[12:15], s84 offen lds\n"
"s_waitcnt vmcnt(12)\n"
"ds_read_b128 a[144:147], v68\n"
"v_xor_b32 v91, v91, v132\n"
"v_add_u32 v148, 67520, v148\n"
"ds_read_b128 a[148:151], v69\n"
"v_add_u32 v183, v185, v147\n"
"ds_read_b64_tr_b16 a[20:21], v23 offset:6816\n"
"ds_read_b64_tr_b16 a[22:23], v23 offset:8352\n"
"ds_read_b64_tr_b16 a[84:85], v23 offset:9824\n"
"s_add_u32 m0, s71, 15552\n"
"ds_read_b64_tr_b16 a[86:87], v23 offset:11360\n"
"buffer_load_dwordx4 v29, s[12:15], s84 offen lds\n"
"ds_read_b64_tr_b16 a[88:89], v23 offset:9856\n"
"s_add_u32 s84, s49, 161064\n"
"ds_read_b64_tr_b16 a[90:91], v23 offset:11392\n"
"v_add_u32 v20, s84, v94\n"
"ds_read_b64_tr_b16 a[92:93], v23 offset:9888\n"
"ds_read_b64_tr_b16 a[94:95], v23 offset:11424\n"
"s_lshl_b32 s84, s51, 10\n"
"v_add_u32 v202, s84, v192\n"
"v_add_u32 v185, v185, v148\n"
"s_mov_b64 exec, s[46:47]\n"
"v_mad_u32_u24 v185, v138, s67, v185\n"
"s_mov_b64 exec, -1\n"
"v_lshl_add_u32 v91, v91, 4, v143\n"
"v_mad_u32_u24 v179, v144, s67, v91\n"
"v_cmp_eq_u32 s[44:45], 32, v144\n"
"v_add_u32 v182, v184, v147\n"
"v_not_b32 v149, v149\n"
"v_add_u32 v184, v184, v148\n"
"v_and_b32 v4, v149, v0\n"
"v_and_b32 v5, v149, v1\n"
"v_and_b32 v6, v149, v2\n"
"v_and_b32 v7, v149, v3\n"
"v_ashrrev_i32 v150, 31, v150\n"
"v_and_b32 v8, v150, v0\n"
"v_and_b32 v9, v150, v1\n"
"v_and_b32 v10, v150, v2\n"
"v_and_b32 v11, v150, v3\n"
"v_lshrrev_b32 v87, 2, v112\n"
"v_sub_u32 v88, 11, v112\n"
"v_and_b32 v88, 0x80000000, v88\n"
"v_and_b32 v89, 1, v114\n"
"v_lshlrev_b32 v193, 3, v112\n"
"v_add_u32 v89, 29, v89\n"
"v_add_u32 v193, 99456, v193\n"
"v_mul_i32_i24 v89, s57, v89\n"
"v_add3_u32 v199, v89, s64, v116\n"
"v_mov_b32 v26, 0xf149f149\n"
"v_mov_b64 v[160:161], v[156:157]\n"
"v_add_u32 v180, 40512, v178\n"
"v_add_u32 v181, 40512, v179\n"
"v_mov_b32 v172, 0x3f803f80\n"
"v_mov_b32 v173, v172\n"
"v_mov_b64 v[164:165], v[156:157]\n"
"v_mov_b64 v[168:169], v[156:157]\n"
"v_mov_b64 v[174:175], v[172:173]\n"
"s_waitcnt vmcnt(11) lgkmcnt(9)\n"
"v_mfma_f32_16x16x32_bf16 v[204:207], a[0:3], a[144:147], 0\n"
"s_waitcnt lgkmcnt(8)\n"
"ds_read_b128 a[152:155], v68 offset:1024\n"
"ds_read_b128 a[156:159], v69 offset:1024\n"
"v_mfma_f32_16x16x32_bf16 v[204:207], a[12:15], a[148:151], v[204:207]\n"
"s_add_u32 m0, s71, 60768\n"
"s_mov_b32 s84, 0xc0000007\n"
"s_cmp_eq_u32 s53, 0\n"
"s_cselect_b32 s84, 0xe0000000, s84\n"
"s_cmp_eq_u32 s53, 3\n"
"s_cselect_b32 s84, 7, s84\n"
"v_mfma_f32_16x16x32_bf16 v[208:211], a[4:7], a[144:147], 0\n"
"s_lshl_b32 s5, s51, 9\n"
"buffer_load_dwordx4 v27, s[12:15], s86 offen lds\n"
"s_mov_b32 s70, 0x3e553b95\n"
"v_mfma_f32_16x16x32_bf16 v[208:211], a[16:19], a[148:151], v[208:211]\n"
"s_add_u32 s5, s5, 4096\n"
"v_add_u32 v62, s5, v193\n"
"s_mul_i32 s20, s33, 3\n"
"v_mfma_f32_16x16x32_bf16 v[212:215], a[8:11], a[144:147], 0\n"
"s_waitcnt vmcnt(10) lgkmcnt(8)\n"
"ds_read_b128 a[160:163], v68 offset:2048\n"
"ds_read_b128 a[164:167], v69 offset:2048\n"
"v_mfma_f32_16x16x32_bf16 v[212:215], a[20:23], a[148:151], v[212:215]\n"
"s_add_u32 m0, s71, 61792\n"
"v_add_u32 v68, s41, v23\n"
"v_add_u32 v69, 154752, v23\n"
"v_mfma_f32_16x16x32_bf16 v[216:219], a[72:75], a[144:147], 0\n"
"s_waitcnt lgkmcnt(8)\n"
"buffer_load_dwordx4 v28, s[12:15], s86 offen lds\n"
"s_mul_i32 s21, s33, 5\n"
"v_mfma_f32_16x16x32_bf16 v[216:219], a[84:87], a[148:151], v[216:219]\n"
"s_mul_i32 s78, s33, 8\n"
"s_mul_i32 s73, s52, 1536\n"
"s_mul_i32 s22, s33, 6\n"
"v_mfma_f32_16x16x32_bf16 v[220:223], a[76:79], a[144:147], 0\n"
"s_waitcnt lgkmcnt(6)\n"
"s_add_u32 m0, s71, 62816\n"
"v_mfma_f32_16x16x32_bf16 v[220:223], a[88:91], a[148:151], v[220:223]\n"
"s_mul_i32 s71, s54, 312\n"
"buffer_load_dwordx4 v29, s[12:15], s86 offen lds\n"
"v_mfma_f32_16x16x32_bf16 v[224:227], a[80:83], a[144:147], 0\n"
"s_waitcnt lgkmcnt(4)\n"
"s_and_b32 s86, s52, 1\n"
"v_mfma_f32_16x16x32_bf16 v[224:227], a[92:95], a[148:151], v[224:227]\n"
"s_lshl1_add_u32 s71, s86, s71\n"
"s_add_u32 s71, s71, 162760\n"
"v_add_u32 v24, s71, v96\n"
"ds_read_u16 v240, v24 offset:6\n"
"ds_read_u16 v241, v24 offset:4\n"
"ds_read_u16 v242, v24 offset:2\n"
"ds_read_u16 v243, v24\n"
"s_and_b32 s86, s51, 1\n"
"s_lshl3_add_u32 s85, s86, s85\n"
"s_add_u32 s85, s85, 4096\n"
"v_add_u32 v203, s85, v192\n"
"s_lshl_b32 s85, s51, 6\n"
"v_add_u32 v81, s85, v112\n"
"v_add_u32 v154, s85, v112\n"
"v_lshrrev_b32 v81, 1, v81\n"
"v_mul_u32_u24 v155, 0xaab, v154\n"
"v_add_u32 v81, 0x100, v81\n"
"v_lshrrev_b32 v155, 16, v155\n"
"v_mul_u32_u24 v82, 0xaab, v81\n"
"v_mul_i32_i24 v80, -24, v155\n"
"v_lshrrev_b32 v82, 16, v82\n"
"v_add_u32 v154, v154, v80\n"
"v_mul_i32_i24 v83, -24, v82\n"
"v_and_b32 v80, 7, v155\n"
"v_add_u32 v81, v81, v83\n"
"v_xor_b32 v154, v154, v80\n"
"v_and_b32 v83, 7, v82\n"
"v_lshrrev_b32 v80, 1, v155\n"
"v_xor_b32 v81, v81, v83\n"
"v_and_b32 v80, 4, v80\n"
"v_lshrrev_b32 v83, 1, v82\n"
"s_lshl_b32 s86, s52, 3\n"
"s_waitcnt lgkmcnt(2)\n"
"v_lshl_or_b32 v240, v241, 16, v240\n"
"v_and_or_b32 v80, v155, 3, v80\n"
"s_lshl2_add_u32 s71, s60, s52\n"
"s_waitcnt lgkmcnt(0)\n"
"v_lshl_or_b32 v241, v243, 16, v242\n"
"ds_write_b64 v30, v[240:241]\n"
"v_and_b32 v83, 4, v83\n"
"v_add_u32 v80, s86, v80\n"
"v_bfe_u32 v247, v155, 2, 1\n"
"s_mul_i32 s38, s71, 6144\n"
"v_and_or_b32 v83, v82, 3, v83\n"
"v_lshrrev_b32 v246, v80, s84\n"
"v_xor_b32 v247, s54, v247\n"
"v_add_u32 v83, s86, v83\n"
"s_lshl_b32 s71, s51, 2\n"
"v_lshlrev_b32 v246, 31, v246\n"
"v_mul_u32_u24 v247, 172032, v247\n"
"v_mul_u32_u24 v80, 0x300, v80\n"
"s_add_u32 s38, s38, s74\n"
"v_lshrrev_b32 v248, v83, s84\n"
"v_bfe_u32 v250, v82, 2, 1\n"
"v_add_u32 v253, s71, v132\n"
"v_add3_u32 v246, v246, v247, v80\n"
"v_lshlrev_b32 v248, 31, v248\n"
"v_xor_b32 v250, s54, v250\n"
"v_xor_b32 v253, v253, v252\n"
"s_lshl1_add_u32 s74, s51, 16\n"
"v_lshl_add_u32 v194, v154, 4, v246\n"
"v_lshl_add_u32 v248, v249, 3, v248\n"
"v_mul_u32_u24 v250, 172032, v250\n"
"v_mul_u32_u24 v83, 0x300, v83\n"
"v_lshl_add_u32 v196, v253, 4, v251\n"
"s_add_u32 m0, s34, 0\n"
"v_add_u32 v254, s74, v254\n"
"buffer_load_dwordx4 v190, s[8:11], s32 offen lds\n"
"v_add3_u32 v248, v248, v250, v83\n"
"v_lshl_add_u32 v251, v138, 3, v251\n"
"v_xor_b32 v254, v254, v252\n"
"s_mov_b64 exec, 0\n"
"s_cmp_eq_u32 s51, 0\n"
"s_cbranch_scc1 .Lic4%=\n"
"s_cmp_eq_u32 s51, 1\n"
"s_cbranch_scc1 .Lic5%=\n"
"s_cmp_eq_u32 s51, 2\n"
"s_cbranch_scc1 .Lic6%=\n"
"s_branch .Lic7%=\n"
".Lib1%=:\n"
"s_mov_b64 exec, -1\n"
"s_add_u32 m0, s34, 3072\n"
"s_waitcnt vmcnt(11)\n"
"buffer_load_dwordx4 v191, s[8:11], s32 offen lds\n"
"s_add_u32 s32, s32, s33\n"
"ds_read_b128 a[168:171], v188 offset:6144\n"
"ds_read_b128 a[172:175], v189 offset:6144\n"
"v_lshl_add_u32 v195, v81, 4, v248\n"
"v_lshl_add_u32 v197, v254, 4, v251\n"
"s_add_u32 s38, s38, 31616\n"
"s_add_u32 s39, s38, 22119552\n"
"s_and_b32 s85, s52, 1\n"
"s_lshl2_add_u32 s85, s85, s61\n"
"v_add_u32 v86, s85, v134\n"
"v_mul_u32_u24 v86, 0x300, v86\n"
"s_add_u32 m0, s34, 1024\n"
"v_lshl_add_u32 v86, v87, 7, v86\n"
"buffer_load_dwordx4 v190, s[8:11], s0 offen lds\n"
"v_or_b32 v238, v88, v86\n"
"s_mov_b32 s71, 0x3e553b95\n"
"s_lshr_b32 s86, s52, 1\n"
"s_mul_i32 s86, s86, 172032\n"
"s_add_u32 s86, s86, s36\n"
"s_add_u32 s78, s78, s86\n"
"s_add_u32 s37, s37, s73\n"
"s_lshl_b32 s73, s51, 5\n"
"s_sub_u32 s73, 256, s73\n"
"v_add_u32 v235, s73, v85\n"
"s_mov_b32 s18, 73728\n"
"s_add_u32 m0, s34, 4096\n"
"s_add_u32 s37, s37, 31616\n"
"buffer_load_dwordx4 v191, s[8:11], s0 offen lds\n"
"s_waitcnt vmcnt(12) lgkmcnt(1)\n"
"v_mfma_f32_16x16x32_bf16 v[244:247], a[0:3], a[168:171], 0\n"
"s_waitcnt lgkmcnt(0)\n"
"ds_read_b128 a[176:179], v188 offset:7168\n"
"ds_read_b128 a[180:183], v189 offset:7168\n"
"v_mfma_f32_16x16x32_bf16 v[244:247], a[12:15], a[172:175], v[244:247]\n"
"s_add_u32 s0, s0, s33\n"
"s_mul_i32 s79, s51, 3072\n"
"v_mfma_f32_16x16x32_bf16 v[248:251], a[4:7], a[168:171], 0\n"
"s_mul_i32 s80, s66, 12288\n"
"s_mov_b32 s26, 19267584\n"
"v_mfma_f32_16x16x32_bf16 v[248:251], a[16:19], a[172:175], v[248:251]\n"
"s_add_u32 m0, s34, 2048\n"
"s_mov_b32 s27, 0x20000\n"
"buffer_load_dwordx4 v190, s[8:11], s1 offen lds\n"
"v_mfma_f32_16x16x32_bf16 v[252:255], a[8:11], a[168:171], 0\n"
"s_mov_b32 s19, 0x20000\n"
"s_add_u32 s40, s37, 22119552\n"
"v_mfma_f32_16x16x32_bf16 v[252:255], a[20:23], a[172:175], v[252:255]\n"
"s_sub_u32 s16, s16, 99456\n"
"s_subb_u32 s17, s17, 0\n"
"v_mfma_f32_16x16x32_bf16 v[104:107], a[72:75], a[168:171], 0\n"
"s_mov_b32 s18, 173184\n"
"s_add_u32 s80, s80, s79\n"
"v_mfma_f32_16x16x32_bf16 v[104:107], a[84:87], a[172:175], v[104:107]\n"
"s_waitcnt vmcnt(11)\n"
"ds_read_b128 a[184:187], v188 offset:8192\n"
"ds_read_b128 a[188:191], v189 offset:8192\n"
"v_mfma_f32_16x16x32_bf16 v[108:111], a[76:79], a[168:171], 0\n"
"s_add_u32 m0, s34, 5120\n"
"s_nop 0\n"
"buffer_load_dwordx4 v191, s[8:11], s1 offen lds\n"
"v_mfma_f32_16x16x32_bf16 v[108:111], a[88:91], a[172:175], v[108:111]\n"
"s_add_u32 s1, s1, s33\n"
"v_mfma_f32_16x16x32_bf16 v[228:231], a[80:83], a[168:171], 0\n"
"v_mfma_f32_16x16x32_bf16 v[228:231], a[92:95], a[172:175], v[228:231]\n"
"s_add_u32 m0, s35, 0\n"
"s_nop 0\n"
"buffer_load_dwordx4 v190, s[8:11], s32 offen lds\n"
"s_add_u32 m0, s35, 3072\n"
"s_nop 0\n"
"buffer_load_dwordx4 v191, s[8:11], s32 offen lds\n"
"s_add_u32 s32, s32, s33\n"
"s_add_u32 m0, s35, 1024\n"
"s_waitcnt vmcnt(11)\n"
"buffer_load_dwordx4 v190, s[8:11], s0 offen lds\n"
"s_barrier\n"
"ds_read_b64_tr_b16 a[24:25], v23 offset:13504\n"
"ds_read_b64_tr_b16 a[26:27], v23 offset:15040\n"
"ds_read_b64_tr_b16 a[28:29], v23 offset:13536\n"
"ds_read_b64_tr_b16 a[30:31], v23 offset:15072\n"
"ds_read_b64_tr_b16 a[32:33], v23 offset:13568\n"
"ds_read_b64_tr_b16 a[34:35], v23 offset:15104\n"
"ds_read_b64_tr_b16 a[96:97], v23 offset:16576\n"
"ds_read_b64_tr_b16 a[98:99], v23 offset:18112\n"
"ds_read_b64_tr_b16 a[100:101], v23 offset:16608\n"
"ds_read_b64_tr_b16 a[102:103], v23 offset:18144\n"
"ds_read_b64_tr_b16 a[104:105], v23 offset:16640\n"
"s_add_u32 m0, s35, 4096\n"
"ds_read_b64_tr_b16 a[106:107], v23 offset:18176\n"
"buffer_load_dwordx4 v191, s[8:11], s0 offen lds\n"
"s_add_u32 s0, s0, s33\n"
"ds_read_b64_tr_b16 a[36:37], v23 offset:20256\n"
"ds_read_b64_tr_b16 a[38:39], v23 offset:21792\n"
"ds_read_b64_tr_b16 a[40:41], v23 offset:20288\n"
"ds_read_b64_tr_b16 a[42:43], v23 offset:21824\n"
"ds_read_b64_tr_b16 a[44:45], v23 offset:20320\n"
"ds_read_b64_tr_b16 a[46:47], v23 offset:21856\n"
"s_waitcnt lgkmcnt(15)\n"
"v_mfma_f32_16x16x32_bf16 v[204:207], a[24:27], a[152:155], v[204:207]\n"
"s_waitcnt lgkmcnt(14)\n"
"ds_read_b64_tr_b16 a[108:109], v23 offset:23328\n"
"ds_read_b64_tr_b16 a[110:111], v23 offset:24864\n"
"v_mfma_f32_16x16x32_bf16 v[208:211], a[28:31], a[152:155], v[208:211]\n"
"s_waitcnt lgkmcnt(14)\n"
"ds_read_b64_tr_b16 a[112:113], v23 offset:23360\n"
"ds_read_b64_tr_b16 a[114:115], v23 offset:24896\n"
"v_mfma_f32_16x16x32_bf16 v[212:215], a[32:35], a[152:155], v[212:215]\n"
"s_waitcnt lgkmcnt(14)\n"
"ds_read_b64_tr_b16 a[116:117], v23 offset:23392\n"
"ds_read_b64_tr_b16 a[118:119], v23 offset:24928\n"
"v_mfma_f32_16x16x32_bf16 v[216:219], a[96:99], a[152:155], v[216:219]\n"
"s_waitcnt lgkmcnt(14)\n"
"s_add_u32 m0, s35, 2048\n"
"v_mfma_f32_16x16x32_bf16 v[220:223], a[100:103], a[152:155], v[220:223]\n"
"s_waitcnt vmcnt(10) lgkmcnt(12)\n"
"buffer_load_dwordx4 v190, s[8:11], s1 offen lds\n"
"v_mfma_f32_16x16x32_bf16 v[224:227], a[104:107], a[152:155], v[224:227]\n"
"s_waitcnt lgkmcnt(10)\n"
"s_barrier\n"
"v_mfma_f32_16x16x32_bf16 v[204:207], a[36:39], a[156:159], v[204:207]\n"
"s_waitcnt lgkmcnt(8)\n"
"ds_read_b64_tr_b16 a[48:49], v23 offset:60768\n"
"ds_read_b64_tr_b16 a[50:51], v23 offset:62304\n"
"v_mfma_f32_16x16x32_bf16 v[208:211], a[40:43], a[156:159], v[208:211]\n"
"s_waitcnt lgkmcnt(8)\n"
"ds_read_b64_tr_b16 a[52:53], v23 offset:60800\n"
"ds_read_b64_tr_b16 a[54:55], v23 offset:62336\n"
"v_mfma_f32_16x16x32_bf16 v[212:215], a[44:47], a[156:159], v[212:215]\n"
"s_waitcnt lgkmcnt(8)\n"
"ds_read_b64_tr_b16 a[56:57], v23 offset:60832\n"
"ds_read_b64_tr_b16 a[58:59], v23 offset:62368\n"
"v_mfma_f32_16x16x32_bf16 v[216:219], a[108:111], a[156:159], v[216:219]\n"
"s_waitcnt lgkmcnt(8)\n"
"ds_read_b64_tr_b16 a[120:121], v72 offset:9824\n"
"ds_read_b64_tr_b16 a[122:123], v72 offset:11360\n"
"v_mfma_f32_16x16x32_bf16 v[220:223], a[112:115], a[156:159], v[220:223]\n"
"s_waitcnt lgkmcnt(8)\n"
"ds_read_b64_tr_b16 a[124:125], v72 offset:9856\n"
"ds_read_b64_tr_b16 a[126:127], v72 offset:11392\n"
"v_mfma_f32_16x16x32_bf16 v[224:227], a[116:119], a[156:159], v[224:227]\n"
"s_add_u32 m0, s35, 5120\n"
"ds_read_b64_tr_b16 a[128:129], v72 offset:9888\n"
"buffer_load_dwordx4 v191, s[8:11], s1 offen lds\n"
"v_mfma_f32_16x16x32_bf16 v[244:247], a[24:27], a[176:179], v[244:247]\n"
"s_add_u32 s1, s1, s33\n"
"ds_read_b64_tr_b16 a[130:131], v72 offset:11424\n"
"ds_read_b64_tr_b16 a[60:61], v72 offset:13504\n"
"v_mfma_f32_16x16x32_bf16 v[244:247], a[36:39], a[180:183], v[244:247]\n"
"s_waitcnt lgkmcnt(11)\n"
"ds_read_b64_tr_b16 a[62:63], v72 offset:15040\n"
"ds_read_b64_tr_b16 a[64:65], v72 offset:13536\n"
"v_mfma_f32_16x16x32_bf16 v[204:207], a[48:51], a[160:163], v[204:207]\n"
"s_waitcnt lgkmcnt(11)\n"
"ds_read_b64_tr_b16 a[66:67], v72 offset:15072\n"
"ds_read_b64_tr_b16 a[68:69], v72 offset:13568\n"
"v_mfma_f32_16x16x32_bf16 v[208:211], a[52:55], a[160:163], v[208:211]\n"
"s_waitcnt lgkmcnt(11)\n"
"ds_read_b64_tr_b16 a[70:71], v72 offset:15104\n"
"ds_read_b64_tr_b16 a[132:133], v72 offset:16576\n"
"v_mfma_f32_16x16x32_bf16 v[212:215], a[56:59], a[160:163], v[212:215]\n"
"s_waitcnt lgkmcnt(11)\n"
"ds_read_b64_tr_b16 a[134:135], v72 offset:18112\n"
"ds_read_b64_tr_b16 a[136:137], v72 offset:16608\n"
"v_mfma_f32_16x16x32_bf16 v[216:219], a[120:123], a[160:163], v[216:219]\n"
"s_waitcnt lgkmcnt(11)\n"
"ds_read_b64_tr_b16 a[138:139], v72 offset:18144\n"
"ds_read_b64_tr_b16 a[140:141], v72 offset:16640\n"
"v_mfma_f32_16x16x32_bf16 v[220:223], a[124:127], a[160:163], v[220:223]\n"
"ds_read_b64_tr_b16 a[142:143], v72 offset:18176\n"
"v_mfma_f32_16x16x32_bf16 v[248:251], a[28:31], a[176:179], v[248:251]\n"
"s_waitcnt lgkmcnt(12)\n"
"v_mfma_f32_16x16x32_bf16 v[224:227], a[128:131], a[160:163], v[224:227]\n"
"s_waitcnt lgkmcnt(10)\n"
"v_mfma_f32_16x16x32_bf16 v[204:207], a[60:63], a[164:167], v[204:207]\n"
"s_waitcnt lgkmcnt(8)\n"
"v_mfma_f32_16x16x32_bf16 v[208:211], a[64:67], a[164:167], v[208:211]\n"
"s_waitcnt lgkmcnt(6)\n"
"v_mfma_f32_16x16x32_bf16 v[212:215], a[68:71], a[164:167], v[212:215]\n"
"s_waitcnt lgkmcnt(4)\n"
"v_mfma_f32_16x16x32_bf16 v[216:219], a[132:135], a[164:167], v[216:219]\n"
"s_waitcnt lgkmcnt(2)\n"
"s_nop 0\n"
"v_cvt_pk_bf16_f32 v204, v204, v205\n"
"v_mfma_f32_16x16x32_bf16 v[220:223], a[136:139], a[164:167], v[220:223]\n"
"s_waitcnt lgkmcnt(0)\n"
"v_cvt_pk_bf16_f32 v205, v206, v207\n"
"v_cvt_pk_bf16_f32 v206, v208, v209\n"
"v_mfma_f32_16x16x32_bf16 v[224:227], a[140:143], a[164:167], v[224:227]\n"
"s_barrier\n"
"v_mfma_f32_16x16x32_bf16 v[252:255], a[32:35], a[176:179], v[252:255]\n"
"v_cvt_pk_bf16_f32 v207, v210, v211\n"
"v_cvt_pk_bf16_f32 v212, v212, v213\n"
"v_mfma_f32_16x16x32_bf16 v[252:255], a[44:47], a[180:183], v[252:255]\n"
"ds_write_b128 v21, v[204:207]\n"
"v_mfma_f32_16x16x32_bf16 v[252:255], a[56:59], a[184:187], v[252:255]\n"
"v_cvt_pk_bf16_f32 v213, v214, v215\n"
"v_cvt_pk_bf16_f32 v214, v224, v225\n"
"v_mfma_f32_16x16x32_bf16 v[252:255], a[68:71], a[188:191], v[252:255]\n"
"v_cvt_pk_bf16_f32 v215, v226, v227\n"
"v_cvt_pk_bf16_f32 v216, v216, v217\n"
"v_mfma_f32_16x16x32_bf16 v[104:107], a[96:99], a[176:179], v[104:107]\n"
"ds_write_b128 v21, v[212:215] offset:64\n"
"v_mfma_f32_16x16x32_bf16 v[104:107], a[108:111], a[180:183], v[104:107]\n"
"s_add_u32 m0, s34, 6144\n"
"v_cvt_pk_bf16_f32 v217, v218, v219\n"
"v_cvt_pk_bf16_f32 v218, v220, v221\n"
"v_mfma_f32_16x16x32_bf16 v[104:107], a[120:123], a[184:187], v[104:107]\n"
"buffer_load_dwordx4 v190, s[8:11], s32 offen lds\n"
"v_cvt_pk_bf16_f32 v219, v222, v223\n"
"v_mfma_f32_16x16x32_bf16 v[104:107], a[132:135], a[188:191], v[104:107]\n"
"ds_write_b128 v21, v[216:219] offset:128\n"
"v_mfma_f32_16x16x32_bf16 v[108:111], a[100:103], a[176:179], v[108:111]\n"
"s_waitcnt vmcnt(11)\n"
"v_cvt_pk_bf16_f32 v252, v252, v253\n"
"v_cvt_pk_bf16_f32 v253, v254, v255\n"
"v_mfma_f32_16x16x32_bf16 v[108:111], a[112:115], a[180:183], v[108:111]\n"
"s_add_u32 m0, s34, 9216\n"
"s_nop 0\n"
"buffer_load_dwordx4 v191, s[8:11], s32 offen lds\n"
"v_mfma_f32_16x16x32_bf16 v[108:111], a[124:127], a[184:187], v[108:111]\n"
"s_add_u32 s32, s32, s33\n"
"ds_read_b128 v[32:35], v188\n"
"ds_read_b128 v[36:39], v189\n"
"v_mfma_f32_16x16x32_bf16 v[108:111], a[136:139], a[188:191], v[108:111]\n"
"v_cvt_pk_bf16_f32 v104, v104, v105\n"
"v_cvt_pk_bf16_f32 v105, v106, v107\n"
"v_mfma_f32_16x16x32_bf16 v[228:231], a[104:107], a[176:179], v[228:231]\n"
"s_add_u32 m0, s34, 7168\n"
"ds_read_b64 v[80:81], v20 offset:272\n"
"buffer_load_dwordx4 v190, s[8:11], s0 offen lds\n"
"v_mfma_f32_16x16x32_bf16 v[228:231], a[116:119], a[180:183], v[228:231]\n"
"ds_read_b64 v[86:87], v20 offset:336\n"
"v_cvt_pk_bf16_f32 v106, v108, v109\n"
"v_mfma_f32_16x16x32_bf16 v[228:231], a[128:131], a[184:187], v[228:231]\n"
"v_cvt_pk_bf16_f32 v107, v110, v111\n"
"v_mfma_f32_16x16x32_bf16 v[228:231], a[140:143], a[188:191], v[228:231]\n"
"ds_write_b128 v22, v[104:107] offset:128\n"
"v_mfma_f32_16x16x32_bf16 v[248:251], a[40:43], a[180:183], v[248:251]\n"
"s_waitcnt vmcnt(11) lgkmcnt(4)\n"
"s_add_u32 m0, s34, 10240\n"
"v_mfma_f32_16x16x32_bf16 v[248:251], a[52:55], a[184:187], v[248:251]\n"
"buffer_load_dwordx4 v191, s[8:11], s0 offen lds\n"
"ds_read_b128 v[40:43], v188 offset:1024\n"
"v_mfma_f32_16x16x32_bf16 v[248:251], a[64:67], a[188:191], v[248:251]\n"
"s_waitcnt lgkmcnt(3)\n"
"ds_read_b128 v[44:47], v189 offset:1024\n"
"v_cvt_pk_bf16_f32 v254, v228, v229\n"
"v_mfma_f32_16x16x32_bf16 v[244:247], a[48:51], a[184:187], v[244:247]\n"
"s_waitcnt lgkmcnt(3)\n"
"v_cvt_pk_bf16_f32 v255, v230, v231\n"
"v_bfi_b32 v80, v4, v80, v26\n"
"v_mfma_f32_16x16x32_bf16 v[244:247], a[60:63], a[188:191], v[244:247]\n"
"s_add_u32 m0, s34, 8192\n"
"ds_write_b128 v22, v[252:255] offset:64\n"
"v_mfma_f32_16x16x32_bf16 v[204:207], a[0:3], v[32:35], 0\n"
"s_add_u32 s0, s0, s33\n"
"buffer_load_dwordx4 v190, s[8:11], s1 offen lds\n"
"v_bfi_b32 v81, v5, v81, v26\n"
"v_mfma_f32_16x16x32_bf16 v[204:207], a[12:15], v[36:39], v[204:207]\n"
"s_waitcnt vmcnt(11)\n"
"v_cvt_pk_bf16_f32 v244, v244, v245\n"
"v_cvt_pk_bf16_f32 v245, v246, v247\n"
"v_mfma_f32_16x16x32_bf16 v[208:211], a[4:7], v[32:35], 0\n"
"s_add_u32 m0, s34, 11264\n"
"ds_read_b128 v[48:51], v188 offset:2048\n"
"ds_read_b128 v[52:55], v189 offset:2048\n"
"v_mfma_f32_16x16x32_bf16 v[208:211], a[16:19], v[36:39], v[208:211]\n"
"s_waitcnt lgkmcnt(4)\n"
"buffer_load_dwordx4 v191, s[8:11], s1 offen lds\n"
"v_cvt_pk_bf16_f32 v246, v248, v249\n"
"v_mfma_f32_16x16x32_bf16 v[208:211], a[28:31], v[40:43], v[208:211]\n"
"s_waitcnt lgkmcnt(3)\n"
"v_cvt_pk_bf16_f32 v247, v250, v251\n"
"v_bfi_b32 v86, v6, v86, v26\n"
"v_mfma_f32_16x16x32_bf16 v[208:211], a[40:43], v[44:47], v[208:211]\n"
"s_add_u32 s1, s1, s33\n"
"ds_write_b128 v22, v[244:247]\n"
"v_mfma_f32_16x16x32_bf16 v[212:215], a[8:11], v[32:35], 0\n"
"s_add_u32 m0, s65, 154752\n"
"v_bfi_b32 v87, v7, v87, v26\n"
"v_mfma_f32_16x16x32_bf16 v[212:215], a[20:23], v[36:39], v[212:215]\n"
"buffer_load_dwordx4 v27, s[12:15], s81 offen lds\n"
"ds_read_b64 v[88:89], v20 offset:544\n"
"v_mfma_f32_16x16x32_bf16 v[212:215], a[32:35], v[40:43], v[212:215]\n"
"s_waitcnt lgkmcnt(3)\n"
"ds_read_b64 v[94:95], v20 offset:608\n"
"ds_read_b64 v[96:97], v20 offset:816\n"
"v_mfma_f32_16x16x32_bf16 v[212:215], a[44:47], v[44:47], v[212:215]\n"
"ds_read_b64 v[102:103], v20 offset:880\n"
"v_mfma_f32_16x16x32_bf16 v[212:215], a[56:59], v[48:51], v[212:215]\n"
"s_waitcnt vmcnt(11) lgkmcnt(5)\n"
"ds_read_b128 a[216:219], v70\n"
"ds_read_b128 a[220:223], v71\n"
"v_mfma_f32_16x16x32_bf16 v[212:215], a[68:71], v[52:55], v[212:215]\n"
"s_add_u32 m0, s65, 155776\n"
"s_nop 0\n"
"buffer_load_dwordx4 v28, s[12:15], s81 offen lds\n"
"v_mfma_f32_16x16x32_bf16 v[216:219], a[72:75], v[32:35], 0\n"
"v_mfma_f32_16x16x32_bf16 v[216:219], a[84:87], v[36:39], v[216:219]\n"
"v_mfma_f32_16x16x32_bf16 v[216:219], a[96:99], v[40:43], v[216:219]\n"
"s_waitcnt lgkmcnt(5)\n"
"v_bfi_b32 v88, v0, v88, v26\n"
"v_bfi_b32 v89, v1, v89, v26\n"
"v_mfma_f32_16x16x32_bf16 v[216:219], a[108:111], v[44:47], v[216:219]\n"
"s_waitcnt lgkmcnt(3)\n"
"v_cvt_pk_bf16_f32 v212, v212, v213\n"
"v_cvt_pk_bf16_f32 v213, v214, v215\n"
"v_mfma_f32_16x16x32_bf16 v[216:219], a[120:123], v[48:51], v[216:219]\n"
"s_waitcnt lgkmcnt(2)\n"
"v_bfi_b32 v94, v2, v94, v26\n"
"v_bfi_b32 v95, v3, v95, v26\n"
"v_mfma_f32_16x16x32_bf16 v[216:219], a[132:135], v[52:55], v[216:219]\n"
"s_waitcnt lgkmcnt(1)\n"
"v_bfi_b32 v96, v0, v96, v26\n"
"v_bfi_b32 v97, v1, v97, v26\n"
"v_mfma_f32_16x16x32_bf16 v[220:223], a[76:79], v[32:35], 0\n"
"s_add_u32 m0, s65, 156800\n"
"v_bfi_b32 v102, v2, v102, v26\n"
"v_bfi_b32 v103, v3, v103, v26\n"
"v_mfma_f32_16x16x32_bf16 v[220:223], a[88:91], v[36:39], v[220:223]\n"
"buffer_load_dwordx4 v29, s[12:15], s81 offen lds\n"
"v_cvt_pk_bf16_f32 v216, v216, v217\n"
"v_mfma_f32_16x16x32_bf16 v[220:223], a[100:103], v[40:43], v[220:223]\n"
"v_cvt_pk_bf16_f32 v217, v218, v219\n"
"v_mfma_f32_16x16x32_bf16 v[220:223], a[112:115], v[44:47], v[220:223]\n"
"v_mfma_f32_16x16x32_bf16 v[220:223], a[124:127], v[48:51], v[220:223]\n"
"v_mfma_f32_16x16x32_bf16 v[220:223], a[136:139], v[52:55], v[220:223]\n"
"v_mfma_f32_16x16x32_bf16 v[224:227], a[80:83], v[32:35], 0\n"
"v_mfma_f32_16x16x32_bf16 v[224:227], a[92:95], v[36:39], v[224:227]\n"
"v_mfma_f32_16x16x32_bf16 v[224:227], a[104:107], v[40:43], v[224:227]\n"
"v_mfma_f32_16x16x32_bf16 v[224:227], a[116:119], v[44:47], v[224:227]\n"
"v_mfma_f32_16x16x32_bf16 v[224:227], a[128:131], v[48:51], v[224:227]\n"
"v_mfma_f32_16x16x32_bf16 v[224:227], a[140:143], v[52:55], v[224:227]\n"
"v_mfma_f32_16x16x32_bf16 v[204:207], a[24:27], v[40:43], v[204:207]\n"
"s_nop 0\n"
"v_cvt_pk_bf16_f32 v218, v220, v221\n"
"v_mfma_f32_16x16x32_bf16 v[204:207], a[36:39], v[44:47], v[204:207]\n"
"v_cvt_pk_bf16_f32 v219, v222, v223\n"
"v_mfma_f32_16x16x32_bf16 v[204:207], a[48:51], v[48:51], v[204:207]\n"
"ds_write_b128 v186, v[216:219] offset:20384\n"
"v_mfma_f32_16x16x32_bf16 v[204:207], a[60:63], v[52:55], v[204:207]\n"
"v_cvt_pk_bf16_f32 v214, v224, v225\n"
"v_cvt_pk_bf16_f32 v215, v226, v227\n"
"v_mfma_f32_16x16x32_bf16 v[208:211], a[52:55], v[48:51], v[208:211]\n"
"ds_write_b128 v186, v[212:215] offset:20320\n"
"v_mfma_f32_16x16x32_bf16 v[208:211], a[64:67], v[52:55], v[208:211]\n"
"v_mfma_f32_16x16x32_bf16 v[244:247], a[0:3], a[216:219], 0\n"
"s_waitcnt lgkmcnt(2)\n"
"s_nop 0\n"
"v_cvt_pk_bf16_f32 v204, v204, v205\n"
"v_mfma_f32_16x16x32_bf16 v[244:247], a[12:15], a[220:223], v[244:247]\n"
"v_cvt_pk_bf16_f32 v205, v206, v207\n"
"v_mfma_f32_16x16x32_bf16 v[248:251], a[4:7], a[216:219], 0\n"
"s_nop 0\n"
"v_cvt_pk_bf16_f32 v206, v208, v209\n"
"v_mfma_f32_16x16x32_bf16 v[248:251], a[16:19], a[220:223], v[248:251]\n"
"v_cvt_pk_bf16_f32 v207, v210, v211\n"
"v_mfma_f32_16x16x32_bf16 v[252:255], a[8:11], a[216:219], 0\n"
"ds_write_b128 v186, v[204:207] offset:20256\n"
"v_mfma_f32_16x16x32_bf16 v[252:255], a[20:23], a[220:223], v[252:255]\n"
"s_waitcnt vmcnt(11)\n"
"s_add_u32 m0, s43, 0\n"
"v_mfma_f32_16x16x32_bf16 v[104:107], a[72:75], a[216:219], 0\n"
"buffer_load_dwordx4 v27, s[12:15], s3 offen lds\n"
"ds_read_b128 a[224:227], v70 offset:1024\n"
"v_mfma_f32_16x16x32_bf16 v[104:107], a[84:87], a[220:223], v[104:107]\n"
"ds_read_b128 a[228:231], v71 offset:1024\n"
"v_mfma_f32_16x16x32_bf16 v[108:111], a[76:79], a[216:219], 0\n"
"s_add_u32 m0, s43, 1024\n"
"v_mfma_f32_16x16x32_bf16 v[108:111], a[88:91], a[220:223], v[108:111]\n"
"buffer_load_dwordx4 v28, s[12:15], s3 offen lds\n"
"v_mfma_f32_16x16x32_bf16 v[228:231], a[80:83], a[216:219], 0\n"
"v_mfma_f32_16x16x32_bf16 v[228:231], a[92:95], a[220:223], v[228:231]\n"
"s_add_u32 m0, s43, 2048\n"
"s_add_u32 s43, s79, 0\n"
"s_waitcnt vmcnt(11)\n"
"ds_read_b128 a[232:235], v70 offset:2048\n"
"buffer_load_dwordx4 v29, s[12:15], s3 offen lds\n"
"ds_read_b128 a[236:239], v71 offset:2048\n"
"s_waitcnt lgkmcnt(3)\n"
"v_mfma_f32_16x16x32_bf16 v[228:231], a[104:107], a[224:227], v[228:231]\n"
"s_waitcnt lgkmcnt(2)\n"
"v_mfma_f32_16x16x32_bf16 v[228:231], a[116:119], a[228:231], v[228:231]\n"
"s_add_u32 m0, s34, 0\n"
"v_mfma_f32_16x16x32_bf16 v[244:247], a[24:27], a[224:227], v[244:247]\n"
"v_mfma_f32_16x16x32_bf16 v[244:247], a[36:39], a[228:231], v[244:247]\n"
"buffer_load_dwordx4 v190, s[8:11], s32 offen lds\n"
"v_mfma_f32_16x16x32_bf16 v[248:251], a[28:31], a[224:227], v[248:251]\n"
"v_mfma_f32_16x16x32_bf16 v[248:251], a[40:43], a[228:231], v[248:251]\n"
"s_add_u32 m0, s34, 3072\n"
"v_mfma_f32_16x16x32_bf16 v[252:255], a[32:35], a[224:227], v[252:255]\n"
"s_waitcnt vmcnt(11) lgkmcnt(1)\n"
"ds_read_b128 a[240:243], v188 offset:6144\n"
"v_mfma_f32_16x16x32_bf16 v[252:255], a[44:47], a[228:231], v[252:255]\n"
"buffer_load_dwordx4 v191, s[8:11], s32 offen lds\n"
"ds_read_b128 a[244:247], v189 offset:6144\n"
"v_mfma_f32_16x16x32_bf16 v[252:255], a[56:59], a[232:235], v[252:255]\n"
"s_waitcnt lgkmcnt(2)\n"
"s_add_u32 s32, s32, s33\n"
"v_mfma_f32_16x16x32_bf16 v[252:255], a[68:71], a[236:239], v[252:255]\n"
"s_add_u32 m0, s34, 1024\n"
"v_mfma_f32_16x16x32_bf16 v[104:107], a[96:99], a[224:227], v[104:107]\n"
"v_mfma_f32_16x16x32_bf16 v[104:107], a[108:111], a[228:231], v[104:107]\n"
"buffer_load_dwordx4 v190, s[8:11], s0 offen lds\n"
"v_mfma_f32_16x16x32_bf16 v[104:107], a[120:123], a[232:235], v[104:107]\n"
"v_mfma_f32_16x16x32_bf16 v[104:107], a[132:135], a[236:239], v[104:107]\n"
"s_waitcnt lgkmcnt(1)\n"
"s_nop 0\n"
"v_cvt_pk_bf16_f32 v252, v252, v253\n"
"v_mfma_f32_16x16x32_bf16 v[108:111], a[100:103], a[224:227], v[108:111]\n"
"s_add_u32 m0, s34, 4096\n"
"v_cvt_pk_bf16_f32 v253, v254, v255\n"
"v_mfma_f32_16x16x32_bf16 v[108:111], a[112:115], a[228:231], v[108:111]\n"
"buffer_load_dwordx4 v191, s[8:11], s0 offen lds\n"
"v_cvt_pk_bf16_f32 v104, v104, v105\n"
"v_mfma_f32_16x16x32_bf16 v[108:111], a[124:127], a[232:235], v[108:111]\n"
"s_waitcnt vmcnt(12)\n"
"ds_read_b128 a[248:251], v188 offset:7168\n"
"ds_read_b128 a[252:255], v189 offset:7168\n"
"v_mfma_f32_16x16x32_bf16 v[108:111], a[136:139], a[236:239], v[108:111]\n"
"s_add_u32 s0, s0, s33\n"
"v_cvt_pk_bf16_f32 v105, v106, v107\n"
"s_add_u32 m0, s34, 2048\n"
"v_mfma_f32_16x16x32_bf16 v[244:247], a[48:51], a[232:235], v[244:247]\n"
"v_mfma_f32_16x16x32_bf16 v[244:247], a[60:63], a[236:239], v[244:247]\n"
"buffer_load_dwordx4 v190, s[8:11], s1 offen lds\n"
"v_mfma_f32_16x16x32_bf16 v[248:251], a[52:55], a[232:235], v[248:251]\n"
"s_nop 0\n"
"v_cvt_pk_bf16_f32 v106, v108, v109\n"
"v_mfma_f32_16x16x32_bf16 v[248:251], a[64:67], a[236:239], v[248:251]\n"
"v_cvt_pk_bf16_f32 v107, v110, v111\n"
"s_add_u32 m0, s34, 5120\n"
"v_mfma_f32_16x16x32_bf16 v[228:231], a[128:131], a[232:235], v[228:231]\n"
"ds_write_b128 v186, v[104:107] offset:33888\n"
"v_mfma_f32_16x16x32_bf16 v[228:231], a[140:143], a[236:239], v[228:231]\n"
"s_waitcnt vmcnt(11)\n"
"buffer_load_dwordx4 v191, s[8:11], s1 offen lds\n"
"v_cvt_pk_bf16_f32 v244, v244, v245\n"
"v_mfma_f32_16x16x32_bf16 v[120:123], a[0:3], a[240:243], 0\n"
"s_waitcnt lgkmcnt(3)\n"
"v_cvt_pk_bf16_f32 v245, v246, v247\n"
"v_cvt_pk_bf16_f32 v246, v248, v249\n"
"v_mfma_f32_16x16x32_bf16 v[120:123], a[12:15], a[244:247], v[120:123]\n"
"s_waitcnt lgkmcnt(2)\n"
"ds_read_b128 v[56:59], v188 offset:8192\n"
"ds_read_b128 v[64:67], v189 offset:8192\n"
"v_mfma_f32_16x16x32_bf16 v[120:123], a[24:27], a[248:251], v[120:123]\n"
"s_waitcnt lgkmcnt(3)\n"
"v_cvt_pk_bf16_f32 v247, v250, v251\n"
"v_cvt_pk_bf16_f32 v254, v228, v229\n"
"v_mfma_f32_16x16x32_bf16 v[120:123], a[36:39], a[252:255], v[120:123]\n"
"s_add_u32 m0, s34, 6144\n"
"ds_write_b128 v186, v[244:247] offset:33760\n"
"v_mfma_f32_16x16x32_bf16 v[124:127], a[4:7], a[240:243], 0\n"
"s_add_u32 s1, s1, s33\n"
"buffer_load_dwordx4 v190, s[8:11], s32 offen lds\n"
"v_cvt_pk_bf16_f32 v255, v230, v231\n"
"v_mfma_f32_16x16x32_bf16 v[124:127], a[16:19], a[244:247], v[124:127]\n"
"ds_write_b128 v186, v[252:255] offset:33824\n"
"v_mfma_f32_16x16x32_bf16 v[124:127], a[28:31], a[248:251], v[124:127]\n"
"s_waitcnt lgkmcnt(3)\n"
"v_mov_b64 v[252:253], 0\n"
"v_mov_b64 v[254:255], 0\n"
"v_mfma_f32_16x16x32_bf16 v[124:127], a[40:43], a[252:255], v[124:127]\n"
"ds_read_b64 v[104:105], v20 offset:1088\n"
"v_mfma_f32_16x16x32_bf16 v[124:127], a[52:55], v[56:59], v[124:127]\n"
"s_waitcnt lgkmcnt(3)\n"
"ds_read_b64 v[110:111], v20 offset:1152\n"
"v_subrev_u32 v20, s49, v20\n"
"v_mfma_f32_16x16x32_bf16 v[124:127], a[64:67], v[64:67], v[124:127]\n"
"v_mfma_f32_16x16x32_bf16 v[128:131], a[8:11], a[240:243], 0\n"
"v_mfma_f32_16x16x32_bf16 v[128:131], a[20:23], a[244:247], v[128:131]\n"
"v_mfma_f32_16x16x32_bf16 v[128:131], a[32:35], a[248:251], v[128:131]\n"
"v_mfma_f32_16x16x32_bf16 v[128:131], a[44:47], a[252:255], v[128:131]\n"
"v_mfma_f32_16x16x32_bf16 v[128:131], a[56:59], v[56:59], v[128:131]\n"
"s_waitcnt lgkmcnt(1)\n"
"v_bfi_b32 v104, v8, v104, v26\n"
"v_bfi_b32 v105, v9, v105, v26\n"
"v_mfma_f32_16x16x32_bf16 v[128:131], a[68:71], v[64:67], v[128:131]\n"
"s_waitcnt lgkmcnt(0)\n"
"v_bfi_b32 v110, v10, v110, v26\n"
"v_bfi_b32 v111, v11, v111, v26\n"
"v_mfma_f32_16x16x32_bf16 v[132:135], a[72:75], a[240:243], 0\n"
"v_mfma_f32_16x16x32_bf16 v[132:135], a[84:87], a[244:247], v[132:135]\n"
"v_mfma_f32_16x16x32_bf16 v[132:135], a[96:99], a[248:251], v[132:135]\n"
"v_mfma_f32_16x16x32_bf16 v[132:135], a[108:111], a[252:255], v[132:135]\n"
"s_nop 0\n"
"v_cvt_pk_bf16_f32 v128, v128, v129\n"
"v_mfma_f32_16x16x32_bf16 v[132:135], a[120:123], v[56:59], v[132:135]\n"
"v_cvt_pk_bf16_f32 v129, v130, v131\n"
"v_mfma_f32_16x16x32_bf16 v[132:135], a[132:135], v[64:67], v[132:135]\n"
"v_mfma_f32_16x16x32_bf16 v[136:139], a[76:79], a[240:243], 0\n"
"v_mfma_f32_16x16x32_bf16 v[136:139], a[88:91], a[244:247], v[136:139]\n"
"v_mfma_f32_16x16x32_bf16 v[136:139], a[100:103], a[248:251], v[136:139]\n"
"v_mfma_f32_16x16x32_bf16 v[136:139], a[112:115], a[252:255], v[136:139]\n"
"v_mfma_f32_16x16x32_bf16 v[136:139], a[124:127], v[56:59], v[136:139]\n"
"s_add_u32 m0, s34, 9216\n"
"s_nop 0\n"
"buffer_load_dwordx4 v191, s[8:11], s32 offen lds\n"
"v_mfma_f32_16x16x32_bf16 v[136:139], a[136:139], v[64:67], v[136:139]\n"
"s_add_u32 s32, s32, s33\n"
"v_cvt_pk_bf16_f32 v132, v132, v133\n"
"v_cvt_pk_bf16_f32 v133, v134, v135\n"
"v_mfma_f32_16x16x32_bf16 v[140:143], a[80:83], a[240:243], 0\n"
"v_mfma_f32_16x16x32_bf16 v[140:143], a[92:95], a[244:247], v[140:143]\n"
"v_mfma_f32_16x16x32_bf16 v[140:143], a[104:107], a[248:251], v[140:143]\n"
"s_add_u32 m0, s34, 7168\n"
"s_nop 0\n"
"buffer_load_dwordx4 v190, s[8:11], s0 offen lds\n"
"v_mfma_f32_16x16x32_bf16 v[140:143], a[116:119], a[252:255], v[140:143]\n"
"v_cvt_pk_bf16_f32 v134, v136, v137\n"
"v_cvt_pk_bf16_f32 v135, v138, v139\n"
"v_mfma_f32_16x16x32_bf16 v[140:143], a[128:131], v[56:59], v[140:143]\n"
"ds_write_b128 v186, v[132:135] offset:47392\n"
"v_mfma_f32_16x16x32_bf16 v[140:143], a[140:143], v[64:67], v[140:143]\n"
"s_add_u32 m0, s34, 10240\n"
"v_mfma_f32_16x16x32_bf16 v[120:123], a[48:51], v[56:59], v[120:123]\n"
"buffer_load_dwordx4 v191, s[8:11], s0 offen lds\n"
"s_add_u32 s0, s0, s33\n"
"v_mfma_f32_16x16x32_bf16 v[120:123], a[60:63], v[64:67], v[120:123]\n"
"s_nop 2\n"
"v_cvt_pk_bf16_f32 v130, v140, v141\n"
"v_cvt_pk_bf16_f32 v131, v142, v143\n"
"ds_write_b128 v186, v[128:131] offset:47328\n"
"s_add_u32 m0, s34, 8192\n"
"s_nop 0\n"
"v_cvt_pk_bf16_f32 v120, v120, v121\n"
"buffer_load_dwordx4 v190, s[8:11], s1 offen lds\n"
"v_cvt_pk_bf16_f32 v121, v122, v123\n"
"v_cvt_pk_bf16_f32 v122, v124, v125\n"
"v_cvt_pk_bf16_f32 v123, v126, v127\n"
"ds_write_b128 v186, v[120:123] offset:47264\n"
"s_add_u32 m0, s34, 11264\n"
"s_waitcnt vmcnt(11)\n"
"buffer_load_dwordx4 v191, s[8:11], s1 offen lds\n"
"s_add_u32 s1, s1, s33\n"
"s_barrier\n"
"ds_read_b64_tr_b16 a[144:145], v68\n"
"ds_read_b64_tr_b16 a[146:147], v68 offset:1536\n"
"ds_read_b64_tr_b16 a[148:149], v68 offset:32\n"
"ds_read_b64_tr_b16 a[150:151], v68 offset:1568\n"
"ds_read_b64_tr_b16 a[152:153], v68 offset:64\n"
"ds_read_b64_tr_b16 a[154:155], v68 offset:1600\n"
"ds_read_b64_tr_b16 a[156:157], v68 offset:3072\n"
"ds_read_b64_tr_b16 a[158:159], v68 offset:4608\n"
"ds_read_b64_tr_b16 a[160:161], v68 offset:3104\n"
"buffer_load_dword v200, off, s[28:31], s39 sc1\n"
"ds_read_b64_tr_b16 a[162:163], v68 offset:4640\n"
"ds_read_b64_tr_b16 a[164:165], v68 offset:3136\n"
"ds_read_b64_tr_b16 a[166:167], v68 offset:4672\n"
"ds_read_b64_tr_b16 a[168:169], v68 offset:6752\n"
"ds_read_b64_tr_b16 a[170:171], v68 offset:8288\n"
"ds_read_b64_tr_b16 a[172:173], v68 offset:6784\n"
"ds_read_b64_tr_b16 a[174:175], v68 offset:8320\n"
"ds_read_b64_tr_b16 a[176:177], v68 offset:6816\n"
"ds_read_b64_tr_b16 a[178:179], v68 offset:8352\n"
"s_waitcnt lgkmcnt(15)\n"
"v_mfma_f32_16x16x32_bf16 v[204:207], a[144:147], v[32:35], 0\n"
"s_waitcnt lgkmcnt(14)\n"
"ds_read_b64_tr_b16 a[180:181], v68 offset:9824\n"
"ds_read_b64_tr_b16 a[182:183], v68 offset:11360\n"
"v_mfma_f32_16x16x32_bf16 v[208:211], a[148:151], v[32:35], 0\n"
"s_waitcnt lgkmcnt(14)\n"
"ds_read_b64_tr_b16 a[184:185], v68 offset:9856\n"
"ds_read_b64_tr_b16 a[186:187], v68 offset:11392\n"
"v_mfma_f32_16x16x32_bf16 v[212:215], a[152:155], v[32:35], 0\n"
"s_waitcnt lgkmcnt(14)\n"
"ds_read_b64_tr_b16 a[188:189], v68 offset:9888\n"
"ds_read_b64_tr_b16 a[190:191], v68 offset:11424\n"
"v_mfma_f32_16x16x32_bf16 v[204:207], a[156:159], v[36:39], v[204:207]\n"
"s_waitcnt lgkmcnt(14)\n"
"ds_read_b64_tr_b16 a[192:193], v69\n"
"ds_read_b64_tr_b16 a[194:195], v69 offset:1536\n"
"v_mfma_f32_16x16x32_bf16 v[208:211], a[160:163], v[36:39], v[208:211]\n"
"s_waitcnt lgkmcnt(14)\n"
"ds_read_b64_tr_b16 a[196:197], v69 offset:32\n"
"ds_read_b64_tr_b16 a[198:199], v69 offset:1568\n"
"v_mfma_f32_16x16x32_bf16 v[212:215], a[164:167], v[36:39], v[212:215]\n"
"s_waitcnt lgkmcnt(14)\n"
"ds_read_b64_tr_b16 a[200:201], v69 offset:64\n"
"ds_read_b64_tr_b16 a[202:203], v69 offset:1600\n"
"v_mfma_f32_16x16x32_bf16 v[204:207], a[168:171], v[40:43], v[204:207]\n"
"s_waitcnt lgkmcnt(14)\n"
"ds_read_b64_tr_b16 a[204:205], v69 offset:3072\n"
"ds_read_b64_tr_b16 a[206:207], v69 offset:4608\n"
"v_mfma_f32_16x16x32_bf16 v[208:211], a[172:175], v[40:43], v[208:211]\n"
"s_waitcnt lgkmcnt(14)\n"
"ds_read_b64_tr_b16 a[208:209], v69 offset:3104\n"
"ds_read_b64_tr_b16 a[210:211], v69 offset:4640\n"
"v_mfma_f32_16x16x32_bf16 v[212:215], a[176:179], v[40:43], v[212:215]\n"
"s_waitcnt lgkmcnt(14)\n"
"ds_read_b64_tr_b16 a[212:213], v69 offset:3136\n"
"ds_read_b64_tr_b16 a[214:215], v69 offset:4672\n"
"v_mfma_f32_16x16x32_bf16 v[204:207], a[180:183], v[44:47], v[204:207]\n"
"s_waitcnt lgkmcnt(14)\n"
"v_mfma_f32_16x16x32_bf16 v[208:211], a[184:187], v[44:47], v[208:211]\n"
"s_waitcnt lgkmcnt(12)\n"
"v_mfma_f32_16x16x32_bf16 v[212:215], a[188:191], v[44:47], v[212:215]\n"
"s_waitcnt lgkmcnt(10)\n"
"v_mfma_f32_16x16x32_bf16 v[204:207], a[192:195], v[48:51], v[204:207]\n"
"s_waitcnt lgkmcnt(8)\n"
"v_mfma_f32_16x16x32_bf16 v[208:211], a[196:199], v[48:51], v[208:211]\n"
"s_waitcnt lgkmcnt(6)\n"
"v_mfma_f32_16x16x32_bf16 v[212:215], a[200:203], v[48:51], v[212:215]\n"
"s_waitcnt lgkmcnt(4)\n"
"v_mfma_f32_16x16x32_bf16 v[204:207], a[204:207], v[52:55], v[204:207]\n"
"s_waitcnt lgkmcnt(2)\n"
"v_mfma_f32_16x16x32_bf16 v[208:211], a[208:211], v[52:55], v[208:211]\n"
"s_waitcnt lgkmcnt(0)\n"
"s_barrier\n"
"v_mfma_f32_16x16x32_bf16 v[212:215], a[212:215], v[52:55], v[212:215]\n"
"ds_read_b128 v[112:115], v178\n"
"ds_read_b64 v[82:83], v178 offset:64\n"
"ds_read_b128 v[120:123], v179\n"
"v_cvt_pk_bf16_f32 v144, v204, v205\n"
"v_cvt_pk_bf16_f32 v145, v206, v207\n"
"v_mfma_f32_16x16x32_bf16 v[204:207], a[144:147], a[216:219], 0\n"
"ds_read_b64 v[84:85], v179 offset:64\n"
"buffer_load_dword v237, v238, s[8:11], s78 offen\n"
"v_mfma_f32_16x16x32_bf16 v[204:207], a[156:159], a[220:223], v[204:207]\n"
"s_add_u32 s78, s78, s33\n"
"ds_read_b128 v[116:119], v178 offset:13504\n"
"v_cvt_pk_bf16_f32 v146, v208, v209\n"
"v_mfma_f32_16x16x32_bf16 v[204:207], a[168:171], a[224:227], v[204:207]\n"
"s_waitcnt vmcnt(12)\n"
"ds_read_b64 v[90:91], v178 offset:13568\n"
"v_cvt_pk_bf16_f32 v147, v210, v211\n"
"v_mfma_f32_16x16x32_bf16 v[204:207], a[180:183], a[228:231], v[204:207]\n"
".Lic0%=:\n"
"s_cbranch_execz .Lib0%=\n"
"ds_read_b128 v[124:127], v179 offset:13504\n"
"v_cvt_pk_bf16_f32 v158, v212, v213\n"
"v_mfma_f32_16x16x32_bf16 v[204:207], a[192:195], a[232:235], v[204:207]\n"
"ds_read_b64 v[92:93], v179 offset:13568\n"
"v_cvt_pk_bf16_f32 v159, v214, v215\n"
"v_mfma_f32_16x16x32_bf16 v[204:207], a[204:207], a[236:239], v[204:207]\n"
"ds_read_b128 v[128:131], v178 offset:27008\n"
"buffer_load_dword v237, v238, s[8:11], s78 offen\n"
"v_mfma_f32_16x16x32_bf16 v[208:211], a[148:151], a[216:219], 0\n"
"s_waitcnt lgkmcnt(7)\n"
"ds_read_b64 v[98:99], v178 offset:27072\n"
"v_subrev_u32 v178, s48, v178\n"
"v_mfma_f32_16x16x32_bf16 v[112:115], v[112:115], v[144:147], 0\n"
"s_add_u32 s78, s78, s33\n"
"ds_read_b128 v[136:139], v179 offset:27008\n"
"v_cvt_pk_bf16_f32 v148, v204, v205\n"
"v_mfma_f32_16x16x32_bf16 v[112:115], v[80:83], v[156:159], v[112:115]\n"
"s_waitcnt lgkmcnt(7)\n"
"ds_read_b64 v[100:101], v179 offset:27072\n"
"v_cvt_pk_bf16_f32 v149, v206, v207\n"
"v_mfma_f32_16x16x32_bf16 v[120:123], v[120:123], v[144:147], 0\n"
"ds_read_b128 v[132:135], v180\n"
"v_subrev_u32 v179, s48, v179\n"
"v_mfma_f32_16x16x32_bf16 v[120:123], v[84:87], v[158:161], v[120:123]\n"
"s_waitcnt vmcnt(11) lgkmcnt(7)\n"
"ds_read_b64 v[106:107], v180 offset:64\n"
"v_subrev_u32 v180, s48, v180\n"
"v_mfma_f32_16x16x32_bf16 v[116:119], v[116:119], v[144:147], 0\n"
"ds_read_b128 v[140:143], v181\n"
"v_mfma_f32_16x16x32_bf16 v[116:119], v[88:91], v[156:159], v[116:119]\n"
"s_waitcnt lgkmcnt(7)\n"
"ds_read_b64 v[108:109], v181 offset:64\n"
"v_subrev_u32 v181, s48, v181\n"
"v_mfma_f32_16x16x32_bf16 v[124:127], v[124:127], v[144:147], 0\n"
"ds_read_b128 v[32:35], v188\n"
"v_max_f32 v112, v112, v120\n"
"v_mfma_f32_16x16x32_bf16 v[124:127], v[92:95], v[158:161], v[124:127]\n"
"s_waitcnt lgkmcnt(7)\n"
"ds_read_b128 v[36:39], v189\n"
"v_max_f32 v113, v113, v121\n"
"v_mfma_f32_16x16x32_bf16 v[128:131], v[128:131], v[144:147], 0\n"
"ds_read_b128 v[40:43], v188 offset:1024\n"
"v_max_f32 v114, v114, v122\n"
"v_mfma_f32_16x16x32_bf16 v[128:131], v[96:99], v[156:159], v[128:131]\n"
"s_waitcnt lgkmcnt(7)\n"
"ds_read_b128 v[44:47], v189 offset:1024\n"
"v_max_f32 v115, v115, v123\n"
"v_mfma_f32_16x16x32_bf16 v[136:139], v[136:139], v[144:147], 0\n"
"ds_read_b64_tr_b16 v[224:225], v182 offset:128\n"
"v_max3_f32 v170, v112, v113, v114\n"
"v_mfma_f32_16x16x32_bf16 v[136:139], v[100:103], v[158:161], v[136:139]\n"
"s_waitcnt lgkmcnt(7)\n"
"ds_read_b64_tr_b16 v[226:227], v183 offset:128\n"
"v_max_f32 v116, v116, v124\n"
"v_mfma_f32_16x16x32_bf16 v[132:135], v[132:135], v[144:147], 0\n"
"ds_read_b64_tr_b16 v[228:229], v182 offset:896\n"
"v_max_f32 v117, v117, v125\n"
"v_mfma_f32_16x16x32_bf16 v[132:135], v[104:107], v[156:159], v[132:135]\n"
"s_waitcnt lgkmcnt(7)\n"
"ds_read_b64_tr_b16 v[230:231], v183 offset:896\n"
"v_max_f32 v118, v118, v126\n"
"v_mfma_f32_16x16x32_bf16 v[140:143], v[140:143], v[144:147], 0\n"
"s_waitcnt vmcnt(9)\n"
"ds_read_b128 v[48:51], v188 offset:2048\n"
"v_max_f32 v119, v119, v127\n"
"v_mfma_f32_16x16x32_bf16 v[140:143], v[108:111], v[158:161], v[140:143]\n"
"s_waitcnt lgkmcnt(7)\n"
"ds_read_b128 v[52:55], v189 offset:2048\n"
"v_max3_f32 v170, v170, v115, v116\n"
"v_mfma_f32_16x16x32_bf16 v[204:207], a[144:147], a[240:243], 0\n"
"s_add_u32 m0, s34, 0\n"
"ds_read_b64_tr_b16 v[244:245], v182 offset:136\n"
"buffer_load_dwordx4 v190, s[8:11], s32 offen lds\n"
"v_mfma_f32_16x16x32_bf16 v[204:207], a[156:159], a[244:247], v[204:207]\n"
"ds_read_b64_tr_b16 v[246:247], v183 offset:136\n"
"v_max3_f32 v170, v170, v117, v118\n"
"v_mfma_f32_16x16x32_bf16 v[204:207], a[168:171], a[248:251], v[204:207]\n"
"ds_read_b64_tr_b16 v[248:249], v182 offset:904\n"
"v_max_f32 v128, v128, v136\n"
"v_mfma_f32_16x16x32_bf16 v[204:207], a[180:183], a[252:255], v[204:207]\n"
"ds_read_b64_tr_b16 v[250:251], v183 offset:904\n"
"v_max_f32 v129, v129, v137\n"
"v_mfma_f32_16x16x32_bf16 v[204:207], a[192:195], v[56:59], v[204:207]\n"
"ds_read_b64_tr_b16 v[120:121], v182 offset:72\n"
"v_max_f32 v130, v130, v138\n"
"v_mfma_f32_16x16x32_bf16 v[204:207], a[204:207], v[64:67], v[204:207]\n"
"ds_read_b64_tr_b16 v[122:123], v183 offset:72\n"
"v_max_f32 v131, v131, v139\n"
"v_mfma_f32_16x16x32_bf16 v[208:211], a[160:163], a[220:223], v[208:211]\n"
"ds_read_b64_tr_b16 v[124:125], v182 offset:840\n"
"v_max3_f32 v170, v170, v119, v128\n"
"v_mfma_f32_16x16x32_bf16 v[208:211], a[172:175], a[224:227], v[208:211]\n"
"ds_read_b64_tr_b16 v[126:127], v183 offset:840\n"
"v_max3_f32 v170, v170, v129, v130\n"
"v_mfma_f32_16x16x32_bf16 v[208:211], a[184:187], a[228:231], v[208:211]\n"
"s_waitcnt lgkmcnt(8)\n"
"v_cvt_pk_bf16_f32 v152, v204, v205\n"
"v_cvt_pk_bf16_f32 v153, v206, v207\n"
"v_mfma_f32_16x16x32_bf16 v[208:211], a[196:199], a[232:235], v[208:211]\n"
"s_add_u32 m0, s34, 3072\n"
"ds_read_b64_tr_b16 v[216:217], v182 offset:27136\n"
"buffer_load_dwordx4 v191, s[8:11], s32 offen lds\n"
"v_mfma_f32_16x16x32_bf16 v[208:211], a[208:211], a[236:239], v[208:211]\n"
"s_add_u32 s32, s32, s33\n"
"ds_read_b64_tr_b16 v[218:219], v183 offset:27136\n"
"ds_read_b64_tr_b16 v[220:221], v182 offset:27904\n"
"v_mfma_f32_16x16x32_bf16 v[204:207], a[0:3], v[32:35], 0\n"
"ds_read_b64_tr_b16 v[222:223], v183 offset:27904\n"
"v_max_f32 v132, v132, v140\n"
"v_mfma_f32_16x16x32_bf16 v[204:207], a[12:15], v[36:39], v[204:207]\n"
"ds_read_b64_tr_b16 v[72:73], v182 offset:27144\n"
"v_cvt_pk_bf16_f32 v150, v208, v209\n"
"v_mfma_f32_16x16x32_bf16 v[204:207], a[24:27], v[40:43], v[204:207]\n"
"v_cvt_pk_bf16_f32 v151, v210, v211\n"
"v_max_f32 v133, v133, v141\n"
"v_mfma_f32_16x16x32_bf16 v[204:207], a[36:39], v[44:47], v[204:207]\n"
"ds_read_b64_tr_b16 v[74:75], v183 offset:27144\n"
"ds_read_b64_tr_b16 v[76:77], v182 offset:27912\n"
"v_mfma_f32_16x16x32_bf16 v[204:207], a[48:51], v[48:51], v[204:207]\n"
"ds_read_b64_tr_b16 v[78:79], v183 offset:27912\n"
"v_max_f32 v134, v134, v142\n"
"v_mfma_f32_16x16x32_bf16 v[204:207], a[60:63], v[52:55], v[204:207]\n"
"ds_read_b64_tr_b16 v[136:137], v182 offset:27080\n"
"v_max_f32 v135, v135, v143\n"
"v_mfma_f32_16x16x32_bf16 v[208:211], a[148:151], a[240:243], 0\n"
"s_add_u32 m0, s34, 1024\n"
"ds_read_b64_tr_b16 v[138:139], v183 offset:27080\n"
"buffer_load_dwordx4 v190, s[8:11], s0 offen lds\n"
"v_mfma_f32_16x16x32_bf16 v[208:211], a[160:163], a[244:247], v[208:211]\n"
"ds_read_b64_tr_b16 v[140:141], v182 offset:27848\n"
"v_cvt_pk_bf16_f32 v204, v204, v205\n"
"v_mfma_f32_16x16x32_bf16 v[208:211], a[172:175], a[248:251], v[208:211]\n"
"v_cvt_pk_bf16_f32 v205, v206, v207\n"
"v_max3_f32 v170, v170, v131, v132\n"
"v_mfma_f32_16x16x32_bf16 v[208:211], a[184:187], a[252:255], v[208:211]\n"
"ds_read_b64_tr_b16 v[142:143], v183 offset:27848\n"
"v_max3_f32 v170, v170, v133, v134\n"
"v_mfma_f32_16x16x32_bf16 v[208:211], a[196:199], v[56:59], v[208:211]\n"
"ds_read_b64 v[80:81], v20 offset:272\n"
"ds_read_b64 v[86:87], v20 offset:336\n"
"v_mfma_f32_16x16x32_bf16 v[208:211], a[208:211], v[64:67], v[208:211]\n"
"ds_read_b64 v[88:89], v20 offset:544\n"
"ds_read_b64 v[94:95], v20 offset:608\n"
"v_mfma_f32_16x16x32_bf16 v[212:215], a[152:155], a[216:219], 0\n"
"v_mov_b64 v[98:99], 0\n"
"v_mov_b64 v[100:101], 0\n"
"v_mfma_f32_16x16x32_bf16 v[212:215], a[164:167], a[220:223], v[212:215]\n"
"ds_read_b64 v[96:97], v20 offset:816\n"
"v_mov_b64 v[106:107], 0\n"
"v_mfma_f32_16x16x32_bf16 v[212:215], a[176:179], a[224:227], v[212:215]\n"
"s_add_u32 m0, s34, 4096\n"
"v_cvt_pk_bf16_f32 v154, v208, v209\n"
"v_cvt_pk_bf16_f32 v155, v210, v211\n"
"v_mfma_f32_16x16x32_bf16 v[212:215], a[188:191], a[228:231], v[212:215]\n"
"buffer_load_dwordx4 v191, s[8:11], s0 offen lds\n"
"v_mov_b64 v[108:109], 0\n"
"v_mfma_f32_16x16x32_bf16 v[212:215], a[200:203], a[232:235], v[212:215]\n"
"s_add_u32 s0, s0, s33\n"
"v_max_f32 v171, v170, v135\n"
"v_max_f32 v170, v170, v135\n"
"v_mfma_f32_16x16x32_bf16 v[212:215], a[212:215], a[236:239], v[212:215]\n"
"s_waitcnt lgkmcnt(3)\n"
"ds_read_b64 v[102:103], v20 offset:880\n"
"ds_read_b64 v[104:105], v20 offset:1088\n"
"v_mfma_f32_16x16x32_bf16 v[208:211], a[4:7], v[32:35], 0\n"
"s_waitcnt lgkmcnt(3)\n"
"v_permlane16_swap_b32 v170, v171\n"
"v_mfma_f32_16x16x32_bf16 v[208:211], a[16:19], v[36:39], v[208:211]\n"
"v_max_f32 v170, v170, v171\n"
"v_cvt_pk_bf16_f32 v162, v212, v213\n"
"v_mfma_f32_16x16x32_bf16 v[208:211], a[28:31], v[40:43], v[208:211]\n"
"s_waitcnt lgkmcnt(2)\n"
"v_cvt_pk_bf16_f32 v163, v214, v215\n"
"v_mov_b32 v171, v170\n"
"v_mfma_f32_16x16x32_bf16 v[208:211], a[40:43], v[44:47], v[208:211]\n"
"s_nop 0\n"
"v_permlane32_swap_b32 v170, v171\n"
"v_max_f32 v170, v170, v171\n"
"v_mul_f32 v170, 0xbe553b95, v170\n"
"v_pk_fma_f32 v[112:113], v[112:113], s[70:71], v[170:171] op_sel_hi:[1,0,0]\n"
"v_pk_fma_f32 v[114:115], v[114:115], s[70:71], v[170:171] op_sel_hi:[1,0,0]\n"
"v_pk_fma_f32 v[116:117], v[116:117], s[70:71], v[170:171] op_sel_hi:[1,0,0]\n"
"v_pk_fma_f32 v[118:119], v[118:119], s[70:71], v[170:171] op_sel_hi:[1,0,0]\n"
"v_pk_fma_f32 v[128:129], v[128:129], s[70:71], v[170:171] op_sel_hi:[1,0,0]\n"
"v_pk_fma_f32 v[130:131], v[130:131], s[70:71], v[170:171] op_sel_hi:[1,0,0]\n"
"v_pk_fma_f32 v[132:133], v[132:133], s[70:71], v[170:171] op_sel_hi:[1,0,0]\n"
"v_pk_fma_f32 v[134:135], v[134:135], s[70:71], v[170:171] op_sel_hi:[1,0,0]\n"
"v_mfma_f32_16x16x32_bf16 v[208:211], a[52:55], v[48:51], v[208:211]\n"
"s_waitcnt lgkmcnt(0)\n"
"ds_read_b64 v[110:111], v20 offset:1152\n"
"v_exp_f32 v112, v112\n"
"v_mfma_f32_16x16x32_bf16 v[208:211], a[64:67], v[52:55], v[208:211]\n"
"s_add_u32 m0, s34, 2048\n"
"v_exp_f32 v113, v113\n"
"v_mfma_f32_16x16x32_bf16 v[212:215], a[152:155], a[240:243], 0\n"
"buffer_load_dwordx4 v190, s[8:11], s1 offen lds\n"
"v_exp_f32 v114, v114\n"
"v_mfma_f32_16x16x32_bf16 v[212:215], a[164:167], a[244:247], v[212:215]\n"
"v_exp_f32 v115, v115\n"
"v_mfma_f32_16x16x32_bf16 v[212:215], a[176:179], a[248:251], v[212:215]\n"
"v_cvt_pk_bf16_f32 v206, v208, v209\n"
"v_cvt_pk_bf16_f32 v207, v210, v211\n"
"v_mfma_f32_16x16x32_bf16 v[212:215], a[188:191], a[252:255], v[212:215]\n"
"ds_write_b128 v186, v[204:207] offset:60768\n"
"v_mfma_f32_16x16x32_bf16 v[212:215], a[200:203], v[56:59], v[212:215]\n"
"s_waitcnt vmcnt(7) lgkmcnt(1)\n"
"v_and_b32 v200, 0xffffff00, v200\n"
"v_add_u32 v200, 0x100, v200\n"
"v_mfma_f32_16x16x32_bf16 v[212:215], a[212:215], v[64:67], v[212:215]\n"
"s_mov_b64 exec, 1\n"
"buffer_store_dword v200, off, s[28:31], s39 offset:128 sc1\n"
"s_mov_b64 exec, -1\n"
"v_mfma_f32_16x16x32_bf16 v[208:211], a[80:83], v[32:35], 0\n"
"s_add_u32 m0, s34, 5120\n"
"v_sub_u32 v201, 0, v200\n"
"s_nop 0\n"
"v_readfirstlane_b32 s50, v201\n"
"v_mfma_f32_16x16x32_bf16 v[208:211], a[92:95], v[36:39], v[208:211]\n"
"buffer_load_dwordx4 v191, s[8:11], s1 offen lds\n"
"v_cvt_pk_bf16_f32 v166, v212, v213\n"
"v_mfma_f32_16x16x32_bf16 v[208:211], a[104:107], v[40:43], v[208:211]\n"
"s_add_u32 s1, s1, s33\n"
"v_cvt_pk_bf16_f32 v167, v214, v215\n"
"v_cvt_pk_bf16_f32 v82, v112, v114\n"
"v_mfma_f32_16x16x32_bf16 v[208:211], a[116:119], v[44:47], v[208:211]\n"
"v_cvt_pk_bf16_f32 v83, v113, v115\n"
"v_exp_f32 v116, v116\n"
"v_mfma_f32_16x16x32_bf16 v[208:211], a[128:131], v[48:51], v[208:211]\n"
"ds_read_b128 v[112:115], v178 offset:13504\n"
"v_exp_f32 v117, v117\n"
"v_mfma_f32_16x16x32_bf16 v[208:211], a[140:143], v[52:55], v[208:211]\n"
"v_exp_f32 v118, v118\n"
"v_mfma_f32_16x16x32_bf16 v[212:215], a[8:11], v[32:35], 0\n"
"v_exp_f32 v119, v119\n"
"v_mfma_f32_16x16x32_bf16 v[212:215], a[20:23], v[36:39], v[212:215]\n"
"v_cvt_pk_bf16_f32 v84, v116, v118\n"
"v_cvt_pk_bf16_f32 v85, v117, v119\n"
"v_mfma_f32_16x16x32_bf16 v[212:215], a[32:35], v[40:43], v[212:215]\n"
"ds_read_b128 v[116:119], v178 offset:27008\n"
"v_exp_f32 v128, v128\n"
"v_mfma_f32_16x16x32_bf16 v[212:215], a[44:47], v[44:47], v[212:215]\n"
"v_exp_f32 v129, v129\n"
"v_mfma_f32_16x16x32_bf16 v[212:215], a[56:59], v[48:51], v[212:215]\n"
"v_exp_f32 v130, v130\n"
"v_mfma_f32_16x16x32_bf16 v[212:215], a[68:71], v[52:55], v[212:215]\n"
"v_exp_f32 v131, v131\n"
"v_mfma_f32_16x16x32_bf16 v[204:207], a[72:75], v[32:35], 0\n"
"v_cvt_pk_bf16_f32 v90, v128, v130\n"
"v_cvt_pk_bf16_f32 v91, v129, v131\n"
"v_mfma_f32_16x16x32_bf16 v[204:207], a[84:87], v[36:39], v[204:207]\n"
"ds_read_b128 v[128:131], v180\n"
"v_subrev_u32 v182, s48, v182\n"
"v_mfma_f32_16x16x32_bf16 v[204:207], a[96:99], v[40:43], v[204:207]\n"
"v_cvt_pk_bf16_f32 v212, v212, v213\n"
"v_cvt_pk_bf16_f32 v213, v214, v215\n"
"v_mfma_f32_16x16x32_bf16 v[204:207], a[108:111], v[44:47], v[204:207]\n"
"v_cvt_pk_bf16_f32 v214, v208, v209\n"
"v_cvt_pk_bf16_f32 v215, v210, v211\n"
"v_mfma_f32_16x16x32_bf16 v[204:207], a[120:123], v[48:51], v[204:207]\n"
"ds_write_b128 v186, v[212:215] offset:60832\n"
"v_mfma_f32_16x16x32_bf16 v[204:207], a[132:135], v[52:55], v[204:207]\n"
"v_exp_f32 v132, v132\n"
"v_mfma_f32_16x16x32_bf16 v[208:211], a[76:79], v[32:35], 0\n"
"v_exp_f32 v133, v133\n"
"v_mfma_f32_16x16x32_bf16 v[208:211], a[88:91], v[36:39], v[208:211]\n"
"v_exp_f32 v134, v134\n"
"v_mfma_f32_16x16x32_bf16 v[208:211], a[100:103], v[40:43], v[208:211]\n"
"v_exp_f32 v135, v135\n"
"v_mfma_f32_16x16x32_bf16 v[208:211], a[112:115], v[44:47], v[208:211]\n"
"v_cvt_pk_bf16_f32 v204, v204, v205\n"
"v_cvt_pk_bf16_f32 v205, v206, v207\n"
"v_mfma_f32_16x16x32_bf16 v[208:211], a[124:127], v[48:51], v[208:211]\n"
"v_cvt_pk_bf16_f32 v92, v132, v134\n"
"v_cvt_pk_bf16_f32 v93, v133, v135\n"
"v_mfma_f32_16x16x32_bf16 v[208:211], a[136:139], v[52:55], v[208:211]\n"
"ds_read_b64_tr_b16 a[240:241], v182 offset:13632\n"
"ds_read_b64_tr_b16 a[244:245], v182 offset:14400\n"
"v_mfma_f32_16x16x32_bf16 v[212:215], a[144:147], v[32:35], 0\n"
"v_subrev_u32 v183, s48, v183\n"
"v_bfi_b32 v80, v4, v80, v26\n"
"v_mfma_f32_16x16x32_bf16 v[212:215], a[156:159], v[36:39], v[212:215]\n"
"ds_read_b64_tr_b16 a[242:243], v183 offset:13632\n"
"ds_read_b64_tr_b16 a[246:247], v183 offset:14400\n"
"v_mfma_f32_16x16x32_bf16 v[212:215], a[168:171], v[40:43], v[212:215]\n"
"v_cvt_pk_bf16_f32 v206, v208, v209\n"
"v_cvt_pk_bf16_f32 v207, v210, v211\n"
"v_mfma_f32_16x16x32_bf16 v[212:215], a[180:183], v[44:47], v[212:215]\n"
"ds_write_b128 v186, v[204:207] offset:60896\n"
"v_mfma_f32_16x16x32_bf16 v[212:215], a[192:195], v[48:51], v[212:215]\n"
"v_bfi_b32 v81, v5, v81, v26\n"
"v_bfi_b32 v86, v6, v86, v26\n"
"v_mfma_f32_16x16x32_bf16 v[212:215], a[204:207], v[52:55], v[212:215]\n"
"v_bfi_b32 v87, v7, v87, v26\n"
"v_bfi_b32 v88, v0, v88, v26\n"
"v_mfma_f32_16x16x32_bf16 v[208:211], a[148:151], v[32:35], 0\n"
"v_bfi_b32 v89, v1, v89, v26\n"
"v_bfi_b32 v94, v2, v94, v26\n"
"v_mfma_f32_16x16x32_bf16 v[208:211], a[160:163], v[36:39], v[208:211]\n"
"v_bfi_b32 v95, v3, v95, v26\n"
"v_bfi_b32 v96, v0, v96, v26\n"
"v_mfma_f32_16x16x32_bf16 v[208:211], a[172:175], v[40:43], v[208:211]\n"
"v_cvt_pk_bf16_f32 v144, v212, v213\n"
"v_cvt_pk_bf16_f32 v145, v214, v215\n"
"v_mfma_f32_16x16x32_bf16 v[208:211], a[184:187], v[44:47], v[208:211]\n"
"v_bfi_b32 v97, v1, v97, v26\n"
"v_bfi_b32 v102, v2, v102, v26\n"
"v_mfma_f32_16x16x32_bf16 v[208:211], a[196:199], v[48:51], v[208:211]\n"
"s_waitcnt lgkmcnt(0)\n"
"v_bfi_b32 v103, v3, v103, v26\n"
"v_bfi_b32 v104, v8, v104, v26\n"
"v_mfma_f32_16x16x32_bf16 v[208:211], a[208:211], v[52:55], v[208:211]\n"
"s_barrier\n"
"v_smfmac_f32_16x16x64_bf16 v[252:255], v[82:85], v[224:231], v63\n"
"s_add_u32 m0, s79, 130176\n"
"s_nop 0\n"
"buffer_load_dwordx4 v192, s[16:19], s43 offen lds\n"
"v_smfmac_f32_16x16x64_bf16 v[252:255], v[90:93], v[216:223], v63\n"
"ds_read_b128 v[132:135], v180 offset:13504\n"
"v_smfmac_f32_16x16x64_bf16 v[98:101], v[82:85], v[244:251], v63\n"
"ds_read_b64_tr_b16 v[224:225], v182 offset:40640\n"
"v_smfmac_f32_16x16x64_bf16 v[98:101], v[90:93], v[72:79], v63\n"
"v_cvt_pk_bf16_f32 v146, v208, v209\n"
"v_mfma_f32_16x16x32_bf16 v[204:207], a[152:155], v[32:35], 0\n"
"ds_read_b128 v[32:35], v188 offset:6144\n"
"v_cvt_pk_bf16_f32 v147, v210, v211\n"
"v_mfma_f32_16x16x32_bf16 v[204:207], a[164:167], v[36:39], v[204:207]\n"
"ds_read_b128 v[36:39], v189 offset:6144\n"
"buffer_load_dword v237, v238, s[8:11], s78 offen\n"
"v_mfma_f32_16x16x32_bf16 v[204:207], a[176:179], v[40:43], v[204:207]\n"
"s_add_u32 s78, s78, s33\n"
"ds_read_b128 v[40:43], v188 offset:7168\n"
"v_cvt_pk_bf16_f32 v252, v252, v253\n"
"v_mfma_f32_16x16x32_bf16 v[204:207], a[188:191], v[44:47], v[204:207]\n"
"ds_read_b128 v[44:47], v189 offset:7168\n"
"v_cvt_pk_bf16_f32 v253, v254, v255\n"
"v_mfma_f32_16x16x32_bf16 v[204:207], a[200:203], v[48:51], v[204:207]\n"
"s_add_u32 m0, s79, 130176\n"
"ds_read_b128 v[48:51], v188 offset:8192\n"
"buffer_load_dwordx4 v192, s[16:19], s43 offen offset:1024 lds\n"
"v_mfma_f32_16x16x32_bf16 v[204:207], a[212:215], v[52:55], v[204:207]\n"
"ds_read_b128 v[52:55], v189 offset:8192\n"
"v_cvt_pk_bf16_f32 v98, v98, v99\n"
"v_smfmac_f32_16x16x64_bf16 v[106:109], v[82:85], v[120:127], v63\n"
"ds_read_b128 v[120:123], v179 offset:13504\n"
"v_smfmac_f32_16x16x64_bf16 v[106:109], v[90:93], v[136:143], v63\n"
"s_waitcnt lgkmcnt(6)\n"
"ds_read_b128 v[124:127], v179 offset:27008\n"
"v_mfma_f32_16x16x32_bf16 v[82:85], v[172:175], v[82:85], 0\n"
"ds_read_b128 v[136:139], v181\n"
"ds_read_b128 v[140:143], v181 offset:13504\n"
"v_mfma_f32_16x16x32_bf16 v[90:93], v[172:175], v[90:93], v[82:85]\n"
"v_cvt_pk_bf16_f32 v158, v204, v205\n"
"v_cvt_pk_bf16_f32 v159, v206, v207\n"
"v_mfma_f32_16x16x32_bf16 v[212:215], a[0:3], v[32:35], 0\n"
"s_waitcnt lgkmcnt(8)\n"
"v_cvt_pk_bf16_f32 v99, v100, v101\n"
"v_cvt_pk_bf16_f32 v106, v106, v107\n"
"v_mfma_f32_16x16x32_bf16 v[212:215], a[12:15], v[36:39], v[212:215]\n"
"s_waitcnt lgkmcnt(7)\n"
"ds_read_b64 v[82:83], v178 offset:13568\n"
"ds_read_b64 v[84:85], v179 offset:13568\n"
"v_mfma_f32_16x16x32_bf16 v[212:215], a[24:27], v[40:43], v[212:215]\n"
"s_waitcnt lgkmcnt(6)\n"
"v_rcp_f32 v171, v90\n"
"v_mfma_f32_16x16x32_bf16 v[212:215], a[36:39], v[44:47], v[212:215]\n"
"s_add_u32 m0, s79, 130176\n"
"ds_read_b64 v[90:91], v178 offset:27072\n"
"buffer_load_dwordx4 v192, s[16:19], s43 offen offset:2048 lds\n"
"v_mfma_f32_16x16x32_bf16 v[212:215], a[48:51], v[48:51], v[212:215]\n"
"s_add_u32 s43, s79, 18432\n"
"v_cvt_pk_bf16_f32 v171, v171, v171\n"
"v_and_b32 v92, v171, v176\n"
"v_mfma_f32_16x16x32_bf16 v[212:215], a[60:63], v[52:55], v[212:215]\n"
"v_and_b32 v93, v171, v177\n"
"v_cvt_pk_bf16_f32 v107, v108, v109\n"
"v_mfma_f32_16x16x32_bf16 v[208:211], a[4:7], v[32:35], 0\n"
"ds_read_b64_tr_b16 v[226:227], v183 offset:40640\n"
"buffer_load_dword v237, v238, s[8:11], s78 offen\n"
"v_mfma_f32_16x16x32_bf16 v[208:211], a[16:19], v[36:39], v[208:211]\n"
"s_add_u32 s78, s78, s33\n"
"ds_read_b64_tr_b16 v[228:229], v182 offset:41408\n"
"v_cvt_pk_bf16_f32 v212, v212, v213\n"
"v_mfma_f32_16x16x32_bf16 v[208:211], a[28:31], v[40:43], v[208:211]\n"
"s_waitcnt lgkmcnt(4)\n"
"v_cvt_pk_bf16_f32 v213, v214, v215\n"
"v_bfi_b32 v105, v9, v105, v26\n"
"v_mfma_f32_16x16x32_bf16 v[208:211], a[40:43], v[44:47], v[208:211]\n"
"s_waitcnt lgkmcnt(3)\n"
"ds_read_b64_tr_b16 v[230:231], v183 offset:41408\n"
"ds_read_b64_tr_b16 v[244:245], v182 offset:40648\n"
"v_mfma_f32_16x16x32_bf16 v[208:211], a[52:55], v[48:51], v[208:211]\n"
"s_waitcnt lgkmcnt(4)\n"
"ds_read_b64_tr_b16 v[246:247], v183 offset:40648\n"
"v_bfi_b32 v110, v10, v110, v26\n"
"v_mfma_f32_16x16x32_bf16 v[208:211], a[64:67], v[52:55], v[208:211]\n"
"s_add_u32 m0, s34, 6144\n"
"ds_read_b64_tr_b16 v[248:249], v182 offset:41416\n"
"buffer_load_dwordx4 v190, s[8:11], s32 offen lds\n"
"v_mfma_f32_16x16x16_bf16 v[252:255], v[252:253], v[92:93], 0\n"
"ds_read_b64_tr_b16 v[250:251], v183 offset:41416\n"
"v_bfi_b32 v111, v11, v111, v26\n"
"v_mfma_f32_16x16x16_bf16 v[98:101], v[98:99], v[92:93], 0\n"
"ds_read_b64_tr_b16 a[248:249], v182 offset:40584\n"
"v_cvt_pk_bf16_f32 v214, v208, v209\n"
"v_mfma_f32_16x16x16_bf16 v[106:109], v[106:107], v[92:93], 0\n"
"ds_read_b64 v[92:93], v179 offset:27072\n"
"v_cvt_pk_bf16_f32 v215, v210, v211\n"
"v_mfma_f32_16x16x32_bf16 v[112:115], v[112:115], v[148:151], 0\n"
"ds_write_b128 v187, v[212:215]\n"
"v_mfma_f32_16x16x32_bf16 v[112:115], v[80:83], v[160:163], v[112:115]\n"
"v_cvt_pk_bf16_f32 v252, v252, v253\n"
"v_cvt_pk_bf16_f32 v253, v254, v255\n"
"v_mfma_f32_16x16x32_bf16 v[120:123], v[120:123], v[148:151], 0\n"
"v_cvt_pk_bf16_f32 v254, v98, v99\n"
"v_cvt_pk_bf16_f32 v255, v100, v101\n"
"v_mfma_f32_16x16x32_bf16 v[120:123], v[84:87], v[162:165], v[120:123]\n"
"s_add_u32 m0, s34, 9216\n"
"buffer_store_dwordx4 v[252:255], v192, s[28:31], s37 offen sc1\n"
"v_mfma_f32_16x16x32_bf16 v[116:119], v[116:119], v[148:151], 0\n"
"ds_read_b64 v[98:99], v180 offset:64\n"
"ds_read_b64 v[100:101], v181 offset:64\n"
"v_mfma_f32_16x16x32_bf16 v[116:119], v[88:91], v[160:163], v[116:119]\n"
"v_cvt_pk_bf16_f32 v106, v106, v107\n"
"v_cvt_pk_bf16_f32 v107, v108, v109\n"
"v_mfma_f32_16x16x32_bf16 v[204:207], a[8:11], v[32:35], 0\n"
"s_waitcnt lgkmcnt(3)\n"
"buffer_load_dwordx4 v191, s[8:11], s32 offen lds\n"
"ds_read_b64 v[108:109], v181 offset:13568\n"
"v_mfma_f32_16x16x32_bf16 v[204:207], a[20:23], v[36:39], v[204:207]\n"
"s_add_u32 s32, s32, s33\n"
"ds_read_b64_tr_b16 a[250:251], v183 offset:40584\n"
"ds_read_b64_tr_b16 a[252:253], v182 offset:41352\n"
"v_mfma_f32_16x16x32_bf16 v[204:207], a[32:35], v[40:43], v[204:207]\n"
"ds_read_b64_tr_b16 a[254:255], v183 offset:41352\n"
"v_max_f32 v112, v112, v120\n"
"v_mfma_f32_16x16x32_bf16 v[204:207], a[44:47], v[44:47], v[204:207]\n"
"v_max_f32 v113, v113, v121\n"
"v_max_f32 v114, v114, v122\n"
"v_mfma_f32_16x16x32_bf16 v[204:207], a[56:59], v[48:51], v[204:207]\n"
"buffer_store_dwordx2 v[106:107], v193, s[28:31], s37 offen offset:1024 sc1\n"
"v_mfma_f32_16x16x32_bf16 v[204:207], a[68:71], v[52:55], v[204:207]\n"
"s_waitcnt lgkmcnt(5)\n"
"ds_read_b64 v[106:107], v180 offset:13568\n"
"ds_read_b64_tr_b16 v[120:121], v182 offset:13640\n"
"v_mfma_f32_16x16x32_bf16 v[124:127], v[124:127], v[148:151], 0\n"
"s_waitcnt lgkmcnt(6)\n"
"v_max_f32 v115, v115, v123\n"
"v_max3_f32 v170, v112, v113, v114\n"
"v_mfma_f32_16x16x32_bf16 v[124:127], v[92:95], v[162:165], v[124:127]\n"
"s_waitcnt lgkmcnt(5)\n"
"ds_read_b64_tr_b16 v[122:123], v183 offset:13640\n"
"v_cvt_pk_bf16_f32 v204, v204, v205\n"
"v_mfma_f32_16x16x32_bf16 v[128:131], v[128:131], v[148:151], 0\n"
"s_add_u32 m0, s34, 7168\n"
"v_cvt_pk_bf16_f32 v205, v206, v207\n"
"v_mov_b64 v[252:253], 0\n"
"v_mfma_f32_16x16x32_bf16 v[128:131], v[96:99], v[160:163], v[128:131]\n"
"s_add_u32 s37, s37, 1572864\n"
"buffer_load_dwordx4 v190, s[8:11], s0 offen lds\n"
"v_max_f32 v116, v116, v124\n"
"v_mfma_f32_16x16x32_bf16 v[136:139], v[136:139], v[148:151], 0\n"
"v_max_f32 v117, v117, v125\n"
"v_max_f32 v118, v118, v126\n"
"v_mfma_f32_16x16x32_bf16 v[136:139], v[100:103], v[162:165], v[136:139]\n"
"ds_read_b64_tr_b16 v[124:125], v182 offset:14408\n"
"v_max_f32 v119, v119, v127\n"
"v_mfma_f32_16x16x32_bf16 v[208:211], a[80:83], v[32:35], 0\n"
"s_waitcnt lgkmcnt(3)\n"
"ds_read_b64_tr_b16 v[126:127], v183 offset:14408\n"
"v_max3_f32 v170, v170, v115, v116\n"
"v_mfma_f32_16x16x32_bf16 v[208:211], a[92:95], v[36:39], v[208:211]\n"
"v_max3_f32 v170, v170, v117, v118\n"
"v_max_f32 v128, v128, v136\n"
"v_mfma_f32_16x16x32_bf16 v[208:211], a[104:107], v[40:43], v[208:211]\n"
"v_max_f32 v129, v129, v137\n"
"v_max_f32 v130, v130, v138\n"
"v_mfma_f32_16x16x32_bf16 v[208:211], a[116:119], v[44:47], v[208:211]\n"
"ds_read_b64_tr_b16 v[136:137], v182 offset:13576\n"
"v_max_f32 v131, v131, v139\n"
"v_mfma_f32_16x16x32_bf16 v[208:211], a[128:131], v[48:51], v[208:211]\n"
"ds_read_b64_tr_b16 v[138:139], v183 offset:13576\n"
"v_max3_f32 v170, v170, v119, v128\n"
"v_mfma_f32_16x16x32_bf16 v[208:211], a[140:143], v[52:55], v[208:211]\n"
"s_add_u32 m0, s34, 10240\n"
"v_max3_f32 v170, v170, v129, v130\n"
"v_mov_b64 v[254:255], 0\n"
"v_mfma_f32_16x16x32_bf16 v[132:135], v[132:135], v[148:151], 0\n"
"buffer_load_dwordx4 v191, s[8:11], s0 offen lds\n"
"ds_read_b64 v[80:81], v20 offset:272\n"
"v_mfma_f32_16x16x32_bf16 v[132:135], v[104:107], v[160:163], v[132:135]\n"
"s_add_u32 s0, s0, s33\n"
"v_cvt_pk_bf16_f32 v206, v208, v209\n"
"v_cvt_pk_bf16_f32 v207, v210, v211\n"
"v_mfma_f32_16x16x32_bf16 v[140:143], v[140:143], v[148:151], 0\n"
"ds_write_b128 v187, v[204:207] offset:64\n"
"v_mfma_f32_16x16x32_bf16 v[140:143], v[108:111], v[162:165], v[140:143]\n"
"v_mov_b64 v[98:99], 0\n"
"v_mov_b64 v[100:101], 0\n"
"v_mfma_f32_16x16x32_bf16 v[212:215], a[72:75], v[32:35], 0\n"
"v_mov_b64 v[106:107], 0\n"
"v_mov_b64 v[108:109], 0\n"
"v_mfma_f32_16x16x32_bf16 v[212:215], a[84:87], v[36:39], v[212:215]\n"
"ds_read_b64 v[86:87], v20 offset:336\n"
"ds_read_b64 v[104:105], v20 offset:1088\n"
"v_mfma_f32_16x16x32_bf16 v[212:215], a[96:99], v[40:43], v[212:215]\n"
"s_waitcnt lgkmcnt(3)\n"
"v_max_f32 v132, v132, v140\n"
"v_max_f32 v133, v133, v141\n"
"v_mfma_f32_16x16x32_bf16 v[212:215], a[108:111], v[44:47], v[212:215]\n"
"s_add_u32 m0, s34, 8192\n"
"ds_read_b64_tr_b16 v[140:141], v182 offset:14344\n"
"buffer_load_dwordx4 v190, s[8:11], s1 offen lds\n"
"v_mfma_f32_16x16x32_bf16 v[212:215], a[120:123], v[48:51], v[212:215]\n"
"v_max_f32 v134, v134, v142\n"
"v_max_f32 v135, v135, v143\n"
"v_mfma_f32_16x16x32_bf16 v[212:215], a[132:135], v[52:55], v[212:215]\n"
"ds_read_b64_tr_b16 v[142:143], v183 offset:14344\n"
"v_max3_f32 v170, v170, v131, v132\n"
"v_mfma_f32_16x16x32_bf16 v[208:211], a[76:79], v[32:35], 0\n"
"v_max3_f32 v170, v170, v133, v134\n"
"v_max_f32 v171, v170, v135\n"
"v_mfma_f32_16x16x32_bf16 v[208:211], a[88:91], v[36:39], v[208:211]\n"
"ds_read_b64 v[110:111], v20 offset:1152\n"
"v_max_f32 v170, v170, v135\n"
"v_mfma_f32_16x16x32_bf16 v[208:211], a[100:103], v[40:43], v[208:211]\n"
"s_waitcnt lgkmcnt(3)\n"
"v_cvt_pk_bf16_f32 v212, v212, v213\n"
"v_cvt_pk_bf16_f32 v213, v214, v215\n"
"v_mfma_f32_16x16x32_bf16 v[208:211], a[112:115], v[44:47], v[208:211]\n"
"v_permlane16_swap_b32 v170, v171\n"
"v_mfma_f32_16x16x32_bf16 v[208:211], a[124:127], v[48:51], v[208:211]\n"
"v_max_f32 v170, v170, v171\n"
"v_mov_b32 v171, v170\n"
"v_mfma_f32_16x16x32_bf16 v[208:211], a[136:139], v[52:55], v[208:211]\n"
"s_add_u32 m0, s34, 11264\n"
"v_permlane32_swap_b32 v170, v171\n"
"v_max_f32 v170, v170, v171\n"
"v_mul_f32 v170, 0xbe553b95, v170\n"
"v_pk_fma_f32 v[112:113], v[112:113], s[70:71], v[170:171] op_sel_hi:[1,0,0]\n"
"v_pk_fma_f32 v[114:115], v[114:115], s[70:71], v[170:171] op_sel_hi:[1,0,0]\n"
"v_pk_fma_f32 v[116:117], v[116:117], s[70:71], v[170:171] op_sel_hi:[1,0,0]\n"
"v_pk_fma_f32 v[118:119], v[118:119], s[70:71], v[170:171] op_sel_hi:[1,0,0]\n"
"v_pk_fma_f32 v[128:129], v[128:129], s[70:71], v[170:171] op_sel_hi:[1,0,0]\n"
"v_pk_fma_f32 v[130:131], v[130:131], s[70:71], v[170:171] op_sel_hi:[1,0,0]\n"
"v_pk_fma_f32 v[132:133], v[132:133], s[70:71], v[170:171] op_sel_hi:[1,0,0]\n"
"v_pk_fma_f32 v[134:135], v[134:135], s[70:71], v[170:171] op_sel_hi:[1,0,0]\n"
"v_mfma_f32_16x16x32_bf16 v[204:207], a[144:147], v[32:35], 0\n"
"s_waitcnt lgkmcnt(0)\n"
"buffer_load_dwordx4 v191, s[8:11], s1 offen lds\n"
"v_cvt_pk_bf16_f32 v214, v208, v209\n"
"v_mfma_f32_16x16x32_bf16 v[204:207], a[156:159], v[36:39], v[204:207]\n"
"s_add_u32 s1, s1, s33\n"
"v_cvt_pk_bf16_f32 v215, v210, v211\n"
"v_exp_f32 v112, v112\n"
"v_mfma_f32_16x16x32_bf16 v[204:207], a[168:171], v[40:43], v[204:207]\n"
"ds_write_b128 v187, v[212:215] offset:128\n"
"v_mfma_f32_16x16x32_bf16 v[204:207], a[180:183], v[44:47], v[204:207]\n"
"v_exp_f32 v113, v113\n"
"v_mfma_f32_16x16x32_bf16 v[204:207], a[192:195], v[48:51], v[204:207]\n"
"v_exp_f32 v114, v114\n"
"v_mfma_f32_16x16x32_bf16 v[204:207], a[204:207], v[52:55], v[204:207]\n"
"v_exp_f32 v115, v115\n"
"v_mfma_f32_16x16x32_bf16 v[208:211], a[148:151], v[32:35], 0\n"
"v_cvt_pk_bf16_f32 v82, v112, v114\n"
"v_cvt_pk_bf16_f32 v83, v113, v115\n"
"v_mfma_f32_16x16x32_bf16 v[208:211], a[160:163], v[36:39], v[208:211]\n"
"ds_read_b128 v[112:115], v178 offset:27008\n"
"v_exp_f32 v116, v116\n"
"v_mfma_f32_16x16x32_bf16 v[208:211], a[172:175], v[40:43], v[208:211]\n"
"v_cvt_pk_bf16_f32 v148, v204, v205\n"
"v_cvt_pk_bf16_f32 v149, v206, v207\n"
"v_mfma_f32_16x16x32_bf16 v[208:211], a[184:187], v[44:47], v[208:211]\n"
"s_waitcnt vmcnt(9) lgkmcnt(1)\n"
"v_exp_f32 v117, v117\n"
"v_mfma_f32_16x16x32_bf16 v[208:211], a[196:199], v[48:51], v[208:211]\n"
"s_barrier\n"
"v_mfma_f32_16x16x32_bf16 v[208:211], a[208:211], v[52:55], v[208:211]\n"
"s_add_u32 m0, s79, 142464\n"
"buffer_load_dword v201, off, s[28:31], s40 offset:128 sc1\n"
"ds_read_b64_tr_b16 a[216:217], v236\n"
"v_mfma_f32_16x16x32_bf16 v[212:215], a[152:155], v[32:35], 0\n"
".Lic1%=:\n"
"s_cbranch_execz .Lib0%=\n"
"buffer_load_dwordx4 v192, s[16:19], s43 offen lds\n"
"ds_read_b128 v[32:35], v188\n"
"v_mfma_f32_16x16x32_bf16 v[212:215], a[164:167], v[36:39], v[212:215]\n"
"ds_read_b128 v[36:39], v189\n"
"v_cvt_pk_bf16_f32 v150, v208, v209\n"
"v_mfma_f32_16x16x32_bf16 v[212:215], a[176:179], v[40:43], v[212:215]\n"
"ds_read_b128 v[40:43], v188 offset:1024\n"
"v_cvt_pk_bf16_f32 v151, v210, v211\n"
"v_mfma_f32_16x16x32_bf16 v[212:215], a[188:191], v[44:47], v[212:215]\n"
"ds_read_b128 v[44:47], v189 offset:1024\n"
"v_exp_f32 v118, v118\n"
"v_mfma_f32_16x16x32_bf16 v[212:215], a[200:203], v[48:51], v[212:215]\n"
"ds_read_b128 v[48:51], v188 offset:2048\n"
"v_cvt_pk_bf16_f32 v84, v116, v118\n"
"v_mfma_f32_16x16x32_bf16 v[212:215], a[212:215], v[52:55], v[212:215]\n"
"ds_read_b128 v[52:55], v189 offset:2048\n"
"v_exp_f32 v119, v119\n"
"v_exp_f32 v128, v128\n"
"v_cvt_pk_bf16_f32 v85, v117, v119\n"
"v_exp_f32 v129, v129\n"
"s_nop 0\n"
"v_smfmac_f32_16x16x64_bf16 v[252:255], v[82:85], a[240:247], v63\n"
"s_add_u32 m0, s79, 142464\n"
"s_nop 0\n"
"buffer_load_dwordx4 v192, s[16:19], s43 offen offset:1024 lds\n"
"v_smfmac_f32_16x16x64_bf16 v[98:101], v[82:85], v[120:127], v63\n"
"s_waitcnt lgkmcnt(5)\n"
"ds_read_b128 v[120:123], v179 offset:27008\n"
"v_smfmac_f32_16x16x64_bf16 v[106:109], v[82:85], v[136:143], v63\n"
"ds_read_b128 v[116:119], v180\n"
"v_mfma_f32_16x16x32_bf16 v[82:85], v[172:175], v[82:85], 0\n"
"ds_read_b128 v[124:127], v181\n"
"ds_read_b128 v[136:139], v180 offset:13504\n"
"v_mfma_f32_16x16x32_bf16 v[204:207], a[0:3], v[32:35], 0\n"
"s_waitcnt lgkmcnt(8)\n"
"ds_read_b128 v[140:143], v180 offset:27008\n"
"v_cvt_pk_bf16_f32 v162, v212, v213\n"
"v_mfma_f32_16x16x32_bf16 v[204:207], a[12:15], v[36:39], v[204:207]\n"
"s_waitcnt lgkmcnt(8)\n"
"v_exp_f32 v130, v130\n"
"v_mfma_f32_16x16x32_bf16 v[204:207], a[24:27], v[40:43], v[204:207]\n"
"s_waitcnt lgkmcnt(7)\n"
"v_exp_f32 v131, v131\n"
"v_mfma_f32_16x16x32_bf16 v[204:207], a[36:39], v[44:47], v[204:207]\n"
"s_waitcnt lgkmcnt(6)\n"
"v_cvt_pk_bf16_f32 v90, v128, v130\n"
"v_cvt_pk_bf16_f32 v91, v129, v131\n"
"v_mfma_f32_16x16x32_bf16 v[204:207], a[48:51], v[48:51], v[204:207]\n"
"s_waitcnt lgkmcnt(5)\n"
"ds_read_b128 v[128:131], v181 offset:13504\n"
"v_cvt_pk_bf16_f32 v163, v214, v215\n"
"v_mfma_f32_16x16x32_bf16 v[204:207], a[60:63], v[52:55], v[204:207]\n"
"s_add_u32 m0, s79, 142464\n"
"v_exp_f32 v132, v132\n"
"v_mfma_f32_16x16x32_bf16 v[208:211], a[4:7], v[32:35], 0\n"
"buffer_load_dwordx4 v192, s[16:19], s43 offen offset:2048 lds\n"
"ds_read_b64_tr_b16 a[240:241], v182 offset:27136\n"
"v_mfma_f32_16x16x32_bf16 v[208:211], a[16:19], v[36:39], v[208:211]\n"
"s_add_u32 s43, s79, 36864\n"
"v_exp_f32 v133, v133\n"
"v_mfma_f32_16x16x32_bf16 v[208:211], a[28:31], v[40:43], v[208:211]\n"
"v_exp_f32 v134, v134\n"
"v_mfma_f32_16x16x32_bf16 v[208:211], a[40:43], v[44:47], v[208:211]\n"
"ds_read_b64_tr_b16 a[242:243], v183 offset:27136\n"
"ds_read_b64_tr_b16 a[244:245], v182 offset:27904\n"
"v_mfma_f32_16x16x32_bf16 v[208:211], a[52:55], v[48:51], v[208:211]\n"
"v_exp_f32 v135, v135\n"
"v_mfma_f32_16x16x32_bf16 v[208:211], a[64:67], v[52:55], v[208:211]\n"
"ds_read_b64_tr_b16 a[246:247], v183 offset:27904\n"
"v_cvt_pk_bf16_f32 v92, v132, v134\n"
"v_mfma_f32_16x16x32_bf16 v[212:215], a[8:11], v[32:35], 0\n"
"v_cvt_pk_bf16_f32 v93, v133, v135\n"
"v_cvt_pk_bf16_f32 v204, v204, v205\n"
"v_mfma_f32_16x16x32_bf16 v[212:215], a[20:23], v[36:39], v[212:215]\n"
"ds_read_b128 v[132:135], v181 offset:27008\n"
"ds_read_b64_tr_b16 a[218:219], v236 offset:6144\n"
"v_smfmac_f32_16x16x64_bf16 v[252:255], v[90:93], v[224:231], v63\n"
"s_add_u32 m0, s34, 0\n"
"s_nop 0\n"
"buffer_load_dwordx4 v190, s[8:11], s32 offen lds\n"
"v_smfmac_f32_16x16x64_bf16 v[98:101], v[90:93], v[244:251], v63\n"
"ds_read_b64_tr_b16 v[216:217], v182 offset:54144\n"
"v_smfmac_f32_16x16x64_bf16 v[106:109], v[90:93], a[248:255], v63\n"
"ds_read_b64_tr_b16 v[218:219], v183 offset:54144\n"
"v_mfma_f32_16x16x32_bf16 v[90:93], v[172:175], v[90:93], v[82:85]\n"
"ds_read_b64_tr_b16 v[220:221], v182 offset:54912\n"
"v_cvt_pk_bf16_f32 v252, v252, v253\n"
"v_mfma_f32_16x16x32_bf16 v[212:215], a[32:35], v[40:43], v[212:215]\n"
"ds_read_b64 v[82:83], v178 offset:27072\n"
"v_cvt_pk_bf16_f32 v253, v254, v255\n"
"v_mfma_f32_16x16x32_bf16 v[212:215], a[44:47], v[44:47], v[212:215]\n"
"ds_read_b64 v[84:85], v179 offset:27072\n"
"buffer_load_dword v237, v238, s[8:11], s78 offen\n"
"v_mfma_f32_16x16x32_bf16 v[212:215], a[56:59], v[48:51], v[212:215]\n"
"s_add_u32 s78, s78, s33\n"
"v_rcp_f32 v171, v90\n"
"v_mfma_f32_16x16x32_bf16 v[212:215], a[68:71], v[52:55], v[212:215]\n"
"v_cvt_pk_bf16_f32 v171, v171, v171\n"
"s_add_u32 m0, s34, 3072\n"
"v_and_b32 v92, v171, v176\n"
"v_and_b32 v93, v171, v177\n"
"v_cvt_pk_bf16_f32 v98, v98, v99\n"
"v_cvt_pk_bf16_f32 v99, v100, v101\n"
"v_mfma_f32_16x16x16_bf16 v[252:255], v[252:253], v[92:93], 0\n"
"buffer_load_dwordx4 v191, s[8:11], s32 offen lds\n"
"ds_read_b64 v[90:91], v180 offset:64\n"
"v_mfma_f32_16x16x16_bf16 v[98:101], v[98:99], v[92:93], 0\n"
"s_add_u32 s32, s32, s33\n"
"v_cvt_pk_bf16_f32 v106, v106, v107\n"
"v_cvt_pk_bf16_f32 v107, v108, v109\n"
"v_cvt_pk_bf16_f32 v205, v206, v207\n"
"v_bfi_b32 v80, v12, v80, v26\n"
"v_mfma_f32_16x16x16_bf16 v[106:109], v[106:107], v[92:93], 0\n"
"ds_read_b64 v[92:93], v181 offset:64\n"
"v_cvt_pk_bf16_f32 v252, v252, v253\n"
"v_cvt_pk_bf16_f32 v253, v254, v255\n"
"v_cvt_pk_bf16_f32 v254, v98, v99\n"
"v_cvt_pk_bf16_f32 v255, v100, v101\n"
"v_bfi_b32 v81, v13, v81, v26\n"
"v_bfi_b32 v86, v14, v86, v26\n"
"v_bfi_b32 v87, v15, v87, v26\n"
"v_cvt_pk_bf16_f32 v106, v106, v107\n"
"v_cvt_pk_bf16_f32 v107, v108, v109\n"
"v_bfi_b32 v104, v16, v104, v26\n"
"v_bfi_b32 v105, v17, v105, v26\n"
"v_bfi_b32 v110, v18, v110, v26\n"
"v_cvt_pk_bf16_f32 v206, v208, v209\n"
"v_bfi_b32 v111, v19, v111, v26\n"
"v_cvt_pk_bf16_f32 v207, v210, v211\n"
"v_mfma_f32_16x16x32_bf16 v[208:211], a[80:83], v[32:35], 0\n"
"s_waitcnt lgkmcnt(1)\n"
"buffer_store_dwordx4 v[252:255], v192, s[28:31], s37 offen sc1\n"
"v_mfma_f32_16x16x32_bf16 v[112:115], v[112:115], v[152:155], 0\n"
"s_add_u32 m0, s34, 1024\n"
"ds_read_b64 v[98:99], v180 offset:13568\n"
"ds_read_b64 v[100:101], v181 offset:13568\n"
"v_mfma_f32_16x16x32_bf16 v[112:115], v[80:83], v[164:167], v[112:115]\n"
"s_waitcnt lgkmcnt(2)\n"
"ds_write_b128 v186, v[204:207] offset:6752\n"
"v_mfma_f32_16x16x32_bf16 v[120:123], v[120:123], v[152:155], 0\n"
"buffer_load_dwordx4 v190, s[8:11], s0 offen lds\n"
"v_cvt_pk_bf16_f32 v212, v212, v213\n"
"v_mfma_f32_16x16x32_bf16 v[120:123], v[84:87], v[166:169], v[120:123]\n"
"v_cvt_pk_bf16_f32 v213, v214, v215\n"
"v_mov_b64 v[252:253], 0\n"
"v_mfma_f32_16x16x32_bf16 v[116:119], v[116:119], v[152:155], 0\n"
"ds_read_b64 v[108:109], v181 offset:27072\n"
"ds_read_b64_tr_b16 v[222:223], v183 offset:54912\n"
"v_mfma_f32_16x16x32_bf16 v[116:119], v[88:91], v[164:167], v[116:119]\n"
"buffer_store_dwordx2 v[106:107], v193, s[28:31], s37 offen offset:1024 sc1\n"
"v_mfma_f32_16x16x32_bf16 v[124:127], v[124:127], v[152:155], 0\n"
"s_waitcnt lgkmcnt(4)\n"
"ds_read_b64 v[106:107], v180 offset:27072\n"
"ds_read_b64_tr_b16 v[72:73], v182 offset:54152\n"
"v_mfma_f32_16x16x32_bf16 v[124:127], v[92:95], v[166:169], v[124:127]\n"
"s_waitcnt lgkmcnt(5)\n"
"ds_read_b64_tr_b16 v[74:75], v183 offset:54152\n"
"v_max_f32 v112, v112, v120\n"
"v_mfma_f32_16x16x32_bf16 v[136:139], v[136:139], v[152:155], 0\n"
"s_add_u32 s37, s37, 1572864\n"
"ds_read_b64_tr_b16 v[76:77], v182 offset:54920\n"
"v_max_f32 v113, v113, v121\n"
"v_mfma_f32_16x16x32_bf16 v[136:139], v[96:99], v[164:167], v[136:139]\n"
"s_add_u32 m0, s34, 4096\n"
"ds_read_b64_tr_b16 v[120:121], v182 offset:27144\n"
"buffer_load_dwordx4 v191, s[8:11], s0 offen lds\n"
"v_mfma_f32_16x16x32_bf16 v[128:131], v[128:131], v[152:155], 0\n"
"s_add_u32 s0, s0, s33\n"
"ds_read_b64_tr_b16 v[78:79], v183 offset:54920\n"
"v_max_f32 v114, v114, v122\n"
"v_mfma_f32_16x16x32_bf16 v[128:131], v[100:103], v[166:169], v[128:131]\n"
"s_waitcnt lgkmcnt(7)\n"
"ds_read_b64_tr_b16 v[8:9], v182 offset:54088\n"
"v_max_f32 v115, v115, v123\n"
"v_mfma_f32_16x16x32_bf16 v[132:135], v[132:135], v[152:155], 0\n"
"ds_read_b64_tr_b16 v[122:123], v183 offset:27144\n"
"v_max3_f32 v170, v112, v113, v114\n"
"v_mfma_f32_16x16x32_bf16 v[132:135], v[108:111], v[166:169], v[132:135]\n"
"s_waitcnt lgkmcnt(7)\n"
"ds_read_b64_tr_b16 v[10:11], v183 offset:54088\n"
"v_max_f32 v116, v116, v124\n"
"v_mfma_f32_16x16x32_bf16 v[140:143], v[140:143], v[152:155], 0\n"
"ds_read_b64_tr_b16 v[12:13], v182 offset:54856\n"
"v_max_f32 v117, v117, v125\n"
"v_mfma_f32_16x16x32_bf16 v[140:143], v[104:107], v[164:167], v[140:143]\n"
"ds_read_b64_tr_b16 v[124:125], v182 offset:27912\n"
"v_max_f32 v118, v118, v126\n"
"v_mfma_f32_16x16x32_bf16 v[208:211], a[92:95], v[36:39], v[208:211]\n"
"ds_read_b64_tr_b16 v[14:15], v183 offset:54856\n"
"v_max_f32 v119, v119, v127\n"
"v_mfma_f32_16x16x32_bf16 v[208:211], a[104:107], v[40:43], v[208:211]\n"
"s_add_u32 m0, s34, 2048\n"
"ds_read_b64_tr_b16 v[126:127], v183 offset:27912\n"
"buffer_load_dwordx4 v190, s[8:11], s1 offen lds\n"
"v_mfma_f32_16x16x32_bf16 v[208:211], a[116:119], v[44:47], v[208:211]\n"
"ds_read_b64_tr_b16 a[220:221], v236 offset:8\n"
"v_max3_f32 v170, v170, v115, v116\n"
"v_mfma_f32_16x16x32_bf16 v[208:211], a[128:131], v[48:51], v[208:211]\n"
"v_max3_f32 v170, v170, v117, v118\n"
"v_max_f32 v136, v136, v128\n"
"v_mfma_f32_16x16x32_bf16 v[208:211], a[140:143], v[52:55], v[208:211]\n"
"ds_read_b64_tr_b16 a[222:223], v236 offset:6152\n"
"v_max_f32 v137, v137, v129\n"
"v_mfma_f32_16x16x32_bf16 v[204:207], a[72:75], v[32:35], 0\n"
"v_max_f32 v138, v138, v130\n"
"v_max_f32 v139, v139, v131\n"
"v_mfma_f32_16x16x32_bf16 v[204:207], a[84:87], v[36:39], v[204:207]\n"
"ds_read_b64_tr_b16 v[128:129], v182 offset:27080\n"
"v_max3_f32 v170, v170, v119, v136\n"
"v_mfma_f32_16x16x32_bf16 v[204:207], a[96:99], v[40:43], v[204:207]\n"
"ds_read_b64_tr_b16 v[130:131], v183 offset:27080\n"
"v_cvt_pk_bf16_f32 v214, v208, v209\n"
"v_mfma_f32_16x16x32_bf16 v[204:207], a[108:111], v[44:47], v[204:207]\n"
"ds_read_b64_tr_b16 a[224:225], v235\n"
"v_cvt_pk_bf16_f32 v215, v210, v211\n"
"v_mfma_f32_16x16x32_bf16 v[204:207], a[120:123], v[48:51], v[204:207]\n"
"s_add_u32 m0, s34, 5120\n"
"v_max3_f32 v170, v170, v137, v138\n"
"v_max_f32 v140, v140, v132\n"
"v_mfma_f32_16x16x32_bf16 v[204:207], a[132:135], v[52:55], v[204:207]\n"
"buffer_load_dwordx4 v191, s[8:11], s1 offen lds\n"
"ds_write_b128 v186, v[212:215] offset:6816\n"
"v_mfma_f32_16x16x32_bf16 v[208:211], a[76:79], v[32:35], 0\n"
"s_add_u32 s1, s1, s33\n"
"v_max_f32 v141, v141, v133\n"
"v_max_f32 v142, v142, v134\n"
"v_mfma_f32_16x16x32_bf16 v[208:211], a[88:91], v[36:39], v[208:211]\n"
"v_max_f32 v143, v143, v135\n"
"v_cvt_pk_bf16_f32 v204, v204, v205\n"
"v_mfma_f32_16x16x32_bf16 v[208:211], a[100:103], v[40:43], v[208:211]\n"
"ds_read_b64_tr_b16 v[132:133], v182 offset:27848\n"
"ds_read_b64_tr_b16 v[134:135], v183 offset:27848\n"
"v_mfma_f32_16x16x32_bf16 v[208:211], a[112:115], v[44:47], v[208:211]\n"
"ds_read_b64_tr_b16 a[226:227], v235 offset:6144\n"
"v_cvt_pk_bf16_f32 v205, v206, v207\n"
"v_mfma_f32_16x16x32_bf16 v[208:211], a[124:127], v[48:51], v[208:211]\n"
"v_max3_f32 v170, v170, v139, v140\n"
"v_max3_f32 v170, v170, v141, v142\n"
"v_mfma_f32_16x16x32_bf16 v[208:211], a[136:139], v[52:55], v[208:211]\n"
"v_mov_b64 v[254:255], 0\n"
"v_mov_b64 v[98:99], 0\n"
"v_mfma_f32_16x16x32_bf16 v[212:215], a[144:147], v[32:35], 0\n"
"v_mov_b64 v[100:101], 0\n"
"v_mov_b64 v[106:107], 0\n"
"v_mfma_f32_16x16x32_bf16 v[212:215], a[156:159], v[36:39], v[212:215]\n"
"v_mov_b64 v[108:109], 0\n"
"v_max_f32 v171, v170, v143\n"
"v_mfma_f32_16x16x32_bf16 v[212:215], a[168:171], v[40:43], v[212:215]\n"
"v_cvt_pk_bf16_f32 v206, v208, v209\n"
"v_cvt_pk_bf16_f32 v207, v210, v211\n"
"v_mfma_f32_16x16x32_bf16 v[212:215], a[180:183], v[44:47], v[212:215]\n"
"ds_write_b128 v186, v[204:207] offset:6880\n"
"v_mfma_f32_16x16x32_bf16 v[212:215], a[192:195], v[48:51], v[212:215]\n"
"v_max_f32 v170, v170, v143\n"
"s_mov_b64 exec, s[44:45]\n"
"v_add_u32 v179, 0xc0, v179\n"
"s_mov_b64 exec, -1\n"
"v_mfma_f32_16x16x32_bf16 v[212:215], a[204:207], v[52:55], v[212:215]\n"
"v_permlane16_swap_b32 v170, v171\n"
"v_mfma_f32_16x16x32_bf16 v[208:211], a[148:151], v[32:35], 0\n"
"v_max_f32 v170, v170, v171\n"
"v_mov_b32 v171, v170\n"
"v_mfma_f32_16x16x32_bf16 v[208:211], a[160:163], v[36:39], v[208:211]\n"
"s_waitcnt vmcnt(12)\n"
"v_readfirstlane_b32 s6, v201\n"
"v_permlane32_swap_b32 v170, v171\n"
"v_max_f32 v170, v170, v171\n"
"v_mul_f32 v170, 0xbe553b95, v170\n"
"v_pk_fma_f32 v[112:113], v[112:113], s[70:71], v[170:171] op_sel_hi:[1,0,0]\n"
"v_pk_fma_f32 v[114:115], v[114:115], s[70:71], v[170:171] op_sel_hi:[1,0,0]\n"
"v_pk_fma_f32 v[116:117], v[116:117], s[70:71], v[170:171] op_sel_hi:[1,0,0]\n"
"v_pk_fma_f32 v[118:119], v[118:119], s[70:71], v[170:171] op_sel_hi:[1,0,0]\n"
"v_pk_fma_f32 v[136:137], v[136:137], s[70:71], v[170:171] op_sel_hi:[1,0,0]\n"
"v_pk_fma_f32 v[138:139], v[138:139], s[70:71], v[170:171] op_sel_hi:[1,0,0]\n"
"v_pk_fma_f32 v[140:141], v[140:141], s[70:71], v[170:171] op_sel_hi:[1,0,0]\n"
"v_pk_fma_f32 v[142:143], v[142:143], s[70:71], v[170:171] op_sel_hi:[1,0,0]\n"
"v_mfma_f32_16x16x32_bf16 v[208:211], a[172:175], v[40:43], v[208:211]\n"
"s_waitcnt vmcnt(9) lgkmcnt(0)\n"
"v_exp_f32 v112, v112\n"
"v_mfma_f32_16x16x32_bf16 v[208:211], a[184:187], v[44:47], v[208:211]\n"
"s_and_b32 s6, s6, 0xffffff00\n"
"v_exp_f32 v113, v113\n"
"v_mfma_f32_16x16x32_bf16 v[208:211], a[196:199], v[48:51], v[208:211]\n"
"s_barrier\n"
"v_mfma_f32_16x16x32_bf16 v[208:211], a[208:211], v[52:55], v[208:211]\n"
"s_add_u32 m0, s79, 130176\n"
"v_exp_f32 v114, v114\n"
"v_mfma_f32_16x16x32_bf16 v[204:207], a[152:155], v[32:35], 0\n"
"buffer_load_dwordx4 v192, s[16:19], s43 offen lds\n"
"ds_read_b128 v[32:35], v188 offset:6144\n"
"v_mfma_f32_16x16x32_bf16 v[204:207], a[164:167], v[36:39], v[204:207]\n"
"ds_read_b128 v[36:39], v189 offset:6144\n"
"v_cvt_pk_bf16_f32 v152, v212, v213\n"
"v_mfma_f32_16x16x32_bf16 v[204:207], a[176:179], v[40:43], v[204:207]\n"
"ds_read_b128 v[40:43], v188 offset:7168\n"
"v_cvt_pk_bf16_f32 v153, v214, v215\n"
"v_mfma_f32_16x16x32_bf16 v[204:207], a[188:191], v[44:47], v[204:207]\n"
"ds_read_b128 v[44:47], v189 offset:7168\n"
"v_cvt_pk_bf16_f32 v82, v112, v114\n"
"v_mfma_f32_16x16x32_bf16 v[204:207], a[200:203], v[48:51], v[204:207]\n"
"ds_read_b128 v[48:51], v188 offset:8192\n"
"v_cvt_pk_bf16_f32 v154, v208, v209\n"
"v_mfma_f32_16x16x32_bf16 v[204:207], a[212:215], v[52:55], v[204:207]\n"
"ds_read_b128 v[52:55], v189 offset:8192\n"
"v_exp_f32 v115, v115\n"
"v_exp_f32 v116, v116\n"
"v_cvt_pk_bf16_f32 v83, v113, v115\n"
"v_exp_f32 v117, v117\n"
"v_exp_f32 v118, v118\n"
"v_exp_f32 v119, v119\n"
"v_cvt_pk_bf16_f32 v84, v116, v118\n"
"v_cvt_pk_bf16_f32 v85, v117, v119\n"
"v_cvt_pk_bf16_f32 v155, v210, v211\n"
"v_exp_f32 v136, v136\n"
"v_smfmac_f32_16x16x64_bf16 v[252:255], v[82:85], a[240:247], v63\n"
"s_waitcnt lgkmcnt(4)\n"
"ds_read_b128 v[112:115], v180\n"
"v_smfmac_f32_16x16x64_bf16 v[98:101], v[82:85], v[120:127], v63\n"
"s_waitcnt lgkmcnt(3)\n"
"ds_read_b128 v[120:123], v181\n"
"v_smfmac_f32_16x16x64_bf16 v[106:109], v[82:85], v[128:135], v63\n"
"s_add_u32 m0, s79, 130176\n"
"s_nop 0\n"
"buffer_load_dwordx4 v192, s[16:19], s43 offen offset:1024 lds\n"
"v_mfma_f32_16x16x32_bf16 v[82:85], v[172:175], v[82:85], 0\n"
"ds_read_b128 v[116:119], v180 offset:13504\n"
"ds_read_b128 v[124:127], v181 offset:13504\n"
"v_mfma_f32_16x16x32_bf16 v[212:215], a[0:3], v[32:35], 0\n"
"ds_read_b128 v[128:131], v180 offset:27008\n"
"v_cvt_pk_bf16_f32 v166, v204, v205\n"
"v_mfma_f32_16x16x32_bf16 v[212:215], a[12:15], v[36:39], v[212:215]\n"
"ds_read_b128 v[132:135], v178\n"
"v_cvt_pk_bf16_f32 v167, v206, v207\n"
"v_mfma_f32_16x16x32_bf16 v[212:215], a[24:27], v[40:43], v[212:215]\n"
"ds_read_b64_tr_b16 a[228:229], v236 offset:12288\n"
"v_exp_f32 v137, v137\n"
"v_mfma_f32_16x16x32_bf16 v[212:215], a[36:39], v[44:47], v[212:215]\n"
"s_waitcnt lgkmcnt(8)\n"
"v_exp_f32 v138, v138\n"
"v_mfma_f32_16x16x32_bf16 v[212:215], a[48:51], v[48:51], v[212:215]\n"
"s_waitcnt lgkmcnt(7)\n"
"v_exp_f32 v139, v139\n"
"v_mfma_f32_16x16x32_bf16 v[212:215], a[60:63], v[52:55], v[212:215]\n"
"v_cvt_pk_bf16_f32 v90, v136, v138\n"
"v_cvt_pk_bf16_f32 v91, v137, v139\n"
"v_mfma_f32_16x16x32_bf16 v[208:211], a[4:7], v[32:35], 0\n"
"s_add_u32 m0, s79, 130176\n"
"ds_read_b128 v[136:139], v181 offset:27008\n"
"buffer_load_dwordx4 v192, s[16:19], s43 offen offset:2048 lds\n"
"v_mfma_f32_16x16x32_bf16 v[208:211], a[16:19], v[36:39], v[208:211]\n"
"s_add_u32 s43, s79, 55296\n"
"v_exp_f32 v140, v140\n"
"v_mfma_f32_16x16x32_bf16 v[208:211], a[28:31], v[40:43], v[208:211]\n"
"v_exp_f32 v141, v141\n"
"v_mfma_f32_16x16x32_bf16 v[208:211], a[40:43], v[44:47], v[208:211]\n"
"v_exp_f32 v142, v142\n"
"v_mfma_f32_16x16x32_bf16 v[208:211], a[52:55], v[48:51], v[208:211]\n"
"v_exp_f32 v143, v143\n"
"v_mfma_f32_16x16x32_bf16 v[208:211], a[64:67], v[52:55], v[208:211]\n"
"v_cvt_pk_bf16_f32 v92, v140, v142\n"
"v_cvt_pk_bf16_f32 v93, v141, v143\n"
"v_mfma_f32_16x16x32_bf16 v[204:207], a[8:11], v[32:35], 0\n"
"ds_read_b128 v[140:143], v179\n"
"ds_read_b64_tr_b16 v[24:25], v184 offset:72\n"
"v_smfmac_f32_16x16x64_bf16 v[252:255], v[90:93], v[216:223], v63\n"
"ds_read_b64_tr_b16 a[230:231], v236 offset:18432\n"
"v_smfmac_f32_16x16x64_bf16 v[98:101], v[90:93], v[72:79], v63\n"
"s_add_u32 m0, s34, 6144\n"
"s_nop 0\n"
"buffer_load_dwordx4 v190, s[8:11], s32 offen lds\n"
"v_smfmac_f32_16x16x64_bf16 v[106:109], v[90:93], v[8:15], v63\n"
"ds_read_b64_tr_b16 v[26:27], v185 offset:72\n"
"v_mfma_f32_16x16x32_bf16 v[90:93], v[172:175], v[90:93], v[82:85]\n"
"ds_read_b64_tr_b16 v[28:29], v184 offset:840\n"
"v_cvt_pk_bf16_f32 v252, v252, v253\n"
"v_mfma_f32_16x16x32_bf16 v[204:207], a[20:23], v[36:39], v[204:207]\n"
"ds_read_b64 v[82:83], v180 offset:64\n"
"v_cvt_pk_bf16_f32 v253, v254, v255\n"
"v_mfma_f32_16x16x32_bf16 v[204:207], a[32:35], v[40:43], v[204:207]\n"
"ds_read_b64 v[84:85], v181 offset:64\n"
"v_cvt_pk_bf16_f32 v98, v98, v99\n"
"v_mfma_f32_16x16x32_bf16 v[204:207], a[44:47], v[44:47], v[204:207]\n"
"v_rcp_f32 v171, v90\n"
"v_mfma_f32_16x16x32_bf16 v[204:207], a[56:59], v[48:51], v[204:207]\n"
"ds_read_b64 v[90:91], v180 offset:13568\n"
"v_cvt_pk_bf16_f32 v171, v171, v171\n"
"v_mfma_f32_16x16x32_bf16 v[204:207], a[68:71], v[52:55], v[204:207]\n"
"v_and_b32 v92, v171, v176\n"
"s_add_u32 m0, s34, 9216\n"
"v_and_b32 v93, v171, v177\n"
"v_cvt_pk_bf16_f32 v99, v100, v101\n"
"v_cvt_pk_bf16_f32 v106, v106, v107\n"
"v_mfma_f32_16x16x16_bf16 v[252:255], v[252:253], v[92:93], 0\n"
"buffer_load_dwordx4 v191, s[8:11], s32 offen lds\n"
"ds_read_b64_tr_b16 v[30:31], v185 offset:840\n"
"v_mfma_f32_16x16x16_bf16 v[98:101], v[98:99], v[92:93], 0\n"
"s_add_u32 s32, s32, s33\n"
"v_cvt_pk_bf16_f32 v107, v108, v109\n"
"v_cvt_pk_bf16_f32 v212, v212, v213\n"
"v_cvt_pk_bf16_f32 v213, v214, v215\n"
"v_mfma_f32_16x16x16_bf16 v[106:109], v[106:107], v[92:93], 0\n"
"s_waitcnt lgkmcnt(3)\n"
"ds_read_b64 v[92:93], v181 offset:13568\n"
"v_cvt_pk_bf16_f32 v252, v252, v253\n"
"v_mfma_f32_16x16x32_bf16 v[112:115], v[112:115], v[144:147], 0\n"
"s_waitcnt lgkmcnt(3)\n"
"buffer_load_dword v237, v238, s[8:11], s78 offen\n"
"v_cvt_pk_bf16_f32 v253, v254, v255\n"
"v_mfma_f32_16x16x32_bf16 v[112:115], v[80:83], v[156:159], v[112:115]\n"
"s_waitcnt lgkmcnt(2)\n"
"v_cvt_pk_bf16_f32 v254, v98, v99\n"
"v_cvt_pk_bf16_f32 v255, v100, v101\n"
"v_mfma_f32_16x16x32_bf16 v[120:123], v[120:123], v[144:147], 0\n"
"s_add_u32 s78, s78, s33\n"
"ds_read_b64 v[98:99], v180 offset:27072\n"
"ds_read_b64 v[100:101], v181 offset:27072\n"
"v_mfma_f32_16x16x32_bf16 v[120:123], v[84:87], v[158:161], v[120:123]\n"
"v_cvt_pk_bf16_f32 v106, v106, v107\n"
"v_cvt_pk_bf16_f32 v107, v108, v109\n"
"v_mfma_f32_16x16x32_bf16 v[116:119], v[116:119], v[144:147], 0\n"
"s_add_u32 m0, s34, 7168\n"
"buffer_store_dwordx4 v[252:255], v192, s[28:31], s37 offen sc1\n"
"v_mfma_f32_16x16x32_bf16 v[116:119], v[88:91], v[156:159], v[116:119]\n"
"s_waitcnt lgkmcnt(2)\n"
"ds_read_b64 v[108:109], v179 offset:64\n"
"v_cvt_pk_bf16_f32 v214, v208, v209\n"
"v_mfma_f32_16x16x32_bf16 v[124:127], v[124:127], v[144:147], 0\n"
"v_cvt_pk_bf16_f32 v215, v210, v211\n"
"v_cvt_pk_bf16_f32 v204, v204, v205\n"
"v_mfma_f32_16x16x32_bf16 v[124:127], v[92:95], v[158:161], v[124:127]\n"
"buffer_load_dwordx4 v190, s[8:11], s0 offen lds\n"
"ds_write_b128 v186, v[212:215] offset:20256\n"
"v_mfma_f32_16x16x32_bf16 v[208:211], a[80:83], v[32:35], 0\n"
"s_waitcnt lgkmcnt(2)\n"
"v_cvt_pk_bf16_f32 v205, v206, v207\n"
"v_max_f32 v112, v112, v120\n"
"v_mfma_f32_16x16x32_bf16 v[208:211], a[92:95], v[36:39], v[208:211]\n"
"v_max_f32 v113, v113, v121\n"
"v_max_f32 v114, v114, v122\n"
"v_mfma_f32_16x16x32_bf16 v[208:211], a[104:107], v[40:43], v[208:211]\n"
"s_waitcnt vmcnt(11)\n"
"v_or_b32 v201, 2, s6\n"
"v_max_f32 v115, v115, v123\n"
"v_mfma_f32_16x16x32_bf16 v[208:211], a[116:119], v[44:47], v[208:211]\n"
"s_waitcnt lgkmcnt(1)\n"
"s_mov_b64 exec, 1\n"
"buffer_store_dword v201, off, s[28:31], s40 sc1\n"
"s_mov_b64 exec, -1\n"
"v_mfma_f32_16x16x32_bf16 v[208:211], a[128:131], v[48:51], v[208:211]\n"
"buffer_store_dwordx2 v[106:107], v193, s[28:31], s37 offen offset:1024 sc1\n"
"v_mfma_f32_16x16x32_bf16 v[208:211], a[140:143], v[52:55], v[208:211]\n"
"s_add_u32 s37, s37, 1572864\n"
"ds_read_b64 v[106:107], v178 offset:64\n"
"ds_read_b64_tr_b16 a[232:233], v236 offset:12296\n"
"v_mfma_f32_16x16x32_bf16 v[128:131], v[128:131], v[144:147], 0\n"
"v_max3_f32 v170, v112, v113, v114\n"
"v_max_f32 v116, v116, v124\n"
"v_mfma_f32_16x16x32_bf16 v[128:131], v[96:99], v[156:159], v[128:131]\n"
"s_add_u32 m0, s34, 10240\n"
"ds_read_b64_tr_b16 a[234:235], v236 offset:18440\n"
"v_cvt_pk_bf16_f32 v206, v208, v209\n"
"v_mfma_f32_16x16x32_bf16 v[136:139], v[136:139], v[144:147], 0\n"
"buffer_load_dwordx4 v191, s[8:11], s0 offen lds\n"
"v_cvt_pk_bf16_f32 v207, v210, v211\n"
"v_mfma_f32_16x16x32_bf16 v[136:139], v[100:103], v[158:161], v[136:139]\n"
"s_add_u32 s0, s0, s33\n"
"ds_write_b128 v186, v[204:207] offset:20320\n"
"v_mfma_f32_16x16x32_bf16 v[140:143], v[140:143], v[144:147], 0\n"
"v_max_f32 v117, v117, v125\n"
"v_max_f32 v118, v118, v126\n"
"v_mfma_f32_16x16x32_bf16 v[140:143], v[108:111], v[158:161], v[140:143]\n"
"s_waitcnt lgkmcnt(3)\n"
"v_max_f32 v119, v119, v127\n"
"v_max3_f32 v170, v170, v115, v116\n"
"v_mfma_f32_16x16x32_bf16 v[132:135], v[132:135], v[144:147], 0\n"
"ds_read_b64_tr_b16 a[236:237], v235 offset:12288\n"
"v_max3_f32 v170, v170, v117, v118\n"
"v_mfma_f32_16x16x32_bf16 v[132:135], v[104:107], v[156:159], v[132:135]\n"
"ds_read_b64_tr_b16 a[238:239], v235 offset:18432\n"
"v_max_f32 v128, v128, v136\n"
"v_mfma_f32_16x16x32_bf16 v[212:215], a[72:75], v[32:35], 0\n"
"v_max_f32 v129, v129, v137\n"
"v_max_f32 v130, v130, v138\n"
"v_mfma_f32_16x16x32_bf16 v[212:215], a[84:87], v[36:39], v[212:215]\n"
"s_add_u32 m0, s34, 8192\n"
"v_max_f32 v131, v131, v139\n"
"v_max3_f32 v170, v170, v119, v128\n"
"v_mfma_f32_16x16x32_bf16 v[212:215], a[96:99], v[40:43], v[212:215]\n"
"buffer_load_dwordx4 v190, s[8:11], s1 offen lds\n"
"v_max3_f32 v170, v170, v129, v130\n"
"v_mfma_f32_16x16x32_bf16 v[212:215], a[108:111], v[44:47], v[212:215]\n"
"v_max_f32 v132, v132, v140\n"
"v_max_f32 v133, v133, v141\n"
"v_mfma_f32_16x16x32_bf16 v[212:215], a[120:123], v[48:51], v[212:215]\n"
"v_max_f32 v134, v134, v142\n"
"v_max_f32 v135, v135, v143\n"
"v_mfma_f32_16x16x32_bf16 v[212:215], a[132:135], v[52:55], v[212:215]\n"
"v_max3_f32 v170, v170, v131, v132\n"
"v_max3_f32 v170, v170, v133, v134\n"
"v_mfma_f32_16x16x32_bf16 v[208:211], a[76:79], v[32:35], 0\n"
"v_mov_b64 v[252:253], 0\n"
"v_mov_b64 v[254:255], 0\n"
"v_mfma_f32_16x16x32_bf16 v[208:211], a[88:91], v[36:39], v[208:211]\n"
"v_mov_b64 v[98:99], 0\n"
"v_mov_b64 v[100:101], 0\n"
"v_mfma_f32_16x16x32_bf16 v[208:211], a[100:103], v[40:43], v[208:211]\n"
"v_cvt_pk_bf16_f32 v212, v212, v213\n"
"v_cvt_pk_bf16_f32 v213, v214, v215\n"
"v_mfma_f32_16x16x32_bf16 v[208:211], a[112:115], v[44:47], v[208:211]\n"
"ds_read_b128 v[120:123], v180 offset:13504\n"
"v_mov_b64 v[106:107], 0\n"
"v_mfma_f32_16x16x32_bf16 v[208:211], a[124:127], v[48:51], v[208:211]\n"
"s_add_u32 m0, s34, 11264\n"
"v_mov_b64 v[108:109], 0\n"
"v_max_f32 v171, v170, v135\n"
"v_mfma_f32_16x16x32_bf16 v[208:211], a[136:139], v[52:55], v[208:211]\n"
"buffer_load_dwordx4 v191, s[8:11], s1 offen lds\n"
"ds_read_b128 v[136:139], v181 offset:13504\n"
"v_mfma_f32_16x16x32_bf16 v[204:207], a[144:147], v[32:35], 0\n"
"s_add_u32 s1, s1, s33\n"
"v_max_f32 v170, v170, v135\n"
"s_mov_b64 exec, s[46:47]\n"
"v_add_u32 v183, 0xc0, v183\n"
"s_mov_b64 exec, -1\n"
"v_mfma_f32_16x16x32_bf16 v[204:207], a[156:159], v[36:39], v[204:207]\n"
"v_permlane16_swap_b32 v170, v171\n"
"v_mfma_f32_16x16x32_bf16 v[204:207], a[168:171], v[40:43], v[204:207]\n"
"v_max_f32 v170, v170, v171\n"
"v_mov_b32 v171, v170\n"
"v_mfma_f32_16x16x32_bf16 v[204:207], a[180:183], v[44:47], v[204:207]\n"
"s_nop 0\n"
"v_permlane32_swap_b32 v170, v171\n"
"v_max_f32 v170, v170, v171\n"
"v_mul_f32 v170, 0xbe553b95, v170\n"
"v_pk_fma_f32 v[112:113], v[112:113], s[70:71], v[170:171] op_sel_hi:[1,0,0]\n"
"v_pk_fma_f32 v[114:115], v[114:115], s[70:71], v[170:171] op_sel_hi:[1,0,0]\n"
"v_pk_fma_f32 v[116:117], v[116:117], s[70:71], v[170:171] op_sel_hi:[1,0,0]\n"
"v_pk_fma_f32 v[118:119], v[118:119], s[70:71], v[170:171] op_sel_hi:[1,0,0]\n"
"v_pk_fma_f32 v[128:129], v[128:129], s[70:71], v[170:171] op_sel_hi:[1,0,0]\n"
"v_pk_fma_f32 v[130:131], v[130:131], s[70:71], v[170:171] op_sel_hi:[1,0,0]\n"
"v_pk_fma_f32 v[132:133], v[132:133], s[70:71], v[170:171] op_sel_hi:[1,0,0]\n"
"v_pk_fma_f32 v[134:135], v[134:135], s[70:71], v[170:171] op_sel_hi:[1,0,0]\n"
"v_mfma_f32_16x16x32_bf16 v[204:207], a[192:195], v[48:51], v[204:207]\n"
"ds_read_b128 v[124:127], v180 offset:27008\n"
"ds_read_b128 v[140:143], v181 offset:27008\n"
"v_mfma_f32_16x16x32_bf16 v[204:207], a[204:207], v[52:55], v[204:207]\n"
"v_cvt_pk_bf16_f32 v214, v208, v209\n"
"v_cvt_pk_bf16_f32 v215, v210, v211\n"
"v_mfma_f32_16x16x32_bf16 v[208:211], a[148:151], v[32:35], 0\n"
"ds_write_b128 v186, v[212:215] offset:20384\n"
"v_mfma_f32_16x16x32_bf16 v[208:211], a[160:163], v[36:39], v[208:211]\n"
"v_exp_f32 v112, v112\n"
"v_mfma_f32_16x16x32_bf16 v[208:211], a[172:175], v[40:43], v[208:211]\n"
"v_exp_f32 v113, v113\n"
"v_mfma_f32_16x16x32_bf16 v[208:211], a[184:187], v[44:47], v[208:211]\n"
"v_exp_f32 v114, v114\n"
"v_mfma_f32_16x16x32_bf16 v[208:211], a[196:199], v[48:51], v[208:211]\n"
"v_exp_f32 v115, v115\n"
"v_mfma_f32_16x16x32_bf16 v[208:211], a[208:211], v[52:55], v[208:211]\n"
"v_cvt_pk_bf16_f32 v82, v112, v114\n"
"v_cvt_pk_bf16_f32 v83, v113, v115\n"
"v_mfma_f32_16x16x32_bf16 v[212:215], a[152:155], v[32:35], 0\n"
"ds_read_b128 v[32:35], v188\n"
"ds_read_b128 v[112:115], v178\n"
"v_mfma_f32_16x16x32_bf16 v[212:215], a[164:167], v[36:39], v[212:215]\n"
"s_waitcnt vmcnt(10) lgkmcnt(2)\n"
"ds_read_b128 v[36:39], v189\n"
"v_cvt_pk_bf16_f32 v144, v204, v205\n"
"v_mfma_f32_16x16x32_bf16 v[212:215], a[176:179], v[40:43], v[212:215]\n"
"s_barrier\n"
"v_mfma_f32_16x16x32_bf16 v[212:215], a[188:191], v[44:47], v[212:215]\n"
"s_add_u32 m0, s79, 142464\n"
"ds_read_b128 v[40:43], v188 offset:1024\n"
"buffer_load_dwordx4 v192, s[16:19], s43 offen lds\n"
"v_mfma_f32_16x16x32_bf16 v[212:215], a[200:203], v[48:51], v[212:215]\n"
"ds_read_b128 v[44:47], v189 offset:1024\n"
"ds_read_b128 v[48:51], v188 offset:2048\n"
"v_mfma_f32_16x16x32_bf16 v[212:215], a[212:215], v[52:55], v[212:215]\n"
"ds_read_b128 v[52:55], v189 offset:2048\n"
"v_exp_f32 v116, v116\n"
"v_exp_f32 v117, v117\n"
"v_exp_f32 v118, v118\n"
"v_exp_f32 v119, v119\n"
"v_cvt_pk_bf16_f32 v84, v116, v118\n"
"v_cvt_pk_bf16_f32 v85, v117, v119\n"
"v_cvt_pk_bf16_f32 v145, v206, v207\n"
"v_exp_f32 v128, v128\n"
"v_smfmac_f32_16x16x64_bf16 v[252:255], v[82:85], v[224:231], v63\n"
".Lic2%=:\n"
"s_cbranch_execz .Lib0%=\n"
"s_waitcnt lgkmcnt(6)\n"
"ds_read_b64_tr_b16 v[224:225], v184 offset:128\n"
"v_smfmac_f32_16x16x64_bf16 v[98:101], v[82:85], v[244:251], v63\n"
"ds_read_b64_tr_b16 v[226:227], v185 offset:128\n"
"v_smfmac_f32_16x16x64_bf16 v[106:109], v[82:85], a[248:255], v63\n"
"s_waitcnt lgkmcnt(4)\n"
"ds_read_b64_tr_b16 v[228:229], v184 offset:896\n"
"v_mfma_f32_16x16x32_bf16 v[82:85], v[172:175], v[82:85], 0\n"
"s_add_u32 m0, s79, 142464\n"
"ds_read_b64_tr_b16 v[230:231], v185 offset:896\n"
"buffer_load_dwordx4 v192, s[16:19], s43 offen offset:1024 lds\n"
"v_mfma_f32_16x16x32_bf16 v[204:207], a[0:3], v[32:35], 0\n"
"ds_read_b64_tr_b16 v[244:245], v184 offset:136\n"
"ds_read_b64_tr_b16 v[246:247], v185 offset:136\n"
"v_mfma_f32_16x16x32_bf16 v[204:207], a[12:15], v[36:39], v[204:207]\n"
"ds_read_b64_tr_b16 v[248:249], v184 offset:904\n"
"v_cvt_pk_bf16_f32 v146, v208, v209\n"
"v_mfma_f32_16x16x32_bf16 v[204:207], a[24:27], v[40:43], v[204:207]\n"
"ds_read_b64_tr_b16 v[250:251], v185 offset:904\n"
"v_cvt_pk_bf16_f32 v147, v210, v211\n"
"v_mfma_f32_16x16x32_bf16 v[204:207], a[36:39], v[44:47], v[204:207]\n"
"s_waitcnt lgkmcnt(9)\n"
"ds_read_b128 v[116:119], v178 offset:13504\n"
"v_cvt_pk_bf16_f32 v158, v212, v213\n"
"v_mfma_f32_16x16x32_bf16 v[204:207], a[48:51], v[48:51], v[204:207]\n"
"s_waitcnt lgkmcnt(9)\n"
"v_exp_f32 v129, v129\n"
"v_mfma_f32_16x16x32_bf16 v[204:207], a[60:63], v[52:55], v[204:207]\n"
"v_exp_f32 v130, v130\n"
"v_mfma_f32_16x16x32_bf16 v[208:211], a[4:7], v[32:35], 0\n"
"v_exp_f32 v131, v131\n"
"v_mfma_f32_16x16x32_bf16 v[208:211], a[16:19], v[36:39], v[208:211]\n"
"s_add_u32 m0, s79, 142464\n"
"ds_read_b64_tr_b16 a[240:241], v236\n"
"buffer_load_dwordx4 v192, s[16:19], s43 offen offset:2048 lds\n"
"v_mfma_f32_16x16x32_bf16 v[208:211], a[28:31], v[40:43], v[208:211]\n"
"s_add_u32 s43, s80, 12288\n"
"v_cvt_pk_bf16_f32 v90, v128, v130\n"
"v_cvt_pk_bf16_f32 v91, v129, v131\n"
"v_mfma_f32_16x16x32_bf16 v[208:211], a[40:43], v[44:47], v[208:211]\n"
"ds_read_b128 v[128:131], v179\n"
"v_cvt_pk_bf16_f32 v159, v214, v215\n"
"v_mfma_f32_16x16x32_bf16 v[208:211], a[52:55], v[48:51], v[208:211]\n"
"v_exp_f32 v132, v132\n"
"v_mfma_f32_16x16x32_bf16 v[208:211], a[64:67], v[52:55], v[208:211]\n"
"v_exp_f32 v133, v133\n"
"v_mfma_f32_16x16x32_bf16 v[212:215], a[8:11], v[32:35], 0\n"
"v_exp_f32 v134, v134\n"
"v_mfma_f32_16x16x32_bf16 v[212:215], a[20:23], v[36:39], v[212:215]\n"
"v_exp_f32 v135, v135\n"
"v_mfma_f32_16x16x32_bf16 v[212:215], a[32:35], v[40:43], v[212:215]\n"
"v_cvt_pk_bf16_f32 v92, v132, v134\n"
"v_cvt_pk_bf16_f32 v93, v133, v135\n"
"v_mfma_f32_16x16x32_bf16 v[212:215], a[44:47], v[44:47], v[212:215]\n"
"s_waitcnt lgkmcnt(3)\n"
"ds_read_b128 v[132:135], v179 offset:13504\n"
"ds_read_b64_tr_b16 a[242:243], v236 offset:6144\n"
"v_smfmac_f32_16x16x64_bf16 v[252:255], v[90:93], v[224:231], v63\n"
"s_add_u32 m0, s34, 0\n"
"s_nop 0\n"
"buffer_load_dwordx4 v190, s[8:11], s32 offen lds\n"
"v_smfmac_f32_16x16x64_bf16 v[98:101], v[90:93], v[244:251], v63\n"
"v_cvt_pk_bf16_f32 v204, v204, v205\n"
"v_smfmac_f32_16x16x64_bf16 v[106:109], v[90:93], v[24:31], v63\n"
"v_cvt_pk_bf16_f32 v205, v206, v207\n"
"v_mfma_f32_16x16x32_bf16 v[90:93], v[172:175], v[90:93], v[82:85]\n"
"v_cvt_pk_bf16_f32 v206, v208, v209\n"
"v_cvt_pk_bf16_f32 v252, v252, v253\n"
"v_mfma_f32_16x16x32_bf16 v[212:215], a[56:59], v[48:51], v[212:215]\n"
"ds_read_b64 v[82:83], v180 offset:13568\n"
"ds_read_b64 v[84:85], v181 offset:13568\n"
"v_mfma_f32_16x16x32_bf16 v[212:215], a[68:71], v[52:55], v[212:215]\n"
"v_cvt_pk_bf16_f32 v253, v254, v255\n"
"s_nop 0\n"
"v_rcp_f32 v171, v90\n"
"v_cvt_pk_bf16_f32 v98, v98, v99\n"
"v_cvt_pk_bf16_f32 v171, v171, v171\n"
"v_and_b32 v92, v171, v176\n"
"v_and_b32 v93, v171, v177\n"
"v_cvt_pk_bf16_f32 v99, v100, v101\n"
"v_cvt_pk_bf16_f32 v106, v106, v107\n"
"v_mfma_f32_16x16x16_bf16 v[252:255], v[252:253], v[92:93], 0\n"
"s_add_u32 m0, s34, 3072\n"
"ds_read_b64 v[90:91], v180 offset:27072\n"
"buffer_load_dwordx4 v191, s[8:11], s32 offen lds\n"
"v_mfma_f32_16x16x16_bf16 v[98:101], v[98:99], v[92:93], 0\n"
"s_add_u32 s32, s32, s33\n"
"v_cvt_pk_bf16_f32 v107, v108, v109\n"
"v_cvt_pk_bf16_f32 v207, v210, v211\n"
"v_mfma_f32_16x16x32_bf16 v[208:211], a[80:83], v[32:35], 0\n"
"ds_write_b128 v186, v[204:207] offset:33760\n"
"v_mfma_f32_16x16x16_bf16 v[106:109], v[106:107], v[92:93], 0\n"
"s_waitcnt lgkmcnt(2)\n"
"buffer_load_dword v237, v238, s[8:11], s78 offen\n"
"v_cvt_pk_bf16_f32 v252, v252, v253\n"
"v_mfma_f32_16x16x32_bf16 v[120:123], v[120:123], v[148:151], 0\n"
"s_add_u32 s78, s78, s33\n"
"v_cvt_pk_bf16_f32 v253, v254, v255\n"
"v_cvt_pk_bf16_f32 v254, v98, v99\n"
"v_mfma_f32_16x16x32_bf16 v[120:123], v[80:83], v[160:163], v[120:123]\n"
"ds_read_b64 v[92:93], v181 offset:27072\n"
"ds_read_b64 v[98:99], v178 offset:64\n"
"v_mfma_f32_16x16x32_bf16 v[136:139], v[136:139], v[148:151], 0\n"
"s_waitcnt lgkmcnt(3)\n"
"v_cvt_pk_bf16_f32 v255, v100, v101\n"
"v_cvt_pk_bf16_f32 v106, v106, v107\n"
"v_mfma_f32_16x16x32_bf16 v[136:139], v[84:87], v[162:165], v[136:139]\n"
"s_add_u32 m0, s34, 1024\n"
"buffer_store_dwordx4 v[252:255], v192, s[28:31], s37 offen sc1\n"
"v_mfma_f32_16x16x32_bf16 v[124:127], v[124:127], v[148:151], 0\n"
"ds_read_b64 v[100:101], v179 offset:64\n"
"v_cvt_pk_bf16_f32 v107, v108, v109\n"
"v_mfma_f32_16x16x32_bf16 v[124:127], v[88:91], v[160:163], v[124:127]\n"
"ds_read_b64 v[108:109], v179 offset:13568\n"
"ds_read_b64_tr_b16 a[244:245], v236 offset:8\n"
"v_mfma_f32_16x16x32_bf16 v[208:211], a[92:95], v[36:39], v[208:211]\n"
"buffer_load_dwordx4 v190, s[8:11], s0 offen lds\n"
"v_cvt_pk_bf16_f32 v212, v212, v213\n"
"v_mfma_f32_16x16x32_bf16 v[208:211], a[104:107], v[40:43], v[208:211]\n"
"s_waitcnt lgkmcnt(4)\n"
"v_cvt_pk_bf16_f32 v213, v214, v215\n"
"v_max_f32 v120, v120, v136\n"
"v_mfma_f32_16x16x32_bf16 v[140:143], v[140:143], v[148:151], 0\n"
"s_waitcnt lgkmcnt(3)\n"
"ds_read_b64_tr_b16 a[246:247], v236 offset:6152\n"
"v_max_f32 v121, v121, v137\n"
"v_mfma_f32_16x16x32_bf16 v[140:143], v[92:95], v[162:165], v[140:143]\n"
"ds_read_b64_tr_b16 v[136:137], v182 offset:72\n"
"v_max_f32 v122, v122, v138\n"
"v_mfma_f32_16x16x32_bf16 v[112:115], v[112:115], v[148:151], 0\n"
"buffer_store_dwordx2 v[106:107], v193, s[28:31], s37 offen offset:1024 sc1\n"
"v_mfma_f32_16x16x32_bf16 v[112:115], v[96:99], v[160:163], v[112:115]\n"
"s_waitcnt lgkmcnt(4)\n"
"ds_read_b64 v[106:107], v178 offset:13568\n"
"v_max_f32 v123, v123, v139\n"
"v_mfma_f32_16x16x32_bf16 v[128:131], v[128:131], v[148:151], 0\n"
"s_waitcnt lgkmcnt(4)\n"
"ds_read_b64_tr_b16 v[138:139], v183 offset:72\n"
"v_max3_f32 v170, v120, v121, v122\n"
"v_mfma_f32_16x16x32_bf16 v[128:131], v[100:103], v[162:165], v[128:131]\n"
"s_add_u32 m0, s34, 4096\n"
"v_max_f32 v124, v124, v140\n"
"v_max_f32 v125, v125, v141\n"
"v_mfma_f32_16x16x32_bf16 v[132:135], v[132:135], v[148:151], 0\n"
"s_add_u32 s37, s37, 1572864\n"
"buffer_load_dwordx4 v191, s[8:11], s0 offen lds\n"
"ds_read_b64_tr_b16 v[140:141], v182 offset:840\n"
"v_mfma_f32_16x16x32_bf16 v[132:135], v[108:111], v[162:165], v[132:135]\n"
"s_add_u32 s0, s0, s33\n"
"ds_read_b64_tr_b16 a[248:249], v235\n"
"v_max_f32 v126, v126, v142\n"
"v_mfma_f32_16x16x32_bf16 v[208:211], a[116:119], v[44:47], v[208:211]\n"
"ds_read_b64_tr_b16 a[250:251], v235 offset:6144\n"
"v_max_f32 v127, v127, v143\n"
"v_mfma_f32_16x16x32_bf16 v[208:211], a[128:131], v[48:51], v[208:211]\n"
"v_max3_f32 v170, v170, v123, v124\n"
"v_max3_f32 v170, v170, v125, v126\n"
"v_mfma_f32_16x16x32_bf16 v[208:211], a[140:143], v[52:55], v[208:211]\n"
"s_waitcnt lgkmcnt(4)\n"
"buffer_load_dword v200, off, s[28:31], s39 sc1\n"
"ds_read_b64_tr_b16 v[142:143], v183 offset:840\n"
"v_mfma_f32_16x16x32_bf16 v[116:119], v[116:119], v[148:151], 0\n"
"v_max_f32 v112, v112, v128\n"
"v_max_f32 v113, v113, v129\n"
"v_mfma_f32_16x16x32_bf16 v[116:119], v[104:107], v[160:163], v[116:119]\n"
"v_max_f32 v114, v114, v130\n"
"v_cvt_pk_bf16_f32 v214, v208, v209\n"
"v_mfma_f32_16x16x32_bf16 v[204:207], a[72:75], v[32:35], 0\n"
"v_cvt_pk_bf16_f32 v215, v210, v211\n"
"v_max_f32 v115, v115, v131\n"
"v_mfma_f32_16x16x32_bf16 v[204:207], a[84:87], v[36:39], v[204:207]\n"
"s_add_u32 m0, s34, 2048\n"
"ds_write_b128 v186, v[212:215] offset:33824\n"
"v_mfma_f32_16x16x32_bf16 v[204:207], a[96:99], v[40:43], v[204:207]\n"
"buffer_load_dwordx4 v190, s[8:11], s1 offen lds\n"
"v_max3_f32 v170, v170, v127, v112\n"
"v_mfma_f32_16x16x32_bf16 v[204:207], a[108:111], v[44:47], v[204:207]\n"
"v_max3_f32 v170, v170, v113, v114\n"
"v_max_f32 v116, v116, v132\n"
"v_mfma_f32_16x16x32_bf16 v[204:207], a[120:123], v[48:51], v[204:207]\n"
"v_max_f32 v117, v117, v133\n"
"v_max_f32 v118, v118, v134\n"
"v_mfma_f32_16x16x32_bf16 v[204:207], a[132:135], v[52:55], v[204:207]\n"
"v_max_f32 v119, v119, v135\n"
"v_max3_f32 v170, v170, v115, v116\n"
"v_mfma_f32_16x16x32_bf16 v[208:211], a[76:79], v[32:35], 0\n"
"v_max3_f32 v170, v170, v117, v118\n"
"v_mov_b64 v[252:253], 0\n"
"v_mfma_f32_16x16x32_bf16 v[208:211], a[88:91], v[36:39], v[208:211]\n"
"v_mov_b64 v[254:255], 0\n"
"v_mov_b64 v[98:99], 0\n"
"v_mfma_f32_16x16x32_bf16 v[208:211], a[100:103], v[40:43], v[208:211]\n"
"v_cvt_pk_bf16_f32 v204, v204, v205\n"
"v_cvt_pk_bf16_f32 v205, v206, v207\n"
"v_mfma_f32_16x16x32_bf16 v[208:211], a[112:115], v[44:47], v[208:211]\n"
"v_mov_b64 v[100:101], 0\n"
"v_mov_b64 v[106:107], 0\n"
"v_mfma_f32_16x16x32_bf16 v[208:211], a[124:127], v[48:51], v[208:211]\n"
"s_add_u32 m0, s34, 5120\n"
"ds_read_b128 v[128:131], v180 offset:27008\n"
"buffer_load_dwordx4 v191, s[8:11], s1 offen lds\n"
"v_mfma_f32_16x16x32_bf16 v[208:211], a[136:139], v[52:55], v[208:211]\n"
"s_add_u32 s1, s1, s33\n"
"v_mov_b64 v[108:109], 0\n"
"v_max_f32 v171, v170, v119\n"
"v_mfma_f32_16x16x32_bf16 v[212:215], a[144:147], v[32:35], 0\n"
"v_max_f32 v170, v170, v119\n"
"s_mov_b64 exec, s[46:47]\n"
"v_add_u32 v185, 0xc0, v185\n"
"s_mov_b64 exec, -1\n"
"v_mfma_f32_16x16x32_bf16 v[212:215], a[156:159], v[36:39], v[212:215]\n"
"v_permlane16_swap_b32 v170, v171\n"
"v_mfma_f32_16x16x32_bf16 v[212:215], a[168:171], v[40:43], v[212:215]\n"
"v_max_f32 v170, v170, v171\n"
"v_mov_b32 v171, v170\n"
"v_mfma_f32_16x16x32_bf16 v[212:215], a[180:183], v[44:47], v[212:215]\n"
"s_nop 0\n"
"v_permlane32_swap_b32 v170, v171\n"
"v_max_f32 v170, v170, v171\n"
"v_mul_f32 v170, 0xbe553b95, v170\n"
"v_pk_fma_f32 v[120:121], v[120:121], s[70:71], v[170:171] op_sel_hi:[1,0,0]\n"
"v_pk_fma_f32 v[122:123], v[122:123], s[70:71], v[170:171] op_sel_hi:[1,0,0]\n"
"v_pk_fma_f32 v[124:125], v[124:125], s[70:71], v[170:171] op_sel_hi:[1,0,0]\n"
"v_pk_fma_f32 v[126:127], v[126:127], s[70:71], v[170:171] op_sel_hi:[1,0,0]\n"
"v_pk_fma_f32 v[112:113], v[112:113], s[70:71], v[170:171] op_sel_hi:[1,0,0]\n"
"v_pk_fma_f32 v[114:115], v[114:115], s[70:71], v[170:171] op_sel_hi:[1,0,0]\n"
"v_pk_fma_f32 v[116:117], v[116:117], s[70:71], v[170:171] op_sel_hi:[1,0,0]\n"
"v_pk_fma_f32 v[118:119], v[118:119], s[70:71], v[170:171] op_sel_hi:[1,0,0]\n"
"v_mfma_f32_16x16x32_bf16 v[212:215], a[192:195], v[48:51], v[212:215]\n"
"ds_read_b128 v[132:135], v178\n"
"v_cvt_pk_bf16_f32 v206, v208, v209\n"
"v_mfma_f32_16x16x32_bf16 v[212:215], a[204:207], v[52:55], v[212:215]\n"
"v_cvt_pk_bf16_f32 v207, v210, v211\n"
"v_exp_f32 v120, v120\n"
"v_mfma_f32_16x16x32_bf16 v[208:211], a[148:151], v[32:35], 0\n"
"ds_write_b128 v186, v[204:207] offset:33888\n"
"v_mfma_f32_16x16x32_bf16 v[208:211], a[160:163], v[36:39], v[208:211]\n"
"v_exp_f32 v121, v121\n"
"v_mfma_f32_16x16x32_bf16 v[208:211], a[172:175], v[40:43], v[208:211]\n"
"v_exp_f32 v122, v122\n"
"v_mfma_f32_16x16x32_bf16 v[208:211], a[184:187], v[44:47], v[208:211]\n"
"v_exp_f32 v123, v123\n"
"v_mfma_f32_16x16x32_bf16 v[208:211], a[196:199], v[48:51], v[208:211]\n"
"v_cvt_pk_bf16_f32 v82, v120, v122\n"
"v_cvt_pk_bf16_f32 v83, v121, v123\n"
"v_mfma_f32_16x16x32_bf16 v[208:211], a[208:211], v[52:55], v[208:211]\n"
"ds_read_b128 v[120:123], v181 offset:27008\n"
"v_cvt_pk_bf16_f32 v148, v212, v213\n"
"v_mfma_f32_16x16x32_bf16 v[204:207], a[152:155], v[32:35], 0\n"
"s_waitcnt vmcnt(21)\n"
"ds_read_b128 v[32:35], v188 offset:6144\n"
"v_cvt_pk_bf16_f32 v149, v214, v215\n"
"v_mfma_f32_16x16x32_bf16 v[204:207], a[164:167], v[36:39], v[204:207]\n"
"s_waitcnt vmcnt(10) lgkmcnt(2)\n"
"ds_read_b128 v[36:39], v189 offset:6144\n"
"v_exp_f32 v124, v124\n"
"v_mfma_f32_16x16x32_bf16 v[204:207], a[176:179], v[40:43], v[204:207]\n"
"s_barrier\n"
"v_mfma_f32_16x16x32_bf16 v[204:207], a[188:191], v[44:47], v[204:207]\n"
"s_add_u32 m0, s79, 130176\n"
"ds_read_b128 v[40:43], v188 offset:7168\n"
"buffer_load_dwordx4 v192, s[16:19], s43 offen lds\n"
"v_mfma_f32_16x16x32_bf16 v[204:207], a[200:203], v[48:51], v[204:207]\n"
"ds_read_b128 v[44:47], v189 offset:7168\n"
"ds_read_b128 v[48:51], v188 offset:8192\n"
"v_mfma_f32_16x16x32_bf16 v[204:207], a[212:215], v[52:55], v[204:207]\n"
"ds_read_b128 v[52:55], v189 offset:8192\n"
"v_exp_f32 v125, v125\n"
"v_exp_f32 v126, v126\n"
"v_exp_f32 v127, v127\n"
"v_cvt_pk_bf16_f32 v84, v124, v126\n"
"v_cvt_pk_bf16_f32 v85, v125, v127\n"
"v_exp_f32 v112, v112\n"
"s_nop 0\n"
"v_smfmac_f32_16x16x64_bf16 v[252:255], v[82:85], v[216:223], v63\n"
"s_waitcnt lgkmcnt(5)\n"
"ds_read_b64_tr_b16 v[216:217], v182 offset:128\n"
"v_smfmac_f32_16x16x64_bf16 v[98:101], v[82:85], v[72:79], v63\n"
"ds_read_b64_tr_b16 v[218:219], v183 offset:128\n"
"v_smfmac_f32_16x16x64_bf16 v[106:109], v[82:85], v[8:15], v63\n"
"ds_read_b64_tr_b16 v[220:221], v182 offset:896\n"
"v_mfma_f32_16x16x32_bf16 v[82:85], v[172:175], v[82:85], 0\n"
"s_waitcnt lgkmcnt(5)\n"
"ds_read_b64_tr_b16 v[222:223], v183 offset:896\n"
"ds_read_b64_tr_b16 v[72:73], v182 offset:136\n"
"v_mfma_f32_16x16x32_bf16 v[212:215], a[0:3], v[32:35], 0\n"
"s_add_u32 m0, s79, 130176\n"
"ds_read_b64_tr_b16 v[74:75], v183 offset:136\n"
"buffer_load_dwordx4 v192, s[16:19], s43 offen offset:1024 lds\n"
"v_mfma_f32_16x16x32_bf16 v[212:215], a[12:15], v[36:39], v[212:215]\n"
"ds_read_b64_tr_b16 v[76:77], v182 offset:904\n"
"v_cvt_pk_bf16_f32 v150, v208, v209\n"
"v_mfma_f32_16x16x32_bf16 v[212:215], a[24:27], v[40:43], v[212:215]\n"
"ds_read_b64_tr_b16 v[78:79], v183 offset:904\n"
"v_cvt_pk_bf16_f32 v151, v210, v211\n"
"v_mfma_f32_16x16x32_bf16 v[212:215], a[36:39], v[44:47], v[212:215]\n"
"s_waitcnt lgkmcnt(9)\n"
"ds_read_b128 v[124:127], v179\n"
"v_exp_f32 v113, v113\n"
"v_mfma_f32_16x16x32_bf16 v[212:215], a[48:51], v[48:51], v[212:215]\n"
"s_waitcnt lgkmcnt(9)\n"
"v_exp_f32 v114, v114\n"
"v_mfma_f32_16x16x32_bf16 v[212:215], a[60:63], v[52:55], v[212:215]\n"
"v_exp_f32 v115, v115\n"
"v_mfma_f32_16x16x32_bf16 v[208:211], a[4:7], v[32:35], 0\n"
"v_exp_f32 v116, v116\n"
"v_mfma_f32_16x16x32_bf16 v[208:211], a[16:19], v[36:39], v[208:211]\n"
"v_exp_f32 v117, v117\n"
"v_mfma_f32_16x16x32_bf16 v[208:211], a[28:31], v[40:43], v[208:211]\n"
"s_add_u32 m0, s79, 130176\n"
"v_exp_f32 v118, v118\n"
"v_mfma_f32_16x16x32_bf16 v[208:211], a[40:43], v[44:47], v[208:211]\n"
"buffer_load_dwordx4 v192, s[16:19], s43 offen offset:2048 lds\n"
"v_cvt_pk_bf16_f32 v90, v112, v114\n"
"v_mfma_f32_16x16x32_bf16 v[208:211], a[52:55], v[48:51], v[208:211]\n"
"s_add_u32 s43, s80, 49152\n"
"v_exp_f32 v119, v119\n"
"v_mfma_f32_16x16x32_bf16 v[208:211], a[64:67], v[52:55], v[208:211]\n"
"v_cvt_pk_bf16_f32 v91, v113, v115\n"
"v_cvt_pk_bf16_f32 v92, v116, v118\n"
"v_cvt_pk_bf16_f32 v93, v117, v119\n"
"v_cvt_pk_bf16_f32 v162, v204, v205\n"
"v_cvt_pk_bf16_f32 v163, v206, v207\n"
"v_smfmac_f32_16x16x64_bf16 v[106:109], v[90:93], v[136:143], v63\n"
"s_waitcnt lgkmcnt(1)\n"
"ds_read_b128 v[112:115], v178 offset:13504\n"
"v_smfmac_f32_16x16x64_bf16 v[252:255], v[90:93], v[216:223], v63\n"
"ds_read_b128 v[136:139], v179 offset:13504\n"
"v_smfmac_f32_16x16x64_bf16 v[98:101], v[90:93], v[72:79], v63\n"
"ds_read_b128 v[116:119], v178 offset:27008\n"
"v_mfma_f32_16x16x32_bf16 v[90:93], v[172:175], v[90:93], v[82:85]\n"
"ds_read_b128 v[140:143], v179 offset:27008\n"
"v_cvt_pk_bf16_f32 v106, v106, v107\n"
"v_mfma_f32_16x16x32_bf16 v[204:207], a[8:11], v[32:35], 0\n"
"s_add_u32 m0, s34, 6144\n"
"ds_read_b64 v[82:83], v180 offset:27072\n"
"buffer_load_dwordx4 v190, s[8:11], s32 offen lds\n"
"v_mfma_f32_16x16x32_bf16 v[204:207], a[20:23], v[36:39], v[204:207]\n"
"ds_read_b64 v[84:85], v181 offset:27072\n"
"v_cvt_pk_bf16_f32 v252, v252, v253\n"
"v_mfma_f32_16x16x32_bf16 v[204:207], a[32:35], v[40:43], v[204:207]\n"
"v_rcp_f32 v171, v90\n"
"v_mfma_f32_16x16x32_bf16 v[204:207], a[44:47], v[44:47], v[204:207]\n"
"ds_read_b64 v[90:91], v178 offset:64\n"
"v_cvt_pk_bf16_f32 v171, v171, v171\n"
"v_mfma_f32_16x16x32_bf16 v[204:207], a[56:59], v[48:51], v[204:207]\n"
"v_and_b32 v92, v171, v176\n"
"v_and_b32 v93, v171, v177\n"
"v_mfma_f32_16x16x32_bf16 v[204:207], a[68:71], v[52:55], v[204:207]\n"
"v_cvt_pk_bf16_f32 v253, v254, v255\n"
"v_cvt_pk_bf16_f32 v98, v98, v99\n"
"v_cvt_pk_bf16_f32 v107, v108, v109\n"
"v_mfma_f32_16x16x16_bf16 v[252:255], v[252:253], v[92:93], 0\n"
"v_cvt_pk_bf16_f32 v99, v100, v101\n"
"v_cvt_pk_bf16_f32 v212, v212, v213\n"
"v_mfma_f32_16x16x16_bf16 v[106:109], v[106:107], v[92:93], 0\n"
"s_waitcnt lgkmcnt(2)\n"
"ds_read_b64_tr_b16 a[252:253], v236 offset:12288\n"
"buffer_load_dword v237, v238, s[8:11], s78 offen\n"
"v_mfma_f32_16x16x16_bf16 v[98:101], v[98:99], v[92:93], 0\n"
"s_waitcnt lgkmcnt(2)\n"
"ds_read_b64 v[92:93], v179 offset:64\n"
"v_cvt_pk_bf16_f32 v252, v252, v253\n"
"v_mfma_f32_16x16x32_bf16 v[128:131], v[128:131], v[152:155], 0\n"
"s_add_u32 m0, s34, 9216\n"
"v_cvt_pk_bf16_f32 v253, v254, v255\n"
"v_cvt_pk_bf16_f32 v106, v106, v107\n"
"v_mfma_f32_16x16x32_bf16 v[128:131], v[80:83], v[164:167], v[128:131]\n"
"s_waitcnt lgkmcnt(2)\n"
"v_cvt_pk_bf16_f32 v254, v98, v99\n"
"v_cvt_pk_bf16_f32 v255, v100, v101\n"
"v_mfma_f32_16x16x32_bf16 v[120:123], v[120:123], v[152:155], 0\n"
"s_add_u32 s78, s78, s33\n"
"buffer_load_dwordx4 v191, s[8:11], s32 offen lds\n"
"ds_read_b64 v[98:99], v178 offset:13568\n"
"v_mfma_f32_16x16x32_bf16 v[120:123], v[84:87], v[166:169], v[120:123]\n"
"s_add_u32 s32, s32, s33\n"
"ds_read_b64 v[100:101], v179 offset:13568\n"
"v_cvt_pk_bf16_f32 v107, v108, v109\n"
"v_mfma_f32_16x16x32_bf16 v[132:135], v[132:135], v[152:155], 0\n"
"ds_read_b64 v[108:109], v179 offset:27072\n"
"ds_read_b64_tr_b16 a[254:255], v236 offset:18432\n"
"v_mfma_f32_16x16x32_bf16 v[132:135], v[88:91], v[164:167], v[132:135]\n"
"v_cvt_pk_bf16_f32 v213, v214, v215\n"
"v_cvt_pk_bf16_f32 v214, v208, v209\n"
"v_cvt_pk_bf16_f32 v215, v210, v211\n"
"v_mfma_f32_16x16x32_bf16 v[208:211], a[80:83], v[32:35], 0\n"
"buffer_store_dwordx4 v[252:255], v192, s[28:31], s37 offen sc1\n"
"v_mfma_f32_16x16x32_bf16 v[208:211], a[92:95], v[36:39], v[208:211]\n"
"s_waitcnt lgkmcnt(4)\n"
"ds_write_b128 v186, v[212:215] offset:47264\n"
"v_mfma_f32_16x16x32_bf16 v[124:127], v[124:127], v[152:155], 0\n"
"s_waitcnt lgkmcnt(4)\n"
"buffer_store_dwordx2 v[106:107], v193, s[28:31], s37 offen offset:1024 sc1\n"
"v_mfma_f32_16x16x32_bf16 v[124:127], v[92:95], v[166:169], v[124:127]\n"
"s_waitcnt lgkmcnt(2)\n"
"ds_read_b64 v[106:107], v178 offset:27072\n"
"ds_read_b64_tr_b16 v[0:1], v236 offset:12296\n"
"v_mfma_f32_16x16x32_bf16 v[112:115], v[112:115], v[152:155], 0\n"
"s_add_u32 m0, s34, 7168\n"
"v_cvt_pk_bf16_f32 v204, v204, v205\n"
"v_cvt_pk_bf16_f32 v205, v206, v207\n"
"v_mfma_f32_16x16x32_bf16 v[112:115], v[96:99], v[164:167], v[112:115]\n"
"s_add_u32 s37, s37, 1572864\n"
"ds_read_b64_tr_b16 v[2:3], v236 offset:18440\n"
"buffer_load_dwordx4 v190, s[8:11], s0 offen lds\n"
"v_mfma_f32_16x16x32_bf16 v[136:139], v[136:139], v[152:155], 0\n"
"v_max_f32 v128, v128, v120\n"
"v_max_f32 v129, v129, v121\n"
"v_mfma_f32_16x16x32_bf16 v[136:139], v[100:103], v[166:169], v[136:139]\n"
"ds_read_b64_tr_b16 v[120:121], v182 offset:13576\n"
"v_max_f32 v130, v130, v122\n"
"v_mfma_f32_16x16x32_bf16 v[140:143], v[140:143], v[152:155], 0\n"
"v_max_f32 v131, v131, v123\n"
"v_max3_f32 v170, v128, v129, v130\n"
"v_mfma_f32_16x16x32_bf16 v[140:143], v[108:111], v[166:169], v[140:143]\n"
"ds_read_b64_tr_b16 v[122:123], v183 offset:13576\n"
"v_max_f32 v132, v132, v124\n"
"v_mfma_f32_16x16x32_bf16 v[208:211], a[104:107], v[40:43], v[208:211]\n"
"s_waitcnt lgkmcnt(4)\n"
"v_max_f32 v133, v133, v125\n"
"v_max_f32 v134, v134, v126\n"
"v_mfma_f32_16x16x32_bf16 v[208:211], a[116:119], v[44:47], v[208:211]\n"
"ds_read_b64_tr_b16 v[124:125], v182 offset:14344\n"
"ds_read_b64_tr_b16 v[4:5], v235 offset:12288\n"
"v_mfma_f32_16x16x32_bf16 v[208:211], a[128:131], v[48:51], v[208:211]\n"
"v_max_f32 v135, v135, v127\n"
"v_max3_f32 v170, v170, v131, v132\n"
"v_mfma_f32_16x16x32_bf16 v[208:211], a[140:143], v[52:55], v[208:211]\n"
"s_add_u32 m0, s34, 10240\n"
"ds_read_b64_tr_b16 v[126:127], v183 offset:14344\n"
"buffer_load_dwordx4 v191, s[8:11], s0 offen lds\n"
"v_mfma_f32_16x16x32_bf16 v[116:119], v[116:119], v[152:155], 0\n"
"s_add_u32 s0, s0, s33\n"
"ds_read_b64_tr_b16 v[6:7], v235 offset:18432\n"
"v_max3_f32 v170, v170, v133, v134\n"
"v_mfma_f32_16x16x32_bf16 v[116:119], v[104:107], v[164:167], v[116:119]\n"
"v_cvt_pk_bf16_f32 v206, v208, v209\n"
"v_cvt_pk_bf16_f32 v207, v210, v211\n"
"v_mfma_f32_16x16x32_bf16 v[212:215], a[72:75], v[32:35], 0\n"
"ds_write_b128 v186, v[204:207] offset:47328\n"
"v_mfma_f32_16x16x32_bf16 v[212:215], a[84:87], v[36:39], v[212:215]\n"
"s_waitcnt vmcnt(12)\n"
"v_add3_u32 v200, v200, s50, -2\n"
"v_cmp_lt_u32 vcc, 64, v200\n"
"v_mfma_f32_16x16x32_bf16 v[212:215], a[96:99], v[40:43], v[212:215]\n"
"v_max_f32 v112, v112, v136\n"
"v_max_f32 v113, v113, v137\n"
"v_mfma_f32_16x16x32_bf16 v[212:215], a[108:111], v[44:47], v[212:215]\n"
"s_nop 0\n"
"s_cbranch_vccnz .Lp1%=\n"
".Lr1%=:\n"
"v_mfma_f32_16x16x32_bf16 v[212:215], a[120:123], v[48:51], v[212:215]\n"
"s_add_u32 m0, s34, 8192\n"
"buffer_load_dword v201, off, s[28:31], s40 offset:128 sc1\n"
"v_max_f32 v114, v114, v138\n"
"v_mfma_f32_16x16x32_bf16 v[212:215], a[132:135], v[52:55], v[212:215]\n"
"buffer_load_dwordx4 v190, s[8:11], s1 offen lds\n"
"v_max_f32 v115, v115, v139\n"
"v_mfma_f32_16x16x32_bf16 v[208:211], a[76:79], v[32:35], 0\n"
"v_max3_f32 v170, v170, v135, v112\n"
"v_max3_f32 v170, v170, v113, v114\n"
"v_mfma_f32_16x16x32_bf16 v[208:211], a[88:91], v[36:39], v[208:211]\n"
"v_max_f32 v116, v116, v140\n"
"v_max_f32 v117, v117, v141\n"
"v_mfma_f32_16x16x32_bf16 v[208:211], a[100:103], v[40:43], v[208:211]\n"
"v_max_f32 v118, v118, v142\n"
"v_max_f32 v119, v119, v143\n"
"v_mfma_f32_16x16x32_bf16 v[208:211], a[112:115], v[44:47], v[208:211]\n"
"buffer_load_dwordx4 v[56:59], v192, s[28:31], s38 offen sc1\n"
"v_max3_f32 v170, v170, v115, v116\n"
"v_mfma_f32_16x16x32_bf16 v[208:211], a[124:127], v[48:51], v[208:211]\n"
"v_max3_f32 v170, v170, v117, v118\n"
"v_mov_b64 v[252:253], 0\n"
"v_mfma_f32_16x16x32_bf16 v[208:211], a[136:139], v[52:55], v[208:211]\n"
"v_mov_b64 v[254:255], 0\n"
"v_mov_b64 v[98:99], 0\n"
"v_mfma_f32_16x16x32_bf16 v[204:207], a[144:147], v[32:35], 0\n"
"v_cvt_pk_bf16_f32 v212, v212, v213\n"
"v_mov_b64 v[100:101], 0\n"
"v_mfma_f32_16x16x32_bf16 v[204:207], a[156:159], v[36:39], v[204:207]\n"
"s_add_u32 m0, s34, 11264\n"
"buffer_load_dwordx2 v[60:61], v193, s[28:31], s38 offen offset:1024 sc1\n"
"v_cvt_pk_bf16_f32 v213, v214, v215\n"
"v_mfma_f32_16x16x32_bf16 v[204:207], a[168:171], v[40:43], v[204:207]\n"
"s_add_u32 s38, s38, 1572864\n"
"v_mov_b64 v[106:107], 0\n"
"v_mov_b64 v[108:109], 0\n"
"v_mfma_f32_16x16x32_bf16 v[204:207], a[180:183], v[44:47], v[204:207]\n"
"v_max_f32 v171, v170, v119\n"
"v_max_f32 v170, v170, v119\n"
"v_mfma_f32_16x16x32_bf16 v[204:207], a[192:195], v[48:51], v[204:207]\n"
"s_nop 0\n"
"v_permlane16_swap_b32 v170, v171\n"
"v_mfma_f32_16x16x32_bf16 v[204:207], a[204:207], v[52:55], v[204:207]\n"
"buffer_load_dwordx4 v191, s[8:11], s1 offen lds\n"
"s_add_u32 s1, s1, s33\n"
"v_max_f32 v170, v170, v171\n"
"v_mov_b32 v171, v170\n"
"v_cvt_pk_bf16_f32 v214, v208, v209\n"
"v_cvt_pk_bf16_f32 v215, v210, v211\n"
"v_mfma_f32_16x16x32_bf16 v[208:211], a[148:151], v[32:35], 0\n"
"ds_write_b128 v186, v[212:215] offset:47392\n"
"v_mfma_f32_16x16x32_bf16 v[208:211], a[160:163], v[36:39], v[208:211]\n"
"v_permlane32_swap_b32 v170, v171\n"
"v_max_f32 v170, v170, v171\n"
"v_mul_f32 v170, 0xbe553b95, v170\n"
"v_pk_fma_f32 v[128:129], v[128:129], s[70:71], v[170:171] op_sel_hi:[1,0,0]\n"
"v_pk_fma_f32 v[130:131], v[130:131], s[70:71], v[170:171] op_sel_hi:[1,0,0]\n"
"v_pk_fma_f32 v[132:133], v[132:133], s[70:71], v[170:171] op_sel_hi:[1,0,0]\n"
"v_pk_fma_f32 v[134:135], v[134:135], s[70:71], v[170:171] op_sel_hi:[1,0,0]\n"
"v_pk_fma_f32 v[112:113], v[112:113], s[70:71], v[170:171] op_sel_hi:[1,0,0]\n"
"v_pk_fma_f32 v[114:115], v[114:115], s[70:71], v[170:171] op_sel_hi:[1,0,0]\n"
"v_pk_fma_f32 v[116:117], v[116:117], s[70:71], v[170:171] op_sel_hi:[1,0,0]\n"
"v_pk_fma_f32 v[118:119], v[118:119], s[70:71], v[170:171] op_sel_hi:[1,0,0]\n"
"v_mfma_f32_16x16x32_bf16 v[208:211], a[172:175], v[40:43], v[208:211]\n"
"ds_read_b128 v[136:139], v178\n"
"ds_read_b128 v[140:143], v178 offset:13504\n"
"v_mfma_f32_16x16x32_bf16 v[208:211], a[184:187], v[44:47], v[208:211]\n"
"v_exp_f32 v128, v128\n"
"v_mfma_f32_16x16x32_bf16 v[208:211], a[196:199], v[48:51], v[208:211]\n"
"v_exp_f32 v129, v129\n"
"v_mfma_f32_16x16x32_bf16 v[208:211], a[208:211], v[52:55], v[208:211]\n"
"s_waitcnt vmcnt(12) lgkmcnt(2)\n"
"v_exp_f32 v130, v130\n"
"v_mfma_f32_16x16x32_bf16 v[212:215], a[152:155], v[32:35], 0\n"
"s_barrier\n"
"v_mfma_f32_16x16x32_bf16 v[212:215], a[164:167], v[36:39], v[212:215]\n"
"s_add_u32 m0, s79, 142464\n"
"ds_read_b128 v[32:35], v188\n"
"buffer_load_dwordx4 v192, s[16:19], s43 offen lds\n"
"v_mfma_f32_16x16x32_bf16 v[212:215], a[176:179], v[40:43], v[212:215]\n"
"ds_read_b128 v[36:39], v189\n"
"ds_read_b128 v[40:43], v188 offset:1024\n"
"v_mfma_f32_16x16x32_bf16 v[212:215], a[188:191], v[44:47], v[212:215]\n"
"ds_read_b128 v[44:47], v189 offset:1024\n"
"v_cvt_pk_bf16_f32 v82, v128, v130\n"
"v_mfma_f32_16x16x32_bf16 v[212:215], a[200:203], v[48:51], v[212:215]\n"
"ds_read_b128 v[48:51], v188 offset:2048\n"
"v_cvt_pk_bf16_f32 v152, v204, v205\n"
"v_mfma_f32_16x16x32_bf16 v[212:215], a[212:215], v[52:55], v[212:215]\n"
"ds_read_b128 v[52:55], v189 offset:2048\n"
"v_exp_f32 v131, v131\n"
"v_exp_f32 v132, v132\n"
"v_cvt_pk_bf16_f32 v83, v129, v131\n"
"v_exp_f32 v133, v133\n"
"v_exp_f32 v134, v134\n"
"v_exp_f32 v135, v135\n"
"v_cvt_pk_bf16_f32 v84, v132, v134\n"
"s_add_u32 m0, s79, 142464\n"
"v_cvt_pk_bf16_f32 v85, v133, v135\n"
"v_cvt_pk_bf16_f32 v153, v206, v207\n"
"v_exp_f32 v112, v112\n"
"v_smfmac_f32_16x16x64_bf16 v[252:255], v[82:85], v[224:231], v63\n"
"s_waitcnt lgkmcnt(5)\n"
"buffer_load_dwordx4 v192, s[16:19], s43 offen offset:1024 lds\n"
"v_smfmac_f32_16x16x64_bf16 v[98:101], v[82:85], v[244:251], v63\n"
"ds_read_b64_tr_b16 v[224:225], v182 offset:13632\n"
"v_smfmac_f32_16x16x64_bf16 v[106:109], v[82:85], v[24:31], v63\n"
"ds_read_b64_tr_b16 v[226:227], v183 offset:13632\n"
"v_mfma_f32_16x16x32_bf16 v[82:85], v[172:175], v[82:85], 0\n"
"ds_read_b64_tr_b16 v[228:229], v182 offset:14400\n"
"ds_read_b64_tr_b16 v[230:231], v183 offset:14400\n"
"v_mfma_f32_16x16x32_bf16 v[204:207], a[0:3], v[32:35], 0\n"
"s_waitcnt lgkmcnt(8)\n"
"ds_read_b64_tr_b16 v[244:245], v182 offset:13640\n"
"v_cvt_pk_bf16_f32 v154, v208, v209\n"
"v_mfma_f32_16x16x32_bf16 v[204:207], a[12:15], v[36:39], v[204:207]\n"
"s_waitcnt lgkmcnt(8)\n"
"ds_read_b64_tr_b16 v[246:247], v183 offset:13640\n"
"v_cvt_pk_bf16_f32 v155, v210, v211\n"
"v_mfma_f32_16x16x32_bf16 v[204:207], a[24:27], v[40:43], v[204:207]\n"
"s_waitcnt lgkmcnt(8)\n"
"ds_read_b64_tr_b16 v[248:249], v182 offset:14408\n"
"v_cvt_pk_bf16_f32 v166, v212, v213\n"
"v_mfma_f32_16x16x32_bf16 v[204:207], a[36:39], v[44:47], v[204:207]\n"
"s_waitcnt lgkmcnt(8)\n"
"ds_read_b64_tr_b16 v[250:251], v183 offset:14408\n"
"v_cvt_pk_bf16_f32 v167, v214, v215\n"
"v_mfma_f32_16x16x32_bf16 v[204:207], a[48:51], v[48:51], v[204:207]\n"
"s_waitcnt lgkmcnt(8)\n"
"ds_read_b128 v[128:131], v179\n"
"v_exp_f32 v113, v113\n"
"v_mfma_f32_16x16x32_bf16 v[204:207], a[60:63], v[52:55], v[204:207]\n"
"s_add_u32 m0, s79, 142464\n"
"ds_read_b128 v[132:135], v179 offset:13504\n"
"buffer_load_dwordx4 v192, s[16:19], s43 offen offset:2048 lds\n"
"v_mfma_f32_16x16x32_bf16 v[208:211], a[4:7], v[32:35], 0\n"
".Lic3%=:\n"
"s_cbranch_execz .Lib0%=\n"
"v_exp_f32 v114, v114\n"
"v_mfma_f32_16x16x32_bf16 v[208:211], a[16:19], v[36:39], v[208:211]\n"
"v_exp_f32 v115, v115\n"
"v_mfma_f32_16x16x32_bf16 v[208:211], a[28:31], v[40:43], v[208:211]\n"
"ds_read_b64_tr_b16 v[8:9], v236\n"
"v_cvt_pk_bf16_f32 v90, v112, v114\n"
"v_mfma_f32_16x16x32_bf16 v[208:211], a[40:43], v[44:47], v[208:211]\n"
"v_cvt_pk_bf16_f32 v91, v113, v115\n"
"v_exp_f32 v116, v116\n"
"v_mfma_f32_16x16x32_bf16 v[208:211], a[52:55], v[48:51], v[208:211]\n"
"ds_read_b128 v[112:115], v178 offset:27008\n"
"v_exp_f32 v117, v117\n"
"v_mfma_f32_16x16x32_bf16 v[208:211], a[64:67], v[52:55], v[208:211]\n"
"v_exp_f32 v118, v118\n"
"v_mfma_f32_16x16x32_bf16 v[212:215], a[8:11], v[32:35], 0\n"
"s_add_u32 m0, s34, 0\n"
"v_exp_f32 v119, v119\n"
"v_mfma_f32_16x16x32_bf16 v[212:215], a[20:23], v[36:39], v[212:215]\n"
"buffer_load_dwordx4 v190, s[8:11], s32 offen lds\n"
"v_cvt_pk_bf16_f32 v92, v116, v118\n"
"v_mfma_f32_16x16x32_bf16 v[212:215], a[32:35], v[40:43], v[212:215]\n"
"v_cvt_pk_bf16_f32 v93, v117, v119\n"
"v_cvt_pk_bf16_f32 v204, v204, v205\n"
"v_mfma_f32_16x16x32_bf16 v[212:215], a[44:47], v[44:47], v[212:215]\n"
"s_waitcnt lgkmcnt(4)\n"
"ds_read_b128 v[116:119], v180\n"
"ds_read_b64_tr_b16 v[10:11], v236 offset:6144\n"
"v_smfmac_f32_16x16x64_bf16 v[252:255], v[90:93], v[224:231], v63\n"
"v_cvt_pk_bf16_f32 v205, v206, v207\n"
"v_smfmac_f32_16x16x64_bf16 v[98:101], v[90:93], v[244:251], v63\n"
"s_mov_b64 exec, s[44:45]\n"
"v_add_u32 v181, 0xc0, v181\n"
"s_mov_b64 exec, -1\n"
"v_smfmac_f32_16x16x64_bf16 v[106:109], v[90:93], v[120:127], v63\n"
"ds_read_b128 v[120:123], v179 offset:27008\n"
"v_mfma_f32_16x16x32_bf16 v[90:93], v[172:175], v[90:93], v[82:85]\n"
"ds_read_b128 v[124:127], v181\n"
"v_cvt_pk_bf16_f32 v252, v252, v253\n"
"v_mfma_f32_16x16x32_bf16 v[212:215], a[56:59], v[48:51], v[212:215]\n"
"s_add_u32 m0, s34, 3072\n"
"ds_read_b64 v[82:83], v178 offset:64\n"
"buffer_load_dwordx4 v191, s[8:11], s32 offen lds\n"
"v_mfma_f32_16x16x32_bf16 v[212:215], a[68:71], v[52:55], v[212:215]\n"
"s_add_u32 s32, s32, s33\n"
"v_rcp_f32 v171, v90\n"
"v_cvt_pk_bf16_f32 v253, v254, v255\n"
"v_cvt_pk_bf16_f32 v171, v171, v171\n"
"v_and_b32 v92, v171, v176\n"
"v_and_b32 v93, v171, v177\n"
"v_cvt_pk_bf16_f32 v98, v98, v99\n"
"v_cvt_pk_bf16_f32 v99, v100, v101\n"
"v_mfma_f32_16x16x16_bf16 v[252:255], v[252:253], v[92:93], 0\n"
"ds_read_b64 v[84:85], v179 offset:64\n"
"ds_read_b64 v[90:91], v178 offset:13568\n"
"v_mfma_f32_16x16x16_bf16 v[98:101], v[98:99], v[92:93], 0\n"
"v_cvt_pk_bf16_f32 v106, v106, v107\n"
"v_cvt_pk_bf16_f32 v107, v108, v109\n"
"v_cvt_pk_bf16_f32 v206, v208, v209\n"
"v_cvt_pk_bf16_f32 v207, v210, v211\n"
"v_mfma_f32_16x16x16_bf16 v[106:109], v[106:107], v[92:93], 0\n"
"ds_read_b64 v[92:93], v179 offset:13568\n"
"ds_write_b128 v186, v[204:207] offset:60768\n"
"v_mfma_f32_16x16x32_bf16 v[208:211], a[80:83], v[32:35], 0\n"
"s_waitcnt lgkmcnt(4)\n"
"v_cvt_pk_bf16_f32 v252, v252, v253\n"
"v_cvt_pk_bf16_f32 v253, v254, v255\n"
"v_mfma_f32_16x16x32_bf16 v[136:139], v[136:139], v[144:147], 0\n"
"s_add_u32 m0, s34, 1024\n"
"v_cvt_pk_bf16_f32 v254, v98, v99\n"
"v_cvt_pk_bf16_f32 v255, v100, v101\n"
"v_mfma_f32_16x16x32_bf16 v[136:139], v[80:83], v[156:159], v[136:139]\n"
"buffer_load_dwordx4 v190, s[8:11], s0 offen lds\n"
"ds_read_b64 v[98:99], v178 offset:27072\n"
"v_mfma_f32_16x16x32_bf16 v[208:211], a[92:95], v[36:39], v[208:211]\n"
"s_waitcnt lgkmcnt(4)\n"
"ds_read_b64 v[100:101], v179 offset:27072\n"
"v_cvt_pk_bf16_f32 v106, v106, v107\n"
"v_mfma_f32_16x16x32_bf16 v[128:131], v[128:131], v[144:147], 0\n"
"s_waitcnt lgkmcnt(4)\n"
"v_cvt_pk_bf16_f32 v107, v108, v109\n"
"v_cvt_pk_bf16_f32 v212, v212, v213\n"
"v_mfma_f32_16x16x32_bf16 v[128:131], v[84:87], v[158:161], v[128:131]\n"
"s_waitcnt lgkmcnt(3)\n"
"buffer_store_dwordx4 v[252:255], v192, s[28:31], s37 offen sc1\n"
"v_mfma_f32_16x16x32_bf16 v[140:143], v[140:143], v[144:147], 0\n"
"ds_read_b64 v[108:109], v181 offset:64\n"
"ds_read_b64_tr_b16 v[12:13], v236 offset:8\n"
"v_mfma_f32_16x16x32_bf16 v[140:143], v[88:91], v[156:159], v[140:143]\n"
"v_cvt_pk_bf16_f32 v213, v214, v215\n"
"v_mov_b64 v[252:253], 0\n"
"v_mfma_f32_16x16x32_bf16 v[132:135], v[132:135], v[144:147], 0\n"
"ds_read_b64_tr_b16 v[14:15], v236 offset:6152\n"
"buffer_store_dwordx2 v[106:107], v193, s[28:31], s37 offen offset:1024 sc1\n"
"v_mfma_f32_16x16x32_bf16 v[132:135], v[92:95], v[158:161], v[132:135]\n"
"s_waitcnt lgkmcnt(3)\n"
"ds_read_b64 v[106:107], v180 offset:64\n"
"v_max_f32 v136, v136, v128\n"
"v_mfma_f32_16x16x32_bf16 v[112:115], v[112:115], v[144:147], 0\n"
"s_add_u32 s37, s37, 1572864\n"
"v_max_f32 v137, v137, v129\n"
"v_max_f32 v138, v138, v130\n"
"v_mfma_f32_16x16x32_bf16 v[112:115], v[96:99], v[156:159], v[112:115]\n"
"s_add_u32 m0, s34, 4096\n"
"ds_read_b64_tr_b16 v[128:129], v182 offset:72\n"
"v_max_f32 v139, v139, v131\n"
"v_mfma_f32_16x16x32_bf16 v[120:123], v[120:123], v[144:147], 0\n"
"buffer_load_dwordx4 v191, s[8:11], s0 offen lds\n"
"ds_read_b64_tr_b16 v[130:131], v183 offset:72\n"
"v_mfma_f32_16x16x32_bf16 v[120:123], v[100:103], v[158:161], v[120:123]\n"
"s_waitcnt lgkmcnt(5)\n"
"v_max3_f32 v170, v136, v137, v138\n"
"v_max_f32 v140, v140, v132\n"
"v_mfma_f32_16x16x32_bf16 v[124:127], v[124:127], v[144:147], 0\n"
"s_add_u32 s0, s0, s33\n"
"ds_read_b64_tr_b16 v[16:17], v235\n"
"v_max_f32 v141, v141, v133\n"
"v_mfma_f32_16x16x32_bf16 v[124:127], v[108:111], v[158:161], v[124:127]\n"
"ds_read_b64_tr_b16 v[132:133], v182 offset:840\n"
"v_max_f32 v142, v142, v134\n"
"v_mfma_f32_16x16x32_bf16 v[208:211], a[104:107], v[40:43], v[208:211]\n"
"ds_read_b64_tr_b16 v[18:19], v235 offset:6144\n"
"v_max_f32 v143, v143, v135\n"
"v_mfma_f32_16x16x32_bf16 v[208:211], a[116:119], v[44:47], v[208:211]\n"
"v_max3_f32 v170, v170, v139, v140\n"
"v_max3_f32 v170, v170, v141, v142\n"
"v_mfma_f32_16x16x32_bf16 v[208:211], a[128:131], v[48:51], v[208:211]\n"
"s_waitcnt lgkmcnt(5)\n"
"ds_read_b64_tr_b16 v[134:135], v183 offset:840\n"
"v_max_f32 v112, v112, v120\n"
"v_mfma_f32_16x16x32_bf16 v[208:211], a[140:143], v[52:55], v[208:211]\n"
"v_max_f32 v113, v113, v121\n"
"v_max_f32 v114, v114, v122\n"
"v_mfma_f32_16x16x32_bf16 v[116:119], v[116:119], v[144:147], 0\n"
"s_add_u32 m0, s34, 2048\n"
"ds_read_b64_tr_b16 v[120:121], v182 offset:27080\n"
"buffer_load_dwordx4 v190, s[8:11], s1 offen lds\n"
"v_mfma_f32_16x16x32_bf16 v[116:119], v[104:107], v[156:159], v[116:119]\n"
"v_max_f32 v115, v115, v123\n"
"v_cvt_pk_bf16_f32 v214, v208, v209\n"
"v_mfma_f32_16x16x32_bf16 v[204:207], a[72:75], v[32:35], 0\n"
"ds_read_b64_tr_b16 v[122:123], v183 offset:27080\n"
"v_cvt_pk_bf16_f32 v215, v210, v211\n"
"v_mfma_f32_16x16x32_bf16 v[204:207], a[84:87], v[36:39], v[204:207]\n"
"ds_write_b128 v186, v[212:215] offset:60832\n"
"v_mfma_f32_16x16x32_bf16 v[204:207], a[96:99], v[40:43], v[204:207]\n"
"s_waitcnt vmcnt(14)\n"
"v_readfirstlane_b32 s6, v201\n"
"v_max3_f32 v170, v170, v143, v112\n"
"v_mfma_f32_16x16x32_bf16 v[204:207], a[108:111], v[44:47], v[204:207]\n"
"s_and_b32 s6, s6, 0xffffff00\n"
"v_or_b32 v201, 5, s6\n"
"v_max3_f32 v170, v170, v113, v114\n"
"v_mfma_f32_16x16x32_bf16 v[204:207], a[120:123], v[48:51], v[204:207]\n"
"s_mov_b64 exec, 1\n"
"buffer_store_dword v201, off, s[28:31], s40 sc1\n"
"s_mov_b64 exec, -1\n"
"v_mfma_f32_16x16x32_bf16 v[204:207], a[132:135], v[52:55], v[204:207]\n"
"s_add_u32 m0, s34, 5120\n"
"v_max_f32 v116, v116, v124\n"
"v_max_f32 v117, v117, v125\n"
"v_mfma_f32_16x16x32_bf16 v[208:211], a[76:79], v[32:35], 0\n"
"buffer_load_dwordx4 v191, s[8:11], s1 offen lds\n"
"ds_read_b64_tr_b16 v[124:125], v182 offset:27848\n"
"v_mfma_f32_16x16x32_bf16 v[208:211], a[88:91], v[36:39], v[208:211]\n"
"s_add_u32 s1, s1, s33\n"
"v_max_f32 v118, v118, v126\n"
"v_max_f32 v119, v119, v127\n"
"v_mfma_f32_16x16x32_bf16 v[208:211], a[100:103], v[40:43], v[208:211]\n"
"ds_read_b64_tr_b16 v[126:127], v183 offset:27848\n"
"v_max3_f32 v170, v170, v115, v116\n"
"v_mfma_f32_16x16x32_bf16 v[208:211], a[112:115], v[44:47], v[208:211]\n"
"v_max3_f32 v170, v170, v117, v118\n"
"v_cvt_pk_bf16_f32 v204, v204, v205\n"
"v_mfma_f32_16x16x32_bf16 v[208:211], a[124:127], v[48:51], v[208:211]\n"
"v_cvt_pk_bf16_f32 v205, v206, v207\n"
"v_mov_b64 v[254:255], 0\n"
"v_mfma_f32_16x16x32_bf16 v[208:211], a[136:139], v[52:55], v[208:211]\n"
"v_mov_b64 v[98:99], 0\n"
"v_mov_b64 v[100:101], 0\n"
"v_mfma_f32_16x16x32_bf16 v[212:215], a[144:147], v[32:35], 0\n"
"v_mov_b64 v[106:107], 0\n"
"v_mov_b64 v[108:109], 0\n"
"v_mfma_f32_16x16x32_bf16 v[212:215], a[156:159], v[36:39], v[212:215]\n"
"v_max_f32 v171, v170, v119\n"
"v_max_f32 v170, v170, v119\n"
"v_mfma_f32_16x16x32_bf16 v[212:215], a[168:171], v[40:43], v[212:215]\n"
"s_nop 0\n"
"v_permlane16_swap_b32 v170, v171\n"
"v_mfma_f32_16x16x32_bf16 v[212:215], a[180:183], v[44:47], v[212:215]\n"
"v_max_f32 v170, v170, v171\n"
"v_mov_b32 v171, v170\n"
"v_mfma_f32_16x16x32_bf16 v[212:215], a[192:195], v[48:51], v[212:215]\n"
"s_nop 0\n"
"v_permlane32_swap_b32 v170, v171\n"
"v_max_f32 v170, v170, v171\n"
"v_mul_f32 v170, 0xbe553b95, v170\n"
"v_pk_fma_f32 v[136:137], v[136:137], s[70:71], v[170:171] op_sel_hi:[1,0,0]\n"
"v_pk_fma_f32 v[138:139], v[138:139], s[70:71], v[170:171] op_sel_hi:[1,0,0]\n"
"v_pk_fma_f32 v[140:141], v[140:141], s[70:71], v[170:171] op_sel_hi:[1,0,0]\n"
"v_pk_fma_f32 v[142:143], v[142:143], s[70:71], v[170:171] op_sel_hi:[1,0,0]\n"
"v_pk_fma_f32 v[112:113], v[112:113], s[70:71], v[170:171] op_sel_hi:[1,0,0]\n"
"v_pk_fma_f32 v[114:115], v[114:115], s[70:71], v[170:171] op_sel_hi:[1,0,0]\n"
"v_pk_fma_f32 v[116:117], v[116:117], s[70:71], v[170:171] op_sel_hi:[1,0,0]\n"
"v_pk_fma_f32 v[118:119], v[118:119], s[70:71], v[170:171] op_sel_hi:[1,0,0]\n"
"v_mfma_f32_16x16x32_bf16 v[212:215], a[204:207], v[52:55], v[212:215]\n"
"v_cvt_pk_bf16_f32 v206, v208, v209\n"
"v_cvt_pk_bf16_f32 v207, v210, v211\n"
"v_mfma_f32_16x16x32_bf16 v[208:211], a[148:151], v[32:35], 0\n"
"ds_write_b128 v186, v[204:207] offset:60896\n"
"v_mfma_f32_16x16x32_bf16 v[208:211], a[160:163], v[36:39], v[208:211]\n"
"v_exp_f32 v136, v136\n"
"v_mfma_f32_16x16x32_bf16 v[208:211], a[172:175], v[40:43], v[208:211]\n"
"v_exp_f32 v137, v137\n"
"v_mfma_f32_16x16x32_bf16 v[208:211], a[184:187], v[44:47], v[208:211]\n"
"v_exp_f32 v138, v138\n"
"v_mfma_f32_16x16x32_bf16 v[208:211], a[196:199], v[48:51], v[208:211]\n"
"s_waitcnt vmcnt(14)\n"
"v_exp_f32 v139, v139\n"
"v_mfma_f32_16x16x32_bf16 v[208:211], a[208:211], v[52:55], v[208:211]\n"
"ds_write_b128 v202, v[56:59] offset:55296\n"
"v_mfma_f32_16x16x32_bf16 v[204:207], a[152:155], v[32:35], 0\n"
"buffer_load_dwordx4 v[56:59], v192, s[28:31], s38 offen sc1\n"
"v_cvt_pk_bf16_f32 v82, v136, v138\n"
"v_mfma_f32_16x16x32_bf16 v[204:207], a[164:167], v[36:39], v[204:207]\n"
"s_waitcnt vmcnt(14)\n"
"v_cvt_pk_bf16_f32 v83, v137, v139\n"
"v_cvt_pk_bf16_f32 v144, v212, v213\n"
"v_mfma_f32_16x16x32_bf16 v[204:207], a[176:179], v[40:43], v[204:207]\n"
"ds_read_b128 v[32:35], v188 offset:6144\n"
"ds_read_b128 v[36:39], v189 offset:6144\n"
"v_mfma_f32_16x16x32_bf16 v[204:207], a[188:191], v[44:47], v[204:207]\n"
"ds_read_b128 v[40:43], v188 offset:7168\n"
"v_cvt_pk_bf16_f32 v145, v214, v215\n"
"v_mfma_f32_16x16x32_bf16 v[204:207], a[200:203], v[48:51], v[204:207]\n"
"s_waitcnt vmcnt(13)\n"
"ds_read_b128 v[44:47], v189 offset:7168\n"
"v_exp_f32 v140, v140\n"
"v_mfma_f32_16x16x32_bf16 v[204:207], a[212:215], v[52:55], v[204:207]\n"
"ds_read_b128 v[48:51], v188 offset:8192\n"
"ds_read_b128 v[52:55], v189 offset:8192\n"
"v_exp_f32 v141, v141\n"
"v_exp_f32 v142, v142\n"
"v_exp_f32 v143, v143\n"
"v_cvt_pk_bf16_f32 v84, v140, v142\n"
"v_cvt_pk_bf16_f32 v85, v141, v143\n"
"v_cvt_pk_bf16_f32 v146, v208, v209\n"
"v_cvt_pk_bf16_f32 v147, v210, v211\n"
"v_smfmac_f32_16x16x64_bf16 v[252:255], v[82:85], v[216:223], v63\n"
"s_waitcnt lgkmcnt(5)\n"
"ds_write_b64 v203, v[60:61] offset:55296\n"
"v_smfmac_f32_16x16x64_bf16 v[98:101], v[82:85], v[72:79], v63\n"
"s_waitcnt lgkmcnt(5)\n"
"buffer_load_dwordx2 v[60:61], v193, s[28:31], s38 offen offset:1024 sc1\n"
"v_smfmac_f32_16x16x64_bf16 v[106:109], v[82:85], v[128:135], v63\n"
"s_add_u32 s38, s38, 1572864\n"
"ds_read_b64_tr_b16 v[216:217], v182 offset:27136\n"
"v_mfma_f32_16x16x32_bf16 v[82:85], v[172:175], v[82:85], 0\n"
"s_waitcnt lgkmcnt(4)\n"
"ds_read_b64_tr_b16 v[218:219], v183 offset:27136\n"
"ds_read_b64_tr_b16 v[220:221], v182 offset:27904\n"
"v_mfma_f32_16x16x32_bf16 v[212:215], a[0:3], v[32:35], 0\n"
"s_add_u32 m0, s34, 6144\n"
"ds_read_b64_tr_b16 v[222:223], v183 offset:27904\n"
"buffer_load_dwordx4 v190, s[8:11], s32 offen lds\n"
"v_mfma_f32_16x16x32_bf16 v[212:215], a[12:15], v[36:39], v[212:215]\n"
"ds_read_b64_tr_b16 v[72:73], v182 offset:27144\n"
"v_cvt_pk_bf16_f32 v158, v204, v205\n"
"v_mfma_f32_16x16x32_bf16 v[212:215], a[24:27], v[40:43], v[212:215]\n"
"ds_read_b64_tr_b16 v[74:75], v183 offset:27144\n"
"v_cvt_pk_bf16_f32 v159, v206, v207\n"
"v_mfma_f32_16x16x32_bf16 v[212:215], a[36:39], v[44:47], v[212:215]\n"
"s_waitcnt vmcnt(12) lgkmcnt(6)\n"
"ds_read_b64_tr_b16 v[76:77], v182 offset:27912\n"
"v_exp_f32 v112, v112\n"
"v_mfma_f32_16x16x32_bf16 v[212:215], a[48:51], v[48:51], v[212:215]\n"
"s_barrier\n"
"v_mfma_f32_16x16x32_bf16 v[212:215], a[60:63], v[52:55], v[212:215]\n"
"ds_read_b64_tr_b16 v[78:79], v183 offset:27912\n"
"ds_read_b128 v[136:139], v178 offset:13504\n"
"v_mfma_f32_16x16x32_bf16 v[208:211], a[4:7], v[32:35], 0\n"
"ds_read_b128 v[128:131], v179 offset:13504\n"
"v_exp_f32 v113, v113\n"
"v_mfma_f32_16x16x32_bf16 v[208:211], a[16:19], v[36:39], v[208:211]\n"
"s_add_u32 m0, s34, 9216\n"
"ds_read_b128 v[140:143], v178 offset:27008\n"
"buffer_load_dwordx4 v191, s[8:11], s32 offen lds\n"
"v_mfma_f32_16x16x32_bf16 v[208:211], a[28:31], v[40:43], v[208:211]\n"
"s_add_u32 s32, s32, s33\n"
"ds_read_b128 v[132:135], v179 offset:27008\n"
"ds_read_b64_tr_b16 v[20:21], v236 offset:12288\n"
"v_mfma_f32_16x16x32_bf16 v[208:211], a[40:43], v[44:47], v[208:211]\n"
"v_exp_f32 v114, v114\n"
"v_mfma_f32_16x16x32_bf16 v[208:211], a[52:55], v[48:51], v[208:211]\n"
"v_exp_f32 v115, v115\n"
"v_mfma_f32_16x16x32_bf16 v[208:211], a[64:67], v[52:55], v[208:211]\n"
"v_cvt_pk_bf16_f32 v90, v112, v114\n"
"v_cvt_pk_bf16_f32 v91, v113, v115\n"
"v_mfma_f32_16x16x32_bf16 v[204:207], a[8:11], v[32:35], 0\n"
"ds_read_b128 v[112:115], v180\n"
"ds_read_b64_tr_b16 v[22:23], v236 offset:18432\n"
"v_mfma_f32_16x16x32_bf16 v[204:207], a[20:23], v[36:39], v[204:207]\n"
"v_exp_f32 v116, v116\n"
"v_mfma_f32_16x16x32_bf16 v[204:207], a[32:35], v[40:43], v[204:207]\n"
"v_exp_f32 v117, v117\n"
"v_mfma_f32_16x16x32_bf16 v[204:207], a[44:47], v[44:47], v[204:207]\n"
"s_add_u32 m0, s34, 7168\n"
"v_exp_f32 v118, v118\n"
"v_mfma_f32_16x16x32_bf16 v[204:207], a[56:59], v[48:51], v[204:207]\n"
"buffer_load_dwordx4 v190, s[8:11], s0 offen lds\n"
"ds_read_b64_tr_b16 v[24:25], v236 offset:12296\n"
"v_mfma_f32_16x16x32_bf16 v[204:207], a[68:71], v[52:55], v[204:207]\n"
"v_exp_f32 v119, v119\n"
"v_cvt_pk_bf16_f32 v92, v116, v118\n"
"v_cvt_pk_bf16_f32 v93, v117, v119\n"
"v_cvt_pk_bf16_f32 v212, v212, v213\n"
"v_cvt_pk_bf16_f32 v213, v214, v215\n"
"v_smfmac_f32_16x16x64_bf16 v[106:109], v[90:93], v[120:127], v63\n"
"s_waitcnt lgkmcnt(8)\n"
"ds_read_b128 v[120:123], v181\n"
"v_smfmac_f32_16x16x64_bf16 v[252:255], v[90:93], v[216:223], v63\n"
"ds_read_b128 v[116:119], v180 offset:13504\n"
"v_smfmac_f32_16x16x64_bf16 v[98:101], v[90:93], v[72:79], v63\n"
"ds_read_b128 v[124:127], v181 offset:13504\n"
"v_mfma_f32_16x16x32_bf16 v[90:93], v[172:175], v[90:93], v[82:85]\n"
"s_nop 0\n"
"v_cvt_pk_bf16_f32 v106, v106, v107\n"
"s_nop 1\n"
"v_cvt_pk_bf16_f32 v252, v252, v253\n"
"s_add_u32 m0, s34, 10240\n"
"v_cvt_pk_bf16_f32 v253, v254, v255\n"
"buffer_load_dwordx4 v191, s[8:11], s0 offen lds\n"
"s_add_u32 s0, s0, s33\n"
"v_rcp_f32 v171, v90\n"
"v_cvt_pk_bf16_f32 v98, v98, v99\n"
"v_cvt_pk_bf16_f32 v171, v171, v171\n"
"v_and_b32 v92, v171, v176\n"
"v_and_b32 v93, v171, v177\n"
"v_cvt_pk_bf16_f32 v99, v100, v101\n"
"v_cvt_pk_bf16_f32 v107, v108, v109\n"
"v_mfma_f32_16x16x16_bf16 v[252:255], v[252:253], v[92:93], 0\n"
"ds_read_b64 v[82:83], v178 offset:13568\n"
"ds_read_b64 v[84:85], v179 offset:13568\n"
"v_mfma_f32_16x16x16_bf16 v[98:101], v[98:99], v[92:93], 0\n"
"ds_read_b64 v[90:91], v178 offset:27072\n"
"v_cvt_pk_bf16_f32 v214, v208, v209\n"
"v_mfma_f32_16x16x16_bf16 v[106:109], v[106:107], v[92:93], 0\n"
"ds_read_b64 v[92:93], v179 offset:27072\n"
"s_nop 0\n"
"v_cvt_pk_bf16_f32 v252, v252, v253\n"
"v_cvt_pk_bf16_f32 v253, v254, v255\n"
"s_nop 0\n"
"v_cvt_pk_bf16_f32 v254, v98, v99\n"
"v_cvt_pk_bf16_f32 v255, v100, v101\n"
"buffer_store_dwordx4 v[252:255], v192, s[28:31], s37 offen sc1\n"
"ds_read_b64 v[98:99], v180 offset:64\n"
"ds_read_b64 v[100:101], v181 offset:64\n"
"ds_read_b64_tr_b16 v[26:27], v236 offset:18440\n"
"v_cvt_pk_bf16_f32 v106, v106, v107\n"
"v_cvt_pk_bf16_f32 v107, v108, v109\n"
"v_cvt_pk_bf16_f32 v215, v210, v211\n"
"v_mfma_f32_16x16x32_bf16 v[208:211], a[80:83], v[32:35], 0\n"
"s_waitcnt lgkmcnt(6)\n"
"buffer_store_dwordx2 v[106:107], v193, s[28:31], s37 offen offset:1024 sc1\n"
"v_mfma_f32_16x16x32_bf16 v[136:139], v[136:139], v[148:151], 0\n"
"s_waitcnt lgkmcnt(4)\n"
"ds_write_b128 v187, v[212:215]\n"
"v_mfma_f32_16x16x32_bf16 v[136:139], v[80:83], v[160:163], v[136:139]\n"
"s_waitcnt lgkmcnt(4)\n"
"v_cvt_pk_bf16_f32 v204, v204, v205\n"
"v_cvt_pk_bf16_f32 v205, v206, v207\n"
"v_mfma_f32_16x16x32_bf16 v[128:131], v[128:131], v[148:151], 0\n"
"s_add_u32 m0, s34, 8192\n"
"v_mov_b64 v[252:253], 0\n"
"v_mov_b64 v[254:255], 0\n"
"v_mfma_f32_16x16x32_bf16 v[128:131], v[84:87], v[162:165], v[128:131]\n"
"s_waitcnt lgkmcnt(3)\n"
"buffer_load_dwordx4 v190, s[8:11], s1 offen lds\n"
"ds_read_b64 v[106:107], v180 offset:13568\n"
"v_mfma_f32_16x16x32_bf16 v[140:143], v[140:143], v[148:151], 0\n"
"s_waitcnt lgkmcnt(3)\n"
"ds_read_b64 v[108:109], v181 offset:13568\n"
"ds_read_b64_tr_b16 v[28:29], v235 offset:12288\n"
"v_mfma_f32_16x16x32_bf16 v[140:143], v[88:91], v[160:163], v[140:143]\n"
"s_add_u32 s37, s37, 1572864\n"
"v_max_f32 v136, v136, v128\n"
"v_max_f32 v137, v137, v129\n"
"v_mfma_f32_16x16x32_bf16 v[132:135], v[132:135], v[148:151], 0\n"
"s_add_u32 s4, s37, 6291456\n"
"ds_read_b64_tr_b16 v[30:31], v235 offset:18432\n"
"v_max_f32 v138, v138, v130\n"
"v_mfma_f32_16x16x32_bf16 v[132:135], v[92:95], v[162:165], v[132:135]\n"
"v_max_f32 v139, v139, v131\n"
"v_max3_f32 v170, v136, v137, v138\n"
"v_mfma_f32_16x16x32_bf16 v[112:115], v[112:115], v[148:151], 0\n"
"ds_read_b64_tr_b16 v[128:129], v182 offset:13576\n"
"v_mfma_f32_16x16x32_bf16 v[112:115], v[96:99], v[160:163], v[112:115]\n"
"ds_read_b64_tr_b16 v[130:131], v183 offset:13576\n"
"v_mov_b64 v[98:99], 0\n"
"v_mfma_f32_16x16x32_bf16 v[120:123], v[120:123], v[148:151], 0\n"
"s_waitcnt lgkmcnt(5)\n"
"v_max_f32 v140, v140, v132\n"
"v_max_f32 v141, v141, v133\n"
"v_mfma_f32_16x16x32_bf16 v[120:123], v[100:103], v[162:165], v[120:123]\n"
"s_waitcnt lgkmcnt(4)\n"
"ds_read_b64_tr_b16 v[132:133], v182 offset:14344\n"
"v_max_f32 v142, v142, v134\n"
"v_mfma_f32_16x16x32_bf16 v[116:119], v[116:119], v[148:151], 0\n"
"s_add_u32 m0, s34, 11264\n"
"v_max_f32 v143, v143, v135\n"
"v_max3_f32 v170, v170, v139, v140\n"
"v_mfma_f32_16x16x32_bf16 v[116:119], v[104:107], v[160:163], v[116:119]\n"
"buffer_load_dwordx4 v191, s[8:11], s1 offen lds\n"
"ds_read_b64_tr_b16 v[134:135], v183 offset:14344\n"
"v_mfma_f32_16x16x32_bf16 v[124:127], v[124:127], v[148:151], 0\n"
"s_add_u32 s1, s1, s33\n"
"v_max3_f32 v170, v170, v141, v142\n"
"v_max_f32 v112, v112, v120\n"
"v_mfma_f32_16x16x32_bf16 v[124:127], v[108:111], v[162:165], v[124:127]\n"
"v_max_f32 v113, v113, v121\n"
"v_max_f32 v114, v114, v122\n"
"v_mfma_f32_16x16x32_bf16 v[208:211], a[92:95], v[36:39], v[208:211]\n"
"ds_read_b64_tr_b16 v[120:121], v182 offset:40584\n"
"v_max_f32 v115, v115, v123\n"
"v_mfma_f32_16x16x32_bf16 v[208:211], a[104:107], v[40:43], v[208:211]\n"
"ds_read_b64_tr_b16 v[122:123], v183 offset:40584\n"
"v_max3_f32 v170, v170, v143, v112\n"
"v_mfma_f32_16x16x32_bf16 v[208:211], a[116:119], v[44:47], v[208:211]\n"
"v_max3_f32 v170, v170, v113, v114\n"
"v_mov_b64 v[64:65], -1\n"
"v_mfma_f32_16x16x32_bf16 v[208:211], a[128:131], v[48:51], v[208:211]\n"
"buffer_load_dword v200, off, s[28:31], s39 sc1\n"
"v_mov_b64 v[66:67], -1\n"
"v_mfma_f32_16x16x32_bf16 v[208:211], a[140:143], v[52:55], v[208:211]\n"
"buffer_store_dwordx4 v[64:67], v192, s[28:31], s4 offen sc1\n"
"v_mfma_f32_16x16x32_bf16 v[212:215], a[72:75], v[32:35], 0\n"
"s_add_u32 s4, s37, 6291456\n"
"ds_read_b128 v[64:67], v192 offset:55296\n"
"v_max_f32 v116, v116, v124\n"
"v_mfma_f32_16x16x32_bf16 v[212:215], a[84:87], v[36:39], v[212:215]\n"
"v_max_f32 v117, v117, v125\n"
"v_max_f32 v118, v118, v126\n"
"v_mfma_f32_16x16x32_bf16 v[212:215], a[96:99], v[40:43], v[212:215]\n"
"ds_read_b64_tr_b16 v[124:125], v182 offset:41352\n"
"v_cvt_pk_bf16_f32 v206, v208, v209\n"
"v_mfma_f32_16x16x32_bf16 v[212:215], a[108:111], v[44:47], v[212:215]\n"
"v_cvt_pk_bf16_f32 v207, v210, v211\n"
"v_max_f32 v119, v119, v127\n"
"v_mfma_f32_16x16x32_bf16 v[212:215], a[120:123], v[48:51], v[212:215]\n"
"ds_write_b128 v187, v[204:207] offset:64\n"
"v_mfma_f32_16x16x32_bf16 v[212:215], a[132:135], v[52:55], v[212:215]\n"
"v_max3_f32 v170, v170, v115, v116\n"
"v_max3_f32 v170, v170, v117, v118\n"
"v_mfma_f32_16x16x32_bf16 v[208:211], a[76:79], v[32:35], 0\n"
"s_waitcnt lgkmcnt(2)\n"
"v_mov_b64 v[100:101], 0\n"
"v_mov_b64 v[106:107], 0\n"
"v_mfma_f32_16x16x32_bf16 v[208:211], a[88:91], v[36:39], v[208:211]\n"
"ds_read_b64_tr_b16 v[126:127], v183 offset:41352\n"
"v_cvt_pk_bf16_f32 v212, v212, v213\n"
"v_mfma_f32_16x16x32_bf16 v[208:211], a[100:103], v[40:43], v[208:211]\n"
"v_cvt_pk_bf16_f32 v213, v214, v215\n"
"v_mov_b64 v[108:109], 0\n"
"v_mfma_f32_16x16x32_bf16 v[208:211], a[112:115], v[44:47], v[208:211]\n"
"v_max_f32 v171, v170, v119\n"
"v_max_f32 v170, v170, v119\n"
"v_mfma_f32_16x16x32_bf16 v[208:211], a[124:127], v[48:51], v[208:211]\n"
"s_nop 0\n"
"v_permlane16_swap_b32 v170, v171\n"
"v_mfma_f32_16x16x32_bf16 v[208:211], a[136:139], v[52:55], v[208:211]\n"
"v_max_f32 v170, v170, v171\n"
"v_mov_b32 v171, v170\n"
"v_mfma_f32_16x16x32_bf16 v[232:235], a[216:219], v[64:67], 0\n"
"s_nop 0\n"
"v_permlane32_swap_b32 v170, v171\n"
"v_max_f32 v170, v170, v171\n"
"v_mul_f32 v170, 0xbe553b95, v170\n"
"v_pk_fma_f32 v[136:137], v[136:137], s[70:71], v[170:171] op_sel_hi:[1,0,0]\n"
"v_pk_fma_f32 v[138:139], v[138:139], s[70:71], v[170:171] op_sel_hi:[1,0,0]\n"
"v_pk_fma_f32 v[140:141], v[140:141], s[70:71], v[170:171] op_sel_hi:[1,0,0]\n"
"v_pk_fma_f32 v[142:143], v[142:143], s[70:71], v[170:171] op_sel_hi:[1,0,0]\n"
"v_pk_fma_f32 v[112:113], v[112:113], s[70:71], v[170:171] op_sel_hi:[1,0,0]\n"
"v_pk_fma_f32 v[114:115], v[114:115], s[70:71], v[170:171] op_sel_hi:[1,0,0]\n"
"v_pk_fma_f32 v[116:117], v[116:117], s[70:71], v[170:171] op_sel_hi:[1,0,0]\n"
"v_pk_fma_f32 v[118:119], v[118:119], s[70:71], v[170:171] op_sel_hi:[1,0,0]\n"
"v_mfma_f32_16x16x32_bf16 v[236:239], a[220:223], v[64:67], 0\n"
"v_cvt_pk_bf16_f32 v214, v208, v209\n"
"v_cvt_pk_bf16_f32 v215, v210, v211\n"
"v_mfma_f32_16x16x32_bf16 v[240:243], a[224:227], v[64:67], 0\n"
"ds_read_b128 v[64:67], v192 offset:57344\n"
"ds_write_b128 v187, v[212:215] offset:128\n"
"v_mfma_f32_16x16x32_bf16 v[204:207], a[144:147], v[32:35], 0\n"
"v_exp_f32 v136, v136\n"
"v_mfma_f32_16x16x32_bf16 v[204:207], a[156:159], v[36:39], v[204:207]\n"
"v_exp_f32 v137, v137\n"
"v_mfma_f32_16x16x32_bf16 v[204:207], a[168:171], v[40:43], v[204:207]\n"
"v_exp_f32 v138, v138\n"
"v_mfma_f32_16x16x32_bf16 v[204:207], a[180:183], v[44:47], v[204:207]\n"
"v_exp_f32 v139, v139\n"
"v_mfma_f32_16x16x32_bf16 v[204:207], a[192:195], v[48:51], v[204:207]\n"
"v_cvt_pk_bf16_f32 v82, v136, v138\n"
"v_cvt_pk_bf16_f32 v83, v137, v139\n"
"v_mfma_f32_16x16x32_bf16 v[204:207], a[204:207], v[52:55], v[204:207]\n"
"ds_read_b128 v[136:139], v178 offset:27008\n"
"v_exp_f32 v140, v140\n"
"v_mfma_f32_16x16x32_bf16 v[208:211], a[148:151], v[32:35], 0\n"
"v_mov_b64 v[68:69], -1\n"
"v_exp_f32 v141, v141\n"
"v_mfma_f32_16x16x32_bf16 v[208:211], a[160:163], v[36:39], v[208:211]\n"
"buffer_store_dwordx2 v[68:69], v193, s[28:31], s4 offen offset:1024 sc1\n"
"v_mfma_f32_16x16x32_bf16 v[208:211], a[172:175], v[40:43], v[208:211]\n"
"ds_read_b128 v[68:71], v192 offset:56320\n"
"v_cvt_pk_bf16_f32 v148, v204, v205\n"
"v_mfma_f32_16x16x32_bf16 v[208:211], a[184:187], v[44:47], v[208:211]\n"
"v_exp_f32 v142, v142\n"
"v_mfma_f32_16x16x32_bf16 v[208:211], a[196:199], v[48:51], v[208:211]\n"
"v_exp_f32 v143, v143\n"
"v_mfma_f32_16x16x32_bf16 v[208:211], a[208:211], v[52:55], v[208:211]\n"
"v_cvt_pk_bf16_f32 v84, v140, v142\n"
"v_cvt_pk_bf16_f32 v85, v141, v143\n"
"v_mfma_f32_16x16x32_bf16 v[212:215], a[152:155], v[32:35], 0\n"
"s_waitcnt vmcnt(20)\n"
"ds_read_b128 v[32:35], v188\n"
"ds_read_b128 v[140:143], v180\n"
"v_mfma_f32_16x16x32_bf16 v[212:215], a[164:167], v[36:39], v[212:215]\n"
"ds_read_b128 v[36:39], v189\n"
"v_cvt_pk_bf16_f32 v149, v206, v207\n"
"v_mfma_f32_16x16x32_bf16 v[212:215], a[176:179], v[40:43], v[212:215]\n"
"s_waitcnt vmcnt(16)\n"
"ds_read_b128 v[40:43], v188 offset:1024\n"
"v_cvt_pk_bf16_f32 v150, v208, v209\n"
"v_mfma_f32_16x16x32_bf16 v[212:215], a[188:191], v[44:47], v[212:215]\n"
"s_waitcnt lgkmcnt(4)\n"
"ds_read_b128 v[44:47], v189 offset:1024\n"
"v_cvt_pk_bf16_f32 v151, v210, v211\n"
"v_mfma_f32_16x16x32_bf16 v[232:235], a[228:231], v[68:71], v[232:235]\n"
"v_exp_f32 v112, v112\n"
"v_mfma_f32_16x16x32_bf16 v[232:235], a[240:243], v[64:67], v[232:235]\n"
"v_exp_f32 v113, v113\n"
"v_mfma_f32_16x16x32_bf16 v[236:239], a[232:235], v[68:71], v[236:239]\n"
"v_exp_f32 v114, v114\n"
"v_mfma_f32_16x16x32_bf16 v[236:239], a[244:247], v[64:67], v[236:239]\n"
"s_waitcnt lgkmcnt(4)\n"
"v_exp_f32 v115, v115\n"
"v_mfma_f32_16x16x32_bf16 v[240:243], a[236:239], v[68:71], v[240:243]\n"
"s_waitcnt vmcnt(12)\n"
"ds_read_b128 v[68:71], v192 offset:58368\n"
"ds_write_b128 v202, v[56:59] offset:36864\n"
"v_mfma_f32_16x16x32_bf16 v[240:243], a[248:251], v[64:67], v[240:243]\n"
"s_waitcnt lgkmcnt(4)\n"
"v_cvt_pk_bf16_f32 v90, v112, v114\n"
"v_cvt_pk_bf16_f32 v91, v113, v115\n"
"v_smfmac_f32_16x16x64_bf16 v[252:255], v[82:85], v[224:231], v63\n"
"s_waitcnt lgkmcnt(2)\n"
"ds_read_b128 v[64:67], v192 offset:59392\n"
"v_smfmac_f32_16x16x64_bf16 v[98:101], v[82:85], v[244:251], v63\n"
"s_add_u32 m0, s34, 0\n"
"s_nop 0\n"
"buffer_load_dwordx4 v190, s[8:11], s32 offen lds\n"
"v_smfmac_f32_16x16x64_bf16 v[106:109], v[82:85], v[128:135], v63\n"
"ds_read_b64_tr_b16 v[224:225], v182 offset:40640\n"
"v_mfma_f32_16x16x32_bf16 v[82:85], v[172:175], v[82:85], 0\n"
"ds_read_b64_tr_b16 v[226:227], v183 offset:40640\n"
"ds_read_b64_tr_b16 v[228:229], v182 offset:41408\n"
"v_mfma_f32_16x16x32_bf16 v[212:215], a[200:203], v[48:51], v[212:215]\n"
"s_waitcnt lgkmcnt(5)\n"
"ds_read_b128 v[48:51], v188 offset:2048\n"
"v_exp_f32 v116, v116\n"
"v_mfma_f32_16x16x32_bf16 v[212:215], a[212:215], v[52:55], v[212:215]\n"
"ds_read_b128 v[52:55], v189 offset:2048\n"
"v_exp_f32 v117, v117\n"
"v_mfma_f32_16x16x32_bf16 v[232:235], a[252:255], v[68:71], v[232:235]\n"
"ds_read_b64_tr_b16 v[230:231], v183 offset:41408\n"
"ds_read_b64_tr_b16 v[244:245], v182 offset:40648\n"
"v_mfma_f32_16x16x32_bf16 v[236:239], v[0:3], v[68:71], v[236:239]\n"
"s_waitcnt vmcnt(12) lgkmcnt(7)\n"
"v_exp_f32 v118, v118\n"
"v_mfma_f32_16x16x32_bf16 v[236:239], v[12:15], v[64:67], v[236:239]\n"
"s_add_u32 m0, s34, 3072\n"
"ds_write_b64 v203, v[60:61] offset:36864\n"
"v_mfma_f32_16x16x32_bf16 v[240:243], v[4:7], v[68:71], v[240:243]\n"
"buffer_load_dwordx4 v191, s[8:11], s32 offen lds\n"
"v_cvt_pk_bf16_f32 v92, v116, v118\n"
"v_mfma_f32_16x16x32_bf16 v[240:243], v[16:19], v[64:67], v[240:243]\n"
"s_add_u32 s32, s32, s33\n"
"ds_read_b128 v[68:71], v192 offset:60416\n"
"ds_read_b64_tr_b16 v[246:247], v183 offset:40648\n"
"v_mfma_f32_16x16x32_bf16 v[232:235], v[8:11], v[64:67], v[232:235]\n"
"ds_read_b64_tr_b16 v[248:249], v182 offset:41416\n"
"v_cvt_pk_bf16_f32 v162, v212, v213\n"
"v_mfma_f32_16x16x32_bf16 v[204:207], a[0:3], v[32:35], 0\n"
".Lic4%=:\n"
"s_cbranch_execz .Lib1%=\n"
"ds_read_b64_tr_b16 v[250:251], v183 offset:41416\n"
"v_cvt_pk_bf16_f32 v163, v214, v215\n"
"v_mfma_f32_16x16x32_bf16 v[204:207], a[12:15], v[36:39], v[204:207]\n"
"ds_read_b128 v[128:131], v179 offset:27008\n"
"v_exp_f32 v119, v119\n"
"v_mfma_f32_16x16x32_bf16 v[204:207], a[24:27], v[40:43], v[204:207]\n"
"ds_read_b128 v[132:135], v181\n"
"ds_read_b128 v[112:115], v180 offset:13504\n"
"v_mfma_f32_16x16x32_bf16 v[204:207], a[36:39], v[44:47], v[204:207]\n"
"s_waitcnt lgkmcnt(11)\n"
"v_cvt_pk_bf16_f32 v93, v117, v119\n"
"v_mfma_f32_16x16x32_bf16 v[204:207], a[48:51], v[48:51], v[204:207]\n"
"s_waitcnt lgkmcnt(9)\n"
"s_add_u32 m0, s34, 1024\n"
"v_mfma_f32_16x16x32_bf16 v[204:207], a[60:63], v[52:55], v[204:207]\n"
"s_waitcnt lgkmcnt(6)\n"
"buffer_load_dwordx4 v190, s[8:11], s0 offen lds\n"
"v_mfma_f32_16x16x32_bf16 v[232:235], v[20:23], v[68:71], v[232:235]\n"
"v_mfma_f32_16x16x32_bf16 v[240:243], v[28:31], v[68:71], v[240:243]\n"
"v_mfma_f32_16x16x32_bf16 v[236:239], v[24:27], v[68:71], v[236:239]\n"
"s_waitcnt lgkmcnt(3)\n"
"v_smfmac_f32_16x16x64_bf16 v[252:255], v[90:93], v[224:231], v63\n"
"v_smfmac_f32_16x16x64_bf16 v[98:101], v[90:93], v[244:251], v63\n"
"v_cvt_pk_bf16_f32 v204, v204, v205\n"
"v_smfmac_f32_16x16x64_bf16 v[106:109], v[90:93], v[120:127], v63\n"
"ds_read_b128 v[120:123], v181 offset:13504\n"
"v_mfma_f32_16x16x32_bf16 v[90:93], v[172:175], v[90:93], v[82:85]\n"
"v_cvt_pk_bf16_f32 v232, v232, v233\n"
"v_cvt_pk_bf16_f32 v233, v234, v235\n"
"v_mfma_f32_16x16x32_bf16 v[208:211], a[4:7], v[32:35], 0\n"
"s_add_u32 m0, s34, 4096\n"
"ds_read_b64 v[82:83], v178 offset:27072\n"
"buffer_load_dwordx4 v191, s[8:11], s0 offen lds\n"
"v_mfma_f32_16x16x32_bf16 v[208:211], a[16:19], v[36:39], v[208:211]\n"
"s_add_u32 s0, s0, s33\n"
"ds_read_b64 v[84:85], v179 offset:27072\n"
"v_cvt_pk_bf16_f32 v240, v240, v241\n"
"v_mfma_f32_16x16x32_bf16 v[208:211], a[28:31], v[40:43], v[208:211]\n"
"v_cvt_pk_bf16_f32 v234, v236, v237\n"
"v_cvt_pk_bf16_f32 v235, v238, v239\n"
"v_mfma_f32_16x16x32_bf16 v[208:211], a[40:43], v[44:47], v[208:211]\n"
"ds_write_b128 v196, v[232:235] offset:30720\n"
"v_mfma_f32_16x16x32_bf16 v[208:211], a[52:55], v[48:51], v[208:211]\n"
"v_cvt_pk_bf16_f32 v241, v242, v243\n"
"v_cvt_pk_bf16_f32 v252, v252, v253\n"
"v_mfma_f32_16x16x32_bf16 v[208:211], a[64:67], v[52:55], v[208:211]\n"
"v_rcp_f32 v171, v90\n"
"v_mfma_f32_16x16x32_bf16 v[212:215], a[8:11], v[32:35], 0\n"
"s_waitcnt lgkmcnt(2)\n"
"ds_write_b64 v197, v[240:241] offset:30720\n"
"v_mfma_f32_16x16x32_bf16 v[136:139], v[136:139], v[152:155], 0\n"
"s_waitcnt lgkmcnt(2)\n"
"v_cvt_pk_bf16_f32 v171, v171, v171\n"
"v_and_b32 v92, v171, v176\n"
"v_mfma_f32_16x16x32_bf16 v[136:139], v[80:83], v[164:167], v[136:139]\n"
"s_add_u32 m0, s34, 2048\n"
"ds_read_b64 v[90:91], v180 offset:64\n"
"buffer_load_dwordx4 v190, s[8:11], s1 offen lds\n"
"v_mfma_f32_16x16x32_bf16 v[128:131], v[128:131], v[152:155], 0\n"
"v_and_b32 v93, v171, v177\n"
"v_cvt_pk_bf16_f32 v253, v254, v255\n"
"v_mfma_f32_16x16x32_bf16 v[128:131], v[84:87], v[166:169], v[128:131]\n"
"v_cvt_pk_bf16_f32 v106, v106, v107\n"
"v_cvt_pk_bf16_f32 v98, v98, v99\n"
"v_mfma_f32_16x16x16_bf16 v[252:255], v[252:253], v[92:93], 0\n"
"v_cvt_pk_bf16_f32 v107, v108, v109\n"
"v_cvt_pk_bf16_f32 v99, v100, v101\n"
"v_mfma_f32_16x16x32_bf16 v[212:215], a[20:23], v[36:39], v[212:215]\n"
"v_cvt_pk_bf16_f32 v205, v206, v207\n"
"v_cvt_pk_bf16_f32 v206, v208, v209\n"
"v_mfma_f32_16x16x16_bf16 v[106:109], v[106:107], v[92:93], 0\n"
"s_waitcnt lgkmcnt(1)\n"
"v_cvt_pk_bf16_f32 v207, v210, v211\n"
"v_cvt_pk_bf16_f32 v252, v252, v253\n"
"v_mfma_f32_16x16x16_bf16 v[98:101], v[98:99], v[92:93], 0\n"
"s_barrier\n"
"v_mfma_f32_16x16x32_bf16 v[212:215], a[32:35], v[40:43], v[212:215]\n"
"s_waitcnt lgkmcnt(0)\n"
"ds_read_b64 v[92:93], v181 offset:64\n"
"ds_read_b128 v[116:119], v180 offset:27008\n"
"v_mfma_f32_16x16x32_bf16 v[212:215], a[44:47], v[44:47], v[212:215]\n"
"s_add_u32 m0, s34, 5120\n"
"ds_write_b128 v186, v[204:207] offset:6752\n"
"v_mfma_f32_16x16x32_bf16 v[212:215], a[56:59], v[48:51], v[212:215]\n"
"buffer_load_dwordx4 v191, s[8:11], s1 offen lds\n"
"v_cvt_pk_bf16_f32 v253, v254, v255\n"
"v_mfma_f32_16x16x32_bf16 v[212:215], a[68:71], v[52:55], v[212:215]\n"
"s_add_u32 s1, s1, s33\n"
"v_cvt_pk_bf16_f32 v106, v106, v107\n"
"v_cvt_pk_bf16_f32 v254, v98, v99\n"
"v_mfma_f32_16x16x32_bf16 v[140:143], v[140:143], v[152:155], 0\n"
"ds_read_b64 v[98:99], v180 offset:13568\n"
"ds_read_b128 v[124:127], v181 offset:27008\n"
"v_mfma_f32_16x16x32_bf16 v[140:143], v[88:91], v[164:167], v[140:143]\n"
"s_waitcnt vmcnt(8)\n"
"v_cvt_pk_bf16_f32 v255, v100, v101\n"
"v_cvt_pk_bf16_f32 v107, v108, v109\n"
"v_mfma_f32_16x16x32_bf16 v[208:211], a[80:83], v[32:35], 0\n"
"s_waitcnt lgkmcnt(4)\n"
"v_add3_u32 v200, v200, s50, -5\n"
"v_cmp_lt_u32 vcc, 64, v200\n"
"v_mfma_f32_16x16x32_bf16 v[132:135], v[132:135], v[152:155], 0\n"
"buffer_store_dwordx4 v[252:255], v192, s[28:31], s37 offen sc1\n"
"v_mfma_f32_16x16x32_bf16 v[132:135], v[92:95], v[166:169], v[132:135]\n"
"s_nop 1\n"
"s_cbranch_vccnz .Lp4%=\n"
".Lr4%=:\n"
"v_mfma_f32_16x16x32_bf16 v[208:211], a[92:95], v[36:39], v[208:211]\n"
"ds_read_b64 v[100:101], v181 offset:13568\n"
"buffer_load_dwordx4 v[56:59], v192, s[28:31], s38 offen sc1\n"
"v_mfma_f32_16x16x32_bf16 v[208:211], a[104:107], v[40:43], v[208:211]\n"
"s_waitcnt lgkmcnt(2)\n"
"ds_read_b64 v[108:109], v181 offset:27072\n"
"v_cvt_pk_bf16_f32 v212, v212, v213\n"
"v_mfma_f32_16x16x32_bf16 v[112:115], v[112:115], v[152:155], 0\n"
"v_cvt_pk_bf16_f32 v213, v214, v215\n"
"v_max_f32 v136, v136, v128\n"
"v_mfma_f32_16x16x32_bf16 v[112:115], v[96:99], v[164:167], v[112:115]\n"
"v_max_f32 v137, v137, v129\n"
"v_max_f32 v138, v138, v130\n"
"v_mfma_f32_16x16x32_bf16 v[208:211], a[116:119], v[44:47], v[208:211]\n"
"ds_read_b64_tr_b16 v[128:129], v182 offset:27080\n"
"buffer_load_dwordx2 v[60:61], v193, s[28:31], s38 offen offset:1024 sc1\n"
"v_mfma_f32_16x16x32_bf16 v[208:211], a[128:131], v[48:51], v[208:211]\n"
"s_add_u32 s38, s38, 1572864\n"
"v_max_f32 v139, v139, v131\n"
"v_max3_f32 v170, v136, v137, v138\n"
"v_mfma_f32_16x16x32_bf16 v[208:211], a[140:143], v[52:55], v[208:211]\n"
"ds_read_b64_tr_b16 v[130:131], v183 offset:27080\n"
"v_max_f32 v140, v140, v132\n"
"v_mfma_f32_16x16x32_bf16 v[204:207], a[72:75], v[32:35], 0\n"
"s_waitcnt lgkmcnt(3)\n"
"v_max_f32 v141, v141, v133\n"
"v_max_f32 v142, v142, v134\n"
"v_mfma_f32_16x16x32_bf16 v[120:123], v[120:123], v[152:155], 0\n"
"s_waitcnt lgkmcnt(2)\n"
"ds_read_b64_tr_b16 v[132:133], v182 offset:27848\n"
"buffer_store_dwordx2 v[106:107], v193, s[28:31], s37 offen offset:1024 sc1\n"
"v_mfma_f32_16x16x32_bf16 v[120:123], v[100:103], v[166:169], v[120:123]\n"
"s_add_u32 s37, s37, 1572864\n"
"ds_read_b64 v[106:107], v180 offset:27072\n"
"v_cvt_pk_bf16_f32 v214, v208, v209\n"
"v_mfma_f32_16x16x32_bf16 v[124:127], v[124:127], v[152:155], 0\n"
"s_add_u32 s4, s37, 6291456\n"
"v_cvt_pk_bf16_f32 v215, v210, v211\n"
"v_max_f32 v143, v143, v135\n"
"v_mfma_f32_16x16x32_bf16 v[124:127], v[108:111], v[166:169], v[124:127]\n"
"ds_write_b128 v186, v[212:215] offset:6816\n"
"v_mfma_f32_16x16x32_bf16 v[204:207], a[84:87], v[36:39], v[204:207]\n"
"v_max3_f32 v170, v170, v139, v140\n"
"v_max3_f32 v170, v170, v141, v142\n"
"v_mfma_f32_16x16x32_bf16 v[204:207], a[96:99], v[40:43], v[204:207]\n"
"v_max_f32 v112, v112, v120\n"
"v_max_f32 v113, v113, v121\n"
"v_mfma_f32_16x16x32_bf16 v[204:207], a[108:111], v[44:47], v[204:207]\n"
"ds_read_b64_tr_b16 v[134:135], v183 offset:27848\n"
"ds_read_b64_tr_b16 v[120:121], v182 offset:54088\n"
"v_mfma_f32_16x16x32_bf16 v[204:207], a[120:123], v[48:51], v[204:207]\n"
"s_waitcnt lgkmcnt(3)\n"
"v_max_f32 v114, v114, v122\n"
"v_max_f32 v115, v115, v123\n"
"v_mfma_f32_16x16x32_bf16 v[116:119], v[116:119], v[152:155], 0\n"
"ds_read_b64_tr_b16 v[122:123], v183 offset:54088\n"
"v_max3_f32 v170, v170, v143, v112\n"
"v_mfma_f32_16x16x32_bf16 v[116:119], v[104:107], v[164:167], v[116:119]\n"
"v_max3_f32 v170, v170, v113, v114\n"
"v_mov_b64 v[252:253], 0\n"
"v_mfma_f32_16x16x32_bf16 v[204:207], a[132:135], v[52:55], v[204:207]\n"
"v_mov_b64 v[254:255], 0\n"
"v_mov_b64 v[98:99], 0\n"
"v_mfma_f32_16x16x32_bf16 v[208:211], a[76:79], v[32:35], 0\n"
"v_mov_b64 v[100:101], 0\n"
"v_mov_b64 v[106:107], 0\n"
"v_mfma_f32_16x16x32_bf16 v[208:211], a[88:91], v[36:39], v[208:211]\n"
"v_max_f32 v116, v116, v124\n"
"v_max_f32 v117, v117, v125\n"
"v_mfma_f32_16x16x32_bf16 v[208:211], a[100:103], v[40:43], v[208:211]\n"
"ds_read_b64_tr_b16 v[124:125], v182 offset:54856\n"
"v_max_f32 v118, v118, v126\n"
"v_mfma_f32_16x16x32_bf16 v[208:211], a[112:115], v[44:47], v[208:211]\n"
"v_mov_b64 v[64:65], -1\n"
"v_mov_b64 v[66:67], -1\n"
"v_mfma_f32_16x16x32_bf16 v[208:211], a[124:127], v[48:51], v[208:211]\n"
"buffer_store_dwordx4 v[64:67], v192, s[28:31], s4 offen sc1\n"
"v_mfma_f32_16x16x32_bf16 v[208:211], a[136:139], v[52:55], v[208:211]\n"
"s_add_u32 s4, s37, 6291456\n"
"ds_read_b128 v[64:67], v202 offset:30720\n"
"v_max_f32 v119, v119, v127\n"
"v_mfma_f32_16x16x32_bf16 v[212:215], a[144:147], v[32:35], 0\n"
"ds_read_b64_tr_b16 v[126:127], v183 offset:54856\n"
"v_max3_f32 v170, v170, v115, v116\n"
"v_mfma_f32_16x16x32_bf16 v[212:215], a[156:159], v[36:39], v[212:215]\n"
"v_max3_f32 v170, v170, v117, v118\n"
"v_cvt_pk_bf16_f32 v204, v204, v205\n"
"v_mfma_f32_16x16x32_bf16 v[212:215], a[168:171], v[40:43], v[212:215]\n"
"v_cvt_pk_bf16_f32 v205, v206, v207\n"
"v_mov_b64 v[108:109], 0\n"
"v_mfma_f32_16x16x32_bf16 v[212:215], a[180:183], v[44:47], v[212:215]\n"
"v_max_f32 v171, v170, v119\n"
"v_max_f32 v170, v170, v119\n"
"v_mfma_f32_16x16x32_bf16 v[212:215], a[192:195], v[48:51], v[212:215]\n"
"s_nop 0\n"
"v_permlane16_swap_b32 v170, v171\n"
"v_mfma_f32_16x16x32_bf16 v[212:215], a[204:207], v[52:55], v[212:215]\n"
"v_max_f32 v170, v170, v171\n"
"v_mov_b32 v171, v170\n"
"v_cvt_pk_bf16_f32 v206, v208, v209\n"
"v_cvt_pk_bf16_f32 v207, v210, v211\n"
"v_mfma_f32_16x16x32_bf16 v[208:211], a[148:151], v[32:35], 0\n"
"s_waitcnt lgkmcnt(1)\n"
"buffer_store_dwordx4 v[64:67], v194, s[24:27], s36 offen sc1\n"
"v_mfma_f32_16x16x32_bf16 v[208:211], a[160:163], v[36:39], v[208:211]\n"
"s_nop 0\n"
"ds_read_b128 v[64:67], v192 offset:36864\n"
"ds_write_b128 v186, v[204:207] offset:6880\n"
"v_mfma_f32_16x16x32_bf16 v[208:211], a[172:175], v[40:43], v[208:211]\n"
"v_permlane32_swap_b32 v170, v171\n"
"v_max_f32 v170, v170, v171\n"
"v_mul_f32 v170, 0xbe553b95, v170\n"
"v_pk_fma_f32 v[136:137], v[136:137], s[70:71], v[170:171] op_sel_hi:[1,0,0]\n"
"v_pk_fma_f32 v[138:139], v[138:139], s[70:71], v[170:171] op_sel_hi:[1,0,0]\n"
"v_pk_fma_f32 v[140:141], v[140:141], s[70:71], v[170:171] op_sel_hi:[1,0,0]\n"
"v_pk_fma_f32 v[142:143], v[142:143], s[70:71], v[170:171] op_sel_hi:[1,0,0]\n"
"v_pk_fma_f32 v[112:113], v[112:113], s[70:71], v[170:171] op_sel_hi:[1,0,0]\n"
"v_pk_fma_f32 v[114:115], v[114:115], s[70:71], v[170:171] op_sel_hi:[1,0,0]\n"
"v_pk_fma_f32 v[116:117], v[116:117], s[70:71], v[170:171] op_sel_hi:[1,0,0]\n"
"v_pk_fma_f32 v[118:119], v[118:119], s[70:71], v[170:171] op_sel_hi:[1,0,0]\n"
"v_mfma_f32_16x16x32_bf16 v[208:211], a[184:187], v[44:47], v[208:211]\n"
"v_exp_f32 v136, v136\n"
"v_mfma_f32_16x16x32_bf16 v[208:211], a[196:199], v[48:51], v[208:211]\n"
"v_exp_f32 v137, v137\n"
"v_mfma_f32_16x16x32_bf16 v[208:211], a[208:211], v[52:55], v[208:211]\n"
"s_waitcnt lgkmcnt(1)\n"
"v_exp_f32 v138, v138\n"
"v_mfma_f32_16x16x32_bf16 v[232:235], a[216:219], v[64:67], 0\n"
"v_exp_f32 v139, v139\n"
"v_mfma_f32_16x16x32_bf16 v[236:239], a[220:223], v[64:67], 0\n"
"v_cvt_pk_bf16_f32 v82, v136, v138\n"
"v_cvt_pk_bf16_f32 v83, v137, v139\n"
"v_mfma_f32_16x16x32_bf16 v[240:243], a[224:227], v[64:67], 0\n"
"ds_read_b128 v[64:67], v192 offset:38912\n"
"ds_read_b128 v[136:139], v180\n"
"v_mfma_f32_16x16x32_bf16 v[204:207], a[152:155], v[32:35], 0\n"
"ds_read_b128 v[32:35], v188 offset:6144\n"
"v_cvt_pk_bf16_f32 v152, v212, v213\n"
"v_mfma_f32_16x16x32_bf16 v[204:207], a[164:167], v[36:39], v[204:207]\n"
"ds_read_b128 v[36:39], v189 offset:6144\n"
"v_cvt_pk_bf16_f32 v153, v214, v215\n"
"v_mfma_f32_16x16x32_bf16 v[204:207], a[176:179], v[40:43], v[204:207]\n"
"ds_read_b128 v[40:43], v188 offset:7168\n"
"v_exp_f32 v140, v140\n"
"v_mfma_f32_16x16x32_bf16 v[204:207], a[188:191], v[44:47], v[204:207]\n"
"ds_read_b128 v[44:47], v189 offset:7168\n"
"v_exp_f32 v141, v141\n"
"v_mfma_f32_16x16x32_bf16 v[204:207], a[200:203], v[48:51], v[204:207]\n"
"ds_read_b128 v[48:51], v188 offset:8192\n"
"v_mov_b64 v[68:69], -1\n"
"v_mfma_f32_16x16x32_bf16 v[204:207], a[212:215], v[52:55], v[204:207]\n"
"buffer_store_dwordx2 v[68:69], v193, s[28:31], s4 offen offset:1024 sc1\n"
"ds_read_b128 v[52:55], v189 offset:8192\n"
"ds_read_b64 v[68:69], v62 offset:30720\n"
"v_exp_f32 v142, v142\n"
"v_exp_f32 v143, v143\n"
"v_cvt_pk_bf16_f32 v84, v140, v142\n"
"v_cvt_pk_bf16_f32 v85, v141, v143\n"
"v_cvt_pk_bf16_f32 v154, v208, v209\n"
"v_exp_f32 v112, v112\n"
"v_smfmac_f32_16x16x64_bf16 v[252:255], v[82:85], v[216:223], v63\n"
"s_waitcnt lgkmcnt(5)\n"
"ds_read_b64_tr_b16 v[216:217], v182 offset:54144\n"
"v_smfmac_f32_16x16x64_bf16 v[98:101], v[82:85], v[72:79], v63\n"
"s_add_u32 m0, s34, 6144\n"
"s_nop 0\n"
"buffer_load_dwordx4 v190, s[8:11], s32 offen lds\n"
"v_smfmac_f32_16x16x64_bf16 v[106:109], v[82:85], v[128:135], v63\n"
"ds_read_b64_tr_b16 v[218:219], v183 offset:54144\n"
"v_mfma_f32_16x16x32_bf16 v[82:85], v[172:175], v[82:85], 0\n"
"ds_read_b64_tr_b16 v[220:221], v182 offset:54912\n"
"ds_read_b64_tr_b16 v[222:223], v183 offset:54912\n"
"v_mfma_f32_16x16x32_bf16 v[212:215], a[0:3], v[32:35], 0\n"
"s_waitcnt lgkmcnt(4)\n"
"ds_read_b64_tr_b16 v[72:73], v182 offset:54152\n"
"v_cvt_pk_bf16_f32 v155, v210, v211\n"
"v_mfma_f32_16x16x32_bf16 v[212:215], a[12:15], v[36:39], v[212:215]\n"
"buffer_store_dwordx2 v[68:69], v195, s[24:27], s36 offen sc1\n"
"v_mfma_f32_16x16x32_bf16 v[212:215], a[24:27], v[40:43], v[212:215]\n"
"s_add_u32 s36, s36, s33\n"
"ds_read_b128 v[68:71], v192 offset:37888\n"
"ds_read_b64_tr_b16 v[74:75], v183 offset:54152\n"
"v_mfma_f32_16x16x32_bf16 v[212:215], a[36:39], v[44:47], v[212:215]\n"
"ds_read_b64_tr_b16 v[76:77], v182 offset:54920\n"
"v_cvt_pk_bf16_f32 v166, v204, v205\n"
"v_mfma_f32_16x16x32_bf16 v[212:215], a[48:51], v[48:51], v[212:215]\n"
"ds_read_b64_tr_b16 v[78:79], v183 offset:54920\n"
"v_cvt_pk_bf16_f32 v167, v206, v207\n"
"v_mfma_f32_16x16x32_bf16 v[212:215], a[60:63], v[52:55], v[212:215]\n"
"s_add_u32 m0, s34, 9216\n"
"ds_read_b128 v[128:131], v181\n"
"buffer_load_dwordx4 v191, s[8:11], s32 offen lds\n"
"v_mfma_f32_16x16x32_bf16 v[208:211], a[4:7], v[32:35], 0\n"
"s_add_u32 s32, s32, s33\n"
"ds_read_b128 v[140:143], v180 offset:13504\n"
"v_exp_f32 v113, v113\n"
"v_mfma_f32_16x16x32_bf16 v[208:211], a[16:19], v[36:39], v[208:211]\n"
"ds_read_b128 v[132:135], v181 offset:13504\n"
"v_exp_f32 v114, v114\n"
"v_mfma_f32_16x16x32_bf16 v[208:211], a[28:31], v[40:43], v[208:211]\n"
"v_exp_f32 v115, v115\n"
"v_mfma_f32_16x16x32_bf16 v[208:211], a[40:43], v[44:47], v[208:211]\n"
"s_waitcnt lgkmcnt(6)\n"
"v_cvt_pk_bf16_f32 v90, v112, v114\n"
"v_cvt_pk_bf16_f32 v91, v113, v115\n"
"v_mfma_f32_16x16x32_bf16 v[232:235], a[228:231], v[68:71], v[232:235]\n"
"ds_read_b128 v[112:115], v180 offset:27008\n"
"v_exp_f32 v116, v116\n"
"v_mfma_f32_16x16x32_bf16 v[232:235], a[240:243], v[64:67], v[232:235]\n"
"v_exp_f32 v117, v117\n"
"v_mfma_f32_16x16x32_bf16 v[236:239], a[232:235], v[68:71], v[236:239]\n"
"s_add_u32 m0, s34, 7168\n"
"v_exp_f32 v118, v118\n"
"v_mfma_f32_16x16x32_bf16 v[236:239], a[244:247], v[64:67], v[236:239]\n"
"buffer_load_dwordx4 v190, s[8:11], s0 offen lds\n"
"v_cvt_pk_bf16_f32 v92, v116, v118\n"
"v_mfma_f32_16x16x32_bf16 v[240:243], a[236:239], v[68:71], v[240:243]\n"
"ds_read_b128 v[68:71], v192 offset:39936\n"
"v_exp_f32 v119, v119\n"
"v_mfma_f32_16x16x32_bf16 v[240:243], a[248:251], v[64:67], v[240:243]\n"
"ds_read_b128 v[64:67], v192 offset:40960\n"
"v_cvt_pk_bf16_f32 v93, v117, v119\n"
"v_mfma_f32_16x16x32_bf16 v[208:211], a[52:55], v[48:51], v[208:211]\n"
"s_waitcnt lgkmcnt(6)\n"
"v_cvt_pk_bf16_f32 v212, v212, v213\n"
"v_cvt_pk_bf16_f32 v213, v214, v215\n"
"v_smfmac_f32_16x16x64_bf16 v[252:255], v[90:93], v[216:223], v63\n"
"s_mov_b64 exec, s[44:45]\n"
"v_add_u32 v179, 0xc0, v179\n"
"s_mov_b64 exec, -1\n"
"v_smfmac_f32_16x16x64_bf16 v[98:101], v[90:93], v[72:79], v63\n"
"v_smfmac_f32_16x16x64_bf16 v[106:109], v[90:93], v[120:127], v63\n"
"ds_read_b128 v[120:123], v181 offset:27008\n"
"v_mfma_f32_16x16x32_bf16 v[90:93], v[172:175], v[90:93], v[82:85]\n"
"s_waitcnt lgkmcnt(2)\n"
"v_cvt_pk_bf16_f32 v252, v252, v253\n"
"v_cvt_pk_bf16_f32 v253, v254, v255\n"
"v_mfma_f32_16x16x32_bf16 v[232:235], a[252:255], v[68:71], v[232:235]\n"
"s_add_u32 m0, s34, 10240\n"
"ds_read_b64 v[82:83], v180 offset:64\n"
"buffer_load_dwordx4 v191, s[8:11], s0 offen lds\n"
"v_mfma_f32_16x16x32_bf16 v[236:239], v[0:3], v[68:71], v[236:239]\n"
"s_waitcnt lgkmcnt(2)\n"
"ds_read_b64 v[84:85], v181 offset:64\n"
"v_cvt_pk_bf16_f32 v98, v98, v99\n"
"v_mfma_f32_16x16x32_bf16 v[236:239], v[12:15], v[64:67], v[236:239]\n"
"s_add_u32 s0, s0, s33\n"
"v_rcp_f32 v171, v90\n"
"v_mfma_f32_16x16x32_bf16 v[240:243], v[4:7], v[68:71], v[240:243]\n"
"ds_read_b128 v[68:71], v192 offset:41984\n"
"ds_read_b64 v[90:91], v180 offset:13568\n"
"v_mfma_f32_16x16x32_bf16 v[240:243], v[16:19], v[64:67], v[240:243]\n"
"v_cvt_pk_bf16_f32 v171, v171, v171\n"
"v_and_b32 v92, v171, v176\n"
"v_mfma_f32_16x16x32_bf16 v[232:235], v[8:11], v[64:67], v[232:235]\n"
"v_and_b32 v93, v171, v177\n"
"v_cvt_pk_bf16_f32 v99, v100, v101\n"
"v_mfma_f32_16x16x32_bf16 v[208:211], a[64:67], v[52:55], v[208:211]\n"
"v_cvt_pk_bf16_f32 v106, v106, v107\n"
"v_cvt_pk_bf16_f32 v107, v108, v109\n"
"v_mfma_f32_16x16x16_bf16 v[252:255], v[252:253], v[92:93], 0\n"
"s_waitcnt lgkmcnt(3)\n"
"s_add_u32 m0, s34, 8192\n"
"v_mfma_f32_16x16x16_bf16 v[98:101], v[98:99], v[92:93], 0\n"
"s_waitcnt lgkmcnt(2)\n"
"buffer_load_dwordx4 v190, s[8:11], s1 offen lds\n"
"v_cvt_pk_bf16_f32 v214, v208, v209\n"
"v_mfma_f32_16x16x16_bf16 v[106:109], v[106:107], v[92:93], 0\n"
"ds_read_b64 v[92:93], v181 offset:13568\n"
"v_cvt_pk_bf16_f32 v252, v252, v253\n"
"v_mfma_f32_16x16x32_bf16 v[136:139], v[136:139], v[144:147], 0\n"
"s_waitcnt lgkmcnt(2)\n"
"v_cvt_pk_bf16_f32 v253, v254, v255\n"
"v_cvt_pk_bf16_f32 v254, v98, v99\n"
"v_mfma_f32_16x16x32_bf16 v[232:235], v[20:23], v[68:71], v[232:235]\n"
"s_waitcnt lgkmcnt(1)\n"
"ds_read_b64 v[98:99], v180 offset:27072\n"
"v_cvt_pk_bf16_f32 v255, v100, v101\n"
"v_mfma_f32_16x16x32_bf16 v[240:243], v[28:31], v[68:71], v[240:243]\n"
"buffer_store_dwordx4 v[252:255], v192, s[28:31], s37 offen sc1\n"
"v_mfma_f32_16x16x32_bf16 v[236:239], v[24:27], v[68:71], v[236:239]\n"
"ds_read_b64 v[100:101], v181 offset:27072\n"
"v_cvt_pk_bf16_f32 v106, v106, v107\n"
"v_mfma_f32_16x16x32_bf16 v[128:131], v[128:131], v[144:147], 0\n"
"v_cvt_pk_bf16_f32 v232, v232, v233\n"
"v_cvt_pk_bf16_f32 v233, v234, v235\n"
"v_mfma_f32_16x16x32_bf16 v[128:131], v[84:87], v[158:161], v[128:131]\n"
"s_waitcnt lgkmcnt(2)\n"
"v_cvt_pk_bf16_f32 v240, v240, v241\n"
"v_cvt_pk_bf16_f32 v234, v236, v237\n"
"v_mfma_f32_16x16x32_bf16 v[136:139], v[80:83], v[156:159], v[136:139]\n"
"s_add_u32 m0, s34, 11264\n"
"v_cvt_pk_bf16_f32 v235, v238, v239\n"
"v_cvt_pk_bf16_f32 v241, v242, v243\n"
"v_mfma_f32_16x16x32_bf16 v[140:143], v[140:143], v[144:147], 0\n"
"s_waitcnt lgkmcnt(1)\n"
"buffer_load_dwordx4 v191, s[8:11], s1 offen lds\n"
"ds_write_b128 v196, v[232:235] offset:55296\n"
"v_mfma_f32_16x16x32_bf16 v[140:143], v[88:91], v[156:159], v[140:143]\n"
"s_add_u32 s1, s1, s33\n"
"v_cvt_pk_bf16_f32 v107, v108, v109\n"
"v_cvt_pk_bf16_f32 v215, v210, v211\n"
"v_mfma_f32_16x16x32_bf16 v[132:135], v[132:135], v[144:147], 0\n"
"s_waitcnt lgkmcnt(1)\n"
"buffer_store_dwordx2 v[106:107], v193, s[28:31], s37 offen offset:1024 sc1\n"
"v_mfma_f32_16x16x32_bf16 v[132:135], v[92:95], v[158:161], v[132:135]\n"
"s_add_u32 s37, s37, 1572864\n"
"ds_write_b64 v197, v[240:241] offset:55296\n"
"v_mfma_f32_16x16x32_bf16 v[112:115], v[112:115], v[144:147], 0\n"
"s_add_u32 s4, s37, 6291456\n"
"v_max_f32 v136, v136, v128\n"
"v_max_f32 v137, v137, v129\n"
"v_mfma_f32_16x16x32_bf16 v[112:115], v[96:99], v[156:159], v[112:115]\n"
"ds_read_b64_tr_b16 v[128:129], v182 offset:40584\n"
"v_max_f32 v138, v138, v130\n"
"v_mfma_f32_16x16x32_bf16 v[120:123], v[120:123], v[144:147], 0\n"
"v_max_f32 v139, v139, v131\n"
"v_max3_f32 v170, v136, v137, v138\n"
"v_mfma_f32_16x16x32_bf16 v[120:123], v[100:103], v[158:161], v[120:123]\n"
"ds_read_b64_tr_b16 v[130:131], v183 offset:40584\n"
"v_max_f32 v140, v140, v132\n"
"v_mfma_f32_16x16x32_bf16 v[204:207], a[8:11], v[32:35], 0\n"
"v_max_f32 v141, v141, v133\n"
"v_max_f32 v142, v142, v134\n"
"v_mfma_f32_16x16x32_bf16 v[204:207], a[20:23], v[36:39], v[204:207]\n"
"ds_read_b64_tr_b16 v[132:133], v182 offset:41352\n"
"v_max_f32 v143, v143, v135\n"
"v_mfma_f32_16x16x32_bf16 v[204:207], a[32:35], v[40:43], v[204:207]\n"
"s_waitcnt vmcnt(14)\n"
"ds_read_b64_tr_b16 v[134:135], v183 offset:41352\n"
"v_max3_f32 v170, v170, v139, v140\n"
"v_mfma_f32_16x16x32_bf16 v[204:207], a[44:47], v[44:47], v[204:207]\n"
"ds_write_b128 v202, v[56:59] offset:43008\n"
"v_mfma_f32_16x16x32_bf16 v[204:207], a[56:59], v[48:51], v[204:207]\n"
"buffer_load_dwordx4 v[56:59], v192, s[28:31], s38 offen sc1\n"
"v_max3_f32 v170, v170, v141, v142\n"
"v_mfma_f32_16x16x32_bf16 v[204:207], a[68:71], v[52:55], v[204:207]\n"
"v_max_f32 v112, v112, v120\n"
"v_max_f32 v113, v113, v121\n"
"v_mfma_f32_16x16x32_bf16 v[208:211], a[80:83], v[32:35], 0\n"
"s_waitcnt vmcnt(13)\n"
"v_or_b32 v201, 8, s6\n"
"v_max_f32 v114, v114, v122\n"
"v_mfma_f32_16x16x32_bf16 v[208:211], a[92:95], v[36:39], v[208:211]\n"
"s_mov_b64 exec, 1\n"
"buffer_store_dword v201, off, s[28:31], s40 sc1\n"
"s_mov_b64 exec, -1\n"
"v_mfma_f32_16x16x32_bf16 v[208:211], a[104:107], v[40:43], v[208:211]\n"
"ds_write_b64 v203, v[60:61] offset:43008\n"
"v_mfma_f32_16x16x32_bf16 v[208:211], a[116:119], v[44:47], v[208:211]\n"
"buffer_load_dwordx2 v[60:61], v193, s[28:31], s38 offen offset:1024 sc1\n"
"v_mov_b64 v[64:65], -1\n"
"v_mfma_f32_16x16x32_bf16 v[208:211], a[128:131], v[48:51], v[208:211]\n"
"s_add_u32 s38, s38, 1572864\n"
"v_mov_b64 v[66:67], -1\n"
"v_cvt_pk_bf16_f32 v204, v204, v205\n"
"v_mfma_f32_16x16x32_bf16 v[208:211], a[140:143], v[52:55], v[208:211]\n"
"v_cvt_pk_bf16_f32 v205, v206, v207\n"
"v_max_f32 v115, v115, v123\n"
"v_max3_f32 v170, v170, v143, v112\n"
"v_max3_f32 v170, v170, v113, v114\n"
"v_mov_b64 v[252:253], 0\n"
"v_mov_b64 v[254:255], 0\n"
"v_mov_b64 v[98:99], 0\n"
"buffer_store_dwordx4 v[64:67], v192, s[28:31], s4 offen sc1\n"
"s_add_u32 s4, s37, 6291456\n"
"v_cvt_pk_bf16_f32 v206, v208, v209\n"
"s_waitcnt lgkmcnt(0)\n"
"s_barrier\n"
"ds_write_b128 v186, v[212:215] offset:20256\n"
"v_mfma_f32_16x16x32_bf16 v[212:215], a[72:75], v[32:35], 0\n"
"v_cvt_pk_bf16_f32 v207, v210, v211\n"
"v_mov_b64 v[100:101], 0\n"
"v_mfma_f32_16x16x32_bf16 v[212:215], a[84:87], v[36:39], v[212:215]\n"
"ds_read_b128 v[116:119], v178\n"
"v_mfma_f32_16x16x32_bf16 v[212:215], a[96:99], v[40:43], v[212:215]\n"
"ds_read_b64 v[106:107], v178 offset:64\n"
"ds_read_b128 v[124:127], v179\n"
"v_mfma_f32_16x16x32_bf16 v[212:215], a[108:111], v[44:47], v[212:215]\n"
"ds_read_b64 v[108:109], v179 offset:64\n"
"v_mfma_f32_16x16x32_bf16 v[212:215], a[120:123], v[48:51], v[212:215]\n"
"ds_read_b128 v[64:67], v202 offset:55296\n"
"v_mfma_f32_16x16x32_bf16 v[212:215], a[132:135], v[52:55], v[212:215]\n"
"ds_write_b128 v186, v[204:207] offset:20320\n"
"v_mfma_f32_16x16x32_bf16 v[208:211], a[76:79], v[32:35], 0\n"
"v_mfma_f32_16x16x32_bf16 v[208:211], a[88:91], v[36:39], v[208:211]\n"
"ds_read_b64_tr_b16 v[120:121], v184 offset:72\n"
"v_mfma_f32_16x16x32_bf16 v[208:211], a[100:103], v[40:43], v[208:211]\n"
"ds_read_b64_tr_b16 v[122:123], v185 offset:72\n"
"v_mfma_f32_16x16x32_bf16 v[208:211], a[112:115], v[44:47], v[208:211]\n"
"s_waitcnt lgkmcnt(6)\n"
"v_cvt_pk_bf16_f32 v212, v212, v213\n"
"v_cvt_pk_bf16_f32 v213, v214, v215\n"
"v_mfma_f32_16x16x32_bf16 v[116:119], v[116:119], v[144:147], 0\n"
"v_mfma_f32_16x16x32_bf16 v[116:119], v[104:107], v[156:159], v[116:119]\n"
"s_waitcnt lgkmcnt(4)\n"
"v_mov_b64 v[106:107], 0\n"
"v_mfma_f32_16x16x32_bf16 v[124:127], v[124:127], v[144:147], 0\n"
"s_waitcnt lgkmcnt(3)\n"
"buffer_store_dwordx4 v[64:67], v194, s[24:27], s36 offen sc1\n"
"v_mfma_f32_16x16x32_bf16 v[124:127], v[108:111], v[158:161], v[124:127]\n"
"s_nop 0\n"
"ds_read_b128 v[64:67], v192 offset:43008\n"
"v_mov_b64 v[108:109], 0\n"
"v_mfma_f32_16x16x32_bf16 v[208:211], a[124:127], v[48:51], v[208:211]\n"
"v_mfma_f32_16x16x32_bf16 v[208:211], a[136:139], v[52:55], v[208:211]\n"
"v_mfma_f32_16x16x32_bf16 v[204:207], a[144:147], v[32:35], 0\n"
"v_mfma_f32_16x16x32_bf16 v[204:207], a[156:159], v[36:39], v[204:207]\n"
"v_mfma_f32_16x16x32_bf16 v[204:207], a[168:171], v[40:43], v[204:207]\n"
"v_max_f32 v116, v116, v124\n"
"v_mfma_f32_16x16x32_bf16 v[204:207], a[180:183], v[44:47], v[204:207]\n"
"v_max_f32 v117, v117, v125\n"
"v_max_f32 v118, v118, v126\n"
"v_mfma_f32_16x16x32_bf16 v[204:207], a[192:195], v[48:51], v[204:207]\n"
"s_waitcnt lgkmcnt(0)\n"
"ds_read_b64_tr_b16 v[124:125], v184 offset:840\n"
"v_mov_b64 v[68:69], -1\n"
"v_mfma_f32_16x16x32_bf16 v[232:235], a[216:219], v[64:67], 0\n"
"buffer_store_dwordx2 v[68:69], v193, s[28:31], s4 offen offset:1024 sc1\n"
"v_mfma_f32_16x16x32_bf16 v[236:239], a[220:223], v[64:67], 0\n"
"ds_read_b64 v[68:69], v62 offset:55296\n"
"v_max_f32 v119, v119, v127\n"
"v_mfma_f32_16x16x32_bf16 v[240:243], a[224:227], v[64:67], 0\n"
"ds_read_b128 v[64:67], v192 offset:45056\n"
"ds_read_b64_tr_b16 v[126:127], v185 offset:840\n"
"v_mfma_f32_16x16x32_bf16 v[204:207], a[204:207], v[52:55], v[204:207]\n"
"v_max3_f32 v170, v170, v115, v116\n"
"v_max3_f32 v170, v170, v117, v118\n"
"v_max_f32 v171, v170, v119\n"
"v_max_f32 v170, v170, v119\n"
"v_cvt_pk_bf16_f32 v214, v208, v209\n"
"v_cvt_pk_bf16_f32 v215, v210, v211\n"
"v_mfma_f32_16x16x32_bf16 v[208:211], a[148:151], v[32:35], 0\n"
"ds_write_b128 v186, v[212:215] offset:20384\n"
"v_mfma_f32_16x16x32_bf16 v[208:211], a[160:163], v[36:39], v[208:211]\n"
"v_permlane16_swap_b32 v170, v171\n"
"v_mfma_f32_16x16x32_bf16 v[208:211], a[172:175], v[40:43], v[208:211]\n"
"s_waitcnt lgkmcnt(3)\n"
"buffer_store_dwordx2 v[68:69], v195, s[24:27], s36 offen sc1\n"
"v_mfma_f32_16x16x32_bf16 v[208:211], a[184:187], v[44:47], v[208:211]\n"
"s_add_u32 s36, s36, s33\n"
"ds_read_b128 v[68:71], v192 offset:44032\n"
"v_max_f32 v170, v170, v171\n"
"v_mfma_f32_16x16x32_bf16 v[208:211], a[196:199], v[48:51], v[208:211]\n"
"v_mov_b32 v171, v170\n"
"v_cvt_pk_bf16_f32 v144, v204, v205\n"
"v_mfma_f32_16x16x32_bf16 v[208:211], a[208:211], v[52:55], v[208:211]\n"
"v_permlane32_swap_b32 v170, v171\n"
"v_max_f32 v170, v170, v171\n"
"v_mul_f32 v170, 0xbe553b95, v170\n"
"v_pk_fma_f32 v[136:137], v[136:137], s[70:71], v[170:171] op_sel_hi:[1,0,0]\n"
"v_pk_fma_f32 v[138:139], v[138:139], s[70:71], v[170:171] op_sel_hi:[1,0,0]\n"
"v_pk_fma_f32 v[140:141], v[140:141], s[70:71], v[170:171] op_sel_hi:[1,0,0]\n"
"v_pk_fma_f32 v[142:143], v[142:143], s[70:71], v[170:171] op_sel_hi:[1,0,0]\n"
"v_pk_fma_f32 v[112:113], v[112:113], s[70:71], v[170:171] op_sel_hi:[1,0,0]\n"
"v_pk_fma_f32 v[114:115], v[114:115], s[70:71], v[170:171] op_sel_hi:[1,0,0]\n"
"v_pk_fma_f32 v[116:117], v[116:117], s[70:71], v[170:171] op_sel_hi:[1,0,0]\n"
"v_pk_fma_f32 v[118:119], v[118:119], s[70:71], v[170:171] op_sel_hi:[1,0,0]\n"
"v_mfma_f32_16x16x32_bf16 v[212:215], a[152:155], v[32:35], 0\n"
"ds_read_b128 v[32:35], v188\n"
"v_exp_f32 v136, v136\n"
"v_mfma_f32_16x16x32_bf16 v[212:215], a[164:167], v[36:39], v[212:215]\n"
"s_waitcnt lgkmcnt(1)\n"
"ds_read_b128 v[36:39], v189\n"
"v_cvt_pk_bf16_f32 v145, v206, v207\n"
"v_mfma_f32_16x16x32_bf16 v[232:235], a[228:231], v[68:71], v[232:235]\n"
"v_exp_f32 v137, v137\n"
"v_mfma_f32_16x16x32_bf16 v[232:235], a[240:243], v[64:67], v[232:235]\n"
"v_exp_f32 v138, v138\n"
"v_mfma_f32_16x16x32_bf16 v[236:239], a[232:235], v[68:71], v[236:239]\n"
"v_exp_f32 v139, v139\n"
"v_mfma_f32_16x16x32_bf16 v[236:239], a[244:247], v[64:67], v[236:239]\n"
"v_cvt_pk_bf16_f32 v82, v136, v138\n"
"v_cvt_pk_bf16_f32 v83, v137, v139\n"
"v_mfma_f32_16x16x32_bf16 v[240:243], a[236:239], v[68:71], v[240:243]\n"
"ds_read_b128 v[68:71], v192 offset:46080\n"
"ds_read_b128 v[136:139], v180 offset:13504\n"
"v_mfma_f32_16x16x32_bf16 v[240:243], a[248:251], v[64:67], v[240:243]\n"
"s_waitcnt lgkmcnt(3)\n"
"ds_read_b128 v[64:67], v192 offset:47104\n"
"v_exp_f32 v140, v140\n"
"v_mfma_f32_16x16x32_bf16 v[212:215], a[176:179], v[40:43], v[212:215]\n"
"s_waitcnt lgkmcnt(3)\n"
"ds_read_b128 v[40:43], v188 offset:1024\n"
"v_exp_f32 v141, v141\n"
".Lic5%=:\n"
"s_cbranch_execz .Lib1%=\n"
"v_mfma_f32_16x16x32_bf16 v[212:215], a[188:191], v[44:47], v[212:215]\n"
"s_add_u32 m0, s34, 0\n"
"ds_read_b128 v[44:47], v189 offset:1024\n"
"buffer_load_dwordx4 v190, s[8:11], s32 offen lds\n"
"v_mfma_f32_16x16x32_bf16 v[212:215], a[200:203], v[48:51], v[212:215]\n"
"s_add_u32 s32, s32, s33\n"
"ds_read_b128 v[48:51], v188 offset:2048\n"
"v_exp_f32 v142, v142\n"
"v_mfma_f32_16x16x32_bf16 v[212:215], a[212:215], v[52:55], v[212:215]\n"
"ds_read_b128 v[52:55], v189 offset:2048\n"
"v_cvt_pk_bf16_f32 v84, v140, v142\n"
"v_mfma_f32_16x16x32_bf16 v[204:207], a[0:3], v[32:35], 0\n"
"s_waitcnt lgkmcnt(6)\n"
"v_exp_f32 v143, v143\n"
"v_mfma_f32_16x16x32_bf16 v[232:235], a[252:255], v[68:71], v[232:235]\n"
"v_cvt_pk_bf16_f32 v85, v141, v143\n"
"v_cvt_pk_bf16_f32 v146, v208, v209\n"
"v_mfma_f32_16x16x32_bf16 v[236:239], v[0:3], v[68:71], v[236:239]\n"
"s_waitcnt lgkmcnt(4)\n"
"ds_read_b128 v[140:143], v180 offset:27008\n"
"v_cvt_pk_bf16_f32 v147, v210, v211\n"
"v_mfma_f32_16x16x32_bf16 v[236:239], v[12:15], v[64:67], v[236:239]\n"
"v_exp_f32 v112, v112\n"
"v_smfmac_f32_16x16x64_bf16 v[252:255], v[82:85], v[224:231], v63\n"
"ds_read_b64_tr_b16 v[224:225], v184 offset:128\n"
"v_smfmac_f32_16x16x64_bf16 v[98:101], v[82:85], v[244:251], v63\n"
"s_waitcnt lgkmcnt(4)\n"
"ds_read_b64_tr_b16 v[226:227], v185 offset:128\n"
"v_mfma_f32_16x16x32_bf16 v[240:243], v[4:7], v[68:71], v[240:243]\n"
"s_waitcnt lgkmcnt(3)\n"
"ds_read_b128 v[68:71], v192 offset:48128\n"
"ds_read_b64_tr_b16 v[228:229], v184 offset:896\n"
"v_mfma_f32_16x16x32_bf16 v[240:243], v[16:19], v[64:67], v[240:243]\n"
"s_add_u32 m0, s34, 3072\n"
"ds_read_b64_tr_b16 v[230:231], v185 offset:896\n"
"buffer_load_dwordx4 v199, s[8:11], s75 offen lds\n"
"v_smfmac_f32_16x16x64_bf16 v[106:109], v[82:85], v[128:135], v63\n"
"ds_read_b64_tr_b16 v[244:245], v184 offset:136\n"
"v_mfma_f32_16x16x32_bf16 v[82:85], v[172:175], v[82:85], 0\n"
"ds_read_b64_tr_b16 v[246:247], v185 offset:136\n"
"v_cvt_pk_bf16_f32 v158, v212, v213\n"
"v_mfma_f32_16x16x32_bf16 v[232:235], v[8:11], v[64:67], v[232:235]\n"
"ds_read_b64_tr_b16 v[248:249], v184 offset:904\n"
"v_cvt_pk_bf16_f32 v159, v214, v215\n"
"v_mfma_f32_16x16x32_bf16 v[208:211], a[4:7], v[32:35], 0\n"
"ds_read_b64_tr_b16 v[250:251], v185 offset:904\n"
"v_exp_f32 v113, v113\n"
"v_mfma_f32_16x16x32_bf16 v[208:211], a[16:19], v[36:39], v[208:211]\n"
"ds_read_b128 v[128:131], v181 offset:13504\n"
"v_exp_f32 v114, v114\n"
"v_mfma_f32_16x16x32_bf16 v[208:211], a[28:31], v[40:43], v[208:211]\n"
"s_waitcnt lgkmcnt(7)\n"
"ds_read_b128 v[132:135], v181 offset:27008\n"
"v_cvt_pk_bf16_f32 v90, v112, v114\n"
"v_mfma_f32_16x16x32_bf16 v[208:211], a[40:43], v[44:47], v[208:211]\n"
"s_add_u32 m0, s34, 1024\n"
"v_exp_f32 v115, v115\n"
"v_mfma_f32_16x16x32_bf16 v[232:235], v[20:23], v[68:71], v[232:235]\n"
"buffer_load_dwordx4 v190, s[8:11], s0 offen lds\n"
"v_cvt_pk_bf16_f32 v91, v113, v115\n"
"v_mfma_f32_16x16x32_bf16 v[240:243], v[28:31], v[68:71], v[240:243]\n"
"s_add_u32 s0, s0, s33\n"
"ds_read_b128 v[112:115], v178\n"
"v_exp_f32 v116, v116\n"
"v_mfma_f32_16x16x32_bf16 v[236:239], v[24:27], v[68:71], v[236:239]\n"
"v_exp_f32 v117, v117\n"
"v_mfma_f32_16x16x32_bf16 v[204:207], a[12:15], v[36:39], v[204:207]\n"
"v_cvt_pk_bf16_f32 v232, v232, v233\n"
"v_cvt_pk_bf16_f32 v233, v234, v235\n"
"v_mfma_f32_16x16x32_bf16 v[204:207], a[24:27], v[40:43], v[204:207]\n"
"v_cvt_pk_bf16_f32 v240, v240, v241\n"
"v_cvt_pk_bf16_f32 v241, v242, v243\n"
"v_mfma_f32_16x16x32_bf16 v[204:207], a[36:39], v[44:47], v[204:207]\n"
"ds_write_b64 v197, v[240:241] offset:49152\n"
"v_mfma_f32_16x16x32_bf16 v[204:207], a[48:51], v[48:51], v[204:207]\n"
"v_cvt_pk_bf16_f32 v234, v236, v237\n"
"v_cvt_pk_bf16_f32 v235, v238, v239\n"
"v_mfma_f32_16x16x32_bf16 v[204:207], a[60:63], v[52:55], v[204:207]\n"
"s_add_u32 m0, s34, 4096\n"
"ds_write_b128 v196, v[232:235] offset:49152\n"
"v_mfma_f32_16x16x32_bf16 v[208:211], a[52:55], v[48:51], v[208:211]\n"
"buffer_load_dwordx4 v199, s[8:11], s76 offen lds\n"
"v_exp_f32 v118, v118\n"
"v_mfma_f32_16x16x32_bf16 v[208:211], a[64:67], v[52:55], v[208:211]\n"
"v_exp_f32 v119, v119\n"
"v_mfma_f32_16x16x32_bf16 v[212:215], a[8:11], v[32:35], 0\n"
"v_cvt_pk_bf16_f32 v92, v116, v118\n"
"v_cvt_pk_bf16_f32 v93, v117, v119\n"
"v_mfma_f32_16x16x32_bf16 v[212:215], a[20:23], v[36:39], v[212:215]\n"
"s_waitcnt lgkmcnt(5)\n"
"v_cvt_pk_bf16_f32 v204, v204, v205\n"
"v_cvt_pk_bf16_f32 v205, v206, v207\n"
"v_mfma_f32_16x16x32_bf16 v[212:215], a[32:35], v[40:43], v[212:215]\n"
"v_cvt_pk_bf16_f32 v206, v208, v209\n"
"v_cvt_pk_bf16_f32 v207, v210, v211\n"
"v_mfma_f32_16x16x32_bf16 v[212:215], a[44:47], v[44:47], v[212:215]\n"
"v_mfma_f32_16x16x32_bf16 v[212:215], a[56:59], v[48:51], v[212:215]\n"
"v_mfma_f32_16x16x32_bf16 v[212:215], a[68:71], v[52:55], v[212:215]\n"
"s_add_u32 m0, s34, 2048\n"
"s_nop 0\n"
"buffer_load_dwordx4 v190, s[8:11], s1 offen lds\n"
"v_smfmac_f32_16x16x64_bf16 v[252:255], v[90:93], v[224:231], v63\n"
"s_add_u32 s1, s1, s33\n"
"v_smfmac_f32_16x16x64_bf16 v[98:101], v[90:93], v[244:251], v63\n"
"v_smfmac_f32_16x16x64_bf16 v[106:109], v[90:93], v[120:127], v63\n"
"buffer_load_dword v200, off, s[28:31], s39 sc1\n"
"v_mfma_f32_16x16x32_bf16 v[90:93], v[172:175], v[90:93], v[82:85]\n"
"ds_read_b128 v[120:123], v179\n"
"v_cvt_pk_bf16_f32 v212, v212, v213\n"
"v_mfma_f32_16x16x32_bf16 v[208:211], a[80:83], v[32:35], 0\n"
"ds_read_b64 v[82:83], v180 offset:13568\n"
"ds_read_b64 v[84:85], v181 offset:13568\n"
"v_mfma_f32_16x16x32_bf16 v[208:211], a[92:95], v[36:39], v[208:211]\n"
"v_cvt_pk_bf16_f32 v252, v252, v253\n"
"v_cvt_pk_bf16_f32 v253, v254, v255\n"
"v_mfma_f32_16x16x32_bf16 v[208:211], a[104:107], v[40:43], v[208:211]\n"
"v_rcp_f32 v171, v90\n"
"v_mfma_f32_16x16x32_bf16 v[208:211], a[116:119], v[44:47], v[208:211]\n"
"s_add_u32 m0, s34, 5120\n"
"ds_read_b64 v[90:91], v180 offset:27072\n"
"buffer_load_dwordx4 v199, s[8:11], s77 offen lds\n"
"v_mfma_f32_16x16x32_bf16 v[208:211], a[128:131], v[48:51], v[208:211]\n"
"s_mul_i32 s8, s33, 0\n"
"v_cvt_pk_bf16_f32 v171, v171, v171\n"
"v_and_b32 v92, v171, v176\n"
"v_mfma_f32_16x16x32_bf16 v[208:211], a[140:143], v[52:55], v[208:211]\n"
"s_mul_i32 s9, s33, 1\n"
"v_and_b32 v93, v171, v177\n"
"v_cvt_pk_bf16_f32 v98, v98, v99\n"
"v_cvt_pk_bf16_f32 v99, v100, v101\n"
"v_mfma_f32_16x16x16_bf16 v[252:255], v[252:253], v[92:93], 0\n"
"s_mul_i32 s10, s33, 2\n"
"v_cvt_pk_bf16_f32 v106, v106, v107\n"
"v_cvt_pk_bf16_f32 v107, v108, v109\n"
"v_mfma_f32_16x16x16_bf16 v[98:101], v[98:99], v[92:93], 0\n"
"s_waitcnt lgkmcnt(2)\n"
"v_cvt_pk_bf16_f32 v213, v214, v215\n"
"v_cvt_pk_bf16_f32 v214, v208, v209\n"
"v_mfma_f32_16x16x16_bf16 v[106:109], v[106:107], v[92:93], 0\n"
"s_waitcnt lgkmcnt(1)\n"
"ds_read_b64 v[92:93], v181 offset:27072\n"
"v_cvt_pk_bf16_f32 v252, v252, v253\n"
"v_mfma_f32_16x16x32_bf16 v[136:139], v[136:139], v[148:151], 0\n"
"s_mul_i32 s11, s33, 4\n"
"v_cvt_pk_bf16_f32 v253, v254, v255\n"
"v_cvt_pk_bf16_f32 v254, v98, v99\n"
"v_mfma_f32_16x16x32_bf16 v[136:139], v[80:83], v[160:163], v[136:139]\n"
"s_waitcnt vmcnt(13) lgkmcnt(1)\n"
"ds_read_b64 v[98:99], v178 offset:64\n"
"v_cvt_pk_bf16_f32 v255, v100, v101\n"
"v_mfma_f32_16x16x32_bf16 v[128:131], v[128:131], v[148:151], 0\n"
"buffer_store_dwordx4 v[252:255], v192, s[28:31], s37 offen sc1\n"
"v_mfma_f32_16x16x32_bf16 v[128:131], v[84:87], v[162:165], v[128:131]\n"
"ds_write_b128 v202, v[56:59] offset:30720\n"
"v_mfma_f32_16x16x32_bf16 v[140:143], v[140:143], v[148:151], 0\n"
"buffer_load_dwordx4 v[56:59], v192, s[28:31], s38 offen sc1\n"
"v_cvt_pk_bf16_f32 v106, v106, v107\n"
"v_mfma_f32_16x16x32_bf16 v[140:143], v[88:91], v[160:163], v[140:143]\n"
"s_waitcnt lgkmcnt(2)\n"
"v_cvt_pk_bf16_f32 v107, v108, v109\n"
"v_cvt_pk_bf16_f32 v215, v210, v211\n"
"v_mfma_f32_16x16x32_bf16 v[208:211], a[76:79], v[32:35], 0\n"
"s_waitcnt vmcnt(13)\n"
"ds_write_b64 v203, v[60:61] offset:30720\n"
"v_mfma_f32_16x16x32_bf16 v[208:211], a[88:91], v[36:39], v[208:211]\n"
"s_waitcnt lgkmcnt(2)\n"
"v_max_f32 v136, v136, v128\n"
"v_max_f32 v137, v137, v129\n"
"v_mfma_f32_16x16x32_bf16 v[208:211], a[100:103], v[40:43], v[208:211]\n"
"buffer_load_dwordx2 v[60:61], v193, s[28:31], s38 offen offset:1024 sc1\n"
"ds_read_b64 v[100:101], v179 offset:64\n"
"v_mfma_f32_16x16x32_bf16 v[208:211], a[112:115], v[44:47], v[208:211]\n"
"s_add_u32 s38, s38, 1572864\n"
"ds_read_b64_tr_b16 v[128:129], v182 offset:54088\n"
"v_max_f32 v138, v138, v130\n"
"v_mfma_f32_16x16x32_bf16 v[208:211], a[124:127], v[48:51], v[208:211]\n"
"v_max_f32 v139, v139, v131\n"
"v_max3_f32 v170, v136, v137, v138\n"
"v_mfma_f32_16x16x32_bf16 v[208:211], a[136:139], v[52:55], v[208:211]\n"
"ds_read_b64_tr_b16 v[130:131], v183 offset:54088\n"
"buffer_store_dwordx2 v[106:107], v193, s[28:31], s37 offen offset:1024 sc1\n"
"v_mfma_f32_16x16x32_bf16 v[132:135], v[132:135], v[148:151], 0\n"
"s_waitcnt lgkmcnt(3)\n"
"v_mov_b64 v[252:253], 0\n"
"v_mov_b64 v[254:255], 0\n"
"v_mfma_f32_16x16x32_bf16 v[132:135], v[92:95], v[162:165], v[132:135]\n"
"s_barrier\n"
"v_mfma_f32_16x16x32_bf16 v[112:115], v[112:115], v[148:151], 0\n"
"s_add_u32 s37, s37, 1572864\n"
"ds_write_b128 v186, v[204:207] offset:33760\n"
"v_mfma_f32_16x16x32_bf16 v[112:115], v[96:99], v[160:163], v[112:115]\n"
"s_waitcnt lgkmcnt(3)\n"
"v_mov_b64 v[98:99], 0\n"
"v_mfma_f32_16x16x32_bf16 v[204:207], a[72:75], v[32:35], 0\n"
"v_max_f32 v140, v140, v132\n"
"v_max_f32 v141, v141, v133\n"
"v_mfma_f32_16x16x32_bf16 v[204:207], a[84:87], v[36:39], v[204:207]\n"
"ds_read_b128 v[116:119], v178 offset:13504\n"
"ds_read_b64 v[106:107], v178 offset:13568\n"
"v_mfma_f32_16x16x32_bf16 v[204:207], a[96:99], v[40:43], v[204:207]\n"
"ds_read_b128 v[124:127], v179 offset:13504\n"
"v_max_f32 v142, v142, v134\n"
"v_mfma_f32_16x16x32_bf16 v[204:207], a[108:111], v[44:47], v[204:207]\n"
"ds_read_b64 v[108:109], v179 offset:13568\n"
"v_max_f32 v143, v143, v135\n"
"v_mfma_f32_16x16x32_bf16 v[204:207], a[120:123], v[48:51], v[204:207]\n"
"ds_read_b128 v[64:67], v202 offset:49152\n"
"v_max3_f32 v170, v170, v139, v140\n"
"v_mfma_f32_16x16x32_bf16 v[204:207], a[132:135], v[52:55], v[204:207]\n"
"ds_read_b64 v[68:69], v62 offset:49152\n"
"ds_write_b128 v186, v[212:215] offset:33824\n"
"v_mfma_f32_16x16x32_bf16 v[120:123], v[120:123], v[148:151], 0\n"
"v_max3_f32 v170, v170, v141, v142\n"
"v_mfma_f32_16x16x32_bf16 v[120:123], v[100:103], v[162:165], v[120:123]\n"
"v_mov_b64 v[100:101], 0\n"
"v_mfma_f32_16x16x32_bf16 v[212:215], a[144:147], v[32:35], 0\n"
"s_waitcnt lgkmcnt(5)\n"
"ds_read_b64_tr_b16 v[132:133], v182 offset:54856\n"
"ds_read_b64_tr_b16 v[134:135], v183 offset:54856\n"
"v_mfma_f32_16x16x32_bf16 v[116:119], v[116:119], v[148:151], 0\n"
"v_cvt_pk_bf16_f32 v204, v204, v205\n"
"v_cvt_pk_bf16_f32 v205, v206, v207\n"
"v_mfma_f32_16x16x32_bf16 v[116:119], v[104:107], v[160:163], v[116:119]\n"
"s_waitcnt lgkmcnt(5)\n"
"v_cvt_pk_bf16_f32 v206, v208, v209\n"
"v_cvt_pk_bf16_f32 v207, v210, v211\n"
"v_mfma_f32_16x16x32_bf16 v[208:211], a[148:151], v[32:35], 0\n"
"s_waitcnt lgkmcnt(4)\n"
"ds_write_b128 v186, v[204:207] offset:33888\n"
"v_mfma_f32_16x16x32_bf16 v[208:211], a[160:163], v[36:39], v[208:211]\n"
"s_waitcnt lgkmcnt(4)\n"
"buffer_store_dwordx4 v[64:67], v194, s[24:27], s36 offen sc1\n"
"v_mfma_f32_16x16x32_bf16 v[208:211], a[172:175], v[40:43], v[208:211]\n"
"s_nop 0\n"
"ds_read_b128 v[64:67], v192 offset:30720\n"
"v_max_f32 v112, v112, v120\n"
"v_mfma_f32_16x16x32_bf16 v[208:211], a[184:187], v[44:47], v[208:211]\n"
"v_max_f32 v113, v113, v121\n"
"v_max_f32 v114, v114, v122\n"
"v_mfma_f32_16x16x32_bf16 v[208:211], a[196:199], v[48:51], v[208:211]\n"
"buffer_store_dwordx2 v[68:69], v195, s[24:27], s36 offen sc1\n"
"v_mfma_f32_16x16x32_bf16 v[204:207], a[152:155], v[32:35], 0\n"
"s_add_u32 s36, s36, s33\n"
"ds_read_b128 v[68:71], v192 offset:31744\n"
"ds_read_b128 v[32:35], v188 offset:6144\n"
"v_mfma_f32_16x16x32_bf16 v[204:207], a[164:167], v[36:39], v[204:207]\n"
"ds_read_b64_tr_b16 v[120:121], v182 offset:72\n"
"v_max_f32 v115, v115, v123\n"
"v_mfma_f32_16x16x32_bf16 v[204:207], a[176:179], v[40:43], v[204:207]\n"
"v_max3_f32 v170, v170, v143, v112\n"
"v_max3_f32 v170, v170, v113, v114\n"
"v_mfma_f32_16x16x32_bf16 v[204:207], a[188:191], v[44:47], v[204:207]\n"
"s_mov_b64 exec, s[46:47]\n"
"v_add_u32 v183, 0xc0, v183\n"
"s_mov_b64 exec, -1\n"
"v_mfma_f32_16x16x32_bf16 v[204:207], a[200:203], v[48:51], v[204:207]\n"
"s_waitcnt lgkmcnt(3)\n"
"ds_read_b64_tr_b16 v[122:123], v183 offset:72\n"
"v_mov_b64 v[106:107], 0\n"
"v_mfma_f32_16x16x32_bf16 v[124:127], v[124:127], v[148:151], 0\n"
"v_mfma_f32_16x16x32_bf16 v[124:127], v[108:111], v[162:165], v[124:127]\n"
"v_mov_b64 v[108:109], 0\n"
"v_mfma_f32_16x16x32_bf16 v[232:235], a[216:219], v[64:67], 0\n"
"s_waitcnt lgkmcnt(3)\n"
"v_mfma_f32_16x16x32_bf16 v[232:235], a[228:231], v[68:71], v[232:235]\n"
"v_mfma_f32_16x16x32_bf16 v[236:239], a[220:223], v[64:67], 0\n"
"v_mfma_f32_16x16x32_bf16 v[236:239], a[232:235], v[68:71], v[236:239]\n"
"v_mfma_f32_16x16x32_bf16 v[240:243], a[224:227], v[64:67], 0\n"
"ds_read_b128 v[64:67], v192 offset:32768\n"
"v_max_f32 v116, v116, v124\n"
"v_mfma_f32_16x16x32_bf16 v[240:243], a[236:239], v[68:71], v[240:243]\n"
"ds_read_b128 v[68:71], v192 offset:33792\n"
"v_max_f32 v117, v117, v125\n"
"v_mfma_f32_16x16x32_bf16 v[212:215], a[156:159], v[36:39], v[212:215]\n"
"ds_read_b128 v[36:39], v189 offset:6144\n"
"ds_read_b64_tr_b16 v[124:125], v182 offset:840\n"
"v_mfma_f32_16x16x32_bf16 v[212:215], a[168:171], v[40:43], v[212:215]\n"
"ds_read_b128 v[40:43], v188 offset:7168\n"
"v_max_f32 v118, v118, v126\n"
"v_mfma_f32_16x16x32_bf16 v[212:215], a[180:183], v[44:47], v[212:215]\n"
"ds_read_b128 v[44:47], v189 offset:7168\n"
"v_max_f32 v119, v119, v127\n"
"v_mfma_f32_16x16x32_bf16 v[212:215], a[192:195], v[48:51], v[212:215]\n"
"ds_read_b128 v[48:51], v188 offset:8192\n"
"v_max3_f32 v170, v170, v115, v116\n"
"v_mfma_f32_16x16x32_bf16 v[212:215], a[204:207], v[52:55], v[212:215]\n"
"ds_read_b64_tr_b16 v[126:127], v183 offset:840\n"
"v_max3_f32 v170, v170, v117, v118\n"
"v_mfma_f32_16x16x32_bf16 v[208:211], a[208:211], v[52:55], v[208:211]\n"
"s_waitcnt lgkmcnt(7)\n"
"v_max_f32 v171, v170, v119\n"
"v_max_f32 v170, v170, v119\n"
"v_mfma_f32_16x16x32_bf16 v[232:235], a[240:243], v[64:67], v[232:235]\n"
"s_waitcnt lgkmcnt(6)\n"
"v_permlane16_swap_b32 v170, v171\n"
"v_mfma_f32_16x16x32_bf16 v[232:235], a[252:255], v[68:71], v[232:235]\n"
"v_max_f32 v170, v170, v171\n"
"v_mov_b32 v171, v170\n"
"v_mfma_f32_16x16x32_bf16 v[236:239], a[244:247], v[64:67], v[236:239]\n"
"s_nop 0\n"
"v_permlane32_swap_b32 v170, v171\n"
"v_max_f32 v170, v170, v171\n"
"v_mul_f32 v170, 0xbe553b95, v170\n"
"v_pk_fma_f32 v[136:137], v[136:137], s[70:71], v[170:171] op_sel_hi:[1,0,0]\n"
"v_pk_fma_f32 v[138:139], v[138:139], s[70:71], v[170:171] op_sel_hi:[1,0,0]\n"
"v_pk_fma_f32 v[140:141], v[140:141], s[70:71], v[170:171] op_sel_hi:[1,0,0]\n"
"v_pk_fma_f32 v[142:143], v[142:143], s[70:71], v[170:171] op_sel_hi:[1,0,0]\n"
"v_pk_fma_f32 v[112:113], v[112:113], s[70:71], v[170:171] op_sel_hi:[1,0,0]\n"
"v_pk_fma_f32 v[114:115], v[114:115], s[70:71], v[170:171] op_sel_hi:[1,0,0]\n"
"v_pk_fma_f32 v[116:117], v[116:117], s[70:71], v[170:171] op_sel_hi:[1,0,0]\n"
"v_pk_fma_f32 v[118:119], v[118:119], s[70:71], v[170:171] op_sel_hi:[1,0,0]\n"
"v_mfma_f32_16x16x32_bf16 v[236:239], v[0:3], v[68:71], v[236:239]\n"
"v_exp_f32 v136, v136\n"
"v_mfma_f32_16x16x32_bf16 v[240:243], a[248:251], v[64:67], v[240:243]\n"
"ds_read_b128 v[64:67], v192 offset:34816\n"
"v_cvt_pk_bf16_f32 v148, v212, v213\n"
"v_mfma_f32_16x16x32_bf16 v[240:243], v[4:7], v[68:71], v[240:243]\n"
"ds_read_b128 v[68:71], v192 offset:35840\n"
"v_cvt_pk_bf16_f32 v149, v214, v215\n"
"v_mfma_f32_16x16x32_bf16 v[204:207], a[212:215], v[52:55], v[204:207]\n"
"ds_read_b128 v[52:55], v189 offset:8192\n"
"v_exp_f32 v137, v137\n"
"v_mfma_f32_16x16x32_bf16 v[212:215], a[0:3], v[32:35], 0\n"
"s_waitcnt lgkmcnt(8)\n"
"v_exp_f32 v138, v138\n"
"v_mfma_f32_16x16x32_bf16 v[212:215], a[12:15], v[36:39], v[212:215]\n"
"s_waitcnt lgkmcnt(6)\n"
"v_exp_f32 v139, v139\n"
"v_mfma_f32_16x16x32_bf16 v[212:215], a[24:27], v[40:43], v[212:215]\n"
"s_waitcnt lgkmcnt(5)\n"
"v_cvt_pk_bf16_f32 v82, v136, v138\n"
"v_cvt_pk_bf16_f32 v83, v137, v139\n"
"v_mfma_f32_16x16x32_bf16 v[212:215], a[36:39], v[44:47], v[212:215]\n"
"s_waitcnt lgkmcnt(4)\n"
"ds_read_b128 v[136:139], v180 offset:27008\n"
"v_exp_f32 v140, v140\n"
"v_mfma_f32_16x16x32_bf16 v[212:215], a[48:51], v[48:51], v[212:215]\n"
"s_waitcnt lgkmcnt(3)\n"
"v_exp_f32 v141, v141\n"
"v_mfma_f32_16x16x32_bf16 v[232:235], v[8:11], v[64:67], v[232:235]\n"
"s_waitcnt lgkmcnt(2)\n"
"v_exp_f32 v142, v142\n"
"v_mfma_f32_16x16x32_bf16 v[232:235], v[20:23], v[68:71], v[232:235]\n"
"s_waitcnt lgkmcnt(1)\n"
"v_exp_f32 v143, v143\n"
"v_mfma_f32_16x16x32_bf16 v[240:243], v[16:19], v[64:67], v[240:243]\n"
"v_cvt_pk_bf16_f32 v84, v140, v142\n"
"v_cvt_pk_bf16_f32 v85, v141, v143\n"
"v_mfma_f32_16x16x32_bf16 v[240:243], v[28:31], v[68:71], v[240:243]\n"
"ds_read_b128 v[140:143], v178\n"
"v_cvt_pk_bf16_f32 v150, v208, v209\n"
"v_smfmac_f32_16x16x64_bf16 v[252:255], v[82:85], v[216:223], v63\n"
"ds_read_b64_tr_b16 v[216:217], v182 offset:128\n"
"v_smfmac_f32_16x16x64_bf16 v[98:101], v[82:85], v[72:79], v63\n"
"ds_read_b64_tr_b16 v[218:219], v183 offset:128\n"
"v_smfmac_f32_16x16x64_bf16 v[106:109], v[82:85], v[128:135], v63\n"
"ds_read_b64_tr_b16 v[220:221], v182 offset:896\n"
"v_mfma_f32_16x16x32_bf16 v[82:85], v[172:175], v[82:85], 0\n"
"ds_read_b64_tr_b16 v[222:223], v183 offset:896\n"
"ds_read_b64_tr_b16 v[72:73], v182 offset:136\n"
"v_mfma_f32_16x16x32_bf16 v[236:239], v[12:15], v[64:67], v[236:239]\n"
"ds_read_b64_tr_b16 v[74:75], v183 offset:136\n"
"v_cvt_pk_bf16_f32 v151, v210, v211\n"
"v_mfma_f32_16x16x32_bf16 v[236:239], v[24:27], v[68:71], v[236:239]\n"
"ds_read_b64_tr_b16 v[76:77], v182 offset:904\n"
"v_cvt_pk_bf16_f32 v232, v232, v233\n"
"v_mfma_f32_16x16x32_bf16 v[212:215], a[60:63], v[52:55], v[212:215]\n"
"ds_read_b64_tr_b16 v[78:79], v183 offset:904\n"
"v_cvt_pk_bf16_f32 v233, v234, v235\n"
"v_mfma_f32_16x16x32_bf16 v[208:211], a[4:7], v[32:35], 0\n"
"ds_read_b128 v[128:131], v181 offset:27008\n"
"v_cvt_pk_bf16_f32 v240, v240, v241\n"
"v_mfma_f32_16x16x32_bf16 v[208:211], a[16:19], v[36:39], v[208:211]\n"
"ds_read_b128 v[132:135], v179\n"
"v_cvt_pk_bf16_f32 v234, v236, v237\n"
"v_mfma_f32_16x16x32_bf16 v[208:211], a[28:31], v[40:43], v[208:211]\n"
"v_exp_f32 v112, v112\n"
"v_mfma_f32_16x16x32_bf16 v[208:211], a[40:43], v[44:47], v[208:211]\n"
"s_waitcnt vmcnt(7)\n"
"v_add3_u32 v200, v200, s50, -8\n"
"v_cmp_lt_u32 vcc, 64, v200\n"
"v_mfma_f32_16x16x32_bf16 v[208:211], a[52:55], v[48:51], v[208:211]\n"
"v_exp_f32 v113, v113\n"
"v_mfma_f32_16x16x32_bf16 v[208:211], a[64:67], v[52:55], v[208:211]\n"
"s_nop 1\n"
"s_cbranch_vccnz .Lp7%=\n"
".Lr7%=:\n"
"buffer_load_dwordx4 a[198:201], v192, s[28:31], s38 offen sc1\n"
"v_exp_f32 v114, v114\n"
"v_exp_f32 v115, v115\n"
"v_cvt_pk_bf16_f32 v90, v112, v114\n"
"v_cvt_pk_bf16_f32 v235, v238, v239\n"
"v_cvt_pk_bf16_f32 v241, v242, v243\n"
"v_cvt_pk_bf16_f32 v91, v113, v115\n"
"v_exp_f32 v116, v116\n"
"v_exp_f32 v117, v117\n"
"buffer_load_dwordx2 a[202:203], v193, s[28:31], s38 offen offset:1024 sc1\n"
"ds_write_b128 v196, v[232:235] offset:36864\n"
"v_exp_f32 v118, v118\n"
"v_exp_f32 v119, v119\n"
"v_cvt_pk_bf16_f32 v92, v116, v118\n"
"v_cvt_pk_bf16_f32 v93, v117, v119\n"
"v_cvt_pk_bf16_f32 v162, v204, v205\n"
"v_cvt_pk_bf16_f32 v163, v206, v207\n"
"v_smfmac_f32_16x16x64_bf16 v[106:109], v[90:93], v[120:127], v63\n"
"s_waitcnt lgkmcnt(3)\n"
"ds_write_b64 v197, v[240:241] offset:36864\n"
"v_smfmac_f32_16x16x64_bf16 v[252:255], v[90:93], v[216:223], v63\n"
"s_add_u32 s38, s38, 1572864\n"
"buffer_load_dwordx4 a[204:207], v192, s[28:31], s38 offen sc1\n"
"v_smfmac_f32_16x16x64_bf16 v[98:101], v[90:93], v[72:79], v63\n"
"ds_read_b128 v[112:115], v178 offset:13504\n"
"v_mfma_f32_16x16x32_bf16 v[90:93], v[172:175], v[90:93], v[82:85]\n"
"ds_read_b128 v[120:123], v179 offset:13504\n"
"v_cvt_pk_bf16_f32 v106, v106, v107\n"
"v_mfma_f32_16x16x32_bf16 v[204:207], a[8:11], v[32:35], 0\n"
"ds_read_b64 v[82:83], v180 offset:27072\n"
"buffer_load_dwordx2 a[208:209], v193, s[28:31], s38 offen offset:1024 sc1\n"
"v_mfma_f32_16x16x32_bf16 v[204:207], a[20:23], v[36:39], v[204:207]\n"
"s_add_u32 s38, s38, 1572864\n"
"ds_read_b64 v[84:85], v181 offset:27072\n"
"v_cvt_pk_bf16_f32 v252, v252, v253\n"
"v_mfma_f32_16x16x32_bf16 v[204:207], a[32:35], v[40:43], v[204:207]\n"
"s_add_u32 s12, s38, 0\n"
"v_rcp_f32 v171, v90\n"
"v_mfma_f32_16x16x32_bf16 v[204:207], a[44:47], v[44:47], v[204:207]\n"
"s_add_u32 s13, s38, 1572864\n"
"ds_read_b64 v[90:91], v178 offset:64\n"
"v_cvt_pk_bf16_f32 v171, v171, v171\n"
"v_mfma_f32_16x16x32_bf16 v[204:207], a[56:59], v[48:51], v[204:207]\n"
"s_add_u32 s14, s38, 3145728\n"
"v_and_b32 v92, v171, v176\n"
"v_and_b32 v93, v171, v177\n"
"v_mfma_f32_16x16x32_bf16 v[204:207], a[68:71], v[52:55], v[204:207]\n"
"s_add_u32 s16, s38, 4718592\n"
"buffer_load_dwordx4 a[144:147], v192, s[28:31], s12 offen sc1\n"
"v_cvt_pk_bf16_f32 v253, v254, v255\n"
"v_cvt_pk_bf16_f32 v98, v98, v99\n"
"v_cvt_pk_bf16_f32 v107, v108, v109\n"
"v_mfma_f32_16x16x16_bf16 v[252:255], v[252:253], v[92:93], 0\n"
"s_add_u32 s15, s38, 6291456\n"
"v_cvt_pk_bf16_f32 v99, v100, v101\n"
"v_cvt_pk_bf16_f32 v212, v212, v213\n"
"v_mfma_f32_16x16x16_bf16 v[106:109], v[106:107], v[92:93], 0\n"
"s_waitcnt lgkmcnt(2)\n"
"v_cvt_pk_bf16_f32 v213, v214, v215\n"
"v_cvt_pk_bf16_f32 v214, v208, v209\n"
"v_mfma_f32_16x16x16_bf16 v[98:101], v[98:99], v[92:93], 0\n"
"s_waitcnt lgkmcnt(1)\n"
"buffer_load_dwordx2 a[148:149], v193, s[28:31], s12 offen offset:1024 sc1\n"
"ds_read_b64 v[92:93], v179 offset:64\n"
"v_mfma_f32_16x16x32_bf16 v[136:139], v[136:139], v[152:155], 0\n"
"s_add_u32 s17, s38, 7864320\n"
"v_cvt_pk_bf16_f32 v252, v252, v253\n"
"v_cvt_pk_bf16_f32 v253, v254, v255\n"
"v_mfma_f32_16x16x32_bf16 v[136:139], v[80:83], v[164:167], v[136:139]\n"
"s_waitcnt lgkmcnt(1)\n"
"v_cvt_pk_bf16_f32 v106, v106, v107\n"
"v_cvt_pk_bf16_f32 v254, v98, v99\n"
"v_mfma_f32_16x16x32_bf16 v[128:131], v[128:131], v[152:155], 0\n"
"s_add_u32 s18, s38, 9437184\n"
"ds_read_b64 v[98:99], v178 offset:13568\n"
"v_cvt_pk_bf16_f32 v255, v100, v101\n"
"v_mfma_f32_16x16x32_bf16 v[128:131], v[84:87], v[166:169], v[128:131]\n"
"buffer_store_dwordx4 v[252:255], v192, s[28:31], s37 offen sc1\n"
"v_mfma_f32_16x16x32_bf16 v[140:143], v[140:143], v[152:155], 0\n"
"ds_read_b64 v[100:101], v179 offset:13568\n"
"v_cvt_pk_bf16_f32 v107, v108, v109\n"
"v_mfma_f32_16x16x32_bf16 v[140:143], v[88:91], v[164:167], v[140:143]\n"
"v_cvt_pk_bf16_f32 v215, v210, v211\n"
"v_cvt_pk_bf16_f32 v204, v204, v205\n"
"v_mfma_f32_16x16x32_bf16 v[208:211], a[80:83], v[32:35], 0\n"
"s_waitcnt lgkmcnt(2)\n"
"buffer_store_dwordx2 v[106:107], v193, s[28:31], s37 offen offset:1024 sc1\n"
"v_mfma_f32_16x16x32_bf16 v[132:135], v[132:135], v[152:155], 0\n"
"s_waitcnt vmcnt(12)\n"
"ds_write_b128 v202, v[56:59] offset:55296\n"
"v_mfma_f32_16x16x32_bf16 v[132:135], v[92:95], v[166:169], v[132:135]\n"
"s_waitcnt lgkmcnt(2)\n"
"v_max_f32 v136, v136, v128\n"
"v_max_f32 v137, v137, v129\n"
"v_mfma_f32_16x16x32_bf16 v[112:115], v[112:115], v[152:155], 0\n"
"s_add_u32 s37, s37, 1572864\n"
"v_max_f32 v138, v138, v130\n"
"v_max_f32 v139, v139, v131\n"
"v_mfma_f32_16x16x32_bf16 v[112:115], v[96:99], v[164:167], v[112:115]\n"
"s_waitcnt vmcnt(10) lgkmcnt(1)\n"
"ds_read_b64_tr_b16 v[128:129], v184 offset:72\n"
"v_or_b32 v201, 10, s6\n"
"v_mfma_f32_16x16x32_bf16 v[120:123], v[120:123], v[152:155], 0\n"
"s_mov_b64 exec, 1\n"
"buffer_store_dword v201, off, s[28:31], s40 sc1\n"
"s_mov_b64 exec, -1\n"
"v_mfma_f32_16x16x32_bf16 v[120:123], v[100:103], v[166:169], v[120:123]\n"
"ds_write_b64 v203, v[60:61] offset:55296\n"
"v_mfma_f32_16x16x32_bf16 v[208:211], a[92:95], v[36:39], v[208:211]\n"
"v_max3_f32 v170, v136, v137, v138\n"
"v_max_f32 v140, v140, v132\n"
"v_mfma_f32_16x16x32_bf16 v[208:211], a[104:107], v[40:43], v[208:211]\n"
"ds_read_b64_tr_b16 v[130:131], v185 offset:72\n"
"v_max_f32 v141, v141, v133\n"
"v_mfma_f32_16x16x32_bf16 v[208:211], a[116:119], v[44:47], v[208:211]\n"
"ds_read_b64_tr_b16 v[132:133], v184 offset:840\n"
"v_max_f32 v142, v142, v134\n"
"v_mfma_f32_16x16x32_bf16 v[208:211], a[128:131], v[48:51], v[208:211]\n"
"v_max_f32 v143, v143, v135\n"
"v_max3_f32 v170, v170, v139, v140\n"
"v_mfma_f32_16x16x32_bf16 v[208:211], a[140:143], v[52:55], v[208:211]\n"
"v_max3_f32 v170, v170, v141, v142\n"
"v_cvt_pk_bf16_f32 v205, v206, v207\n"
"v_max_f32 v112, v112, v120\n"
"v_max_f32 v113, v113, v121\n"
"v_max_f32 v114, v114, v122\n"
"v_max_f32 v115, v115, v123\n"
"v_max3_f32 v170, v170, v143, v112\n"
"v_max3_f32 v170, v170, v113, v114\n"
"s_waitcnt lgkmcnt(2)\n"
"s_barrier\n"
"ds_read_b128 v[116:119], v178 offset:27008\n"
"ds_read_b64 v[106:107], v178 offset:27072\n"
"ds_read_b128 v[124:127], v179 offset:27008\n"
"v_mov_b64 v[252:253], 0\n"
"v_mov_b64 v[254:255], 0\n"
"v_mov_b64 v[98:99], 0\n"
"ds_read_b64 v[108:109], v179 offset:27072\n"
"v_mov_b64 v[100:101], 0\n"
"ds_write_b128 v186, v[212:215] offset:47264\n"
"v_mfma_f32_16x16x32_bf16 v[212:215], a[72:75], v[32:35], 0\n"
"v_cvt_pk_bf16_f32 v206, v208, v209\n"
"v_cvt_pk_bf16_f32 v207, v210, v211\n"
"v_mfma_f32_16x16x32_bf16 v[212:215], a[84:87], v[36:39], v[212:215]\n"
"s_mov_b64 exec, s[44:45]\n"
"v_add_u32 v181, 0xc0, v181\n"
"s_mov_b64 exec, -1\n"
"v_mfma_f32_16x16x32_bf16 v[212:215], a[96:99], v[40:43], v[212:215]\n"
"ds_read_b128 v[64:67], v202 offset:36864\n"
"ds_read_b64 v[68:69], v62 offset:36864\n"
"v_mfma_f32_16x16x32_bf16 v[212:215], a[108:111], v[44:47], v[212:215]\n"
"s_waitcnt lgkmcnt(5)\n"
"ds_read_b64_tr_b16 v[134:135], v185 offset:840\n"
"ds_read_b64_tr_b16 a[168:169], v182 offset:13576\n"
"v_mfma_f32_16x16x32_bf16 v[116:119], v[116:119], v[152:155], 0\n"
"ds_read_b64_tr_b16 a[170:171], v183 offset:13576\n"
"v_mfma_f32_16x16x32_bf16 v[116:119], v[104:107], v[164:167], v[116:119]\n"
"s_waitcnt lgkmcnt(6)\n"
"ds_read_b64_tr_b16 a[172:173], v182 offset:14344\n"
"v_mov_b64 v[106:107], 0\n"
"v_mfma_f32_16x16x32_bf16 v[124:127], v[124:127], v[152:155], 0\n"
"ds_read_b64_tr_b16 a[174:175], v183 offset:14344\n"
"v_mfma_f32_16x16x32_bf16 v[124:127], v[108:111], v[166:169], v[124:127]\n"
"ds_write_b128 v186, v[204:207] offset:47328\n"
"v_mfma_f32_16x16x32_bf16 v[212:215], a[120:123], v[48:51], v[212:215]\n"
"s_waitcnt lgkmcnt(7)\n"
"v_mov_b64 v[108:109], 0\n"
"v_mfma_f32_16x16x32_bf16 v[212:215], a[132:135], v[52:55], v[212:215]\n"
"s_waitcnt lgkmcnt(6)\n"
"buffer_store_dwordx4 v[64:67], v194, s[24:27], s36 offen sc1\n"
"v_mfma_f32_16x16x32_bf16 v[208:211], a[76:79], v[32:35], 0\n"
"s_nop 0\n"
"ds_read_b128 v[64:67], v192 offset:55296\n"
"ds_read_b128 v[32:35], v188\n"
"v_mfma_f32_16x16x32_bf16 v[208:211], a[88:91], v[36:39], v[208:211]\n"
"ds_read_b128 v[36:39], v189\n"
"v_max_f32 v116, v116, v124\n"
"v_mfma_f32_16x16x32_bf16 v[208:211], a[100:103], v[40:43], v[208:211]\n"
"ds_read_b128 v[40:43], v188 offset:1024\n"
"buffer_store_dwordx2 v[68:69], v195, s[24:27], s36 offen sc1\n"
"v_mfma_f32_16x16x32_bf16 v[208:211], a[112:115], v[44:47], v[208:211]\n"
"s_add_u32 s36, s36, s33\n"
"ds_read_b128 v[68:71], v192 offset:56320\n"
"ds_read_b128 v[44:47], v189 offset:1024\n"
"v_mfma_f32_16x16x32_bf16 v[208:211], a[124:127], v[48:51], v[208:211]\n"
"ds_read_b128 v[48:51], v188 offset:2048\n"
"v_max_f32 v117, v117, v125\n"
"v_mfma_f32_16x16x32_bf16 v[208:211], a[136:139], v[52:55], v[208:211]\n"
"ds_read_b128 v[52:55], v189 offset:2048\n"
"v_max_f32 v118, v118, v126\n"
"v_max_f32 v119, v119, v127\n"
"v_max3_f32 v170, v170, v115, v116\n"
"v_max3_f32 v170, v170, v117, v118\n"
"v_max_f32 v171, v170, v119\n"
"v_max_f32 v170, v170, v119\n"
"v_cvt_pk_bf16_f32 v212, v212, v213\n"
"v_cvt_pk_bf16_f32 v213, v214, v215\n"
"v_permlane16_swap_b32 v170, v171\n"
"v_max_f32 v170, v170, v171\n"
"v_mov_b32 v171, v170\n"
"v_cvt_pk_bf16_f32 v214, v208, v209\n"
"v_cvt_pk_bf16_f32 v215, v210, v211\n"
"v_permlane32_swap_b32 v170, v171\n"
"v_max_f32 v170, v170, v171\n"
"v_mul_f32 v170, 0xbe553b95, v170\n"
".Lic6%=:\n"
"s_cbranch_execz .Lib1%=\n"
"v_pk_fma_f32 v[136:137], v[136:137], s[70:71], v[170:171] op_sel_hi:[1,0,0]\n"
"v_pk_fma_f32 v[138:139], v[138:139], s[70:71], v[170:171] op_sel_hi:[1,0,0]\n"
"v_pk_fma_f32 v[140:141], v[140:141], s[70:71], v[170:171] op_sel_hi:[1,0,0]\n"
"v_pk_fma_f32 v[142:143], v[142:143], s[70:71], v[170:171] op_sel_hi:[1,0,0]\n"
"v_pk_fma_f32 v[112:113], v[112:113], s[70:71], v[170:171] op_sel_hi:[1,0,0]\n"
"v_pk_fma_f32 v[114:115], v[114:115], s[70:71], v[170:171] op_sel_hi:[1,0,0]\n"
"v_pk_fma_f32 v[116:117], v[116:117], s[70:71], v[170:171] op_sel_hi:[1,0,0]\n"
"v_pk_fma_f32 v[118:119], v[118:119], s[70:71], v[170:171] op_sel_hi:[1,0,0]\n"
"v_exp_f32 v136, v136\n"
"v_exp_f32 v137, v137\n"
"v_exp_f32 v138, v138\n"
"v_exp_f32 v139, v139\n"
"v_cvt_pk_bf16_f32 v82, v136, v138\n"
"v_cvt_pk_bf16_f32 v83, v137, v139\n"
"v_exp_f32 v140, v140\n"
"v_exp_f32 v141, v141\n"
"v_exp_f32 v142, v142\n"
"v_exp_f32 v143, v143\n"
"v_cvt_pk_bf16_f32 v84, v140, v142\n"
"v_cvt_pk_bf16_f32 v85, v141, v143\n"
"v_exp_f32 v112, v112\n"
"s_nop 0\n"
"v_smfmac_f32_16x16x64_bf16 v[252:255], v[82:85], v[224:231], v63\n"
"s_waitcnt lgkmcnt(6)\n"
"ds_read_b64_tr_b16 v[224:225], v182 offset:13632\n"
"v_mfma_f32_16x16x32_bf16 v[232:235], a[216:219], v[64:67], 0\n"
"s_waitcnt lgkmcnt(4)\n"
"ds_read_b64_tr_b16 v[226:227], v183 offset:13632\n"
"ds_read_b64_tr_b16 v[228:229], v182 offset:14400\n"
"v_mfma_f32_16x16x32_bf16 v[232:235], a[228:231], v[68:71], v[232:235]\n"
"ds_read_b64_tr_b16 v[230:231], v183 offset:14400\n"
"v_exp_f32 v113, v113\n"
"v_mfma_f32_16x16x32_bf16 v[236:239], a[220:223], v[64:67], 0\n"
"ds_write_b128 v186, v[212:215] offset:47392\n"
"v_mfma_f32_16x16x32_bf16 v[236:239], a[232:235], v[68:71], v[236:239]\n"
"v_exp_f32 v114, v114\n"
"v_mfma_f32_16x16x32_bf16 v[240:243], a[224:227], v[64:67], 0\n"
"v_exp_f32 v115, v115\n"
"v_mfma_f32_16x16x32_bf16 v[240:243], a[236:239], v[68:71], v[240:243]\n"
"ds_read_b128 v[64:67], v192 offset:57344\n"
"ds_read_b128 v[68:71], v192 offset:58368\n"
"v_smfmac_f32_16x16x64_bf16 v[98:101], v[82:85], v[244:251], v63\n"
"ds_read_b64_tr_b16 v[244:245], v182 offset:13640\n"
"v_smfmac_f32_16x16x64_bf16 v[106:109], v[82:85], v[128:135], v63\n"
"ds_read_b64_tr_b16 v[246:247], v183 offset:13640\n"
"v_mfma_f32_16x16x32_bf16 v[82:85], v[172:175], v[82:85], 0\n"
"ds_read_b64_tr_b16 v[248:249], v182 offset:14408\n"
"v_cvt_pk_bf16_f32 v90, v112, v114\n"
"v_mfma_f32_16x16x32_bf16 v[204:207], a[0:3], v[32:35], 0\n"
"ds_read_b64_tr_b16 v[250:251], v183 offset:14408\n"
"v_cvt_pk_bf16_f32 v91, v113, v115\n"
"v_mfma_f32_16x16x32_bf16 v[204:207], a[12:15], v[36:39], v[204:207]\n"
"ds_read_b128 v[120:123], v178\n"
"v_exp_f32 v116, v116\n"
"v_mfma_f32_16x16x32_bf16 v[204:207], a[24:27], v[40:43], v[204:207]\n"
"s_waitcnt lgkmcnt(14)\n"
"ds_read_b128 v[136:139], v179\n"
"ds_read_b128 v[124:127], v178 offset:13504\n"
"v_mfma_f32_16x16x32_bf16 v[204:207], a[36:39], v[44:47], v[204:207]\n"
"s_waitcnt lgkmcnt(8)\n"
"v_exp_f32 v117, v117\n"
"v_mfma_f32_16x16x32_bf16 v[232:235], a[240:243], v[64:67], v[232:235]\n"
"s_waitcnt lgkmcnt(7)\n"
"ds_read_b128 v[140:143], v179 offset:13504\n"
"ds_read_b128 v[128:131], v178 offset:27008\n"
"v_mfma_f32_16x16x32_bf16 v[232:235], a[252:255], v[68:71], v[232:235]\n"
"v_exp_f32 v118, v118\n"
"v_mfma_f32_16x16x32_bf16 v[236:239], a[244:247], v[64:67], v[236:239]\n"
"ds_read_b128 v[112:115], v179 offset:27008\n"
"v_cvt_pk_bf16_f32 v92, v116, v118\n"
"v_mfma_f32_16x16x32_bf16 v[236:239], v[0:3], v[68:71], v[236:239]\n"
"v_exp_f32 v119, v119\n"
"v_mfma_f32_16x16x32_bf16 v[240:243], a[248:251], v[64:67], v[240:243]\n"
"ds_read_b128 v[64:67], v192 offset:59392\n"
"v_cvt_pk_bf16_f32 v93, v117, v119\n"
"v_mfma_f32_16x16x32_bf16 v[240:243], v[4:7], v[68:71], v[240:243]\n"
"s_waitcnt lgkmcnt(7)\n"
"ds_read_b128 v[68:71], v192 offset:60416\n"
"v_smfmac_f32_16x16x64_bf16 v[252:255], v[90:93], v[224:231], v63\n"
"v_smfmac_f32_16x16x64_bf16 v[98:101], v[90:93], v[244:251], v63\n"
"v_smfmac_f32_16x16x64_bf16 v[106:109], v[90:93], a[168:175], v63\n"
"v_mfma_f32_16x16x32_bf16 v[90:93], v[172:175], v[90:93], v[82:85]\n"
"v_mfma_f32_16x16x32_bf16 v[204:207], a[48:51], v[48:51], v[204:207]\n"
"s_nop 1\n"
"ds_read_b64 v[82:83], v178 offset:64\n"
"ds_read_b64 v[84:85], v179 offset:64\n"
"v_mfma_f32_16x16x32_bf16 v[204:207], a[60:63], v[52:55], v[204:207]\n"
"s_waitcnt lgkmcnt(3)\n"
"v_cvt_pk_bf16_f32 v252, v252, v253\n"
"v_cvt_pk_bf16_f32 v253, v254, v255\n"
"v_mfma_f32_16x16x32_bf16 v[232:235], v[8:11], v[64:67], v[232:235]\n"
"s_waitcnt lgkmcnt(2)\n"
"v_rcp_f32 v171, v90\n"
"v_mfma_f32_16x16x32_bf16 v[232:235], v[20:23], v[68:71], v[232:235]\n"
"ds_read_b64 v[90:91], v178 offset:13568\n"
"v_cvt_pk_bf16_f32 v171, v171, v171\n"
"v_mfma_f32_16x16x32_bf16 v[240:243], v[16:19], v[64:67], v[240:243]\n"
"v_and_b32 v92, v171, v176\n"
"v_and_b32 v93, v171, v177\n"
"v_mfma_f32_16x16x32_bf16 v[240:243], v[28:31], v[68:71], v[240:243]\n"
"v_cvt_pk_bf16_f32 v98, v98, v99\n"
"v_cvt_pk_bf16_f32 v99, v100, v101\n"
"v_mfma_f32_16x16x32_bf16 v[236:239], v[12:15], v[64:67], v[236:239]\n"
"v_cvt_pk_bf16_f32 v232, v232, v233\n"
"v_cvt_pk_bf16_f32 v233, v234, v235\n"
"v_mfma_f32_16x16x32_bf16 v[236:239], v[24:27], v[68:71], v[236:239]\n"
"s_waitcnt lgkmcnt(2)\n"
"v_cvt_pk_bf16_f32 v106, v106, v107\n"
"v_cvt_pk_bf16_f32 v240, v240, v241\n"
"v_mfma_f32_16x16x16_bf16 v[252:255], v[252:253], v[92:93], 0\n"
"s_waitcnt vmcnt(10) lgkmcnt(1)\n"
"v_cvt_pk_bf16_f32 v241, v242, v243\n"
"v_cvt_pk_bf16_f32 v107, v108, v109\n"
"v_mfma_f32_16x16x16_bf16 v[98:101], v[98:99], v[92:93], 0\n"
"ds_write_b128 v202, a[198:201] offset:30720\n"
"v_mfma_f32_16x16x16_bf16 v[106:109], v[106:107], v[92:93], 0\n"
"s_waitcnt lgkmcnt(1)\n"
"v_cvt_pk_bf16_f32 v234, v236, v237\n"
"v_cvt_pk_bf16_f32 v235, v238, v239\n"
"v_mfma_f32_16x16x32_bf16 v[120:123], v[120:123], v[144:147], 0\n"
"s_waitcnt vmcnt(9)\n"
"buffer_load_dword v200, off, s[28:31], s39 sc1\n"
"ds_write_b64 v203, a[202:203] offset:30720\n"
"v_mfma_f32_16x16x32_bf16 v[120:123], v[80:83], v[156:159], v[120:123]\n"
"v_cvt_pk_bf16_f32 v252, v252, v253\n"
"v_cvt_pk_bf16_f32 v253, v254, v255\n"
"v_mfma_f32_16x16x32_bf16 v[136:139], v[136:139], v[144:147], 0\n"
"ds_write_b128 v196, v[232:235] offset:43008\n"
"v_mfma_f32_16x16x32_bf16 v[136:139], v[84:87], v[158:161], v[136:139]\n"
"v_cvt_pk_bf16_f32 v254, v98, v99\n"
"v_cvt_pk_bf16_f32 v255, v100, v101\n"
"v_mfma_f32_16x16x32_bf16 v[208:211], a[4:7], v[32:35], 0\n"
"v_cvt_pk_bf16_f32 v106, v106, v107\n"
"v_cvt_pk_bf16_f32 v107, v108, v109\n"
"v_mfma_f32_16x16x32_bf16 v[208:211], a[16:19], v[36:39], v[208:211]\n"
"ds_write_b64 v197, v[240:241] offset:43008\n"
"v_mfma_f32_16x16x32_bf16 v[208:211], a[28:31], v[40:43], v[208:211]\n"
"v_min_u32 v252, -2, v252\n"
"v_min_u32 v253, -2, v253\n"
"v_mfma_f32_16x16x32_bf16 v[208:211], a[40:43], v[44:47], v[208:211]\n"
"ds_read_b64 v[92:93], v179 offset:13568\n"
"ds_read_b64 v[98:99], v178 offset:27072\n"
"v_mfma_f32_16x16x32_bf16 v[208:211], a[52:55], v[48:51], v[208:211]\n"
"ds_read_b64 v[100:101], v179 offset:27072\n"
"v_min_u32 v254, -2, v254\n"
"v_mfma_f32_16x16x32_bf16 v[208:211], a[64:67], v[52:55], v[208:211]\n"
"v_min_u32 v255, -2, v255\n"
"v_min_u32 v106, -2, v106\n"
"v_mfma_f32_16x16x32_bf16 v[124:127], v[124:127], v[144:147], 0\n"
"buffer_store_dwordx4 v[252:255], v192, s[28:31], s37 offen sc1\n"
"v_mfma_f32_16x16x32_bf16 v[124:127], v[88:91], v[156:159], v[124:127]\n"
"s_waitcnt lgkmcnt(3)\n"
"v_min_u32 v107, -2, v107\n"
"v_cvt_pk_bf16_f32 v204, v204, v205\n"
"v_mfma_f32_16x16x32_bf16 v[212:215], a[8:11], v[32:35], 0\n"
"s_barrier\n"
"v_mfma_f32_16x16x32_bf16 v[212:215], a[20:23], v[36:39], v[212:215]\n"
"s_waitcnt vmcnt(8) lgkmcnt(2)\n"
"buffer_store_dwordx2 v[106:107], v193, s[28:31], s37 offen offset:1024 sc1\n"
"v_mfma_f32_16x16x32_bf16 v[140:143], v[140:143], v[144:147], 0\n"
"s_waitcnt lgkmcnt(1)\n"
"ds_write_b128 v202, a[144:147]\n"
"v_mfma_f32_16x16x32_bf16 v[140:143], v[92:95], v[158:161], v[140:143]\n"
"s_waitcnt vmcnt(6) lgkmcnt(1)\n"
"v_or_b32 v201, 11, s6\n"
"v_cvt_pk_bf16_f32 v205, v206, v207\n"
"v_mfma_f32_16x16x32_bf16 v[128:131], v[128:131], v[144:147], 0\n"
"s_add_u32 s37, s37, 1572864\n"
"s_mov_b64 exec, 1\n"
"buffer_store_dword v201, off, s[28:31], s40 sc1\n"
"s_mov_b64 exec, -1\n"
"v_mfma_f32_16x16x32_bf16 v[128:131], v[96:99], v[156:159], v[128:131]\n"
"ds_write_b64 v203, a[148:149]\n"
"v_mfma_f32_16x16x32_bf16 v[112:115], v[112:115], v[144:147], 0\n"
"v_cvt_pk_bf16_f32 v206, v208, v209\n"
"v_cvt_pk_bf16_f32 v207, v210, v211\n"
"v_mfma_f32_16x16x32_bf16 v[112:115], v[100:103], v[158:161], v[112:115]\n"
"ds_read_b128 v[132:135], v180\n"
"ds_read_b64 v[106:107], v180 offset:64\n"
"v_mfma_f32_16x16x32_bf16 v[212:215], a[32:35], v[40:43], v[212:215]\n"
"ds_read_b128 v[116:119], v181\n"
"v_max_f32 v120, v120, v136\n"
"v_mfma_f32_16x16x32_bf16 v[212:215], a[44:47], v[44:47], v[212:215]\n"
"ds_read_b64 v[108:109], v181 offset:64\n"
"v_max_f32 v121, v121, v137\n"
"v_mfma_f32_16x16x32_bf16 v[212:215], a[56:59], v[48:51], v[212:215]\n"
"ds_write_b128 v202, a[204:207] offset:55296\n"
"v_mfma_f32_16x16x32_bf16 v[212:215], a[68:71], v[52:55], v[212:215]\n"
"v_max_f32 v122, v122, v138\n"
"v_max_f32 v123, v123, v139\n"
"v_mfma_f32_16x16x32_bf16 v[208:211], a[80:83], v[32:35], 0\n"
"v_max3_f32 v170, v120, v121, v122\n"
"v_max_f32 v124, v124, v140\n"
"v_mfma_f32_16x16x32_bf16 v[208:211], a[92:95], v[36:39], v[208:211]\n"
"ds_write_b128 v198, v[204:207]\n"
"v_mfma_f32_16x16x32_bf16 v[208:211], a[104:107], v[40:43], v[208:211]\n"
"s_waitcnt lgkmcnt(4)\n"
"v_max_f32 v125, v125, v141\n"
"v_max_f32 v126, v126, v142\n"
"v_mfma_f32_16x16x32_bf16 v[132:135], v[132:135], v[144:147], 0\n"
"v_max_f32 v127, v127, v143\n"
"v_max3_f32 v170, v170, v123, v124\n"
"v_mfma_f32_16x16x32_bf16 v[132:135], v[104:107], v[156:159], v[132:135]\n"
"s_waitcnt lgkmcnt(2)\n"
"ds_read_b128 v[64:67], v202 offset:43008\n"
"ds_read_b64 v[68:69], v62 offset:43008\n"
"v_mfma_f32_16x16x32_bf16 v[116:119], v[116:119], v[144:147], 0\n"
"ds_read_b64_tr_b16 v[136:137], v182 offset:72\n"
"ds_read_b64_tr_b16 v[138:139], v183 offset:72\n"
"v_mfma_f32_16x16x32_bf16 v[116:119], v[108:111], v[158:161], v[116:119]\n"
"ds_read_b64_tr_b16 v[140:141], v182 offset:840\n"
"v_cvt_pk_bf16_f32 v212, v212, v213\n"
"v_mfma_f32_16x16x32_bf16 v[208:211], a[116:119], v[44:47], v[208:211]\n"
"ds_read_b64_tr_b16 v[142:143], v183 offset:840\n"
"v_max3_f32 v170, v170, v125, v126\n"
"v_mfma_f32_16x16x32_bf16 v[208:211], a[128:131], v[48:51], v[208:211]\n"
"ds_write_b64 v203, a[208:209] offset:55296\n"
"v_mfma_f32_16x16x32_bf16 v[208:211], a[140:143], v[52:55], v[208:211]\n"
"v_cvt_pk_bf16_f32 v213, v214, v215\n"
"v_max_f32 v128, v128, v112\n"
"v_mfma_f32_16x16x32_bf16 v[204:207], a[72:75], v[32:35], 0\n"
"v_max_f32 v129, v129, v113\n"
"v_max_f32 v130, v130, v114\n"
"v_mfma_f32_16x16x32_bf16 v[204:207], a[84:87], v[36:39], v[204:207]\n"
"s_waitcnt lgkmcnt(5)\n"
"buffer_store_dwordx4 v[64:67], v194, s[24:27], s36 offen sc1\n"
"v_mfma_f32_16x16x32_bf16 v[204:207], a[96:99], v[40:43], v[204:207]\n"
"s_nop 0\n"
"ds_read_b128 v[64:67], v192 offset:30720\n"
"ds_read_b64_tr_b16 v[112:113], v182 offset:27080\n"
"v_mfma_f32_16x16x32_bf16 v[204:207], a[108:111], v[44:47], v[204:207]\n"
"v_max_f32 v131, v131, v115\n"
"v_max3_f32 v170, v170, v127, v128\n"
"v_mfma_f32_16x16x32_bf16 v[204:207], a[120:123], v[48:51], v[204:207]\n"
"buffer_store_dwordx2 v[68:69], v195, s[24:27], s36 offen sc1\n"
"v_mfma_f32_16x16x32_bf16 v[204:207], a[132:135], v[52:55], v[204:207]\n"
"s_add_u32 s36, s36, s33\n"
"ds_read_b128 v[68:71], v192 offset:31744\n"
"v_max3_f32 v170, v170, v129, v130\n"
"v_max_f32 v132, v132, v116\n"
"v_max_f32 v133, v133, v117\n"
"v_max_f32 v134, v134, v118\n"
"v_max_f32 v135, v135, v119\n"
"v_max3_f32 v170, v170, v131, v132\n"
"v_max3_f32 v170, v170, v133, v134\n"
"v_mov_b64 v[252:253], 0\n"
"v_mov_b64 v[254:255], 0\n"
"v_mov_b64 v[98:99], 0\n"
"v_mov_b64 v[100:101], 0\n"
"v_mov_b64 v[106:107], 0\n"
"v_mov_b64 v[108:109], 0\n"
"v_max_f32 v171, v170, v135\n"
"v_max_f32 v170, v170, v135\n"
"v_cvt_pk_bf16_f32 v214, v208, v209\n"
"v_cvt_pk_bf16_f32 v215, v210, v211\n"
"v_mfma_f32_16x16x32_bf16 v[208:211], a[76:79], v[32:35], 0\n"
"s_waitcnt lgkmcnt(2)\n"
"ds_read_b64_tr_b16 v[114:115], v183 offset:27080\n"
"ds_read_b64_tr_b16 v[116:117], v182 offset:27848\n"
"v_mfma_f32_16x16x32_bf16 v[232:235], a[216:219], v[64:67], 0\n"
"ds_read_b64_tr_b16 v[118:119], v183 offset:27848\n"
"v_permlane16_swap_b32 v170, v171\n"
"v_mfma_f32_16x16x32_bf16 v[236:239], a[220:223], v[64:67], 0\n"
"s_waitcnt lgkmcnt(3)\n"
"ds_write_b128 v198, v[212:215] offset:64\n"
"v_mfma_f32_16x16x32_bf16 v[236:239], a[232:235], v[68:71], v[236:239]\n"
"v_max_f32 v170, v170, v171\n"
"v_mov_b32 v171, v170\n"
"v_mfma_f32_16x16x32_bf16 v[240:243], a[224:227], v[64:67], 0\n"
"s_waitcnt vmcnt(5)\n"
"v_permlane32_swap_b32 v170, v171\n"
"v_max_f32 v170, v170, v171\n"
"v_mul_f32 v170, 0xbe553b95, v170\n"
"v_pk_fma_f32 v[120:121], v[120:121], s[70:71], v[170:171] op_sel_hi:[1,0,0]\n"
"v_pk_fma_f32 v[122:123], v[122:123], s[70:71], v[170:171] op_sel_hi:[1,0,0]\n"
"v_pk_fma_f32 v[124:125], v[124:125], s[70:71], v[170:171] op_sel_hi:[1,0,0]\n"
"v_pk_fma_f32 v[126:127], v[126:127], s[70:71], v[170:171] op_sel_hi:[1,0,0]\n"
"v_pk_fma_f32 v[128:129], v[128:129], s[70:71], v[170:171] op_sel_hi:[1,0,0]\n"
"v_pk_fma_f32 v[130:131], v[130:131], s[70:71], v[170:171] op_sel_hi:[1,0,0]\n"
"v_pk_fma_f32 v[132:133], v[132:133], s[70:71], v[170:171] op_sel_hi:[1,0,0]\n"
"v_pk_fma_f32 v[134:135], v[134:135], s[70:71], v[170:171] op_sel_hi:[1,0,0]\n"
"v_mfma_f32_16x16x32_bf16 v[240:243], a[236:239], v[68:71], v[240:243]\n"
"v_add3_u32 v200, v200, s50, -10\n"
"v_cmp_lt_u32 vcc, 64, v200\n"
"v_mfma_f32_16x16x32_bf16 v[232:235], a[228:231], v[68:71], v[232:235]\n"
"ds_read_b128 v[64:67], v192 offset:32768\n"
"ds_read_b128 v[68:71], v192 offset:33792\n"
"v_mfma_f32_16x16x32_bf16 v[208:211], a[88:91], v[36:39], v[208:211]\n"
"s_nop 0\n"
"s_cbranch_vccnz .Lp9%=\n"
".Lr9%=:\n"
"v_mfma_f32_16x16x32_bf16 v[208:211], a[100:103], v[40:43], v[208:211]\n"
"buffer_load_dwordx4 a[150:153], v192, s[28:31], s13 offen sc1\n"
"v_exp_f32 v120, v120\n"
"v_mfma_f32_16x16x32_bf16 v[208:211], a[112:115], v[44:47], v[208:211]\n"
"v_exp_f32 v121, v121\n"
"v_mfma_f32_16x16x32_bf16 v[208:211], a[124:127], v[48:51], v[208:211]\n"
"v_exp_f32 v122, v122\n"
"v_mfma_f32_16x16x32_bf16 v[208:211], a[136:139], v[52:55], v[208:211]\n"
"buffer_load_dwordx2 a[154:155], v193, s[28:31], s13 offen offset:1024 sc1\n"
"v_exp_f32 v123, v123\n"
"v_cvt_pk_bf16_f32 v82, v120, v122\n"
"v_cvt_pk_bf16_f32 v83, v121, v123\n"
"v_exp_f32 v124, v124\n"
"v_cvt_pk_bf16_f32 v204, v204, v205\n"
"v_exp_f32 v125, v125\n"
"v_exp_f32 v126, v126\n"
"v_exp_f32 v127, v127\n"
"v_cvt_pk_bf16_f32 v84, v124, v126\n"
"v_cvt_pk_bf16_f32 v85, v125, v127\n"
"v_cvt_pk_bf16_f32 v205, v206, v207\n"
"v_exp_f32 v128, v128\n"
"v_smfmac_f32_16x16x64_bf16 v[252:255], v[82:85], v[216:223], v63\n"
"s_waitcnt lgkmcnt(1)\n"
"buffer_load_dwordx4 a[156:159], v192, s[28:31], s14 offen sc1\n"
"v_mfma_f32_16x16x32_bf16 v[232:235], a[240:243], v[64:67], v[232:235]\n"
"s_waitcnt lgkmcnt(0)\n"
"ds_read_b64_tr_b16 v[216:217], v182 offset:27136\n"
"ds_read_b64_tr_b16 v[218:219], v183 offset:27136\n"
"v_mfma_f32_16x16x32_bf16 v[232:235], a[252:255], v[68:71], v[232:235]\n"
"ds_read_b64_tr_b16 v[220:221], v182 offset:27904\n"
"v_cvt_pk_bf16_f32 v206, v208, v209\n"
"v_mfma_f32_16x16x32_bf16 v[236:239], a[244:247], v[64:67], v[236:239]\n"
"ds_read_b64_tr_b16 v[222:223], v183 offset:27904\n"
"buffer_load_dwordx2 a[160:161], v193, s[28:31], s14 offen offset:1024 sc1\n"
"v_mfma_f32_16x16x32_bf16 v[236:239], v[0:3], v[68:71], v[236:239]\n"
"ds_read_b128 v[120:123], v178 offset:13504\n"
"v_cvt_pk_bf16_f32 v207, v210, v211\n"
"v_smfmac_f32_16x16x64_bf16 v[98:101], v[82:85], v[72:79], v63\n"
"ds_read_b64_tr_b16 v[72:73], v182 offset:27144\n"
"v_mfma_f32_16x16x32_bf16 v[240:243], a[248:251], v[64:67], v[240:243]\n"
"ds_read_b128 v[64:67], v192 offset:34816\n"
"v_exp_f32 v129, v129\n"
"v_mfma_f32_16x16x32_bf16 v[240:243], v[4:7], v[68:71], v[240:243]\n"
"ds_read_b128 v[68:71], v192 offset:35840\n"
"ds_read_b64_tr_b16 v[74:75], v183 offset:27144\n"
"v_smfmac_f32_16x16x64_bf16 v[106:109], v[82:85], v[136:143], v63\n"
"ds_read_b64_tr_b16 v[76:77], v182 offset:27912\n"
"v_mfma_f32_16x16x32_bf16 v[82:85], v[172:175], v[82:85], 0\n"
"v_exp_f32 v130, v130\n"
"ds_read_b64_tr_b16 v[78:79], v183 offset:27912\n"
"v_exp_f32 v131, v131\n"
"v_cvt_pk_bf16_f32 v90, v128, v130\n"
"v_cvt_pk_bf16_f32 v91, v129, v131\n"
"v_exp_f32 v132, v132\n"
"v_exp_f32 v133, v133\n"
"v_exp_f32 v134, v134\n"
"v_exp_f32 v135, v135\n"
"v_cvt_pk_bf16_f32 v92, v132, v134\n"
"v_cvt_pk_bf16_f32 v93, v133, v135\n"
"s_waitcnt lgkmcnt(4)\n"
"s_nop 0\n"
"v_smfmac_f32_16x16x64_bf16 v[252:255], v[90:93], v[216:223], v63\n"
"ds_write_b128 v198, v[204:207] offset:128\n"
"v_mfma_f32_16x16x32_bf16 v[232:235], v[8:11], v[64:67], v[232:235]\n"
"s_waitcnt lgkmcnt(4)\n"
"v_mfma_f32_16x16x32_bf16 v[232:235], v[20:23], v[68:71], v[232:235]\n"
"s_waitcnt lgkmcnt(1)\n"
"ds_read_b128 v[136:139], v179 offset:13504\n"
"v_smfmac_f32_16x16x64_bf16 v[98:101], v[90:93], v[72:79], v63\n"
"ds_read_b128 v[124:127], v178 offset:27008\n"
"v_smfmac_f32_16x16x64_bf16 v[106:109], v[90:93], v[112:119], v63\n"
"ds_read_b128 v[140:143], v179 offset:27008\n"
"v_mfma_f32_16x16x32_bf16 v[90:93], v[172:175], v[90:93], v[82:85]\n"
"ds_read_b128 v[128:131], v180\n"
"ds_read_b128 v[112:115], v181\n"
"v_mfma_f32_16x16x32_bf16 v[236:239], v[12:15], v[64:67], v[236:239]\n"
"ds_read_b64 v[82:83], v178 offset:13568\n"
"v_cvt_pk_bf16_f32 v252, v252, v253\n"
"v_mfma_f32_16x16x32_bf16 v[236:239], v[24:27], v[68:71], v[236:239]\n"
"s_waitcnt lgkmcnt(6)\n"
"ds_read_b64 v[84:85], v179 offset:13568\n"
"v_cvt_pk_bf16_f32 v253, v254, v255\n"
"v_mfma_f32_16x16x32_bf16 v[240:243], v[16:19], v[64:67], v[240:243]\n"
"s_barrier\n"
"v_mfma_f32_16x16x32_bf16 v[240:243], v[28:31], v[68:71], v[240:243]\n"
"ds_read_b128 v[64:67], v192 offset:55296\n"
"ds_read_b128 v[68:71], v192 offset:56320\n"
"v_rcp_f32 v171, v90\n"
"buffer_load_dwordx4 v[52:55], v192, s[28:31], s15 offen sc1\n"
"v_cvt_pk_bf16_f32 v171, v171, v171\n"
"v_and_b32 v92, v171, v176\n"
"v_and_b32 v93, v171, v177\n"
"v_cvt_pk_bf16_f32 v98, v98, v99\n"
"v_cvt_pk_bf16_f32 v99, v100, v101\n"
"v_mfma_f32_16x16x16_bf16 v[252:255], v[252:253], v[92:93], 0\n"
"ds_read_b64 v[90:91], v178 offset:27072\n"
"ds_read_b128 v[132:135], v180 offset:13504\n"
"v_mfma_f32_16x16x16_bf16 v[98:101], v[98:99], v[92:93], 0\n"
"buffer_load_dwordx2 v[152:153], v193, s[28:31], s15 offen offset:1024 sc1\n"
"ds_read_b128 v[116:119], v181 offset:13504\n"
"v_cvt_pk_bf16_f32 v106, v106, v107\n"
"v_cvt_pk_bf16_f32 v107, v108, v109\n"
"v_cvt_pk_bf16_f32 v232, v232, v233\n"
"v_cvt_pk_bf16_f32 v252, v252, v253\n"
"v_mfma_f32_16x16x16_bf16 v[106:109], v[106:107], v[92:93], 0\n"
"s_waitcnt lgkmcnt(5)\n"
"ds_read_b64 v[92:93], v179 offset:27072\n"
"v_cvt_pk_bf16_f32 v253, v254, v255\n"
"v_mfma_f32_16x16x32_bf16 v[120:123], v[120:123], v[148:151], 0\n"
"v_cvt_pk_bf16_f32 v254, v98, v99\n"
"v_cvt_pk_bf16_f32 v255, v100, v101\n"
"v_mfma_f32_16x16x32_bf16 v[120:123], v[80:83], v[160:163], v[120:123]\n"
"buffer_load_dword v201, off, s[28:31], s39 sc1\n"
"ds_read_b64 v[98:99], v180 offset:64\n"
"v_mfma_f32_16x16x32_bf16 v[136:139], v[136:139], v[148:151], 0\n"
"ds_read_b64 v[100:101], v181 offset:64\n"
"v_cvt_pk_bf16_f32 v106, v106, v107\n"
"v_mfma_f32_16x16x32_bf16 v[136:139], v[84:87], v[162:165], v[136:139]\n"
"s_waitcnt lgkmcnt(5)\n"
"v_cvt_pk_bf16_f32 v107, v108, v109\n"
"v_min_u32 v252, -2, v252\n"
"v_mfma_f32_16x16x32_bf16 v[124:127], v[124:127], v[148:151], 0\n"
"ds_read_b64 v[108:109], v181 offset:13568\n"
"v_min_u32 v253, -2, v253\n"
"v_mfma_f32_16x16x32_bf16 v[124:127], v[88:91], v[160:163], v[124:127]\n"
"v_min_u32 v254, -2, v254\n"
"v_min_u32 v255, -2, v255\n"
"v_min_u32 v106, -2, v106\n"
"v_min_u32 v107, -2, v107\n"
"v_cvt_pk_bf16_f32 v233, v234, v235\n"
"v_cvt_pk_bf16_f32 v234, v236, v237\n"
"v_cvt_pk_bf16_f32 v235, v238, v239\n"
"v_mfma_f32_16x16x32_bf16 v[236:239], a[220:223], v[64:67], 0\n"
"s_waitcnt lgkmcnt(3)\n"
"buffer_store_dwordx4 v[252:255], v192, s[28:31], s37 offen sc1\n"
"v_mfma_f32_16x16x32_bf16 v[236:239], a[232:235], v[68:71], v[236:239]\n"
"s_waitcnt lgkmcnt(1)\n"
"ds_write_b128 v196, v[232:235] offset:49152\n"
"v_mfma_f32_16x16x32_bf16 v[140:143], v[140:143], v[148:151], 0\n"
"s_waitcnt lgkmcnt(1)\n"
"buffer_store_dwordx2 v[106:107], v193, s[28:31], s37 offen offset:1024 sc1\n"
"v_mfma_f32_16x16x32_bf16 v[140:143], v[92:95], v[162:165], v[140:143]\n"
"s_add_u32 s37, s37, 1572864\n"
"ds_read_b64 v[106:107], v180 offset:13568\n"
"v_cvt_pk_bf16_f32 v240, v240, v241\n"
"v_mfma_f32_16x16x32_bf16 v[128:131], v[128:131], v[148:151], 0\n"
"v_cvt_pk_bf16_f32 v241, v242, v243\n"
"v_max_f32 v120, v120, v136\n"
"v_mfma_f32_16x16x32_bf16 v[128:131], v[96:99], v[160:163], v[128:131]\n"
"ds_write_b64 v197, v[240:241] offset:49152\n"
"v_mfma_f32_16x16x32_bf16 v[112:115], v[112:115], v[148:151], 0\n"
"v_max_f32 v121, v121, v137\n"
"v_max_f32 v122, v122, v138\n"
"v_mfma_f32_16x16x32_bf16 v[112:115], v[100:103], v[162:165], v[112:115]\n"
"ds_read_b64_tr_b16 v[136:137], v182 offset:40584\n"
"v_max_f32 v123, v123, v139\n"
"v_mfma_f32_16x16x32_bf16 v[116:119], v[116:119], v[148:151], 0\n"
"ds_read_b64_tr_b16 v[138:139], v183 offset:40584\n"
"v_max3_f32 v170, v120, v121, v122\n"
"v_mfma_f32_16x16x32_bf16 v[116:119], v[108:111], v[162:165], v[116:119]\n"
"v_max_f32 v124, v124, v140\n"
"v_max_f32 v125, v125, v141\n"
"v_mfma_f32_16x16x32_bf16 v[232:235], a[216:219], v[64:67], 0\n"
"s_waitcnt lgkmcnt(3)\n"
"ds_read_b64_tr_b16 v[140:141], v182 offset:41352\n"
"v_max_f32 v126, v126, v142\n"
"v_mfma_f32_16x16x32_bf16 v[232:235], a[228:231], v[68:71], v[232:235]\n"
"v_max3_f32 v170, v170, v123, v124\n"
"v_max_f32 v127, v127, v143\n"
"v_mfma_f32_16x16x32_bf16 v[132:135], v[132:135], v[148:151], 0\n"
"ds_read_b64_tr_b16 v[142:143], v183 offset:41352\n"
"v_max3_f32 v170, v170, v125, v126\n"
"v_mfma_f32_16x16x32_bf16 v[132:135], v[104:107], v[160:163], v[132:135]\n"
"v_max_f32 v128, v128, v112\n"
"v_max_f32 v129, v129, v113\n"
"v_mfma_f32_16x16x32_bf16 v[240:243], a[224:227], v[64:67], 0\n"
"ds_read_b128 v[64:67], v192 offset:57344\n"
"v_max_f32 v130, v130, v114\n"
"v_mfma_f32_16x16x32_bf16 v[240:243], a[236:239], v[68:71], v[240:243]\n"
"ds_read_b128 v[68:71], v192 offset:58368\n"
"v_max3_f32 v170, v170, v127, v128\n"
"v_max_f32 v131, v131, v115\n"
"v_max3_f32 v170, v170, v129, v130\n"
"v_max_f32 v132, v132, v116\n"
"v_max_f32 v133, v133, v117\n"
"v_max_f32 v134, v134, v118\n"
"v_max3_f32 v170, v170, v131, v132\n"
"v_max_f32 v135, v135, v119\n"
"v_max3_f32 v170, v170, v133, v134\n"
"v_max_f32 v171, v170, v135\n"
"v_max_f32 v170, v170, v135\n"
"v_mov_b64 v[252:253], 0\n"
"v_mov_b64 v[254:255], 0\n"
"v_permlane16_swap_b32 v170, v171\n"
"v_max_f32 v170, v170, v171\n"
"v_mov_b32 v171, v170\n"
"v_mov_b64 v[98:99], 0\n"
"v_mov_b64 v[100:101], 0\n"
"v_permlane32_swap_b32 v170, v171\n"
"v_max_f32 v170, v170, v171\n"
"v_mul_f32 v170, 0xbe553b95, v170\n"
"v_pk_fma_f32 v[120:121], v[120:121], s[70:71], v[170:171] op_sel_hi:[1,0,0]\n"
"v_pk_fma_f32 v[122:123], v[122:123], s[70:71], v[170:171] op_sel_hi:[1,0,0]\n"
"v_pk_fma_f32 v[124:125], v[124:125], s[70:71], v[170:171] op_sel_hi:[1,0,0]\n"
"v_pk_fma_f32 v[126:127], v[126:127], s[70:71], v[170:171] op_sel_hi:[1,0,0]\n"
"v_pk_fma_f32 v[128:129], v[128:129], s[70:71], v[170:171] op_sel_hi:[1,0,0]\n"
"v_pk_fma_f32 v[130:131], v[130:131], s[70:71], v[170:171] op_sel_hi:[1,0,0]\n"
"v_pk_fma_f32 v[132:133], v[132:133], s[70:71], v[170:171] op_sel_hi:[1,0,0]\n"
"v_pk_fma_f32 v[134:135], v[134:135], s[70:71], v[170:171] op_sel_hi:[1,0,0]\n"
"v_exp_f32 v120, v120\n"
"v_exp_f32 v121, v121\n"
"v_exp_f32 v122, v122\n"
"v_exp_f32 v123, v123\n"
"v_exp_f32 v124, v124\n"
"v_exp_f32 v125, v125\n"
"v_exp_f32 v126, v126\n"
"v_exp_f32 v127, v127\n"
"v_cvt_pk_bf16_f32 v82, v120, v122\n"
"v_cvt_pk_bf16_f32 v83, v121, v123\n"
"v_cvt_pk_bf16_f32 v84, v124, v126\n"
"v_cvt_pk_bf16_f32 v85, v125, v127\n"
"v_mov_b64 v[106:107], 0\n"
"v_mov_b64 v[108:109], 0\n"
"v_smfmac_f32_16x16x64_bf16 v[252:255], v[82:85], v[224:231], v63\n"
"s_waitcnt lgkmcnt(1)\n"
"ds_read_b64_tr_b16 v[224:225], v182 offset:40640\n"
"v_mfma_f32_16x16x32_bf16 v[232:235], a[240:243], v[64:67], v[232:235]\n"
"s_waitcnt lgkmcnt(1)\n"
"ds_read_b64_tr_b16 v[226:227], v183 offset:40640\n"
"ds_read_b64_tr_b16 v[228:229], v182 offset:41408\n"
"v_mfma_f32_16x16x32_bf16 v[232:235], a[252:255], v[68:71], v[232:235]\n"
"ds_read_b64_tr_b16 v[230:231], v183 offset:41408\n"
"v_exp_f32 v128, v128\n"
"v_mfma_f32_16x16x32_bf16 v[236:239], a[244:247], v[64:67], v[236:239]\n"
"v_exp_f32 v129, v129\n"
"v_mfma_f32_16x16x32_bf16 v[236:239], v[0:3], v[68:71], v[236:239]\n"
"v_exp_f32 v130, v130\n"
"v_mfma_f32_16x16x32_bf16 v[240:243], a[248:251], v[64:67], v[240:243]\n"
"ds_read_b128 v[64:67], v192 offset:59392\n"
"v_cvt_pk_bf16_f32 v90, v128, v130\n"
"v_mfma_f32_16x16x32_bf16 v[240:243], v[4:7], v[68:71], v[240:243]\n"
"ds_read_b128 v[68:71], v192 offset:60416\n"
"v_exp_f32 v131, v131\n"
"v_smfmac_f32_16x16x64_bf16 v[98:101], v[82:85], v[244:251], v63\n"
"ds_read_b64_tr_b16 v[244:245], v182 offset:40648\n"
"v_smfmac_f32_16x16x64_bf16 v[106:109], v[82:85], a[168:175], v63\n"
"ds_read_b64_tr_b16 v[246:247], v183 offset:40648\n"
"v_mfma_f32_16x16x32_bf16 v[82:85], v[172:175], v[82:85], 0\n"
"ds_read_b64_tr_b16 v[248:249], v182 offset:41416\n"
"ds_read_b64_tr_b16 v[250:251], v183 offset:41416\n"
"v_exp_f32 v132, v132\n"
"v_exp_f32 v133, v133\n"
"v_exp_f32 v134, v134\n"
"v_exp_f32 v135, v135\n"
"v_cvt_pk_bf16_f32 v91, v129, v131\n"
"v_cvt_pk_bf16_f32 v92, v132, v134\n"
"v_cvt_pk_bf16_f32 v93, v133, v135\n"
"s_waitcnt lgkmcnt(5)\n"
"v_mfma_f32_16x16x32_bf16 v[232:235], v[8:11], v[64:67], v[232:235]\n"
"s_waitcnt lgkmcnt(4)\n"
"s_barrier\n"
"v_mfma_f32_16x16x32_bf16 v[232:235], v[20:23], v[68:71], v[232:235]\n"
"ds_read_b128 v[44:47], v192\n"
"ds_read_b128 v[48:51], v192 offset:1024\n"
"v_mfma_f32_16x16x32_bf16 v[240:243], v[16:19], v[64:67], v[240:243]\n"
"ds_read_b128 a[0:3], v192 offset:2048\n"
"v_mfma_f32_16x16x32_bf16 v[240:243], v[28:31], v[68:71], v[240:243]\n"
"ds_read_b128 a[4:7], v192 offset:3072\n"
"v_mfma_f32_16x16x32_bf16 v[236:239], v[12:15], v[64:67], v[236:239]\n"
"s_waitcnt lgkmcnt(4)\n"
"ds_read_b128 a[8:11], v192 offset:4096\n"
"v_cvt_pk_bf16_f32 v232, v232, v233\n"
"v_mfma_f32_16x16x32_bf16 v[236:239], v[24:27], v[68:71], v[236:239]\n"
"ds_read_b128 a[12:15], v192 offset:5120\n"
"v_cvt_pk_bf16_f32 v233, v234, v235\n"
"v_smfmac_f32_16x16x64_bf16 v[252:255], v[90:93], v[224:231], v63\n"
"ds_read_b128 v[64:67], v202 offset:49152\n"
"v_smfmac_f32_16x16x64_bf16 v[98:101], v[90:93], v[244:251], v63\n"
"ds_read_b64 v[68:69], v62 offset:49152\n"
"v_smfmac_f32_16x16x64_bf16 v[106:109], v[90:93], v[136:143], v63\n"
"s_waitcnt lgkmcnt(7)\n"
"v_cvt_pk_bf16_f32 v240, v240, v241\n"
"v_mfma_f32_16x16x32_bf16 v[90:93], v[172:175], v[90:93], v[82:85]\n"
"v_cvt_pk_bf16_f32 v234, v236, v237\n"
"v_cvt_pk_bf16_f32 v235, v238, v239\n"
"v_mfma_f32_16x16x32_bf16 v[204:207], a[216:219], v[44:47], 0\n"
"s_waitcnt lgkmcnt(6)\n"
"ds_write_b128 v196, v[232:235] offset:36864\n"
"v_mfma_f32_16x16x32_bf16 v[204:207], a[228:231], v[48:51], v[204:207]\n"
"s_waitcnt lgkmcnt(6)\n"
"v_cvt_pk_bf16_f32 v241, v242, v243\n"
"v_cvt_pk_bf16_f32 v252, v252, v253\n"
"v_mfma_f32_16x16x32_bf16 v[204:207], a[240:243], a[0:3], v[204:207]\n"
"s_waitcnt lgkmcnt(5)\n"
"v_rcp_f32 v171, v90\n"
"v_mfma_f32_16x16x32_bf16 v[204:207], a[252:255], a[4:7], v[204:207]\n"
"s_waitcnt lgkmcnt(2)\n"
"ds_write_b64 v197, v[240:241] offset:36864\n"
"v_mfma_f32_16x16x32_bf16 v[204:207], v[8:11], a[8:11], v[204:207]\n"
"s_waitcnt lgkmcnt(2)\n"
"buffer_store_dwordx4 v[64:67], v194, s[24:27], s36 offen sc1\n"
"v_mfma_f32_16x16x32_bf16 v[204:207], v[20:23], a[12:15], v[204:207]\n"
"v_cvt_pk_bf16_f32 v171, v171, v171\n"
"v_and_b32 v92, v171, v176\n"
"v_mfma_f32_16x16x32_bf16 v[208:211], a[220:223], v[44:47], 0\n"
"v_and_b32 v93, v171, v177\n"
"v_cvt_pk_bf16_f32 v253, v254, v255\n"
"v_mfma_f32_16x16x32_bf16 v[208:211], a[232:235], v[48:51], v[208:211]\n"
"buffer_store_dwordx2 v[68:69], v195, s[24:27], s36 offen sc1\n"
"v_mfma_f32_16x16x16_bf16 v[252:255], v[252:253], v[92:93], 0\n"
"s_waitcnt vmcnt(10)\n"
"ds_write_b128 v202, a[150:153] offset:24576\n"
"v_mfma_f32_16x16x32_bf16 v[212:215], a[224:227], v[44:47], 0\n"
"s_add_u32 s36, s36, s33\n"
"v_cvt_pk_bf16_f32 v98, v98, v99\n"
".Lic7%=:\n"
"s_cbranch_execz .Lib1%=\n"
"v_cvt_pk_bf16_f32 v99, v100, v101\n"
"v_mfma_f32_16x16x32_bf16 v[212:215], a[236:239], v[48:51], v[212:215]\n"
"s_waitcnt vmcnt(4)\n"
"v_add3_u32 v201, v201, s50, -11\n"
"v_cmp_lt_u32 vcc, 64, v201\n"
"v_mfma_f32_16x16x16_bf16 v[98:101], v[98:99], v[92:93], 0\n"
"ds_write_b64 v203, a[154:155] offset:24576\n"
"v_mfma_f32_16x16x32_bf16 v[208:211], a[244:247], a[0:3], v[208:211]\n"
"s_nop 1\n"
"s_cbranch_vccnz .Lp10%=\n"
".Lr10%=:\n"
"v_mfma_f32_16x16x32_bf16 v[208:211], v[0:3], a[4:7], v[208:211]\n"
"buffer_load_dwordx4 a[162:165], v192, s[28:31], s16 offen sc1\n"
"ds_write_b128 v202, a[156:159] offset:30720\n"
"v_mfma_f32_16x16x32_bf16 v[208:211], v[12:15], a[8:11], v[208:211]\n"
"v_cvt_pk_bf16_f32 v106, v106, v107\n"
"v_cvt_pk_bf16_f32 v107, v108, v109\n"
"v_mfma_f32_16x16x32_bf16 v[208:211], v[24:27], a[12:15], v[208:211]\n"
"buffer_load_dwordx2 a[166:167], v193, s[28:31], s16 offen offset:1024 sc1\n"
"ds_write_b64 v203, a[160:161] offset:30720\n"
"v_mfma_f32_16x16x16_bf16 v[106:109], v[106:107], v[92:93], 0\n"
"v_cvt_pk_bf16_f32 v252, v252, v253\n"
"v_cvt_pk_bf16_f32 v253, v254, v255\n"
"v_mfma_f32_16x16x32_bf16 v[212:215], a[248:251], a[0:3], v[212:215]\n"
"v_cvt_pk_bf16_f32 v254, v98, v99\n"
"v_cvt_pk_bf16_f32 v255, v100, v101\n"
"v_mfma_f32_16x16x32_bf16 v[212:215], v[4:7], a[4:7], v[212:215]\n"
"v_min_u32 v252, -2, v252\n"
"v_min_u32 v253, -2, v253\n"
"v_mfma_f32_16x16x32_bf16 v[212:215], v[16:19], a[8:11], v[212:215]\n"
"v_cvt_pk_bf16_f32 v106, v106, v107\n"
"v_min_u32 v254, -2, v254\n"
"v_mfma_f32_16x16x32_bf16 v[212:215], v[28:31], a[12:15], v[212:215]\n"
"v_min_u32 v255, -2, v255\n"
"buffer_store_dwordx4 v[252:255], v192, s[28:31], s37 offen sc1\n"
"s_waitcnt lgkmcnt(0)\n"
"s_barrier\n"
"ds_read_b128 v[44:47], v192 offset:24576\n"
"ds_read_b128 v[48:51], v192 offset:25600\n"
"ds_read_b128 a[0:3], v192 offset:26624\n"
"v_cvt_pk_bf16_f32 v107, v108, v109\n"
"v_min_u32 v106, -2, v106\n"
"v_min_u32 v107, -2, v107\n"
"buffer_store_dwordx2 v[106:107], v193, s[28:31], s37 offen offset:1024 sc1\n"
"ds_read_b128 a[4:7], v192 offset:27648\n"
"ds_read_b128 a[8:11], v192 offset:28672\n"
"s_add_u32 s37, s37, 1572864\n"
"ds_read_b128 a[12:15], v192 offset:29696\n"
"v_cvt_pk_bf16_f32 v204, v204, v205\n"
"v_cvt_pk_bf16_f32 v205, v206, v207\n"
"v_cvt_pk_bf16_f32 v206, v208, v209\n"
"v_cvt_pk_bf16_f32 v207, v210, v211\n"
"v_cvt_pk_bf16_f32 v212, v212, v213\n"
"v_cvt_pk_bf16_f32 v213, v214, v215\n"
"buffer_load_dwordx4 v[144:147], v192, s[28:31], s17 offen sc1\n"
"ds_write_b128 v196, v[204:207] offset:12288\n"
"s_waitcnt lgkmcnt(6)\n"
"v_mfma_f32_16x16x32_bf16 v[232:235], a[216:219], v[44:47], 0\n"
"s_waitcnt lgkmcnt(5)\n"
"v_mfma_f32_16x16x32_bf16 v[232:235], a[228:231], v[48:51], v[232:235]\n"
"s_waitcnt lgkmcnt(4)\n"
"buffer_load_dwordx2 v[154:155], v193, s[28:31], s17 offen offset:1024 sc1\n"
"ds_write_b64 v197, v[212:213] offset:12288\n"
"v_mfma_f32_16x16x32_bf16 v[232:235], a[240:243], a[0:3], v[232:235]\n"
"s_waitcnt lgkmcnt(4)\n"
"v_mfma_f32_16x16x32_bf16 v[232:235], a[252:255], a[4:7], v[232:235]\n"
"s_waitcnt lgkmcnt(3)\n"
"ds_read_b128 v[64:67], v202 offset:36864\n"
"ds_read_b64 v[68:69], v62 offset:36864\n"
"v_mfma_f32_16x16x32_bf16 v[232:235], v[8:11], a[8:11], v[232:235]\n"
"s_waitcnt lgkmcnt(4)\n"
"v_mfma_f32_16x16x32_bf16 v[232:235], v[20:23], a[12:15], v[232:235]\n"
"v_mfma_f32_16x16x32_bf16 v[236:239], a[220:223], v[44:47], 0\n"
"v_mfma_f32_16x16x32_bf16 v[236:239], a[232:235], v[48:51], v[236:239]\n"
"v_mfma_f32_16x16x32_bf16 v[236:239], a[244:247], a[0:3], v[236:239]\n"
"v_mfma_f32_16x16x32_bf16 v[236:239], v[0:3], a[4:7], v[236:239]\n"
"v_mfma_f32_16x16x32_bf16 v[236:239], v[12:15], a[8:11], v[236:239]\n"
"s_waitcnt lgkmcnt(1)\n"
"buffer_store_dwordx4 v[64:67], v194, s[24:27], s36 offen sc1\n"
"v_mfma_f32_16x16x32_bf16 v[236:239], v[24:27], a[12:15], v[236:239]\n"
"s_waitcnt lgkmcnt(0)\n"
"ds_read_b128 v[64:67], v192 offset:30720\n"
"v_cvt_pk_bf16_f32 v232, v232, v233\n"
"v_mfma_f32_16x16x32_bf16 v[240:243], a[224:227], v[44:47], 0\n"
"ds_read_b128 v[44:47], v192 offset:32768\n"
"v_cvt_pk_bf16_f32 v233, v234, v235\n"
"v_mfma_f32_16x16x32_bf16 v[240:243], a[236:239], v[48:51], v[240:243]\n"
"ds_read_b128 v[48:51], v192 offset:33792\n"
"buffer_store_dwordx2 v[68:69], v195, s[24:27], s36 offen sc1\n"
"v_mfma_f32_16x16x32_bf16 v[240:243], a[248:251], a[0:3], v[240:243]\n"
"s_add_u32 s36, s36, s33\n"
"ds_read_b128 v[68:71], v192 offset:31744\n"
"ds_read_b128 a[0:3], v192 offset:34816\n"
"v_mfma_f32_16x16x32_bf16 v[240:243], v[4:7], a[4:7], v[240:243]\n"
"s_add_u32 s8, s8, s36\n"
"ds_read_b128 a[4:7], v192 offset:35840\n"
"v_cvt_pk_bf16_f32 v234, v236, v237\n"
"v_mfma_f32_16x16x32_bf16 v[240:243], v[16:19], a[8:11], v[240:243]\n"
"s_add_u32 s9, s9, s36\n"
"v_cvt_pk_bf16_f32 v235, v238, v239\n"
"v_max3_u32 v200, v52, v53, v54\n"
"v_mfma_f32_16x16x32_bf16 v[240:243], v[28:31], a[12:15], v[240:243]\n"
"s_add_u32 s10, s10, s36\n"
"ds_write_b128 v196, v[232:235] offset:43008\n"
"v_max3_u32 v200, v200, v55, v152\n"
"v_max_u32 v200, v200, v153\n"
"v_cmp_eq_u32 vcc, -1, v200\n"
"s_waitcnt lgkmcnt(6)\n"
"v_mfma_f32_16x16x32_bf16 v[32:35], a[216:219], v[64:67], 0\n"
"s_add_u32 s11, s11, s36\n"
"v_cvt_pk_bf16_f32 v240, v240, v241\n"
"v_cvt_pk_bf16_f32 v241, v242, v243\n"
"v_mfma_f32_16x16x32_bf16 v[36:39], a[220:223], v[64:67], 0\n"
"s_waitcnt lgkmcnt(3)\n"
"ds_write_b64 v197, v[240:241] offset:43008\n"
"v_mfma_f32_16x16x32_bf16 v[36:39], a[232:235], v[68:71], v[36:39]\n"
"s_cbranch_vccnz .Lp11%=\n"
".Lr11%=:\n"
"v_mfma_f32_16x16x32_bf16 v[36:39], a[244:247], v[44:47], v[36:39]\n"
"s_add_u32 s20, s20, s36\n"
"ds_write_b128 v202, v[52:55] offset:18432\n"
"v_mfma_f32_16x16x32_bf16 v[36:39], v[0:3], v[48:51], v[36:39]\n"
"s_waitcnt lgkmcnt(4)\n"
"s_add_u32 s21, s21, s36\n"
"s_add_u32 s22, s22, s36\n"
"v_mfma_f32_16x16x32_bf16 v[36:39], v[12:15], a[0:3], v[36:39]\n"
"s_waitcnt lgkmcnt(3)\n"
"ds_write_b64 v203, v[152:153] offset:18432\n"
"v_mfma_f32_16x16x32_bf16 v[36:39], v[24:27], a[4:7], v[36:39]\n"
"v_mfma_f32_16x16x32_bf16 v[40:43], a[224:227], v[64:67], 0\n"
"v_mfma_f32_16x16x32_bf16 v[40:43], a[236:239], v[68:71], v[40:43]\n"
"v_mfma_f32_16x16x32_bf16 v[40:43], a[248:251], v[44:47], v[40:43]\n"
"v_mfma_f32_16x16x32_bf16 v[40:43], v[4:7], v[48:51], v[40:43]\n"
"v_mfma_f32_16x16x32_bf16 v[40:43], v[16:19], a[0:3], v[40:43]\n"
"v_mfma_f32_16x16x32_bf16 v[40:43], v[28:31], a[4:7], v[40:43]\n"
"s_waitcnt lgkmcnt(0)\n"
"s_barrier\n"
"v_mfma_f32_16x16x32_bf16 v[32:35], a[228:231], v[68:71], v[32:35]\n"
"s_waitcnt vmcnt(7)\n"
"ds_read_b128 v[64:67], v192 offset:18432\n"
"ds_write_b128 v202, a[162:165] offset:49152\n"
"v_mfma_f32_16x16x32_bf16 v[32:35], a[240:243], v[44:47], v[32:35]\n"
"s_nop 0\n"
"v_cvt_pk_bf16_f32 v40, v40, v41\n"
"v_mfma_f32_16x16x32_bf16 v[32:35], a[252:255], v[48:51], v[32:35]\n"
"s_waitcnt vmcnt(6)\n"
"v_cvt_pk_bf16_f32 v41, v42, v43\n"
"v_mfma_f32_16x16x32_bf16 v[32:35], v[8:11], a[0:3], v[32:35]\n"
"ds_write_b64 v203, a[166:167] offset:49152\n"
"v_mfma_f32_16x16x32_bf16 v[32:35], v[20:23], a[4:7], v[32:35]\n"
"ds_read_b128 v[68:71], v192 offset:19456\n"
"ds_read_b128 v[44:47], v192 offset:20480\n"
"ds_read_b128 v[48:51], v192 offset:21504\n"
"ds_read_b128 a[0:3], v192 offset:22528\n"
"s_waitcnt lgkmcnt(6)\n"
"v_mfma_f32_16x16x32_bf16 v[204:207], a[216:219], v[64:67], 0\n"
"ds_read_b128 a[4:7], v192 offset:23552\n"
"v_mfma_f32_16x16x32_bf16 v[208:211], a[220:223], v[64:67], 0\n"
"ds_read_b128 a[8:11], v202 offset:12288\n"
"v_cvt_pk_bf16_f32 v32, v32, v33\n"
"v_mfma_f32_16x16x32_bf16 v[212:215], a[224:227], v[64:67], 0\n"
"v_cvt_pk_bf16_f32 v33, v34, v35\n"
"v_cvt_pk_bf16_f32 v34, v36, v37\n"
"v_cvt_pk_bf16_f32 v35, v38, v39\n"
"ds_write_b128 v196, v[32:35] offset:6144\n"
"s_waitcnt lgkmcnt(6)\n"
"v_mfma_f32_16x16x32_bf16 v[212:215], a[236:239], v[68:71], v[212:215]\n"
"s_waitcnt lgkmcnt(5)\n"
"v_mfma_f32_16x16x32_bf16 v[212:215], a[248:251], v[44:47], v[212:215]\n"
"s_waitcnt lgkmcnt(4)\n"
"ds_write_b64 v197, v[40:41] offset:6144\n"
"v_mfma_f32_16x16x32_bf16 v[212:215], v[4:7], v[48:51], v[212:215]\n"
"s_waitcnt lgkmcnt(4)\n"
"v_mfma_f32_16x16x32_bf16 v[212:215], v[16:19], a[0:3], v[212:215]\n"
"s_waitcnt lgkmcnt(3)\n"
"ds_read_b64 a[12:13], v62 offset:12288\n"
"v_mfma_f32_16x16x32_bf16 v[212:215], v[28:31], a[4:7], v[212:215]\n"
"s_waitcnt lgkmcnt(3)\n"
"buffer_store_dwordx4 a[8:11], v194, s[24:27], s8 offen sc1\n"
"v_mfma_f32_16x16x32_bf16 v[204:207], a[228:231], v[68:71], v[204:207]\n"
"v_mfma_f32_16x16x32_bf16 v[204:207], a[240:243], v[44:47], v[204:207]\n"
"v_mfma_f32_16x16x32_bf16 v[204:207], a[252:255], v[48:51], v[204:207]\n"
"v_mfma_f32_16x16x32_bf16 v[204:207], v[8:11], a[0:3], v[204:207]\n"
"v_mfma_f32_16x16x32_bf16 v[204:207], v[20:23], a[4:7], v[204:207]\n"
"s_waitcnt lgkmcnt(0)\n"
"buffer_store_dwordx2 a[12:13], v195, s[24:27], s8 offen sc1\n"
"v_mfma_f32_16x16x32_bf16 v[208:211], a[232:235], v[68:71], v[208:211]\n"
"s_barrier\n"
"v_mfma_f32_16x16x32_bf16 v[208:211], a[244:247], v[44:47], v[208:211]\n"
"ds_read_b128 v[44:47], v192 offset:49152\n"
"ds_read_b128 a[8:11], v202 offset:43008\n"
"v_mfma_f32_16x16x32_bf16 v[208:211], v[0:3], v[48:51], v[208:211]\n"
"ds_read_b128 v[48:51], v192 offset:50176\n"
"v_cvt_pk_bf16_f32 v204, v204, v205\n"
"v_mfma_f32_16x16x32_bf16 v[208:211], v[12:15], a[0:3], v[208:211]\n"
"ds_read_b128 a[0:3], v192 offset:51200\n"
"v_cvt_pk_bf16_f32 v205, v206, v207\n"
"v_mfma_f32_16x16x32_bf16 v[208:211], v[24:27], a[4:7], v[208:211]\n"
"ds_read_b128 a[4:7], v192 offset:52224\n"
"v_cvt_pk_bf16_f32 v212, v212, v213\n"
"v_cvt_pk_bf16_f32 v213, v214, v215\n"
"ds_read_b64 a[12:13], v62 offset:43008\n"
"ds_write_b64 v197, v[212:213] offset:55296\n"
"s_nop 2\n"
"v_cvt_pk_bf16_f32 v206, v208, v209\n"
"v_cvt_pk_bf16_f32 v207, v210, v211\n"
"s_waitcnt vmcnt(5)\n"
"v_max3_u32 v200, v144, v145, v146\n"
"s_waitcnt vmcnt(4)\n"
"v_max3_u32 v200, v200, v147, v154\n"
"v_max_u32 v200, v200, v155\n"
"v_cmp_eq_u32 vcc, -1, v200\n"
"s_waitcnt lgkmcnt(6)\n"
"v_mfma_f32_16x16x32_bf16 v[232:235], a[216:219], v[44:47], 0\n"
"s_waitcnt lgkmcnt(4)\n"
"ds_write_b128 v196, v[204:207] offset:55296\n"
"v_mfma_f32_16x16x32_bf16 v[232:235], a[228:231], v[48:51], v[232:235]\n"
"s_waitcnt lgkmcnt(4)\n"
"buffer_store_dwordx4 a[8:11], v194, s[24:27], s9 offen sc1\n"
"v_mfma_f32_16x16x32_bf16 v[232:235], a[240:243], a[0:3], v[232:235]\n"
"s_waitcnt lgkmcnt(2)\n"
"ds_read_b128 a[8:11], v192 offset:53248\n"
"ds_read_b128 v[64:67], v202 offset:6144\n"
"v_mfma_f32_16x16x32_bf16 v[232:235], a[252:255], a[4:7], v[232:235]\n"
"s_cbranch_vccnz .Lp12%=\n"
".Lr12%=:\n"
"v_mfma_f32_16x16x32_bf16 v[236:239], a[220:223], v[44:47], 0\n"
"ds_write_b128 v202, v[144:147] offset:30720\n"
"v_mfma_f32_16x16x32_bf16 v[236:239], a[232:235], v[48:51], v[236:239]\n"
"buffer_store_dwordx2 a[12:13], v195, s[24:27], s9 offen sc1\n"
"v_mfma_f32_16x16x32_bf16 v[236:239], a[244:247], a[0:3], v[236:239]\n"
"ds_write_b64 v203, v[154:155] offset:30720\n"
"v_mfma_f32_16x16x32_bf16 v[236:239], v[0:3], a[4:7], v[236:239]\n"
"v_mfma_f32_16x16x32_bf16 v[240:243], a[224:227], v[44:47], 0\n"
"s_waitcnt lgkmcnt(3)\n"
"buffer_load_dwordx4 v[56:59], v192, s[28:31], s18 offen sc1\n"
"ds_read_b128 a[12:15], v192 offset:54272\n"
"v_mfma_f32_16x16x32_bf16 v[240:243], a[236:239], v[48:51], v[240:243]\n"
"s_waitcnt lgkmcnt(3)\n"
"ds_read_b64 v[68:69], v62 offset:6144\n"
"v_mfma_f32_16x16x32_bf16 v[240:243], a[248:251], a[0:3], v[240:243]\n"
"v_mfma_f32_16x16x32_bf16 v[240:243], v[4:7], a[4:7], v[240:243]\n"
"v_mfma_f32_16x16x32_bf16 v[240:243], v[16:19], a[8:11], v[240:243]\n"
"buffer_load_dwordx2 v[60:61], v193, s[28:31], s18 offen offset:1024 sc1\n"
"v_mfma_f32_16x16x32_bf16 v[232:235], v[8:11], a[8:11], v[232:235]\n"
"s_waitcnt lgkmcnt(2)\n"
"s_barrier\n"
"v_mfma_f32_16x16x32_bf16 v[236:239], v[12:15], a[8:11], v[236:239]\n"
"ds_read_b128 v[44:47], v192 offset:30720\n"
"ds_read_b128 v[48:51], v192 offset:31744\n"
"ds_read_b128 a[0:3], v192 offset:32768\n"
"s_waitcnt lgkmcnt(4)\n"
"v_mfma_f32_16x16x32_bf16 v[236:239], v[24:27], a[12:15], v[236:239]\n"
"s_waitcnt lgkmcnt(3)\n"
"ds_read_b128 a[4:7], v192 offset:33792\n"
"buffer_store_dwordx4 v[64:67], v194, s[24:27], s10 offen sc1\n"
"v_mfma_f32_16x16x32_bf16 v[232:235], v[20:23], a[12:15], v[232:235]\n"
"ds_read_b128 a[8:11], v192 offset:34816\n"
"ds_read_b128 v[64:67], v202 offset:55296\n"
"v_mfma_f32_16x16x32_bf16 v[240:243], v[28:31], a[12:15], v[240:243]\n"
"ds_read_b128 a[12:15], v192 offset:35840\n"
"buffer_store_dwordx2 v[68:69], v195, s[24:27], s10 offen sc1\n"
"ds_read_b64 v[68:69], v62 offset:55296\n"
"s_waitcnt lgkmcnt(7)\n"
"v_mfma_f32_16x16x32_bf16 v[32:35], a[216:219], v[44:47], 0\n"
"s_waitcnt lgkmcnt(6)\n"
"v_cvt_pk_bf16_f32 v232, v232, v233\n"
"v_cvt_pk_bf16_f32 v233, v234, v235\n"
"v_mfma_f32_16x16x32_bf16 v[32:35], a[228:231], v[48:51], v[32:35]\n"
"s_waitcnt lgkmcnt(5)\n"
"v_cvt_pk_bf16_f32 v234, v236, v237\n"
"v_cvt_pk_bf16_f32 v235, v238, v239\n"
"v_mfma_f32_16x16x32_bf16 v[32:35], a[240:243], a[0:3], v[32:35]\n"
"s_waitcnt lgkmcnt(4)\n"
"ds_write_b128 v196, v[232:235]\n"
"v_mfma_f32_16x16x32_bf16 v[32:35], a[252:255], a[4:7], v[32:35]\n"
"s_waitcnt lgkmcnt(3)\n"
"v_cvt_pk_bf16_f32 v240, v240, v241\n"
"v_cvt_pk_bf16_f32 v241, v242, v243\n"
"v_mfma_f32_16x16x32_bf16 v[32:35], v[8:11], a[8:11], v[32:35]\n"
"s_waitcnt lgkmcnt(2)\n"
"buffer_store_dwordx4 v[64:67], v194, s[24:27], s11 offen sc1\n"
"v_mfma_f32_16x16x32_bf16 v[32:35], v[20:23], a[12:15], v[32:35]\n"
"s_waitcnt lgkmcnt(1)\n"
"ds_write_b64 v197, v[240:241]\n"
"v_mfma_f32_16x16x32_bf16 v[40:43], a[224:227], v[44:47], 0\n"
"v_mfma_f32_16x16x32_bf16 v[40:43], a[236:239], v[48:51], v[40:43]\n"
"buffer_store_dwordx2 v[68:69], v195, s[24:27], s11 offen sc1\n"
"v_mfma_f32_16x16x32_bf16 v[40:43], a[248:251], a[0:3], v[40:43]\n"
"v_mfma_f32_16x16x32_bf16 v[40:43], v[4:7], a[4:7], v[40:43]\n"
"s_nop 0\n"
"v_cvt_pk_bf16_f32 v32, v32, v33\n"
"v_mfma_f32_16x16x32_bf16 v[40:43], v[16:19], a[8:11], v[40:43]\n"
"v_cvt_pk_bf16_f32 v33, v34, v35\n"
"v_mfma_f32_16x16x32_bf16 v[40:43], v[28:31], a[12:15], v[40:43]\n"
"v_mfma_f32_16x16x32_bf16 v[36:39], a[220:223], v[44:47], 0\n"
"v_mfma_f32_16x16x32_bf16 v[36:39], a[232:235], v[48:51], v[36:39]\n"
"v_mfma_f32_16x16x32_bf16 v[36:39], a[244:247], a[0:3], v[36:39]\n"
"v_mfma_f32_16x16x32_bf16 v[36:39], v[0:3], a[4:7], v[36:39]\n"
"v_mfma_f32_16x16x32_bf16 v[36:39], v[12:15], a[8:11], v[36:39]\n"
"v_mfma_f32_16x16x32_bf16 v[36:39], v[24:27], a[12:15], v[36:39]\n"
"s_nop 1\n"
"v_cvt_pk_bf16_f32 v40, v40, v41\n"
"v_cvt_pk_bf16_f32 v41, v42, v43\n"
"ds_write_b64 v197, v[40:41] offset:36864\n"
"s_nop 2\n"
"v_cvt_pk_bf16_f32 v34, v36, v37\n"
"v_cvt_pk_bf16_f32 v35, v38, v39\n"
"ds_write_b128 v196, v[32:35] offset:36864\n"
"s_waitcnt vmcnt(5)\n"
"v_max3_u32 v200, v56, v57, v58\n"
"s_waitcnt vmcnt(4)\n"
"v_max3_u32 v200, v200, v59, v60\n"
"v_max_u32 v200, v200, v61\n"
"v_cmp_eq_u32 vcc, -1, v200\n"
"s_nop 4\n"
"s_cbranch_vccnz .Lp13%=\n"
".Lr13%=:\n"
"ds_write_b128 v202, v[56:59] offset:24576\n"
"ds_write_b64 v203, v[60:61] offset:24576\n"
"s_waitcnt lgkmcnt(0)\n"
"s_barrier\n"
"ds_read_b128 a[0:3], v192 offset:24576\n"
"ds_read_b128 a[4:7], v192 offset:25600\n"
"ds_read_b128 a[8:11], v192 offset:26624\n"
"ds_read_b128 a[12:15], v192 offset:27648\n"
"ds_read_b128 v[64:67], v202\n"
"ds_read_b64 v[68:69], v62\n"
"ds_read_b128 v[44:47], v202 offset:36864\n"
"ds_read_b64 v[48:49], v62 offset:36864\n"
"s_waitcnt lgkmcnt(7)\n"
"v_mfma_f32_16x16x32_bf16 v[204:207], a[216:219], a[0:3], 0\n"
"s_waitcnt lgkmcnt(6)\n"
"v_mfma_f32_16x16x32_bf16 v[204:207], a[228:231], a[4:7], v[204:207]\n"
"s_waitcnt lgkmcnt(5)\n"
"v_mfma_f32_16x16x32_bf16 v[204:207], a[240:243], a[8:11], v[204:207]\n"
"s_waitcnt lgkmcnt(4)\n"
"v_mfma_f32_16x16x32_bf16 v[204:207], a[252:255], a[12:15], v[204:207]\n"
"s_waitcnt lgkmcnt(3)\n"
"buffer_store_dwordx4 v[64:67], v194, s[24:27], s20 offen sc1\n"
"v_mfma_f32_16x16x32_bf16 v[212:215], a[224:227], a[0:3], 0\n"
"s_waitcnt lgkmcnt(0)\n"
"ds_read_b128 v[64:67], v192 offset:28672\n"
"v_mfma_f32_16x16x32_bf16 v[212:215], a[236:239], a[4:7], v[212:215]\n"
"v_mfma_f32_16x16x32_bf16 v[212:215], a[248:251], a[8:11], v[212:215]\n"
"buffer_store_dwordx2 v[68:69], v195, s[24:27], s20 offen sc1\n"
"v_mfma_f32_16x16x32_bf16 v[212:215], v[4:7], a[12:15], v[212:215]\n"
"ds_read_b128 v[68:71], v192 offset:29696\n"
"v_mfma_f32_16x16x32_bf16 v[208:211], a[220:223], a[0:3], 0\n"
"v_mfma_f32_16x16x32_bf16 v[208:211], a[232:235], a[4:7], v[208:211]\n"
"v_mfma_f32_16x16x32_bf16 v[208:211], a[244:247], a[8:11], v[208:211]\n"
"buffer_store_dwordx4 v[44:47], v194, s[24:27], s21 offen sc1\n"
"v_mfma_f32_16x16x32_bf16 v[208:211], v[0:3], a[12:15], v[208:211]\n"
"s_waitcnt lgkmcnt(1)\n"
"v_mfma_f32_16x16x32_bf16 v[208:211], v[12:15], v[64:67], v[208:211]\n"
"v_mfma_f32_16x16x32_bf16 v[204:207], v[8:11], v[64:67], v[204:207]\n"
"s_waitcnt lgkmcnt(0)\n"
"v_mfma_f32_16x16x32_bf16 v[204:207], v[20:23], v[68:71], v[204:207]\n"
"buffer_store_dwordx2 v[48:49], v195, s[24:27], s21 offen sc1\n"
"v_mfma_f32_16x16x32_bf16 v[212:215], v[16:19], v[64:67], v[212:215]\n"
"v_mfma_f32_16x16x32_bf16 v[212:215], v[28:31], v[68:71], v[212:215]\n"
"v_mfma_f32_16x16x32_bf16 v[208:211], v[24:27], v[68:71], v[208:211]\n"
"s_nop 3\n"
"v_cvt_pk_bf16_f32 v204, v204, v205\n"
"v_cvt_pk_bf16_f32 v205, v206, v207\n"
"s_nop 0\n"
"v_cvt_pk_bf16_f32 v212, v212, v213\n"
"v_cvt_pk_bf16_f32 v206, v208, v209\n"
"v_cvt_pk_bf16_f32 v207, v210, v211\n"
"v_cvt_pk_bf16_f32 v213, v214, v215\n"
"ds_write_b128 v196, v[204:207] offset:12288\n"
"ds_write_b64 v197, v[212:213] offset:12288\n"
"s_waitcnt lgkmcnt(0)\n"
"s_barrier\n"
"ds_read_b128 v[44:47], v202 offset:12288\n"
"ds_read_b64 v[48:49], v62 offset:12288\n"
"s_waitcnt lgkmcnt(1)\n"
"buffer_store_dwordx4 v[44:47], v194, s[24:27], s22 offen sc1\n"
"s_waitcnt lgkmcnt(0)\n"
"buffer_store_dwordx2 v[48:49], v195, s[24:27], s22 offen sc1\n"
"s_branch .Lend%=\n"
".Lp1%=:\n"
"s_sub_u32 s42, s42, 1\n"
"s_cbranch_scc1 .Lt1%=\n"
"s_sleep 1\n"
"buffer_load_dword v200, off, s[28:31], s39 sc1\n"
"s_waitcnt vmcnt(0)\n"
"v_add3_u32 v200, v200, s50, -2\n"
"v_cmp_lt_u32 vcc, 64, v200\n"
"s_nop 4\n"
"s_cbranch_vccnz .Lp1%=\n"
"s_branch .Lr1%=\n"
".Lt1%=:\n"
"s_mov_b32 s42, 0\n"
"s_branch .Lr1%=\n"
".Lp4%=:\n"
"s_sub_u32 s42, s42, 1\n"
"s_cbranch_scc1 .Lt4%=\n"
"s_sleep 1\n"
"buffer_load_dword v200, off, s[28:31], s39 sc1\n"
"s_waitcnt vmcnt(0)\n"
"v_add3_u32 v200, v200, s50, -5\n"
"v_cmp_lt_u32 vcc, 64, v200\n"
"s_nop 4\n"
"s_cbranch_vccnz .Lp4%=\n"
"s_branch .Lr4%=\n"
".Lt4%=:\n"
"s_mov_b32 s42, 0\n"
"s_branch .Lr4%=\n"
".Lp7%=:\n"
"s_sub_u32 s42, s42, 1\n"
"s_cbranch_scc1 .Lt7%=\n"
"s_sleep 1\n"
"buffer_load_dword v200, off, s[28:31], s39 sc1\n"
"s_waitcnt vmcnt(0)\n"
"v_add3_u32 v200, v200, s50, -8\n"
"v_cmp_lt_u32 vcc, 64, v200\n"
"s_nop 4\n"
"s_cbranch_vccnz .Lp7%=\n"
"s_branch .Lr7%=\n"
".Lt7%=:\n"
"s_mov_b32 s42, 0\n"
"s_branch .Lr7%=\n"
".Lp9%=:\n"
"s_sub_u32 s42, s42, 1\n"
"s_cbranch_scc1 .Lt9%=\n"
"s_sleep 1\n"
"buffer_load_dword v200, off, s[28:31], s39 sc1\n"
"s_waitcnt vmcnt(0)\n"
"v_add3_u32 v200, v200, s50, -10\n"
"v_cmp_lt_u32 vcc, 64, v200\n"
"s_nop 4\n"
"s_cbranch_vccnz .Lp9%=\n"
"s_branch .Lr9%=\n"
".Lt9%=:\n"
"s_mov_b32 s42, 0\n"
"s_branch .Lr9%=\n"
".Lp10%=:\n"
"s_sub_u32 s42, s42, 1\n"
"s_cbranch_scc1 .Lt10%=\n"
"s_sleep 1\n"
"buffer_load_dword v201, off, s[28:31], s39 sc1\n"
"s_waitcnt vmcnt(0)\n"
"v_add3_u32 v201, v201, s50, -11\n"
"v_cmp_lt_u32 vcc, 64, v201\n"
"s_nop 4\n"
"s_cbranch_vccnz .Lp10%=\n"
"s_branch .Lr10%=\n"
".Lt10%=:\n"
"s_mov_b32 s42, 0\n"
"s_branch .Lr10%=\n"
".Lp11%=:\n"
"s_sub_u32 s42, s42, 1\n"
"s_cbranch_scc1 .Lt11%=\n"
"buffer_load_dwordx4 v[52:55], v192, s[28:31], s15 offen sc1\n"
"buffer_load_dwordx2 v[152:153], v193, s[28:31], s15 offen offset:1024 sc1\n"
"s_waitcnt vmcnt(0)\n"
"v_max3_u32 v200, v52, v53, v54\n"
"v_max3_u32 v200, v200, v55, v152\n"
"v_max_u32 v200, v200, v153\n"
"v_cmp_eq_u32 vcc, -1, v200\n"
"s_nop 4\n"
"s_cbranch_vccnz .Lp11%=\n"
"s_branch .Lr11%=\n"
".Lt11%=:\n"
"s_mov_b32 s42, 0\n"
"s_branch .Lr11%=\n"
".Lp12%=:\n"
"s_sub_u32 s42, s42, 1\n"
"s_cbranch_scc1 .Lt12%=\n"
"buffer_load_dwordx4 v[144:147], v192, s[28:31], s17 offen sc1\n"
"buffer_load_dwordx2 v[154:155], v193, s[28:31], s17 offen offset:1024 sc1\n"
"s_waitcnt vmcnt(0)\n"
"v_max3_u32 v200, v144, v145, v146\n"
"v_max3_u32 v200, v200, v147, v154\n"
"v_max_u32 v200, v200, v155\n"
"v_cmp_eq_u32 vcc, -1, v200\n"
"s_nop 4\n"
"s_cbranch_vccnz .Lp12%=\n"
"s_branch .Lr12%=\n"
".Lt12%=:\n"
"s_mov_b32 s42, 0\n"
"s_branch .Lr12%=\n"
".Lp13%=:\n"
"s_sub_u32 s42, s42, 1\n"
"s_cbranch_scc1 .Lt13%=\n"
"buffer_load_dwordx4 v[56:59], v192, s[28:31], s18 offen sc1\n"
"buffer_load_dwordx2 v[60:61], v193, s[28:31], s18 offen offset:1024 sc1\n"
"s_waitcnt vmcnt(0)\n"
"v_max3_u32 v200, v56, v57, v58\n"
"v_max3_u32 v200, v200, v59, v60\n"
"v_max_u32 v200, v200, v61\n"
"v_cmp_eq_u32 vcc, -1, v200\n"
"s_nop 4\n"
"s_cbranch_vccnz .Lp13%=\n"
"s_branch .Lr13%=\n"
".Lt13%=:\n"
"s_mov_b32 s42, 0\n"
"s_branch .Lr13%=\n"
".Lend%=:\n"
      : "+{v31}"(tid), "+{s2}"(bid), "+{s[0:1]}"(ka)
      : "i"((__attribute__((address_space(3))) char*)smem)
      : "memory", "scc", "vcc", VCLOB, ACLOB, SCLOB);
}

extern "C" void kernel_launch(void* const* d_in, const int* in_sizes, int n_in,
                              void* d_out, int out_size, void* d_ws, size_t ws_size,
                              hipStream_t stream) {
  const uint16_t* x = (const uint16_t*)d_in[0];
  const uint16_t* wqkv = (const uint16_t*)d_in[1];
  const uint16_t* wo = (const uint16_t*)d_in[2];
  const uint16_t* rpb = (const uint16_t*)d_in[3];
  dinat_hs<<<256, 256, 0, stream>>>(x, wqkv, wo, rpb, (uint16_t*)d_out, (char*)d_ws + (2 << 20)
  );
}
```

```cpp
#include <hip/hip_runtime.h>
#include <hip/hip_bf16.h>
#include <stdint.h>

#define VCLOB "v0", "v1", "v2", "v3", "v4", "v5", "v6", "v7", "v8", "v9", "v10", "v11", "v12", "v13", "v14", "v15", "v16", "v17", "v18", "v19", "v20", "v21", "v22", "v23", "v24", "v25", "v26", "v27", "v28", "v29", "v30", "v32", "v33", "v34", "v35", "v36", "v37", "v38", "v39", "v40", "v41", "v42", "v43", "v44", "v45", "v46", "v47", "v48", "v49", "v50", "v51", "v52", "v53", "v54", "v55", "v56", "v57", "v58", "v59", "v60", "v61", "v62", "v63", "v64", "v65", "v66", "v67", "v68", "v69", "v70", "v71", "v72", "v73", "v74", "v75", "v76", "v77", "v78", "v79", "v80", "v81", "v82", "v83", "v84", "v85", "v86", "v87", "v88", "v89", "v90", "v91", "v92", "v93", "v94", "v95", "v96", "v97", "v98", "v99", "v100", "v101", "v102", "v103", "v104", "v105", "v106", "v107", "v108", "v109", "v110", "v111", "v112", "v113", "v114", "v115", "v116", "v117", "v118", "v119", "v120", "v121", "v122", "v123", "v124", "v125", "v126", "v127", "v128", "v129", "v130", "v131", "v132", "v133", "v134", "v135", "v136", "v137", "v138", "v139", "v140", "v141", "v142", "v143", "v144", "v145", "v146", "v147", "v148", "v149", "v150", "v151", "v152", "v153", "v154", "v155", "v156", "v157", "v158", "v159", "v160", "v161", "v162", "v163", "v164", "v165", "v166", "v167", "v168", "v169", "v170", "v171", "v172", "v173", "v174", "v175", "v176", "v177", "v178", "v179", "v180", "v181", "v182", "v183", "v184", "v185", "v186", "v187", "v188", "v189", "v190", "v191", "v192", "v193", "v194", "v195", "v196", "v197", "v198", "v199", "v200", "v201", "v202", "v203", "v204", "v205", "v206", "v207", "v208", "v209", "v210", "v211", "v212", "v213", "v214", "v215", "v216", "v217", "v218", "v219", "v220", "v221", "v222", "v223", "v224", "v225", "v226", "v227", "v228", "v229", "v230", "v231", "v232", "v233", "v234", "v235", "v236", "v237", "v238", "v239", "v240", "v241", "v242", "v243", "v244", "v245", "v246", "v247", "v248", "v249", "v250", "v251", "v252", "v253", "v254", "v255"
#define ACLOB "a0", "a1", "a2", "a3", "a4", "a5", "a6", "a7", "a8", "a9", "a10", "a11", "a12", "a13", "a14", "a15", "a16", "a17", "a18", "a19", "a20", "a21", "a22", "a23", "a24", "a25", "a26", "a27", "a28", "a29", "a30", "a31", "a32", "a33", "a34", "a35", "a36", "a37", "a38", "a39", "a40", "a41", "a42", "a43", "a44", "a45", "a46", "a47", "a48", "a49", "a50", "a51", "a52", "a53", "a54", "a55", "a56", "a57", "a58", "a59", "a60", "a61", "a62", "a63", "a64", "a65", "a66", "a67", "a68", "a69", "a70", "a71", "a72", "a73", "a74", "a75", "a76", "a77", "a78", "a79", "a80", "a81", "a82", "a83", "a84", "a85", "a86", "a87", "a88", "a89", "a90", "a91", "a92", "a93", "a94", "a95", "a96", "a97", "a98", "a99", "a100", "a101", "a102", "a103", "a104", "a105", "a106", "a107", "a108", "a109", "a110", "a111", "a112", "a113", "a114", "a115", "a116", "a117", "a118", "a119", "a120", "a121", "a122", "a123", "a124", "a125", "a126", "a127", "a128", "a129", "a130", "a131", "a132", "a133", "a134", "a135", "a136", "a137", "a138", "a139", "a140", "a141", "a142", "a143", "a144", "a145", "a146", "a147", "a148", "a149", "a150", "a151", "a152", "a153", "a154", "a155", "a156", "a157", "a158", "a159", "a160", "a161", "a162", "a163", "a164", "a165", "a166", "a167", "a168", "a169", "a170", "a171", "a172", "a173", "a174", "a175", "a176", "a177", "a178", "a179", "a180", "a181", "a182", "a183", "a184", "a185", "a186", "a187", "a188", "a189", "a190", "a191", "a192", "a193", "a194", "a195", "a196", "a197", "a198", "a199", "a200", "a201", "a202", "a203", "a204", "a205", "a206", "a207", "a208", "a209", "a210", "a211", "a212", "a213", "a214", "a215", "a216", "a217", "a218", "a219", "a220", "a221", "a222", "a223", "a224", "a225", "a226", "a227", "a228", "a229", "a230", "a231", "a232", "a233", "a234", "a235", "a236", "a237", "a238", "a239", "a240", "a241", "a242", "a243", "a244", "a245", "a246", "a247", "a248", "a249", "a250", "a251", "a252", "a253", "a254", "a255"
#define SCLOB "s3", "s4", "s5", "s6", "s7", "s8", "s9", "s10", "s11", "s12", "s13", "s14", "s15", "s16", "s17", "s18", "s19", "s20", "s21", "s22", "s23", "s24", "s25", "s26", "s27", "s28", "s29", "s30", "s31", "s32", "s33", "s34", "s35", "s36", "s37", "s38", "s39", "s40", "s41", "s42", "s43", "s44", "s45", "s46", "s47", "s48", "s49", "s50", "s51", "s52", "s53", "s54", "s55", "s56", "s57", "s58", "s59", "s60", "s61", "s62", "s63", "s64", "s65", "s66", "s67", "s68", "s69", "s70", "s71", "s72", "s73", "s74", "s75", "s76", "s77", "s78", "s79", "s80", "s81", "s82", "s83", "s84", "s85", "s86", "s87", "s88", "s89", "s90", "s91", "s92", "s93", "s94", "s95", "s96", "s97", "s98", "s99", "s100", "s101"
#define LDS_BYTES 163392

__global__ __launch_bounds__(256, 1) __attribute__((aligned(4096))) void dinat_hs(const uint16_t* __restrict__ x, const uint16_t* __restrict__ wqkv,
                                                   const uint16_t* __restrict__ wo, const uint16_t* __restrict__ rpb,
                                                   uint16_t* __restrict__ out, char* __restrict__ ws
                                                   ) {
  __shared__ __attribute__((aligned(16))) char smem[LDS_BYTES];
  unsigned tid = threadIdx.x, bid = blockIdx.x;
  auto ka = __builtin_amdgcn_kernarg_segment_ptr();
  asm volatile(
"s_load_dwordx2 s[8:9], s[0:1], 0\n"
"s_load_dwordx2 s[12:13], s[0:1], 8\n"
"s_load_dwordx2 s[16:17], s[0:1], 16\n"
"s_load_dwordx2 s[20:21], s[0:1], 24\n"
"s_load_dwordx2 s[24:25], s[0:1], 32\n"
"s_load_dwordx2 s[28:29], s[0:1], 40\n"
"s_getpc_b64 s[4:5]\n"
".Lpc%=:\n"
"s_and_b32 s56, s2, 1\n"
"s_mul_i32 s55, s56, 112\n"
"s_bfe_u32 s53, s2, 0x20005\n"
"s_lshr_b32 s54, s2, 7\n"
"s_sub_u32 s55, s55, 1\n"
"s_mul_i32 s55, s55, s54\n"
"s_mul_i32 s71, s56, -56\n"
"s_mul_i32 s59, s53, 27\n"
"v_lshrrev_b32 v113, 6, v31\n"
"s_add_u32 s55, s55, s71\n"
"s_max_u32 s59, s59, 1\n"
"s_nop 0\n"
"v_readfirstlane_b32 s51, v113\n"
"s_add_u32 s55, s55, 56\n"
"s_sub_u32 s59, s59, 1\n"
"v_and_b32 v112, 63, v31\n"
"s_mul_i32 s58, s55, 172032\n"
"s_mul_i32 s72, s59, 768\n"
"s_lshr_b32 s71, s2, 3\n"
"s_add_u32 s58, s58, s72\n"
"s_and_b32 s73, s2, 4\n"
"s_lshl2_add_u32 s71, s71, s51\n"
"v_min_u32 v121, 2, v112\n"
"s_mul_i32 s73, s73, 21504\n"
"s_and_b32 s74, s2, 2\n"
"s_mul_i32 s72, s71, 384\n"
"v_lshlrev_b32 v121, 7, v121\n"
"s_add_u32 s58, s58, s73\n"
"s_mul_i32 s74, s74, 192\n"
"v_add_u32 v121, s72, v121\n"
"s_bfe_u32 s52, s2, 0x20003\n"
"s_mul_i32 s57, s54, -344064\n"
"s_add_u32 s58, s58, s74\n"
"v_lshlrev_b32 v120, 4, v112\n"
"s_mul_i32 s85, s56, 3\n"
"v_min_u32 v121, .Lend%=-.Lpc%=-4, v121\n"
"s_add_u32 s57, s57, 172032\n"
"s_lshl_b32 s61, s51, 3\n"
"v_lshrrev_b32 v114, 3, v112\n"
"v_xor_b32 v115, v120, v112\n"
"s_sub_u32 s86, s85, 3\n"
"s_mov_b32 m0, 163136\n"
"s_mul_i32 s73, s52, 338\n"
"global_load_lds_dword v121, s[4:5]\n"
"s_and_b32 s74, s51, 1\n"
"s_lshr_b32 s84, s2, 5\n"
"v_and_b32 v115, 0x70, v115\n"
"v_add_u32 v114, s61, v114\n"
"s_mul_i32 s86, s86, s57\n"
"s_and_b32 s73, s73, -4\n"
"s_mul_i32 s74, s74, 88\n"
"v_mul_u32_u24 v123, 0x2aab, v112\n"
"v_add_u32 v124, 64, v112\n"
"s_mov_b32 s22, 1352\n"
"s_mov_b32 s23, 0x20000\n"
"s_add_u32 s84, s84, 0\n"
"v_xor_b32 v116, 64, v115\n"
"v_mul_u32_u24 v117, 0x300, v114\n"
"s_add_u32 s86, s86, s58\n"
"v_max_u32 v118, s85, v114\n"
"s_add_u32 s73, s73, s74\n"
"v_lshlrev_b32 v122, 2, v112\n"
"v_lshrrev_b32 v123, 16, v123\n"
"v_mul_u32_u24 v124, 0x2aab, v124\n"
"s_and_b32 s65, s51, 1\n"
"s_mul_i32 s84, s84, 2\n"
"v_add3_u32 v119, v117, s86, v116\n"
"v_lshlrev_b32 v126, 1, v123\n"
"v_lshrrev_b32 v124, 16, v124\n"
"s_lshr_b32 s66, s51, 1\n"
"s_mul_i32 s70, s52, 96\n"
"s_mul_i32 s85, s65, 384\n"
"s_lshr_b32 s75, 0x9249200, s84\n"
"v_and_b32 v126, 24, v126\n"
"v_lshlrev_b32 v127, 1, v124\n"
"s_add_u32 s85, s85, s70\n"
"s_mul_i32 s86, s66, 36864\n"
"s_movk_i32 s69, 1152\n"
"s_and_b32 s75, s75, 0x180\n"
"v_and_or_b32 v126, v123, 3, v126\n"
"v_and_b32 v127, 24, v127\n"
"v_add_u32 v125, 128, v112\n"
"s_add_u32 s85, s85, s86\n"
"v_mad_u32_u24 v126, v126, s69, v120\n"
"v_mul_u32_u24 v27, 96, v123\n"
"v_and_or_b32 v127, v124, 3, v127\n"
"v_lshrrev_b32 v128, 2, v124\n"
"v_mul_u32_u24 v125, 0x2aab, v125\n"
"s_add_u32 s85, s85, 384\n"
"s_mul_i32 s65, s65, 3072\n"
"s_mul_i32 s71, s66, 6752\n"
"s_mov_b32 s14, 221184\n"
"s_mov_b32 s15, 0x20000\n"
"v_sub_u32 v27, v126, v27\n"
"v_and_or_b32 v127, v128, 4, v127\n"
"v_lshrrev_b32 v125, 16, v125\n"
"s_add_u32 s71, s71, s65\n"
"v_mad_u32_u24 v127, v127, s69, v120\n"
"v_mul_u32_u24 v28, 96, v124\n"
"v_lshlrev_b32 v129, 1, v125\n"
"v_sub_u32 v28, v127, v28\n"
"v_and_b32 v129, 24, v129\n"
"v_add_u32 v28, 1024, v28\n"
"v_and_or_b32 v129, v125, 3, v129\n"
"v_mad_u32_u24 v129, v129, s69, v120\n"
"v_mul_u32_u24 v29, 96, v125\n"
"s_sub_u32 s64, 111, s59\n"
"v_sub_u32 v29, v129, v29\n"
"s_min_u32 s64, s64, 32\n"
"v_add_u32 v29, 6656, v29\n"
"s_mul_i32 s64, s64, 768\n"
"v_add_u32 v118, -3, v118\n"
"s_add_u32 s64, s64, s58\n"
"s_mul_i32 s50, s51, 6752\n"
"v_mul_i32_i24 v118, s57, v118\n"
"s_mov_b32 s10, 19267584\n"
"s_mov_b32 s11, 0x20000\n"
"s_add_u32 s50, s50, 27008\n"
"v_add3_u32 v118, v118, s64, v115\n"
"s_lshr_b32 s76, 0x12492480, s84\n"
"s_and_b32 s76, s76, 0x180\n"
"s_lshr_b32 s77, 0x4924900, s84\n"
"s_and_b32 s77, s77, 0x180\n"
"s_lshl_b32 s72, s56, 3\n"
"s_sub_u32 s72, s72, 2\n"
"s_waitcnt lgkmcnt(0)\n"
"s_add_u32 m0, s74, 162768\n"
"s_mul_i32 s72, s72, s57\n"
"buffer_load_dword v122, s[20:23], s73 offen lds\n"
"s_mul_i32 s73, s75, 576\n"
"s_add_u32 s73, s73, s85\n"
"s_add_u32 m0, s71, 0\n"
"s_mul_i32 s34, s51, 12288\n"
"buffer_load_dwordx4 v27, s[12:15], s73 offen lds\n"
"s_add_u32 s72, s72, s58\n"
"s_add_u32 s34, s34, 81024\n"
"v_add3_u32 v130, v117, s72, v115\n"
"s_add_u32 s74, s72, s57\n"
"v_add3_u32 v131, v117, s74, v116\n"
"s_and_b32 s60, s2, 7\n"
"s_add_u32 m0, s71, 1024\n"
"s_lshr_b32 s84, s2, 5\n"
"s_lshl3_add_u32 s60, s60, s84\n"
"s_mul_i32 s84, s76, 576\n"
"s_add_u32 s84, s84, s85\n"
"s_mul_i32 s86, s77, 576\n"
"buffer_load_dwordx4 v28, s[12:15], s73 offen lds\n"
"s_add_u32 s86, s86, s85\n"
"s_mul_i32 s72, s54, 172032\n"
"s_sub_u32 s36, s58, s72\n"
"s_add_u32 s32, s36, s75\n"
"v_add3_u32 v190, v117, s72, v115\n"
"s_sub_u32 s74, 172032, s72\n"
"v_add3_u32 v191, v117, s74, v116\n"
"v_and_b32 v134, 3, v112\n"
"v_lshrrev_b32 v137, 1, v112\n"
"s_add_u32 m0, s71, 2048\n"
"v_and_or_b32 v137, v137, 4, v134\n"
"v_bfe_u32 v136, v112, 2, 1\n"
"v_lshrrev_b32 v140, 1, v137\n"
"v_min_u32 v96, 0xdc, v31\n"
"s_add_u32 s0, s36, s76\n"
"buffer_load_dwordx4 v29, s[12:15], s73 offen lds\n"
"v_lshrrev_b32 v132, 4, v112\n"
"v_lshl_add_u32 v85, v136, 2, v140\n"
"v_mul_u32_u24 v97, 0xf1, v96\n"
"s_mul_i32 s85, s54, -52\n"
"v_xor_b32 v85, v85, v132\n"
"v_lshrrev_b32 v97, 12, v97\n"
"s_sub_u32 s85, s85, 8\n"
"v_lshlrev_b32 v85, 4, v85\n"
"s_add_u32 m0, s50, 0\n"
"v_lshlrev_b32 v30, 3, v96\n"
"v_mul_i32_i24 v97, s85, v97\n"
"v_lshl_add_u32 v85, v137, 7, v85\n"
"v_mul_u32_u24 v25, 0xc00, v136\n"
"v_lshl_add_u32 v96, v96, 1, v97\n"
"buffer_load_dwordx4 v118, s[8:11], s75 offen lds\n"
"v_add_u32 v25, v25, v85\n"
"v_add_u32 v188, s34, v25\n"
"v_xad_u32 v189, v25, 64, s34\n"
"s_add_u32 s1, s36, s77\n"
"s_mul_i32 s35, s51, 6144\n"
"s_lshl_b32 s33, s57, 1\n"
"s_add_u32 s35, s35, 130176\n"
"s_add_u32 m0, s50, 3072\n"
"v_lshrrev_b32 v84, 2, v112\n"
"v_mul_u32_u24 v84, 96, v84\n"
"v_lshl_add_u32 v23, v134, 3, v84\n"
"v_xor_b32 v69, 64, v25\n"
"v_add_u32 v30, 160896, v30\n"
"buffer_load_dwordx4 v119, s[8:11], s75 offen lds\n"
"v_add_u32 v72, 54016, v23\n"
"v_add_u32 v68, s50, v25\n"
"v_add_u32 v69, s50, v69\n"
"s_mul_i32 s3, s65, 12\n"
"s_add_u32 s70, s70, s3\n"
"s_mul_i32 s81, s77, 576\n"
"s_add_u32 s81, s81, s70\n"
"s_add_u32 m0, s50, 1024\n"
"s_sub_u32 s3, s76, s75\n"
"s_mul_i32 s3, s3, s66\n"
"s_add_u32 s3, s3, s75\n"
"s_mul_i32 s41, s56, -60768\n"
"s_mul_i32 s43, s66, 6752\n"
"buffer_load_dwordx4 v118, s[8:11], s76 offen lds\n"
"s_mul_i32 s3, s3, 576\n"
"s_add_u32 s41, s41, 60768\n"
"s_add_u32 s43, s43, s65\n"
"s_add_u32 s3, s3, s70\n"
"s_add_u32 s43, s43, s41\n"
"s_mov_b32 s42, 0x80000\n"
"v_add_u32 v142, s61, v137\n"
"v_bfe_u32 v86, v142, 1, 1\n"
"v_mul_u32_u24 v87, 43, v142\n"
"s_add_u32 m0, s50, 4096\n"
"s_movk_i32 s68, 6752\n"
"v_and_b32 v133, 48, v112\n"
"v_xor_b32 v86, v86, v132\n"
"v_lshrrev_b32 v87, 9, v87\n"
"s_movk_i32 s67, 192\n"
"buffer_load_dwordx4 v119, s[8:11], s76 offen lds\n"
"v_lshlrev_b32 v86, 4, v86\n"
"v_mul_u32_u24 v87, 80832, v87\n"
"v_mad_u32_u24 v21, v142, s68, v133\n"
"v_mad_u32_u24 v141, v142, s67, v86\n"
"v_sub_u32 v21, v21, v87\n"
"v_and_b32 v88, 1, v112\n"
"s_mul_i32 s48, s56, 13504\n"
"v_add_u32 v21, 6144, v21\n"
"s_add_u32 m0, s50, 2048\n"
"v_add_u32 v187, 74272, v141\n"
"v_add_u32 v198, 60768, v141\n"
"v_mad_u32_u24 v88, v88, s68, v133\n"
"s_mov_b32 s6, 0xf0f0f0f0\n"
"s_mov_b32 s7, 0xf0f0f0f0\n"
"buffer_load_dwordx4 v118, s[8:11], s77 offen lds\n"
"s_mov_b64 exec, s[6:7]\n"
"v_add_u32 v21, s48, v141\n"
"v_mov_b32 v187, v141\n"
"v_add_u32 v198, 60544, v88\n"
"s_mov_b64 exec, -1\n"
"s_lshl_b32 s85, s66, 10\n"
"v_add_u32 v192, 99456, v120\n"
"v_bfe_u32 v138, v112, 4, 1\n"
"v_lshrrev_b32 v139, 3, v112\n"
"s_add_u32 m0, s50, 5120\n"
"s_add_u32 s62, s59, s61\n"
"v_and_b32 v139, 4, v139\n"
"v_sub_u32 v94, v136, v138\n"
"s_sub_u32 s63, 108, s62\n"
"s_mul_i32 s49, s56, 272\n"
"buffer_load_dwordx4 v119, s[8:11], s77 offen lds\n"
"v_mul_i32_i24 v94, 0x88, v94\n"
"v_sub_u32 v95, v137, v139\n"
"s_sub_u32 s62, 3, s62\n"
"v_add_u32 v150, 1, v139\n"
"v_add_u32 v151, 2, v139\n"
"v_add_u32 v152, 3, v139\n"
"v_lshl_add_u32 v94, v95, 3, v94\n"
"v_max_i32 v149, s62, v139\n"
"v_max_i32 v150, s62, v150\n"
"s_add_u32 m0, s34, 6144\n"
"v_max_i32 v151, s62, v151\n"
"v_max_i32 v152, s62, v152\n"
"v_mov_b32 v153, 0x7f\n"
"v_min_i32 v149, s63, v149\n"
"v_min_i32 v150, s63, v150\n"
"buffer_load_dwordx4 v130, s[8:11], s75 offen lds\n"
"v_min_i32 v151, s63, v151\n"
"v_min_i32 v152, s63, v152\n"
"v_lshlrev_b32 v149, v149, v153\n"
"v_lshlrev_b32 v150, v150, v153\n"
"v_lshlrev_b32 v151, v151, v153\n"
"v_lshlrev_b32 v152, v152, v153\n"
"s_sub_u32 s74, 3, s61\n"
"s_add_u32 m0, s34, 9216\n"
"v_lshrrev_b32 v149, v137, v149\n"
"v_lshrrev_b32 v150, v137, v150\n"
"v_lshrrev_b32 v151, v137, v151\n"
"v_lshrrev_b32 v152, v137, v152\n"
"s_max_i32 s74, s74, s62\n"
"buffer_load_dwordx4 v131, s[8:11], s75 offen lds\n"
"v_bfe_i32 v98, v149, 0, 1\n"
"v_bfe_i32 v99, v150, 0, 1\n"
"v_bfe_i32 v102, v149, 8, 1\n"
"v_bfe_i32 v103, v150, 8, 1\n"
"v_sub_u32 v61, v136, v138\n"
"s_mov_b32 s72, 0xffff\n"
"v_bfe_i32 v100, v151, 0, 1\n"
"v_bfe_i32 v101, v152, 0, 1\n"
"s_add_u32 m0, s34, 7168\n"
"v_bfe_i32 v56, v151, 8, 1\n"
"v_bfe_i32 v57, v152, 8, 1\n"
"v_max_i32 v58, s74, v137\n"
"v_ashrrev_i32 v149, 31, v61\n"
"v_add_u32 v150, -1, v61\n"
"buffer_load_dwordx4 v130, s[8:11], s76 offen lds\n"
"v_mov_b32 v160, 0x40de\n"
"v_mov_b32 v161, 0\n"
"v_lshlrev_b32 v245, 4, v134\n"
"v_mov_b64 v[156:157], 0\n"
"s_mov_b32 s6, 0x00f0000f\n"
"s_mov_b32 s7, 0xf0000f00\n"
"v_bfi_b32 v0, s72, v98, v99\n"
"v_bfi_b32 v1, s72, v100, v101\n"
"v_bfi_b32 v2, s72, v102, v103\n"
"v_bfi_b32 v3, s72, v56, v57\n"
"s_mul_i32 s74, s51, 1536\n"
"v_and_b32 v251, 15, v112\n"
"v_not_b32 v149, v149\n"
"v_ashrrev_i32 v150, 31, v150\n"
"v_or_b32 v244, s56, v138\n"
"s_mov_b64 exec, s[6:7]\n"
"v_lshlrev_b64 v[156:157], v245, v[160:161]\n"
"s_mov_b64 exec, -1\n"
"s_lshl2_add_u32 s72, s60, s51\n"
"v_mul_u32_u24 v251, 0x180, v251\n"
"v_and_b32 v252, 7, v112\n"
"v_and_b32 v12, v149, v0\n"
"v_and_b32 v16, v150, v0\n"
"v_and_b32 v13, v149, v1\n"
"v_and_b32 v17, v150, v1\n"
"v_and_b32 v14, v149, v2\n"
"v_and_b32 v18, v150, v2\n"
"v_and_b32 v15, v149, v3\n"
"v_and_b32 v19, v150, v3\n"
"v_sub_u32 v244, v136, v244\n"
"v_mov_b32 v160, 0xffff\n"
"v_mov_b64 v[176:177], 0\n"
"s_mul_i32 s37, s72, 6144\n"
"v_and_b32 v249, 1, v112\n"
"v_add_u32 v251, 99456, v251\n"
"v_lshrrev_b32 v254, 5, v112\n"
"v_ashrrev_i32 v149, 31, v244\n"
"v_add_u32 v150, -1, v244\n"
"s_mov_b64 exec, s[6:7]\n"
"v_lshlrev_b64 v[176:177], v245, v[160:161]\n"
"s_mov_b64 exec, -1\n"
"s_lshl_b32 s72, s51, 6\n"
"v_lshlrev_b32 v255, 3, v134\n"
"v_lshl_add_u32 v84, v23, 2, s72\n"
"v_sub_u32 v84, v84, v255\n"
"v_sub_u32 v84, v84, v255\n"
"v_add_u32 v236, 130176, v84\n"
"v_sub_u32 v85, v236, v255\n"
"s_lshl_b32 s73, s48, 2\n"
"v_mad_u32_u24 v186, v136, s68, v141\n"
"s_add_u32 s73, s73, 6752\n"
"v_add_u32 v22, s73, v186\n"
"v_add_u32 v70, s35, v25\n"
"v_xor_b32 v71, 64, v25\n"
"v_add_u32 v71, s35, v71\n"
"s_sub_u32 s73, 28, s61\n"
"s_min_i32 s73, s73, s63\n"
"v_min_i32 v58, s73, v58\n"
"v_lshlrev_b32 v58, v58, -1\n"
"v_lshrrev_b32 v58, v139, v58\n"
"v_not_b32 v58, v58\n"
"v_and_b32 v59, 5, v58\n"
"v_and_b32 v60, 10, v58\n"
"v_lshlrev_b32 v59, 1, v59\n"
"v_lshl_or_b32 v59, v60, 4, v59\n"
"v_or_b32 v59, 0x44, v59\n"
"s_mov_b64 exec, 0\n"
"s_cmp_eq_u32 s51, 0\n"
"s_cbranch_scc1 .Lic0%=\n"
"s_cmp_eq_u32 s51, 1\n"
"s_cbranch_scc1 .Lic1%=\n"
"s_cmp_eq_u32 s51, 2\n"
"s_cbranch_scc1 .Lic2%=\n"
"s_branch .Lic3%=\n"
".Lib0%=:\n"
"s_mov_b64 exec, -1\n"
"v_lshl_or_b32 v63, v59, 8, v59\n"
"s_mov_b32 s30, 23724032\n"
"s_mov_b32 s31, 0x20000\n"
"v_and_b32 v146, 8, v112\n"
"v_lshl_add_u32 v146, v136, 1, v146\n"
"v_add3_u32 v146, v146, s61, 1\n"
"v_lshrrev_b32 v145, 5, v112\n"
"s_add_u32 m0, s34, 10240\n"
"v_max_u32 v144, 3, v142\n"
"buffer_load_dwordx4 v131, s[8:11], s76 offen lds\n"
"v_mul_u32_u24 v145, s68, v145\n"
"v_bfe_u32 v92, v146, 1, 1\n"
"v_add_u32 v144, -3, v144\n"
"v_add_u32 v145, -768, v145\n"
"v_xor_b32 v92, v92, v134\n"
"v_mov_b32 v143, s48\n"
"v_bfe_u32 v90, v144, 1, 1\n"
"v_lshl_add_u32 v92, v92, 4, v145\n"
"v_mad_u32_u24 v143, v136, s68, v143\n"
"v_xor_b32 v90, v90, v132\n"
"v_cmp_eq_u32 s[46:47], 35, v146\n"
"s_add_u32 m0, s34, 8192\n"
"v_mad_u32_u24 v184, v146, s67, v92\n"
"v_lshl_add_u32 v90, v90, 4, v143\n"
"v_add_u32 v146, 1, v146\n"
"buffer_load_dwordx4 v130, s[8:11], s77 offen lds\n"
"v_mad_u32_u24 v178, v144, s67, v90\n"
"v_bfe_u32 v93, v146, 1, 1\n"
"v_add_u32 v144, 5, v142\n"
"v_xor_b32 v93, v93, v134\n"
"v_min_u32 v144, 32, v144\n"
"v_mul_u32_u24 v147, 13504, v138\n"
"v_lshl_add_u32 v93, v93, 4, v145\n"
"v_bfe_u32 v91, v144, 1, 1\n"
"v_add_u32 v147, s48, v147\n"
"s_add_u32 m0, s34, 11264\n"
"v_mul_i32_i24 v148, -67520, v138\n"
"s_waitcnt vmcnt(11)\n"
"s_barrier\n"
"buffer_load_dwordx4 v131, s[8:11], s77 offen lds\n"
"ds_read_b64_tr_b16 a[0:1], v23\n"
"ds_read_b64_tr_b16 a[2:3], v23 offset:1536\n"
"ds_read_b64_tr_b16 a[4:5], v23 offset:32\n"
"ds_read_b64_tr_b16 a[6:7], v23 offset:1568\n"
"ds_read_b64_tr_b16 a[8:9], v23 offset:64\n"
"v_mad_u32_u24 v185, v146, s67, v93\n"
"ds_read_b64_tr_b16 a[10:11], v23 offset:1600\n"
"s_add_u32 m0, s71, 13504\n"
"ds_read_b64_tr_b16 a[72:73], v23 offset:3072\n"
"ds_read_b64_tr_b16 a[74:75], v23 offset:4608\n"
"ds_read_b64_tr_b16 a[76:77], v23 offset:3104\n"
"v_bfe_u32 v135, v112, 2, 2\n"
"ds_read_b64_tr_b16 a[78:79], v23 offset:4640\n"
"buffer_load_dwordx4 v27, s[12:15], s84 offen lds\n"
"ds_read_b64_tr_b16 a[80:81], v23 offset:3136\n"
"ds_read_b64_tr_b16 a[82:83], v23 offset:4672\n"
"ds_read_b64_tr_b16 a[12:13], v23 offset:6752\n"
"v_xor_b32 v91, v91, v132\n"
"ds_read_b64_tr_b16 a[14:15], v23 offset:8288\n"
"ds_read_b64_tr_b16 a[16:17], v23 offset:6784\n"
"ds_read_b64_tr_b16 a[18:19], v23 offset:8320\n"
"s_add_u32 m0, s71, 14528\n"
"s_waitcnt vmcnt(11)\n"
"ds_read_b128 a[144:147], v68\n"
"v_add_u32 v148, 67520, v148\n"
"v_add_u32 v183, v185, v147\n"
"ds_read_b128 a[148:151], v69\n"
"buffer_load_dwordx4 v28, s[12:15], s84 offen lds\n"
"ds_read_b64_tr_b16 a[20:21], v23 offset:6816\n"
"v_add_u32 v185, v185, v148\n"
"ds_read_b64_tr_b16 a[22:23], v23 offset:8352\n"
"ds_read_b64_tr_b16 a[84:85], v23 offset:9824\n"
"ds_read_b64_tr_b16 a[86:87], v23 offset:11360\n"
"s_mov_b64 exec, s[46:47]\n"
"v_mad_u32_u24 v185, v138, s67, v185\n"
"s_mov_b64 exec, -1\n"
"s_add_u32 m0, s71, 15552\n"
"ds_read_b64_tr_b16 a[88:89], v23 offset:9856\n"
"ds_read_b64_tr_b16 a[90:91], v23 offset:11392\n"
"ds_read_b64_tr_b16 a[92:93], v23 offset:9888\n"
"ds_read_b64_tr_b16 a[94:95], v23 offset:11424\n"
"buffer_load_dwordx4 v29, s[12:15], s84 offen lds\n"
"v_lshl_add_u32 v91, v91, 4, v143\n"
"v_mad_u32_u24 v179, v144, s67, v91\n"
"v_cmp_eq_u32 s[44:45], 32, v144\n"
"v_add_u32 v182, v184, v147\n"
"v_not_b32 v149, v149\n"
"v_add_u32 v184, v184, v148\n"
"v_and_b32 v4, v149, v0\n"
"v_and_b32 v5, v149, v1\n"
"v_and_b32 v6, v149, v2\n"
"v_and_b32 v7, v149, v3\n"
"v_ashrrev_i32 v150, 31, v150\n"
"v_and_b32 v8, v150, v0\n"
"v_and_b32 v9, v150, v1\n"
"v_and_b32 v10, v150, v2\n"
"v_and_b32 v11, v150, v3\n"
"v_lshrrev_b32 v87, 2, v112\n"
"v_sub_u32 v88, 11, v112\n"
"v_and_b32 v88, 0x80000000, v88\n"
"v_and_b32 v89, 1, v114\n"
"v_add_u32 v89, 29, v89\n"
"v_mul_i32_i24 v89, s57, v89\n"
"v_lshlrev_b32 v193, 3, v112\n"
"v_add3_u32 v199, v89, s64, v116\n"
"v_add_u32 v193, 99456, v193\n"
"v_mov_b32 v26, 0xf149f149\n"
"v_mov_b64 v[160:161], v[156:157]\n"
"v_add_u32 v180, 40512, v178\n"
"v_add_u32 v181, 40512, v179\n"
"v_mov_b32 v172, 0x3f803f80\n"
"v_mov_b32 v173, v172\n"
"v_mov_b64 v[164:165], v[156:157]\n"
"v_mov_b64 v[168:169], v[156:157]\n"
"v_mov_b64 v[174:175], v[172:173]\n"
"s_waitcnt vmcnt(11) lgkmcnt(9)\n"
"v_mfma_f32_16x16x32_bf16 v[204:207], a[0:3], a[144:147], 0\n"
"s_waitcnt lgkmcnt(8)\n"
"ds_read_b128 a[152:155], v68 offset:1024\n"
"ds_read_b128 a[156:159], v69 offset:1024\n"
"v_mfma_f32_16x16x32_bf16 v[204:207], a[12:15], a[148:151], v[204:207]\n"
"s_add_u32 m0, s71, 60768\n"
"s_add_u32 s84, s49, 161064\n"
"buffer_load_dwordx4 v27, s[12:15], s86 offen lds\n"
"v_mfma_f32_16x16x32_bf16 v[208:211], a[4:7], a[144:147], 0\n"
"s_lshl_b32 s5, s51, 9\n"
"v_add_u32 v20, s84, v94\n"
"s_mov_b32 s70, 0x3e553b95\n"
"v_mfma_f32_16x16x32_bf16 v[208:211], a[16:19], a[148:151], v[208:211]\n"
"s_lshl_b32 s84, s51, 10\n"
"v_add_u32 v202, s84, v192\n"
"s_mul_i32 s20, s33, 3\n"
"v_mfma_f32_16x16x32_bf16 v[212:215], a[8:11], a[144:147], 0\n"
"s_waitcnt lgkmcnt(8)\n"
"s_add_u32 m0, s71, 61792\n"
"s_mul_i32 s21, s33, 5\n"
"v_mfma_f32_16x16x32_bf16 v[212:215], a[20:23], a[148:151], v[212:215]\n"
"s_mov_b32 s84, 0xc0000007\n"
"s_cmp_eq_u32 s53, 0\n"
"s_cselect_b32 s84, 0xe0000000, s84\n"
"s_cmp_eq_u32 s53, 3\n"
"s_cselect_b32 s84, 7, s84\n"
"v_mfma_f32_16x16x32_bf16 v[216:219], a[72:75], a[144:147], 0\n"
"s_waitcnt vmcnt(10) lgkmcnt(6)\n"
"buffer_load_dwordx4 v28, s[12:15], s86 offen lds\n"
"ds_read_b128 a[160:163], v68 offset:2048\n"
"v_mfma_f32_16x16x32_bf16 v[216:219], a[84:87], a[148:151], v[216:219]\n"
"s_add_u32 s5, s5, 4096\n"
"ds_read_b128 a[164:167], v69 offset:2048\n"
"v_add_u32 v68, s41, v23\n"
"v_mfma_f32_16x16x32_bf16 v[220:223], a[76:79], a[144:147], 0\n"
"s_waitcnt lgkmcnt(6)\n"
"v_add_u32 v69, 154752, v23\n"
"v_add_u32 v62, s5, v193\n"
"v_mfma_f32_16x16x32_bf16 v[220:223], a[88:91], a[148:151], v[220:223]\n"
"s_add_u32 m0, s71, 62816\n"
"s_mul_i32 s71, s54, 312\n"
"s_mul_i32 s22, s33, 6\n"
"v_mfma_f32_16x16x32_bf16 v[224:227], a[80:83], a[144:147], 0\n"
"s_waitcnt lgkmcnt(4)\n"
"buffer_load_dwordx4 v29, s[12:15], s86 offen lds\n"
"v_mfma_f32_16x16x32_bf16 v[224:227], a[92:95], a[148:151], v[224:227]\n"
"s_and_b32 s86, s52, 1\n"
"s_lshl1_add_u32 s71, s86, s71\n"
"s_add_u32 s71, s71, 162760\n"
"v_add_u32 v24, s71, v96\n"
"ds_read_u16 v240, v24 offset:6\n"
"ds_read_u16 v241, v24 offset:4\n"
"ds_read_u16 v242, v24 offset:2\n"
"ds_read_u16 v243, v24\n"
"s_and_b32 s86, s51, 1\n"
"s_lshl3_add_u32 s85, s86, s85\n"
"s_add_u32 s85, s85, 4096\n"
"v_add_u32 v203, s85, v192\n"
"s_lshl_b32 s85, s51, 6\n"
"v_add_u32 v81, s85, v112\n"
"v_add_u32 v154, s85, v112\n"
"v_lshrrev_b32 v81, 1, v81\n"
"v_mul_u32_u24 v155, 0xaab, v154\n"
"v_add_u32 v81, 0x100, v81\n"
"v_lshrrev_b32 v155, 16, v155\n"
"v_mul_u32_u24 v82, 0xaab, v81\n"
"v_mul_i32_i24 v80, -24, v155\n"
"v_lshrrev_b32 v82, 16, v82\n"
"v_add_u32 v154, v154, v80\n"
"v_mul_i32_i24 v83, -24, v82\n"
"v_and_b32 v80, 7, v155\n"
"v_add_u32 v81, v81, v83\n"
"v_xor_b32 v154, v154, v80\n"
"v_and_b32 v83, 7, v82\n"
"v_lshrrev_b32 v80, 1, v155\n"
"v_xor_b32 v81, v81, v83\n"
"v_and_b32 v80, 4, v80\n"
"v_lshrrev_b32 v83, 1, v82\n"
"s_lshl_b32 s86, s52, 3\n"
"s_waitcnt lgkmcnt(2)\n"
"v_lshl_or_b32 v240, v241, 16, v240\n"
"v_and_or_b32 v80, v155, 3, v80\n"
"s_lshl2_add_u32 s71, s60, s52\n"
"s_waitcnt lgkmcnt(0)\n"
"v_lshl_or_b32 v241, v243, 16, v242\n"
"ds_write_b64 v30, v[240:241]\n"
"v_and_b32 v83, 4, v83\n"
"v_add_u32 v80, s86, v80\n"
"v_bfe_u32 v247, v155, 2, 1\n"
"s_mul_i32 s38, s71, 6144\n"
"v_and_or_b32 v83, v82, 3, v83\n"
"v_lshrrev_b32 v246, v80, s84\n"
"v_xor_b32 v247, s54, v247\n"
"v_add_u32 v83, s86, v83\n"
"s_lshl_b32 s71, s51, 2\n"
"v_lshlrev_b32 v246, 31, v246\n"
"v_mul_u32_u24 v247, 172032, v247\n"
"v_mul_u32_u24 v80, 0x300, v80\n"
"s_add_u32 s38, s38, s74\n"
"v_lshrrev_b32 v248, v83, s84\n"
"v_bfe_u32 v250, v82, 2, 1\n"
"v_add_u32 v253, s71, v132\n"
"v_add3_u32 v246, v246, v247, v80\n"
"v_lshlrev_b32 v248, 31, v248\n"
"v_xor_b32 v250, s54, v250\n"
"v_xor_b32 v253, v253, v252\n"
"s_lshl1_add_u32 s74, s51, 16\n"
"v_lshl_add_u32 v194, v154, 4, v246\n"
"v_lshl_add_u32 v248, v249, 3, v248\n"
"v_mul_u32_u24 v250, 172032, v250\n"
"v_mul_u32_u24 v83, 0x300, v83\n"
"v_lshl_add_u32 v196, v253, 4, v251\n"
"v_add_u32 v254, s74, v254\n"
"v_add3_u32 v248, v248, v250, v83\n"
"v_lshl_add_u32 v251, v138, 3, v251\n"
"v_xor_b32 v254, v254, v252\n"
"v_lshl_add_u32 v195, v81, 4, v248\n"
"v_lshl_add_u32 v197, v254, 4, v251\n"
"s_add_u32 m0, s34, 0\n"
"s_add_u32 s38, s38, 31616\n"
"buffer_load_dwordx4 v190, s[8:11], s32 offen lds\n"
"s_add_u32 s39, s38, 22119552\n"
"s_and_b32 s85, s52, 1\n"
"s_lshl2_add_u32 s85, s85, s61\n"
"v_add_u32 v86, s85, v134\n"
"v_mul_u32_u24 v86, 0x300, v86\n"
"s_mov_b64 exec, 0\n"
"s_cmp_eq_u32 s51, 0\n"
"s_cbranch_scc1 .Lic4%=\n"
"s_cmp_eq_u32 s51, 1\n"
"s_cbranch_scc1 .Lic5%=\n"
"s_cmp_eq_u32 s51, 2\n"
"s_cbranch_scc1 .Lic6%=\n"
"s_branch .Lic7%=\n"
".Lib1%=:\n"
"s_mov_b64 exec, -1\n"
"s_add_u32 m0, s34, 3072\n"
"s_waitcnt vmcnt(11)\n"
"buffer_load_dwordx4 v191, s[8:11], s32 offen lds\n"
"s_add_u32 s32, s32, s33\n"
"ds_read_b128 a[168:171], v188 offset:6144\n"
"ds_read_b128 a[172:175], v189 offset:6144\n"
"v_lshl_add_u32 v86, v87, 7, v86\n"
"v_or_b32 v238, v88, v86\n"
"s_mov_b32 s71, 0x3e553b95\n"
"s_lshr_b32 s86, s52, 1\n"
"s_mul_i32 s86, s86, 172032\n"
"s_add_u32 s86, s86, s36\n"
"s_mul_i32 s78, s33, 8\n"
"s_add_u32 s78, s78, s86\n"
"s_add_u32 m0, s34, 1024\n"
"s_mul_i32 s73, s52, 1536\n"
"s_add_u32 s37, s37, s73\n"
"s_lshl_b32 s73, s51, 5\n"
"buffer_load_dwordx4 v190, s[8:11], s0 offen lds\n"
"s_sub_u32 s73, 256, s73\n"
"v_add_u32 v235, s73, v85\n"
"s_mov_b32 s18, 73728\n"
"s_add_u32 s37, s37, 31616\n"
"s_mul_i32 s79, s51, 3072\n"
"s_mul_i32 s80, s66, 12288\n"
"s_mov_b32 s26, 19267584\n"
"s_mov_b32 s27, 0x20000\n"
"s_mov_b32 s19, 0x20000\n"
"s_add_u32 s40, s37, 22119552\n"
"s_sub_u32 s16, s16, 99456\n"
"s_subb_u32 s17, s17, 0\n"
"s_add_u32 m0, s34, 4096\n"
"s_waitcnt vmcnt(11) lgkmcnt(1)\n"
"v_mfma_f32_16x16x32_bf16 v[244:247], a[0:3], a[168:171], 0\n"
"s_waitcnt lgkmcnt(0)\n"
"buffer_load_dwordx4 v191, s[8:11], s0 offen lds\n"
"ds_read_b128 a[176:179], v188 offset:7168\n"
"v_mfma_f32_16x16x32_bf16 v[244:247], a[12:15], a[172:175], v[244:247]\n"
"s_add_u32 s0, s0, s33\n"
"ds_read_b128 a[180:183], v189 offset:7168\n"
"v_mfma_f32_16x16x32_bf16 v[248:251], a[4:7], a[168:171], 0\n"
"s_mov_b32 s18, 173184\n"
"s_add_u32 s80, s80, s79\n"
"v_mfma_f32_16x16x32_bf16 v[248:251], a[16:19], a[172:175], v[248:251]\n"
"s_add_u32 m0, s34, 2048\n"
"v_mfma_f32_16x16x32_bf16 v[252:255], a[8:11], a[168:171], 0\n"
"buffer_load_dwordx4 v190, s[8:11], s1 offen lds\n"
"v_mfma_f32_16x16x32_bf16 v[252:255], a[20:23], a[172:175], v[252:255]\n"
"v_mfma_f32_16x16x32_bf16 v[104:107], a[72:75], a[168:171], 0\n"
"v_mfma_f32_16x16x32_bf16 v[104:107], a[84:87], a[172:175], v[104:107]\n"
"s_waitcnt vmcnt(11)\n"
"ds_read_b128 a[184:187], v188 offset:8192\n"
"ds_read_b128 a[188:191], v189 offset:8192\n"
"v_mfma_f32_16x16x32_bf16 v[108:111], a[76:79], a[168:171], 0\n"
"s_add_u32 m0, s34, 5120\n"
"s_nop 0\n"
"buffer_load_dwordx4 v191, s[8:11], s1 offen lds\n"
"v_mfma_f32_16x16x32_bf16 v[108:111], a[88:91], a[172:175], v[108:111]\n"
"s_add_u32 s1, s1, s33\n"
"v_mfma_f32_16x16x32_bf16 v[228:231], a[80:83], a[168:171], 0\n"
"v_mfma_f32_16x16x32_bf16 v[228:231], a[92:95], a[172:175], v[228:231]\n"
"s_add_u32 m0, s35, 0\n"
"s_nop 0\n"
"buffer_load_dwordx4 v190, s[8:11], s32 offen lds\n"
"s_add_u32 m0, s35, 3072\n"
"s_nop 0\n"
"buffer_load_dwordx4 v191, s[8:11], s32 offen lds\n"
"s_add_u32 s32, s32, s33\n"
"s_add_u32 m0, s35, 1024\n"
"s_waitcnt vmcnt(11)\n"
"s_barrier\n"
"ds_read_b64_tr_b16 a[24:25], v23 offset:13504\n"
"buffer_load_dwordx4 v190, s[8:11], s0 offen lds\n"
"ds_read_b64_tr_b16 a[26:27], v23 offset:15040\n"
"ds_read_b64_tr_b16 a[28:29], v23 offset:13536\n"
"ds_read_b64_tr_b16 a[30:31], v23 offset:15072\n"
"ds_read_b64_tr_b16 a[32:33], v23 offset:13568\n"
"ds_read_b64_tr_b16 a[34:35], v23 offset:15104\n"
"ds_read_b64_tr_b16 a[96:97], v23 offset:16576\n"
"ds_read_b64_tr_b16 a[98:99], v23 offset:18112\n"
"ds_read_b64_tr_b16 a[100:101], v23 offset:16608\n"
"ds_read_b64_tr_b16 a[102:103], v23 offset:18144\n"
"s_add_u32 m0, s35, 4096\n"
"ds_read_b64_tr_b16 a[104:105], v23 offset:16640\n"
"buffer_load_dwordx4 v191, s[8:11], s0 offen lds\n"
"ds_read_b64_tr_b16 a[106:107], v23 offset:18176\n"
"ds_read_b64_tr_b16 a[36:37], v23 offset:20256\n"
"s_add_u32 s0, s0, s33\n"
"ds_read_b64_tr_b16 a[38:39], v23 offset:21792\n"
"ds_read_b64_tr_b16 a[40:41], v23 offset:20288\n"
"ds_read_b64_tr_b16 a[42:43], v23 offset:21824\n"
"ds_read_b64_tr_b16 a[44:45], v23 offset:20320\n"
"s_waitcnt lgkmcnt(15)\n"
"v_mfma_f32_16x16x32_bf16 v[204:207], a[24:27], a[152:155], v[204:207]\n"
"s_waitcnt lgkmcnt(13)\n"
"ds_read_b64_tr_b16 a[46:47], v23 offset:21856\n"
"ds_read_b64_tr_b16 a[108:109], v23 offset:23328\n"
"v_mfma_f32_16x16x32_bf16 v[208:211], a[28:31], a[152:155], v[208:211]\n"
"s_waitcnt lgkmcnt(13)\n"
"ds_read_b64_tr_b16 a[110:111], v23 offset:24864\n"
"ds_read_b64_tr_b16 a[112:113], v23 offset:23360\n"
"v_mfma_f32_16x16x32_bf16 v[212:215], a[32:35], a[152:155], v[212:215]\n"
"s_waitcnt lgkmcnt(13)\n"
"ds_read_b64_tr_b16 a[114:115], v23 offset:24896\n"
"ds_read_b64_tr_b16 a[116:117], v23 offset:23392\n"
"v_mfma_f32_16x16x32_bf16 v[216:219], a[96:99], a[152:155], v[216:219]\n"
"s_waitcnt lgkmcnt(13)\n"
"ds_read_b64_tr_b16 a[118:119], v23 offset:24928\n"
"v_mfma_f32_16x16x32_bf16 v[220:223], a[100:103], a[152:155], v[220:223]\n"
"s_waitcnt vmcnt(10) lgkmcnt(12)\n"
"s_add_u32 m0, s35, 2048\n"
"v_mfma_f32_16x16x32_bf16 v[224:227], a[104:107], a[152:155], v[224:227]\n"
"s_waitcnt lgkmcnt(10)\n"
"buffer_load_dwordx4 v190, s[8:11], s1 offen lds\n"
"v_mfma_f32_16x16x32_bf16 v[204:207], a[36:39], a[156:159], v[204:207]\n"
"s_waitcnt lgkmcnt(8)\n"
"s_barrier\n"
"v_mfma_f32_16x16x32_bf16 v[208:211], a[40:43], a[156:159], v[208:211]\n"
"s_waitcnt lgkmcnt(6)\n"
"ds_read_b64_tr_b16 a[48:49], v23 offset:60768\n"
"ds_read_b64_tr_b16 a[50:51], v23 offset:62304\n"
"v_mfma_f32_16x16x32_bf16 v[212:215], a[44:47], a[156:159], v[212:215]\n"
"s_waitcnt lgkmcnt(6)\n"
"ds_read_b64_tr_b16 a[52:53], v23 offset:60800\n"
"ds_read_b64_tr_b16 a[54:55], v23 offset:62336\n"
"v_mfma_f32_16x16x32_bf16 v[216:219], a[108:111], a[156:159], v[216:219]\n"
"s_waitcnt lgkmcnt(6)\n"
"ds_read_b64_tr_b16 a[56:57], v23 offset:60832\n"
"ds_read_b64_tr_b16 a[58:59], v23 offset:62368\n"
"v_mfma_f32_16x16x32_bf16 v[220:223], a[112:115], a[156:159], v[220:223]\n"
"s_waitcnt lgkmcnt(6)\n"
"ds_read_b64_tr_b16 a[120:121], v72 offset:9824\n"
"ds_read_b64_tr_b16 a[122:123], v72 offset:11360\n"
"v_mfma_f32_16x16x32_bf16 v[224:227], a[116:119], a[156:159], v[224:227]\n"
"s_add_u32 m0, s35, 5120\n"
"ds_read_b64_tr_b16 a[124:125], v72 offset:9856\n"
"buffer_load_dwordx4 v191, s[8:11], s1 offen lds\n"
"v_mfma_f32_16x16x32_bf16 v[244:247], a[24:27], a[176:179], v[244:247]\n"
"s_add_u32 s1, s1, s33\n"
"ds_read_b64_tr_b16 a[126:127], v72 offset:11392\n"
"ds_read_b64_tr_b16 a[128:129], v72 offset:9888\n"
"v_mfma_f32_16x16x32_bf16 v[244:247], a[36:39], a[180:183], v[244:247]\n"
"ds_read_b64_tr_b16 a[130:131], v72 offset:11424\n"
"ds_read_b64_tr_b16 a[60:61], v72 offset:13504\n"
"v_mfma_f32_16x16x32_bf16 v[248:251], a[28:31], a[176:179], v[248:251]\n"
"s_waitcnt lgkmcnt(11)\n"
"ds_read_b64_tr_b16 a[62:63], v72 offset:15040\n"
"ds_read_b64_tr_b16 a[64:65], v72 offset:13536\n"
"v_mfma_f32_16x16x32_bf16 v[204:207], a[48:51], a[160:163], v[204:207]\n"
"s_waitcnt lgkmcnt(11)\n"
"ds_read_b64_tr_b16 a[66:67], v72 offset:15072\n"
"ds_read_b64_tr_b16 a[68:69], v72 offset:13568\n"
"v_mfma_f32_16x16x32_bf16 v[208:211], a[52:55], a[160:163], v[208:211]\n"
"s_waitcnt lgkmcnt(11)\n"
"ds_read_b64_tr_b16 a[70:71], v72 offset:15104\n"
"ds_read_b64_tr_b16 a[132:133], v72 offset:16576\n"
"v_mfma_f32_16x16x32_bf16 v[212:215], a[56:59], a[160:163], v[212:215]\n"
"s_waitcnt lgkmcnt(11)\n"
"ds_read_b64_tr_b16 a[134:135], v72 offset:18112\n"
"ds_read_b64_tr_b16 a[136:137], v72 offset:16608\n"
"v_mfma_f32_16x16x32_bf16 v[216:219], a[120:123], a[160:163], v[216:219]\n"
"s_waitcnt lgkmcnt(11)\n"
"ds_read_b64_tr_b16 a[138:139], v72 offset:18144\n"
"ds_read_b64_tr_b16 a[140:141], v72 offset:16640\n"
"v_mfma_f32_16x16x32_bf16 v[252:255], a[32:35], a[176:179], v[252:255]\n"
"s_waitcnt lgkmcnt(11)\n"
"ds_read_b64_tr_b16 a[142:143], v72 offset:18176\n"
"v_mfma_f32_16x16x32_bf16 v[220:223], a[124:127], a[160:163], v[220:223]\n"
"s_waitcnt lgkmcnt(10)\n"
"v_mfma_f32_16x16x32_bf16 v[224:227], a[128:131], a[160:163], v[224:227]\n"
"s_waitcnt lgkmcnt(8)\n"
"v_mfma_f32_16x16x32_bf16 v[204:207], a[60:63], a[164:167], v[204:207]\n"
"s_waitcnt lgkmcnt(6)\n"
"v_mfma_f32_16x16x32_bf16 v[208:211], a[64:67], a[164:167], v[208:211]\n"
"s_waitcnt lgkmcnt(4)\n"
"v_mfma_f32_16x16x32_bf16 v[212:215], a[68:71], a[164:167], v[212:215]\n"
"s_waitcnt lgkmcnt(2)\n"
"v_mfma_f32_16x16x32_bf16 v[216:219], a[132:135], a[164:167], v[216:219]\n"
"s_waitcnt lgkmcnt(0)\n"
"s_nop 0\n"
"v_cvt_pk_bf16_f32 v204, v204, v205\n"
"v_mfma_f32_16x16x32_bf16 v[220:223], a[136:139], a[164:167], v[220:223]\n"
"s_barrier\n"
"v_mfma_f32_16x16x32_bf16 v[224:227], a[140:143], a[164:167], v[224:227]\n"
"v_cvt_pk_bf16_f32 v205, v206, v207\n"
"v_cvt_pk_bf16_f32 v206, v208, v209\n"
"v_mfma_f32_16x16x32_bf16 v[104:107], a[96:99], a[176:179], v[104:107]\n"
"v_cvt_pk_bf16_f32 v207, v210, v211\n"
"v_cvt_pk_bf16_f32 v212, v212, v213\n"
"v_mfma_f32_16x16x32_bf16 v[104:107], a[108:111], a[180:183], v[104:107]\n"
"ds_write_b128 v21, v[204:207]\n"
"v_mfma_f32_16x16x32_bf16 v[104:107], a[120:123], a[184:187], v[104:107]\n"
"v_cvt_pk_bf16_f32 v213, v214, v215\n"
"v_cvt_pk_bf16_f32 v214, v224, v225\n"
"v_mfma_f32_16x16x32_bf16 v[104:107], a[132:135], a[188:191], v[104:107]\n"
"v_cvt_pk_bf16_f32 v215, v226, v227\n"
"v_cvt_pk_bf16_f32 v216, v216, v217\n"
"v_mfma_f32_16x16x32_bf16 v[108:111], a[100:103], a[176:179], v[108:111]\n"
"ds_write_b128 v21, v[212:215] offset:64\n"
"v_mfma_f32_16x16x32_bf16 v[108:111], a[112:115], a[180:183], v[108:111]\n"
"s_add_u32 m0, s34, 6144\n"
"v_cvt_pk_bf16_f32 v217, v218, v219\n"
"v_cvt_pk_bf16_f32 v218, v220, v221\n"
"v_mfma_f32_16x16x32_bf16 v[108:111], a[124:127], a[184:187], v[108:111]\n"
"buffer_load_dwordx4 v190, s[8:11], s32 offen lds\n"
"v_cvt_pk_bf16_f32 v219, v222, v223\n"
"v_mfma_f32_16x16x32_bf16 v[108:111], a[136:139], a[188:191], v[108:111]\n"
"s_waitcnt vmcnt(11)\n"
"ds_write_b128 v21, v[216:219] offset:128\n"
"v_mfma_f32_16x16x32_bf16 v[228:231], a[104:107], a[176:179], v[228:231]\n"
"s_add_u32 m0, s34, 9216\n"
"v_cvt_pk_bf16_f32 v104, v104, v105\n"
"v_cvt_pk_bf16_f32 v105, v106, v107\n"
"v_mfma_f32_16x16x32_bf16 v[228:231], a[116:119], a[180:183], v[228:231]\n"
"s_nop 0\n"
"v_cvt_pk_bf16_f32 v106, v108, v109\n"
"v_mfma_f32_16x16x32_bf16 v[228:231], a[128:131], a[184:187], v[228:231]\n"
"buffer_load_dwordx4 v191, s[8:11], s32 offen lds\n"
"ds_read_b128 v[32:35], v188\n"
"v_mfma_f32_16x16x32_bf16 v[228:231], a[140:143], a[188:191], v[228:231]\n"
"s_add_u32 s32, s32, s33\n"
"ds_read_b128 v[36:39], v189\n"
"v_cvt_pk_bf16_f32 v107, v110, v111\n"
"v_mfma_f32_16x16x32_bf16 v[248:251], a[40:43], a[180:183], v[248:251]\n"
"ds_write_b128 v22, v[104:107] offset:128\n"
"v_mfma_f32_16x16x32_bf16 v[248:251], a[52:55], a[184:187], v[248:251]\n"
"s_add_u32 m0, s34, 7168\n"
"s_nop 0\n"
"buffer_load_dwordx4 v190, s[8:11], s0 offen lds\n"
"v_mfma_f32_16x16x32_bf16 v[248:251], a[64:67], a[188:191], v[248:251]\n"
"s_waitcnt vmcnt(11)\n"
"ds_read_b128 v[40:43], v188 offset:1024\n"
"v_mfma_f32_16x16x32_bf16 v[252:255], a[44:47], a[180:183], v[252:255]\n"
"ds_read_b128 v[44:47], v189 offset:1024\n"
"ds_read_b64 v[80:81], v20 offset:272\n"
"v_mfma_f32_16x16x32_bf16 v[252:255], a[56:59], a[184:187], v[252:255]\n"
"s_waitcnt lgkmcnt(5)\n"
"ds_read_b64 v[86:87], v20 offset:336\n"
"ds_read_b64 v[88:89], v20 offset:544\n"
"v_mfma_f32_16x16x32_bf16 v[252:255], a[68:71], a[188:191], v[252:255]\n"
"s_add_u32 m0, s34, 10240\n"
"ds_read_b64 v[94:95], v20 offset:608\n"
"buffer_load_dwordx4 v191, s[8:11], s0 offen lds\n"
"v_mfma_f32_16x16x32_bf16 v[244:247], a[48:51], a[184:187], v[244:247]\n"
"s_add_u32 s0, s0, s33\n"
"ds_read_b64 v[96:97], v20 offset:816\n"
"ds_read_b64 v[102:103], v20 offset:880\n"
"v_mfma_f32_16x16x32_bf16 v[244:247], a[60:63], a[188:191], v[244:247]\n"
"v_cvt_pk_bf16_f32 v252, v252, v253\n"
"v_cvt_pk_bf16_f32 v253, v254, v255\n"
"v_mfma_f32_16x16x32_bf16 v[204:207], a[0:3], v[32:35], 0\n"
"s_waitcnt lgkmcnt(9)\n"
"v_cvt_pk_bf16_f32 v254, v228, v229\n"
"v_cvt_pk_bf16_f32 v255, v230, v231\n"
"v_mfma_f32_16x16x32_bf16 v[204:207], a[12:15], v[36:39], v[204:207]\n"
"s_waitcnt lgkmcnt(7)\n"
"ds_write_b128 v22, v[252:255] offset:64\n"
"v_mfma_f32_16x16x32_bf16 v[204:207], a[24:27], v[40:43], v[204:207]\n"
"s_waitcnt vmcnt(10) lgkmcnt(5)\n"
"v_cvt_pk_bf16_f32 v244, v244, v245\n"
"v_cvt_pk_bf16_f32 v245, v246, v247\n"
"v_mfma_f32_16x16x32_bf16 v[204:207], a[36:39], v[44:47], v[204:207]\n"
"s_waitcnt lgkmcnt(3)\n"
"v_cvt_pk_bf16_f32 v246, v248, v249\n"
"v_cvt_pk_bf16_f32 v247, v250, v251\n"
"v_mfma_f32_16x16x32_bf16 v[208:211], a[4:7], v[32:35], 0\n"
"s_waitcnt lgkmcnt(1)\n"
"ds_read_b128 v[48:51], v188 offset:2048\n"
"ds_read_b128 v[52:55], v189 offset:2048\n"
"v_mfma_f32_16x16x32_bf16 v[208:211], a[16:19], v[36:39], v[208:211]\n"
"s_add_u32 m0, s34, 8192\n"
"ds_write_b128 v22, v[244:247]\n"
"v_mfma_f32_16x16x32_bf16 v[208:211], a[28:31], v[40:43], v[208:211]\n"
"buffer_load_dwordx4 v190, s[8:11], s1 offen lds\n"
"v_bfi_b32 v80, v4, v80, v26\n"
"v_mfma_f32_16x16x32_bf16 v[208:211], a[40:43], v[44:47], v[208:211]\n"
"v_bfi_b32 v81, v5, v81, v26\n"
"v_bfi_b32 v86, v6, v86, v26\n"
"v_mfma_f32_16x16x32_bf16 v[212:215], a[8:11], v[32:35], 0\n"
"v_bfi_b32 v87, v7, v87, v26\n"
"v_bfi_b32 v88, v0, v88, v26\n"
"v_mfma_f32_16x16x32_bf16 v[212:215], a[20:23], v[36:39], v[212:215]\n"
"s_add_u32 m0, s34, 11264\n"
"v_bfi_b32 v89, v1, v89, v26\n"
"v_bfi_b32 v94, v2, v94, v26\n"
"v_mfma_f32_16x16x32_bf16 v[212:215], a[32:35], v[40:43], v[212:215]\n"
"s_waitcnt vmcnt(9) lgkmcnt(2)\n"
"buffer_load_dwordx4 v191, s[8:11], s1 offen lds\n"
"ds_read_b128 a[216:219], v70\n"
"v_mfma_f32_16x16x32_bf16 v[212:215], a[44:47], v[44:47], v[212:215]\n"
"s_add_u32 s1, s1, s33\n"
"ds_read_b128 a[220:223], v71\n"
"v_bfi_b32 v95, v3, v95, v26\n"
"v_mfma_f32_16x16x32_bf16 v[212:215], a[56:59], v[48:51], v[212:215]\n"
"s_waitcnt lgkmcnt(3)\n"
"v_bfi_b32 v96, v0, v96, v26\n"
"v_bfi_b32 v97, v1, v97, v26\n"
"v_mfma_f32_16x16x32_bf16 v[212:215], a[68:71], v[52:55], v[212:215]\n"
"s_add_u32 m0, s65, 154752\n"
"v_bfi_b32 v102, v2, v102, v26\n"
"v_bfi_b32 v103, v3, v103, v26\n"
"v_mfma_f32_16x16x32_bf16 v[216:219], a[72:75], v[32:35], 0\n"
"buffer_load_dwordx4 v27, s[12:15], s81 offen lds\n"
"v_mfma_f32_16x16x32_bf16 v[216:219], a[84:87], v[36:39], v[216:219]\n"
"v_mfma_f32_16x16x32_bf16 v[216:219], a[96:99], v[40:43], v[216:219]\n"
"s_nop 0\n"
"v_cvt_pk_bf16_f32 v212, v212, v213\n"
"v_mfma_f32_16x16x32_bf16 v[216:219], a[108:111], v[44:47], v[216:219]\n"
"s_waitcnt lgkmcnt(1)\n"
"v_cvt_pk_bf16_f32 v213, v214, v215\n"
"v_mfma_f32_16x16x32_bf16 v[216:219], a[120:123], v[48:51], v[216:219]\n"
"s_add_u32 m0, s65, 155776\n"
"s_nop 0\n"
"buffer_load_dwordx4 v28, s[12:15], s81 offen lds\n"
"v_mfma_f32_16x16x32_bf16 v[216:219], a[132:135], v[52:55], v[216:219]\n"
"v_mfma_f32_16x16x32_bf16 v[220:223], a[76:79], v[32:35], 0\n"
"v_mfma_f32_16x16x32_bf16 v[220:223], a[88:91], v[36:39], v[220:223]\n"
"s_add_u32 m0, s65, 156800\n"
"v_mfma_f32_16x16x32_bf16 v[220:223], a[100:103], v[40:43], v[220:223]\n"
"buffer_load_dwordx4 v29, s[12:15], s81 offen lds\n"
"v_mfma_f32_16x16x32_bf16 v[220:223], a[112:115], v[44:47], v[220:223]\n"
"v_mfma_f32_16x16x32_bf16 v[220:223], a[124:127], v[48:51], v[220:223]\n"
"s_nop 0\n"
"v_cvt_pk_bf16_f32 v216, v216, v217\n"
"v_mfma_f32_16x16x32_bf16 v[220:223], a[136:139], v[52:55], v[220:223]\n"
"v_cvt_pk_bf16_f32 v217, v218, v219\n"
"v_mfma_f32_16x16x32_bf16 v[224:227], a[80:83], v[32:35], 0\n"
"v_mfma_f32_16x16x32_bf16 v[224:227], a[92:95], v[36:39], v[224:227]\n"
"v_mfma_f32_16x16x32_bf16 v[224:227], a[104:107], v[40:43], v[224:227]\n"
"v_mfma_f32_16x16x32_bf16 v[224:227], a[116:119], v[44:47], v[224:227]\n"
"v_mfma_f32_16x16x32_bf16 v[224:227], a[128:131], v[48:51], v[224:227]\n"
"v_mfma_f32_16x16x32_bf16 v[224:227], a[140:143], v[52:55], v[224:227]\n"
"s_nop 0\n"
"v_cvt_pk_bf16_f32 v218, v220, v221\n"
"v_mfma_f32_16x16x32_bf16 v[204:207], a[48:51], v[48:51], v[204:207]\n"
"v_cvt_pk_bf16_f32 v219, v222, v223\n"
"v_mfma_f32_16x16x32_bf16 v[204:207], a[60:63], v[52:55], v[204:207]\n"
"ds_write_b128 v186, v[216:219] offset:20384\n"
"v_mfma_f32_16x16x32_bf16 v[208:211], a[52:55], v[48:51], v[208:211]\n"
"v_mfma_f32_16x16x32_bf16 v[208:211], a[64:67], v[52:55], v[208:211]\n"
"v_cvt_pk_bf16_f32 v214, v224, v225\n"
"v_cvt_pk_bf16_f32 v215, v226, v227\n"
"v_mfma_f32_16x16x32_bf16 v[244:247], a[0:3], a[216:219], 0\n"
"s_waitcnt lgkmcnt(1)\n"
"ds_write_b128 v186, v[212:215] offset:20320\n"
"v_mfma_f32_16x16x32_bf16 v[244:247], a[12:15], a[220:223], v[244:247]\n"
"v_cvt_pk_bf16_f32 v204, v204, v205\n"
"v_cvt_pk_bf16_f32 v205, v206, v207\n"
"v_mfma_f32_16x16x32_bf16 v[248:251], a[4:7], a[216:219], 0\n"
"v_cvt_pk_bf16_f32 v206, v208, v209\n"
"v_cvt_pk_bf16_f32 v207, v210, v211\n"
"v_mfma_f32_16x16x32_bf16 v[248:251], a[16:19], a[220:223], v[248:251]\n"
"ds_write_b128 v186, v[204:207] offset:20256\n"
"v_mfma_f32_16x16x32_bf16 v[252:255], a[8:11], a[216:219], 0\n"
"v_mfma_f32_16x16x32_bf16 v[252:255], a[20:23], a[220:223], v[252:255]\n"
"s_waitcnt vmcnt(11)\n"
"s_add_u32 m0, s43, 0\n"
"ds_read_b128 a[224:227], v70 offset:1024\n"
"v_mfma_f32_16x16x32_bf16 v[104:107], a[72:75], a[216:219], 0\n"
"buffer_load_dwordx4 v27, s[12:15], s3 offen lds\n"
"ds_read_b128 a[228:231], v71 offset:1024\n"
"v_mfma_f32_16x16x32_bf16 v[104:107], a[84:87], a[220:223], v[104:107]\n"
"v_mfma_f32_16x16x32_bf16 v[108:111], a[76:79], a[216:219], 0\n"
"v_mfma_f32_16x16x32_bf16 v[108:111], a[88:91], a[220:223], v[108:111]\n"
"s_add_u32 m0, s43, 1024\n"
"v_mfma_f32_16x16x32_bf16 v[228:231], a[80:83], a[216:219], 0\n"
"buffer_load_dwordx4 v28, s[12:15], s3 offen lds\n"
"v_mfma_f32_16x16x32_bf16 v[228:231], a[92:95], a[220:223], v[228:231]\n"
"s_waitcnt lgkmcnt(1)\n"
"v_mfma_f32_16x16x32_bf16 v[228:231], a[104:107], a[224:227], v[228:231]\n"
"s_waitcnt vmcnt(11) lgkmcnt(0)\n"
"ds_read_b128 a[232:235], v70 offset:2048\n"
"ds_read_b128 a[236:239], v71 offset:2048\n"
"v_mfma_f32_16x16x32_bf16 v[228:231], a[116:119], a[228:231], v[228:231]\n"
"s_add_u32 m0, s43, 2048\n"
"s_add_u32 s43, s79, 0\n"
"buffer_load_dwordx4 v29, s[12:15], s3 offen lds\n"
"v_mfma_f32_16x16x32_bf16 v[244:247], a[24:27], a[224:227], v[244:247]\n"
"v_mfma_f32_16x16x32_bf16 v[244:247], a[36:39], a[228:231], v[244:247]\n"
"v_mfma_f32_16x16x32_bf16 v[248:251], a[28:31], a[224:227], v[248:251]\n"
"s_add_u32 m0, s34, 0\n"
"v_mfma_f32_16x16x32_bf16 v[248:251], a[40:43], a[228:231], v[248:251]\n"
"buffer_load_dwordx4 v190, s[8:11], s32 offen lds\n"
"v_mfma_f32_16x16x32_bf16 v[252:255], a[32:35], a[224:227], v[252:255]\n"
"v_mfma_f32_16x16x32_bf16 v[252:255], a[44:47], a[228:231], v[252:255]\n"
"s_waitcnt vmcnt(11) lgkmcnt(1)\n"
"v_mfma_f32_16x16x32_bf16 v[252:255], a[56:59], a[232:235], v[252:255]\n"
"s_waitcnt lgkmcnt(0)\n"
"ds_read_b128 a[240:243], v188 offset:6144\n"
"ds_read_b128 a[244:247], v189 offset:6144\n"
"v_mfma_f32_16x16x32_bf16 v[252:255], a[68:71], a[236:239], v[252:255]\n"
"s_add_u32 m0, s34, 3072\n"
"s_nop 0\n"
"buffer_load_dwordx4 v191, s[8:11], s32 offen lds\n"
"v_mfma_f32_16x16x32_bf16 v[104:107], a[96:99], a[224:227], v[104:107]\n"
"s_add_u32 s32, s32, s33\n"
"v_mfma_f32_16x16x32_bf16 v[104:107], a[108:111], a[228:231], v[104:107]\n"
"v_mfma_f32_16x16x32_bf16 v[104:107], a[120:123], a[232:235], v[104:107]\n"
"s_add_u32 m0, s34, 1024\n"
"v_mfma_f32_16x16x32_bf16 v[104:107], a[132:135], a[236:239], v[104:107]\n"
"v_cvt_pk_bf16_f32 v252, v252, v253\n"
"v_cvt_pk_bf16_f32 v253, v254, v255\n"
"v_mfma_f32_16x16x32_bf16 v[108:111], a[100:103], a[224:227], v[108:111]\n"
"buffer_load_dwordx4 v190, s[8:11], s0 offen lds\n"
"v_mfma_f32_16x16x32_bf16 v[108:111], a[112:115], a[228:231], v[108:111]\n"
"s_waitcnt lgkmcnt(1)\n"
"v_mfma_f32_16x16x32_bf16 v[108:111], a[124:127], a[232:235], v[108:111]\n"
"s_nop 0\n"
"v_cvt_pk_bf16_f32 v104, v104, v105\n"
"v_mfma_f32_16x16x32_bf16 v[108:111], a[136:139], a[236:239], v[108:111]\n"
"s_waitcnt vmcnt(11)\n"
"v_cvt_pk_bf16_f32 v105, v106, v107\n"
"s_add_u32 m0, s34, 4096\n"
"v_mfma_f32_16x16x32_bf16 v[244:247], a[48:51], a[232:235], v[244:247]\n"
"buffer_load_dwordx4 v191, s[8:11], s0 offen lds\n"
"ds_read_b128 a[248:251], v188 offset:7168\n"
"v_mfma_f32_16x16x32_bf16 v[244:247], a[60:63], a[236:239], v[244:247]\n"
"s_add_u32 s0, s0, s33\n"
"ds_read_b128 a[252:255], v189 offset:7168\n"
"v_cvt_pk_bf16_f32 v106, v108, v109\n"
"v_mfma_f32_16x16x32_bf16 v[248:251], a[52:55], a[232:235], v[248:251]\n"
"v_cvt_pk_bf16_f32 v107, v110, v111\n"
"v_mfma_f32_16x16x32_bf16 v[248:251], a[64:67], a[236:239], v[248:251]\n"
"s_add_u32 m0, s34, 2048\n"
"ds_write_b128 v186, v[104:107] offset:33888\n"
"v_mfma_f32_16x16x32_bf16 v[228:231], a[128:131], a[232:235], v[228:231]\n"
"buffer_load_dwordx4 v190, s[8:11], s1 offen lds\n"
"v_cvt_pk_bf16_f32 v244, v244, v245\n"
"v_mfma_f32_16x16x32_bf16 v[228:231], a[140:143], a[236:239], v[228:231]\n"
"v_cvt_pk_bf16_f32 v245, v246, v247\n"
"v_mfma_f32_16x16x32_bf16 v[120:123], a[0:3], a[240:243], 0\n"
"s_waitcnt vmcnt(11) lgkmcnt(3)\n"
"v_cvt_pk_bf16_f32 v246, v248, v249\n"
"v_cvt_pk_bf16_f32 v247, v250, v251\n"
"v_mfma_f32_16x16x32_bf16 v[120:123], a[12:15], a[244:247], v[120:123]\n"
"s_waitcnt lgkmcnt(2)\n"
"ds_read_b128 v[56:59], v188 offset:8192\n"
"ds_read_b128 v[64:67], v189 offset:8192\n"
"v_mfma_f32_16x16x32_bf16 v[120:123], a[24:27], a[248:251], v[120:123]\n"
"s_waitcnt lgkmcnt(3)\n"
"ds_write_b128 v186, v[244:247] offset:33760\n"
"v_mfma_f32_16x16x32_bf16 v[120:123], a[36:39], a[252:255], v[120:123]\n"
"s_add_u32 m0, s34, 5120\n"
"v_cvt_pk_bf16_f32 v254, v228, v229\n"
"v_cvt_pk_bf16_f32 v255, v230, v231\n"
"v_mfma_f32_16x16x32_bf16 v[124:127], a[4:7], a[240:243], 0\n"
"buffer_load_dwordx4 v191, s[8:11], s1 offen lds\n"
"ds_write_b128 v186, v[252:255] offset:33824\n"
"v_mfma_f32_16x16x32_bf16 v[124:127], a[16:19], a[244:247], v[124:127]\n"
"s_add_u32 s1, s1, s33\n"
"v_mov_b64 v[252:253], 0\n"
"v_mov_b64 v[254:255], 0\n"
"v_mfma_f32_16x16x32_bf16 v[124:127], a[28:31], a[248:251], v[124:127]\n"
"s_waitcnt lgkmcnt(3)\n"
"s_add_u32 m0, s34, 6144\n"
"ds_read_b64 v[104:105], v20 offset:1088\n"
"v_mfma_f32_16x16x32_bf16 v[124:127], a[40:43], a[252:255], v[124:127]\n"
"buffer_load_dwordx4 v190, s[8:11], s32 offen lds\n"
"ds_read_b64 v[110:111], v20 offset:1152\n"
"v_mfma_f32_16x16x32_bf16 v[124:127], a[52:55], v[56:59], v[124:127]\n"
"s_waitcnt lgkmcnt(4)\n"
"v_subrev_u32 v20, s49, v20\n"
"v_mfma_f32_16x16x32_bf16 v[124:127], a[64:67], v[64:67], v[124:127]\n"
"v_mfma_f32_16x16x32_bf16 v[128:131], a[8:11], a[240:243], 0\n"
"v_mfma_f32_16x16x32_bf16 v[128:131], a[20:23], a[244:247], v[128:131]\n"
"v_mfma_f32_16x16x32_bf16 v[128:131], a[32:35], a[248:251], v[128:131]\n"
"v_mfma_f32_16x16x32_bf16 v[128:131], a[44:47], a[252:255], v[128:131]\n"
"s_waitcnt lgkmcnt(1)\n"
"v_bfi_b32 v104, v8, v104, v26\n"
"v_bfi_b32 v105, v9, v105, v26\n"
"v_mfma_f32_16x16x32_bf16 v[128:131], a[56:59], v[56:59], v[128:131]\n"
"s_waitcnt lgkmcnt(0)\n"
"v_bfi_b32 v110, v10, v110, v26\n"
"v_bfi_b32 v111, v11, v111, v26\n"
"v_mfma_f32_16x16x32_bf16 v[128:131], a[68:71], v[64:67], v[128:131]\n"
"v_mfma_f32_16x16x32_bf16 v[132:135], a[72:75], a[240:243], 0\n"
"v_mfma_f32_16x16x32_bf16 v[132:135], a[84:87], a[244:247], v[132:135]\n"
"v_mfma_f32_16x16x32_bf16 v[132:135], a[96:99], a[248:251], v[132:135]\n"
"v_mfma_f32_16x16x32_bf16 v[132:135], a[108:111], a[252:255], v[132:135]\n"
"v_mfma_f32_16x16x32_bf16 v[132:135], a[120:123], v[56:59], v[132:135]\n"
"v_mfma_f32_16x16x32_bf16 v[132:135], a[132:135], v[64:67], v[132:135]\n"
"v_mfma_f32_16x16x32_bf16 v[136:139], a[76:79], a[240:243], 0\n"
"s_nop 0\n"
"v_cvt_pk_bf16_f32 v128, v128, v129\n"
"v_mfma_f32_16x16x32_bf16 v[136:139], a[88:91], a[244:247], v[136:139]\n"
"v_cvt_pk_bf16_f32 v129, v130, v131\n"
"v_mfma_f32_16x16x32_bf16 v[136:139], a[100:103], a[248:251], v[136:139]\n"
"v_mfma_f32_16x16x32_bf16 v[136:139], a[112:115], a[252:255], v[136:139]\n"
"s_nop 0\n"
"v_cvt_pk_bf16_f32 v132, v132, v133\n"
"v_mfma_f32_16x16x32_bf16 v[136:139], a[124:127], v[56:59], v[136:139]\n"
"v_cvt_pk_bf16_f32 v133, v134, v135\n"
"v_mfma_f32_16x16x32_bf16 v[136:139], a[136:139], v[64:67], v[136:139]\n"
"s_add_u32 m0, s34, 9216\n"
"v_mfma_f32_16x16x32_bf16 v[140:143], a[80:83], a[240:243], 0\n"
"buffer_load_dwordx4 v191, s[8:11], s32 offen lds\n"
"s_add_u32 s32, s32, s33\n"
"v_mfma_f32_16x16x32_bf16 v[140:143], a[92:95], a[244:247], v[140:143]\n"
"v_mfma_f32_16x16x32_bf16 v[140:143], a[104:107], a[248:251], v[140:143]\n"
"v_mfma_f32_16x16x32_bf16 v[140:143], a[116:119], a[252:255], v[140:143]\n"
"s_nop 0\n"
"v_cvt_pk_bf16_f32 v134, v136, v137\n"
"v_mfma_f32_16x16x32_bf16 v[140:143], a[128:131], v[56:59], v[140:143]\n"
"s_add_u32 m0, s34, 7168\n"
"v_cvt_pk_bf16_f32 v135, v138, v139\n"
"v_mfma_f32_16x16x32_bf16 v[140:143], a[140:143], v[64:67], v[140:143]\n"
"buffer_load_dwordx4 v190, s[8:11], s0 offen lds\n"
"ds_write_b128 v186, v[132:135] offset:47392\n"
"v_mfma_f32_16x16x32_bf16 v[120:123], a[48:51], v[56:59], v[120:123]\n"
"v_mfma_f32_16x16x32_bf16 v[120:123], a[60:63], v[64:67], v[120:123]\n"
"s_add_u32 m0, s34, 10240\n"
"s_nop 2\n"
"v_cvt_pk_bf16_f32 v130, v140, v141\n"
"buffer_load_dwordx4 v191, s[8:11], s0 offen lds\n"
"s_add_u32 s0, s0, s33\n"
"s_nop 0\n"
"v_cvt_pk_bf16_f32 v120, v120, v121\n"
"v_cvt_pk_bf16_f32 v121, v122, v123\n"
"v_cvt_pk_bf16_f32 v122, v124, v125\n"
"v_cvt_pk_bf16_f32 v123, v126, v127\n"
"v_cvt_pk_bf16_f32 v131, v142, v143\n"
"ds_write_b128 v186, v[120:123] offset:47264\n"
"s_add_u32 m0, s34, 8192\n"
"s_nop 0\n"
"buffer_load_dwordx4 v190, s[8:11], s1 offen lds\n"
"ds_write_b128 v186, v[128:131] offset:47328\n"
"s_add_u32 m0, s34, 11264\n"
"s_waitcnt vmcnt(11)\n"
"s_barrier\n"
"buffer_load_dwordx4 v191, s[8:11], s1 offen lds\n"
"s_add_u32 s1, s1, s33\n"
"ds_read_b64_tr_b16 a[144:145], v68\n"
"ds_read_b64_tr_b16 a[146:147], v68 offset:1536\n"
"ds_read_b64_tr_b16 a[148:149], v68 offset:32\n"
"ds_read_b64_tr_b16 a[150:151], v68 offset:1568\n"
"ds_read_b64_tr_b16 a[152:153], v68 offset:64\n"
"ds_read_b64_tr_b16 a[154:155], v68 offset:1600\n"
"ds_read_b64_tr_b16 a[156:157], v68 offset:3072\n"
"ds_read_b64_tr_b16 a[158:159], v68 offset:4608\n"
"ds_read_b64_tr_b16 a[160:161], v68 offset:3104\n"
"ds_read_b64_tr_b16 a[162:163], v68 offset:4640\n"
"buffer_load_dword v200, off, s[28:31], s39 sc1\n"
"ds_read_b64_tr_b16 a[164:165], v68 offset:3136\n"
"ds_read_b64_tr_b16 a[166:167], v68 offset:4672\n"
"ds_read_b64_tr_b16 a[168:169], v68 offset:6752\n"
"ds_read_b64_tr_b16 a[170:171], v68 offset:8288\n"
"ds_read_b64_tr_b16 a[172:173], v68 offset:6784\n"
"ds_read_b64_tr_b16 a[174:175], v68 offset:8320\n"
"ds_read_b64_tr_b16 a[176:177], v68 offset:6816\n"
"s_waitcnt lgkmcnt(15)\n"
"v_mfma_f32_16x16x32_bf16 v[204:207], a[144:147], v[32:35], 0\n"
"s_waitcnt lgkmcnt(13)\n"
"ds_read_b64_tr_b16 a[178:179], v68 offset:8352\n"
"ds_read_b64_tr_b16 a[180:181], v68 offset:9824\n"
"v_mfma_f32_16x16x32_bf16 v[208:211], a[148:151], v[32:35], 0\n"
"s_waitcnt lgkmcnt(13)\n"
"ds_read_b64_tr_b16 a[182:183], v68 offset:11360\n"
"ds_read_b64_tr_b16 a[184:185], v68 offset:9856\n"
"v_mfma_f32_16x16x32_bf16 v[212:215], a[152:155], v[32:35], 0\n"
"s_waitcnt lgkmcnt(13)\n"
"ds_read_b64_tr_b16 a[186:187], v68 offset:11392\n"
"ds_read_b64_tr_b16 a[188:189], v68 offset:9888\n"
"v_mfma_f32_16x16x32_bf16 v[204:207], a[156:159], v[36:39], v[204:207]\n"
"s_waitcnt lgkmcnt(13)\n"
"ds_read_b64_tr_b16 a[190:191], v68 offset:11424\n"
"ds_read_b64_tr_b16 a[192:193], v69\n"
"v_mfma_f32_16x16x32_bf16 v[208:211], a[160:163], v[36:39], v[208:211]\n"
"s_waitcnt lgkmcnt(13)\n"
"ds_read_b64_tr_b16 a[194:195], v69 offset:1536\n"
"ds_read_b64_tr_b16 a[196:197], v69 offset:32\n"
"v_mfma_f32_16x16x32_bf16 v[212:215], a[164:167], v[36:39], v[212:215]\n"
"s_waitcnt lgkmcnt(13)\n"
"ds_read_b64_tr_b16 a[198:199], v69 offset:1568\n"
"ds_read_b64_tr_b16 a[200:201], v69 offset:64\n"
"v_mfma_f32_16x16x32_bf16 v[204:207], a[168:171], v[40:43], v[204:207]\n"
"s_waitcnt lgkmcnt(13)\n"
"ds_read_b64_tr_b16 a[202:203], v69 offset:1600\n"
"ds_read_b64_tr_b16 a[204:205], v69 offset:3072\n"
"v_mfma_f32_16x16x32_bf16 v[208:211], a[172:175], v[40:43], v[208:211]\n"
"s_waitcnt lgkmcnt(13)\n"
"ds_read_b64_tr_b16 a[206:207], v69 offset:4608\n"
"ds_read_b64_tr_b16 a[208:209], v69 offset:3104\n"
"v_mfma_f32_16x16x32_bf16 v[212:215], a[176:179], v[40:43], v[212:215]\n"
"s_waitcnt lgkmcnt(13)\n"
"ds_read_b64_tr_b16 a[210:211], v69 offset:4640\n"
"ds_read_b64_tr_b16 a[212:213], v69 offset:3136\n"
"v_mfma_f32_16x16x32_bf16 v[204:207], a[180:183], v[44:47], v[204:207]\n"
"s_waitcnt lgkmcnt(13)\n"
"ds_read_b64_tr_b16 a[214:215], v69 offset:4672\n"
"v_mfma_f32_16x16x32_bf16 v[208:211], a[184:187], v[44:47], v[208:211]\n"
"s_waitcnt lgkmcnt(12)\n"
"v_mfma_f32_16x16x32_bf16 v[212:215], a[188:191], v[44:47], v[212:215]\n"
"s_waitcnt lgkmcnt(10)\n"
"v_mfma_f32_16x16x32_bf16 v[204:207], a[192:195], v[48:51], v[204:207]\n"
"s_waitcnt lgkmcnt(8)\n"
"v_mfma_f32_16x16x32_bf16 v[208:211], a[196:199], v[48:51], v[208:211]\n"
"s_waitcnt lgkmcnt(6)\n"
"v_mfma_f32_16x16x32_bf16 v[212:215], a[200:203], v[48:51], v[212:215]\n"
"s_waitcnt lgkmcnt(4)\n"
"v_mfma_f32_16x16x32_bf16 v[204:207], a[204:207], v[52:55], v[204:207]\n"
"s_waitcnt lgkmcnt(2)\n"
"v_mfma_f32_16x16x32_bf16 v[208:211], a[208:211], v[52:55], v[208:211]\n"
"s_waitcnt lgkmcnt(0)\n"
"s_barrier\n"
"v_mfma_f32_16x16x32_bf16 v[212:215], a[212:215], v[52:55], v[212:215]\n"
"ds_read_b128 v[112:115], v178\n"
"ds_read_b64 v[82:83], v178 offset:64\n"
"s_nop 0\n"
"v_cvt_pk_bf16_f32 v144, v204, v205\n"
"ds_read_b128 v[120:123], v179\n"
"v_cvt_pk_bf16_f32 v145, v206, v207\n"
"v_mfma_f32_16x16x32_bf16 v[204:207], a[144:147], a[216:219], 0\n"
"ds_read_b64 v[84:85], v179 offset:64\n"
"buffer_load_dword v237, v238, s[8:11], s78 offen\n"
"v_mfma_f32_16x16x32_bf16 v[204:207], a[156:159], a[220:223], v[204:207]\n"
"s_add_u32 s78, s78, s33\n"
"ds_read_b128 v[116:119], v178 offset:13504\n"
"v_cvt_pk_bf16_f32 v146, v208, v209\n"
"v_mfma_f32_16x16x32_bf16 v[204:207], a[168:171], a[224:227], v[204:207]\n"
"s_waitcnt vmcnt(12)\n"
"ds_read_b64 v[90:91], v178 offset:13568\n"
"v_cvt_pk_bf16_f32 v147, v210, v211\n"
"v_mfma_f32_16x16x32_bf16 v[204:207], a[180:183], a[228:231], v[204:207]\n"
".Lic0%=:\n"
"s_cbranch_execz .Lib0%=\n"
"ds_read_b128 v[32:35], v188\n"
"v_cvt_pk_bf16_f32 v158, v212, v213\n"
"v_mfma_f32_16x16x32_bf16 v[204:207], a[192:195], a[232:235], v[204:207]\n"
"ds_read_b128 v[36:39], v189\n"
"v_cvt_pk_bf16_f32 v159, v214, v215\n"
"v_mfma_f32_16x16x32_bf16 v[204:207], a[204:207], a[236:239], v[204:207]\n"
"s_waitcnt lgkmcnt(6)\n"
"ds_read_b128 v[124:127], v179 offset:13504\n"
"buffer_load_dword v237, v238, s[8:11], s78 offen\n"
"v_mfma_f32_16x16x32_bf16 v[208:211], a[148:151], a[216:219], 0\n"
"s_add_u32 s78, s78, s33\n"
"ds_read_b64 v[92:93], v179 offset:13568\n"
"v_mfma_f32_16x16x32_bf16 v[112:115], v[112:115], v[144:147], 0\n"
"s_waitcnt lgkmcnt(6)\n"
"ds_read_b128 v[128:131], v178 offset:27008\n"
"v_cvt_pk_bf16_f32 v148, v204, v205\n"
"v_mfma_f32_16x16x32_bf16 v[112:115], v[80:83], v[156:159], v[112:115]\n"
"ds_read_b64 v[98:99], v178 offset:27072\n"
"v_cvt_pk_bf16_f32 v149, v206, v207\n"
"v_mfma_f32_16x16x32_bf16 v[120:123], v[120:123], v[144:147], 0\n"
"s_waitcnt lgkmcnt(6)\n"
"ds_read_b128 v[136:139], v179 offset:27008\n"
"v_subrev_u32 v178, s48, v178\n"
"v_mfma_f32_16x16x32_bf16 v[120:123], v[84:87], v[158:161], v[120:123]\n"
"ds_read_b64 v[100:101], v179 offset:27072\n"
"v_subrev_u32 v179, s48, v179\n"
"v_mfma_f32_16x16x32_bf16 v[116:119], v[116:119], v[144:147], 0\n"
"s_waitcnt vmcnt(11) lgkmcnt(6)\n"
"ds_read_b128 v[40:43], v188 offset:1024\n"
"v_mfma_f32_16x16x32_bf16 v[116:119], v[88:91], v[156:159], v[116:119]\n"
"s_add_u32 m0, s34, 0\n"
"ds_read_b128 v[44:47], v189 offset:1024\n"
"buffer_load_dwordx4 v190, s[8:11], s32 offen lds\n"
"v_mfma_f32_16x16x32_bf16 v[204:207], a[144:147], a[240:243], 0\n"
"s_waitcnt lgkmcnt(6)\n"
"ds_read_b128 v[132:135], v180\n"
"v_max_f32 v112, v112, v120\n"
"v_mfma_f32_16x16x32_bf16 v[204:207], a[156:159], a[244:247], v[204:207]\n"
"ds_read_b64 v[106:107], v180 offset:64\n"
"v_subrev_u32 v180, s48, v180\n"
"v_mfma_f32_16x16x32_bf16 v[124:127], v[124:127], v[144:147], 0\n"
"s_waitcnt lgkmcnt(6)\n"
"ds_read_b128 v[140:143], v181\n"
"v_max_f32 v113, v113, v121\n"
"v_mfma_f32_16x16x32_bf16 v[124:127], v[92:95], v[158:161], v[124:127]\n"
"ds_read_b64 v[108:109], v181 offset:64\n"
"v_subrev_u32 v181, s48, v181\n"
"v_mfma_f32_16x16x32_bf16 v[128:131], v[128:131], v[144:147], 0\n"
"s_waitcnt lgkmcnt(6)\n"
"ds_read_b64_tr_b16 v[224:225], v182 offset:128\n"
"v_max_f32 v114, v114, v122\n"
"v_mfma_f32_16x16x32_bf16 v[128:131], v[96:99], v[156:159], v[128:131]\n"
"ds_read_b64_tr_b16 v[226:227], v183 offset:128\n"
"v_max_f32 v115, v115, v123\n"
"v_mfma_f32_16x16x32_bf16 v[136:139], v[136:139], v[144:147], 0\n"
"ds_read_b64_tr_b16 v[228:229], v182 offset:896\n"
"v_max3_f32 v170, v112, v113, v114\n"
"v_mfma_f32_16x16x32_bf16 v[136:139], v[100:103], v[158:161], v[136:139]\n"
"s_waitcnt lgkmcnt(7)\n"
"ds_read_b64_tr_b16 v[230:231], v183 offset:896\n"
"v_max_f32 v116, v116, v124\n"
"v_mfma_f32_16x16x32_bf16 v[204:207], a[168:171], a[248:251], v[204:207]\n"
"s_waitcnt lgkmcnt(6)\n"
"ds_read_b64_tr_b16 v[244:245], v182 offset:136\n"
"v_max_f32 v117, v117, v125\n"
"v_mfma_f32_16x16x32_bf16 v[204:207], a[180:183], a[252:255], v[204:207]\n"
"s_waitcnt vmcnt(10)\n"
"ds_read_b64_tr_b16 v[246:247], v183 offset:136\n"
"v_max_f32 v118, v118, v126\n"
"v_mfma_f32_16x16x32_bf16 v[132:135], v[132:135], v[144:147], 0\n"
"s_waitcnt lgkmcnt(6)\n"
"ds_read_b128 v[48:51], v188 offset:2048\n"
"v_max_f32 v119, v119, v127\n"
"v_mfma_f32_16x16x32_bf16 v[132:135], v[104:107], v[156:159], v[132:135]\n"
"s_add_u32 m0, s34, 3072\n"
"ds_read_b128 v[52:55], v189 offset:2048\n"
"buffer_load_dwordx4 v191, s[8:11], s32 offen lds\n"
"v_mfma_f32_16x16x32_bf16 v[140:143], v[140:143], v[144:147], 0\n"
"s_add_u32 s32, s32, s33\n"
"ds_read_b64_tr_b16 v[248:249], v182 offset:904\n"
"v_max3_f32 v170, v170, v115, v116\n"
"v_mfma_f32_16x16x32_bf16 v[140:143], v[108:111], v[158:161], v[140:143]\n"
"ds_read_b64_tr_b16 v[250:251], v183 offset:904\n"
"v_max3_f32 v170, v170, v117, v118\n"
"v_mfma_f32_16x16x32_bf16 v[208:211], a[160:163], a[220:223], v[208:211]\n"
"ds_read_b64_tr_b16 v[120:121], v182 offset:72\n"
"v_max_f32 v128, v128, v136\n"
"v_mfma_f32_16x16x32_bf16 v[208:211], a[172:175], a[224:227], v[208:211]\n"
"ds_read_b64_tr_b16 v[122:123], v183 offset:72\n"
"v_max_f32 v129, v129, v137\n"
"v_mfma_f32_16x16x32_bf16 v[208:211], a[184:187], a[228:231], v[208:211]\n"
"ds_read_b64_tr_b16 v[124:125], v182 offset:840\n"
"v_max_f32 v130, v130, v138\n"
"v_mfma_f32_16x16x32_bf16 v[208:211], a[196:199], a[232:235], v[208:211]\n"
"ds_read_b64_tr_b16 v[126:127], v183 offset:840\n"
"v_max_f32 v131, v131, v139\n"
"v_mfma_f32_16x16x32_bf16 v[208:211], a[208:211], a[236:239], v[208:211]\n"
"ds_read_b64_tr_b16 v[216:217], v182 offset:27136\n"
"v_max3_f32 v170, v170, v119, v128\n"
"v_mfma_f32_16x16x32_bf16 v[204:207], a[192:195], v[56:59], v[204:207]\n"
"s_waitcnt lgkmcnt(7)\n"
"ds_read_b64_tr_b16 v[218:219], v183 offset:27136\n"
"v_max3_f32 v170, v170, v129, v130\n"
"v_mfma_f32_16x16x32_bf16 v[204:207], a[204:207], v[64:67], v[204:207]\n"
"s_add_u32 m0, s34, 1024\n"
"v_cvt_pk_bf16_f32 v150, v208, v209\n"
"v_cvt_pk_bf16_f32 v151, v210, v211\n"
"v_mfma_f32_16x16x32_bf16 v[208:211], a[148:151], a[240:243], 0\n"
"buffer_load_dwordx4 v190, s[8:11], s0 offen lds\n"
"ds_read_b64_tr_b16 v[220:221], v182 offset:27904\n"
"v_mfma_f32_16x16x32_bf16 v[208:211], a[160:163], a[244:247], v[208:211]\n"
"ds_read_b64_tr_b16 v[222:223], v183 offset:27904\n"
"v_cvt_pk_bf16_f32 v152, v204, v205\n"
"v_mfma_f32_16x16x32_bf16 v[208:211], a[172:175], a[248:251], v[208:211]\n"
"v_cvt_pk_bf16_f32 v153, v206, v207\n"
"v_max_f32 v132, v132, v140\n"
"v_mfma_f32_16x16x32_bf16 v[208:211], a[184:187], a[252:255], v[208:211]\n"
"ds_read_b64_tr_b16 v[72:73], v182 offset:27144\n"
"ds_read_b64_tr_b16 v[74:75], v183 offset:27144\n"
"v_mfma_f32_16x16x32_bf16 v[208:211], a[196:199], v[56:59], v[208:211]\n"
"ds_read_b64_tr_b16 v[76:77], v182 offset:27912\n"
"v_max_f32 v133, v133, v141\n"
"v_mfma_f32_16x16x32_bf16 v[208:211], a[208:211], v[64:67], v[208:211]\n"
"ds_read_b64_tr_b16 v[78:79], v183 offset:27912\n"
"v_max_f32 v134, v134, v142\n"
"v_mfma_f32_16x16x32_bf16 v[204:207], a[0:3], v[32:35], 0\n"
"ds_read_b64_tr_b16 v[136:137], v182 offset:27080\n"
"v_max_f32 v135, v135, v143\n"
"v_mfma_f32_16x16x32_bf16 v[204:207], a[12:15], v[36:39], v[204:207]\n"
"ds_read_b64_tr_b16 v[138:139], v183 offset:27080\n"
"v_max3_f32 v170, v170, v131, v132\n"
"v_mfma_f32_16x16x32_bf16 v[204:207], a[24:27], v[40:43], v[204:207]\n"
"s_add_u32 m0, s34, 4096\n"
"ds_read_b64_tr_b16 v[140:141], v182 offset:27848\n"
"buffer_load_dwordx4 v191, s[8:11], s0 offen lds\n"
"v_mfma_f32_16x16x32_bf16 v[204:207], a[36:39], v[44:47], v[204:207]\n"
"s_add_u32 s0, s0, s33\n"
"ds_read_b64_tr_b16 v[142:143], v183 offset:27848\n"
"v_cvt_pk_bf16_f32 v154, v208, v209\n"
"v_mfma_f32_16x16x32_bf16 v[204:207], a[48:51], v[48:51], v[204:207]\n"
"v_cvt_pk_bf16_f32 v155, v210, v211\n"
"v_max3_f32 v170, v170, v133, v134\n"
"v_mfma_f32_16x16x32_bf16 v[204:207], a[60:63], v[52:55], v[204:207]\n"
"ds_read_b64 v[80:81], v20 offset:272\n"
"ds_read_b64 v[86:87], v20 offset:336\n"
"v_mfma_f32_16x16x32_bf16 v[212:215], a[152:155], a[216:219], 0\n"
"ds_read_b64 v[88:89], v20 offset:544\n"
"ds_read_b64 v[94:95], v20 offset:608\n"
"v_mfma_f32_16x16x32_bf16 v[212:215], a[164:167], a[220:223], v[212:215]\n"
"ds_read_b64 v[96:97], v20 offset:816\n"
"ds_read_b64 v[102:103], v20 offset:880\n"
"v_mfma_f32_16x16x32_bf16 v[212:215], a[176:179], a[224:227], v[212:215]\n"
"v_cvt_pk_bf16_f32 v204, v204, v205\n"
"v_cvt_pk_bf16_f32 v205, v206, v207\n"
"v_mfma_f32_16x16x32_bf16 v[212:215], a[188:191], a[228:231], v[212:215]\n"
"ds_read_b64 v[104:105], v20 offset:1088\n"
"ds_read_b64 v[110:111], v20 offset:1152\n"
"v_mfma_f32_16x16x32_bf16 v[212:215], a[200:203], a[232:235], v[212:215]\n"
"s_add_u32 m0, s34, 2048\n"
"v_mov_b64 v[98:99], 0\n"
"v_mov_b64 v[100:101], 0\n"
"v_mfma_f32_16x16x32_bf16 v[212:215], a[212:215], a[236:239], v[212:215]\n"
"s_waitcnt lgkmcnt(7)\n"
"buffer_load_dwordx4 v190, s[8:11], s1 offen lds\n"
"v_mov_b64 v[106:107], 0\n"
"v_mfma_f32_16x16x32_bf16 v[208:211], a[4:7], v[32:35], 0\n"
"s_waitcnt lgkmcnt(5)\n"
"v_mov_b64 v[108:109], 0\n"
"v_max_f32 v171, v170, v135\n"
"v_mfma_f32_16x16x32_bf16 v[208:211], a[16:19], v[36:39], v[208:211]\n"
"s_waitcnt lgkmcnt(3)\n"
"v_cvt_pk_bf16_f32 v162, v212, v213\n"
"v_cvt_pk_bf16_f32 v163, v214, v215\n"
"v_mfma_f32_16x16x32_bf16 v[208:211], a[28:31], v[40:43], v[208:211]\n"
"s_waitcnt lgkmcnt(2)\n"
"v_max_f32 v170, v170, v135\n"
"v_subrev_u32 v182, s48, v182\n"
"v_mfma_f32_16x16x32_bf16 v[208:211], a[40:43], v[44:47], v[208:211]\n"
"s_waitcnt lgkmcnt(1)\n"
"v_permlane16_swap_b32 v170, v171\n"
"v_mfma_f32_16x16x32_bf16 v[208:211], a[52:55], v[48:51], v[208:211]\n"
"s_waitcnt lgkmcnt(0)\n"
"v_max_f32 v170, v170, v171\n"
"v_mov_b32 v171, v170\n"
"v_mfma_f32_16x16x32_bf16 v[208:211], a[64:67], v[52:55], v[208:211]\n"
"s_nop 0\n"
"v_permlane32_swap_b32 v170, v171\n"
"v_max_f32 v170, v170, v171\n"
"v_mul_f32 v170, 0xbe553b95, v170\n"
"v_pk_fma_f32 v[112:113], v[112:113], s[70:71], v[170:171] op_sel_hi:[1,0,0]\n"
"v_pk_fma_f32 v[114:115], v[114:115], s[70:71], v[170:171] op_sel_hi:[1,0,0]\n"
"v_pk_fma_f32 v[116:117], v[116:117], s[70:71], v[170:171] op_sel_hi:[1,0,0]\n"
"v_pk_fma_f32 v[118:119], v[118:119], s[70:71], v[170:171] op_sel_hi:[1,0,0]\n"
"v_pk_fma_f32 v[128:129], v[128:129], s[70:71], v[170:171] op_sel_hi:[1,0,0]\n"
"v_pk_fma_f32 v[130:131], v[130:131], s[70:71], v[170:171] op_sel_hi:[1,0,0]\n"
"v_pk_fma_f32 v[132:133], v[132:133], s[70:71], v[170:171] op_sel_hi:[1,0,0]\n"
"v_pk_fma_f32 v[134:135], v[134:135], s[70:71], v[170:171] op_sel_hi:[1,0,0]\n"
"v_mfma_f32_16x16x32_bf16 v[212:215], a[152:155], a[240:243], 0\n"
"s_add_u32 m0, s34, 5120\n"
"v_cvt_pk_bf16_f32 v206, v208, v209\n"
"v_cvt_pk_bf16_f32 v207, v210, v211\n"
"v_mfma_f32_16x16x32_bf16 v[212:215], a[164:167], a[244:247], v[212:215]\n"
"buffer_load_dwordx4 v191, s[8:11], s1 offen lds\n"
"ds_write_b128 v186, v[204:207] offset:60768\n"
"v_mfma_f32_16x16x32_bf16 v[212:215], a[176:179], a[248:251], v[212:215]\n"
"s_add_u32 s1, s1, s33\n"
"v_exp_f32 v112, v112\n"
"v_mfma_f32_16x16x32_bf16 v[212:215], a[188:191], a[252:255], v[212:215]\n"
"s_waitcnt vmcnt(8)\n"
"v_exp_f32 v113, v113\n"
"v_mfma_f32_16x16x32_bf16 v[212:215], a[200:203], v[56:59], v[212:215]\n"
"v_and_b32 v200, 0xffffff00, v200\n"
"v_add_u32 v200, 0x100, v200\n"
"v_mfma_f32_16x16x32_bf16 v[212:215], a[212:215], v[64:67], v[212:215]\n"
"s_mov_b64 exec, 1\n"
"buffer_store_dword v200, off, s[28:31], s39 offset:128 sc1\n"
"s_mov_b64 exec, -1\n"
"v_mfma_f32_16x16x32_bf16 v[208:211], a[80:83], v[32:35], 0\n"
"v_sub_u32 v201, 0, v200\n"
"s_nop 0\n"
"v_readfirstlane_b32 s50, v201\n"
"v_mfma_f32_16x16x32_bf16 v[208:211], a[92:95], v[36:39], v[208:211]\n"
"v_cvt_pk_bf16_f32 v166, v212, v213\n"
"v_cvt_pk_bf16_f32 v167, v214, v215\n"
"v_mfma_f32_16x16x32_bf16 v[208:211], a[104:107], v[40:43], v[208:211]\n"
"v_exp_f32 v114, v114\n"
"v_mfma_f32_16x16x32_bf16 v[208:211], a[116:119], v[44:47], v[208:211]\n"
"v_exp_f32 v115, v115\n"
"v_mfma_f32_16x16x32_bf16 v[208:211], a[128:131], v[48:51], v[208:211]\n"
"v_cvt_pk_bf16_f32 v82, v112, v114\n"
"v_cvt_pk_bf16_f32 v83, v113, v115\n"
"v_mfma_f32_16x16x32_bf16 v[208:211], a[140:143], v[52:55], v[208:211]\n"
"ds_read_b128 v[112:115], v178 offset:13504\n"
"v_exp_f32 v116, v116\n"
"v_mfma_f32_16x16x32_bf16 v[212:215], a[8:11], v[32:35], 0\n"
"v_exp_f32 v117, v117\n"
"v_mfma_f32_16x16x32_bf16 v[212:215], a[20:23], v[36:39], v[212:215]\n"
"v_exp_f32 v118, v118\n"
"v_mfma_f32_16x16x32_bf16 v[212:215], a[32:35], v[40:43], v[212:215]\n"
"v_exp_f32 v119, v119\n"
"v_mfma_f32_16x16x32_bf16 v[212:215], a[44:47], v[44:47], v[212:215]\n"
"v_cvt_pk_bf16_f32 v84, v116, v118\n"
"v_cvt_pk_bf16_f32 v85, v117, v119\n"
"v_mfma_f32_16x16x32_bf16 v[212:215], a[56:59], v[48:51], v[212:215]\n"
"ds_read_b128 v[116:119], v178 offset:27008\n"
"ds_read_b64_tr_b16 a[240:241], v182 offset:13632\n"
"v_mfma_f32_16x16x32_bf16 v[212:215], a[68:71], v[52:55], v[212:215]\n"
"v_exp_f32 v128, v128\n"
"v_mfma_f32_16x16x32_bf16 v[204:207], a[72:75], v[32:35], 0\n"
"v_exp_f32 v129, v129\n"
"v_mfma_f32_16x16x32_bf16 v[204:207], a[84:87], v[36:39], v[204:207]\n"
"v_exp_f32 v130, v130\n"
"v_mfma_f32_16x16x32_bf16 v[204:207], a[96:99], v[40:43], v[204:207]\n"
"v_exp_f32 v131, v131\n"
"v_mfma_f32_16x16x32_bf16 v[204:207], a[108:111], v[44:47], v[204:207]\n"
"v_cvt_pk_bf16_f32 v212, v212, v213\n"
"v_cvt_pk_bf16_f32 v213, v214, v215\n"
"v_mfma_f32_16x16x32_bf16 v[204:207], a[120:123], v[48:51], v[204:207]\n"
"v_cvt_pk_bf16_f32 v214, v208, v209\n"
"v_cvt_pk_bf16_f32 v215, v210, v211\n"
"v_mfma_f32_16x16x32_bf16 v[204:207], a[132:135], v[52:55], v[204:207]\n"
"ds_write_b128 v186, v[212:215] offset:60832\n"
"v_mfma_f32_16x16x32_bf16 v[208:211], a[76:79], v[32:35], 0\n"
"v_cvt_pk_bf16_f32 v90, v128, v130\n"
"v_cvt_pk_bf16_f32 v91, v129, v131\n"
"v_mfma_f32_16x16x32_bf16 v[208:211], a[88:91], v[36:39], v[208:211]\n"
"v_exp_f32 v132, v132\n"
"v_mfma_f32_16x16x32_bf16 v[208:211], a[100:103], v[40:43], v[208:211]\n"
"ds_read_b128 v[128:131], v180\n"
"v_cvt_pk_bf16_f32 v204, v204, v205\n"
"v_mfma_f32_16x16x32_bf16 v[208:211], a[112:115], v[44:47], v[208:211]\n"
"v_cvt_pk_bf16_f32 v205, v206, v207\n"
"v_subrev_u32 v183, s48, v183\n"
"v_mfma_f32_16x16x32_bf16 v[208:211], a[124:127], v[48:51], v[208:211]\n"
"ds_read_b64_tr_b16 a[242:243], v183 offset:13632\n"
"ds_read_b64_tr_b16 a[244:245], v182 offset:14400\n"
"v_mfma_f32_16x16x32_bf16 v[208:211], a[136:139], v[52:55], v[208:211]\n"
"ds_read_b64_tr_b16 a[246:247], v183 offset:14400\n"
"v_exp_f32 v133, v133\n"
"v_mfma_f32_16x16x32_bf16 v[212:215], a[144:147], v[32:35], 0\n"
"v_exp_f32 v134, v134\n"
"v_mfma_f32_16x16x32_bf16 v[212:215], a[156:159], v[36:39], v[212:215]\n"
"v_exp_f32 v135, v135\n"
"v_mfma_f32_16x16x32_bf16 v[212:215], a[168:171], v[40:43], v[212:215]\n"
"v_cvt_pk_bf16_f32 v92, v132, v134\n"
"v_cvt_pk_bf16_f32 v206, v208, v209\n"
"v_mfma_f32_16x16x32_bf16 v[212:215], a[180:183], v[44:47], v[212:215]\n"
"v_cvt_pk_bf16_f32 v207, v210, v211\n"
"v_cvt_pk_bf16_f32 v93, v133, v135\n"
"v_mfma_f32_16x16x32_bf16 v[212:215], a[192:195], v[48:51], v[212:215]\n"
"ds_write_b128 v186, v[204:207] offset:60896\n"
"v_mfma_f32_16x16x32_bf16 v[212:215], a[204:207], v[52:55], v[212:215]\n"
"v_bfi_b32 v80, v4, v80, v26\n"
"v_bfi_b32 v81, v5, v81, v26\n"
"v_smfmac_f32_16x16x64_bf16 v[252:255], v[82:85], v[224:231], v63\n"
"v_bfi_b32 v86, v6, v86, v26\n"
"v_smfmac_f32_16x16x64_bf16 v[252:255], v[90:93], v[216:223], v63\n"
"v_bfi_b32 v87, v7, v87, v26\n"
"v_mfma_f32_16x16x32_bf16 v[208:211], a[148:151], v[32:35], 0\n"
"v_bfi_b32 v88, v0, v88, v26\n"
"v_cvt_pk_bf16_f32 v144, v212, v213\n"
"v_mfma_f32_16x16x32_bf16 v[208:211], a[160:163], v[36:39], v[208:211]\n"
"v_cvt_pk_bf16_f32 v145, v214, v215\n"
"v_bfi_b32 v89, v1, v89, v26\n"
"v_mfma_f32_16x16x32_bf16 v[208:211], a[172:175], v[40:43], v[208:211]\n"
"v_cvt_pk_bf16_f32 v252, v252, v253\n"
"v_cvt_pk_bf16_f32 v253, v254, v255\n"
"v_mfma_f32_16x16x32_bf16 v[208:211], a[184:187], v[44:47], v[208:211]\n"
"s_waitcnt lgkmcnt(0)\n"
"v_bfi_b32 v94, v2, v94, v26\n"
"v_bfi_b32 v95, v3, v95, v26\n"
"v_mfma_f32_16x16x32_bf16 v[208:211], a[196:199], v[48:51], v[208:211]\n"
"s_barrier\n"
"v_mfma_f32_16x16x32_bf16 v[208:211], a[208:211], v[52:55], v[208:211]\n"
"s_add_u32 m0, s79, 130176\n"
"ds_read_b128 v[132:135], v180 offset:13504\n"
"buffer_load_dwordx4 v192, s[16:19], s43 offen lds\n"
"v_smfmac_f32_16x16x64_bf16 v[98:101], v[82:85], v[244:251], v63\n"
"ds_read_b64_tr_b16 v[224:225], v182 offset:40640\n"
"v_smfmac_f32_16x16x64_bf16 v[98:101], v[90:93], v[72:79], v63\n"
"ds_read_b64_tr_b16 v[226:227], v183 offset:40640\n"
"v_smfmac_f32_16x16x64_bf16 v[106:109], v[82:85], v[120:127], v63\n"
"ds_read_b128 v[120:123], v179 offset:13504\n"
"v_smfmac_f32_16x16x64_bf16 v[106:109], v[90:93], v[136:143], v63\n"
"ds_read_b128 v[124:127], v179 offset:27008\n"
"v_mfma_f32_16x16x32_bf16 v[82:85], v[172:175], v[82:85], 0\n"
"ds_read_b128 v[136:139], v181\n"
"ds_read_b128 v[140:143], v181 offset:13504\n"
"v_mfma_f32_16x16x32_bf16 v[90:93], v[172:175], v[90:93], v[82:85]\n"
"buffer_load_dword v237, v238, s[8:11], s78 offen\n"
"v_cvt_pk_bf16_f32 v146, v208, v209\n"
"v_mfma_f32_16x16x32_bf16 v[204:207], a[152:155], v[32:35], 0\n"
"s_add_u32 s78, s78, s33\n"
"ds_read_b128 v[32:35], v188 offset:6144\n"
"ds_read_b64 v[82:83], v178 offset:13568\n"
"v_mfma_f32_16x16x32_bf16 v[204:207], a[164:167], v[36:39], v[204:207]\n"
"v_cvt_pk_bf16_f32 v147, v210, v211\n"
"v_cvt_pk_bf16_f32 v98, v98, v99\n"
"v_mfma_f32_16x16x32_bf16 v[204:207], a[176:179], v[40:43], v[204:207]\n"
"s_add_u32 m0, s79, 130176\n"
"ds_read_b128 v[36:39], v189 offset:6144\n"
"ds_read_b128 v[40:43], v188 offset:7168\n"
"v_mfma_f32_16x16x32_bf16 v[204:207], a[188:191], v[44:47], v[204:207]\n"
"buffer_load_dwordx4 v192, s[16:19], s43 offen offset:1024 lds\n"
"ds_read_b128 v[44:47], v189 offset:7168\n"
"v_mfma_f32_16x16x32_bf16 v[204:207], a[200:203], v[48:51], v[204:207]\n"
"v_rcp_f32 v171, v90\n"
"v_mfma_f32_16x16x32_bf16 v[204:207], a[212:215], v[52:55], v[204:207]\n"
"ds_read_b128 v[48:51], v188 offset:8192\n"
"ds_read_b128 v[52:55], v189 offset:8192\n"
"v_cvt_pk_bf16_f32 v171, v171, v171\n"
"v_and_b32 v92, v171, v176\n"
"v_and_b32 v93, v171, v177\n"
"v_cvt_pk_bf16_f32 v99, v100, v101\n"
"v_cvt_pk_bf16_f32 v106, v106, v107\n"
"v_mfma_f32_16x16x16_bf16 v[252:255], v[252:253], v[92:93], 0\n"
"s_waitcnt lgkmcnt(6)\n"
"ds_read_b64 v[84:85], v179 offset:13568\n"
"ds_read_b64 v[90:91], v178 offset:27072\n"
"v_mfma_f32_16x16x32_bf16 v[212:215], a[0:3], v[32:35], 0\n"
"s_waitcnt lgkmcnt(7)\n"
"buffer_load_dword v237, v238, s[8:11], s78 offen\n"
"v_cvt_pk_bf16_f32 v158, v204, v205\n"
"v_mfma_f32_16x16x32_bf16 v[112:115], v[112:115], v[148:151], 0\n"
"s_waitcnt lgkmcnt(6)\n"
"v_cvt_pk_bf16_f32 v159, v206, v207\n"
"v_cvt_pk_bf16_f32 v107, v108, v109\n"
"v_mfma_f32_16x16x32_bf16 v[112:115], v[80:83], v[160:163], v[112:115]\n"
"s_add_u32 s78, s78, s33\n"
"ds_read_b64_tr_b16 v[228:229], v182 offset:41408\n"
"ds_read_b64_tr_b16 v[230:231], v183 offset:41408\n"
"v_mfma_f32_16x16x32_bf16 v[212:215], a[12:15], v[36:39], v[212:215]\n"
"s_waitcnt lgkmcnt(6)\n"
"ds_read_b64_tr_b16 v[244:245], v182 offset:40648\n"
"v_cvt_pk_bf16_f32 v252, v252, v253\n"
"v_mfma_f32_16x16x32_bf16 v[212:215], a[24:27], v[40:43], v[212:215]\n"
"s_add_u32 m0, s79, 130176\n"
"ds_read_b64_tr_b16 v[246:247], v183 offset:40648\n"
"buffer_load_dwordx4 v192, s[16:19], s43 offen offset:2048 lds\n"
"v_mfma_f32_16x16x32_bf16 v[212:215], a[36:39], v[44:47], v[212:215]\n"
"s_waitcnt lgkmcnt(7)\n"
"ds_read_b64_tr_b16 v[248:249], v182 offset:41416\n"
"v_cvt_pk_bf16_f32 v253, v254, v255\n"
"v_mfma_f32_16x16x32_bf16 v[212:215], a[48:51], v[48:51], v[212:215]\n"
"s_waitcnt lgkmcnt(6)\n"
"ds_read_b64_tr_b16 v[250:251], v183 offset:41416\n"
"v_bfi_b32 v96, v0, v96, v26\n"
"v_mfma_f32_16x16x32_bf16 v[212:215], a[60:63], v[52:55], v[212:215]\n"
"s_waitcnt lgkmcnt(6)\n"
"ds_read_b64_tr_b16 a[248:249], v182 offset:40584\n"
"v_bfi_b32 v97, v1, v97, v26\n"
"v_mfma_f32_16x16x32_bf16 v[120:123], v[120:123], v[148:151], 0\n"
"s_add_u32 s43, s79, 18432\n"
"ds_read_b64_tr_b16 a[250:251], v183 offset:40584\n"
"v_bfi_b32 v102, v2, v102, v26\n"
"v_mfma_f32_16x16x32_bf16 v[120:123], v[84:87], v[162:165], v[120:123]\n"
"ds_read_b64_tr_b16 a[252:253], v182 offset:41352\n"
"v_cvt_pk_bf16_f32 v212, v212, v213\n"
"v_mfma_f32_16x16x32_bf16 v[116:119], v[116:119], v[148:151], 0\n"
"ds_read_b64_tr_b16 a[254:255], v183 offset:41352\n"
"v_cvt_pk_bf16_f32 v213, v214, v215\n"
"v_mfma_f32_16x16x32_bf16 v[116:119], v[88:91], v[160:163], v[116:119]\n"
"v_bfi_b32 v103, v3, v103, v26\n"
"v_bfi_b32 v104, v8, v104, v26\n"
"v_mfma_f32_16x16x16_bf16 v[98:101], v[98:99], v[92:93], 0\n"
"s_add_u32 m0, s34, 6144\n"
"v_bfi_b32 v105, v9, v105, v26\n"
"v_bfi_b32 v110, v10, v110, v26\n"
"v_mfma_f32_16x16x16_bf16 v[106:109], v[106:107], v[92:93], 0\n"
"buffer_load_dwordx4 v190, s[8:11], s32 offen lds\n"
"ds_read_b64 v[92:93], v179 offset:27072\n"
"v_mfma_f32_16x16x32_bf16 v[208:211], a[4:7], v[32:35], 0\n"
"v_bfi_b32 v111, v11, v111, v26\n"
"v_cvt_pk_bf16_f32 v254, v98, v99\n"
"v_mfma_f32_16x16x32_bf16 v[208:211], a[16:19], v[36:39], v[208:211]\n"
"ds_read_b64 v[98:99], v180 offset:64\n"
"v_cvt_pk_bf16_f32 v255, v100, v101\n"
"v_mfma_f32_16x16x32_bf16 v[208:211], a[28:31], v[40:43], v[208:211]\n"
"ds_read_b64 v[100:101], v181 offset:64\n"
"v_cvt_pk_bf16_f32 v106, v106, v107\n"
"v_mfma_f32_16x16x32_bf16 v[208:211], a[40:43], v[44:47], v[208:211]\n"
"buffer_store_dwordx4 v[252:255], v192, s[28:31], s37 offen sc1\n"
"v_mfma_f32_16x16x32_bf16 v[208:211], a[52:55], v[48:51], v[208:211]\n"
"v_cvt_pk_bf16_f32 v107, v108, v109\n"
"v_max_f32 v112, v112, v120\n"
"v_mfma_f32_16x16x32_bf16 v[208:211], a[64:67], v[52:55], v[208:211]\n"
"s_waitcnt lgkmcnt(2)\n"
"ds_read_b64 v[108:109], v181 offset:13568\n"
"v_max_f32 v113, v113, v121\n"
"v_mfma_f32_16x16x32_bf16 v[204:207], a[8:11], v[32:35], 0\n"
"ds_read_b64_tr_b16 v[120:121], v182 offset:13640\n"
"v_max_f32 v114, v114, v122\n"
"v_mfma_f32_16x16x32_bf16 v[204:207], a[20:23], v[36:39], v[204:207]\n"
"s_waitcnt lgkmcnt(3)\n"
"v_cvt_pk_bf16_f32 v214, v208, v209\n"
"v_cvt_pk_bf16_f32 v215, v210, v211\n"
"v_mfma_f32_16x16x32_bf16 v[124:127], v[124:127], v[148:151], 0\n"
"s_waitcnt lgkmcnt(2)\n"
"ds_write_b128 v187, v[212:215]\n"
"v_mfma_f32_16x16x32_bf16 v[124:127], v[92:95], v[162:165], v[124:127]\n"
"s_add_u32 m0, s34, 9216\n"
"v_max_f32 v115, v115, v123\n"
"v_max3_f32 v170, v112, v113, v114\n"
"v_mfma_f32_16x16x32_bf16 v[128:131], v[128:131], v[148:151], 0\n"
"v_mov_b64 v[252:253], 0\n"
"v_mov_b64 v[254:255], 0\n"
"v_mfma_f32_16x16x32_bf16 v[128:131], v[96:99], v[160:163], v[128:131]\n"
"s_waitcnt lgkmcnt(2)\n"
"buffer_load_dwordx4 v191, s[8:11], s32 offen lds\n"
"ds_read_b64_tr_b16 v[122:123], v183 offset:13640\n"
"v_mfma_f32_16x16x32_bf16 v[136:139], v[136:139], v[148:151], 0\n"
"s_add_u32 s32, s32, s33\n"
"v_max_f32 v116, v116, v124\n"
"v_max_f32 v117, v117, v125\n"
"v_mfma_f32_16x16x32_bf16 v[136:139], v[100:103], v[162:165], v[136:139]\n"
"ds_read_b64_tr_b16 v[124:125], v182 offset:14408\n"
"v_max_f32 v118, v118, v126\n"
"v_mfma_f32_16x16x32_bf16 v[140:143], v[140:143], v[148:151], 0\n"
"v_max_f32 v119, v119, v127\n"
"v_max3_f32 v170, v170, v115, v116\n"
"v_mfma_f32_16x16x32_bf16 v[140:143], v[108:111], v[162:165], v[140:143]\n"
"buffer_store_dwordx2 v[106:107], v193, s[28:31], s37 offen offset:1024 sc1\n"
"v_mfma_f32_16x16x32_bf16 v[204:207], a[32:35], v[40:43], v[204:207]\n"
"s_add_u32 s37, s37, 1572864\n"
"ds_read_b64 v[106:107], v180 offset:13568\n"
"ds_read_b64_tr_b16 v[126:127], v183 offset:14408\n"
"v_mfma_f32_16x16x32_bf16 v[204:207], a[44:47], v[44:47], v[204:207]\n"
"v_max3_f32 v170, v170, v117, v118\n"
"v_max_f32 v128, v128, v136\n"
"v_mfma_f32_16x16x32_bf16 v[204:207], a[56:59], v[48:51], v[204:207]\n"
"v_max_f32 v129, v129, v137\n"
"v_max_f32 v130, v130, v138\n"
"v_mfma_f32_16x16x32_bf16 v[204:207], a[68:71], v[52:55], v[204:207]\n"
"ds_read_b64_tr_b16 v[136:137], v182 offset:13576\n"
"v_max_f32 v131, v131, v139\n"
"v_mfma_f32_16x16x32_bf16 v[208:211], a[80:83], v[32:35], 0\n"
"s_add_u32 m0, s34, 7168\n"
"ds_read_b64_tr_b16 v[138:139], v183 offset:13576\n"
"v_max3_f32 v170, v170, v119, v128\n"
"v_mfma_f32_16x16x32_bf16 v[208:211], a[92:95], v[36:39], v[208:211]\n"
"ds_read_b64 v[80:81], v20 offset:272\n"
"buffer_load_dwordx4 v190, s[8:11], s0 offen lds\n"
"v_mfma_f32_16x16x32_bf16 v[208:211], a[104:107], v[40:43], v[208:211]\n"
"v_cvt_pk_bf16_f32 v204, v204, v205\n"
"v_cvt_pk_bf16_f32 v205, v206, v207\n"
"v_mfma_f32_16x16x32_bf16 v[208:211], a[116:119], v[44:47], v[208:211]\n"
"s_waitcnt lgkmcnt(4)\n"
"v_max3_f32 v170, v170, v129, v130\n"
"v_mov_b64 v[98:99], 0\n"
"v_mfma_f32_16x16x32_bf16 v[132:135], v[132:135], v[148:151], 0\n"
"ds_read_b64 v[86:87], v20 offset:336\n"
"ds_read_b64 v[110:111], v20 offset:1152\n"
"v_mfma_f32_16x16x32_bf16 v[132:135], v[104:107], v[160:163], v[132:135]\n"
"ds_read_b64 v[104:105], v20 offset:1088\n"
"v_mov_b64 v[100:101], 0\n"
"v_mfma_f32_16x16x32_bf16 v[208:211], a[128:131], v[48:51], v[208:211]\n"
"v_mov_b64 v[106:107], 0\n"
"v_mov_b64 v[108:109], 0\n"
"v_mfma_f32_16x16x32_bf16 v[208:211], a[140:143], v[52:55], v[208:211]\n"
"s_waitcnt lgkmcnt(3)\n"
"s_nop 0\n"
"v_max_f32 v132, v132, v140\n"
"v_mfma_f32_16x16x32_bf16 v[212:215], a[72:75], v[32:35], 0\n"
"v_max_f32 v133, v133, v141\n"
"v_max_f32 v134, v134, v142\n"
"v_mfma_f32_16x16x32_bf16 v[212:215], a[84:87], v[36:39], v[212:215]\n"
"s_add_u32 m0, s34, 10240\n"
"ds_read_b64_tr_b16 v[140:141], v182 offset:14344\n"
"buffer_load_dwordx4 v191, s[8:11], s0 offen lds\n"
"v_mfma_f32_16x16x32_bf16 v[212:215], a[96:99], v[40:43], v[212:215]\n"
"s_waitcnt lgkmcnt(3)\n"
"v_cvt_pk_bf16_f32 v206, v208, v209\n"
"v_cvt_pk_bf16_f32 v207, v210, v211\n"
"v_mfma_f32_16x16x32_bf16 v[212:215], a[108:111], v[44:47], v[212:215]\n"
"s_waitcnt lgkmcnt(1)\n"
"ds_write_b128 v187, v[204:207] offset:64\n"
"v_mfma_f32_16x16x32_bf16 v[212:215], a[120:123], v[48:51], v[212:215]\n"
"s_add_u32 s0, s0, s33\n"
"v_max_f32 v135, v135, v143\n"
"v_max3_f32 v170, v170, v131, v132\n"
"v_mfma_f32_16x16x32_bf16 v[212:215], a[132:135], v[52:55], v[212:215]\n"
"v_max3_f32 v170, v170, v133, v134\n"
"v_max_f32 v171, v170, v135\n"
"v_mfma_f32_16x16x32_bf16 v[208:211], a[76:79], v[32:35], 0\n"
"ds_read_b64_tr_b16 v[142:143], v183 offset:14344\n"
"v_max_f32 v170, v170, v135\n"
"v_mfma_f32_16x16x32_bf16 v[208:211], a[88:91], v[36:39], v[208:211]\n"
"s_nop 0\n"
"v_permlane16_swap_b32 v170, v171\n"
"v_mfma_f32_16x16x32_bf16 v[208:211], a[100:103], v[40:43], v[208:211]\n"
"s_add_u32 m0, s34, 8192\n"
"v_cvt_pk_bf16_f32 v212, v212, v213\n"
"v_cvt_pk_bf16_f32 v213, v214, v215\n"
"v_mfma_f32_16x16x32_bf16 v[208:211], a[112:115], v[44:47], v[208:211]\n"
"buffer_load_dwordx4 v190, s[8:11], s1 offen lds\n"
"v_max_f32 v170, v170, v171\n"
"v_mfma_f32_16x16x32_bf16 v[208:211], a[124:127], v[48:51], v[208:211]\n"
"v_mov_b32 v171, v170\n"
"v_bfi_b32 v80, v12, v80, v26\n"
"v_mfma_f32_16x16x32_bf16 v[208:211], a[136:139], v[52:55], v[208:211]\n"
"v_permlane32_swap_b32 v170, v171\n"
"v_max_f32 v170, v170, v171\n"
"v_mul_f32 v170, 0xbe553b95, v170\n"
"v_pk_fma_f32 v[112:113], v[112:113], s[70:71], v[170:171] op_sel_hi:[1,0,0]\n"
"v_pk_fma_f32 v[114:115], v[114:115], s[70:71], v[170:171] op_sel_hi:[1,0,0]\n"
"v_pk_fma_f32 v[116:117], v[116:117], s[70:71], v[170:171] op_sel_hi:[1,0,0]\n"
"v_pk_fma_f32 v[118:119], v[118:119], s[70:71], v[170:171] op_sel_hi:[1,0,0]\n"
"v_pk_fma_f32 v[128:129], v[128:129], s[70:71], v[170:171] op_sel_hi:[1,0,0]\n"
"v_pk_fma_f32 v[130:131], v[130:131], s[70:71], v[170:171] op_sel_hi:[1,0,0]\n"
"v_pk_fma_f32 v[132:133], v[132:133], s[70:71], v[170:171] op_sel_hi:[1,0,0]\n"
"v_pk_fma_f32 v[134:135], v[134:135], s[70:71], v[170:171] op_sel_hi:[1,0,0]\n"
"v_mfma_f32_16x16x32_bf16 v[204:207], a[144:147], v[32:35], 0\n"
"v_cvt_pk_bf16_f32 v214, v208, v209\n"
"v_cvt_pk_bf16_f32 v215, v210, v211\n"
"v_mfma_f32_16x16x32_bf16 v[204:207], a[156:159], v[36:39], v[204:207]\n"
"ds_write_b128 v187, v[212:215] offset:128\n"
"v_mfma_f32_16x16x32_bf16 v[204:207], a[168:171], v[40:43], v[204:207]\n"
"s_add_u32 m0, s34, 11264\n"
"v_exp_f32 v112, v112\n"
"v_mfma_f32_16x16x32_bf16 v[204:207], a[180:183], v[44:47], v[204:207]\n"
"buffer_load_dwordx4 v191, s[8:11], s1 offen lds\n"
"v_exp_f32 v113, v113\n"
"v_mfma_f32_16x16x32_bf16 v[204:207], a[192:195], v[48:51], v[204:207]\n"
"s_add_u32 s1, s1, s33\n"
"v_exp_f32 v114, v114\n"
"v_mfma_f32_16x16x32_bf16 v[204:207], a[204:207], v[52:55], v[204:207]\n"
"v_exp_f32 v115, v115\n"
"v_mfma_f32_16x16x32_bf16 v[208:211], a[148:151], v[32:35], 0\n"
"v_cvt_pk_bf16_f32 v82, v112, v114\n"
"v_cvt_pk_bf16_f32 v83, v113, v115\n"
"v_mfma_f32_16x16x32_bf16 v[208:211], a[160:163], v[36:39], v[208:211]\n"
"ds_read_b128 v[112:115], v178 offset:27008\n"
"v_exp_f32 v116, v116\n"
"v_mfma_f32_16x16x32_bf16 v[208:211], a[172:175], v[40:43], v[208:211]\n"
"s_waitcnt vmcnt(8) lgkmcnt(1)\n"
"v_exp_f32 v117, v117\n"
"v_mfma_f32_16x16x32_bf16 v[208:211], a[184:187], v[44:47], v[208:211]\n"
"s_barrier\n"
"v_mfma_f32_16x16x32_bf16 v[208:211], a[196:199], v[48:51], v[208:211]\n"
"s_add_u32 m0, s79, 142464\n"
"buffer_load_dword v201, off, s[28:31], s40 offset:128 sc1\n"
"ds_read_b64_tr_b16 a[216:217], v236\n"
"v_mfma_f32_16x16x32_bf16 v[208:211], a[208:211], v[52:55], v[208:211]\n"
".Lic1%=:\n"
"s_cbranch_execz .Lib0%=\n"
"v_exp_f32 v118, v118\n"
"v_mfma_f32_16x16x32_bf16 v[212:215], a[152:155], v[32:35], 0\n"
"buffer_load_dwordx4 v192, s[16:19], s43 offen lds\n"
"ds_read_b128 v[32:35], v188\n"
"v_mfma_f32_16x16x32_bf16 v[212:215], a[164:167], v[36:39], v[212:215]\n"
"ds_read_b128 v[36:39], v189\n"
"v_cvt_pk_bf16_f32 v84, v116, v118\n"
"v_mfma_f32_16x16x32_bf16 v[212:215], a[176:179], v[40:43], v[212:215]\n"
"ds_read_b128 v[40:43], v188 offset:1024\n"
"v_cvt_pk_bf16_f32 v148, v204, v205\n"
"v_mfma_f32_16x16x32_bf16 v[212:215], a[188:191], v[44:47], v[212:215]\n"
"ds_read_b128 v[44:47], v189 offset:1024\n"
"v_cvt_pk_bf16_f32 v149, v206, v207\n"
"v_mfma_f32_16x16x32_bf16 v[212:215], a[200:203], v[48:51], v[212:215]\n"
"ds_read_b128 v[48:51], v188 offset:2048\n"
"v_exp_f32 v119, v119\n"
"v_mfma_f32_16x16x32_bf16 v[212:215], a[212:215], v[52:55], v[212:215]\n"
"ds_read_b128 v[52:55], v189 offset:2048\n"
"v_cvt_pk_bf16_f32 v85, v117, v119\n"
"v_exp_f32 v128, v128\n"
"s_nop 0\n"
"v_smfmac_f32_16x16x64_bf16 v[252:255], v[82:85], a[240:247], v63\n"
"s_waitcnt lgkmcnt(5)\n"
"ds_read_b128 v[116:119], v180\n"
"v_smfmac_f32_16x16x64_bf16 v[98:101], v[82:85], v[120:127], v63\n"
"s_waitcnt lgkmcnt(5)\n"
"ds_read_b128 v[120:123], v179 offset:27008\n"
"v_smfmac_f32_16x16x64_bf16 v[106:109], v[82:85], v[136:143], v63\n"
"s_add_u32 m0, s79, 142464\n"
"s_nop 0\n"
"buffer_load_dwordx4 v192, s[16:19], s43 offen offset:1024 lds\n"
"v_mfma_f32_16x16x32_bf16 v[82:85], v[172:175], v[82:85], 0\n"
"ds_read_b128 v[124:127], v181\n"
"ds_read_b128 v[136:139], v180 offset:13504\n"
"v_mfma_f32_16x16x32_bf16 v[204:207], a[0:3], v[32:35], 0\n"
"ds_read_b128 v[140:143], v180 offset:27008\n"
"v_cvt_pk_bf16_f32 v150, v208, v209\n"
"v_mfma_f32_16x16x32_bf16 v[204:207], a[12:15], v[36:39], v[204:207]\n"
"s_waitcnt lgkmcnt(8)\n"
"ds_read_b64_tr_b16 a[240:241], v182 offset:27136\n"
"v_cvt_pk_bf16_f32 v151, v210, v211\n"
"v_mfma_f32_16x16x32_bf16 v[204:207], a[24:27], v[40:43], v[204:207]\n"
"s_waitcnt lgkmcnt(8)\n"
"ds_read_b64_tr_b16 a[242:243], v183 offset:27136\n"
"buffer_load_dword v237, v238, s[8:11], s78 offen\n"
"v_mfma_f32_16x16x32_bf16 v[204:207], a[36:39], v[44:47], v[204:207]\n"
"s_waitcnt lgkmcnt(8)\n"
"ds_read_b64_tr_b16 a[244:245], v182 offset:27904\n"
"v_exp_f32 v129, v129\n"
"v_mfma_f32_16x16x32_bf16 v[204:207], a[48:51], v[48:51], v[204:207]\n"
"s_waitcnt lgkmcnt(8)\n"
"ds_read_b64_tr_b16 a[246:247], v183 offset:27904\n"
"v_exp_f32 v130, v130\n"
"v_mfma_f32_16x16x32_bf16 v[204:207], a[60:63], v[52:55], v[204:207]\n"
"s_add_u32 m0, s79, 142464\n"
"v_exp_f32 v131, v131\n"
"v_mfma_f32_16x16x32_bf16 v[208:211], a[4:7], v[32:35], 0\n"
"s_add_u32 s78, s78, s33\n"
"buffer_load_dwordx4 v192, s[16:19], s43 offen offset:2048 lds\n"
"v_cvt_pk_bf16_f32 v90, v128, v130\n"
"v_mfma_f32_16x16x32_bf16 v[208:211], a[16:19], v[36:39], v[208:211]\n"
"s_add_u32 s43, s79, 36864\n"
"v_cvt_pk_bf16_f32 v91, v129, v131\n"
"v_cvt_pk_bf16_f32 v162, v212, v213\n"
"v_mfma_f32_16x16x32_bf16 v[208:211], a[28:31], v[40:43], v[208:211]\n"
"ds_read_b128 v[128:131], v181 offset:13504\n"
"ds_read_b64_tr_b16 a[218:219], v236 offset:6144\n"
"v_mfma_f32_16x16x32_bf16 v[208:211], a[40:43], v[44:47], v[208:211]\n"
"v_exp_f32 v132, v132\n"
"v_mfma_f32_16x16x32_bf16 v[208:211], a[52:55], v[48:51], v[208:211]\n"
"v_exp_f32 v133, v133\n"
"v_mfma_f32_16x16x32_bf16 v[208:211], a[64:67], v[52:55], v[208:211]\n"
"v_exp_f32 v134, v134\n"
"v_exp_f32 v135, v135\n"
"v_cvt_pk_bf16_f32 v92, v132, v134\n"
"v_cvt_pk_bf16_f32 v93, v133, v135\n"
"v_cvt_pk_bf16_f32 v163, v214, v215\n"
"v_mfma_f32_16x16x32_bf16 v[212:215], a[8:11], v[32:35], 0\n"
"s_add_u32 m0, s34, 0\n"
"ds_read_b128 v[132:135], v181 offset:27008\n"
"buffer_load_dwordx4 v190, s[8:11], s32 offen lds\n"
"v_smfmac_f32_16x16x64_bf16 v[252:255], v[90:93], v[224:231], v63\n"
"ds_read_b64_tr_b16 v[216:217], v182 offset:54144\n"
"v_smfmac_f32_16x16x64_bf16 v[98:101], v[90:93], v[244:251], v63\n"
"ds_read_b64_tr_b16 v[218:219], v183 offset:54144\n"
"v_smfmac_f32_16x16x64_bf16 v[106:109], v[90:93], a[248:255], v63\n"
"ds_read_b64_tr_b16 v[220:221], v182 offset:54912\n"
"v_mfma_f32_16x16x32_bf16 v[90:93], v[172:175], v[90:93], v[82:85]\n"
"ds_read_b64_tr_b16 v[222:223], v183 offset:54912\n"
"ds_read_b64_tr_b16 v[72:73], v182 offset:54152\n"
"v_mfma_f32_16x16x32_bf16 v[212:215], a[20:23], v[36:39], v[212:215]\n"
"v_cvt_pk_bf16_f32 v252, v252, v253\n"
"v_cvt_pk_bf16_f32 v253, v254, v255\n"
"v_mfma_f32_16x16x32_bf16 v[212:215], a[32:35], v[40:43], v[212:215]\n"
"ds_read_b64 v[82:83], v178 offset:27072\n"
"ds_read_b64 v[84:85], v179 offset:27072\n"
"v_mfma_f32_16x16x32_bf16 v[212:215], a[44:47], v[44:47], v[212:215]\n"
"v_rcp_f32 v171, v90\n"
"v_mfma_f32_16x16x32_bf16 v[212:215], a[56:59], v[48:51], v[212:215]\n"
"s_add_u32 m0, s34, 3072\n"
"ds_read_b64 v[90:91], v180 offset:64\n"
"buffer_load_dwordx4 v191, s[8:11], s32 offen lds\n"
"v_mfma_f32_16x16x32_bf16 v[212:215], a[68:71], v[52:55], v[212:215]\n"
"s_add_u32 s32, s32, s33\n"
"v_cvt_pk_bf16_f32 v171, v171, v171\n"
"v_and_b32 v92, v171, v176\n"
"v_and_b32 v93, v171, v177\n"
"v_cvt_pk_bf16_f32 v98, v98, v99\n"
"v_cvt_pk_bf16_f32 v99, v100, v101\n"
"v_mfma_f32_16x16x16_bf16 v[252:255], v[252:253], v[92:93], 0\n"
"ds_read_b64_tr_b16 v[74:75], v183 offset:54152\n"
"ds_read_b64_tr_b16 v[76:77], v182 offset:54920\n"
"v_mfma_f32_16x16x16_bf16 v[98:101], v[98:99], v[92:93], 0\n"
"v_cvt_pk_bf16_f32 v106, v106, v107\n"
"v_cvt_pk_bf16_f32 v107, v108, v109\n"
"v_cvt_pk_bf16_f32 v204, v204, v205\n"
"v_cvt_pk_bf16_f32 v205, v206, v207\n"
"v_mfma_f32_16x16x16_bf16 v[106:109], v[106:107], v[92:93], 0\n"
"s_waitcnt lgkmcnt(4)\n"
"ds_read_b64 v[92:93], v181 offset:64\n"
"ds_read_b64_tr_b16 v[78:79], v183 offset:54920\n"
"v_mfma_f32_16x16x32_bf16 v[112:115], v[112:115], v[152:155], 0\n"
"s_waitcnt lgkmcnt(5)\n"
"ds_read_b64_tr_b16 v[8:9], v182 offset:54088\n"
"v_cvt_pk_bf16_f32 v252, v252, v253\n"
"v_mfma_f32_16x16x32_bf16 v[120:123], v[120:123], v[152:155], 0\n"
"s_waitcnt lgkmcnt(5)\n"
"ds_read_b64_tr_b16 v[10:11], v183 offset:54088\n"
"v_cvt_pk_bf16_f32 v253, v254, v255\n"
"v_mfma_f32_16x16x32_bf16 v[116:119], v[116:119], v[152:155], 0\n"
"s_add_u32 m0, s34, 1024\n"
"v_cvt_pk_bf16_f32 v254, v98, v99\n"
"v_cvt_pk_bf16_f32 v255, v100, v101\n"
"v_mfma_f32_16x16x32_bf16 v[116:119], v[88:91], v[164:167], v[116:119]\n"
"buffer_load_dwordx4 v190, s[8:11], s0 offen lds\n"
"ds_read_b64 v[98:99], v180 offset:13568\n"
"ds_read_b64 v[100:101], v181 offset:13568\n"
"v_cvt_pk_bf16_f32 v106, v106, v107\n"
"v_cvt_pk_bf16_f32 v107, v108, v109\n"
"v_bfi_b32 v81, v13, v81, v26\n"
"v_bfi_b32 v86, v14, v86, v26\n"
"v_bfi_b32 v87, v15, v87, v26\n"
"v_mfma_f32_16x16x32_bf16 v[112:115], v[80:83], v[164:167], v[112:115]\n"
"s_waitcnt lgkmcnt(5)\n"
"ds_read_b64 v[108:109], v181 offset:27072\n"
"ds_read_b64_tr_b16 v[12:13], v182 offset:54856\n"
"v_mfma_f32_16x16x32_bf16 v[120:123], v[84:87], v[166:169], v[120:123]\n"
"ds_read_b64_tr_b16 v[14:15], v183 offset:54856\n"
"v_bfi_b32 v104, v16, v104, v26\n"
"v_mfma_f32_16x16x32_bf16 v[124:127], v[124:127], v[152:155], 0\n"
"buffer_store_dwordx4 v[252:255], v192, s[28:31], s37 offen sc1\n"
"v_mfma_f32_16x16x32_bf16 v[124:127], v[92:95], v[166:169], v[124:127]\n"
"v_bfi_b32 v105, v17, v105, v26\n"
"v_bfi_b32 v110, v18, v110, v26\n"
"v_bfi_b32 v111, v19, v111, v26\n"
"v_cvt_pk_bf16_f32 v206, v208, v209\n"
"v_cvt_pk_bf16_f32 v207, v210, v211\n"
"v_mfma_f32_16x16x32_bf16 v[208:211], a[80:83], v[32:35], 0\n"
"s_waitcnt lgkmcnt(3)\n"
"ds_write_b128 v186, v[204:207] offset:6752\n"
"v_mfma_f32_16x16x32_bf16 v[136:139], v[136:139], v[152:155], 0\n"
"s_waitcnt lgkmcnt(3)\n"
"v_cvt_pk_bf16_f32 v212, v212, v213\n"
"v_cvt_pk_bf16_f32 v213, v214, v215\n"
"v_mfma_f32_16x16x32_bf16 v[136:139], v[96:99], v[164:167], v[136:139]\n"
"v_max_f32 v112, v112, v120\n"
"v_max_f32 v113, v113, v121\n"
"v_mfma_f32_16x16x32_bf16 v[128:131], v[128:131], v[152:155], 0\n"
"ds_read_b64_tr_b16 v[120:121], v182 offset:27144\n"
"ds_read_b64_tr_b16 a[220:221], v236 offset:8\n"
"v_mfma_f32_16x16x32_bf16 v[128:131], v[100:103], v[166:169], v[128:131]\n"
"s_add_u32 m0, s34, 4096\n"
"v_max_f32 v114, v114, v122\n"
"v_max_f32 v115, v115, v123\n"
"v_mfma_f32_16x16x32_bf16 v[132:135], v[132:135], v[152:155], 0\n"
"buffer_load_dwordx4 v191, s[8:11], s0 offen lds\n"
"ds_read_b64_tr_b16 v[122:123], v183 offset:27144\n"
"v_mfma_f32_16x16x32_bf16 v[132:135], v[108:111], v[166:169], v[132:135]\n"
"s_add_u32 s0, s0, s33\n"
"ds_read_b64_tr_b16 a[222:223], v236 offset:6152\n"
"v_max3_f32 v170, v112, v113, v114\n"
"v_mfma_f32_16x16x32_bf16 v[208:211], a[92:95], v[36:39], v[208:211]\n"
"v_max_f32 v116, v116, v124\n"
"v_max_f32 v117, v117, v125\n"
"v_mfma_f32_16x16x32_bf16 v[208:211], a[104:107], v[40:43], v[208:211]\n"
"ds_read_b64_tr_b16 v[124:125], v182 offset:27912\n"
"v_max_f32 v118, v118, v126\n"
"v_mfma_f32_16x16x32_bf16 v[208:211], a[116:119], v[44:47], v[208:211]\n"
"buffer_store_dwordx2 v[106:107], v193, s[28:31], s37 offen offset:1024 sc1\n"
"v_mfma_f32_16x16x32_bf16 v[208:211], a[128:131], v[48:51], v[208:211]\n"
"s_add_u32 s37, s37, 1572864\n"
"ds_read_b64 v[106:107], v180 offset:27072\n"
"ds_read_b64_tr_b16 a[224:225], v235\n"
"v_mfma_f32_16x16x32_bf16 v[208:211], a[140:143], v[52:55], v[208:211]\n"
"v_max_f32 v119, v119, v127\n"
"v_max3_f32 v170, v170, v115, v116\n"
"v_mfma_f32_16x16x32_bf16 v[204:207], a[72:75], v[32:35], 0\n"
"ds_read_b64_tr_b16 v[126:127], v183 offset:27912\n"
"v_max3_f32 v170, v170, v117, v118\n"
"v_mfma_f32_16x16x32_bf16 v[204:207], a[84:87], v[36:39], v[204:207]\n"
"ds_read_b64_tr_b16 a[226:227], v235 offset:6144\n"
"v_max_f32 v136, v136, v128\n"
"v_mfma_f32_16x16x32_bf16 v[204:207], a[96:99], v[40:43], v[204:207]\n"
"s_add_u32 m0, s34, 2048\n"
"v_cvt_pk_bf16_f32 v214, v208, v209\n"
"v_cvt_pk_bf16_f32 v215, v210, v211\n"
"v_mfma_f32_16x16x32_bf16 v[204:207], a[108:111], v[44:47], v[204:207]\n"
"ds_write_b128 v186, v[212:215] offset:6816\n"
"v_mfma_f32_16x16x32_bf16 v[204:207], a[120:123], v[48:51], v[204:207]\n"
"s_waitcnt lgkmcnt(4)\n"
"buffer_load_dwordx4 v190, s[8:11], s1 offen lds\n"
"v_max_f32 v137, v137, v129\n"
"v_mfma_f32_16x16x32_bf16 v[140:143], v[140:143], v[152:155], 0\n"
"v_max_f32 v138, v138, v130\n"
"v_max_f32 v139, v139, v131\n"
"v_mfma_f32_16x16x32_bf16 v[140:143], v[104:107], v[164:167], v[140:143]\n"
"ds_read_b64_tr_b16 v[128:129], v182 offset:27080\n"
"ds_read_b64_tr_b16 v[130:131], v183 offset:27080\n"
"v_mfma_f32_16x16x32_bf16 v[204:207], a[132:135], v[52:55], v[204:207]\n"
"v_max3_f32 v170, v170, v119, v136\n"
"v_max3_f32 v170, v170, v137, v138\n"
"v_mfma_f32_16x16x32_bf16 v[208:211], a[76:79], v[32:35], 0\n"
"v_mov_b64 v[252:253], 0\n"
"v_mov_b64 v[254:255], 0\n"
"v_mfma_f32_16x16x32_bf16 v[208:211], a[88:91], v[36:39], v[208:211]\n"
"v_max_f32 v140, v140, v132\n"
"v_max_f32 v141, v141, v133\n"
"v_mfma_f32_16x16x32_bf16 v[208:211], a[100:103], v[40:43], v[208:211]\n"
"ds_read_b64_tr_b16 v[132:133], v182 offset:27848\n"
"v_max_f32 v142, v142, v134\n"
"v_mfma_f32_16x16x32_bf16 v[208:211], a[112:115], v[44:47], v[208:211]\n"
"v_max_f32 v143, v143, v135\n"
"v_max3_f32 v170, v170, v139, v140\n"
"v_mfma_f32_16x16x32_bf16 v[208:211], a[124:127], v[48:51], v[208:211]\n"
"s_add_u32 m0, s34, 5120\n"
"ds_read_b64_tr_b16 v[134:135], v183 offset:27848\n"
"buffer_load_dwordx4 v191, s[8:11], s1 offen lds\n"
"v_mfma_f32_16x16x32_bf16 v[208:211], a[136:139], v[52:55], v[208:211]\n"
"s_add_u32 s1, s1, s33\n"
"v_max3_f32 v170, v170, v141, v142\n"
"v_cvt_pk_bf16_f32 v204, v204, v205\n"
"v_mfma_f32_16x16x32_bf16 v[212:215], a[144:147], v[32:35], 0\n"
"v_cvt_pk_bf16_f32 v205, v206, v207\n"
"v_mov_b64 v[98:99], 0\n"
"v_mfma_f32_16x16x32_bf16 v[212:215], a[156:159], v[36:39], v[212:215]\n"
"v_mov_b64 v[100:101], 0\n"
"v_mov_b64 v[106:107], 0\n"
"v_mfma_f32_16x16x32_bf16 v[212:215], a[168:171], v[40:43], v[212:215]\n"
"v_mov_b64 v[108:109], 0\n"
"v_max_f32 v171, v170, v143\n"
"v_mfma_f32_16x16x32_bf16 v[212:215], a[180:183], v[44:47], v[212:215]\n"
"v_max_f32 v170, v170, v143\n"
"v_cvt_pk_bf16_f32 v206, v208, v209\n"
"v_mfma_f32_16x16x32_bf16 v[212:215], a[192:195], v[48:51], v[212:215]\n"
"v_permlane16_swap_b32 v170, v171\n"
"v_mfma_f32_16x16x32_bf16 v[212:215], a[204:207], v[52:55], v[212:215]\n"
"v_max_f32 v170, v170, v171\n"
"v_mov_b32 v171, v170\n"
"v_cvt_pk_bf16_f32 v207, v210, v211\n"
"v_mfma_f32_16x16x32_bf16 v[208:211], a[148:151], v[32:35], 0\n"
"ds_write_b128 v186, v[204:207] offset:6880\n"
"v_mfma_f32_16x16x32_bf16 v[208:211], a[160:163], v[36:39], v[208:211]\n"
"v_permlane32_swap_b32 v170, v171\n"
"v_max_f32 v170, v170, v171\n"
"v_mul_f32 v170, 0xbe553b95, v170\n"
"v_pk_fma_f32 v[112:113], v[112:113], s[70:71], v[170:171] op_sel_hi:[1,0,0]\n"
"v_pk_fma_f32 v[114:115], v[114:115], s[70:71], v[170:171] op_sel_hi:[1,0,0]\n"
"v_pk_fma_f32 v[116:117], v[116:117], s[70:71], v[170:171] op_sel_hi:[1,0,0]\n"
"v_pk_fma_f32 v[118:119], v[118:119], s[70:71], v[170:171] op_sel_hi:[1,0,0]\n"
"v_pk_fma_f32 v[136:137], v[136:137], s[70:71], v[170:171] op_sel_hi:[1,0,0]\n"
"v_pk_fma_f32 v[138:139], v[138:139], s[70:71], v[170:171] op_sel_hi:[1,0,0]\n"
"v_pk_fma_f32 v[140:141], v[140:141], s[70:71], v[170:171] op_sel_hi:[1,0,0]\n"
"v_pk_fma_f32 v[142:143], v[142:143], s[70:71], v[170:171] op_sel_hi:[1,0,0]\n"
"v_mfma_f32_16x16x32_bf16 v[208:211], a[172:175], v[40:43], v[208:211]\n"
"v_exp_f32 v112, v112\n"
"v_mfma_f32_16x16x32_bf16 v[208:211], a[184:187], v[44:47], v[208:211]\n"
"s_waitcnt vmcnt(12)\n"
"v_exp_f32 v113, v113\n"
"v_mfma_f32_16x16x32_bf16 v[208:211], a[196:199], v[48:51], v[208:211]\n"
"v_readfirstlane_b32 s6, v201\n"
"v_cvt_pk_bf16_f32 v152, v212, v213\n"
"v_mfma_f32_16x16x32_bf16 v[208:211], a[208:211], v[52:55], v[208:211]\n"
"s_waitcnt vmcnt(8) lgkmcnt(0)\n"
"v_exp_f32 v114, v114\n"
"v_mfma_f32_16x16x32_bf16 v[204:207], a[152:155], v[32:35], 0\n"
"s_and_b32 s6, s6, 0xffffff00\n"
"ds_read_b128 v[32:35], v188 offset:6144\n"
"v_cvt_pk_bf16_f32 v82, v112, v114\n"
"v_mfma_f32_16x16x32_bf16 v[204:207], a[164:167], v[36:39], v[204:207]\n"
"s_barrier\n"
"v_mfma_f32_16x16x32_bf16 v[204:207], a[176:179], v[40:43], v[204:207]\n"
"s_add_u32 m0, s79, 130176\n"
"ds_read_b128 v[36:39], v189 offset:6144\n"
"buffer_load_dwordx4 v192, s[16:19], s43 offen lds\n"
"v_mfma_f32_16x16x32_bf16 v[204:207], a[188:191], v[44:47], v[204:207]\n"
"ds_read_b128 v[40:43], v188 offset:7168\n"
"ds_read_b128 v[44:47], v189 offset:7168\n"
"v_mfma_f32_16x16x32_bf16 v[204:207], a[200:203], v[48:51], v[204:207]\n"
"ds_read_b128 v[48:51], v188 offset:8192\n"
"v_cvt_pk_bf16_f32 v153, v214, v215\n"
"v_mfma_f32_16x16x32_bf16 v[204:207], a[212:215], v[52:55], v[204:207]\n"
"ds_read_b128 v[52:55], v189 offset:8192\n"
"v_exp_f32 v115, v115\n"
"v_exp_f32 v116, v116\n"
"v_cvt_pk_bf16_f32 v83, v113, v115\n"
"v_exp_f32 v117, v117\n"
"v_exp_f32 v118, v118\n"
"v_exp_f32 v119, v119\n"
"v_cvt_pk_bf16_f32 v84, v116, v118\n"
"v_cvt_pk_bf16_f32 v85, v117, v119\n"
"v_exp_f32 v136, v136\n"
"s_nop 0\n"
"v_smfmac_f32_16x16x64_bf16 v[252:255], v[82:85], a[240:247], v63\n"
"s_waitcnt lgkmcnt(4)\n"
"ds_read_b128 v[112:115], v180\n"
"v_smfmac_f32_16x16x64_bf16 v[98:101], v[82:85], v[120:127], v63\n"
"s_waitcnt lgkmcnt(3)\n"
"ds_read_b128 v[120:123], v181\n"
"v_smfmac_f32_16x16x64_bf16 v[106:109], v[82:85], v[128:135], v63\n"
"s_add_u32 m0, s79, 130176\n"
"s_nop 0\n"
"buffer_load_dwordx4 v192, s[16:19], s43 offen offset:1024 lds\n"
"v_mfma_f32_16x16x32_bf16 v[82:85], v[172:175], v[82:85], 0\n"
"ds_read_b128 v[116:119], v180 offset:13504\n"
"ds_read_b128 v[124:127], v181 offset:13504\n"
"v_mfma_f32_16x16x32_bf16 v[212:215], a[0:3], v[32:35], 0\n"
"ds_read_b128 v[128:131], v180 offset:27008\n"
"v_cvt_pk_bf16_f32 v154, v208, v209\n"
"v_mfma_f32_16x16x32_bf16 v[212:215], a[12:15], v[36:39], v[212:215]\n"
"ds_read_b128 v[132:135], v178\n"
"v_cvt_pk_bf16_f32 v155, v210, v211\n"
"v_mfma_f32_16x16x32_bf16 v[212:215], a[24:27], v[40:43], v[212:215]\n"
"ds_read_b64_tr_b16 a[228:229], v236 offset:12288\n"
"v_exp_f32 v137, v137\n"
"v_mfma_f32_16x16x32_bf16 v[212:215], a[36:39], v[44:47], v[212:215]\n"
"s_waitcnt lgkmcnt(8)\n"
"v_exp_f32 v138, v138\n"
"v_mfma_f32_16x16x32_bf16 v[212:215], a[48:51], v[48:51], v[212:215]\n"
"s_waitcnt lgkmcnt(7)\n"
"v_exp_f32 v139, v139\n"
"v_mfma_f32_16x16x32_bf16 v[212:215], a[60:63], v[52:55], v[212:215]\n"
"v_cvt_pk_bf16_f32 v90, v136, v138\n"
"v_cvt_pk_bf16_f32 v91, v137, v139\n"
"v_mfma_f32_16x16x32_bf16 v[208:211], a[4:7], v[32:35], 0\n"
"s_add_u32 m0, s79, 130176\n"
"ds_read_b128 v[136:139], v181 offset:27008\n"
"buffer_load_dwordx4 v192, s[16:19], s43 offen offset:2048 lds\n"
"v_mfma_f32_16x16x32_bf16 v[208:211], a[16:19], v[36:39], v[208:211]\n"
"s_add_u32 s43, s79, 55296\n"
"v_exp_f32 v140, v140\n"
"v_mfma_f32_16x16x32_bf16 v[208:211], a[28:31], v[40:43], v[208:211]\n"
"v_exp_f32 v141, v141\n"
"v_mfma_f32_16x16x32_bf16 v[208:211], a[40:43], v[44:47], v[208:211]\n"
"v_exp_f32 v142, v142\n"
"v_mfma_f32_16x16x32_bf16 v[208:211], a[52:55], v[48:51], v[208:211]\n"
"v_exp_f32 v143, v143\n"
"v_mfma_f32_16x16x32_bf16 v[208:211], a[64:67], v[52:55], v[208:211]\n"
"v_cvt_pk_bf16_f32 v92, v140, v142\n"
"v_cvt_pk_bf16_f32 v93, v141, v143\n"
"v_cvt_pk_bf16_f32 v166, v204, v205\n"
"v_cvt_pk_bf16_f32 v167, v206, v207\n"
"v_smfmac_f32_16x16x64_bf16 v[252:255], v[90:93], v[216:223], v63\n"
"ds_read_b64_tr_b16 v[24:25], v184 offset:72\n"
"v_smfmac_f32_16x16x64_bf16 v[98:101], v[90:93], v[72:79], v63\n"
"s_add_u32 m0, s34, 6144\n"
"s_nop 0\n"
"buffer_load_dwordx4 v190, s[8:11], s32 offen lds\n"
"v_smfmac_f32_16x16x64_bf16 v[106:109], v[90:93], v[8:15], v63\n"
"ds_read_b64_tr_b16 v[26:27], v185 offset:72\n"
"v_mfma_f32_16x16x32_bf16 v[90:93], v[172:175], v[90:93], v[82:85]\n"
"ds_read_b64_tr_b16 v[28:29], v184 offset:840\n"
"ds_read_b64_tr_b16 v[30:31], v185 offset:840\n"
"v_mfma_f32_16x16x32_bf16 v[204:207], a[8:11], v[32:35], 0\n"
"v_cvt_pk_bf16_f32 v252, v252, v253\n"
"v_cvt_pk_bf16_f32 v253, v254, v255\n"
"v_mfma_f32_16x16x32_bf16 v[204:207], a[20:23], v[36:39], v[204:207]\n"
"ds_read_b64 v[82:83], v180 offset:64\n"
"ds_read_b64 v[84:85], v181 offset:64\n"
"v_mfma_f32_16x16x32_bf16 v[204:207], a[32:35], v[40:43], v[204:207]\n"
"buffer_load_dword v237, v238, s[8:11], s78 offen\n"
"ds_read_b64_tr_b16 a[230:231], v236 offset:18432\n"
"v_mfma_f32_16x16x32_bf16 v[204:207], a[44:47], v[44:47], v[204:207]\n"
"s_add_u32 s78, s78, s33\n"
"v_rcp_f32 v171, v90\n"
"v_mfma_f32_16x16x32_bf16 v[204:207], a[56:59], v[48:51], v[204:207]\n"
"v_cvt_pk_bf16_f32 v171, v171, v171\n"
"v_and_b32 v92, v171, v176\n"
"v_mfma_f32_16x16x32_bf16 v[204:207], a[68:71], v[52:55], v[204:207]\n"
"s_add_u32 m0, s34, 9216\n"
"ds_read_b64 v[90:91], v180 offset:13568\n"
"v_and_b32 v93, v171, v177\n"
"v_cvt_pk_bf16_f32 v98, v98, v99\n"
"v_cvt_pk_bf16_f32 v99, v100, v101\n"
"v_mfma_f32_16x16x16_bf16 v[252:255], v[252:253], v[92:93], 0\n"
"buffer_load_dwordx4 v191, s[8:11], s32 offen lds\n"
"v_cvt_pk_bf16_f32 v106, v106, v107\n"
"v_mfma_f32_16x16x16_bf16 v[98:101], v[98:99], v[92:93], 0\n"
"s_add_u32 s32, s32, s33\n"
"v_cvt_pk_bf16_f32 v107, v108, v109\n"
"v_cvt_pk_bf16_f32 v212, v212, v213\n"
"v_cvt_pk_bf16_f32 v213, v214, v215\n"
"v_mfma_f32_16x16x16_bf16 v[106:109], v[106:107], v[92:93], 0\n"
"s_waitcnt lgkmcnt(2)\n"
"ds_read_b64 v[92:93], v181 offset:13568\n"
"v_cvt_pk_bf16_f32 v252, v252, v253\n"
"v_mfma_f32_16x16x32_bf16 v[112:115], v[112:115], v[144:147], 0\n"
"v_cvt_pk_bf16_f32 v253, v254, v255\n"
"v_cvt_pk_bf16_f32 v254, v98, v99\n"
"v_mfma_f32_16x16x32_bf16 v[112:115], v[80:83], v[156:159], v[112:115]\n"
"ds_read_b64 v[98:99], v180 offset:27072\n"
"v_cvt_pk_bf16_f32 v255, v100, v101\n"
"v_mfma_f32_16x16x32_bf16 v[120:123], v[120:123], v[144:147], 0\n"
"s_waitcnt lgkmcnt(2)\n"
"buffer_store_dwordx4 v[252:255], v192, s[28:31], s37 offen sc1\n"
"v_mfma_f32_16x16x32_bf16 v[120:123], v[84:87], v[158:161], v[120:123]\n"
"ds_read_b64 v[100:101], v181 offset:27072\n"
"v_cvt_pk_bf16_f32 v106, v106, v107\n"
"v_mfma_f32_16x16x32_bf16 v[116:119], v[116:119], v[144:147], 0\n"
"v_cvt_pk_bf16_f32 v107, v108, v109\n"
"s_mov_b64 exec, s[44:45]\n"
"v_add_u32 v179, 0xc0, v179\n"
"s_mov_b64 exec, -1\n"
"v_mfma_f32_16x16x32_bf16 v[116:119], v[88:91], v[156:159], v[116:119]\n"
"s_waitcnt lgkmcnt(2)\n"
"ds_read_b128 v[140:143], v179\n"
"ds_read_b64 v[108:109], v179 offset:64\n"
"v_mfma_f32_16x16x32_bf16 v[124:127], v[124:127], v[144:147], 0\n"
"ds_read_b64_tr_b16 a[232:233], v236 offset:12296\n"
"v_cvt_pk_bf16_f32 v214, v208, v209\n"
"v_mfma_f32_16x16x32_bf16 v[124:127], v[92:95], v[158:161], v[124:127]\n"
"s_waitcnt lgkmcnt(4)\n"
"v_cvt_pk_bf16_f32 v215, v210, v211\n"
"v_cvt_pk_bf16_f32 v204, v204, v205\n"
"v_mfma_f32_16x16x32_bf16 v[128:131], v[128:131], v[144:147], 0\n"
"ds_write_b128 v186, v[212:215] offset:20256\n"
"v_mfma_f32_16x16x32_bf16 v[128:131], v[96:99], v[156:159], v[128:131]\n"
"s_waitcnt lgkmcnt(4)\n"
"buffer_store_dwordx2 v[106:107], v193, s[28:31], s37 offen offset:1024 sc1\n"
"v_mfma_f32_16x16x32_bf16 v[136:139], v[136:139], v[144:147], 0\n"
"s_add_u32 s37, s37, 1572864\n"
"ds_read_b64 v[106:107], v178 offset:64\n"
"v_cvt_pk_bf16_f32 v205, v206, v207\n"
"v_mfma_f32_16x16x32_bf16 v[136:139], v[100:103], v[158:161], v[136:139]\n"
"v_max_f32 v112, v112, v120\n"
"v_max_f32 v113, v113, v121\n"
"v_mfma_f32_16x16x32_bf16 v[208:211], a[80:83], v[32:35], 0\n"
"s_waitcnt lgkmcnt(3)\n"
"v_max_f32 v114, v114, v122\n"
"v_max_f32 v115, v115, v123\n"
"v_mfma_f32_16x16x32_bf16 v[140:143], v[140:143], v[144:147], 0\n"
"ds_read_b128 v[120:123], v180 offset:13504\n"
"ds_read_b64_tr_b16 a[234:235], v236 offset:18440\n"
"v_mfma_f32_16x16x32_bf16 v[140:143], v[108:111], v[158:161], v[140:143]\n"
"s_add_u32 m0, s34, 7168\n"
"v_max3_f32 v170, v112, v113, v114\n"
"v_max_f32 v116, v116, v124\n"
"v_mfma_f32_16x16x32_bf16 v[208:211], a[92:95], v[36:39], v[208:211]\n"
"v_max_f32 v117, v117, v125\n"
"v_max_f32 v118, v118, v126\n"
"v_mfma_f32_16x16x32_bf16 v[208:211], a[104:107], v[40:43], v[208:211]\n"
"buffer_load_dwordx4 v190, s[8:11], s0 offen lds\n"
"v_max_f32 v119, v119, v127\n"
"v_mfma_f32_16x16x32_bf16 v[208:211], a[116:119], v[44:47], v[208:211]\n"
"s_waitcnt lgkmcnt(2)\n"
"ds_read_b128 v[124:127], v180 offset:27008\n"
"v_max3_f32 v170, v170, v115, v116\n"
"v_mfma_f32_16x16x32_bf16 v[132:135], v[132:135], v[144:147], 0\n"
"ds_read_b64_tr_b16 a[236:237], v235 offset:12288\n"
"v_max3_f32 v170, v170, v117, v118\n"
"v_mfma_f32_16x16x32_bf16 v[132:135], v[104:107], v[156:159], v[132:135]\n"
"ds_read_b64_tr_b16 a[238:239], v235 offset:18432\n"
"v_max_f32 v128, v128, v136\n"
"v_mfma_f32_16x16x32_bf16 v[208:211], a[128:131], v[48:51], v[208:211]\n"
"s_waitcnt vmcnt(11)\n"
"v_max_f32 v129, v129, v137\n"
"v_or_b32 v201, 2, s6\n"
"v_mfma_f32_16x16x32_bf16 v[208:211], a[140:143], v[52:55], v[208:211]\n"
"s_mov_b64 exec, 1\n"
"buffer_store_dword v201, off, s[28:31], s40 sc1\n"
"s_mov_b64 exec, -1\n"
"v_mfma_f32_16x16x32_bf16 v[212:215], a[72:75], v[32:35], 0\n"
"v_max_f32 v130, v130, v138\n"
"v_max_f32 v131, v131, v139\n"
"v_mfma_f32_16x16x32_bf16 v[212:215], a[84:87], v[36:39], v[212:215]\n"
"s_add_u32 m0, s34, 10240\n"
"ds_read_b128 v[136:139], v181 offset:13504\n"
"buffer_load_dwordx4 v191, s[8:11], s0 offen lds\n"
"v_mfma_f32_16x16x32_bf16 v[212:215], a[96:99], v[40:43], v[212:215]\n"
"s_add_u32 s0, s0, s33\n"
"v_cvt_pk_bf16_f32 v206, v208, v209\n"
"v_cvt_pk_bf16_f32 v207, v210, v211\n"
"v_mfma_f32_16x16x32_bf16 v[212:215], a[108:111], v[44:47], v[212:215]\n"
"ds_write_b128 v186, v[204:207] offset:20320\n"
"v_mfma_f32_16x16x32_bf16 v[212:215], a[120:123], v[48:51], v[212:215]\n"
"v_max3_f32 v170, v170, v119, v128\n"
"v_max3_f32 v170, v170, v129, v130\n"
"v_mfma_f32_16x16x32_bf16 v[212:215], a[132:135], v[52:55], v[212:215]\n"
"v_max_f32 v132, v132, v140\n"
"v_max_f32 v133, v133, v141\n"
"v_mfma_f32_16x16x32_bf16 v[208:211], a[76:79], v[32:35], 0\n"
"v_max_f32 v134, v134, v142\n"
"v_max_f32 v135, v135, v143\n"
"v_mfma_f32_16x16x32_bf16 v[208:211], a[88:91], v[36:39], v[208:211]\n"
"ds_read_b128 v[140:143], v181 offset:27008\n"
"v_max3_f32 v170, v170, v131, v132\n"
"v_mfma_f32_16x16x32_bf16 v[208:211], a[100:103], v[40:43], v[208:211]\n"
"s_add_u32 m0, s34, 8192\n"
"v_max3_f32 v170, v170, v133, v134\n"
"v_mov_b64 v[252:253], 0\n"
"v_mfma_f32_16x16x32_bf16 v[208:211], a[112:115], v[44:47], v[208:211]\n"
"buffer_load_dwordx4 v190, s[8:11], s1 offen lds\n"
"v_mov_b64 v[254:255], 0\n"
"v_mfma_f32_16x16x32_bf16 v[208:211], a[124:127], v[48:51], v[208:211]\n"
"v_mov_b64 v[98:99], 0\n"
"v_mov_b64 v[100:101], 0\n"
"v_mfma_f32_16x16x32_bf16 v[208:211], a[136:139], v[52:55], v[208:211]\n"
"v_mov_b64 v[106:107], 0\n"
"v_mov_b64 v[108:109], 0\n"
"v_mfma_f32_16x16x32_bf16 v[204:207], a[144:147], v[32:35], 0\n"
"v_max_f32 v171, v170, v135\n"
"v_max_f32 v170, v170, v135\n"
"v_mfma_f32_16x16x32_bf16 v[204:207], a[156:159], v[36:39], v[204:207]\n"
"s_nop 0\n"
"v_permlane16_swap_b32 v170, v171\n"
"v_mfma_f32_16x16x32_bf16 v[204:207], a[168:171], v[40:43], v[204:207]\n"
"v_max_f32 v170, v170, v171\n"
"v_mov_b32 v171, v170\n"
"v_mfma_f32_16x16x32_bf16 v[204:207], a[180:183], v[44:47], v[204:207]\n"
"s_nop 0\n"
"v_permlane32_swap_b32 v170, v171\n"
"v_max_f32 v170, v170, v171\n"
"v_mul_f32 v170, 0xbe553b95, v170\n"
"v_pk_fma_f32 v[112:113], v[112:113], s[70:71], v[170:171] op_sel_hi:[1,0,0]\n"
"v_pk_fma_f32 v[114:115], v[114:115], s[70:71], v[170:171] op_sel_hi:[1,0,0]\n"
"v_pk_fma_f32 v[116:117], v[116:117], s[70:71], v[170:171] op_sel_hi:[1,0,0]\n"
"v_pk_fma_f32 v[118:119], v[118:119], s[70:71], v[170:171] op_sel_hi:[1,0,0]\n"
"v_pk_fma_f32 v[128:129], v[128:129], s[70:71], v[170:171] op_sel_hi:[1,0,0]\n"
"v_pk_fma_f32 v[130:131], v[130:131], s[70:71], v[170:171] op_sel_hi:[1,0,0]\n"
"v_pk_fma_f32 v[132:133], v[132:133], s[70:71], v[170:171] op_sel_hi:[1,0,0]\n"
"v_pk_fma_f32 v[134:135], v[134:135], s[70:71], v[170:171] op_sel_hi:[1,0,0]\n"
"v_mfma_f32_16x16x32_bf16 v[204:207], a[192:195], v[48:51], v[204:207]\n"
"s_add_u32 m0, s34, 11264\n"
"v_cvt_pk_bf16_f32 v212, v212, v213\n"
"v_cvt_pk_bf16_f32 v213, v214, v215\n"
"v_mfma_f32_16x16x32_bf16 v[204:207], a[204:207], v[52:55], v[204:207]\n"
"buffer_load_dwordx4 v191, s[8:11], s1 offen lds\n"
"s_add_u32 s1, s1, s33\n"
"v_exp_f32 v112, v112\n"
"v_exp_f32 v113, v113\n"
"v_exp_f32 v114, v114\n"
"v_exp_f32 v115, v115\n"
"v_cvt_pk_bf16_f32 v82, v112, v114\n"
"v_cvt_pk_bf16_f32 v83, v113, v115\n"
"v_cvt_pk_bf16_f32 v214, v208, v209\n"
"v_exp_f32 v116, v116\n"
"v_cvt_pk_bf16_f32 v215, v210, v211\n"
"v_mfma_f32_16x16x32_bf16 v[208:211], a[148:151], v[32:35], 0\n"
"ds_write_b128 v186, v[212:215] offset:20384\n"
"v_mfma_f32_16x16x32_bf16 v[208:211], a[160:163], v[36:39], v[208:211]\n"
"v_exp_f32 v117, v117\n"
"v_mfma_f32_16x16x32_bf16 v[208:211], a[172:175], v[40:43], v[208:211]\n"
"v_exp_f32 v118, v118\n"
"v_mfma_f32_16x16x32_bf16 v[208:211], a[184:187], v[44:47], v[208:211]\n"
"ds_read_b128 v[112:115], v178\n"
"v_cvt_pk_bf16_f32 v84, v116, v118\n"
"v_mfma_f32_16x16x32_bf16 v[208:211], a[196:199], v[48:51], v[208:211]\n"
"v_exp_f32 v119, v119\n"
"v_mfma_f32_16x16x32_bf16 v[208:211], a[208:211], v[52:55], v[208:211]\n"
"v_cvt_pk_bf16_f32 v85, v117, v119\n"
"v_cvt_pk_bf16_f32 v144, v204, v205\n"
"v_mfma_f32_16x16x32_bf16 v[212:215], a[152:155], v[32:35], 0\n"
"ds_read_b128 v[32:35], v188\n"
"v_cvt_pk_bf16_f32 v145, v206, v207\n"
"v_mfma_f32_16x16x32_bf16 v[212:215], a[164:167], v[36:39], v[212:215]\n"
"s_waitcnt vmcnt(10) lgkmcnt(2)\n"
"ds_read_b128 v[36:39], v189\n"
"v_cvt_pk_bf16_f32 v146, v208, v209\n"
"v_mfma_f32_16x16x32_bf16 v[212:215], a[176:179], v[40:43], v[212:215]\n"
"s_barrier\n"
"v_mfma_f32_16x16x32_bf16 v[212:215], a[188:191], v[44:47], v[212:215]\n"
"s_add_u32 m0, s79, 142464\n"
"ds_read_b128 v[40:43], v188 offset:1024\n"
"buffer_load_dwordx4 v192, s[16:19], s43 offen lds\n"
"v_mfma_f32_16x16x32_bf16 v[212:215], a[200:203], v[48:51], v[212:215]\n"
".Lic2%=:\n"
"s_cbranch_execz .Lib0%=\n"
"ds_read_b128 v[44:47], v189 offset:1024\n"
"ds_read_b128 v[48:51], v188 offset:2048\n"
"v_mfma_f32_16x16x32_bf16 v[212:215], a[212:215], v[52:55], v[212:215]\n"
"ds_read_b128 v[52:55], v189 offset:2048\n"
"v_cvt_pk_bf16_f32 v147, v210, v211\n"
"v_smfmac_f32_16x16x64_bf16 v[252:255], v[82:85], v[224:231], v63\n"
"ds_read_b64_tr_b16 v[224:225], v184 offset:128\n"
"v_smfmac_f32_16x16x64_bf16 v[98:101], v[82:85], v[244:251], v63\n"
"s_waitcnt lgkmcnt(6)\n"
"ds_read_b64_tr_b16 v[226:227], v185 offset:128\n"
"v_smfmac_f32_16x16x64_bf16 v[106:109], v[82:85], a[248:255], v63\n"
"ds_read_b64_tr_b16 v[228:229], v184 offset:896\n"
"v_mfma_f32_16x16x32_bf16 v[82:85], v[172:175], v[82:85], 0\n"
"ds_read_b64_tr_b16 v[230:231], v185 offset:896\n"
"v_exp_f32 v128, v128\n"
"v_mfma_f32_16x16x32_bf16 v[204:207], a[0:3], v[32:35], 0\n"
"s_waitcnt lgkmcnt(8)\n"
"ds_read_b64_tr_b16 v[244:245], v184 offset:136\n"
"v_exp_f32 v129, v129\n"
"v_mfma_f32_16x16x32_bf16 v[204:207], a[12:15], v[36:39], v[204:207]\n"
"s_waitcnt lgkmcnt(7)\n"
"ds_read_b64_tr_b16 v[246:247], v185 offset:136\n"
"v_exp_f32 v130, v130\n"
"v_mfma_f32_16x16x32_bf16 v[204:207], a[24:27], v[40:43], v[204:207]\n"
"s_waitcnt lgkmcnt(6)\n"
"ds_read_b64_tr_b16 v[248:249], v184 offset:904\n"
"ds_read_b64_tr_b16 v[250:251], v185 offset:904\n"
"v_mfma_f32_16x16x32_bf16 v[204:207], a[36:39], v[44:47], v[204:207]\n"
"s_add_u32 m0, s79, 142464\n"
"v_exp_f32 v131, v131\n"
"v_mfma_f32_16x16x32_bf16 v[204:207], a[48:51], v[48:51], v[204:207]\n"
"buffer_load_dwordx4 v192, s[16:19], s43 offen offset:1024 lds\n"
"ds_read_b128 v[116:119], v178 offset:13504\n"
"v_mfma_f32_16x16x32_bf16 v[204:207], a[60:63], v[52:55], v[204:207]\n"
"ds_read_b64_tr_b16 a[240:241], v236\n"
"v_cvt_pk_bf16_f32 v90, v128, v130\n"
"v_mfma_f32_16x16x32_bf16 v[208:211], a[4:7], v[32:35], 0\n"
"v_cvt_pk_bf16_f32 v91, v129, v131\n"
"v_exp_f32 v132, v132\n"
"v_mfma_f32_16x16x32_bf16 v[208:211], a[16:19], v[36:39], v[208:211]\n"
"ds_read_b128 v[128:131], v179\n"
"v_exp_f32 v133, v133\n"
"v_mfma_f32_16x16x32_bf16 v[208:211], a[28:31], v[40:43], v[208:211]\n"
"v_exp_f32 v134, v134\n"
"v_mfma_f32_16x16x32_bf16 v[208:211], a[40:43], v[44:47], v[208:211]\n"
"v_exp_f32 v135, v135\n"
"v_mfma_f32_16x16x32_bf16 v[208:211], a[52:55], v[48:51], v[208:211]\n"
"v_cvt_pk_bf16_f32 v92, v132, v134\n"
"v_cvt_pk_bf16_f32 v93, v133, v135\n"
"v_mfma_f32_16x16x32_bf16 v[208:211], a[64:67], v[52:55], v[208:211]\n"
"s_waitcnt lgkmcnt(3)\n"
"ds_read_b128 v[132:135], v179 offset:13504\n"
"buffer_load_dword v237, v238, s[8:11], s78 offen\n"
"v_smfmac_f32_16x16x64_bf16 v[252:255], v[90:93], v[224:231], v63\n"
"s_add_u32 s78, s78, s33\n"
"ds_read_b64_tr_b16 a[242:243], v236 offset:6144\n"
"v_smfmac_f32_16x16x64_bf16 v[98:101], v[90:93], v[244:251], v63\n"
"v_cvt_pk_bf16_f32 v158, v212, v213\n"
"v_smfmac_f32_16x16x64_bf16 v[106:109], v[90:93], v[24:31], v63\n"
"s_add_u32 m0, s79, 142464\n"
"v_cvt_pk_bf16_f32 v159, v214, v215\n"
"v_mfma_f32_16x16x32_bf16 v[90:93], v[172:175], v[90:93], v[82:85]\n"
"v_cvt_pk_bf16_f32 v252, v252, v253\n"
"v_cvt_pk_bf16_f32 v253, v254, v255\n"
"v_mfma_f32_16x16x32_bf16 v[212:215], a[8:11], v[32:35], 0\n"
"buffer_load_dwordx4 v192, s[16:19], s43 offen offset:2048 lds\n"
"ds_read_b64 v[82:83], v180 offset:13568\n"
"v_mfma_f32_16x16x32_bf16 v[212:215], a[20:23], v[36:39], v[212:215]\n"
"s_add_u32 s43, s80, 12288\n"
"ds_read_b64 v[84:85], v181 offset:13568\n"
"v_cvt_pk_bf16_f32 v98, v98, v99\n"
"v_mfma_f32_16x16x32_bf16 v[212:215], a[32:35], v[40:43], v[212:215]\n"
"v_rcp_f32 v171, v90\n"
"v_mfma_f32_16x16x32_bf16 v[212:215], a[44:47], v[44:47], v[212:215]\n"
"ds_read_b64 v[90:91], v180 offset:27072\n"
"v_cvt_pk_bf16_f32 v171, v171, v171\n"
"v_mfma_f32_16x16x32_bf16 v[212:215], a[56:59], v[48:51], v[212:215]\n"
"v_and_b32 v92, v171, v176\n"
"v_and_b32 v93, v171, v177\n"
"v_mfma_f32_16x16x32_bf16 v[212:215], a[68:71], v[52:55], v[212:215]\n"
"v_cvt_pk_bf16_f32 v99, v100, v101\n"
"v_cvt_pk_bf16_f32 v106, v106, v107\n"
"v_mfma_f32_16x16x16_bf16 v[252:255], v[252:253], v[92:93], 0\n"
"v_cvt_pk_bf16_f32 v107, v108, v109\n"
"v_cvt_pk_bf16_f32 v204, v204, v205\n"
"v_mfma_f32_16x16x16_bf16 v[98:101], v[98:99], v[92:93], 0\n"
"s_waitcnt lgkmcnt(2)\n"
"v_cvt_pk_bf16_f32 v205, v206, v207\n"
"v_cvt_pk_bf16_f32 v206, v208, v209\n"
"v_mfma_f32_16x16x32_bf16 v[120:123], v[120:123], v[148:151], 0\n"
"s_waitcnt lgkmcnt(1)\n"
"v_cvt_pk_bf16_f32 v252, v252, v253\n"
"v_cvt_pk_bf16_f32 v253, v254, v255\n"
"v_mfma_f32_16x16x32_bf16 v[120:123], v[80:83], v[160:163], v[120:123]\n"
"s_add_u32 m0, s34, 0\n"
"v_cvt_pk_bf16_f32 v254, v98, v99\n"
"v_cvt_pk_bf16_f32 v255, v100, v101\n"
"v_mfma_f32_16x16x32_bf16 v[136:139], v[136:139], v[148:151], 0\n"
"s_waitcnt lgkmcnt(0)\n"
"buffer_load_dwordx4 v190, s[8:11], s32 offen lds\n"
"ds_read_b64 v[98:99], v178 offset:64\n"
"v_mfma_f32_16x16x32_bf16 v[136:139], v[84:87], v[162:165], v[136:139]\n"
"ds_read_b64 v[100:101], v179 offset:64\n"
"ds_read_b64_tr_b16 a[244:245], v236 offset:8\n"
"v_mfma_f32_16x16x32_bf16 v[124:127], v[124:127], v[148:151], 0\n"
"v_cvt_pk_bf16_f32 v207, v210, v211\n"
"v_cvt_pk_bf16_f32 v212, v212, v213\n"
"v_mfma_f32_16x16x32_bf16 v[124:127], v[88:91], v[160:163], v[124:127]\n"
"v_cvt_pk_bf16_f32 v213, v214, v215\n"
"s_mov_b64 exec, s[46:47]\n"
"v_add_u32 v183, 0xc0, v183\n"
"s_mov_b64 exec, -1\n"
"v_mfma_f32_16x16x16_bf16 v[106:109], v[106:107], v[92:93], 0\n"
"buffer_store_dwordx4 v[252:255], v192, s[28:31], s37 offen sc1\n"
"v_mfma_f32_16x16x32_bf16 v[208:211], a[80:83], v[32:35], 0\n"
"ds_read_b64 v[92:93], v181 offset:27072\n"
"ds_write_b128 v186, v[204:207] offset:33760\n"
"v_mfma_f32_16x16x32_bf16 v[208:211], a[92:95], v[36:39], v[208:211]\n"
"s_waitcnt lgkmcnt(3)\n"
"v_max_f32 v120, v120, v136\n"
"v_max_f32 v121, v121, v137\n"
"v_mfma_f32_16x16x32_bf16 v[112:115], v[112:115], v[148:151], 0\n"
"v_cvt_pk_bf16_f32 v106, v106, v107\n"
"v_cvt_pk_bf16_f32 v107, v108, v109\n"
"v_mfma_f32_16x16x32_bf16 v[112:115], v[96:99], v[160:163], v[112:115]\n"
"ds_read_b64 v[108:109], v179 offset:13568\n"
"ds_read_b64_tr_b16 v[136:137], v182 offset:72\n"
"v_mfma_f32_16x16x32_bf16 v[128:131], v[128:131], v[148:151], 0\n"
"v_max_f32 v122, v122, v138\n"
"v_max_f32 v123, v123, v139\n"
"v_mfma_f32_16x16x32_bf16 v[128:131], v[100:103], v[162:165], v[128:131]\n"
"s_add_u32 m0, s34, 3072\n"
"ds_read_b64_tr_b16 v[138:139], v183 offset:72\n"
"v_max3_f32 v170, v120, v121, v122\n"
"v_mfma_f32_16x16x32_bf16 v[208:211], a[104:107], v[40:43], v[208:211]\n"
"s_waitcnt lgkmcnt(4)\n"
"buffer_load_dwordx4 v191, s[8:11], s32 offen lds\n"
"ds_read_b64_tr_b16 a[246:247], v236 offset:6152\n"
"v_mfma_f32_16x16x32_bf16 v[140:143], v[140:143], v[148:151], 0\n"
"s_add_u32 s32, s32, s33\n"
"v_max_f32 v112, v112, v128\n"
"v_max_f32 v113, v113, v129\n"
"v_mfma_f32_16x16x32_bf16 v[140:143], v[92:95], v[162:165], v[140:143]\n"
"ds_read_b64_tr_b16 a[248:249], v235\n"
"v_max_f32 v114, v114, v130\n"
"v_mfma_f32_16x16x32_bf16 v[208:211], a[116:119], v[44:47], v[208:211]\n"
"v_max_f32 v115, v115, v131\n"
"v_mov_b64 v[252:253], 0\n"
"v_mfma_f32_16x16x32_bf16 v[208:211], a[128:131], v[48:51], v[208:211]\n"
"s_waitcnt lgkmcnt(4)\n"
"buffer_store_dwordx2 v[106:107], v193, s[28:31], s37 offen offset:1024 sc1\n"
"v_mfma_f32_16x16x32_bf16 v[132:135], v[132:135], v[148:151], 0\n"
"s_add_u32 s37, s37, 1572864\n"
"ds_read_b64 v[106:107], v178 offset:13568\n"
"ds_read_b128 v[128:131], v180 offset:27008\n"
"v_mfma_f32_16x16x32_bf16 v[132:135], v[108:111], v[162:165], v[132:135]\n"
"v_max_f32 v124, v124, v140\n"
"v_max_f32 v125, v125, v141\n"
"v_mfma_f32_16x16x32_bf16 v[208:211], a[140:143], v[52:55], v[208:211]\n"
"ds_read_b64_tr_b16 v[140:141], v182 offset:840\n"
"v_max_f32 v126, v126, v142\n"
"v_mfma_f32_16x16x32_bf16 v[204:207], a[72:75], v[32:35], 0\n"
"v_max_f32 v127, v127, v143\n"
"v_max3_f32 v170, v170, v123, v124\n"
"v_mfma_f32_16x16x32_bf16 v[204:207], a[84:87], v[36:39], v[204:207]\n"
"s_add_u32 m0, s34, 1024\n"
"ds_read_b64_tr_b16 v[142:143], v183 offset:840\n"
"v_max3_f32 v170, v170, v125, v126\n"
"v_mfma_f32_16x16x32_bf16 v[204:207], a[96:99], v[40:43], v[204:207]\n"
"ds_read_b64_tr_b16 a[250:251], v235 offset:6144\n"
"buffer_load_dwordx4 v190, s[8:11], s0 offen lds\n"
"v_mfma_f32_16x16x32_bf16 v[204:207], a[108:111], v[44:47], v[204:207]\n"
"v_max3_f32 v170, v170, v127, v112\n"
"v_cvt_pk_bf16_f32 v214, v208, v209\n"
"v_mfma_f32_16x16x32_bf16 v[204:207], a[120:123], v[48:51], v[204:207]\n"
"s_waitcnt lgkmcnt(4)\n"
"v_max3_f32 v170, v170, v113, v114\n"
"v_cvt_pk_bf16_f32 v215, v210, v211\n"
"v_mfma_f32_16x16x32_bf16 v[116:119], v[116:119], v[148:151], 0\n"
"ds_write_b128 v186, v[212:215] offset:33824\n"
"v_mfma_f32_16x16x32_bf16 v[116:119], v[104:107], v[160:163], v[116:119]\n"
"buffer_load_dword v200, off, s[28:31], s39 sc1\n"
"v_mov_b64 v[254:255], 0\n"
"v_mfma_f32_16x16x32_bf16 v[204:207], a[132:135], v[52:55], v[204:207]\n"
"v_mov_b64 v[98:99], 0\n"
"v_mov_b64 v[100:101], 0\n"
"v_mfma_f32_16x16x32_bf16 v[208:211], a[76:79], v[32:35], 0\n"
"v_mov_b64 v[106:107], 0\n"
"v_mov_b64 v[108:109], 0\n"
"v_mfma_f32_16x16x32_bf16 v[208:211], a[88:91], v[36:39], v[208:211]\n"
"s_add_u32 m0, s34, 4096\n"
"v_max_f32 v116, v116, v132\n"
"v_max_f32 v117, v117, v133\n"
"v_mfma_f32_16x16x32_bf16 v[208:211], a[100:103], v[40:43], v[208:211]\n"
"buffer_load_dwordx4 v191, s[8:11], s0 offen lds\n"
"v_max_f32 v118, v118, v134\n"
"v_mfma_f32_16x16x32_bf16 v[208:211], a[112:115], v[44:47], v[208:211]\n"
"s_add_u32 s0, s0, s33\n"
"v_max_f32 v119, v119, v135\n"
"v_max3_f32 v170, v170, v115, v116\n"
"v_mfma_f32_16x16x32_bf16 v[208:211], a[124:127], v[48:51], v[208:211]\n"
"ds_read_b128 v[132:135], v178\n"
"v_max3_f32 v170, v170, v117, v118\n"
"v_mfma_f32_16x16x32_bf16 v[208:211], a[136:139], v[52:55], v[208:211]\n"
"v_max_f32 v171, v170, v119\n"
"v_max_f32 v170, v170, v119\n"
"v_mfma_f32_16x16x32_bf16 v[212:215], a[144:147], v[32:35], 0\n"
"s_nop 0\n"
"v_permlane16_swap_b32 v170, v171\n"
"v_mfma_f32_16x16x32_bf16 v[212:215], a[156:159], v[36:39], v[212:215]\n"
"v_max_f32 v170, v170, v171\n"
"v_mov_b32 v171, v170\n"
"v_mfma_f32_16x16x32_bf16 v[212:215], a[168:171], v[40:43], v[212:215]\n"
"s_nop 0\n"
"v_permlane32_swap_b32 v170, v171\n"
"v_max_f32 v170, v170, v171\n"
"v_mul_f32 v170, 0xbe553b95, v170\n"
"v_pk_fma_f32 v[120:121], v[120:121], s[70:71], v[170:171] op_sel_hi:[1,0,0]\n"
"v_pk_fma_f32 v[122:123], v[122:123], s[70:71], v[170:171] op_sel_hi:[1,0,0]\n"
"v_pk_fma_f32 v[124:125], v[124:125], s[70:71], v[170:171] op_sel_hi:[1,0,0]\n"
"v_pk_fma_f32 v[126:127], v[126:127], s[70:71], v[170:171] op_sel_hi:[1,0,0]\n"
"v_pk_fma_f32 v[112:113], v[112:113], s[70:71], v[170:171] op_sel_hi:[1,0,0]\n"
"v_pk_fma_f32 v[114:115], v[114:115], s[70:71], v[170:171] op_sel_hi:[1,0,0]\n"
"v_pk_fma_f32 v[116:117], v[116:117], s[70:71], v[170:171] op_sel_hi:[1,0,0]\n"
"v_pk_fma_f32 v[118:119], v[118:119], s[70:71], v[170:171] op_sel_hi:[1,0,0]\n"
"v_mfma_f32_16x16x32_bf16 v[212:215], a[180:183], v[44:47], v[212:215]\n"
"s_add_u32 m0, s34, 2048\n"
"v_cvt_pk_bf16_f32 v204, v204, v205\n"
"v_cvt_pk_bf16_f32 v205, v206, v207\n"
"v_mfma_f32_16x16x32_bf16 v[212:215], a[192:195], v[48:51], v[212:215]\n"
"buffer_load_dwordx4 v190, s[8:11], s1 offen lds\n"
"v_cvt_pk_bf16_f32 v206, v208, v209\n"
"v_mfma_f32_16x16x32_bf16 v[212:215], a[204:207], v[52:55], v[212:215]\n"
"v_exp_f32 v120, v120\n"
"v_exp_f32 v121, v121\n"
"v_exp_f32 v122, v122\n"
"v_exp_f32 v123, v123\n"
"v_cvt_pk_bf16_f32 v82, v120, v122\n"
"v_cvt_pk_bf16_f32 v83, v121, v123\n"
"v_exp_f32 v124, v124\n"
"v_cvt_pk_bf16_f32 v207, v210, v211\n"
"v_mfma_f32_16x16x32_bf16 v[208:211], a[148:151], v[32:35], 0\n"
"ds_write_b128 v186, v[204:207] offset:33888\n"
"v_mfma_f32_16x16x32_bf16 v[208:211], a[160:163], v[36:39], v[208:211]\n"
"v_exp_f32 v125, v125\n"
"v_mfma_f32_16x16x32_bf16 v[208:211], a[172:175], v[40:43], v[208:211]\n"
"s_add_u32 m0, s34, 5120\n"
"v_exp_f32 v126, v126\n"
"v_mfma_f32_16x16x32_bf16 v[208:211], a[184:187], v[44:47], v[208:211]\n"
"buffer_load_dwordx4 v191, s[8:11], s1 offen lds\n"
"ds_read_b128 v[120:123], v181 offset:27008\n"
"v_mfma_f32_16x16x32_bf16 v[208:211], a[196:199], v[48:51], v[208:211]\n"
"s_add_u32 s1, s1, s33\n"
"v_exp_f32 v127, v127\n"
"v_mfma_f32_16x16x32_bf16 v[208:211], a[208:211], v[52:55], v[208:211]\n"
"v_cvt_pk_bf16_f32 v84, v124, v126\n"
"v_cvt_pk_bf16_f32 v85, v125, v127\n"
"v_mfma_f32_16x16x32_bf16 v[204:207], a[152:155], v[32:35], 0\n"
"s_waitcnt vmcnt(20)\n"
"ds_read_b128 v[32:35], v188 offset:6144\n"
"ds_read_b128 v[124:127], v179\n"
"v_mfma_f32_16x16x32_bf16 v[204:207], a[164:167], v[36:39], v[204:207]\n"
"s_waitcnt vmcnt(9) lgkmcnt(3)\n"
"ds_read_b128 v[36:39], v189 offset:6144\n"
"v_cvt_pk_bf16_f32 v148, v212, v213\n"
"v_mfma_f32_16x16x32_bf16 v[204:207], a[176:179], v[40:43], v[204:207]\n"
"s_barrier\n"
"v_mfma_f32_16x16x32_bf16 v[204:207], a[188:191], v[44:47], v[204:207]\n"
"ds_read_b128 v[40:43], v188 offset:7168\n"
"ds_read_b128 v[44:47], v189 offset:7168\n"
"v_mfma_f32_16x16x32_bf16 v[204:207], a[200:203], v[48:51], v[204:207]\n"
"ds_read_b128 v[48:51], v188 offset:8192\n"
"v_cvt_pk_bf16_f32 v149, v214, v215\n"
"v_mfma_f32_16x16x32_bf16 v[204:207], a[212:215], v[52:55], v[204:207]\n"
"s_add_u32 m0, s79, 130176\n"
"ds_read_b128 v[52:55], v189 offset:8192\n"
"buffer_load_dwordx4 v192, s[16:19], s43 offen lds\n"
"v_smfmac_f32_16x16x64_bf16 v[252:255], v[82:85], v[216:223], v63\n"
"ds_read_b64_tr_b16 v[216:217], v182 offset:128\n"
"v_smfmac_f32_16x16x64_bf16 v[98:101], v[82:85], v[72:79], v63\n"
"s_waitcnt lgkmcnt(7)\n"
"ds_read_b64_tr_b16 v[218:219], v183 offset:128\n"
"v_smfmac_f32_16x16x64_bf16 v[106:109], v[82:85], v[8:15], v63\n"
"ds_read_b64_tr_b16 v[220:221], v182 offset:896\n"
"v_mfma_f32_16x16x32_bf16 v[82:85], v[172:175], v[82:85], 0\n"
"ds_read_b64_tr_b16 v[222:223], v183 offset:896\n"
"v_exp_f32 v112, v112\n"
"v_mfma_f32_16x16x32_bf16 v[212:215], a[0:3], v[32:35], 0\n"
"s_waitcnt lgkmcnt(8)\n"
"ds_read_b64_tr_b16 v[72:73], v182 offset:136\n"
"v_exp_f32 v113, v113\n"
"v_mfma_f32_16x16x32_bf16 v[212:215], a[12:15], v[36:39], v[212:215]\n"
"s_waitcnt lgkmcnt(8)\n"
"ds_read_b64_tr_b16 v[74:75], v183 offset:136\n"
"v_exp_f32 v114, v114\n"
"v_mfma_f32_16x16x32_bf16 v[212:215], a[24:27], v[40:43], v[212:215]\n"
"s_waitcnt lgkmcnt(6)\n"
"ds_read_b64_tr_b16 v[76:77], v182 offset:904\n"
"ds_read_b64_tr_b16 v[78:79], v183 offset:904\n"
"v_mfma_f32_16x16x32_bf16 v[212:215], a[36:39], v[44:47], v[212:215]\n"
"s_add_u32 m0, s79, 130176\n"
"v_exp_f32 v115, v115\n"
"v_mfma_f32_16x16x32_bf16 v[212:215], a[48:51], v[48:51], v[212:215]\n"
"buffer_load_dwordx4 v192, s[16:19], s43 offen offset:1024 lds\n"
"ds_read_b64_tr_b16 a[252:253], v236 offset:12288\n"
"v_mfma_f32_16x16x32_bf16 v[212:215], a[60:63], v[52:55], v[212:215]\n"
"v_exp_f32 v116, v116\n"
"v_exp_f32 v117, v117\n"
"v_exp_f32 v118, v118\n"
"v_exp_f32 v119, v119\n"
"v_cvt_pk_bf16_f32 v90, v112, v114\n"
"v_cvt_pk_bf16_f32 v91, v113, v115\n"
"v_cvt_pk_bf16_f32 v92, v116, v118\n"
"v_cvt_pk_bf16_f32 v93, v117, v119\n"
"v_cvt_pk_bf16_f32 v150, v208, v209\n"
"v_cvt_pk_bf16_f32 v151, v210, v211\n"
"v_smfmac_f32_16x16x64_bf16 v[106:109], v[90:93], v[136:143], v63\n"
"s_waitcnt lgkmcnt(5)\n"
"ds_read_b128 v[112:115], v178 offset:13504\n"
"v_smfmac_f32_16x16x64_bf16 v[252:255], v[90:93], v[216:223], v63\n"
"s_waitcnt lgkmcnt(2)\n"
"ds_read_b128 v[136:139], v179 offset:13504\n"
"v_smfmac_f32_16x16x64_bf16 v[98:101], v[90:93], v[72:79], v63\n"
"ds_read_b128 v[116:119], v178 offset:27008\n"
"v_mfma_f32_16x16x32_bf16 v[90:93], v[172:175], v[90:93], v[82:85]\n"
"s_add_u32 m0, s79, 130176\n"
"ds_read_b128 v[140:143], v179 offset:27008\n"
"buffer_load_dwordx4 v192, s[16:19], s43 offen offset:2048 lds\n"
"v_mfma_f32_16x16x32_bf16 v[208:211], a[4:7], v[32:35], 0\n"
"s_add_u32 s43, s80, 49152\n"
"ds_read_b64 v[82:83], v180 offset:27072\n"
"ds_read_b64 v[84:85], v181 offset:27072\n"
"v_mfma_f32_16x16x32_bf16 v[208:211], a[16:19], v[36:39], v[208:211]\n"
"v_rcp_f32 v171, v90\n"
"v_mfma_f32_16x16x32_bf16 v[208:211], a[28:31], v[40:43], v[208:211]\n"
"ds_read_b64 v[90:91], v178 offset:64\n"
"v_cvt_pk_bf16_f32 v171, v171, v171\n"
"v_mfma_f32_16x16x32_bf16 v[208:211], a[40:43], v[44:47], v[208:211]\n"
"v_cvt_pk_bf16_f32 v252, v252, v253\n"
"v_and_b32 v92, v171, v176\n"
"v_mfma_f32_16x16x32_bf16 v[208:211], a[52:55], v[48:51], v[208:211]\n"
"v_and_b32 v93, v171, v177\n"
"v_cvt_pk_bf16_f32 v253, v254, v255\n"
"v_mfma_f32_16x16x32_bf16 v[208:211], a[64:67], v[52:55], v[208:211]\n"
"v_cvt_pk_bf16_f32 v106, v106, v107\n"
"v_cvt_pk_bf16_f32 v98, v98, v99\n"
"v_mfma_f32_16x16x16_bf16 v[252:255], v[252:253], v[92:93], 0\n"
"v_cvt_pk_bf16_f32 v107, v108, v109\n"
"v_cvt_pk_bf16_f32 v99, v100, v101\n"
"v_cvt_pk_bf16_f32 v162, v204, v205\n"
"v_mfma_f32_16x16x16_bf16 v[106:109], v[106:107], v[92:93], 0\n"
"s_waitcnt lgkmcnt(2)\n"
"buffer_load_dword v237, v238, s[8:11], s78 offen\n"
"ds_read_b64_tr_b16 a[254:255], v236 offset:18432\n"
"v_mfma_f32_16x16x32_bf16 v[128:131], v[128:131], v[152:155], 0\n"
"s_waitcnt lgkmcnt(2)\n"
"v_cvt_pk_bf16_f32 v252, v252, v253\n"
"v_cvt_pk_bf16_f32 v253, v254, v255\n"
"v_mfma_f32_16x16x32_bf16 v[128:131], v[80:83], v[164:167], v[128:131]\n"
"s_waitcnt lgkmcnt(1)\n"
"v_cvt_pk_bf16_f32 v106, v106, v107\n"
"v_cvt_pk_bf16_f32 v107, v108, v109\n"
"v_mfma_f32_16x16x32_bf16 v[120:123], v[120:123], v[152:155], 0\n"
"s_add_u32 m0, s34, 6144\n"
"ds_read_b64 v[108:109], v179 offset:27072\n"
"v_cvt_pk_bf16_f32 v163, v206, v207\n"
"v_mfma_f32_16x16x32_bf16 v[120:123], v[84:87], v[166:169], v[120:123]\n"
"s_add_u32 s78, s78, s33\n"
"buffer_load_dwordx4 v190, s[8:11], s32 offen lds\n"
"v_cvt_pk_bf16_f32 v212, v212, v213\n"
"v_mfma_f32_16x16x16_bf16 v[98:101], v[98:99], v[92:93], 0\n"
"ds_read_b64 v[92:93], v179 offset:64\n"
"v_cvt_pk_bf16_f32 v213, v214, v215\n"
"v_mfma_f32_16x16x32_bf16 v[132:135], v[132:135], v[152:155], 0\n"
"ds_read_b64_tr_b16 v[0:1], v236 offset:12296\n"
"v_cvt_pk_bf16_f32 v214, v208, v209\n"
"v_mfma_f32_16x16x32_bf16 v[132:135], v[88:91], v[164:167], v[132:135]\n"
"v_cvt_pk_bf16_f32 v215, v210, v211\n"
"v_max_f32 v128, v128, v120\n"
"v_mfma_f32_16x16x32_bf16 v[204:207], a[8:11], v[32:35], 0\n"
"buffer_store_dwordx2 v[106:107], v193, s[28:31], s37 offen offset:1024 sc1\n"
"v_mfma_f32_16x16x32_bf16 v[204:207], a[20:23], v[36:39], v[204:207]\n"
"ds_read_b64 v[106:107], v178 offset:27072\n"
"ds_write_b128 v186, v[212:215] offset:47264\n"
"v_mfma_f32_16x16x32_bf16 v[204:207], a[32:35], v[40:43], v[204:207]\n"
"s_waitcnt lgkmcnt(4)\n"
"v_cvt_pk_bf16_f32 v254, v98, v99\n"
"v_cvt_pk_bf16_f32 v255, v100, v101\n"
"v_mfma_f32_16x16x32_bf16 v[204:207], a[44:47], v[44:47], v[204:207]\n"
"s_waitcnt lgkmcnt(3)\n"
"v_max_f32 v129, v129, v121\n"
"v_max_f32 v130, v130, v122\n"
"v_mfma_f32_16x16x32_bf16 v[124:127], v[124:127], v[152:155], 0\n"
"s_add_u32 m0, s34, 9216\n"
"ds_read_b64 v[98:99], v178 offset:13568\n"
"ds_read_b64 v[100:101], v179 offset:13568\n"
"v_mfma_f32_16x16x32_bf16 v[124:127], v[92:95], v[166:169], v[124:127]\n"
"ds_read_b64_tr_b16 v[120:121], v182 offset:13576\n"
"buffer_load_dwordx4 v191, s[8:11], s32 offen lds\n"
"v_mfma_f32_16x16x32_bf16 v[140:143], v[140:143], v[152:155], 0\n"
"s_add_u32 s32, s32, s33\n"
"v_max_f32 v131, v131, v123\n"
"v_max3_f32 v170, v128, v129, v130\n"
"v_mfma_f32_16x16x32_bf16 v[140:143], v[108:111], v[166:169], v[140:143]\n"
"s_waitcnt lgkmcnt(4)\n"
"ds_read_b64_tr_b16 v[122:123], v183 offset:13576\n"
"v_max_f32 v132, v132, v124\n"
"v_mfma_f32_16x16x32_bf16 v[116:119], v[116:119], v[152:155], 0\n"
"ds_read_b64_tr_b16 v[2:3], v236 offset:18440\n"
"v_max_f32 v133, v133, v125\n"
"v_mfma_f32_16x16x32_bf16 v[116:119], v[104:107], v[164:167], v[116:119]\n"
"ds_read_b64_tr_b16 v[124:125], v182 offset:14344\n"
"buffer_store_dwordx4 v[252:255], v192, s[28:31], s37 offen sc1\n"
"v_mfma_f32_16x16x32_bf16 v[204:207], a[56:59], v[48:51], v[204:207]\n"
"s_waitcnt lgkmcnt(5)\n"
"v_max_f32 v134, v134, v126\n"
"v_max_f32 v135, v135, v127\n"
"v_mfma_f32_16x16x32_bf16 v[112:115], v[112:115], v[152:155], 0\n"
"s_waitcnt lgkmcnt(4)\n"
"ds_read_b64_tr_b16 v[126:127], v183 offset:14344\n"
"v_max3_f32 v170, v170, v131, v132\n"
"v_mfma_f32_16x16x32_bf16 v[112:115], v[96:99], v[164:167], v[112:115]\n"
"s_add_u32 s37, s37, 1572864\n"
"v_max3_f32 v170, v170, v133, v134\n"
"v_max_f32 v116, v116, v140\n"
"v_mfma_f32_16x16x32_bf16 v[136:139], v[136:139], v[152:155], 0\n"
"ds_read_b64_tr_b16 v[4:5], v235 offset:12288\n"
"v_max_f32 v117, v117, v141\n"
"v_mfma_f32_16x16x32_bf16 v[136:139], v[100:103], v[166:169], v[136:139]\n"
"ds_read_b64_tr_b16 v[6:7], v235 offset:18432\n"
"v_max_f32 v118, v118, v142\n"
"v_mfma_f32_16x16x32_bf16 v[204:207], a[68:71], v[52:55], v[204:207]\n"
"v_max_f32 v119, v119, v143\n"
"v_mov_b64 v[252:253], 0\n"
"v_mfma_f32_16x16x32_bf16 v[208:211], a[80:83], v[32:35], 0\n"
"s_add_u32 m0, s34, 7168\n"
"ds_read_b128 v[140:143], v178 offset:13504\n"
"v_max_f32 v112, v112, v136\n"
"v_mfma_f32_16x16x32_bf16 v[208:211], a[92:95], v[36:39], v[208:211]\n"
"buffer_load_dwordx4 v190, s[8:11], s0 offen lds\n"
"v_cvt_pk_bf16_f32 v204, v204, v205\n"
"v_mfma_f32_16x16x32_bf16 v[208:211], a[104:107], v[40:43], v[208:211]\n"
"v_cvt_pk_bf16_f32 v205, v206, v207\n"
"v_max_f32 v113, v113, v137\n"
"v_mfma_f32_16x16x32_bf16 v[208:211], a[116:119], v[44:47], v[208:211]\n"
"v_max_f32 v114, v114, v138\n"
"v_max_f32 v115, v115, v139\n"
"v_mfma_f32_16x16x32_bf16 v[208:211], a[128:131], v[48:51], v[208:211]\n"
"ds_read_b128 v[136:139], v178\n"
"v_max3_f32 v170, v170, v135, v112\n"
"v_mfma_f32_16x16x32_bf16 v[208:211], a[140:143], v[52:55], v[208:211]\n"
"v_max3_f32 v170, v170, v113, v114\n"
"v_max3_f32 v170, v170, v115, v116\n"
"v_mfma_f32_16x16x32_bf16 v[212:215], a[72:75], v[32:35], 0\n"
"v_max3_f32 v170, v170, v117, v118\n"
"v_mov_b64 v[254:255], 0\n"
"v_mfma_f32_16x16x32_bf16 v[212:215], a[84:87], v[36:39], v[212:215]\n"
"v_mov_b64 v[98:99], 0\n"
"v_mov_b64 v[100:101], 0\n"
"v_mfma_f32_16x16x32_bf16 v[212:215], a[96:99], v[40:43], v[212:215]\n"
"v_cvt_pk_bf16_f32 v206, v208, v209\n"
"v_cvt_pk_bf16_f32 v207, v210, v211\n"
"v_mfma_f32_16x16x32_bf16 v[212:215], a[108:111], v[44:47], v[212:215]\n"
"s_add_u32 m0, s34, 10240\n"
"ds_write_b128 v186, v[204:207] offset:47328\n"
"v_mfma_f32_16x16x32_bf16 v[212:215], a[120:123], v[48:51], v[212:215]\n"
"buffer_load_dwordx4 v191, s[8:11], s0 offen lds\n"
"v_mov_b64 v[106:107], 0\n"
"v_mfma_f32_16x16x32_bf16 v[212:215], a[132:135], v[52:55], v[212:215]\n"
"s_waitcnt vmcnt(13)\n"
"v_mov_b64 v[108:109], 0\n"
"v_max_f32 v171, v170, v119\n"
"v_mfma_f32_16x16x32_bf16 v[208:211], a[76:79], v[32:35], 0\n"
"s_add_u32 s0, s0, s33\n"
"v_add3_u32 v200, v200, s50, -2\n"
"v_cmp_lt_u32 vcc, 64, v200\n"
"v_mfma_f32_16x16x32_bf16 v[208:211], a[88:91], v[36:39], v[208:211]\n"
"v_max_f32 v170, v170, v119\n"
"v_cvt_pk_bf16_f32 v212, v212, v213\n"
"v_mfma_f32_16x16x32_bf16 v[208:211], a[100:103], v[40:43], v[208:211]\n"
"s_nop 0\n"
"s_cbranch_vccnz .Lp1%=\n"
".Lr1%=:\n"
"v_mfma_f32_16x16x32_bf16 v[208:211], a[112:115], v[44:47], v[208:211]\n"
"buffer_load_dword v201, off, s[28:31], s40 offset:128 sc1\n"
"v_cvt_pk_bf16_f32 v213, v214, v215\n"
"v_mfma_f32_16x16x32_bf16 v[208:211], a[124:127], v[48:51], v[208:211]\n"
"v_permlane16_swap_b32 v170, v171\n"
"v_mfma_f32_16x16x32_bf16 v[208:211], a[136:139], v[52:55], v[208:211]\n"
"buffer_load_dwordx4 v[56:59], v192, s[28:31], s38 offen sc1\n"
"v_max_f32 v170, v170, v171\n"
"v_mfma_f32_16x16x32_bf16 v[204:207], a[144:147], v[32:35], 0\n"
"v_mov_b32 v171, v170\n"
"s_mov_b64 exec, s[44:45]\n"
"v_add_u32 v181, 0xc0, v181\n"
"s_mov_b64 exec, -1\n"
"v_mfma_f32_16x16x32_bf16 v[204:207], a[156:159], v[36:39], v[204:207]\n"
"v_permlane32_swap_b32 v170, v171\n"
"v_max_f32 v170, v170, v171\n"
"v_mul_f32 v170, 0xbe553b95, v170\n"
"v_pk_fma_f32 v[128:129], v[128:129], s[70:71], v[170:171] op_sel_hi:[1,0,0]\n"
"v_pk_fma_f32 v[130:131], v[130:131], s[70:71], v[170:171] op_sel_hi:[1,0,0]\n"
"v_pk_fma_f32 v[132:133], v[132:133], s[70:71], v[170:171] op_sel_hi:[1,0,0]\n"
"v_pk_fma_f32 v[134:135], v[134:135], s[70:71], v[170:171] op_sel_hi:[1,0,0]\n"
"v_pk_fma_f32 v[112:113], v[112:113], s[70:71], v[170:171] op_sel_hi:[1,0,0]\n"
"v_pk_fma_f32 v[114:115], v[114:115], s[70:71], v[170:171] op_sel_hi:[1,0,0]\n"
"v_pk_fma_f32 v[116:117], v[116:117], s[70:71], v[170:171] op_sel_hi:[1,0,0]\n"
"v_pk_fma_f32 v[118:119], v[118:119], s[70:71], v[170:171] op_sel_hi:[1,0,0]\n"
"v_mfma_f32_16x16x32_bf16 v[204:207], a[168:171], v[40:43], v[204:207]\n"
"s_add_u32 m0, s34, 8192\n"
"buffer_load_dwordx2 v[60:61], v193, s[28:31], s38 offen offset:1024 sc1\n"
"v_cvt_pk_bf16_f32 v214, v208, v209\n"
"v_mfma_f32_16x16x32_bf16 v[204:207], a[180:183], v[44:47], v[204:207]\n"
"s_add_u32 s38, s38, 1572864\n"
"v_exp_f32 v128, v128\n"
"v_mfma_f32_16x16x32_bf16 v[204:207], a[192:195], v[48:51], v[204:207]\n"
"v_exp_f32 v129, v129\n"
"v_mfma_f32_16x16x32_bf16 v[204:207], a[204:207], v[52:55], v[204:207]\n"
"v_exp_f32 v130, v130\n"
"v_exp_f32 v131, v131\n"
"buffer_load_dwordx4 v190, s[8:11], s1 offen lds\n"
"v_cvt_pk_bf16_f32 v82, v128, v130\n"
"v_cvt_pk_bf16_f32 v83, v129, v131\n"
"v_exp_f32 v132, v132\n"
"v_cvt_pk_bf16_f32 v215, v210, v211\n"
"v_mfma_f32_16x16x32_bf16 v[208:211], a[148:151], v[32:35], 0\n"
"ds_write_b128 v186, v[212:215] offset:47392\n"
"v_mfma_f32_16x16x32_bf16 v[208:211], a[160:163], v[36:39], v[208:211]\n"
"v_exp_f32 v133, v133\n"
"v_mfma_f32_16x16x32_bf16 v[208:211], a[172:175], v[40:43], v[208:211]\n"
"v_exp_f32 v134, v134\n"
"v_mfma_f32_16x16x32_bf16 v[208:211], a[184:187], v[44:47], v[208:211]\n"
"ds_read_b128 v[128:131], v179\n"
"v_cvt_pk_bf16_f32 v84, v132, v134\n"
"v_mfma_f32_16x16x32_bf16 v[208:211], a[196:199], v[48:51], v[208:211]\n"
"v_exp_f32 v135, v135\n"
"v_mfma_f32_16x16x32_bf16 v[208:211], a[208:211], v[52:55], v[208:211]\n"
"s_add_u32 m0, s34, 11264\n"
"v_cvt_pk_bf16_f32 v85, v133, v135\n"
"v_cvt_pk_bf16_f32 v152, v204, v205\n"
"v_mfma_f32_16x16x32_bf16 v[212:215], a[152:155], v[32:35], 0\n"
"buffer_load_dwordx4 v191, s[8:11], s1 offen lds\n"
"ds_read_b128 v[32:35], v188\n"
"v_mfma_f32_16x16x32_bf16 v[212:215], a[164:167], v[36:39], v[212:215]\n"
"s_add_u32 s1, s1, s33\n"
"ds_read_b128 v[36:39], v189\n"
"ds_read_b128 v[132:135], v179 offset:13504\n"
"v_mfma_f32_16x16x32_bf16 v[212:215], a[176:179], v[40:43], v[212:215]\n"
"s_waitcnt vmcnt(12) lgkmcnt(4)\n"
"v_exp_f32 v112, v112\n"
"v_mfma_f32_16x16x32_bf16 v[212:215], a[188:191], v[44:47], v[212:215]\n"
"s_barrier\n"
"v_mfma_f32_16x16x32_bf16 v[212:215], a[200:203], v[48:51], v[212:215]\n"
"ds_read_b128 v[40:43], v188 offset:1024\n"
"ds_read_b128 v[44:47], v189 offset:1024\n"
"v_mfma_f32_16x16x32_bf16 v[212:215], a[212:215], v[52:55], v[212:215]\n"
"ds_read_b128 v[48:51], v188 offset:2048\n"
"v_cvt_pk_bf16_f32 v153, v206, v207\n"
"v_smfmac_f32_16x16x64_bf16 v[252:255], v[82:85], v[224:231], v63\n"
"ds_read_b128 v[52:55], v189 offset:2048\n"
"v_smfmac_f32_16x16x64_bf16 v[98:101], v[82:85], v[244:251], v63\n"
"s_waitcnt lgkmcnt(6)\n"
"ds_read_b64_tr_b16 v[224:225], v182 offset:13632\n"
"v_smfmac_f32_16x16x64_bf16 v[106:109], v[82:85], v[24:31], v63\n"
"s_waitcnt lgkmcnt(6)\n"
"ds_read_b64_tr_b16 v[226:227], v183 offset:13632\n"
"v_mfma_f32_16x16x32_bf16 v[82:85], v[172:175], v[82:85], 0\n"
"s_add_u32 m0, s79, 142464\n"
"ds_read_b64_tr_b16 v[228:229], v182 offset:14400\n"
"buffer_load_dwordx4 v192, s[16:19], s43 offen lds\n"
"v_mfma_f32_16x16x32_bf16 v[204:207], a[0:3], v[32:35], 0\n"
"ds_read_b64_tr_b16 v[230:231], v183 offset:14400\n"
"v_cvt_pk_bf16_f32 v154, v208, v209\n"
"v_mfma_f32_16x16x32_bf16 v[204:207], a[12:15], v[36:39], v[204:207]\n"
"s_waitcnt lgkmcnt(7)\n"
"ds_read_b64_tr_b16 v[244:245], v182 offset:13640\n"
"v_cvt_pk_bf16_f32 v155, v210, v211\n"
"v_mfma_f32_16x16x32_bf16 v[204:207], a[24:27], v[40:43], v[204:207]\n"
"s_waitcnt lgkmcnt(7)\n"
"ds_read_b64_tr_b16 v[246:247], v183 offset:13640\n"
"v_exp_f32 v113, v113\n"
"v_mfma_f32_16x16x32_bf16 v[204:207], a[36:39], v[44:47], v[204:207]\n"
"s_waitcnt lgkmcnt(7)\n"
"ds_read_b64_tr_b16 v[248:249], v182 offset:14408\n"
"v_exp_f32 v114, v114\n"
"v_mfma_f32_16x16x32_bf16 v[204:207], a[48:51], v[48:51], v[204:207]\n"
"s_waitcnt lgkmcnt(7)\n"
"ds_read_b64_tr_b16 v[250:251], v183 offset:14408\n"
"v_cvt_pk_bf16_f32 v90, v112, v114\n"
"v_mfma_f32_16x16x32_bf16 v[204:207], a[60:63], v[52:55], v[204:207]\n"
".Lic3%=:\n"
"s_cbranch_execz .Lib0%=\n"
"ds_read_b64_tr_b16 v[8:9], v236\n"
"v_exp_f32 v115, v115\n"
"v_mfma_f32_16x16x32_bf16 v[208:211], a[4:7], v[32:35], 0\n"
"s_add_u32 m0, s79, 142464\n"
"v_cvt_pk_bf16_f32 v91, v113, v115\n"
"v_exp_f32 v116, v116\n"
"v_mfma_f32_16x16x32_bf16 v[208:211], a[16:19], v[36:39], v[208:211]\n"
"buffer_load_dwordx4 v192, s[16:19], s43 offen offset:1024 lds\n"
"ds_read_b128 v[112:115], v178 offset:27008\n"
"v_mfma_f32_16x16x32_bf16 v[208:211], a[28:31], v[40:43], v[208:211]\n"
"v_exp_f32 v117, v117\n"
"v_mfma_f32_16x16x32_bf16 v[208:211], a[40:43], v[44:47], v[208:211]\n"
"v_exp_f32 v118, v118\n"
"v_mfma_f32_16x16x32_bf16 v[208:211], a[52:55], v[48:51], v[208:211]\n"
"v_exp_f32 v119, v119\n"
"v_mfma_f32_16x16x32_bf16 v[208:211], a[64:67], v[52:55], v[208:211]\n"
"v_cvt_pk_bf16_f32 v92, v116, v118\n"
"v_cvt_pk_bf16_f32 v93, v117, v119\n"
"v_cvt_pk_bf16_f32 v166, v212, v213\n"
"v_cvt_pk_bf16_f32 v167, v214, v215\n"
"v_smfmac_f32_16x16x64_bf16 v[106:109], v[90:93], v[120:127], v63\n"
"s_waitcnt lgkmcnt(2)\n"
"ds_read_b128 v[120:123], v179 offset:27008\n"
"v_smfmac_f32_16x16x64_bf16 v[252:255], v[90:93], v[224:231], v63\n"
"ds_read_b128 v[116:119], v180\n"
"v_smfmac_f32_16x16x64_bf16 v[98:101], v[90:93], v[244:251], v63\n"
"s_add_u32 m0, s79, 142464\n"
"s_nop 0\n"
"buffer_load_dwordx4 v192, s[16:19], s43 offen offset:2048 lds\n"
"v_mfma_f32_16x16x32_bf16 v[90:93], v[172:175], v[90:93], v[82:85]\n"
"ds_read_b128 v[124:127], v181\n"
"ds_read_b64_tr_b16 v[10:11], v236 offset:6144\n"
"v_mfma_f32_16x16x32_bf16 v[212:215], a[8:11], v[32:35], 0\n"
"v_cvt_pk_bf16_f32 v252, v252, v253\n"
"v_cvt_pk_bf16_f32 v253, v254, v255\n"
"v_mfma_f32_16x16x32_bf16 v[212:215], a[20:23], v[36:39], v[212:215]\n"
"ds_read_b64 v[82:83], v178 offset:64\n"
"v_cvt_pk_bf16_f32 v98, v98, v99\n"
"v_mfma_f32_16x16x32_bf16 v[212:215], a[32:35], v[40:43], v[212:215]\n"
"ds_read_b64 v[84:85], v179 offset:64\n"
"v_cvt_pk_bf16_f32 v99, v100, v101\n"
"v_mfma_f32_16x16x32_bf16 v[212:215], a[44:47], v[44:47], v[212:215]\n"
"v_rcp_f32 v171, v90\n"
"v_mfma_f32_16x16x32_bf16 v[212:215], a[56:59], v[48:51], v[212:215]\n"
"ds_read_b64 v[90:91], v178 offset:13568\n"
"v_cvt_pk_bf16_f32 v171, v171, v171\n"
"v_mfma_f32_16x16x32_bf16 v[212:215], a[68:71], v[52:55], v[212:215]\n"
"v_and_b32 v92, v171, v176\n"
"s_add_u32 m0, s34, 0\n"
"v_and_b32 v93, v171, v177\n"
"v_cvt_pk_bf16_f32 v106, v106, v107\n"
"v_cvt_pk_bf16_f32 v107, v108, v109\n"
"v_mfma_f32_16x16x16_bf16 v[252:255], v[252:253], v[92:93], 0\n"
"buffer_load_dwordx4 v190, s[8:11], s32 offen lds\n"
"v_cvt_pk_bf16_f32 v204, v204, v205\n"
"v_mfma_f32_16x16x16_bf16 v[98:101], v[98:99], v[92:93], 0\n"
"v_cvt_pk_bf16_f32 v205, v206, v207\n"
"v_cvt_pk_bf16_f32 v206, v208, v209\n"
"v_mfma_f32_16x16x16_bf16 v[106:109], v[106:107], v[92:93], 0\n"
"s_waitcnt lgkmcnt(2)\n"
"ds_read_b64 v[92:93], v179 offset:13568\n"
"v_cvt_pk_bf16_f32 v252, v252, v253\n"
"v_mfma_f32_16x16x32_bf16 v[136:139], v[136:139], v[144:147], 0\n"
"s_waitcnt lgkmcnt(2)\n"
"v_cvt_pk_bf16_f32 v253, v254, v255\n"
"v_cvt_pk_bf16_f32 v254, v98, v99\n"
"v_mfma_f32_16x16x32_bf16 v[136:139], v[80:83], v[156:159], v[136:139]\n"
"s_waitcnt lgkmcnt(1)\n"
"ds_read_b64 v[98:99], v178 offset:27072\n"
"v_cvt_pk_bf16_f32 v255, v100, v101\n"
"v_mfma_f32_16x16x32_bf16 v[128:131], v[128:131], v[144:147], 0\n"
"buffer_store_dwordx4 v[252:255], v192, s[28:31], s37 offen sc1\n"
"v_mfma_f32_16x16x32_bf16 v[128:131], v[84:87], v[158:161], v[128:131]\n"
"ds_read_b64 v[100:101], v179 offset:27072\n"
"v_cvt_pk_bf16_f32 v106, v106, v107\n"
"v_mfma_f32_16x16x32_bf16 v[140:143], v[140:143], v[144:147], 0\n"
"v_cvt_pk_bf16_f32 v107, v108, v109\n"
"v_cvt_pk_bf16_f32 v207, v210, v211\n"
"v_mfma_f32_16x16x32_bf16 v[140:143], v[88:91], v[156:159], v[140:143]\n"
"s_waitcnt lgkmcnt(2)\n"
"ds_read_b64 v[108:109], v181 offset:64\n"
"ds_write_b128 v186, v[204:207] offset:60768\n"
"v_mfma_f32_16x16x32_bf16 v[132:135], v[132:135], v[144:147], 0\n"
"s_waitcnt lgkmcnt(3)\n"
"v_cvt_pk_bf16_f32 v212, v212, v213\n"
"v_cvt_pk_bf16_f32 v213, v214, v215\n"
"v_mfma_f32_16x16x32_bf16 v[132:135], v[92:95], v[158:161], v[132:135]\n"
"v_max_f32 v136, v136, v128\n"
"v_max_f32 v137, v137, v129\n"
"v_mfma_f32_16x16x32_bf16 v[112:115], v[112:115], v[144:147], 0\n"
"s_add_u32 m0, s34, 3072\n"
"ds_read_b64_tr_b16 v[128:129], v182 offset:72\n"
"ds_read_b64_tr_b16 v[12:13], v236 offset:8\n"
"v_mfma_f32_16x16x32_bf16 v[112:115], v[96:99], v[156:159], v[112:115]\n"
"s_waitcnt lgkmcnt(4)\n"
"buffer_load_dwordx4 v191, s[8:11], s32 offen lds\n"
"v_max_f32 v138, v138, v130\n"
"v_mfma_f32_16x16x32_bf16 v[120:123], v[120:123], v[144:147], 0\n"
"s_add_u32 s32, s32, s33\n"
"v_max_f32 v139, v139, v131\n"
"v_max3_f32 v170, v136, v137, v138\n"
"v_mfma_f32_16x16x32_bf16 v[120:123], v[100:103], v[158:161], v[120:123]\n"
"s_waitcnt lgkmcnt(3)\n"
"ds_read_b64_tr_b16 v[130:131], v183 offset:72\n"
"ds_read_b64_tr_b16 v[14:15], v236 offset:6152\n"
"v_mfma_f32_16x16x32_bf16 v[124:127], v[124:127], v[144:147], 0\n"
"v_max_f32 v140, v140, v132\n"
"v_max_f32 v141, v141, v133\n"
"v_mfma_f32_16x16x32_bf16 v[124:127], v[108:111], v[158:161], v[124:127]\n"
"buffer_store_dwordx2 v[106:107], v193, s[28:31], s37 offen offset:1024 sc1\n"
"v_mfma_f32_16x16x32_bf16 v[208:211], a[80:83], v[32:35], 0\n"
"s_add_u32 s37, s37, 1572864\n"
"ds_read_b64 v[106:107], v180 offset:64\n"
"ds_read_b64_tr_b16 v[132:133], v182 offset:840\n"
"v_mfma_f32_16x16x32_bf16 v[208:211], a[92:95], v[36:39], v[208:211]\n"
"s_add_u32 s4, s37, 7864320\n"
"v_max_f32 v142, v142, v134\n"
"v_max_f32 v143, v143, v135\n"
"v_mfma_f32_16x16x32_bf16 v[208:211], a[104:107], v[40:43], v[208:211]\n"
"ds_read_b64_tr_b16 v[134:135], v183 offset:840\n"
"v_max3_f32 v170, v170, v139, v140\n"
"v_mfma_f32_16x16x32_bf16 v[208:211], a[116:119], v[44:47], v[208:211]\n"
"v_max3_f32 v170, v170, v141, v142\n"
"v_max_f32 v112, v112, v120\n"
"v_mfma_f32_16x16x32_bf16 v[208:211], a[128:131], v[48:51], v[208:211]\n"
"s_add_u32 m0, s34, 1024\n"
"ds_read_b64_tr_b16 v[16:17], v235\n"
"v_max_f32 v113, v113, v121\n"
"v_mfma_f32_16x16x32_bf16 v[208:211], a[140:143], v[52:55], v[208:211]\n"
"ds_read_b64_tr_b16 v[120:121], v182 offset:27080\n"
"buffer_load_dwordx4 v190, s[8:11], s0 offen lds\n"
"v_mfma_f32_16x16x32_bf16 v[204:207], a[72:75], v[32:35], 0\n"
"ds_read_b64_tr_b16 v[18:19], v235 offset:6144\n"
"v_max_f32 v114, v114, v122\n"
"v_mfma_f32_16x16x32_bf16 v[204:207], a[84:87], v[36:39], v[204:207]\n"
"s_waitcnt lgkmcnt(5)\n"
"v_mov_b64 v[64:65], -1\n"
"v_mov_b64 v[66:67], -1\n"
"v_mfma_f32_16x16x32_bf16 v[116:119], v[116:119], v[144:147], 0\n"
"v_cvt_pk_bf16_f32 v214, v208, v209\n"
"v_cvt_pk_bf16_f32 v215, v210, v211\n"
"v_mfma_f32_16x16x32_bf16 v[116:119], v[104:107], v[156:159], v[116:119]\n"
"ds_write_b128 v186, v[212:215] offset:60832\n"
"v_mfma_f32_16x16x32_bf16 v[204:207], a[96:99], v[40:43], v[204:207]\n"
"buffer_store_dwordx4 v[64:67], v192, s[28:31], s4 offen sc1\n"
"v_mfma_f32_16x16x32_bf16 v[204:207], a[108:111], v[44:47], v[204:207]\n"
"v_max_f32 v115, v115, v123\n"
"v_max3_f32 v170, v170, v143, v112\n"
"v_mfma_f32_16x16x32_bf16 v[204:207], a[120:123], v[48:51], v[204:207]\n"
"ds_read_b64_tr_b16 v[122:123], v183 offset:27080\n"
"v_max3_f32 v170, v170, v113, v114\n"
"v_mfma_f32_16x16x32_bf16 v[204:207], a[132:135], v[52:55], v[204:207]\n"
"v_max_f32 v116, v116, v124\n"
"v_max_f32 v117, v117, v125\n"
"v_mfma_f32_16x16x32_bf16 v[208:211], a[76:79], v[32:35], 0\n"
"ds_read_b64_tr_b16 v[124:125], v182 offset:27848\n"
"v_max_f32 v118, v118, v126\n"
"v_mfma_f32_16x16x32_bf16 v[208:211], a[88:91], v[36:39], v[208:211]\n"
"v_max_f32 v119, v119, v127\n"
"v_max3_f32 v170, v170, v115, v116\n"
"v_mfma_f32_16x16x32_bf16 v[208:211], a[100:103], v[40:43], v[208:211]\n"
"ds_read_b64_tr_b16 v[126:127], v183 offset:27848\n"
"v_max3_f32 v170, v170, v117, v118\n"
"v_mfma_f32_16x16x32_bf16 v[208:211], a[112:115], v[44:47], v[208:211]\n"
"s_add_u32 m0, s34, 4096\n"
"v_mov_b64 v[252:253], 0\n"
"v_mov_b64 v[254:255], 0\n"
"v_mfma_f32_16x16x32_bf16 v[208:211], a[124:127], v[48:51], v[208:211]\n"
"buffer_load_dwordx4 v191, s[8:11], s0 offen lds\n"
"v_mov_b64 v[98:99], 0\n"
"v_mfma_f32_16x16x32_bf16 v[208:211], a[136:139], v[52:55], v[208:211]\n"
"s_waitcnt vmcnt(12)\n"
"v_readfirstlane_b32 s6, v201\n"
"v_mov_b64 v[100:101], 0\n"
"v_mfma_f32_16x16x32_bf16 v[212:215], a[144:147], v[32:35], 0\n"
"s_and_b32 s6, s6, 0xffffff00\n"
"v_or_b32 v201, 5, s6\n"
"v_mov_b64 v[106:107], 0\n"
"v_mfma_f32_16x16x32_bf16 v[212:215], a[156:159], v[36:39], v[212:215]\n"
"s_add_u32 s0, s0, s33\n"
"ds_write_b128 v202, v[56:59] offset:55296\n"
"v_mfma_f32_16x16x32_bf16 v[212:215], a[168:171], v[40:43], v[212:215]\n"
"s_mov_b64 exec, 1\n"
"buffer_store_dword v201, off, s[28:31], s40 sc1\n"
"s_mov_b64 exec, -1\n"
"v_mfma_f32_16x16x32_bf16 v[212:215], a[180:183], v[44:47], v[212:215]\n"
"ds_write_b64 v203, v[60:61] offset:55296\n"
"v_mfma_f32_16x16x32_bf16 v[212:215], a[192:195], v[48:51], v[212:215]\n"
"s_add_u32 m0, s34, 2048\n"
"buffer_load_dwordx4 v[56:59], v192, s[28:31], s38 offen sc1\n"
"v_mov_b64 v[108:109], 0\n"
"v_mfma_f32_16x16x32_bf16 v[212:215], a[204:207], v[52:55], v[212:215]\n"
"v_max_f32 v171, v170, v119\n"
"v_max_f32 v170, v170, v119\n"
"v_cvt_pk_bf16_f32 v204, v204, v205\n"
"v_cvt_pk_bf16_f32 v205, v206, v207\n"
"v_permlane16_swap_b32 v170, v171\n"
"v_max_f32 v170, v170, v171\n"
"v_mov_b32 v171, v170\n"
"v_cvt_pk_bf16_f32 v206, v208, v209\n"
"v_cvt_pk_bf16_f32 v207, v210, v211\n"
"v_mfma_f32_16x16x32_bf16 v[208:211], a[148:151], v[32:35], 0\n"
"buffer_load_dwordx4 v190, s[8:11], s1 offen lds\n"
"ds_write_b128 v186, v[204:207] offset:60896\n"
"v_mfma_f32_16x16x32_bf16 v[208:211], a[160:163], v[36:39], v[208:211]\n"
"v_permlane32_swap_b32 v170, v171\n"
"v_max_f32 v170, v170, v171\n"
"v_mul_f32 v170, 0xbe553b95, v170\n"
"v_pk_fma_f32 v[136:137], v[136:137], s[70:71], v[170:171] op_sel_hi:[1,0,0]\n"
"v_pk_fma_f32 v[138:139], v[138:139], s[70:71], v[170:171] op_sel_hi:[1,0,0]\n"
"v_pk_fma_f32 v[140:141], v[140:141], s[70:71], v[170:171] op_sel_hi:[1,0,0]\n"
"v_pk_fma_f32 v[142:143], v[142:143], s[70:71], v[170:171] op_sel_hi:[1,0,0]\n"
"v_pk_fma_f32 v[112:113], v[112:113], s[70:71], v[170:171] op_sel_hi:[1,0,0]\n"
"v_pk_fma_f32 v[114:115], v[114:115], s[70:71], v[170:171] op_sel_hi:[1,0,0]\n"
"v_pk_fma_f32 v[116:117], v[116:117], s[70:71], v[170:171] op_sel_hi:[1,0,0]\n"
"v_pk_fma_f32 v[118:119], v[118:119], s[70:71], v[170:171] op_sel_hi:[1,0,0]\n"
"v_mfma_f32_16x16x32_bf16 v[208:211], a[172:175], v[40:43], v[208:211]\n"
"buffer_load_dwordx2 v[60:61], v193, s[28:31], s38 offen offset:1024 sc1\n"
"v_exp_f32 v136, v136\n"
"v_mfma_f32_16x16x32_bf16 v[208:211], a[184:187], v[44:47], v[208:211]\n"
"s_add_u32 s38, s38, 1572864\n"
"v_exp_f32 v137, v137\n"
"v_mfma_f32_16x16x32_bf16 v[208:211], a[196:199], v[48:51], v[208:211]\n"
"v_exp_f32 v138, v138\n"
"v_mfma_f32_16x16x32_bf16 v[208:211], a[208:211], v[52:55], v[208:211]\n"
"s_add_u32 m0, s34, 5120\n"
"v_exp_f32 v139, v139\n"
"v_mfma_f32_16x16x32_bf16 v[204:207], a[152:155], v[32:35], 0\n"
"buffer_load_dwordx4 v191, s[8:11], s1 offen lds\n"
"ds_read_b128 v[32:35], v188 offset:6144\n"
"v_mfma_f32_16x16x32_bf16 v[204:207], a[164:167], v[36:39], v[204:207]\n"
"s_add_u32 s1, s1, s33\n"
"ds_read_b128 v[36:39], v189 offset:6144\n"
"v_cvt_pk_bf16_f32 v82, v136, v138\n"
"v_mfma_f32_16x16x32_bf16 v[204:207], a[176:179], v[40:43], v[204:207]\n"
"ds_read_b128 v[40:43], v188 offset:7168\n"
"v_cvt_pk_bf16_f32 v83, v137, v139\n"
"v_mfma_f32_16x16x32_bf16 v[204:207], a[188:191], v[44:47], v[204:207]\n"
"ds_read_b128 v[44:47], v189 offset:7168\n"
"ds_read_b128 v[136:139], v178 offset:13504\n"
"v_mfma_f32_16x16x32_bf16 v[204:207], a[200:203], v[48:51], v[204:207]\n"
"v_exp_f32 v140, v140\n"
"v_mfma_f32_16x16x32_bf16 v[204:207], a[212:215], v[52:55], v[204:207]\n"
"v_exp_f32 v141, v141\n"
"v_exp_f32 v142, v142\n"
"s_waitcnt vmcnt(12) lgkmcnt(5)\n"
"ds_read_b128 v[48:51], v188 offset:8192\n"
"ds_read_b128 v[52:55], v189 offset:8192\n"
"v_exp_f32 v143, v143\n"
"v_cvt_pk_bf16_f32 v84, v140, v142\n"
"v_cvt_pk_bf16_f32 v85, v141, v143\n"
"v_cvt_pk_bf16_f32 v144, v212, v213\n"
"v_cvt_pk_bf16_f32 v145, v214, v215\n"
"v_smfmac_f32_16x16x64_bf16 v[252:255], v[82:85], v[216:223], v63\n"
"s_waitcnt lgkmcnt(5)\n"
"ds_read_b64_tr_b16 v[216:217], v182 offset:27136\n"
"v_smfmac_f32_16x16x64_bf16 v[98:101], v[82:85], v[72:79], v63\n"
"s_waitcnt lgkmcnt(4)\n"
"ds_read_b64_tr_b16 v[218:219], v183 offset:27136\n"
"v_smfmac_f32_16x16x64_bf16 v[106:109], v[82:85], v[128:135], v63\n"
"s_add_u32 m0, s34, 6144\n"
"s_nop 0\n"
"buffer_load_dwordx4 v190, s[8:11], s32 offen lds\n"
"v_mfma_f32_16x16x32_bf16 v[82:85], v[172:175], v[82:85], 0\n"
"s_barrier\n"
"v_mfma_f32_16x16x32_bf16 v[212:215], a[0:3], v[32:35], 0\n"
"ds_read_b64_tr_b16 v[220:221], v182 offset:27904\n"
"ds_read_b64_tr_b16 v[222:223], v183 offset:27904\n"
"v_mfma_f32_16x16x32_bf16 v[212:215], a[12:15], v[36:39], v[212:215]\n"
"ds_read_b128 v[128:131], v179 offset:13504\n"
"v_cvt_pk_bf16_f32 v146, v208, v209\n"
"v_mfma_f32_16x16x32_bf16 v[212:215], a[24:27], v[40:43], v[212:215]\n"
"ds_read_b64_tr_b16 v[72:73], v182 offset:27144\n"
"v_cvt_pk_bf16_f32 v147, v210, v211\n"
"v_mfma_f32_16x16x32_bf16 v[212:215], a[36:39], v[44:47], v[212:215]\n"
"s_waitcnt lgkmcnt(7)\n"
"ds_read_b64_tr_b16 v[74:75], v183 offset:27144\n"
"v_exp_f32 v112, v112\n"
"v_mfma_f32_16x16x32_bf16 v[212:215], a[48:51], v[48:51], v[212:215]\n"
"s_waitcnt lgkmcnt(7)\n"
"ds_read_b64_tr_b16 v[76:77], v182 offset:27912\n"
"v_exp_f32 v113, v113\n"
"v_mfma_f32_16x16x32_bf16 v[212:215], a[60:63], v[52:55], v[212:215]\n"
"s_add_u32 m0, s34, 9216\n"
"ds_read_b64_tr_b16 v[78:79], v183 offset:27912\n"
"buffer_load_dwordx4 v191, s[8:11], s32 offen lds\n"
"v_mfma_f32_16x16x32_bf16 v[208:211], a[4:7], v[32:35], 0\n"
"s_add_u32 s32, s32, s33\n"
"ds_read_b128 v[140:143], v178 offset:27008\n"
"v_exp_f32 v114, v114\n"
"v_mfma_f32_16x16x32_bf16 v[208:211], a[16:19], v[36:39], v[208:211]\n"
"ds_read_b128 v[132:135], v179 offset:27008\n"
"ds_read_b64_tr_b16 v[20:21], v236 offset:12288\n"
"v_mfma_f32_16x16x32_bf16 v[208:211], a[28:31], v[40:43], v[208:211]\n"
"v_exp_f32 v115, v115\n"
"v_mfma_f32_16x16x32_bf16 v[208:211], a[40:43], v[44:47], v[208:211]\n"
"v_cvt_pk_bf16_f32 v90, v112, v114\n"
"v_cvt_pk_bf16_f32 v91, v113, v115\n"
"v_mfma_f32_16x16x32_bf16 v[208:211], a[52:55], v[48:51], v[208:211]\n"
"ds_read_b128 v[112:115], v180\n"
"ds_read_b64_tr_b16 v[22:23], v236 offset:18432\n"
"v_mfma_f32_16x16x32_bf16 v[208:211], a[64:67], v[52:55], v[208:211]\n"
"v_exp_f32 v116, v116\n"
"v_exp_f32 v117, v117\n"
"s_add_u32 m0, s34, 7168\n"
"v_exp_f32 v118, v118\n"
"buffer_load_dwordx4 v190, s[8:11], s0 offen lds\n"
"v_exp_f32 v119, v119\n"
"v_cvt_pk_bf16_f32 v92, v116, v118\n"
"v_cvt_pk_bf16_f32 v93, v117, v119\n"
"v_cvt_pk_bf16_f32 v158, v204, v205\n"
"v_cvt_pk_bf16_f32 v159, v206, v207\n"
"v_smfmac_f32_16x16x64_bf16 v[106:109], v[90:93], v[120:127], v63\n"
"s_waitcnt lgkmcnt(5)\n"
"ds_read_b128 v[120:123], v181\n"
"v_smfmac_f32_16x16x64_bf16 v[252:255], v[90:93], v[216:223], v63\n"
"ds_read_b128 v[116:119], v180 offset:13504\n"
"v_smfmac_f32_16x16x64_bf16 v[98:101], v[90:93], v[72:79], v63\n"
"ds_read_b128 v[124:127], v181 offset:13504\n"
"v_mfma_f32_16x16x32_bf16 v[90:93], v[172:175], v[90:93], v[82:85]\n"
"ds_read_b64_tr_b16 v[24:25], v236 offset:12296\n"
"v_cvt_pk_bf16_f32 v106, v106, v107\n"
"v_mfma_f32_16x16x32_bf16 v[204:207], a[8:11], v[32:35], 0\n"
"v_cvt_pk_bf16_f32 v107, v108, v109\n"
"v_cvt_pk_bf16_f32 v252, v252, v253\n"
"v_mfma_f32_16x16x32_bf16 v[204:207], a[20:23], v[36:39], v[204:207]\n"
"s_add_u32 m0, s34, 10240\n"
"ds_read_b64 v[82:83], v178 offset:13568\n"
"buffer_load_dwordx4 v191, s[8:11], s0 offen lds\n"
"v_mfma_f32_16x16x32_bf16 v[204:207], a[32:35], v[40:43], v[204:207]\n"
"s_add_u32 s0, s0, s33\n"
"ds_read_b64 v[84:85], v179 offset:13568\n"
"ds_read_b64_tr_b16 v[26:27], v236 offset:18440\n"
"v_mfma_f32_16x16x32_bf16 v[204:207], a[44:47], v[44:47], v[204:207]\n"
"v_rcp_f32 v171, v90\n"
"v_mfma_f32_16x16x32_bf16 v[204:207], a[56:59], v[48:51], v[204:207]\n"
"v_cvt_pk_bf16_f32 v171, v171, v171\n"
"v_and_b32 v92, v171, v176\n"
"v_mfma_f32_16x16x32_bf16 v[204:207], a[68:71], v[52:55], v[204:207]\n"
"ds_read_b64 v[90:91], v178 offset:27072\n"
"v_and_b32 v93, v171, v177\n"
"v_cvt_pk_bf16_f32 v253, v254, v255\n"
"v_cvt_pk_bf16_f32 v98, v98, v99\n"
"v_mfma_f32_16x16x16_bf16 v[106:109], v[106:107], v[92:93], 0\n"
"ds_read_b64_tr_b16 v[28:29], v235 offset:12288\n"
"v_cvt_pk_bf16_f32 v99, v100, v101\n"
"v_mfma_f32_16x16x16_bf16 v[252:255], v[252:253], v[92:93], 0\n"
"ds_read_b64_tr_b16 v[30:31], v235 offset:18432\n"
"v_cvt_pk_bf16_f32 v212, v212, v213\n"
"v_mfma_f32_16x16x16_bf16 v[98:101], v[98:99], v[92:93], 0\n"
"s_waitcnt lgkmcnt(5)\n"
"v_cvt_pk_bf16_f32 v213, v214, v215\n"
"v_cvt_pk_bf16_f32 v106, v106, v107\n"
"v_mfma_f32_16x16x32_bf16 v[136:139], v[136:139], v[148:151], 0\n"
"s_waitcnt lgkmcnt(4)\n"
"ds_read_b64 v[92:93], v179 offset:27072\n"
"v_cvt_pk_bf16_f32 v252, v252, v253\n"
"v_mfma_f32_16x16x32_bf16 v[136:139], v[80:83], v[160:163], v[136:139]\n"
"s_add_u32 m0, s34, 8192\n"
"v_cvt_pk_bf16_f32 v253, v254, v255\n"
"v_cvt_pk_bf16_f32 v254, v98, v99\n"
"v_mfma_f32_16x16x32_bf16 v[128:131], v[128:131], v[148:151], 0\n"
"buffer_load_dwordx4 v190, s[8:11], s1 offen lds\n"
"ds_read_b64 v[98:99], v180 offset:64\n"
"v_mfma_f32_16x16x32_bf16 v[128:131], v[84:87], v[162:165], v[128:131]\n"
"s_waitcnt lgkmcnt(4)\n"
"v_cvt_pk_bf16_f32 v255, v100, v101\n"
"v_cvt_pk_bf16_f32 v107, v108, v109\n"
"v_mfma_f32_16x16x32_bf16 v[140:143], v[140:143], v[148:151], 0\n"
"ds_read_b64 v[100:101], v181 offset:64\n"
"ds_read_b64 v[108:109], v181 offset:13568\n"
"v_mfma_f32_16x16x32_bf16 v[140:143], v[88:91], v[160:163], v[140:143]\n"
"s_mov_b64 exec, s[46:47]\n"
"v_add_u32 v185, 0xc0, v185\n"
"s_mov_b64 exec, -1\n"
"v_cvt_pk_bf16_f32 v214, v208, v209\n"
"v_cvt_pk_bf16_f32 v215, v210, v211\n"
"v_mfma_f32_16x16x32_bf16 v[208:211], a[80:83], v[32:35], 0\n"
"buffer_store_dwordx4 v[252:255], v192, s[28:31], s37 offen sc1\n"
"v_mfma_f32_16x16x32_bf16 v[208:211], a[92:95], v[36:39], v[208:211]\n"
"s_waitcnt lgkmcnt(3)\n"
"ds_write_b128 v187, v[212:215]\n"
"v_mfma_f32_16x16x32_bf16 v[132:135], v[132:135], v[148:151], 0\n"
"s_waitcnt lgkmcnt(3)\n"
"v_cvt_pk_bf16_f32 v204, v204, v205\n"
"v_cvt_pk_bf16_f32 v205, v206, v207\n"
"v_mfma_f32_16x16x32_bf16 v[132:135], v[92:95], v[162:165], v[132:135]\n"
"s_waitcnt lgkmcnt(2)\n"
"v_max_f32 v136, v136, v128\n"
"v_max_f32 v137, v137, v129\n"
"v_mfma_f32_16x16x32_bf16 v[112:115], v[112:115], v[148:151], 0\n"
"s_waitcnt lgkmcnt(1)\n"
"ds_read_b64_tr_b16 v[128:129], v182 offset:13576\n"
"v_max_f32 v138, v138, v130\n"
"v_mfma_f32_16x16x32_bf16 v[112:115], v[96:99], v[160:163], v[112:115]\n"
"v_max_f32 v139, v139, v131\n"
"v_max3_f32 v170, v136, v137, v138\n"
"v_mfma_f32_16x16x32_bf16 v[120:123], v[120:123], v[148:151], 0\n"
"s_add_u32 m0, s34, 11264\n"
"ds_read_b64_tr_b16 v[130:131], v183 offset:13576\n"
"v_max_f32 v140, v140, v132\n"
"v_mfma_f32_16x16x32_bf16 v[120:123], v[100:103], v[162:165], v[120:123]\n"
"v_max_f32 v141, v141, v133\n"
"v_max_f32 v142, v142, v134\n"
"v_mfma_f32_16x16x32_bf16 v[124:127], v[124:127], v[148:151], 0\n"
"buffer_load_dwordx4 v191, s[8:11], s1 offen lds\n"
"ds_read_b64_tr_b16 v[132:133], v182 offset:14344\n"
"v_mfma_f32_16x16x32_bf16 v[124:127], v[108:111], v[162:165], v[124:127]\n"
"s_add_u32 s1, s1, s33\n"
"v_max_f32 v143, v143, v135\n"
"v_max3_f32 v170, v170, v139, v140\n"
"v_mfma_f32_16x16x32_bf16 v[208:211], a[104:107], v[40:43], v[208:211]\n"
"ds_read_b64_tr_b16 v[134:135], v183 offset:14344\n"
"v_max3_f32 v170, v170, v141, v142\n"
"v_mfma_f32_16x16x32_bf16 v[208:211], a[116:119], v[44:47], v[208:211]\n"
"v_max_f32 v112, v112, v120\n"
"v_max_f32 v113, v113, v121\n"
"v_mfma_f32_16x16x32_bf16 v[208:211], a[128:131], v[48:51], v[208:211]\n"
"buffer_store_dwordx2 v[106:107], v193, s[28:31], s37 offen offset:1024 sc1\n"
"v_mfma_f32_16x16x32_bf16 v[208:211], a[140:143], v[52:55], v[208:211]\n"
"s_add_u32 s37, s37, 1572864\n"
"ds_read_b64 v[106:107], v180 offset:13568\n"
"ds_read_b64_tr_b16 v[120:121], v182 offset:40584\n"
"v_mfma_f32_16x16x32_bf16 v[212:215], a[72:75], v[32:35], 0\n"
"s_add_u32 s4, s37, 6291456\n"
"v_max_f32 v114, v114, v122\n"
"v_max_f32 v115, v115, v123\n"
"v_mfma_f32_16x16x32_bf16 v[212:215], a[84:87], v[36:39], v[212:215]\n"
"ds_read_b64_tr_b16 v[122:123], v183 offset:40584\n"
"v_cvt_pk_bf16_f32 v206, v208, v209\n"
"v_mfma_f32_16x16x32_bf16 v[212:215], a[96:99], v[40:43], v[212:215]\n"
"v_cvt_pk_bf16_f32 v207, v210, v211\n"
"v_max3_f32 v170, v170, v143, v112\n"
"v_mfma_f32_16x16x32_bf16 v[212:215], a[108:111], v[44:47], v[212:215]\n"
"ds_write_b128 v187, v[204:207] offset:64\n"
"v_mfma_f32_16x16x32_bf16 v[212:215], a[120:123], v[48:51], v[212:215]\n"
"v_max3_f32 v170, v170, v113, v114\n"
"v_mov_b64 v[252:253], 0\n"
"v_mfma_f32_16x16x32_bf16 v[212:215], a[132:135], v[52:55], v[212:215]\n"
"s_waitcnt lgkmcnt(3)\n"
"v_mov_b64 v[254:255], 0\n"
"v_mov_b64 v[68:69], -1\n"
"v_mfma_f32_16x16x32_bf16 v[116:119], v[116:119], v[148:151], 0\n"
"buffer_store_dwordx2 v[68:69], v193, s[28:31], s4 offen offset:1024 sc1\n"
"v_mfma_f32_16x16x32_bf16 v[116:119], v[104:107], v[160:163], v[116:119]\n"
"s_add_u32 s4, s37, 7864320\n"
"ds_read_b128 v[68:71], v192 offset:55296\n"
"v_mov_b64 v[98:99], 0\n"
"v_mfma_f32_16x16x32_bf16 v[208:211], a[76:79], v[32:35], 0\n"
"v_mov_b64 v[100:101], 0\n"
"v_mov_b64 v[106:107], 0\n"
"v_mfma_f32_16x16x32_bf16 v[208:211], a[88:91], v[36:39], v[208:211]\n"
"v_mov_b64 v[108:109], 0\n"
"v_max_f32 v116, v116, v124\n"
"v_mfma_f32_16x16x32_bf16 v[208:211], a[100:103], v[40:43], v[208:211]\n"
"v_max_f32 v117, v117, v125\n"
"v_max_f32 v118, v118, v126\n"
"v_mfma_f32_16x16x32_bf16 v[208:211], a[112:115], v[44:47], v[208:211]\n"
"ds_read_b64_tr_b16 v[124:125], v182 offset:41352\n"
"v_max_f32 v119, v119, v127\n"
"v_mfma_f32_16x16x32_bf16 v[208:211], a[124:127], v[48:51], v[208:211]\n"
"s_waitcnt vmcnt(12)\n"
"ds_read_b64_tr_b16 v[126:127], v183 offset:41352\n"
"buffer_load_dword v200, off, s[28:31], s39 sc1\n"
"v_mfma_f32_16x16x32_bf16 v[208:211], a[136:139], v[52:55], v[208:211]\n"
"ds_write_b128 v202, v[56:59] offset:36864\n"
"v_mfma_f32_16x16x32_bf16 v[204:207], a[144:147], v[32:35], 0\n"
"s_waitcnt lgkmcnt(3)\n"
"v_max3_f32 v170, v170, v115, v116\n"
"v_max3_f32 v170, v170, v117, v118\n"
"v_mfma_f32_16x16x32_bf16 v[232:235], a[216:219], v[68:71], 0\n"
"v_max_f32 v171, v170, v119\n"
"v_max_f32 v170, v170, v119\n"
"v_mfma_f32_16x16x32_bf16 v[236:239], a[220:223], v[68:71], 0\n"
"s_nop 0\n"
"v_permlane16_swap_b32 v170, v171\n"
"v_mfma_f32_16x16x32_bf16 v[240:243], a[224:227], v[68:71], 0\n"
"ds_read_b128 v[68:71], v192 offset:57344\n"
"v_max_f32 v170, v170, v171\n"
"v_mfma_f32_16x16x32_bf16 v[204:207], a[156:159], v[36:39], v[204:207]\n"
"v_cvt_pk_bf16_f32 v212, v212, v213\n"
"v_mov_b32 v171, v170\n"
"v_mfma_f32_16x16x32_bf16 v[204:207], a[168:171], v[40:43], v[204:207]\n"
"v_mov_b64 v[64:65], -1\n"
"v_mov_b64 v[66:67], -1\n"
"v_mfma_f32_16x16x32_bf16 v[204:207], a[180:183], v[44:47], v[204:207]\n"
"buffer_store_dwordx4 v[64:67], v192, s[28:31], s4 offen sc1\n"
"v_mfma_f32_16x16x32_bf16 v[204:207], a[192:195], v[48:51], v[204:207]\n"
"s_nop 0\n"
"ds_read_b128 v[64:67], v192 offset:56320\n"
"v_permlane32_swap_b32 v170, v171\n"
"v_max_f32 v170, v170, v171\n"
"v_mul_f32 v170, 0xbe553b95, v170\n"
"v_pk_fma_f32 v[136:137], v[136:137], s[70:71], v[170:171] op_sel_hi:[1,0,0]\n"
"v_pk_fma_f32 v[138:139], v[138:139], s[70:71], v[170:171] op_sel_hi:[1,0,0]\n"
"v_pk_fma_f32 v[140:141], v[140:141], s[70:71], v[170:171] op_sel_hi:[1,0,0]\n"
"v_pk_fma_f32 v[142:143], v[142:143], s[70:71], v[170:171] op_sel_hi:[1,0,0]\n"
"v_pk_fma_f32 v[112:113], v[112:113], s[70:71], v[170:171] op_sel_hi:[1,0,0]\n"
"v_pk_fma_f32 v[114:115], v[114:115], s[70:71], v[170:171] op_sel_hi:[1,0,0]\n"
"v_pk_fma_f32 v[116:117], v[116:117], s[70:71], v[170:171] op_sel_hi:[1,0,0]\n"
"v_pk_fma_f32 v[118:119], v[118:119], s[70:71], v[170:171] op_sel_hi:[1,0,0]\n"
"v_mfma_f32_16x16x32_bf16 v[204:207], a[204:207], v[52:55], v[204:207]\n"
"s_waitcnt vmcnt(12)\n"
"ds_write_b64 v203, v[60:61] offset:36864\n"
"v_cvt_pk_bf16_f32 v213, v214, v215\n"
"v_exp_f32 v136, v136\n"
"v_exp_f32 v137, v137\n"
"v_exp_f32 v138, v138\n"
"v_exp_f32 v139, v139\n"
"v_cvt_pk_bf16_f32 v214, v208, v209\n"
"v_cvt_pk_bf16_f32 v82, v136, v138\n"
"v_cvt_pk_bf16_f32 v215, v210, v211\n"
"v_mfma_f32_16x16x32_bf16 v[208:211], a[148:151], v[32:35], 0\n"
"s_waitcnt lgkmcnt(1)\n"
"ds_write_b128 v187, v[212:215] offset:128\n"
"v_mfma_f32_16x16x32_bf16 v[208:211], a[160:163], v[36:39], v[208:211]\n"
"v_cvt_pk_bf16_f32 v83, v137, v139\n"
"v_exp_f32 v140, v140\n"
"v_mfma_f32_16x16x32_bf16 v[232:235], a[228:231], v[64:67], v[232:235]\n"
"ds_read_b128 v[136:139], v178 offset:27008\n"
"v_exp_f32 v141, v141\n"
"v_mfma_f32_16x16x32_bf16 v[232:235], a[240:243], v[68:71], v[232:235]\n"
"v_exp_f32 v142, v142\n"
"v_mfma_f32_16x16x32_bf16 v[236:239], a[232:235], v[64:67], v[236:239]\n"
"v_exp_f32 v143, v143\n"
"v_mfma_f32_16x16x32_bf16 v[236:239], a[244:247], v[68:71], v[236:239]\n"
"v_cvt_pk_bf16_f32 v84, v140, v142\n"
"v_cvt_pk_bf16_f32 v85, v141, v143\n"
"v_mfma_f32_16x16x32_bf16 v[240:243], a[236:239], v[64:67], v[240:243]\n"
"ds_read_b128 v[64:67], v192 offset:58368\n"
"ds_read_b128 v[140:143], v180\n"
"v_mfma_f32_16x16x32_bf16 v[240:243], a[248:251], v[68:71], v[240:243]\n"
"ds_read_b128 v[68:71], v192 offset:59392\n"
"v_cvt_pk_bf16_f32 v148, v204, v205\n"
"v_smfmac_f32_16x16x64_bf16 v[252:255], v[82:85], v[224:231], v63\n"
"ds_read_b64_tr_b16 v[224:225], v182 offset:40640\n"
"v_smfmac_f32_16x16x64_bf16 v[98:101], v[82:85], v[244:251], v63\n"
"ds_read_b64_tr_b16 v[226:227], v183 offset:40640\n"
"v_smfmac_f32_16x16x64_bf16 v[106:109], v[82:85], v[128:135], v63\n"
"ds_read_b64_tr_b16 v[228:229], v182 offset:41408\n"
"v_mfma_f32_16x16x32_bf16 v[82:85], v[172:175], v[82:85], 0\n"
"ds_read_b64_tr_b16 v[230:231], v183 offset:41408\n"
"v_cvt_pk_bf16_f32 v149, v206, v207\n"
"v_mfma_f32_16x16x32_bf16 v[208:211], a[172:175], v[40:43], v[208:211]\n"
"ds_read_b64_tr_b16 v[244:245], v182 offset:40648\n"
"v_exp_f32 v112, v112\n"
"v_mfma_f32_16x16x32_bf16 v[208:211], a[184:187], v[44:47], v[208:211]\n"
"s_waitcnt lgkmcnt(7)\n"
"ds_read_b64_tr_b16 v[246:247], v183 offset:40648\n"
"v_exp_f32 v113, v113\n"
"v_mfma_f32_16x16x32_bf16 v[208:211], a[196:199], v[48:51], v[208:211]\n"
"ds_read_b64_tr_b16 v[248:249], v182 offset:41416\n"
"v_exp_f32 v114, v114\n"
"v_mfma_f32_16x16x32_bf16 v[208:211], a[208:211], v[52:55], v[208:211]\n"
"ds_read_b64_tr_b16 v[250:251], v183 offset:41416\n"
"ds_read_b128 v[128:131], v179 offset:27008\n"
"v_mfma_f32_16x16x32_bf16 v[232:235], a[252:255], v[64:67], v[232:235]\n"
"s_waitcnt lgkmcnt(9)\n"
"v_exp_f32 v115, v115\n"
"v_mfma_f32_16x16x32_bf16 v[232:235], v[8:11], v[68:71], v[232:235]\n"
"ds_read_b128 v[132:135], v181\n"
"v_cvt_pk_bf16_f32 v90, v112, v114\n"
"v_mfma_f32_16x16x32_bf16 v[236:239], v[0:3], v[64:67], v[236:239]\n"
"v_cvt_pk_bf16_f32 v91, v113, v115\n"
"v_cvt_pk_bf16_f32 v150, v208, v209\n"
"v_mfma_f32_16x16x32_bf16 v[236:239], v[12:15], v[68:71], v[236:239]\n"
"ds_read_b128 v[112:115], v180 offset:13504\n"
"v_cvt_pk_bf16_f32 v151, v210, v211\n"
"v_mfma_f32_16x16x32_bf16 v[240:243], v[4:7], v[64:67], v[240:243]\n"
"ds_read_b128 v[64:67], v192 offset:60416\n"
"v_exp_f32 v116, v116\n"
"v_mfma_f32_16x16x32_bf16 v[240:243], v[16:19], v[68:71], v[240:243]\n"
"v_exp_f32 v117, v117\n"
"v_mfma_f32_16x16x32_bf16 v[212:215], a[152:155], v[32:35], 0\n"
"ds_read_b128 v[32:35], v188\n"
"v_exp_f32 v118, v118\n"
"v_mfma_f32_16x16x32_bf16 v[212:215], a[164:167], v[36:39], v[212:215]\n"
"ds_read_b128 v[36:39], v189\n"
"v_cvt_pk_bf16_f32 v92, v116, v118\n"
"v_mfma_f32_16x16x32_bf16 v[212:215], a[176:179], v[40:43], v[212:215]\n"
"ds_read_b128 v[40:43], v188 offset:1024\n"
"v_exp_f32 v119, v119\n"
"v_mfma_f32_16x16x32_bf16 v[212:215], a[188:191], v[44:47], v[212:215]\n"
"ds_read_b128 v[44:47], v189 offset:1024\n"
"v_cvt_pk_bf16_f32 v93, v117, v119\n"
"v_mfma_f32_16x16x32_bf16 v[212:215], a[200:203], v[48:51], v[212:215]\n"
"s_waitcnt vmcnt(11) lgkmcnt(4)\n"
"ds_read_b128 v[48:51], v188 offset:2048\n"
"v_mfma_f32_16x16x32_bf16 v[212:215], a[212:215], v[52:55], v[212:215]\n"
".Lic4%=:\n"
"s_cbranch_execz .Lib1%=\n"
"ds_read_b128 v[52:55], v189 offset:2048\n"
"v_smfmac_f32_16x16x64_bf16 v[252:255], v[90:93], v[224:231], v63\n"
"s_waitcnt lgkmcnt(5)\n"
"v_smfmac_f32_16x16x64_bf16 v[98:101], v[90:93], v[244:251], v63\n"
"v_mfma_f32_16x16x32_bf16 v[232:235], v[20:23], v[64:67], v[232:235]\n"
"s_waitcnt lgkmcnt(4)\n"
"s_add_u32 m0, s34, 0\n"
"v_mfma_f32_16x16x32_bf16 v[240:243], v[28:31], v[64:67], v[240:243]\n"
"buffer_load_dwordx4 v190, s[8:11], s32 offen lds\n"
"v_cvt_pk_bf16_f32 v162, v212, v213\n"
"v_mfma_f32_16x16x32_bf16 v[236:239], v[24:27], v[64:67], v[236:239]\n"
"v_cvt_pk_bf16_f32 v252, v252, v253\n"
"v_cvt_pk_bf16_f32 v253, v254, v255\n"
"v_smfmac_f32_16x16x64_bf16 v[106:109], v[90:93], v[120:127], v63\n"
"ds_read_b128 v[120:123], v181 offset:13504\n"
"v_mfma_f32_16x16x32_bf16 v[90:93], v[172:175], v[90:93], v[82:85]\n"
"v_cvt_pk_bf16_f32 v232, v232, v233\n"
"v_cvt_pk_bf16_f32 v233, v234, v235\n"
"v_mfma_f32_16x16x32_bf16 v[204:207], a[0:3], v[32:35], 0\n"
"ds_read_b64 v[82:83], v178 offset:27072\n"
"ds_read_b64 v[84:85], v179 offset:27072\n"
"v_mfma_f32_16x16x32_bf16 v[204:207], a[12:15], v[36:39], v[204:207]\n"
"s_waitcnt lgkmcnt(6)\n"
"v_cvt_pk_bf16_f32 v240, v240, v241\n"
"v_cvt_pk_bf16_f32 v234, v236, v237\n"
"v_mfma_f32_16x16x32_bf16 v[204:207], a[24:27], v[40:43], v[204:207]\n"
"s_waitcnt lgkmcnt(5)\n"
"v_cvt_pk_bf16_f32 v235, v238, v239\n"
"v_cvt_pk_bf16_f32 v241, v242, v243\n"
"v_mfma_f32_16x16x32_bf16 v[204:207], a[36:39], v[44:47], v[204:207]\n"
"s_waitcnt lgkmcnt(4)\n"
"ds_write_b128 v196, v[232:235] offset:30720\n"
"v_mfma_f32_16x16x32_bf16 v[204:207], a[48:51], v[48:51], v[204:207]\n"
"s_waitcnt lgkmcnt(4)\n"
"v_rcp_f32 v171, v90\n"
"v_mfma_f32_16x16x32_bf16 v[204:207], a[60:63], v[52:55], v[204:207]\n"
"s_add_u32 m0, s34, 3072\n"
"v_cvt_pk_bf16_f32 v171, v171, v171\n"
"v_and_b32 v92, v171, v176\n"
"v_mfma_f32_16x16x32_bf16 v[208:211], a[4:7], v[32:35], 0\n"
"s_waitcnt lgkmcnt(2)\n"
"buffer_load_dwordx4 v191, s[8:11], s32 offen lds\n"
"ds_read_b64 v[90:91], v180 offset:64\n"
"v_mfma_f32_16x16x32_bf16 v[136:139], v[136:139], v[152:155], 0\n"
"s_waitcnt lgkmcnt(2)\n"
"ds_write_b64 v197, v[240:241] offset:30720\n"
"v_mfma_f32_16x16x32_bf16 v[136:139], v[80:83], v[164:167], v[136:139]\n"
"s_add_u32 s32, s32, s33\n"
"v_and_b32 v93, v171, v177\n"
"v_cvt_pk_bf16_f32 v163, v214, v215\n"
"v_mfma_f32_16x16x32_bf16 v[128:131], v[128:131], v[152:155], 0\n"
"v_cvt_pk_bf16_f32 v98, v98, v99\n"
"v_cvt_pk_bf16_f32 v99, v100, v101\n"
"v_mfma_f32_16x16x32_bf16 v[128:131], v[84:87], v[166:169], v[128:131]\n"
"v_cvt_pk_bf16_f32 v106, v106, v107\n"
"v_cvt_pk_bf16_f32 v107, v108, v109\n"
"v_mfma_f32_16x16x16_bf16 v[252:255], v[252:253], v[92:93], 0\n"
"v_cvt_pk_bf16_f32 v204, v204, v205\n"
"v_cvt_pk_bf16_f32 v205, v206, v207\n"
"v_mfma_f32_16x16x16_bf16 v[98:101], v[98:99], v[92:93], 0\n"
"s_mov_b64 exec, s[44:45]\n"
"v_add_u32 v179, 0xc0, v179\n"
"s_mov_b64 exec, -1\n"
"v_mfma_f32_16x16x16_bf16 v[106:109], v[106:107], v[92:93], 0\n"
"s_waitcnt lgkmcnt(1)\n"
"ds_read_b64 v[92:93], v181 offset:64\n"
"v_cvt_pk_bf16_f32 v252, v252, v253\n"
"v_mfma_f32_16x16x32_bf16 v[140:143], v[140:143], v[152:155], 0\n"
"s_waitcnt lgkmcnt(1)\n"
"v_cvt_pk_bf16_f32 v253, v254, v255\n"
"v_cvt_pk_bf16_f32 v254, v98, v99\n"
"v_mfma_f32_16x16x32_bf16 v[140:143], v[88:91], v[164:167], v[140:143]\n"
"s_add_u32 m0, s34, 1024\n"
"ds_read_b64 v[98:99], v180 offset:13568\n"
"buffer_load_dwordx4 v190, s[8:11], s0 offen lds\n"
"v_mfma_f32_16x16x32_bf16 v[208:211], a[16:19], v[36:39], v[208:211]\n"
"s_barrier\n"
"v_mfma_f32_16x16x32_bf16 v[208:211], a[28:31], v[40:43], v[208:211]\n"
"ds_read_b128 v[116:119], v180 offset:27008\n"
"ds_read_b128 v[124:127], v181 offset:27008\n"
"v_mfma_f32_16x16x32_bf16 v[208:211], a[40:43], v[44:47], v[208:211]\n"
"v_cvt_pk_bf16_f32 v255, v100, v101\n"
"v_cvt_pk_bf16_f32 v106, v106, v107\n"
"v_mfma_f32_16x16x32_bf16 v[208:211], a[52:55], v[48:51], v[208:211]\n"
"ds_read_b64 v[100:101], v181 offset:13568\n"
"buffer_store_dwordx4 v[252:255], v192, s[28:31], s37 offen sc1\n"
"v_mfma_f32_16x16x32_bf16 v[208:211], a[64:67], v[52:55], v[208:211]\n"
"s_waitcnt lgkmcnt(4)\n"
"v_cvt_pk_bf16_f32 v107, v108, v109\n"
"v_max_f32 v136, v136, v128\n"
"v_mfma_f32_16x16x32_bf16 v[132:135], v[132:135], v[152:155], 0\n"
"s_waitcnt lgkmcnt(3)\n"
"ds_read_b64 v[108:109], v181 offset:27072\n"
"v_max_f32 v137, v137, v129\n"
"v_mfma_f32_16x16x32_bf16 v[132:135], v[92:95], v[166:169], v[132:135]\n"
"s_waitcnt vmcnt(5)\n"
"ds_read_b64_tr_b16 v[128:129], v182 offset:27080\n"
"v_add3_u32 v200, v200, s50, -5\n"
"v_mfma_f32_16x16x32_bf16 v[112:115], v[112:115], v[152:155], 0\n"
"v_cmp_lt_u32 vcc, 64, v200\n"
"v_cvt_pk_bf16_f32 v206, v208, v209\n"
"v_mfma_f32_16x16x32_bf16 v[112:115], v[96:99], v[164:167], v[112:115]\n"
"v_cvt_pk_bf16_f32 v207, v210, v211\n"
"v_max_f32 v138, v138, v130\n"
"v_mfma_f32_16x16x32_bf16 v[212:215], a[8:11], v[32:35], 0\n"
"s_waitcnt lgkmcnt(2)\n"
"ds_write_b128 v186, v[204:207] offset:6752\n"
"v_mfma_f32_16x16x32_bf16 v[120:123], v[120:123], v[152:155], 0\n"
"s_cbranch_vccnz .Lp4%=\n"
".Lr4%=:\n"
"v_mfma_f32_16x16x32_bf16 v[120:123], v[100:103], v[166:169], v[120:123]\n"
"s_waitcnt lgkmcnt(2)\n"
"buffer_load_dwordx4 v[56:59], v192, s[28:31], s38 offen sc1\n"
"v_max_f32 v139, v139, v131\n"
"v_mfma_f32_16x16x32_bf16 v[124:127], v[124:127], v[152:155], 0\n"
"ds_read_b64_tr_b16 v[130:131], v183 offset:27080\n"
"v_max3_f32 v170, v136, v137, v138\n"
"v_mfma_f32_16x16x32_bf16 v[124:127], v[108:111], v[166:169], v[124:127]\n"
"v_max_f32 v140, v140, v132\n"
"v_max_f32 v141, v141, v133\n"
"v_mfma_f32_16x16x32_bf16 v[212:215], a[20:23], v[36:39], v[212:215]\n"
"s_add_u32 m0, s34, 4096\n"
"ds_read_b64_tr_b16 v[132:133], v182 offset:27848\n"
"v_max_f32 v142, v142, v134\n"
"v_mfma_f32_16x16x32_bf16 v[212:215], a[32:35], v[40:43], v[212:215]\n"
"buffer_load_dwordx4 v191, s[8:11], s0 offen lds\n"
"v_max_f32 v143, v143, v135\n"
"v_mfma_f32_16x16x32_bf16 v[212:215], a[44:47], v[44:47], v[212:215]\n"
"s_add_u32 s0, s0, s33\n"
"ds_read_b64_tr_b16 v[134:135], v183 offset:27848\n"
"v_max3_f32 v170, v170, v139, v140\n"
"v_mfma_f32_16x16x32_bf16 v[212:215], a[56:59], v[48:51], v[212:215]\n"
"v_max3_f32 v170, v170, v141, v142\n"
"v_max_f32 v112, v112, v120\n"
"v_mfma_f32_16x16x32_bf16 v[212:215], a[68:71], v[52:55], v[212:215]\n"
"v_max_f32 v113, v113, v121\n"
"v_max_f32 v114, v114, v122\n"
"v_mfma_f32_16x16x32_bf16 v[208:211], a[80:83], v[32:35], 0\n"
"buffer_load_dwordx2 v[60:61], v193, s[28:31], s38 offen offset:1024 sc1\n"
"ds_read_b64_tr_b16 v[120:121], v182 offset:54088\n"
"v_mfma_f32_16x16x32_bf16 v[208:211], a[92:95], v[36:39], v[208:211]\n"
"s_add_u32 s38, s38, 1572864\n"
"v_max_f32 v115, v115, v123\n"
"v_cvt_pk_bf16_f32 v212, v212, v213\n"
"v_mfma_f32_16x16x32_bf16 v[208:211], a[104:107], v[40:43], v[208:211]\n"
"ds_read_b64_tr_b16 v[122:123], v183 offset:54088\n"
"v_cvt_pk_bf16_f32 v213, v214, v215\n"
"v_mfma_f32_16x16x32_bf16 v[208:211], a[116:119], v[44:47], v[208:211]\n"
"v_max3_f32 v170, v170, v143, v112\n"
"v_max3_f32 v170, v170, v113, v114\n"
"v_mfma_f32_16x16x32_bf16 v[208:211], a[128:131], v[48:51], v[208:211]\n"
"s_add_u32 m0, s34, 2048\n"
"buffer_store_dwordx2 v[106:107], v193, s[28:31], s37 offen offset:1024 sc1\n"
"v_mfma_f32_16x16x32_bf16 v[208:211], a[140:143], v[52:55], v[208:211]\n"
"s_add_u32 s37, s37, 1572864\n"
"ds_read_b64 v[106:107], v180 offset:27072\n"
"v_mov_b64 v[252:253], 0\n"
"v_mfma_f32_16x16x32_bf16 v[204:207], a[72:75], v[32:35], 0\n"
"s_add_u32 s4, s37, 6291456\n"
"v_mov_b64 v[254:255], 0\n"
"v_mov_b64 v[98:99], 0\n"
"v_mfma_f32_16x16x32_bf16 v[204:207], a[84:87], v[36:39], v[204:207]\n"
"v_cvt_pk_bf16_f32 v214, v208, v209\n"
"v_cvt_pk_bf16_f32 v215, v210, v211\n"
"v_mfma_f32_16x16x32_bf16 v[204:207], a[96:99], v[40:43], v[204:207]\n"
"ds_write_b128 v186, v[212:215] offset:6816\n"
"v_mfma_f32_16x16x32_bf16 v[204:207], a[108:111], v[44:47], v[204:207]\n"
"v_mov_b64 v[100:101], 0\n"
"v_mov_b64 v[108:109], 0\n"
"v_mfma_f32_16x16x32_bf16 v[204:207], a[120:123], v[48:51], v[204:207]\n"
"buffer_load_dwordx4 v190, s[8:11], s1 offen lds\n"
"v_mfma_f32_16x16x32_bf16 v[204:207], a[132:135], v[52:55], v[204:207]\n"
"s_waitcnt lgkmcnt(1)\n"
"v_mov_b64 v[68:69], -1\n"
"v_mfma_f32_16x16x32_bf16 v[116:119], v[116:119], v[152:155], 0\n"
"v_mfma_f32_16x16x32_bf16 v[116:119], v[104:107], v[164:167], v[116:119]\n"
"v_mov_b64 v[106:107], 0\n"
"v_mfma_f32_16x16x32_bf16 v[208:211], a[76:79], v[32:35], 0\n"
"buffer_store_dwordx2 v[68:69], v193, s[28:31], s4 offen offset:1024 sc1\n"
"v_mfma_f32_16x16x32_bf16 v[208:211], a[88:91], v[36:39], v[208:211]\n"
"s_add_u32 s4, s37, 7864320\n"
"ds_read_b128 v[68:71], v202 offset:30720\n"
"v_cvt_pk_bf16_f32 v204, v204, v205\n"
"v_mfma_f32_16x16x32_bf16 v[208:211], a[100:103], v[40:43], v[208:211]\n"
"v_max_f32 v116, v116, v124\n"
"v_max_f32 v117, v117, v125\n"
"v_mfma_f32_16x16x32_bf16 v[208:211], a[112:115], v[44:47], v[208:211]\n"
"ds_read_b64_tr_b16 v[124:125], v182 offset:54856\n"
"v_max_f32 v118, v118, v126\n"
"v_mfma_f32_16x16x32_bf16 v[208:211], a[124:127], v[48:51], v[208:211]\n"
"v_max_f32 v119, v119, v127\n"
"v_max3_f32 v170, v170, v115, v116\n"
"v_mfma_f32_16x16x32_bf16 v[208:211], a[136:139], v[52:55], v[208:211]\n"
"s_add_u32 m0, s34, 5120\n"
"ds_read_b64_tr_b16 v[126:127], v183 offset:54856\n"
"v_max3_f32 v170, v170, v117, v118\n"
"v_mfma_f32_16x16x32_bf16 v[212:215], a[144:147], v[32:35], 0\n"
"v_max_f32 v171, v170, v119\n"
"v_max_f32 v170, v170, v119\n"
"v_mfma_f32_16x16x32_bf16 v[212:215], a[156:159], v[36:39], v[212:215]\n"
"buffer_load_dwordx4 v191, s[8:11], s1 offen lds\n"
"v_cvt_pk_bf16_f32 v205, v206, v207\n"
"v_mfma_f32_16x16x32_bf16 v[212:215], a[168:171], v[40:43], v[212:215]\n"
"s_waitcnt lgkmcnt(2)\n"
"v_permlane16_swap_b32 v170, v171\n"
"v_mfma_f32_16x16x32_bf16 v[212:215], a[180:183], v[44:47], v[212:215]\n"
"s_add_u32 s1, s1, s33\n"
"v_max_f32 v170, v170, v171\n"
"v_mov_b32 v171, v170\n"
"v_mfma_f32_16x16x32_bf16 v[212:215], a[192:195], v[48:51], v[212:215]\n"
"s_nop 0\n"
"v_permlane32_swap_b32 v170, v171\n"
"v_max_f32 v170, v170, v171\n"
"v_mul_f32 v170, 0xbe553b95, v170\n"
"v_pk_fma_f32 v[136:137], v[136:137], s[70:71], v[170:171] op_sel_hi:[1,0,0]\n"
"v_pk_fma_f32 v[138:139], v[138:139], s[70:71], v[170:171] op_sel_hi:[1,0,0]\n"
"v_pk_fma_f32 v[140:141], v[140:141], s[70:71], v[170:171] op_sel_hi:[1,0,0]\n"
"v_pk_fma_f32 v[142:143], v[142:143], s[70:71], v[170:171] op_sel_hi:[1,0,0]\n"
"v_pk_fma_f32 v[112:113], v[112:113], s[70:71], v[170:171] op_sel_hi:[1,0,0]\n"
"v_pk_fma_f32 v[114:115], v[114:115], s[70:71], v[170:171] op_sel_hi:[1,0,0]\n"
"v_pk_fma_f32 v[116:117], v[116:117], s[70:71], v[170:171] op_sel_hi:[1,0,0]\n"
"v_pk_fma_f32 v[118:119], v[118:119], s[70:71], v[170:171] op_sel_hi:[1,0,0]\n"
"v_mfma_f32_16x16x32_bf16 v[212:215], a[204:207], v[52:55], v[212:215]\n"
"buffer_store_dwordx4 v[68:71], v194, s[24:27], s36 offen sc1\n"
"s_nop 1\n"
"ds_read_b128 v[68:71], v192 offset:36864\n"
"v_mov_b64 v[64:65], -1\n"
"v_mov_b64 v[66:67], -1\n"
"v_exp_f32 v136, v136\n"
"v_exp_f32 v137, v137\n"
"v_exp_f32 v138, v138\n"
"v_exp_f32 v139, v139\n"
"v_cvt_pk_bf16_f32 v82, v136, v138\n"
"v_cvt_pk_bf16_f32 v206, v208, v209\n"
"v_cvt_pk_bf16_f32 v83, v137, v139\n"
"v_exp_f32 v140, v140\n"
"v_cvt_pk_bf16_f32 v207, v210, v211\n"
"v_mfma_f32_16x16x32_bf16 v[208:211], a[148:151], v[32:35], 0\n"
"ds_write_b128 v186, v[204:207] offset:6880\n"
"v_mfma_f32_16x16x32_bf16 v[208:211], a[160:163], v[36:39], v[208:211]\n"
"s_waitcnt lgkmcnt(1)\n"
"v_exp_f32 v141, v141\n"
"v_mfma_f32_16x16x32_bf16 v[208:211], a[172:175], v[40:43], v[208:211]\n"
"buffer_store_dwordx4 v[64:67], v192, s[28:31], s4 offen sc1\n"
"v_mfma_f32_16x16x32_bf16 v[232:235], a[216:219], v[68:71], 0\n"
"s_nop 0\n"
"ds_read_b64 v[64:65], v62 offset:30720\n"
"ds_read_b128 v[136:139], v180\n"
"v_mfma_f32_16x16x32_bf16 v[236:239], a[220:223], v[68:71], 0\n"
"v_exp_f32 v142, v142\n"
"v_mfma_f32_16x16x32_bf16 v[240:243], a[224:227], v[68:71], 0\n"
"ds_read_b128 v[68:71], v192 offset:38912\n"
"v_cvt_pk_bf16_f32 v84, v140, v142\n"
"v_mfma_f32_16x16x32_bf16 v[208:211], a[184:187], v[44:47], v[208:211]\n"
"v_exp_f32 v143, v143\n"
"v_mfma_f32_16x16x32_bf16 v[208:211], a[196:199], v[48:51], v[208:211]\n"
"v_cvt_pk_bf16_f32 v85, v141, v143\n"
"v_cvt_pk_bf16_f32 v152, v212, v213\n"
"v_mfma_f32_16x16x32_bf16 v[208:211], a[208:211], v[52:55], v[208:211]\n"
"ds_read_b128 v[140:143], v180 offset:13504\n"
"v_cvt_pk_bf16_f32 v153, v214, v215\n"
"v_smfmac_f32_16x16x64_bf16 v[252:255], v[82:85], v[216:223], v63\n"
"s_waitcnt lgkmcnt(3)\n"
"ds_read_b64_tr_b16 v[216:217], v182 offset:54144\n"
"v_smfmac_f32_16x16x64_bf16 v[98:101], v[82:85], v[72:79], v63\n"
"buffer_store_dwordx2 v[64:65], v195, s[24:27], s36 offen sc1\n"
"v_smfmac_f32_16x16x64_bf16 v[106:109], v[82:85], v[128:135], v63\n"
"s_add_u32 s36, s36, s33\n"
"ds_read_b128 v[64:67], v192 offset:37888\n"
"v_mfma_f32_16x16x32_bf16 v[82:85], v[172:175], v[82:85], 0\n"
"ds_read_b64_tr_b16 v[218:219], v183 offset:54144\n"
"ds_read_b64_tr_b16 v[220:221], v182 offset:54912\n"
"v_mfma_f32_16x16x32_bf16 v[204:207], a[152:155], v[32:35], 0\n"
"v_exp_f32 v112, v112\n"
"v_mfma_f32_16x16x32_bf16 v[204:207], a[164:167], v[36:39], v[204:207]\n"
"ds_read_b128 v[32:35], v188 offset:6144\n"
"ds_read_b128 v[36:39], v189 offset:6144\n"
"v_mfma_f32_16x16x32_bf16 v[204:207], a[176:179], v[40:43], v[204:207]\n"
"v_exp_f32 v113, v113\n"
"v_mfma_f32_16x16x32_bf16 v[204:207], a[188:191], v[44:47], v[204:207]\n"
"ds_read_b128 v[40:43], v188 offset:7168\n"
"ds_read_b128 v[44:47], v189 offset:7168\n"
"v_mfma_f32_16x16x32_bf16 v[204:207], a[200:203], v[48:51], v[204:207]\n"
"v_exp_f32 v114, v114\n"
"v_mfma_f32_16x16x32_bf16 v[204:207], a[212:215], v[52:55], v[204:207]\n"
"s_waitcnt lgkmcnt(6)\n"
"ds_read_b128 v[48:51], v188 offset:8192\n"
"ds_read_b128 v[52:55], v189 offset:8192\n"
"v_mfma_f32_16x16x32_bf16 v[232:235], a[228:231], v[64:67], v[232:235]\n"
"v_exp_f32 v115, v115\n"
"v_mfma_f32_16x16x32_bf16 v[232:235], a[240:243], v[68:71], v[232:235]\n"
"ds_read_b64_tr_b16 v[222:223], v183 offset:54912\n"
"ds_read_b64_tr_b16 v[72:73], v182 offset:54152\n"
"v_mfma_f32_16x16x32_bf16 v[236:239], a[232:235], v[64:67], v[236:239]\n"
"s_waitcnt lgkmcnt(7)\n"
"v_cvt_pk_bf16_f32 v90, v112, v114\n"
"v_cvt_pk_bf16_f32 v91, v113, v115\n"
"v_mfma_f32_16x16x32_bf16 v[236:239], a[244:247], v[68:71], v[236:239]\n"
"s_waitcnt lgkmcnt(6)\n"
"ds_read_b64_tr_b16 v[74:75], v183 offset:54152\n"
"ds_read_b64_tr_b16 v[76:77], v182 offset:54920\n"
"v_mfma_f32_16x16x32_bf16 v[240:243], a[236:239], v[64:67], v[240:243]\n"
"s_add_u32 m0, s34, 6144\n"
"v_exp_f32 v116, v116\n"
"v_mfma_f32_16x16x32_bf16 v[240:243], a[248:251], v[68:71], v[240:243]\n"
"buffer_load_dwordx4 v190, s[8:11], s32 offen lds\n"
"ds_read_b128 v[64:67], v192 offset:39936\n"
"v_mfma_f32_16x16x32_bf16 v[212:215], a[0:3], v[32:35], 0\n"
"ds_read_b128 v[68:71], v192 offset:40960\n"
"ds_read_b64_tr_b16 v[78:79], v183 offset:54920\n"
"v_mfma_f32_16x16x32_bf16 v[212:215], a[12:15], v[36:39], v[212:215]\n"
"s_waitcnt lgkmcnt(10)\n"
"v_exp_f32 v117, v117\n"
"v_mfma_f32_16x16x32_bf16 v[212:215], a[24:27], v[40:43], v[212:215]\n"
"s_waitcnt lgkmcnt(9)\n"
"ds_read_b128 v[128:131], v181\n"
"ds_read_b128 v[132:135], v181 offset:13504\n"
"v_mfma_f32_16x16x32_bf16 v[212:215], a[36:39], v[44:47], v[212:215]\n"
"s_waitcnt lgkmcnt(10)\n"
"v_exp_f32 v118, v118\n"
"v_mfma_f32_16x16x32_bf16 v[212:215], a[48:51], v[48:51], v[212:215]\n"
"s_waitcnt lgkmcnt(9)\n"
"ds_read_b128 v[112:115], v180 offset:27008\n"
"v_cvt_pk_bf16_f32 v92, v116, v118\n"
"v_mfma_f32_16x16x32_bf16 v[212:215], a[60:63], v[52:55], v[212:215]\n"
"v_exp_f32 v119, v119\n"
"v_cvt_pk_bf16_f32 v154, v208, v209\n"
"v_cvt_pk_bf16_f32 v93, v117, v119\n"
"v_cvt_pk_bf16_f32 v155, v210, v211\n"
"v_mfma_f32_16x16x32_bf16 v[208:211], a[4:7], v[32:35], 0\n"
"s_waitcnt lgkmcnt(5)\n"
"v_cvt_pk_bf16_f32 v166, v204, v205\n"
"v_cvt_pk_bf16_f32 v167, v206, v207\n"
"v_mfma_f32_16x16x32_bf16 v[208:211], a[16:19], v[36:39], v[208:211]\n"
"s_waitcnt lgkmcnt(3)\n"
"v_cvt_pk_bf16_f32 v212, v212, v213\n"
"v_cvt_pk_bf16_f32 v213, v214, v215\n"
"v_mfma_f32_16x16x32_bf16 v[232:235], a[252:255], v[64:67], v[232:235]\n"
"s_add_u32 m0, s34, 9216\n"
"s_nop 0\n"
"buffer_load_dwordx4 v191, s[8:11], s32 offen lds\n"
"v_mfma_f32_16x16x32_bf16 v[232:235], v[8:11], v[68:71], v[232:235]\n"
"s_add_u32 s32, s32, s33\n"
"v_mfma_f32_16x16x32_bf16 v[236:239], v[0:3], v[64:67], v[236:239]\n"
"v_mfma_f32_16x16x32_bf16 v[236:239], v[12:15], v[68:71], v[236:239]\n"
"v_mfma_f32_16x16x32_bf16 v[240:243], v[4:7], v[64:67], v[240:243]\n"
"ds_read_b128 v[64:67], v192 offset:41984\n"
"v_mfma_f32_16x16x32_bf16 v[240:243], v[16:19], v[68:71], v[240:243]\n"
"v_smfmac_f32_16x16x64_bf16 v[252:255], v[90:93], v[216:223], v63\n"
"v_smfmac_f32_16x16x64_bf16 v[98:101], v[90:93], v[72:79], v63\n"
"s_add_u32 m0, s34, 7168\n"
"v_smfmac_f32_16x16x64_bf16 v[106:109], v[90:93], v[120:127], v63\n"
"s_waitcnt vmcnt(11)\n"
"buffer_load_dwordx4 v190, s[8:11], s0 offen lds\n"
"v_mfma_f32_16x16x32_bf16 v[90:93], v[172:175], v[90:93], v[82:85]\n"
"ds_write_b128 v202, v[56:59] offset:43008\n"
"v_mfma_f32_16x16x32_bf16 v[208:211], a[28:31], v[40:43], v[208:211]\n"
"s_waitcnt lgkmcnt(1)\n"
"v_cvt_pk_bf16_f32 v252, v252, v253\n"
"v_cvt_pk_bf16_f32 v253, v254, v255\n"
"v_mfma_f32_16x16x32_bf16 v[208:211], a[40:43], v[44:47], v[208:211]\n"
"v_cvt_pk_bf16_f32 v98, v98, v99\n"
"v_cvt_pk_bf16_f32 v99, v100, v101\n"
"v_mfma_f32_16x16x32_bf16 v[208:211], a[52:55], v[48:51], v[208:211]\n"
"buffer_load_dwordx4 v[56:59], v192, s[28:31], s38 offen sc1\n"
"ds_read_b64 v[82:83], v180 offset:64\n"
"v_mfma_f32_16x16x32_bf16 v[208:211], a[64:67], v[52:55], v[208:211]\n"
"ds_read_b64 v[84:85], v181 offset:64\n"
"ds_read_b128 v[120:123], v181 offset:27008\n"
"v_mfma_f32_16x16x32_bf16 v[232:235], v[20:23], v[64:67], v[232:235]\n"
"v_rcp_f32 v171, v90\n"
"v_mfma_f32_16x16x32_bf16 v[240:243], v[28:31], v[64:67], v[240:243]\n"
"s_add_u32 m0, s34, 10240\n"
"ds_read_b64 v[90:91], v180 offset:13568\n"
"v_cvt_pk_bf16_f32 v171, v171, v171\n"
"v_mfma_f32_16x16x32_bf16 v[236:239], v[24:27], v[64:67], v[236:239]\n"
"buffer_load_dwordx4 v191, s[8:11], s0 offen lds\n"
"v_and_b32 v92, v171, v176\n"
"v_mfma_f32_16x16x32_bf16 v[204:207], a[8:11], v[32:35], 0\n"
"s_add_u32 s0, s0, s33\n"
"v_cvt_pk_bf16_f32 v232, v232, v233\n"
"v_cvt_pk_bf16_f32 v233, v234, v235\n"
"v_mfma_f32_16x16x32_bf16 v[204:207], a[20:23], v[36:39], v[204:207]\n"
"v_cvt_pk_bf16_f32 v240, v240, v241\n"
"v_cvt_pk_bf16_f32 v234, v236, v237\n"
"v_mfma_f32_16x16x32_bf16 v[204:207], a[32:35], v[40:43], v[204:207]\n"
"s_waitcnt lgkmcnt(3)\n"
"v_cvt_pk_bf16_f32 v235, v238, v239\n"
"v_cvt_pk_bf16_f32 v241, v242, v243\n"
"v_mfma_f32_16x16x32_bf16 v[136:139], v[136:139], v[144:147], 0\n"
"s_waitcnt lgkmcnt(2)\n"
"ds_write_b128 v196, v[232:235] offset:55296\n"
"v_mfma_f32_16x16x32_bf16 v[136:139], v[80:83], v[156:159], v[136:139]\n"
"s_waitcnt lgkmcnt(1)\n"
"v_and_b32 v93, v171, v177\n"
"v_cvt_pk_bf16_f32 v106, v106, v107\n"
"v_mfma_f32_16x16x32_bf16 v[128:131], v[128:131], v[144:147], 0\n"
"v_cvt_pk_bf16_f32 v107, v108, v109\n"
"v_cvt_pk_bf16_f32 v214, v208, v209\n"
"v_mfma_f32_16x16x32_bf16 v[128:131], v[84:87], v[158:161], v[128:131]\n"
"s_add_u32 m0, s34, 8192\n"
"ds_write_b64 v197, v[240:241] offset:55296\n"
"v_mfma_f32_16x16x32_bf16 v[140:143], v[140:143], v[144:147], 0\n"
"s_waitcnt vmcnt(11)\n"
"buffer_load_dwordx4 v190, s[8:11], s1 offen lds\n"
"v_or_b32 v201, 8, s6\n"
"v_mfma_f32_16x16x32_bf16 v[140:143], v[88:91], v[156:159], v[140:143]\n"
"ds_write_b64 v203, v[60:61] offset:43008\n"
"v_mfma_f32_16x16x16_bf16 v[252:255], v[252:253], v[92:93], 0\n"
"v_cvt_pk_bf16_f32 v215, v210, v211\n"
"v_max_f32 v136, v136, v128\n"
"v_mfma_f32_16x16x16_bf16 v[98:101], v[98:99], v[92:93], 0\n"
"v_max_f32 v137, v137, v129\n"
"v_max_f32 v138, v138, v130\n"
"v_mfma_f32_16x16x16_bf16 v[106:109], v[106:107], v[92:93], 0\n"
"s_mov_b64 exec, 1\n"
"buffer_store_dword v201, off, s[28:31], s40 sc1\n"
"s_mov_b64 exec, -1\n"
"v_mfma_f32_16x16x32_bf16 v[204:207], a[44:47], v[44:47], v[204:207]\n"
"ds_read_b64 v[92:93], v181 offset:13568\n"
"ds_read_b64_tr_b16 v[128:129], v182 offset:40584\n"
"v_mfma_f32_16x16x32_bf16 v[204:207], a[56:59], v[48:51], v[204:207]\n"
"buffer_load_dwordx2 v[60:61], v193, s[28:31], s38 offen offset:1024 sc1\n"
"v_cvt_pk_bf16_f32 v252, v252, v253\n"
"v_mfma_f32_16x16x32_bf16 v[204:207], a[68:71], v[52:55], v[204:207]\n"
"s_waitcnt lgkmcnt(2)\n"
"v_cvt_pk_bf16_f32 v253, v254, v255\n"
"v_cvt_pk_bf16_f32 v254, v98, v99\n"
"v_mfma_f32_16x16x32_bf16 v[208:211], a[80:83], v[32:35], 0\n"
"s_barrier\n"
"v_mfma_f32_16x16x32_bf16 v[208:211], a[92:95], v[36:39], v[208:211]\n"
"s_add_u32 m0, s34, 11264\n"
"ds_read_b128 v[64:67], v202 offset:55296\n"
"ds_read_b64 v[98:99], v180 offset:27072\n"
"v_mfma_f32_16x16x32_bf16 v[208:211], a[104:107], v[40:43], v[208:211]\n"
"s_add_u32 s38, s38, 1572864\n"
"buffer_load_dwordx4 v191, s[8:11], s1 offen lds\n"
"ds_read_b128 v[116:119], v178\n"
"v_mfma_f32_16x16x32_bf16 v[208:211], a[116:119], v[44:47], v[208:211]\n"
"s_add_u32 s1, s1, s33\n"
"ds_read_b128 v[124:127], v179\n"
"v_cvt_pk_bf16_f32 v255, v100, v101\n"
"v_mfma_f32_16x16x32_bf16 v[208:211], a[128:131], v[48:51], v[208:211]\n"
"s_waitcnt lgkmcnt(5)\n"
"ds_read_b64 v[100:101], v181 offset:27072\n"
"v_cvt_pk_bf16_f32 v106, v106, v107\n"
"v_mfma_f32_16x16x32_bf16 v[132:135], v[132:135], v[144:147], 0\n"
"ds_write_b128 v186, v[212:215] offset:20256\n"
"v_mfma_f32_16x16x32_bf16 v[132:135], v[92:95], v[158:161], v[132:135]\n"
"buffer_store_dwordx4 v[252:255], v192, s[28:31], s37 offen sc1\n"
"v_mfma_f32_16x16x32_bf16 v[208:211], a[140:143], v[52:55], v[208:211]\n"
"s_waitcnt lgkmcnt(4)\n"
"v_cvt_pk_bf16_f32 v107, v108, v109\n"
"v_cvt_pk_bf16_f32 v204, v204, v205\n"
"v_mfma_f32_16x16x32_bf16 v[212:215], a[72:75], v[32:35], 0\n"
"ds_read_b64 v[108:109], v179 offset:64\n"
"v_cvt_pk_bf16_f32 v205, v206, v207\n"
"v_mfma_f32_16x16x32_bf16 v[112:115], v[112:115], v[144:147], 0\n"
"v_max_f32 v139, v139, v131\n"
"v_max3_f32 v170, v136, v137, v138\n"
"v_mfma_f32_16x16x32_bf16 v[112:115], v[96:99], v[156:159], v[112:115]\n"
"s_waitcnt lgkmcnt(2)\n"
"ds_read_b64_tr_b16 v[130:131], v183 offset:40584\n"
"v_max_f32 v140, v140, v132\n"
"v_mfma_f32_16x16x32_bf16 v[212:215], a[84:87], v[36:39], v[212:215]\n"
"v_max_f32 v141, v141, v133\n"
"v_max_f32 v142, v142, v134\n"
"v_mfma_f32_16x16x32_bf16 v[120:123], v[120:123], v[144:147], 0\n"
"ds_read_b64_tr_b16 v[132:133], v182 offset:41352\n"
"v_max_f32 v143, v143, v135\n"
"v_mfma_f32_16x16x32_bf16 v[120:123], v[100:103], v[158:161], v[120:123]\n"
"ds_read_b64_tr_b16 v[134:135], v183 offset:41352\n"
"v_max3_f32 v170, v170, v139, v140\n"
"v_mfma_f32_16x16x32_bf16 v[212:215], a[96:99], v[40:43], v[212:215]\n"
"buffer_store_dwordx2 v[106:107], v193, s[28:31], s37 offen offset:1024 sc1\n"
"v_mfma_f32_16x16x32_bf16 v[212:215], a[108:111], v[44:47], v[212:215]\n"
"s_waitcnt lgkmcnt(3)\n"
"ds_read_b64 v[106:107], v178 offset:64\n"
"v_max3_f32 v170, v170, v141, v142\n"
"v_mfma_f32_16x16x32_bf16 v[124:127], v[124:127], v[144:147], 0\n"
"s_add_u32 s37, s37, 1572864\n"
"v_cvt_pk_bf16_f32 v206, v208, v209\n"
"v_cvt_pk_bf16_f32 v207, v210, v211\n"
"v_mfma_f32_16x16x32_bf16 v[124:127], v[108:111], v[158:161], v[124:127]\n"
"s_add_u32 s4, s37, 6291456\n"
"ds_write_b128 v186, v[204:207] offset:20320\n"
"v_mfma_f32_16x16x32_bf16 v[212:215], a[120:123], v[48:51], v[212:215]\n"
"v_max_f32 v112, v112, v120\n"
"v_max_f32 v113, v113, v121\n"
"v_mfma_f32_16x16x32_bf16 v[212:215], a[132:135], v[52:55], v[212:215]\n"
"v_max_f32 v114, v114, v122\n"
"v_max_f32 v115, v115, v123\n"
"v_mfma_f32_16x16x32_bf16 v[208:211], a[76:79], v[32:35], 0\n"
"buffer_store_dwordx4 v[64:67], v194, s[24:27], s36 offen sc1\n"
"v_mfma_f32_16x16x32_bf16 v[208:211], a[88:91], v[36:39], v[208:211]\n"
"s_waitcnt lgkmcnt(1)\n"
"ds_read_b128 v[64:67], v192 offset:43008\n"
"ds_read_b64_tr_b16 v[120:121], v184 offset:72\n"
"v_mfma_f32_16x16x32_bf16 v[116:119], v[116:119], v[144:147], 0\n"
"ds_read_b64_tr_b16 v[122:123], v185 offset:72\n"
"v_mov_b64 v[68:69], -1\n"
"v_mfma_f32_16x16x32_bf16 v[116:119], v[104:107], v[156:159], v[116:119]\n"
"v_max3_f32 v170, v170, v143, v112\n"
"v_max3_f32 v170, v170, v113, v114\n"
"v_mfma_f32_16x16x32_bf16 v[208:211], a[100:103], v[40:43], v[208:211]\n"
"v_mov_b64 v[252:253], 0\n"
"v_mov_b64 v[254:255], 0\n"
"v_mfma_f32_16x16x32_bf16 v[208:211], a[112:115], v[44:47], v[208:211]\n"
"v_mov_b64 v[98:99], 0\n"
"v_mov_b64 v[100:101], 0\n"
"v_mfma_f32_16x16x32_bf16 v[208:211], a[124:127], v[48:51], v[208:211]\n"
"v_max_f32 v116, v116, v124\n"
"v_max_f32 v117, v117, v125\n"
"v_mfma_f32_16x16x32_bf16 v[208:211], a[136:139], v[52:55], v[208:211]\n"
"ds_read_b64_tr_b16 v[124:125], v184 offset:840\n"
"v_max_f32 v118, v118, v126\n"
"v_mfma_f32_16x16x32_bf16 v[204:207], a[144:147], v[32:35], 0\n"
"s_waitcnt lgkmcnt(3)\n"
"buffer_store_dwordx2 v[68:69], v193, s[28:31], s4 offen offset:1024 sc1\n"
"v_mfma_f32_16x16x32_bf16 v[204:207], a[156:159], v[36:39], v[204:207]\n"
"ds_read_b64 v[68:69], v62 offset:55296\n"
"v_max_f32 v119, v119, v127\n"
"v_mfma_f32_16x16x32_bf16 v[204:207], a[168:171], v[40:43], v[204:207]\n"
"ds_read_b64_tr_b16 v[126:127], v185 offset:840\n"
"v_max3_f32 v170, v170, v115, v116\n"
"v_mfma_f32_16x16x32_bf16 v[204:207], a[180:183], v[44:47], v[204:207]\n"
"v_max3_f32 v170, v170, v117, v118\n"
"v_mov_b64 v[106:107], 0\n"
"v_mfma_f32_16x16x32_bf16 v[232:235], a[216:219], v[64:67], 0\n"
"v_mov_b64 v[108:109], 0\n"
"v_max_f32 v171, v170, v119\n"
"v_mfma_f32_16x16x32_bf16 v[236:239], a[220:223], v[64:67], 0\n"
"v_max_f32 v170, v170, v119\n"
"v_cvt_pk_bf16_f32 v212, v212, v213\n"
"v_mfma_f32_16x16x32_bf16 v[240:243], a[224:227], v[64:67], 0\n"
"ds_read_b128 v[64:67], v192 offset:45056\n"
"v_cvt_pk_bf16_f32 v213, v214, v215\n"
"v_mfma_f32_16x16x32_bf16 v[204:207], a[192:195], v[48:51], v[204:207]\n"
"s_waitcnt lgkmcnt(2)\n"
"v_permlane16_swap_b32 v170, v171\n"
"v_mfma_f32_16x16x32_bf16 v[204:207], a[204:207], v[52:55], v[204:207]\n"
"buffer_store_dwordx2 v[68:69], v195, s[24:27], s36 offen sc1\n"
"ds_read_b128 v[68:71], v192 offset:44032\n"
"s_add_u32 s36, s36, s33\n"
"v_max_f32 v170, v170, v171\n"
"v_mov_b32 v171, v170\n"
"v_cvt_pk_bf16_f32 v214, v208, v209\n"
"v_cvt_pk_bf16_f32 v215, v210, v211\n"
"v_mfma_f32_16x16x32_bf16 v[208:211], a[148:151], v[32:35], 0\n"
"ds_write_b128 v186, v[212:215] offset:20384\n"
"v_mfma_f32_16x16x32_bf16 v[208:211], a[160:163], v[36:39], v[208:211]\n"
"v_permlane32_swap_b32 v170, v171\n"
"v_max_f32 v170, v170, v171\n"
"v_mul_f32 v170, 0xbe553b95, v170\n"
"v_pk_fma_f32 v[136:137], v[136:137], s[70:71], v[170:171] op_sel_hi:[1,0,0]\n"
"v_pk_fma_f32 v[138:139], v[138:139], s[70:71], v[170:171] op_sel_hi:[1,0,0]\n"
"v_pk_fma_f32 v[140:141], v[140:141], s[70:71], v[170:171] op_sel_hi:[1,0,0]\n"
"v_pk_fma_f32 v[142:143], v[142:143], s[70:71], v[170:171] op_sel_hi:[1,0,0]\n"
"v_pk_fma_f32 v[112:113], v[112:113], s[70:71], v[170:171] op_sel_hi:[1,0,0]\n"
"v_pk_fma_f32 v[114:115], v[114:115], s[70:71], v[170:171] op_sel_hi:[1,0,0]\n"
"v_pk_fma_f32 v[116:117], v[116:117], s[70:71], v[170:171] op_sel_hi:[1,0,0]\n"
"v_pk_fma_f32 v[118:119], v[118:119], s[70:71], v[170:171] op_sel_hi:[1,0,0]\n"
"v_mfma_f32_16x16x32_bf16 v[208:211], a[172:175], v[40:43], v[208:211]\n"
"s_waitcnt lgkmcnt(1)\n"
"v_exp_f32 v136, v136\n"
"v_mfma_f32_16x16x32_bf16 v[208:211], a[184:187], v[44:47], v[208:211]\n"
"v_exp_f32 v137, v137\n"
"v_mfma_f32_16x16x32_bf16 v[208:211], a[196:199], v[48:51], v[208:211]\n"
"v_exp_f32 v138, v138\n"
"v_mfma_f32_16x16x32_bf16 v[208:211], a[208:211], v[52:55], v[208:211]\n"
"v_exp_f32 v139, v139\n"
"v_mfma_f32_16x16x32_bf16 v[232:235], a[228:231], v[68:71], v[232:235]\n"
"v_cvt_pk_bf16_f32 v82, v136, v138\n"
"v_cvt_pk_bf16_f32 v83, v137, v139\n"
"v_mfma_f32_16x16x32_bf16 v[232:235], a[240:243], v[64:67], v[232:235]\n"
"ds_read_b128 v[136:139], v180 offset:13504\n"
"v_exp_f32 v140, v140\n"
"v_mfma_f32_16x16x32_bf16 v[236:239], a[232:235], v[68:71], v[236:239]\n"
"v_exp_f32 v141, v141\n"
"v_mfma_f32_16x16x32_bf16 v[236:239], a[244:247], v[64:67], v[236:239]\n"
"v_exp_f32 v142, v142\n"
"v_mfma_f32_16x16x32_bf16 v[240:243], a[236:239], v[68:71], v[240:243]\n"
"ds_read_b128 v[68:71], v192 offset:46080\n"
"v_cvt_pk_bf16_f32 v84, v140, v142\n"
"v_mfma_f32_16x16x32_bf16 v[240:243], a[248:251], v[64:67], v[240:243]\n"
"ds_read_b128 v[64:67], v192 offset:47104\n"
"v_exp_f32 v143, v143\n"
"v_mfma_f32_16x16x32_bf16 v[212:215], a[152:155], v[32:35], 0\n"
"ds_read_b128 v[32:35], v188\n"
"v_cvt_pk_bf16_f32 v85, v141, v143\n"
"v_mfma_f32_16x16x32_bf16 v[212:215], a[164:167], v[36:39], v[212:215]\n"
".Lic5%=:\n"
"s_cbranch_execz .Lib1%=\n"
"ds_read_b128 v[36:39], v189\n"
"ds_read_b128 v[140:143], v180 offset:27008\n"
"v_mfma_f32_16x16x32_bf16 v[212:215], a[176:179], v[40:43], v[212:215]\n"
"ds_read_b128 v[40:43], v188 offset:1024\n"
"v_cvt_pk_bf16_f32 v144, v204, v205\n"
"v_mfma_f32_16x16x32_bf16 v[212:215], a[188:191], v[44:47], v[212:215]\n"
"ds_read_b128 v[44:47], v189 offset:1024\n"
"v_cvt_pk_bf16_f32 v145, v206, v207\n"
"v_mfma_f32_16x16x32_bf16 v[212:215], a[200:203], v[48:51], v[212:215]\n"
"s_waitcnt vmcnt(17)\n"
"ds_read_b128 v[48:51], v188 offset:2048\n"
"v_cvt_pk_bf16_f32 v146, v208, v209\n"
"v_mfma_f32_16x16x32_bf16 v[212:215], a[212:215], v[52:55], v[212:215]\n"
"s_waitcnt lgkmcnt(7)\n"
"ds_read_b128 v[52:55], v189 offset:2048\n"
"v_exp_f32 v112, v112\n"
"v_smfmac_f32_16x16x64_bf16 v[252:255], v[82:85], v[224:231], v63\n"
"ds_read_b64_tr_b16 v[224:225], v184 offset:128\n"
"v_smfmac_f32_16x16x64_bf16 v[98:101], v[82:85], v[244:251], v63\n"
"s_waitcnt lgkmcnt(7)\n"
"ds_read_b64_tr_b16 v[226:227], v185 offset:128\n"
"v_smfmac_f32_16x16x64_bf16 v[106:109], v[82:85], v[128:135], v63\n"
"s_waitcnt lgkmcnt(7)\n"
"ds_read_b64_tr_b16 v[228:229], v184 offset:896\n"
"v_mfma_f32_16x16x32_bf16 v[82:85], v[172:175], v[82:85], 0\n"
"s_add_u32 m0, s34, 0\n"
"ds_read_b64_tr_b16 v[230:231], v185 offset:896\n"
"buffer_load_dwordx4 v190, s[8:11], s32 offen lds\n"
"v_mfma_f32_16x16x32_bf16 v[232:235], a[252:255], v[68:71], v[232:235]\n"
"s_add_u32 s32, s32, s33\n"
"ds_read_b64_tr_b16 v[244:245], v184 offset:136\n"
"v_cvt_pk_bf16_f32 v147, v210, v211\n"
"v_mfma_f32_16x16x32_bf16 v[232:235], v[8:11], v[64:67], v[232:235]\n"
"ds_read_b64_tr_b16 v[246:247], v185 offset:136\n"
"v_exp_f32 v113, v113\n"
"v_mfma_f32_16x16x32_bf16 v[236:239], v[0:3], v[68:71], v[236:239]\n"
"ds_read_b64_tr_b16 v[248:249], v184 offset:904\n"
"v_exp_f32 v114, v114\n"
"v_mfma_f32_16x16x32_bf16 v[236:239], v[12:15], v[64:67], v[236:239]\n"
"ds_read_b64_tr_b16 v[250:251], v185 offset:904\n"
"ds_read_b128 v[128:131], v181 offset:13504\n"
"v_mfma_f32_16x16x32_bf16 v[240:243], v[4:7], v[68:71], v[240:243]\n"
"v_exp_f32 v115, v115\n"
"v_mfma_f32_16x16x32_bf16 v[240:243], v[16:19], v[64:67], v[240:243]\n"
"s_waitcnt vmcnt(11)\n"
"ds_read_b128 v[68:71], v192 offset:48128\n"
"ds_write_b128 v202, v[56:59] offset:30720\n"
"v_mfma_f32_16x16x32_bf16 v[204:207], a[0:3], v[32:35], 0\n"
"s_waitcnt lgkmcnt(11)\n"
"buffer_load_dwordx4 v[56:59], v192, s[28:31], s38 offen sc1\n"
"v_cvt_pk_bf16_f32 v90, v112, v114\n"
"v_mfma_f32_16x16x32_bf16 v[204:207], a[12:15], v[36:39], v[204:207]\n"
"v_cvt_pk_bf16_f32 v91, v113, v115\n"
"v_exp_f32 v116, v116\n"
"v_mfma_f32_16x16x32_bf16 v[204:207], a[24:27], v[40:43], v[204:207]\n"
"ds_read_b128 v[132:135], v181 offset:27008\n"
"ds_read_b128 v[112:115], v178\n"
"v_mfma_f32_16x16x32_bf16 v[204:207], a[36:39], v[44:47], v[204:207]\n"
"s_add_u32 m0, s34, 3072\n"
"v_exp_f32 v117, v117\n"
"v_mfma_f32_16x16x32_bf16 v[204:207], a[48:51], v[48:51], v[204:207]\n"
"buffer_load_dwordx4 v199, s[8:11], s75 offen lds\n"
"v_exp_f32 v118, v118\n"
"v_mfma_f32_16x16x32_bf16 v[204:207], a[60:63], v[52:55], v[204:207]\n"
"s_waitcnt lgkmcnt(3)\n"
"v_exp_f32 v119, v119\n"
"v_mfma_f32_16x16x32_bf16 v[232:235], v[20:23], v[68:71], v[232:235]\n"
"v_cvt_pk_bf16_f32 v92, v116, v118\n"
"v_cvt_pk_bf16_f32 v93, v117, v119\n"
"v_mfma_f32_16x16x32_bf16 v[240:243], v[28:31], v[68:71], v[240:243]\n"
"v_cvt_pk_bf16_f32 v158, v212, v213\n"
"v_cvt_pk_bf16_f32 v159, v214, v215\n"
"v_smfmac_f32_16x16x64_bf16 v[252:255], v[90:93], v[224:231], v63\n"
"v_cvt_pk_bf16_f32 v204, v204, v205\n"
"v_smfmac_f32_16x16x64_bf16 v[98:101], v[90:93], v[244:251], v63\n"
"v_cvt_pk_bf16_f32 v232, v232, v233\n"
"v_mfma_f32_16x16x32_bf16 v[236:239], v[24:27], v[68:71], v[236:239]\n"
"v_cvt_pk_bf16_f32 v233, v234, v235\n"
"v_cvt_pk_bf16_f32 v240, v240, v241\n"
"v_smfmac_f32_16x16x64_bf16 v[106:109], v[90:93], v[120:127], v63\n"
"s_add_u32 m0, s34, 1024\n"
"s_nop 0\n"
"buffer_load_dwordx4 v190, s[8:11], s0 offen lds\n"
"v_mfma_f32_16x16x32_bf16 v[90:93], v[172:175], v[90:93], v[82:85]\n"
"s_add_u32 s0, s0, s33\n"
"ds_read_b128 v[120:123], v179\n"
"v_cvt_pk_bf16_f32 v234, v236, v237\n"
"v_mfma_f32_16x16x32_bf16 v[208:211], a[4:7], v[32:35], 0\n"
"ds_read_b64 v[82:83], v180 offset:13568\n"
"ds_read_b64 v[84:85], v181 offset:13568\n"
"v_mfma_f32_16x16x32_bf16 v[208:211], a[16:19], v[36:39], v[208:211]\n"
"v_cvt_pk_bf16_f32 v235, v238, v239\n"
"v_cvt_pk_bf16_f32 v241, v242, v243\n"
"v_mfma_f32_16x16x32_bf16 v[208:211], a[28:31], v[40:43], v[208:211]\n"
"s_waitcnt vmcnt(10)\n"
"ds_write_b64 v203, v[60:61] offset:30720\n"
"v_mfma_f32_16x16x32_bf16 v[208:211], a[40:43], v[44:47], v[208:211]\n"
"buffer_load_dword v200, off, s[28:31], s39 sc1\n"
"v_cvt_pk_bf16_f32 v252, v252, v253\n"
"v_mfma_f32_16x16x32_bf16 v[208:211], a[52:55], v[48:51], v[208:211]\n"
"ds_write_b128 v196, v[232:235] offset:49152\n"
"v_mfma_f32_16x16x32_bf16 v[208:211], a[64:67], v[52:55], v[208:211]\n"
"s_add_u32 m0, s34, 4096\n"
"buffer_load_dwordx2 v[60:61], v193, s[28:31], s38 offen offset:1024 sc1\n"
"v_cvt_pk_bf16_f32 v253, v254, v255\n"
"v_mfma_f32_16x16x32_bf16 v[212:215], a[8:11], v[32:35], 0\n"
"s_waitcnt lgkmcnt(3)\n"
"v_rcp_f32 v171, v90\n"
"v_mfma_f32_16x16x32_bf16 v[136:139], v[136:139], v[148:151], 0\n"
"s_waitcnt lgkmcnt(2)\n"
"ds_read_b64 v[90:91], v180 offset:27072\n"
"ds_write_b64 v197, v[240:241] offset:49152\n"
"v_mfma_f32_16x16x32_bf16 v[136:139], v[80:83], v[160:163], v[136:139]\n"
"s_add_u32 s38, s38, 1572864\n"
"v_cvt_pk_bf16_f32 v171, v171, v171\n"
"v_and_b32 v92, v171, v176\n"
"v_mfma_f32_16x16x32_bf16 v[128:131], v[128:131], v[148:151], 0\n"
"buffer_load_dwordx4 v199, s[8:11], s76 offen lds\n"
"v_and_b32 v93, v171, v177\n"
"v_mfma_f32_16x16x32_bf16 v[128:131], v[84:87], v[162:165], v[128:131]\n"
"v_cvt_pk_bf16_f32 v98, v98, v99\n"
"v_cvt_pk_bf16_f32 v99, v100, v101\n"
"v_mfma_f32_16x16x16_bf16 v[252:255], v[252:253], v[92:93], 0\n"
"v_cvt_pk_bf16_f32 v106, v106, v107\n"
"v_cvt_pk_bf16_f32 v107, v108, v109\n"
"v_mfma_f32_16x16x16_bf16 v[98:101], v[98:99], v[92:93], 0\n"
"v_cvt_pk_bf16_f32 v205, v206, v207\n"
"v_cvt_pk_bf16_f32 v206, v208, v209\n"
"v_mfma_f32_16x16x16_bf16 v[106:109], v[106:107], v[92:93], 0\n"
"s_waitcnt lgkmcnt(1)\n"
"ds_read_b64 v[92:93], v181 offset:27072\n"
"v_cvt_pk_bf16_f32 v252, v252, v253\n"
"v_mfma_f32_16x16x32_bf16 v[212:215], a[20:23], v[36:39], v[212:215]\n"
"s_waitcnt lgkmcnt(1)\n"
"v_cvt_pk_bf16_f32 v253, v254, v255\n"
"v_cvt_pk_bf16_f32 v254, v98, v99\n"
"v_mfma_f32_16x16x32_bf16 v[140:143], v[140:143], v[148:151], 0\n"
"s_barrier\n"
"v_mfma_f32_16x16x32_bf16 v[140:143], v[88:91], v[160:163], v[140:143]\n"
"ds_read_b128 v[64:67], v202 offset:49152\n"
"ds_read_b64 v[68:69], v62 offset:49152\n"
"v_mfma_f32_16x16x32_bf16 v[212:215], a[32:35], v[40:43], v[212:215]\n"
"s_add_u32 m0, s34, 2048\n"
"ds_read_b64 v[98:99], v178 offset:64\n"
"buffer_load_dwordx4 v190, s[8:11], s1 offen lds\n"
"v_mfma_f32_16x16x32_bf16 v[212:215], a[44:47], v[44:47], v[212:215]\n"
"s_add_u32 s1, s1, s33\n"
"ds_read_b128 v[116:119], v178 offset:13504\n"
"v_cvt_pk_bf16_f32 v255, v100, v101\n"
"v_mfma_f32_16x16x32_bf16 v[212:215], a[56:59], v[48:51], v[212:215]\n"
"ds_read_b64 v[100:101], v179 offset:64\n"
"v_cvt_pk_bf16_f32 v106, v106, v107\n"
"v_mfma_f32_16x16x32_bf16 v[212:215], a[68:71], v[52:55], v[212:215]\n"
"s_waitcnt lgkmcnt(5)\n"
"ds_read_b128 v[124:127], v179 offset:13504\n"
"v_cvt_pk_bf16_f32 v107, v108, v109\n"
"v_mfma_f32_16x16x32_bf16 v[132:135], v[132:135], v[148:151], 0\n"
"ds_read_b64 v[108:109], v179 offset:13568\n"
"buffer_store_dwordx4 v[252:255], v192, s[28:31], s37 offen sc1\n"
"v_mfma_f32_16x16x32_bf16 v[132:135], v[92:95], v[162:165], v[132:135]\n"
"s_waitcnt lgkmcnt(5)\n"
"v_cvt_pk_bf16_f32 v207, v210, v211\n"
"v_cvt_pk_bf16_f32 v212, v212, v213\n"
"v_mfma_f32_16x16x32_bf16 v[208:211], a[80:83], v[32:35], 0\n"
"s_waitcnt lgkmcnt(4)\n"
"ds_write_b128 v186, v[204:207] offset:33760\n"
"v_mfma_f32_16x16x32_bf16 v[112:115], v[112:115], v[148:151], 0\n"
"v_cvt_pk_bf16_f32 v213, v214, v215\n"
"v_max_f32 v136, v136, v128\n"
"v_mfma_f32_16x16x32_bf16 v[112:115], v[96:99], v[160:163], v[112:115]\n"
"s_waitcnt lgkmcnt(3)\n"
"v_max_f32 v137, v137, v129\n"
"v_max_f32 v138, v138, v130\n"
"v_mfma_f32_16x16x32_bf16 v[120:123], v[120:123], v[148:151], 0\n"
"ds_read_b64_tr_b16 v[128:129], v182 offset:54088\n"
"v_max_f32 v139, v139, v131\n"
"v_mfma_f32_16x16x32_bf16 v[120:123], v[100:103], v[162:165], v[120:123]\n"
"s_waitcnt lgkmcnt(2)\n"
"ds_read_b64_tr_b16 v[130:131], v183 offset:54088\n"
"v_max3_f32 v170, v136, v137, v138\n"
"v_mfma_f32_16x16x32_bf16 v[124:127], v[124:127], v[148:151], 0\n"
"s_add_u32 m0, s34, 5120\n"
"v_max_f32 v140, v140, v132\n"
"v_max_f32 v141, v141, v133\n"
"v_mfma_f32_16x16x32_bf16 v[124:127], v[108:111], v[162:165], v[124:127]\n"
"buffer_load_dwordx4 v199, s[8:11], s77 offen lds\n"
"ds_read_b64_tr_b16 v[132:133], v182 offset:54856\n"
"v_mfma_f32_16x16x32_bf16 v[208:211], a[92:95], v[36:39], v[208:211]\n"
"s_mul_i32 s8, s33, 0\n"
"v_max_f32 v142, v142, v134\n"
"v_max_f32 v143, v143, v135\n"
"v_mfma_f32_16x16x32_bf16 v[208:211], a[104:107], v[40:43], v[208:211]\n"
"s_mul_i32 s9, s33, 1\n"
"ds_read_b64_tr_b16 v[134:135], v183 offset:54856\n"
"v_max3_f32 v170, v170, v139, v140\n"
"v_mfma_f32_16x16x32_bf16 v[208:211], a[116:119], v[44:47], v[208:211]\n"
"s_mul_i32 s10, s33, 2\n"
"v_max3_f32 v170, v170, v141, v142\n"
"v_max_f32 v112, v112, v120\n"
"v_mfma_f32_16x16x32_bf16 v[208:211], a[128:131], v[48:51], v[208:211]\n"
"s_mul_i32 s11, s33, 4\n"
"buffer_store_dwordx2 v[106:107], v193, s[28:31], s37 offen offset:1024 sc1\n"
"v_mfma_f32_16x16x32_bf16 v[208:211], a[140:143], v[52:55], v[208:211]\n"
"s_add_u32 s37, s37, 1572864\n"
"ds_read_b64 v[106:107], v178 offset:13568\n"
"v_max_f32 v113, v113, v121\n"
"v_mfma_f32_16x16x32_bf16 v[204:207], a[72:75], v[32:35], 0\n"
"ds_read_b64_tr_b16 v[120:121], v182 offset:72\n"
"v_max_f32 v114, v114, v122\n"
"v_mfma_f32_16x16x32_bf16 v[204:207], a[84:87], v[36:39], v[204:207]\n"
"v_max_f32 v115, v115, v123\n"
"v_cvt_pk_bf16_f32 v214, v208, v209\n"
"v_mfma_f32_16x16x32_bf16 v[204:207], a[96:99], v[40:43], v[204:207]\n"
"v_cvt_pk_bf16_f32 v215, v210, v211\n"
"v_max3_f32 v170, v170, v143, v112\n"
"v_mfma_f32_16x16x32_bf16 v[204:207], a[108:111], v[44:47], v[204:207]\n"
"ds_write_b128 v186, v[212:215] offset:33824\n"
"v_mfma_f32_16x16x32_bf16 v[204:207], a[120:123], v[48:51], v[204:207]\n"
"buffer_store_dwordx4 v[64:67], v194, s[24:27], s36 offen sc1\n"
"v_mfma_f32_16x16x32_bf16 v[204:207], a[132:135], v[52:55], v[204:207]\n"
"s_waitcnt lgkmcnt(2)\n"
"ds_read_b128 v[64:67], v192 offset:30720\n"
"v_max3_f32 v170, v170, v113, v114\n"
"v_mfma_f32_16x16x32_bf16 v[116:119], v[116:119], v[148:151], 0\n"
"s_mov_b64 exec, s[46:47]\n"
"v_add_u32 v183, 0xc0, v183\n"
"s_mov_b64 exec, -1\n"
"v_mfma_f32_16x16x32_bf16 v[116:119], v[104:107], v[160:163], v[116:119]\n"
"ds_read_b64_tr_b16 v[122:123], v183 offset:72\n"
"v_cvt_pk_bf16_f32 v204, v204, v205\n"
"v_mfma_f32_16x16x32_bf16 v[208:211], a[76:79], v[32:35], 0\n"
"v_cvt_pk_bf16_f32 v205, v206, v207\n"
"v_mov_b64 v[252:253], 0\n"
"v_mfma_f32_16x16x32_bf16 v[208:211], a[88:91], v[36:39], v[208:211]\n"
"v_mov_b64 v[254:255], 0\n"
"v_mov_b64 v[98:99], 0\n"
"v_mfma_f32_16x16x32_bf16 v[208:211], a[100:103], v[40:43], v[208:211]\n"
"v_max_f32 v116, v116, v124\n"
"v_max_f32 v117, v117, v125\n"
"v_mfma_f32_16x16x32_bf16 v[208:211], a[112:115], v[44:47], v[208:211]\n"
"ds_read_b64_tr_b16 v[124:125], v182 offset:840\n"
"v_max_f32 v118, v118, v126\n"
"v_mfma_f32_16x16x32_bf16 v[208:211], a[124:127], v[48:51], v[208:211]\n"
"s_waitcnt lgkmcnt(2)\n"
"buffer_store_dwordx2 v[68:69], v195, s[24:27], s36 offen sc1\n"
"v_mfma_f32_16x16x32_bf16 v[208:211], a[136:139], v[52:55], v[208:211]\n"
"s_add_u32 s36, s36, s33\n"
"ds_read_b128 v[68:71], v192 offset:31744\n"
"v_max_f32 v119, v119, v127\n"
"v_mfma_f32_16x16x32_bf16 v[232:235], a[216:219], v[64:67], 0\n"
"ds_read_b64_tr_b16 v[126:127], v183 offset:840\n"
"v_max3_f32 v170, v170, v115, v116\n"
"v_mfma_f32_16x16x32_bf16 v[236:239], a[220:223], v[64:67], 0\n"
"v_max3_f32 v170, v170, v117, v118\n"
"v_cvt_pk_bf16_f32 v206, v208, v209\n"
"v_mfma_f32_16x16x32_bf16 v[240:243], a[224:227], v[64:67], 0\n"
"ds_read_b128 v[64:67], v192 offset:32768\n"
"v_cvt_pk_bf16_f32 v207, v210, v211\n"
"v_mfma_f32_16x16x32_bf16 v[208:211], a[148:151], v[32:35], 0\n"
"ds_write_b128 v186, v[204:207] offset:33888\n"
"v_mfma_f32_16x16x32_bf16 v[208:211], a[160:163], v[36:39], v[208:211]\n"
"v_mov_b64 v[100:101], 0\n"
"v_mov_b64 v[106:107], 0\n"
"v_mfma_f32_16x16x32_bf16 v[208:211], a[172:175], v[40:43], v[208:211]\n"
"s_waitcnt lgkmcnt(3)\n"
"v_mov_b64 v[108:109], 0\n"
"v_max_f32 v171, v170, v119\n"
"v_mfma_f32_16x16x32_bf16 v[208:211], a[184:187], v[44:47], v[208:211]\n"
"v_max_f32 v170, v170, v119\n"
"v_mfma_f32_16x16x32_bf16 v[208:211], a[196:199], v[48:51], v[208:211]\n"
"s_nop 0\n"
"v_permlane16_swap_b32 v170, v171\n"
"v_mfma_f32_16x16x32_bf16 v[204:207], a[152:155], v[32:35], 0\n"
"v_max_f32 v170, v170, v171\n"
"v_mov_b32 v171, v170\n"
"v_mfma_f32_16x16x32_bf16 v[204:207], a[164:167], v[36:39], v[204:207]\n"
"s_nop 0\n"
"v_permlane32_swap_b32 v170, v171\n"
"v_max_f32 v170, v170, v171\n"
"v_mul_f32 v170, 0xbe553b95, v170\n"
"v_pk_fma_f32 v[136:137], v[136:137], s[70:71], v[170:171] op_sel_hi:[1,0,0]\n"
"v_pk_fma_f32 v[138:139], v[138:139], s[70:71], v[170:171] op_sel_hi:[1,0,0]\n"
"v_pk_fma_f32 v[140:141], v[140:141], s[70:71], v[170:171] op_sel_hi:[1,0,0]\n"
"v_pk_fma_f32 v[142:143], v[142:143], s[70:71], v[170:171] op_sel_hi:[1,0,0]\n"
"v_pk_fma_f32 v[112:113], v[112:113], s[70:71], v[170:171] op_sel_hi:[1,0,0]\n"
"v_pk_fma_f32 v[114:115], v[114:115], s[70:71], v[170:171] op_sel_hi:[1,0,0]\n"
"v_pk_fma_f32 v[116:117], v[116:117], s[70:71], v[170:171] op_sel_hi:[1,0,0]\n"
"v_pk_fma_f32 v[118:119], v[118:119], s[70:71], v[170:171] op_sel_hi:[1,0,0]\n"
"v_mfma_f32_16x16x32_bf16 v[204:207], a[176:179], v[40:43], v[204:207]\n"
"v_exp_f32 v136, v136\n"
"v_mfma_f32_16x16x32_bf16 v[204:207], a[188:191], v[44:47], v[204:207]\n"
"v_exp_f32 v137, v137\n"
"v_mfma_f32_16x16x32_bf16 v[204:207], a[200:203], v[48:51], v[204:207]\n"
"v_exp_f32 v138, v138\n"
"v_mfma_f32_16x16x32_bf16 v[232:235], a[228:231], v[68:71], v[232:235]\n"
"s_waitcnt lgkmcnt(1)\n"
"v_exp_f32 v139, v139\n"
"v_mfma_f32_16x16x32_bf16 v[232:235], a[240:243], v[64:67], v[232:235]\n"
"v_cvt_pk_bf16_f32 v82, v136, v138\n"
"v_cvt_pk_bf16_f32 v83, v137, v139\n"
"v_mfma_f32_16x16x32_bf16 v[236:239], a[232:235], v[68:71], v[236:239]\n"
"ds_read_b128 v[136:139], v180 offset:27008\n"
"v_exp_f32 v140, v140\n"
"v_mfma_f32_16x16x32_bf16 v[236:239], a[244:247], v[64:67], v[236:239]\n"
"v_exp_f32 v141, v141\n"
"v_mfma_f32_16x16x32_bf16 v[212:215], a[144:147], v[32:35], 0\n"
"ds_read_b128 v[32:35], v188 offset:6144\n"
"v_exp_f32 v142, v142\n"
"v_mfma_f32_16x16x32_bf16 v[212:215], a[156:159], v[36:39], v[212:215]\n"
"ds_read_b128 v[36:39], v189 offset:6144\n"
"v_cvt_pk_bf16_f32 v84, v140, v142\n"
"v_mfma_f32_16x16x32_bf16 v[212:215], a[168:171], v[40:43], v[212:215]\n"
"ds_read_b128 v[40:43], v188 offset:7168\n"
"v_exp_f32 v143, v143\n"
"v_mfma_f32_16x16x32_bf16 v[212:215], a[180:183], v[44:47], v[212:215]\n"
"s_waitcnt vmcnt(11)\n"
"ds_read_b128 v[44:47], v189 offset:7168\n"
"ds_write_b128 v202, v[56:59] offset:55296\n"
"v_mfma_f32_16x16x32_bf16 v[212:215], a[192:195], v[48:51], v[212:215]\n"
"v_cvt_pk_bf16_f32 v85, v141, v143\n"
"v_exp_f32 v112, v112\n"
"v_mfma_f32_16x16x32_bf16 v[212:215], a[204:207], v[52:55], v[212:215]\n"
"ds_read_b128 v[48:51], v188 offset:8192\n"
"ds_read_b128 v[140:143], v178\n"
"v_mfma_f32_16x16x32_bf16 v[240:243], a[236:239], v[68:71], v[240:243]\n"
"ds_read_b128 v[68:71], v192 offset:33792\n"
"v_exp_f32 v113, v113\n"
"v_mfma_f32_16x16x32_bf16 v[240:243], a[248:251], v[64:67], v[240:243]\n"
"ds_read_b128 v[64:67], v192 offset:34816\n"
"v_exp_f32 v114, v114\n"
"v_smfmac_f32_16x16x64_bf16 v[252:255], v[82:85], v[216:223], v63\n"
"ds_read_b64_tr_b16 v[216:217], v182 offset:128\n"
"v_smfmac_f32_16x16x64_bf16 v[98:101], v[82:85], v[72:79], v63\n"
"ds_read_b64_tr_b16 v[218:219], v183 offset:128\n"
"v_smfmac_f32_16x16x64_bf16 v[106:109], v[82:85], v[128:135], v63\n"
"ds_read_b64_tr_b16 v[220:221], v182 offset:896\n"
"v_mfma_f32_16x16x32_bf16 v[82:85], v[172:175], v[82:85], 0\n"
"ds_read_b64_tr_b16 v[222:223], v183 offset:896\n"
"v_cvt_pk_bf16_f32 v148, v212, v213\n"
"v_mfma_f32_16x16x32_bf16 v[208:211], a[208:211], v[52:55], v[208:211]\n"
"ds_read_b64_tr_b16 v[72:73], v182 offset:136\n"
"v_cvt_pk_bf16_f32 v149, v214, v215\n"
"v_mfma_f32_16x16x32_bf16 v[204:207], a[212:215], v[52:55], v[204:207]\n"
"s_waitcnt lgkmcnt(6)\n"
"ds_read_b128 v[52:55], v189 offset:8192\n"
"v_cvt_pk_bf16_f32 v90, v112, v114\n"
"v_mfma_f32_16x16x32_bf16 v[212:215], a[0:3], v[32:35], 0\n"
"ds_read_b64_tr_b16 v[74:75], v183 offset:136\n"
"v_cvt_pk_bf16_f32 v150, v208, v209\n"
"v_mfma_f32_16x16x32_bf16 v[212:215], a[12:15], v[36:39], v[212:215]\n"
"ds_read_b64_tr_b16 v[76:77], v182 offset:904\n"
"v_cvt_pk_bf16_f32 v151, v210, v211\n"
"v_mfma_f32_16x16x32_bf16 v[212:215], a[24:27], v[40:43], v[212:215]\n"
"ds_read_b64_tr_b16 v[78:79], v183 offset:904\n"
"v_exp_f32 v115, v115\n"
"v_mfma_f32_16x16x32_bf16 v[212:215], a[36:39], v[44:47], v[212:215]\n"
"ds_read_b128 v[128:131], v181 offset:27008\n"
"ds_read_b128 v[132:135], v179\n"
"v_mfma_f32_16x16x32_bf16 v[212:215], a[48:51], v[48:51], v[212:215]\n"
"v_cvt_pk_bf16_f32 v91, v113, v115\n"
"v_exp_f32 v116, v116\n"
"v_mfma_f32_16x16x32_bf16 v[232:235], a[252:255], v[68:71], v[232:235]\n"
"s_waitcnt lgkmcnt(11)\n"
"ds_read_b128 v[112:115], v178 offset:13504\n"
"v_exp_f32 v117, v117\n"
"v_mfma_f32_16x16x32_bf16 v[232:235], v[8:11], v[64:67], v[232:235]\n"
"s_waitcnt vmcnt(7) lgkmcnt(6)\n"
"v_exp_f32 v118, v118\n"
"v_mfma_f32_16x16x32_bf16 v[236:239], v[0:3], v[68:71], v[236:239]\n"
"v_add3_u32 v200, v200, s50, -8\n"
"v_cmp_lt_u32 vcc, 64, v200\n"
"v_mfma_f32_16x16x32_bf16 v[236:239], v[12:15], v[64:67], v[236:239]\n"
"ds_write_b64 v203, v[60:61] offset:55296\n"
"v_mfma_f32_16x16x32_bf16 v[240:243], v[4:7], v[68:71], v[240:243]\n"
"s_nop 1\n"
"s_cbranch_vccnz .Lp7%=\n"
".Lr7%=:\n"
"v_mfma_f32_16x16x32_bf16 v[240:243], v[16:19], v[64:67], v[240:243]\n"
"buffer_load_dwordx4 a[198:201], v192, s[28:31], s38 offen sc1\n"
"ds_read_b128 v[68:71], v192 offset:35840\n"
"v_mfma_f32_16x16x32_bf16 v[212:215], a[60:63], v[52:55], v[212:215]\n"
"v_exp_f32 v119, v119\n"
"v_mfma_f32_16x16x32_bf16 v[208:211], a[4:7], v[32:35], 0\n"
"v_cvt_pk_bf16_f32 v92, v116, v118\n"
"v_cvt_pk_bf16_f32 v93, v117, v119\n"
"v_mfma_f32_16x16x32_bf16 v[208:211], a[16:19], v[36:39], v[208:211]\n"
"s_waitcnt lgkmcnt(5)\n"
"v_cvt_pk_bf16_f32 v162, v204, v205\n"
"v_cvt_pk_bf16_f32 v163, v206, v207\n"
"v_smfmac_f32_16x16x64_bf16 v[252:255], v[90:93], v[216:223], v63\n"
"buffer_load_dwordx2 a[202:203], v193, s[28:31], s38 offen offset:1024 sc1\n"
"v_smfmac_f32_16x16x64_bf16 v[98:101], v[90:93], v[72:79], v63\n"
"s_add_u32 s38, s38, 1572864\n"
"v_cvt_pk_bf16_f32 v212, v212, v213\n"
"v_smfmac_f32_16x16x64_bf16 v[106:109], v[90:93], v[120:127], v63\n"
"ds_read_b128 v[120:123], v179 offset:13504\n"
"v_mfma_f32_16x16x32_bf16 v[90:93], v[172:175], v[90:93], v[82:85]\n"
"s_waitcnt lgkmcnt(1)\n"
"v_cvt_pk_bf16_f32 v252, v252, v253\n"
"v_cvt_pk_bf16_f32 v253, v254, v255\n"
"v_mfma_f32_16x16x32_bf16 v[232:235], v[20:23], v[68:71], v[232:235]\n"
"ds_read_b64 v[82:83], v180 offset:27072\n"
"buffer_load_dwordx4 a[204:207], v192, s[28:31], s38 offen sc1\n"
"v_mfma_f32_16x16x32_bf16 v[240:243], v[28:31], v[68:71], v[240:243]\n"
"ds_read_b64 v[84:85], v181 offset:27072\n"
"v_cvt_pk_bf16_f32 v106, v106, v107\n"
"v_mfma_f32_16x16x32_bf16 v[236:239], v[24:27], v[68:71], v[236:239]\n"
"v_rcp_f32 v171, v90\n"
"v_mfma_f32_16x16x32_bf16 v[208:211], a[28:31], v[40:43], v[208:211]\n"
"ds_read_b64 v[90:91], v178 offset:64\n"
"v_cvt_pk_bf16_f32 v171, v171, v171\n"
"v_mfma_f32_16x16x32_bf16 v[208:211], a[40:43], v[44:47], v[208:211]\n"
"v_and_b32 v92, v171, v176\n"
"v_and_b32 v93, v171, v177\n"
"v_mfma_f32_16x16x32_bf16 v[208:211], a[52:55], v[48:51], v[208:211]\n"
"buffer_load_dwordx2 a[208:209], v193, s[28:31], s38 offen offset:1024 sc1\n"
"v_cvt_pk_bf16_f32 v98, v98, v99\n"
"v_mfma_f32_16x16x16_bf16 v[252:255], v[252:253], v[92:93], 0\n"
"s_add_u32 s38, s38, 1572864\n"
"v_cvt_pk_bf16_f32 v107, v108, v109\n"
"v_cvt_pk_bf16_f32 v99, v100, v101\n"
"v_mfma_f32_16x16x32_bf16 v[208:211], a[64:67], v[52:55], v[208:211]\n"
"s_waitcnt lgkmcnt(2)\n"
"v_cvt_pk_bf16_f32 v232, v232, v233\n"
"v_cvt_pk_bf16_f32 v233, v234, v235\n"
"v_mfma_f32_16x16x32_bf16 v[136:139], v[136:139], v[152:155], 0\n"
"s_waitcnt lgkmcnt(1)\n"
"v_cvt_pk_bf16_f32 v240, v240, v241\n"
"v_cvt_pk_bf16_f32 v252, v252, v253\n"
"v_mfma_f32_16x16x32_bf16 v[136:139], v[80:83], v[164:167], v[136:139]\n"
"s_add_u32 s12, s38, 0\n"
"buffer_load_dwordx4 a[144:147], v192, s[28:31], s12 offen sc1\n"
"v_cvt_pk_bf16_f32 v234, v236, v237\n"
"v_mfma_f32_16x16x32_bf16 v[128:131], v[128:131], v[152:155], 0\n"
"s_waitcnt lgkmcnt(0)\n"
"v_cvt_pk_bf16_f32 v235, v238, v239\n"
"v_cvt_pk_bf16_f32 v241, v242, v243\n"
"v_mfma_f32_16x16x32_bf16 v[128:131], v[84:87], v[166:169], v[128:131]\n"
"s_add_u32 s13, s38, 1572864\n"
"ds_write_b128 v196, v[232:235] offset:36864\n"
"v_mfma_f32_16x16x32_bf16 v[140:143], v[140:143], v[152:155], 0\n"
"s_add_u32 s14, s38, 3145728\n"
"v_cvt_pk_bf16_f32 v253, v254, v255\n"
"v_cvt_pk_bf16_f32 v213, v214, v215\n"
"v_mfma_f32_16x16x32_bf16 v[140:143], v[88:91], v[164:167], v[140:143]\n"
"s_add_u32 s16, s38, 4718592\n"
"buffer_load_dwordx2 a[148:149], v193, s[28:31], s12 offen offset:1024 sc1\n"
"ds_write_b64 v197, v[240:241] offset:36864\n"
"v_mfma_f32_16x16x16_bf16 v[106:109], v[106:107], v[92:93], 0\n"
"s_add_u32 s15, s38, 6291456\n"
"v_cvt_pk_bf16_f32 v214, v208, v209\n"
"v_cvt_pk_bf16_f32 v215, v210, v211\n"
"v_mfma_f32_16x16x16_bf16 v[98:101], v[98:99], v[92:93], 0\n"
"s_add_u32 s17, s38, 7864320\n"
"ds_read_b64 v[92:93], v179 offset:64\n"
"v_max_f32 v136, v136, v128\n"
"v_mfma_f32_16x16x32_bf16 v[204:207], a[8:11], v[32:35], 0\n"
"s_add_u32 s18, s38, 9437184\n"
"v_cvt_pk_bf16_f32 v106, v106, v107\n"
"v_cvt_pk_bf16_f32 v107, v108, v109\n"
"v_mfma_f32_16x16x32_bf16 v[204:207], a[20:23], v[36:39], v[204:207]\n"
"buffer_store_dwordx2 v[106:107], v193, s[28:31], s37 offen offset:1024 sc1\n"
"v_mfma_f32_16x16x32_bf16 v[204:207], a[32:35], v[40:43], v[204:207]\n"
"v_cvt_pk_bf16_f32 v254, v98, v99\n"
"v_cvt_pk_bf16_f32 v255, v100, v101\n"
"v_mfma_f32_16x16x32_bf16 v[204:207], a[44:47], v[44:47], v[204:207]\n"
"s_waitcnt lgkmcnt(1)\n"
"ds_read_b64 v[98:99], v178 offset:13568\n"
"ds_read_b64 v[100:101], v179 offset:13568\n"
"v_mfma_f32_16x16x32_bf16 v[204:207], a[56:59], v[48:51], v[204:207]\n"
"s_waitcnt vmcnt(9)\n"
"v_max_f32 v137, v137, v129\n"
"v_max_f32 v138, v138, v130\n"
"v_mfma_f32_16x16x32_bf16 v[204:207], a[68:71], v[52:55], v[204:207]\n"
"s_barrier\n"
"v_mfma_f32_16x16x32_bf16 v[208:211], a[80:83], v[32:35], 0\n"
"s_waitcnt lgkmcnt(2)\n"
"v_or_b32 v201, 10, s6\n"
"v_max_f32 v139, v139, v131\n"
"v_mfma_f32_16x16x32_bf16 v[132:135], v[132:135], v[152:155], 0\n"
"ds_read_b128 v[64:67], v202 offset:36864\n"
"s_mov_b64 exec, 1\n"
"buffer_store_dword v201, off, s[28:31], s40 sc1\n"
"s_mov_b64 exec, -1\n"
"v_mfma_f32_16x16x32_bf16 v[132:135], v[92:95], v[166:169], v[132:135]\n"
"ds_read_b64 v[68:69], v62 offset:36864\n"
"ds_read_b128 v[116:119], v178 offset:27008\n"
"v_mfma_f32_16x16x32_bf16 v[208:211], a[92:95], v[36:39], v[208:211]\n"
"s_waitcnt lgkmcnt(4)\n"
"buffer_store_dwordx4 v[252:255], v192, s[28:31], s37 offen sc1\n"
"v_mfma_f32_16x16x32_bf16 v[112:115], v[112:115], v[152:155], 0\n"
"s_waitcnt lgkmcnt(3)\n"
"ds_read_b64 v[106:107], v178 offset:27072\n"
"ds_read_b128 v[124:127], v179 offset:27008\n"
"v_mfma_f32_16x16x32_bf16 v[112:115], v[96:99], v[164:167], v[112:115]\n"
"s_add_u32 s37, s37, 1572864\n"
"ds_read_b64 v[108:109], v179 offset:27072\n"
"v_max3_f32 v170, v136, v137, v138\n"
"v_mfma_f32_16x16x32_bf16 v[120:123], v[120:123], v[152:155], 0\n"
"s_waitcnt lgkmcnt(5)\n"
"ds_write_b128 v186, v[212:215] offset:47264\n"
"v_mfma_f32_16x16x32_bf16 v[120:123], v[100:103], v[166:169], v[120:123]\n"
"s_waitcnt lgkmcnt(5)\n"
"v_max_f32 v140, v140, v132\n"
"v_max_f32 v141, v141, v133\n"
"v_mfma_f32_16x16x32_bf16 v[208:211], a[104:107], v[40:43], v[208:211]\n"
"v_max_f32 v142, v142, v134\n"
"v_max_f32 v143, v143, v135\n"
"v_mfma_f32_16x16x32_bf16 v[208:211], a[116:119], v[44:47], v[208:211]\n"
"ds_read_b64_tr_b16 v[128:129], v184 offset:72\n"
"ds_read_b64_tr_b16 v[130:131], v185 offset:72\n"
"v_mfma_f32_16x16x32_bf16 v[208:211], a[128:131], v[48:51], v[208:211]\n"
"s_waitcnt lgkmcnt(5)\n"
"ds_read_b64_tr_b16 v[132:133], v184 offset:840\n"
"buffer_store_dwordx4 v[64:67], v194, s[24:27], s36 offen sc1\n"
"v_mfma_f32_16x16x32_bf16 v[116:119], v[116:119], v[152:155], 0\n"
"s_waitcnt lgkmcnt(4)\n"
"ds_read_b128 v[64:67], v192 offset:55296\n"
"ds_read_b64_tr_b16 v[134:135], v185 offset:840\n"
"v_mfma_f32_16x16x32_bf16 v[116:119], v[104:107], v[164:167], v[116:119]\n"
"ds_read_b64_tr_b16 a[168:169], v182 offset:13576\n"
"v_max3_f32 v170, v170, v139, v140\n"
"v_mfma_f32_16x16x32_bf16 v[124:127], v[124:127], v[152:155], 0\n"
"ds_read_b64_tr_b16 a[170:171], v183 offset:13576\n"
"v_max3_f32 v170, v170, v141, v142\n"
"v_mfma_f32_16x16x32_bf16 v[124:127], v[108:111], v[166:169], v[124:127]\n"
"ds_read_b64_tr_b16 a[172:173], v182 offset:14344\n"
"v_max_f32 v112, v112, v120\n"
"v_mfma_f32_16x16x32_bf16 v[208:211], a[140:143], v[52:55], v[208:211]\n"
"ds_read_b64_tr_b16 a[174:175], v183 offset:14344\n"
"v_max_f32 v113, v113, v121\n"
"v_mfma_f32_16x16x32_bf16 v[212:215], a[72:75], v[32:35], 0\n"
"v_max_f32 v114, v114, v122\n"
"v_max_f32 v115, v115, v123\n"
"v_mfma_f32_16x16x32_bf16 v[212:215], a[84:87], v[36:39], v[212:215]\n"
"ds_read_b128 v[120:123], v178\n"
"v_max3_f32 v170, v170, v143, v112\n"
"v_mfma_f32_16x16x32_bf16 v[212:215], a[96:99], v[40:43], v[212:215]\n"
"s_waitcnt lgkmcnt(6)\n"
"buffer_store_dwordx2 v[68:69], v195, s[24:27], s36 offen sc1\n"
"v_mfma_f32_16x16x32_bf16 v[212:215], a[108:111], v[44:47], v[212:215]\n"
"s_add_u32 s36, s36, s33\n"
"ds_read_b128 v[68:71], v192 offset:56320\n"
"v_max3_f32 v170, v170, v113, v114\n"
"v_mfma_f32_16x16x32_bf16 v[212:215], a[120:123], v[48:51], v[212:215]\n"
"v_max_f32 v116, v116, v124\n"
"v_max_f32 v117, v117, v125\n"
"v_mfma_f32_16x16x32_bf16 v[232:235], a[216:219], v[64:67], 0\n"
"v_cvt_pk_bf16_f32 v204, v204, v205\n"
"v_max_f32 v118, v118, v126\n"
"v_mfma_f32_16x16x32_bf16 v[236:239], a[220:223], v[64:67], 0\n"
"v_max_f32 v119, v119, v127\n"
"v_cvt_pk_bf16_f32 v205, v206, v207\n"
"v_mfma_f32_16x16x32_bf16 v[240:243], a[224:227], v[64:67], 0\n"
"ds_read_b128 v[64:67], v192 offset:57344\n"
"ds_read_b128 v[124:127], v178 offset:13504\n"
"v_mfma_f32_16x16x32_bf16 v[212:215], a[132:135], v[52:55], v[212:215]\n"
"v_max3_f32 v170, v170, v115, v116\n"
"v_max3_f32 v170, v170, v117, v118\n"
"v_mov_b64 v[252:253], 0\n"
"v_mov_b64 v[254:255], 0\n"
"v_mov_b64 v[98:99], 0\n"
"v_mov_b64 v[100:101], 0\n"
"v_mov_b64 v[106:107], 0\n"
"v_mov_b64 v[108:109], 0\n"
"v_max_f32 v171, v170, v119\n"
"v_max_f32 v170, v170, v119\n"
"v_cvt_pk_bf16_f32 v206, v208, v209\n"
"v_cvt_pk_bf16_f32 v207, v210, v211\n"
"v_mfma_f32_16x16x32_bf16 v[208:211], a[76:79], v[32:35], 0\n"
"s_waitcnt lgkmcnt(2)\n"
"ds_read_b128 v[32:35], v188\n"
"ds_write_b128 v186, v[204:207] offset:47328\n"
"v_mfma_f32_16x16x32_bf16 v[208:211], a[88:91], v[36:39], v[208:211]\n"
"v_permlane16_swap_b32 v170, v171\n"
"v_mfma_f32_16x16x32_bf16 v[208:211], a[100:103], v[40:43], v[208:211]\n"
"v_max_f32 v170, v170, v171\n"
"v_mov_b32 v171, v170\n"
"v_mfma_f32_16x16x32_bf16 v[208:211], a[112:115], v[44:47], v[208:211]\n"
"ds_read_b128 v[36:39], v189\n"
"ds_read_b128 v[40:43], v188 offset:1024\n"
"v_mfma_f32_16x16x32_bf16 v[232:235], a[228:231], v[68:71], v[232:235]\n"
"s_waitcnt lgkmcnt(5)\n"
"ds_read_b128 v[44:47], v189 offset:1024\n"
"v_permlane32_swap_b32 v170, v171\n"
"v_max_f32 v170, v170, v171\n"
"v_mul_f32 v170, 0xbe553b95, v170\n"
"v_pk_fma_f32 v[136:137], v[136:137], s[70:71], v[170:171] op_sel_hi:[1,0,0]\n"
"v_pk_fma_f32 v[138:139], v[138:139], s[70:71], v[170:171] op_sel_hi:[1,0,0]\n"
"v_pk_fma_f32 v[140:141], v[140:141], s[70:71], v[170:171] op_sel_hi:[1,0,0]\n"
"v_pk_fma_f32 v[142:143], v[142:143], s[70:71], v[170:171] op_sel_hi:[1,0,0]\n"
"v_pk_fma_f32 v[112:113], v[112:113], s[70:71], v[170:171] op_sel_hi:[1,0,0]\n"
".Lic6%=:\n"
"s_cbranch_execz .Lib1%=\n"
"v_pk_fma_f32 v[114:115], v[114:115], s[70:71], v[170:171] op_sel_hi:[1,0,0]\n"
"v_pk_fma_f32 v[116:117], v[116:117], s[70:71], v[170:171] op_sel_hi:[1,0,0]\n"
"v_pk_fma_f32 v[118:119], v[118:119], s[70:71], v[170:171] op_sel_hi:[1,0,0]\n"
"v_mfma_f32_16x16x32_bf16 v[232:235], a[240:243], v[64:67], v[232:235]\n"
"s_waitcnt lgkmcnt(4)\n"
"v_exp_f32 v136, v136\n"
"v_mfma_f32_16x16x32_bf16 v[236:239], a[232:235], v[68:71], v[236:239]\n"
"v_exp_f32 v137, v137\n"
"v_mfma_f32_16x16x32_bf16 v[236:239], a[244:247], v[64:67], v[236:239]\n"
"v_exp_f32 v138, v138\n"
"v_mfma_f32_16x16x32_bf16 v[240:243], a[236:239], v[68:71], v[240:243]\n"
"ds_read_b128 v[68:71], v192 offset:58368\n"
"v_cvt_pk_bf16_f32 v82, v136, v138\n"
"v_mfma_f32_16x16x32_bf16 v[240:243], a[248:251], v[64:67], v[240:243]\n"
"ds_read_b128 v[64:67], v192 offset:59392\n"
"v_exp_f32 v139, v139\n"
"v_mfma_f32_16x16x32_bf16 v[208:211], a[124:127], v[48:51], v[208:211]\n"
"ds_read_b128 v[48:51], v188 offset:2048\n"
"v_cvt_pk_bf16_f32 v83, v137, v139\n"
"v_mfma_f32_16x16x32_bf16 v[208:211], a[136:139], v[52:55], v[208:211]\n"
"ds_read_b128 v[52:55], v189 offset:2048\n"
"ds_read_b128 v[136:139], v179\n"
"v_mfma_f32_16x16x32_bf16 v[204:207], a[0:3], v[32:35], 0\n"
"s_waitcnt lgkmcnt(7)\n"
"v_exp_f32 v140, v140\n"
"v_mfma_f32_16x16x32_bf16 v[204:207], a[12:15], v[36:39], v[204:207]\n"
"s_waitcnt lgkmcnt(6)\n"
"v_exp_f32 v141, v141\n"
"v_mfma_f32_16x16x32_bf16 v[204:207], a[24:27], v[40:43], v[204:207]\n"
"s_waitcnt vmcnt(10) lgkmcnt(5)\n"
"v_exp_f32 v142, v142\n"
"v_mfma_f32_16x16x32_bf16 v[204:207], a[36:39], v[44:47], v[204:207]\n"
"s_waitcnt lgkmcnt(4)\n"
"ds_write_b128 v202, a[198:201] offset:30720\n"
"v_mfma_f32_16x16x32_bf16 v[232:235], a[252:255], v[68:71], v[232:235]\n"
"s_waitcnt lgkmcnt(3)\n"
"v_exp_f32 v143, v143\n"
"v_mfma_f32_16x16x32_bf16 v[232:235], v[8:11], v[64:67], v[232:235]\n"
"v_cvt_pk_bf16_f32 v84, v140, v142\n"
"v_cvt_pk_bf16_f32 v85, v141, v143\n"
"v_mfma_f32_16x16x32_bf16 v[236:239], v[0:3], v[68:71], v[236:239]\n"
"s_waitcnt vmcnt(9)\n"
"ds_read_b128 v[140:143], v179 offset:13504\n"
"ds_write_b64 v203, a[202:203] offset:30720\n"
"v_mfma_f32_16x16x32_bf16 v[236:239], v[12:15], v[64:67], v[236:239]\n"
"v_exp_f32 v112, v112\n"
"v_smfmac_f32_16x16x64_bf16 v[252:255], v[82:85], v[224:231], v63\n"
"ds_read_b64_tr_b16 v[224:225], v182 offset:13632\n"
"v_smfmac_f32_16x16x64_bf16 v[98:101], v[82:85], v[244:251], v63\n"
"ds_read_b64_tr_b16 v[226:227], v183 offset:13632\n"
"v_smfmac_f32_16x16x64_bf16 v[106:109], v[82:85], v[128:135], v63\n"
"ds_read_b64_tr_b16 v[228:229], v182 offset:14400\n"
"v_mfma_f32_16x16x32_bf16 v[82:85], v[172:175], v[82:85], 0\n"
"ds_read_b64_tr_b16 v[230:231], v183 offset:14400\n"
"ds_read_b64_tr_b16 v[244:245], v182 offset:13640\n"
"v_mfma_f32_16x16x32_bf16 v[240:243], v[4:7], v[68:71], v[240:243]\n"
"v_cvt_pk_bf16_f32 v212, v212, v213\n"
"v_cvt_pk_bf16_f32 v213, v214, v215\n"
"v_mfma_f32_16x16x32_bf16 v[240:243], v[16:19], v[64:67], v[240:243]\n"
"ds_read_b128 v[68:71], v192 offset:60416\n"
"ds_read_b64_tr_b16 v[246:247], v183 offset:13640\n"
"v_mfma_f32_16x16x32_bf16 v[204:207], a[48:51], v[48:51], v[204:207]\n"
"s_waitcnt lgkmcnt(11)\n"
"v_exp_f32 v113, v113\n"
"v_mfma_f32_16x16x32_bf16 v[204:207], a[60:63], v[52:55], v[204:207]\n"
"ds_read_b64_tr_b16 v[248:249], v182 offset:14408\n"
"ds_read_b64_tr_b16 v[250:251], v183 offset:14408\n"
"v_exp_f32 v114, v114\n"
"v_exp_f32 v115, v115\n"
"v_cvt_pk_bf16_f32 v90, v112, v114\n"
"v_cvt_pk_bf16_f32 v91, v113, v115\n"
"v_exp_f32 v116, v116\n"
"v_exp_f32 v117, v117\n"
"v_exp_f32 v118, v118\n"
"v_exp_f32 v119, v119\n"
"v_cvt_pk_bf16_f32 v92, v116, v118\n"
"v_cvt_pk_bf16_f32 v93, v117, v119\n"
"v_cvt_pk_bf16_f32 v214, v208, v209\n"
"v_cvt_pk_bf16_f32 v215, v210, v211\n"
"v_smfmac_f32_16x16x64_bf16 v[106:109], v[90:93], a[168:175], v63\n"
"s_waitcnt lgkmcnt(3)\n"
"ds_read_b128 v[128:131], v178 offset:27008\n"
"v_smfmac_f32_16x16x64_bf16 v[252:255], v[90:93], v[224:231], v63\n"
"ds_read_b128 v[112:115], v179 offset:27008\n"
"v_mfma_f32_16x16x32_bf16 v[232:235], v[20:23], v[68:71], v[232:235]\n"
"ds_write_b128 v186, v[212:215] offset:47392\n"
"v_mfma_f32_16x16x32_bf16 v[240:243], v[28:31], v[68:71], v[240:243]\n"
"s_waitcnt lgkmcnt(3)\n"
"v_cvt_pk_bf16_f32 v106, v106, v107\n"
"v_cvt_pk_bf16_f32 v107, v108, v109\n"
"v_smfmac_f32_16x16x64_bf16 v[98:101], v[90:93], v[244:251], v63\n"
"v_cvt_pk_bf16_f32 v252, v252, v253\n"
"v_mfma_f32_16x16x32_bf16 v[90:93], v[172:175], v[90:93], v[82:85]\n"
"v_cvt_pk_bf16_f32 v232, v232, v233\n"
"v_cvt_pk_bf16_f32 v233, v234, v235\n"
"v_mfma_f32_16x16x32_bf16 v[236:239], v[24:27], v[68:71], v[236:239]\n"
"ds_read_b64 v[82:83], v178 offset:64\n"
"ds_read_b64 v[84:85], v179 offset:64\n"
"v_mfma_f32_16x16x32_bf16 v[208:211], a[4:7], v[32:35], 0\n"
"v_cvt_pk_bf16_f32 v240, v240, v241\n"
"v_cvt_pk_bf16_f32 v241, v242, v243\n"
"v_mfma_f32_16x16x32_bf16 v[208:211], a[16:19], v[36:39], v[208:211]\n"
"ds_write_b64 v197, v[240:241] offset:43008\n"
"v_mfma_f32_16x16x32_bf16 v[208:211], a[28:31], v[40:43], v[208:211]\n"
"s_waitcnt vmcnt(2)\n"
"v_or_b32 v201, 11, s6\n"
"v_cvt_pk_bf16_f32 v234, v236, v237\n"
"v_mfma_f32_16x16x32_bf16 v[208:211], a[40:43], v[44:47], v[208:211]\n"
"s_mov_b64 exec, 1\n"
"buffer_store_dword v201, off, s[28:31], s40 sc1\n"
"s_mov_b64 exec, -1\n"
"v_mfma_f32_16x16x32_bf16 v[208:211], a[52:55], v[48:51], v[208:211]\n"
"v_rcp_f32 v171, v90\n"
"v_mfma_f32_16x16x32_bf16 v[208:211], a[64:67], v[52:55], v[208:211]\n"
"s_waitcnt lgkmcnt(2)\n"
"ds_read_b64 v[90:91], v178 offset:13568\n"
"v_cvt_pk_bf16_f32 v171, v171, v171\n"
"v_mfma_f32_16x16x32_bf16 v[120:123], v[120:123], v[144:147], 0\n"
"s_waitcnt lgkmcnt(2)\n"
"buffer_load_dword v200, off, s[28:31], s39 sc1\n"
"v_and_b32 v92, v171, v176\n"
"v_mfma_f32_16x16x32_bf16 v[120:123], v[80:83], v[156:159], v[120:123]\n"
"v_cvt_pk_bf16_f32 v235, v238, v239\n"
"v_and_b32 v93, v171, v177\n"
"v_mfma_f32_16x16x32_bf16 v[136:139], v[136:139], v[144:147], 0\n"
"ds_write_b128 v196, v[232:235] offset:43008\n"
"v_mfma_f32_16x16x32_bf16 v[136:139], v[84:87], v[158:161], v[136:139]\n"
"v_cvt_pk_bf16_f32 v253, v254, v255\n"
"v_cvt_pk_bf16_f32 v98, v98, v99\n"
"v_mfma_f32_16x16x16_bf16 v[106:109], v[106:107], v[92:93], 0\n"
"v_cvt_pk_bf16_f32 v99, v100, v101\n"
"v_cvt_pk_bf16_f32 v204, v204, v205\n"
"v_mfma_f32_16x16x16_bf16 v[252:255], v[252:253], v[92:93], 0\n"
"s_waitcnt lgkmcnt(1)\n"
"v_cvt_pk_bf16_f32 v205, v206, v207\n"
"s_mov_b64 exec, s[44:45]\n"
"v_add_u32 v181, 0xc0, v181\n"
"s_mov_b64 exec, -1\n"
"v_mfma_f32_16x16x32_bf16 v[124:127], v[124:127], v[144:147], 0\n"
"v_cvt_pk_bf16_f32 v106, v106, v107\n"
"v_cvt_pk_bf16_f32 v107, v108, v109\n"
"v_mfma_f32_16x16x32_bf16 v[124:127], v[88:91], v[156:159], v[124:127]\n"
"v_cvt_pk_bf16_f32 v252, v252, v253\n"
"v_cvt_pk_bf16_f32 v253, v254, v255\n"
"v_mfma_f32_16x16x16_bf16 v[98:101], v[98:99], v[92:93], 0\n"
"ds_read_b64 v[92:93], v179 offset:13568\n"
"v_min_u32 v252, -2, v252\n"
"v_mfma_f32_16x16x32_bf16 v[212:215], a[8:11], v[32:35], 0\n"
"s_waitcnt lgkmcnt(1)\n"
"v_min_u32 v253, -2, v253\n"
"v_min_u32 v106, -2, v106\n"
"v_mfma_f32_16x16x32_bf16 v[212:215], a[20:23], v[36:39], v[212:215]\n"
"s_barrier\n"
"v_mfma_f32_16x16x32_bf16 v[212:215], a[32:35], v[40:43], v[212:215]\n"
"ds_read_b128 v[64:67], v202 offset:43008\n"
"ds_read_b64 v[68:69], v62 offset:43008\n"
"v_mfma_f32_16x16x32_bf16 v[212:215], a[44:47], v[44:47], v[212:215]\n"
"ds_write_b128 v202, a[144:147]\n"
"v_mfma_f32_16x16x32_bf16 v[212:215], a[56:59], v[48:51], v[212:215]\n"
"v_cvt_pk_bf16_f32 v254, v98, v99\n"
"v_cvt_pk_bf16_f32 v255, v100, v101\n"
"v_mfma_f32_16x16x32_bf16 v[212:215], a[68:71], v[52:55], v[212:215]\n"
"s_waitcnt lgkmcnt(3)\n"
"v_min_u32 v254, -2, v254\n"
"v_min_u32 v255, -2, v255\n"
"v_mfma_f32_16x16x32_bf16 v[140:143], v[140:143], v[144:147], 0\n"
"buffer_store_dwordx4 v[252:255], v192, s[28:31], s37 offen sc1\n"
"v_mfma_f32_16x16x32_bf16 v[140:143], v[92:95], v[158:161], v[140:143]\n"
"s_waitcnt lgkmcnt(2)\n"
"ds_write_b64 v203, a[148:149]\n"
"v_min_u32 v107, -2, v107\n"
"v_cvt_pk_bf16_f32 v206, v208, v209\n"
"v_cvt_pk_bf16_f32 v207, v210, v211\n"
"v_mfma_f32_16x16x32_bf16 v[208:211], a[80:83], v[32:35], 0\n"
"s_waitcnt lgkmcnt(2)\n"
"ds_read_b64 v[98:99], v178 offset:27072\n"
"ds_read_b64 v[100:101], v179 offset:27072\n"
"v_mfma_f32_16x16x32_bf16 v[208:211], a[92:95], v[36:39], v[208:211]\n"
"ds_read_b128 v[132:135], v180\n"
"v_max_f32 v120, v120, v136\n"
"v_mfma_f32_16x16x32_bf16 v[208:211], a[104:107], v[40:43], v[208:211]\n"
"ds_read_b128 v[116:119], v181\n"
"v_max_f32 v121, v121, v137\n"
"v_mfma_f32_16x16x32_bf16 v[208:211], a[116:119], v[44:47], v[208:211]\n"
"ds_read_b64 v[108:109], v181 offset:64\n"
"v_max_f32 v122, v122, v138\n"
"v_mfma_f32_16x16x32_bf16 v[208:211], a[128:131], v[48:51], v[208:211]\n"
"ds_write_b128 v202, a[204:207] offset:55296\n"
"v_mfma_f32_16x16x32_bf16 v[208:211], a[140:143], v[52:55], v[208:211]\n"
"buffer_store_dwordx2 v[106:107], v193, s[28:31], s37 offen offset:1024 sc1\n"
"v_max_f32 v123, v123, v139\n"
"v_max3_f32 v170, v120, v121, v122\n"
"v_max_f32 v124, v124, v140\n"
"v_max_f32 v125, v125, v141\n"
"v_max_f32 v126, v126, v142\n"
"v_max_f32 v127, v127, v143\n"
"v_max3_f32 v170, v170, v123, v124\n"
"v_max3_f32 v170, v170, v125, v126\n"
"v_cvt_pk_bf16_f32 v212, v212, v213\n"
"v_cvt_pk_bf16_f32 v213, v214, v215\n"
"v_mov_b64 v[252:253], 0\n"
"v_mov_b64 v[254:255], 0\n"
"v_cvt_pk_bf16_f32 v214, v208, v209\n"
"v_cvt_pk_bf16_f32 v215, v210, v211\n"
"v_mfma_f32_16x16x32_bf16 v[208:211], a[76:79], v[32:35], 0\n"
"s_waitcnt lgkmcnt(1)\n"
"ds_read_b64 v[106:107], v180 offset:64\n"
"ds_write_b128 v198, v[204:207]\n"
"v_mfma_f32_16x16x32_bf16 v[128:131], v[128:131], v[144:147], 0\n"
"s_add_u32 s37, s37, 1572864\n"
"buffer_store_dwordx4 v[64:67], v194, s[24:27], s36 offen sc1\n"
"v_mfma_f32_16x16x32_bf16 v[128:131], v[96:99], v[156:159], v[128:131]\n"
"s_nop 0\n"
"ds_read_b128 v[64:67], v192 offset:30720\n"
"ds_read_b64_tr_b16 v[136:137], v182 offset:72\n"
"v_mfma_f32_16x16x32_bf16 v[112:115], v[112:115], v[144:147], 0\n"
"ds_read_b64_tr_b16 v[138:139], v183 offset:72\n"
"v_mov_b64 v[98:99], 0\n"
"v_mfma_f32_16x16x32_bf16 v[112:115], v[100:103], v[158:161], v[112:115]\n"
"ds_read_b64_tr_b16 v[140:141], v182 offset:840\n"
"v_mov_b64 v[100:101], 0\n"
"v_mfma_f32_16x16x32_bf16 v[116:119], v[116:119], v[144:147], 0\n"
"s_waitcnt lgkmcnt(5)\n"
"ds_read_b64_tr_b16 v[142:143], v183 offset:840\n"
"v_mfma_f32_16x16x32_bf16 v[116:119], v[108:111], v[158:161], v[116:119]\n"
"ds_write_b64 v203, a[208:209] offset:55296\n"
"v_mfma_f32_16x16x32_bf16 v[132:135], v[132:135], v[144:147], 0\n"
"v_max_f32 v128, v128, v112\n"
"v_max_f32 v129, v129, v113\n"
"v_mfma_f32_16x16x32_bf16 v[132:135], v[104:107], v[156:159], v[132:135]\n"
"ds_read_b64_tr_b16 v[112:113], v182 offset:27080\n"
"ds_write_b128 v198, v[212:215] offset:64\n"
"v_mfma_f32_16x16x32_bf16 v[204:207], a[72:75], v[32:35], 0\n"
"s_waitcnt lgkmcnt(7)\n"
"buffer_store_dwordx2 v[68:69], v195, s[24:27], s36 offen sc1\n"
"v_mfma_f32_16x16x32_bf16 v[204:207], a[84:87], v[36:39], v[204:207]\n"
"s_add_u32 s36, s36, s33\n"
"ds_read_b128 v[68:71], v192 offset:31744\n"
"v_max_f32 v130, v130, v114\n"
"v_mfma_f32_16x16x32_bf16 v[204:207], a[96:99], v[40:43], v[204:207]\n"
"v_max_f32 v131, v131, v115\n"
"v_max3_f32 v170, v170, v127, v128\n"
"v_mfma_f32_16x16x32_bf16 v[204:207], a[108:111], v[44:47], v[204:207]\n"
"ds_read_b64_tr_b16 v[114:115], v183 offset:27080\n"
"v_max3_f32 v170, v170, v129, v130\n"
"v_mfma_f32_16x16x32_bf16 v[204:207], a[120:123], v[48:51], v[204:207]\n"
"v_max_f32 v132, v132, v116\n"
"v_max_f32 v133, v133, v117\n"
"v_mfma_f32_16x16x32_bf16 v[232:235], a[216:219], v[64:67], 0\n"
"ds_read_b64_tr_b16 v[116:117], v182 offset:27848\n"
"v_max_f32 v134, v134, v118\n"
"v_mfma_f32_16x16x32_bf16 v[236:239], a[220:223], v[64:67], 0\n"
"v_max_f32 v135, v135, v119\n"
"v_max3_f32 v170, v170, v131, v132\n"
"v_mfma_f32_16x16x32_bf16 v[240:243], a[224:227], v[64:67], 0\n"
"ds_read_b128 v[64:67], v192 offset:32768\n"
"ds_read_b64_tr_b16 v[118:119], v183 offset:27848\n"
"v_mfma_f32_16x16x32_bf16 v[204:207], a[132:135], v[52:55], v[204:207]\n"
"s_waitcnt lgkmcnt(4)\n"
"v_max3_f32 v170, v170, v133, v134\n"
"v_mov_b64 v[106:107], 0\n"
"v_mfma_f32_16x16x32_bf16 v[232:235], a[228:231], v[68:71], v[232:235]\n"
"v_mov_b64 v[108:109], 0\n"
"v_max_f32 v171, v170, v135\n"
"v_mfma_f32_16x16x32_bf16 v[236:239], a[232:235], v[68:71], v[236:239]\n"
"v_max_f32 v170, v170, v135\n"
"v_cvt_pk_bf16_f32 v204, v204, v205\n"
"v_mfma_f32_16x16x32_bf16 v[240:243], a[236:239], v[68:71], v[240:243]\n"
"ds_read_b128 v[68:71], v192 offset:33792\n"
"v_permlane16_swap_b32 v170, v171\n"
"v_mfma_f32_16x16x32_bf16 v[208:211], a[88:91], v[36:39], v[208:211]\n"
"v_max_f32 v170, v170, v171\n"
"v_mov_b32 v171, v170\n"
"v_mfma_f32_16x16x32_bf16 v[208:211], a[100:103], v[40:43], v[208:211]\n"
"s_nop 0\n"
"v_permlane32_swap_b32 v170, v171\n"
"v_max_f32 v170, v170, v171\n"
"v_mul_f32 v170, 0xbe553b95, v170\n"
"v_pk_fma_f32 v[120:121], v[120:121], s[70:71], v[170:171] op_sel_hi:[1,0,0]\n"
"v_pk_fma_f32 v[122:123], v[122:123], s[70:71], v[170:171] op_sel_hi:[1,0,0]\n"
"v_pk_fma_f32 v[124:125], v[124:125], s[70:71], v[170:171] op_sel_hi:[1,0,0]\n"
"v_pk_fma_f32 v[126:127], v[126:127], s[70:71], v[170:171] op_sel_hi:[1,0,0]\n"
"v_pk_fma_f32 v[128:129], v[128:129], s[70:71], v[170:171] op_sel_hi:[1,0,0]\n"
"v_pk_fma_f32 v[130:131], v[130:131], s[70:71], v[170:171] op_sel_hi:[1,0,0]\n"
"v_pk_fma_f32 v[132:133], v[132:133], s[70:71], v[170:171] op_sel_hi:[1,0,0]\n"
"v_pk_fma_f32 v[134:135], v[134:135], s[70:71], v[170:171] op_sel_hi:[1,0,0]\n"
"v_mfma_f32_16x16x32_bf16 v[208:211], a[112:115], v[44:47], v[208:211]\n"
"s_waitcnt lgkmcnt(2)\n"
"v_exp_f32 v120, v120\n"
"v_mfma_f32_16x16x32_bf16 v[208:211], a[124:127], v[48:51], v[208:211]\n"
"v_exp_f32 v121, v121\n"
"v_mfma_f32_16x16x32_bf16 v[208:211], a[136:139], v[52:55], v[208:211]\n"
"v_exp_f32 v122, v122\n"
"v_mfma_f32_16x16x32_bf16 v[232:235], a[240:243], v[64:67], v[232:235]\n"
"s_waitcnt lgkmcnt(0)\n"
"v_exp_f32 v123, v123\n"
"v_mfma_f32_16x16x32_bf16 v[232:235], a[252:255], v[68:71], v[232:235]\n"
"v_cvt_pk_bf16_f32 v82, v120, v122\n"
"v_cvt_pk_bf16_f32 v83, v121, v123\n"
"v_mfma_f32_16x16x32_bf16 v[236:239], a[244:247], v[64:67], v[236:239]\n"
"ds_read_b128 v[120:123], v178 offset:13504\n"
"v_exp_f32 v124, v124\n"
"v_mfma_f32_16x16x32_bf16 v[236:239], v[0:3], v[68:71], v[236:239]\n"
"v_exp_f32 v125, v125\n"
"v_mfma_f32_16x16x32_bf16 v[240:243], a[248:251], v[64:67], v[240:243]\n"
"ds_read_b128 v[64:67], v192 offset:34816\n"
"v_exp_f32 v126, v126\n"
"v_mfma_f32_16x16x32_bf16 v[240:243], v[4:7], v[68:71], v[240:243]\n"
"ds_read_b128 v[68:71], v192 offset:35840\n"
"v_exp_f32 v127, v127\n"
"v_cvt_pk_bf16_f32 v84, v124, v126\n"
"v_cvt_pk_bf16_f32 v85, v125, v127\n"
"v_exp_f32 v128, v128\n"
"s_nop 0\n"
"v_smfmac_f32_16x16x64_bf16 v[252:255], v[82:85], v[216:223], v63\n"
"s_waitcnt vmcnt(4)\n"
"ds_read_b64_tr_b16 v[216:217], v182 offset:27136\n"
"v_smfmac_f32_16x16x64_bf16 v[98:101], v[82:85], v[72:79], v63\n"
"v_add3_u32 v200, v200, s50, -10\n"
"v_smfmac_f32_16x16x64_bf16 v[106:109], v[82:85], v[136:143], v63\n"
"v_cmp_lt_u32 vcc, 64, v200\n"
"v_mfma_f32_16x16x32_bf16 v[82:85], v[172:175], v[82:85], 0\n"
"s_waitcnt lgkmcnt(2)\n"
"ds_read_b64_tr_b16 v[218:219], v183 offset:27136\n"
"ds_read_b64_tr_b16 v[220:221], v182 offset:27904\n"
"v_cvt_pk_bf16_f32 v205, v206, v207\n"
"v_mfma_f32_16x16x32_bf16 v[232:235], v[8:11], v[64:67], v[232:235]\n"
"s_waitcnt lgkmcnt(3)\n"
"ds_read_b64_tr_b16 v[222:223], v183 offset:27904\n"
"v_exp_f32 v129, v129\n"
"v_mfma_f32_16x16x32_bf16 v[232:235], v[20:23], v[68:71], v[232:235]\n"
"s_cbranch_vccnz .Lp9%=\n"
".Lr9%=:\n"
"v_mfma_f32_16x16x32_bf16 v[236:239], v[12:15], v[64:67], v[236:239]\n"
"buffer_load_dwordx4 a[150:153], v192, s[28:31], s13 offen sc1\n"
"ds_read_b64_tr_b16 v[72:73], v182 offset:27144\n"
"v_mfma_f32_16x16x32_bf16 v[236:239], v[24:27], v[68:71], v[236:239]\n"
"ds_read_b64_tr_b16 v[74:75], v183 offset:27144\n"
"ds_read_b64_tr_b16 v[76:77], v182 offset:27912\n"
"v_mfma_f32_16x16x32_bf16 v[240:243], v[16:19], v[64:67], v[240:243]\n"
"v_exp_f32 v130, v130\n"
"v_mfma_f32_16x16x32_bf16 v[240:243], v[28:31], v[68:71], v[240:243]\n"
"ds_read_b64_tr_b16 v[78:79], v183 offset:27912\n"
"ds_read_b128 v[136:139], v179 offset:13504\n"
"v_exp_f32 v131, v131\n"
"buffer_load_dword v201, off, s[28:31], s39 sc1\n"
"v_cvt_pk_bf16_f32 v90, v128, v130\n"
"v_cvt_pk_bf16_f32 v91, v129, v131\n"
"v_exp_f32 v132, v132\n"
"v_exp_f32 v133, v133\n"
"buffer_load_dwordx2 a[154:155], v193, s[28:31], s13 offen offset:1024 sc1\n"
"ds_read_b128 v[124:127], v178 offset:27008\n"
"v_exp_f32 v134, v134\n"
"v_exp_f32 v135, v135\n"
"v_cvt_pk_bf16_f32 v92, v132, v134\n"
"v_cvt_pk_bf16_f32 v93, v133, v135\n"
"v_cvt_pk_bf16_f32 v206, v208, v209\n"
"v_cvt_pk_bf16_f32 v207, v210, v211\n"
"v_smfmac_f32_16x16x64_bf16 v[106:109], v[90:93], v[112:119], v63\n"
"s_waitcnt lgkmcnt(2)\n"
"ds_read_b128 v[140:143], v179 offset:27008\n"
"v_smfmac_f32_16x16x64_bf16 v[252:255], v[90:93], v[216:223], v63\n"
"buffer_load_dwordx4 a[156:159], v192, s[28:31], s14 offen sc1\n"
"v_smfmac_f32_16x16x64_bf16 v[98:101], v[90:93], v[72:79], v63\n"
"ds_read_b128 v[128:131], v180\n"
"v_mfma_f32_16x16x32_bf16 v[90:93], v[172:175], v[90:93], v[82:85]\n"
"ds_read_b128 v[112:115], v181\n"
"ds_write_b128 v198, v[204:207] offset:128\n"
"s_nop 1\n"
"v_cvt_pk_bf16_f32 v252, v252, v253\n"
"buffer_load_dwordx2 a[160:161], v193, s[28:31], s14 offen offset:1024 sc1\n"
"v_cvt_pk_bf16_f32 v253, v254, v255\n"
"ds_read_b64 v[82:83], v178 offset:13568\n"
"ds_read_b64 v[84:85], v179 offset:13568\n"
"v_rcp_f32 v171, v90\n"
"ds_read_b64 v[90:91], v178 offset:27072\n"
"v_cvt_pk_bf16_f32 v171, v171, v171\n"
"v_and_b32 v92, v171, v176\n"
"v_and_b32 v93, v171, v177\n"
"v_cvt_pk_bf16_f32 v98, v98, v99\n"
"v_cvt_pk_bf16_f32 v99, v100, v101\n"
"v_mfma_f32_16x16x16_bf16 v[252:255], v[252:253], v[92:93], 0\n"
"v_cvt_pk_bf16_f32 v106, v106, v107\n"
"v_cvt_pk_bf16_f32 v107, v108, v109\n"
"v_mfma_f32_16x16x16_bf16 v[98:101], v[98:99], v[92:93], 0\n"
"v_cvt_pk_bf16_f32 v232, v232, v233\n"
"v_cvt_pk_bf16_f32 v233, v234, v235\n"
"v_mfma_f32_16x16x16_bf16 v[106:109], v[106:107], v[92:93], 0\n"
"s_waitcnt lgkmcnt(3)\n"
"ds_read_b64 v[92:93], v179 offset:27072\n"
"v_cvt_pk_bf16_f32 v252, v252, v253\n"
"v_cvt_pk_bf16_f32 v253, v254, v255\n"
"v_min_u32 v252, -2, v252\n"
"v_cvt_pk_bf16_f32 v254, v98, v99\n"
"v_cvt_pk_bf16_f32 v255, v100, v101\n"
"v_min_u32 v253, -2, v253\n"
"v_min_u32 v254, -2, v254\n"
"v_cvt_pk_bf16_f32 v106, v106, v107\n"
"v_cvt_pk_bf16_f32 v107, v108, v109\n"
"v_min_u32 v255, -2, v255\n"
"v_min_u32 v106, -2, v106\n"
"v_min_u32 v107, -2, v107\n"
"v_cvt_pk_bf16_f32 v240, v240, v241\n"
"v_cvt_pk_bf16_f32 v234, v236, v237\n"
"v_cvt_pk_bf16_f32 v235, v238, v239\n"
"v_cvt_pk_bf16_f32 v241, v242, v243\n"
"s_waitcnt lgkmcnt(1)\n"
"v_mfma_f32_16x16x32_bf16 v[120:123], v[120:123], v[148:151], 0\n"
"s_barrier\n"
"v_mfma_f32_16x16x32_bf16 v[120:123], v[80:83], v[160:163], v[120:123]\n"
"buffer_store_dwordx4 v[252:255], v192, s[28:31], s37 offen sc1\n"
"v_mfma_f32_16x16x32_bf16 v[136:139], v[136:139], v[148:151], 0\n"
"s_waitcnt lgkmcnt(0)\n"
"ds_read_b128 v[64:67], v192 offset:55296\n"
"ds_read_b128 v[68:71], v192 offset:56320\n"
"v_mfma_f32_16x16x32_bf16 v[136:139], v[84:87], v[162:165], v[136:139]\n"
"ds_read_b64 v[98:99], v180 offset:64\n"
"v_mov_b64 v[252:253], 0\n"
"v_mfma_f32_16x16x32_bf16 v[124:127], v[124:127], v[148:151], 0\n"
"ds_read_b64 v[100:101], v181 offset:64\n"
"v_mov_b64 v[254:255], 0\n"
"v_mfma_f32_16x16x32_bf16 v[124:127], v[88:91], v[160:163], v[124:127]\n"
"ds_read_b128 v[132:135], v180 offset:13504\n"
"v_mfma_f32_16x16x32_bf16 v[140:143], v[140:143], v[148:151], 0\n"
"ds_read_b128 v[116:119], v181 offset:13504\n"
"v_max_f32 v120, v120, v136\n"
"v_mfma_f32_16x16x32_bf16 v[140:143], v[92:95], v[162:165], v[140:143]\n"
"ds_read_b64 v[108:109], v181 offset:13568\n"
"v_max_f32 v121, v121, v137\n"
"v_max_f32 v122, v122, v138\n"
"v_max_f32 v123, v123, v139\n"
"v_max3_f32 v170, v120, v121, v122\n"
"ds_write_b128 v196, v[232:235] offset:49152\n"
"buffer_store_dwordx2 v[106:107], v193, s[28:31], s37 offen offset:1024 sc1\n"
"s_waitcnt lgkmcnt(7)\n"
"v_mfma_f32_16x16x32_bf16 v[232:235], a[216:219], v[64:67], 0\n"
"s_waitcnt lgkmcnt(4)\n"
"v_max_f32 v124, v124, v140\n"
"v_max_f32 v125, v125, v141\n"
"v_mfma_f32_16x16x32_bf16 v[232:235], a[228:231], v[68:71], v[232:235]\n"
"s_add_u32 s37, s37, 1572864\n"
"ds_read_b64 v[106:107], v180 offset:13568\n"
"ds_write_b64 v197, v[240:241] offset:49152\n"
"v_mfma_f32_16x16x32_bf16 v[128:131], v[128:131], v[148:151], 0\n"
"v_max_f32 v126, v126, v142\n"
"v_max3_f32 v170, v170, v123, v124\n"
"v_mfma_f32_16x16x32_bf16 v[128:131], v[96:99], v[160:163], v[128:131]\n"
"s_waitcnt lgkmcnt(3)\n"
"ds_read_b64_tr_b16 v[136:137], v182 offset:40584\n"
"ds_read_b64_tr_b16 v[138:139], v183 offset:40584\n"
"v_mfma_f32_16x16x32_bf16 v[112:115], v[112:115], v[148:151], 0\n"
"ds_read_b64_tr_b16 v[140:141], v182 offset:41352\n"
"v_max_f32 v127, v127, v143\n"
"v_mfma_f32_16x16x32_bf16 v[112:115], v[100:103], v[162:165], v[112:115]\n"
"buffer_load_dwordx4 v[52:55], v192, s[28:31], s15 offen sc1\n"
"ds_read_b64_tr_b16 v[142:143], v183 offset:41352\n"
"v_mfma_f32_16x16x32_bf16 v[116:119], v[116:119], v[148:151], 0\n"
"v_max3_f32 v170, v170, v125, v126\n"
"v_mov_b64 v[98:99], 0\n"
"v_mfma_f32_16x16x32_bf16 v[116:119], v[108:111], v[162:165], v[116:119]\n"
"s_waitcnt lgkmcnt(5)\n"
"v_mov_b64 v[100:101], 0\n"
"v_max_f32 v128, v128, v112\n"
"v_mfma_f32_16x16x32_bf16 v[236:239], a[220:223], v[64:67], 0\n"
"v_max_f32 v129, v129, v113\n"
"v_max_f32 v130, v130, v114\n"
"v_mfma_f32_16x16x32_bf16 v[236:239], a[232:235], v[68:71], v[236:239]\n"
"buffer_load_dwordx2 v[152:153], v193, s[28:31], s15 offen offset:1024 sc1\n"
"v_max3_f32 v170, v170, v127, v128\n"
"v_mfma_f32_16x16x32_bf16 v[132:135], v[132:135], v[148:151], 0\n"
"v_max_f32 v131, v131, v115\n"
"v_max3_f32 v170, v170, v129, v130\n"
"v_mfma_f32_16x16x32_bf16 v[132:135], v[104:107], v[160:163], v[132:135]\n"
"v_mov_b64 v[106:107], 0\n"
"v_mov_b64 v[108:109], 0\n"
"v_mfma_f32_16x16x32_bf16 v[240:243], a[224:227], v[64:67], 0\n"
"ds_read_b128 v[64:67], v192 offset:57344\n"
"v_mfma_f32_16x16x32_bf16 v[240:243], a[236:239], v[68:71], v[240:243]\n"
"ds_read_b128 v[68:71], v192 offset:58368\n"
"s_nop 1\n"
"v_max_f32 v132, v132, v116\n"
"v_max_f32 v133, v133, v117\n"
"v_max_f32 v134, v134, v118\n"
"v_max3_f32 v170, v170, v131, v132\n"
"v_max_f32 v135, v135, v119\n"
"v_max3_f32 v170, v170, v133, v134\n"
"v_max_f32 v171, v170, v135\n"
"v_max_f32 v170, v170, v135\n"
"s_nop 1\n"
"v_permlane16_swap_b32 v170, v171\n"
"v_max_f32 v170, v170, v171\n"
"v_mov_b32 v171, v170\n"
"s_nop 1\n"
"v_permlane32_swap_b32 v170, v171\n"
"v_max_f32 v170, v170, v171\n"
"v_mul_f32 v170, 0xbe553b95, v170\n"
"v_pk_fma_f32 v[120:121], v[120:121], s[70:71], v[170:171] op_sel_hi:[1,0,0]\n"
"v_pk_fma_f32 v[122:123], v[122:123], s[70:71], v[170:171] op_sel_hi:[1,0,0]\n"
"v_pk_fma_f32 v[124:125], v[124:125], s[70:71], v[170:171] op_sel_hi:[1,0,0]\n"
"v_pk_fma_f32 v[126:127], v[126:127], s[70:71], v[170:171] op_sel_hi:[1,0,0]\n"
"v_pk_fma_f32 v[128:129], v[128:129], s[70:71], v[170:171] op_sel_hi:[1,0,0]\n"
"v_pk_fma_f32 v[130:131], v[130:131], s[70:71], v[170:171] op_sel_hi:[1,0,0]\n"
"v_pk_fma_f32 v[132:133], v[132:133], s[70:71], v[170:171] op_sel_hi:[1,0,0]\n"
"v_pk_fma_f32 v[134:135], v[134:135], s[70:71], v[170:171] op_sel_hi:[1,0,0]\n"
"v_exp_f32 v120, v120\n"
"v_exp_f32 v121, v121\n"
"v_exp_f32 v122, v122\n"
"v_exp_f32 v123, v123\n"
"v_exp_f32 v124, v124\n"
"v_exp_f32 v125, v125\n"
"v_exp_f32 v126, v126\n"
"v_exp_f32 v127, v127\n"
"v_cvt_pk_bf16_f32 v82, v120, v122\n"
"v_cvt_pk_bf16_f32 v83, v121, v123\n"
"v_cvt_pk_bf16_f32 v84, v124, v126\n"
"v_cvt_pk_bf16_f32 v85, v125, v127\n"
"v_exp_f32 v128, v128\n"
"s_nop 0\n"
"v_smfmac_f32_16x16x64_bf16 v[252:255], v[82:85], v[224:231], v63\n"
"s_waitcnt lgkmcnt(1)\n"
"ds_read_b64_tr_b16 v[224:225], v182 offset:40640\n"
"v_mfma_f32_16x16x32_bf16 v[232:235], a[240:243], v[64:67], v[232:235]\n"
"s_waitcnt lgkmcnt(1)\n"
"ds_read_b64_tr_b16 v[226:227], v183 offset:40640\n"
"ds_read_b64_tr_b16 v[228:229], v182 offset:41408\n"
"v_mfma_f32_16x16x32_bf16 v[232:235], a[252:255], v[68:71], v[232:235]\n"
"v_exp_f32 v129, v129\n"
"v_mfma_f32_16x16x32_bf16 v[236:239], a[244:247], v[64:67], v[236:239]\n"
"ds_read_b64_tr_b16 v[230:231], v183 offset:41408\n"
"v_exp_f32 v130, v130\n"
"v_mfma_f32_16x16x32_bf16 v[236:239], v[0:3], v[68:71], v[236:239]\n"
"v_exp_f32 v131, v131\n"
"v_mfma_f32_16x16x32_bf16 v[240:243], a[248:251], v[64:67], v[240:243]\n"
"ds_read_b128 v[64:67], v192 offset:59392\n"
"v_cvt_pk_bf16_f32 v90, v128, v130\n"
"v_mfma_f32_16x16x32_bf16 v[240:243], v[4:7], v[68:71], v[240:243]\n"
"ds_read_b128 v[68:71], v192 offset:60416\n"
"v_cvt_pk_bf16_f32 v91, v129, v131\n"
"v_smfmac_f32_16x16x64_bf16 v[98:101], v[82:85], v[244:251], v63\n"
"ds_read_b64_tr_b16 v[244:245], v182 offset:40648\n"
"v_smfmac_f32_16x16x64_bf16 v[106:109], v[82:85], a[168:175], v63\n"
"ds_read_b64_tr_b16 v[246:247], v183 offset:40648\n"
"v_mfma_f32_16x16x32_bf16 v[82:85], v[172:175], v[82:85], 0\n"
"ds_read_b64_tr_b16 v[248:249], v182 offset:41416\n"
"ds_read_b64_tr_b16 v[250:251], v183 offset:41416\n"
"v_exp_f32 v132, v132\n"
"v_exp_f32 v133, v133\n"
"v_exp_f32 v134, v134\n"
"v_exp_f32 v135, v135\n"
"v_cvt_pk_bf16_f32 v92, v132, v134\n"
"v_cvt_pk_bf16_f32 v93, v133, v135\n"
"s_waitcnt lgkmcnt(5)\n"
"v_mfma_f32_16x16x32_bf16 v[232:235], v[8:11], v[64:67], v[232:235]\n"
"s_waitcnt lgkmcnt(4)\n"
"s_barrier\n"
"v_mfma_f32_16x16x32_bf16 v[232:235], v[20:23], v[68:71], v[232:235]\n"
"s_waitcnt vmcnt(7)\n"
"v_add3_u32 v201, v201, s50, -11\n"
"v_cmp_lt_u32 vcc, 64, v201\n"
"v_mfma_f32_16x16x32_bf16 v[240:243], v[16:19], v[64:67], v[240:243]\n"
"ds_write_b128 v202, a[150:153] offset:24576\n"
"v_mfma_f32_16x16x32_bf16 v[240:243], v[28:31], v[68:71], v[240:243]\n"
"s_waitcnt lgkmcnt(1)\n"
"s_nop 0\n"
"s_cbranch_vccnz .Lp10%=\n"
".Lr10%=:\n"
"v_mfma_f32_16x16x32_bf16 v[236:239], v[12:15], v[64:67], v[236:239]\n"
"buffer_load_dwordx4 a[162:165], v192, s[28:31], s16 offen sc1\n"
"ds_read_b128 v[44:47], v192\n"
"v_mfma_f32_16x16x32_bf16 v[236:239], v[24:27], v[68:71], v[236:239]\n"
"ds_read_b128 v[48:51], v192 offset:1024\n"
"ds_read_b128 a[0:3], v192 offset:2048\n"
"v_smfmac_f32_16x16x64_bf16 v[252:255], v[90:93], v[224:231], v63\n"
"s_waitcnt vmcnt(5)\n"
"ds_read_b128 a[4:7], v192 offset:3072\n"
"v_smfmac_f32_16x16x64_bf16 v[98:101], v[90:93], v[244:251], v63\n"
"ds_write_b64 v203, a[154:155] offset:24576\n"
"v_smfmac_f32_16x16x64_bf16 v[106:109], v[90:93], v[136:143], v63\n"
"buffer_load_dwordx2 a[166:167], v193, s[28:31], s16 offen offset:1024 sc1\n"
"v_mfma_f32_16x16x32_bf16 v[90:93], v[172:175], v[90:93], v[82:85]\n"
"ds_write_b128 v202, a[156:159] offset:30720\n"
"v_cvt_pk_bf16_f32 v232, v232, v233\n"
"v_cvt_pk_bf16_f32 v233, v234, v235\n"
"v_cvt_pk_bf16_f32 v240, v240, v241\n"
"v_cvt_pk_bf16_f32 v234, v236, v237\n"
"v_cvt_pk_bf16_f32 v235, v238, v239\n"
"v_cvt_pk_bf16_f32 v241, v242, v243\n"
"v_cvt_pk_bf16_f32 v252, v252, v253\n"
"v_rcp_f32 v171, v90\n"
"v_cvt_pk_bf16_f32 v253, v254, v255\n"
"v_cvt_pk_bf16_f32 v171, v171, v171\n"
"v_and_b32 v92, v171, v176\n"
"v_and_b32 v93, v171, v177\n"
"v_cvt_pk_bf16_f32 v98, v98, v99\n"
"v_cvt_pk_bf16_f32 v99, v100, v101\n"
"v_mfma_f32_16x16x16_bf16 v[252:255], v[252:253], v[92:93], 0\n"
"s_waitcnt lgkmcnt(5)\n"
"ds_write_b64 v203, a[160:161] offset:30720\n"
"v_mfma_f32_16x16x16_bf16 v[98:101], v[98:99], v[92:93], 0\n"
"v_cvt_pk_bf16_f32 v106, v106, v107\n"
"v_cvt_pk_bf16_f32 v107, v108, v109\n"
"v_mfma_f32_16x16x32_bf16 v[204:207], a[216:219], v[44:47], 0\n"
"s_waitcnt lgkmcnt(5)\n"
"ds_read_b128 a[8:11], v192 offset:4096\n"
"ds_read_b128 a[12:15], v192 offset:5120\n"
"v_mfma_f32_16x16x16_bf16 v[106:109], v[106:107], v[92:93], 0\n"
".Lic7%=:\n"
"s_cbranch_execz .Lib1%=\n"
"ds_read_b128 v[64:67], v202 offset:49152\n"
"v_cvt_pk_bf16_f32 v252, v252, v253\n"
"v_mfma_f32_16x16x32_bf16 v[208:211], a[220:223], v[44:47], 0\n"
"ds_read_b64 v[68:69], v62 offset:49152\n"
"v_cvt_pk_bf16_f32 v253, v254, v255\n"
"v_mfma_f32_16x16x32_bf16 v[208:211], a[232:235], v[48:51], v[208:211]\n"
"s_waitcnt lgkmcnt(8)\n"
"ds_write_b128 v196, v[232:235] offset:36864\n"
"v_mfma_f32_16x16x32_bf16 v[208:211], a[244:247], a[0:3], v[208:211]\n"
"s_waitcnt lgkmcnt(8)\n"
"v_cvt_pk_bf16_f32 v254, v98, v99\n"
"v_cvt_pk_bf16_f32 v255, v100, v101\n"
"v_mfma_f32_16x16x32_bf16 v[208:211], v[0:3], a[4:7], v[208:211]\n"
"v_cvt_pk_bf16_f32 v106, v106, v107\n"
"v_min_u32 v252, -2, v252\n"
"v_mfma_f32_16x16x32_bf16 v[212:215], a[224:227], v[44:47], 0\n"
"s_waitcnt lgkmcnt(4)\n"
"ds_write_b64 v197, v[240:241] offset:36864\n"
"v_mfma_f32_16x16x32_bf16 v[212:215], a[236:239], v[48:51], v[212:215]\n"
"v_min_u32 v253, -2, v253\n"
"v_min_u32 v254, -2, v254\n"
"v_mfma_f32_16x16x32_bf16 v[212:215], a[248:251], a[0:3], v[212:215]\n"
"s_waitcnt lgkmcnt(2)\n"
"v_min_u32 v255, -2, v255\n"
"v_cvt_pk_bf16_f32 v107, v108, v109\n"
"v_mfma_f32_16x16x32_bf16 v[212:215], v[4:7], a[4:7], v[212:215]\n"
"buffer_store_dwordx4 v[252:255], v192, s[28:31], s37 offen sc1\n"
"v_mfma_f32_16x16x32_bf16 v[212:215], v[16:19], a[8:11], v[212:215]\n"
"v_min_u32 v106, -2, v106\n"
"v_min_u32 v107, -2, v107\n"
"v_mfma_f32_16x16x32_bf16 v[212:215], v[28:31], a[12:15], v[212:215]\n"
"v_mfma_f32_16x16x32_bf16 v[204:207], a[228:231], v[48:51], v[204:207]\n"
"s_waitcnt lgkmcnt(0)\n"
"s_barrier\n"
"v_mfma_f32_16x16x32_bf16 v[204:207], a[240:243], a[0:3], v[204:207]\n"
"ds_read_b128 v[44:47], v192 offset:24576\n"
"ds_read_b128 v[48:51], v192 offset:25600\n"
"v_mfma_f32_16x16x32_bf16 v[204:207], a[252:255], a[4:7], v[204:207]\n"
"ds_read_b128 a[0:3], v192 offset:26624\n"
"v_cvt_pk_bf16_f32 v212, v212, v213\n"
"v_mfma_f32_16x16x32_bf16 v[204:207], v[8:11], a[8:11], v[204:207]\n"
"ds_read_b128 a[4:7], v192 offset:27648\n"
"v_cvt_pk_bf16_f32 v213, v214, v215\n"
"v_mfma_f32_16x16x32_bf16 v[204:207], v[20:23], a[12:15], v[204:207]\n"
"ds_write_b64 v197, v[212:213] offset:12288\n"
"v_mfma_f32_16x16x32_bf16 v[208:211], v[12:15], a[8:11], v[208:211]\n"
"buffer_store_dwordx2 v[106:107], v193, s[28:31], s37 offen offset:1024 sc1\n"
"v_mfma_f32_16x16x32_bf16 v[208:211], v[24:27], a[12:15], v[208:211]\n"
"s_add_u32 s37, s37, 1572864\n"
"ds_read_b128 a[8:11], v192 offset:28672\n"
"ds_read_b128 a[12:15], v192 offset:29696\n"
"s_nop 0\n"
"v_cvt_pk_bf16_f32 v204, v204, v205\n"
"v_cvt_pk_bf16_f32 v205, v206, v207\n"
"s_waitcnt lgkmcnt(6)\n"
"v_mfma_f32_16x16x32_bf16 v[232:235], a[216:219], v[44:47], 0\n"
"s_waitcnt lgkmcnt(5)\n"
"v_cvt_pk_bf16_f32 v206, v208, v209\n"
"v_cvt_pk_bf16_f32 v207, v210, v211\n"
"v_mfma_f32_16x16x32_bf16 v[232:235], a[228:231], v[48:51], v[232:235]\n"
"s_waitcnt lgkmcnt(4)\n"
"ds_write_b128 v196, v[204:207] offset:12288\n"
"v_mfma_f32_16x16x32_bf16 v[232:235], a[240:243], a[0:3], v[232:235]\n"
"s_waitcnt lgkmcnt(4)\n"
"buffer_load_dwordx4 v[144:147], v192, s[28:31], s17 offen sc1\n"
"v_mfma_f32_16x16x32_bf16 v[232:235], a[252:255], a[4:7], v[232:235]\n"
"v_mfma_f32_16x16x32_bf16 v[236:239], a[220:223], v[44:47], 0\n"
"s_waitcnt lgkmcnt(2)\n"
"v_mfma_f32_16x16x32_bf16 v[236:239], a[232:235], v[48:51], v[236:239]\n"
"v_mfma_f32_16x16x32_bf16 v[236:239], a[244:247], a[0:3], v[236:239]\n"
"buffer_load_dwordx2 v[154:155], v193, s[28:31], s17 offen offset:1024 sc1\n"
"v_mfma_f32_16x16x32_bf16 v[236:239], v[0:3], a[4:7], v[236:239]\n"
"v_mfma_f32_16x16x32_bf16 v[236:239], v[12:15], a[8:11], v[236:239]\n"
"s_waitcnt lgkmcnt(1)\n"
"v_mfma_f32_16x16x32_bf16 v[236:239], v[24:27], a[12:15], v[236:239]\n"
"v_mfma_f32_16x16x32_bf16 v[240:243], a[224:227], v[44:47], 0\n"
"buffer_store_dwordx4 v[64:67], v194, s[24:27], s36 offen sc1\n"
"v_mfma_f32_16x16x32_bf16 v[240:243], a[236:239], v[48:51], v[240:243]\n"
"s_waitcnt vmcnt(8)\n"
"ds_read_b128 v[44:47], v192 offset:32768\n"
"ds_read_b128 v[48:51], v192 offset:33792\n"
"v_mfma_f32_16x16x32_bf16 v[240:243], a[248:251], a[0:3], v[240:243]\n"
"ds_read_b128 a[0:3], v192 offset:34816\n"
"v_max3_u32 v200, v52, v53, v54\n"
"v_mfma_f32_16x16x32_bf16 v[240:243], v[4:7], a[4:7], v[240:243]\n"
"s_waitcnt vmcnt(7)\n"
"ds_read_b128 a[4:7], v192 offset:35840\n"
"v_max3_u32 v200, v200, v55, v152\n"
"v_mfma_f32_16x16x32_bf16 v[240:243], v[16:19], a[8:11], v[240:243]\n"
"buffer_store_dwordx2 v[68:69], v195, s[24:27], s36 offen sc1\n"
"v_mfma_f32_16x16x32_bf16 v[240:243], v[28:31], a[12:15], v[240:243]\n"
"s_add_u32 s36, s36, s33\n"
"ds_read_b128 v[64:67], v202 offset:36864\n"
"ds_read_b64 v[68:69], v62 offset:36864\n"
"v_mfma_f32_16x16x32_bf16 v[232:235], v[8:11], a[8:11], v[232:235]\n"
"v_max_u32 v200, v200, v153\n"
"v_cmp_eq_u32 vcc, -1, v200\n"
"v_mfma_f32_16x16x32_bf16 v[232:235], v[20:23], a[12:15], v[232:235]\n"
"s_nop 0\n"
"v_cvt_pk_bf16_f32 v240, v240, v241\n"
"v_cvt_pk_bf16_f32 v241, v242, v243\n"
"ds_write_b64 v197, v[240:241] offset:43008\n"
"s_nop 3\n"
"v_cvt_pk_bf16_f32 v232, v232, v233\n"
"s_waitcnt vmcnt(7)\n"
"ds_write_b128 v202, a[162:165] offset:49152\n"
"v_cvt_pk_bf16_f32 v233, v234, v235\n"
"s_waitcnt lgkmcnt(3)\n"
"buffer_store_dwordx4 v[64:67], v194, s[24:27], s36 offen sc1\n"
"s_waitcnt vmcnt(7) lgkmcnt(2)\n"
"ds_write_b64 v203, a[166:167] offset:49152\n"
"v_cvt_pk_bf16_f32 v234, v236, v237\n"
"v_cvt_pk_bf16_f32 v235, v238, v239\n"
"s_cbranch_vccnz .Lp11%=\n"
".Lr11%=:\n"
"ds_write_b128 v202, v[52:55] offset:18432\n"
"buffer_store_dwordx2 v[68:69], v195, s[24:27], s36 offen sc1\n"
"s_add_u32 s36, s36, s33\n"
"s_add_u32 s8, s8, s36\n"
"s_add_u32 s9, s9, s36\n"
"s_add_u32 s10, s10, s36\n"
"ds_write_b64 v203, v[152:153] offset:18432\n"
"s_add_u32 s11, s11, s36\n"
"s_add_u32 s20, s20, s36\n"
"s_add_u32 s21, s21, s36\n"
"s_add_u32 s22, s22, s36\n"
"ds_read_b128 v[64:67], v192 offset:30720\n"
"ds_read_b128 v[68:71], v192 offset:31744\n"
"ds_write_b128 v196, v[232:235] offset:43008\n"
"s_waitcnt lgkmcnt(2)\n"
"v_mfma_f32_16x16x32_bf16 v[32:35], a[216:219], v[64:67], 0\n"
"s_waitcnt lgkmcnt(1)\n"
"s_barrier\n"
"v_mfma_f32_16x16x32_bf16 v[32:35], a[228:231], v[68:71], v[32:35]\n"
"ds_read_b128 a[8:11], v202 offset:12288\n"
"ds_read_b64 a[12:13], v62 offset:12288\n"
"v_mfma_f32_16x16x32_bf16 v[32:35], a[240:243], v[44:47], v[32:35]\n"
"v_mfma_f32_16x16x32_bf16 v[32:35], a[252:255], v[48:51], v[32:35]\n"
"v_mfma_f32_16x16x32_bf16 v[32:35], v[8:11], a[0:3], v[32:35]\n"
"v_mfma_f32_16x16x32_bf16 v[32:35], v[20:23], a[4:7], v[32:35]\n"
"v_mfma_f32_16x16x32_bf16 v[36:39], a[220:223], v[64:67], 0\n"
"v_mfma_f32_16x16x32_bf16 v[36:39], a[232:235], v[68:71], v[36:39]\n"
"v_mfma_f32_16x16x32_bf16 v[36:39], a[244:247], v[44:47], v[36:39]\n"
"s_waitcnt lgkmcnt(0)\n"
"buffer_store_dwordx4 a[8:11], v194, s[24:27], s8 offen sc1\n"
"v_mfma_f32_16x16x32_bf16 v[36:39], v[0:3], v[48:51], v[36:39]\n"
"v_mfma_f32_16x16x32_bf16 v[36:39], v[12:15], a[0:3], v[36:39]\n"
"s_nop 0\n"
"v_cvt_pk_bf16_f32 v32, v32, v33\n"
"v_mfma_f32_16x16x32_bf16 v[36:39], v[24:27], a[4:7], v[36:39]\n"
"v_cvt_pk_bf16_f32 v33, v34, v35\n"
"v_mfma_f32_16x16x32_bf16 v[40:43], a[224:227], v[64:67], 0\n"
"ds_read_b128 v[64:67], v192 offset:18432\n"
"buffer_store_dwordx2 a[12:13], v195, s[24:27], s8 offen sc1\n"
"v_mfma_f32_16x16x32_bf16 v[40:43], a[236:239], v[68:71], v[40:43]\n"
"ds_read_b128 v[68:71], v192 offset:19456\n"
"v_mfma_f32_16x16x32_bf16 v[40:43], a[248:251], v[44:47], v[40:43]\n"
"ds_read_b128 v[44:47], v192 offset:20480\n"
"v_cvt_pk_bf16_f32 v34, v36, v37\n"
"v_mfma_f32_16x16x32_bf16 v[40:43], v[4:7], v[48:51], v[40:43]\n"
"ds_read_b128 v[48:51], v192 offset:21504\n"
"v_cvt_pk_bf16_f32 v35, v38, v39\n"
"v_mfma_f32_16x16x32_bf16 v[40:43], v[16:19], a[0:3], v[40:43]\n"
"ds_read_b128 a[0:3], v192 offset:22528\n"
"ds_write_b128 v196, v[32:35] offset:6144\n"
"v_mfma_f32_16x16x32_bf16 v[40:43], v[28:31], a[4:7], v[40:43]\n"
"s_waitcnt lgkmcnt(5)\n"
"v_mfma_f32_16x16x32_bf16 v[204:207], a[216:219], v[64:67], 0\n"
"ds_read_b128 a[4:7], v192 offset:23552\n"
"v_mfma_f32_16x16x32_bf16 v[208:211], a[220:223], v[64:67], 0\n"
"s_waitcnt lgkmcnt(5)\n"
"v_mfma_f32_16x16x32_bf16 v[208:211], a[232:235], v[68:71], v[208:211]\n"
"s_waitcnt lgkmcnt(4)\n"
"s_nop 0\n"
"v_cvt_pk_bf16_f32 v40, v40, v41\n"
"v_mfma_f32_16x16x32_bf16 v[208:211], a[244:247], v[44:47], v[208:211]\n"
"s_waitcnt lgkmcnt(3)\n"
"v_cvt_pk_bf16_f32 v41, v42, v43\n"
"v_mfma_f32_16x16x32_bf16 v[208:211], v[0:3], v[48:51], v[208:211]\n"
"s_waitcnt lgkmcnt(2)\n"
"ds_write_b64 v197, v[40:41] offset:6144\n"
"v_mfma_f32_16x16x32_bf16 v[208:211], v[12:15], a[0:3], v[208:211]\n"
"v_mfma_f32_16x16x32_bf16 v[212:215], a[224:227], v[64:67], 0\n"
"v_mfma_f32_16x16x32_bf16 v[212:215], a[236:239], v[68:71], v[212:215]\n"
"s_waitcnt lgkmcnt(1)\n"
"v_mfma_f32_16x16x32_bf16 v[212:215], a[248:251], v[44:47], v[212:215]\n"
"v_mfma_f32_16x16x32_bf16 v[212:215], v[4:7], v[48:51], v[212:215]\n"
"s_waitcnt vmcnt(7)\n"
"v_max3_u32 v200, v144, v145, v146\n"
"s_waitcnt vmcnt(6)\n"
"v_mfma_f32_16x16x32_bf16 v[212:215], v[16:19], a[0:3], v[212:215]\n"
"v_max3_u32 v200, v200, v147, v154\n"
"v_max_u32 v200, v200, v155\n"
"v_mfma_f32_16x16x32_bf16 v[212:215], v[28:31], a[4:7], v[212:215]\n"
"s_waitcnt lgkmcnt(0)\n"
"v_cmp_eq_u32 vcc, -1, v200\n"
"s_barrier\n"
"v_mfma_f32_16x16x32_bf16 v[204:207], a[228:231], v[68:71], v[204:207]\n"
"ds_read_b128 a[8:11], v202 offset:43008\n"
"ds_read_b64 a[12:13], v62 offset:43008\n"
"v_mfma_f32_16x16x32_bf16 v[204:207], a[240:243], v[44:47], v[204:207]\n"
"s_cbranch_vccnz .Lp12%=\n"
".Lr12%=:\n"
"v_mfma_f32_16x16x32_bf16 v[204:207], a[252:255], v[48:51], v[204:207]\n"
"ds_write_b128 v202, v[144:147] offset:30720\n"
"v_mfma_f32_16x16x32_bf16 v[204:207], v[8:11], a[0:3], v[204:207]\n"
"v_cvt_pk_bf16_f32 v212, v212, v213\n"
"v_cvt_pk_bf16_f32 v213, v214, v215\n"
"v_mfma_f32_16x16x32_bf16 v[204:207], v[20:23], a[4:7], v[204:207]\n"
"ds_write_b64 v203, v[154:155] offset:30720\n"
"v_mfma_f32_16x16x32_bf16 v[208:211], v[24:27], a[4:7], v[208:211]\n"
"ds_read_b128 v[44:47], v192 offset:49152\n"
"ds_read_b128 v[48:51], v192 offset:50176\n"
"s_waitcnt lgkmcnt(5)\n"
"buffer_store_dwordx4 a[8:11], v194, s[24:27], s9 offen sc1\n"
"ds_read_b128 a[0:3], v192 offset:51200\n"
"ds_read_b128 a[4:7], v192 offset:52224\n"
"v_cvt_pk_bf16_f32 v204, v204, v205\n"
"v_cvt_pk_bf16_f32 v205, v206, v207\n"
"ds_read_b128 a[8:11], v192 offset:53248\n"
"v_cvt_pk_bf16_f32 v206, v208, v209\n"
"ds_read_b128 v[64:67], v202 offset:6144\n"
"v_cvt_pk_bf16_f32 v207, v210, v211\n"
"ds_read_b64 v[68:69], v62 offset:6144\n"
"buffer_load_dwordx4 v[56:59], v192, s[28:31], s18 offen sc1\n"
"ds_write_b128 v196, v[204:207] offset:55296\n"
"s_waitcnt lgkmcnt(7)\n"
"v_mfma_f32_16x16x32_bf16 v[232:235], a[216:219], v[44:47], 0\n"
"s_waitcnt lgkmcnt(6)\n"
"v_mfma_f32_16x16x32_bf16 v[232:235], a[228:231], v[48:51], v[232:235]\n"
"s_waitcnt lgkmcnt(5)\n"
"ds_write_b64 v197, v[212:213] offset:55296\n"
"v_mfma_f32_16x16x32_bf16 v[232:235], a[240:243], a[0:3], v[232:235]\n"
"s_waitcnt lgkmcnt(5)\n"
"buffer_load_dwordx2 v[60:61], v193, s[28:31], s18 offen offset:1024 sc1\n"
"v_mfma_f32_16x16x32_bf16 v[232:235], a[252:255], a[4:7], v[232:235]\n"
"s_waitcnt lgkmcnt(4)\n"
"v_mfma_f32_16x16x32_bf16 v[232:235], v[8:11], a[8:11], v[232:235]\n"
"s_waitcnt lgkmcnt(2)\n"
"v_mfma_f32_16x16x32_bf16 v[236:239], a[220:223], v[44:47], 0\n"
"v_mfma_f32_16x16x32_bf16 v[236:239], a[232:235], v[48:51], v[236:239]\n"
"buffer_store_dwordx2 a[12:13], v195, s[24:27], s9 offen sc1\n"
"v_mfma_f32_16x16x32_bf16 v[236:239], a[244:247], a[0:3], v[236:239]\n"
"s_waitcnt lgkmcnt(0)\n"
"ds_read_b128 a[12:15], v192 offset:54272\n"
"v_mfma_f32_16x16x32_bf16 v[236:239], v[0:3], a[4:7], v[236:239]\n"
"s_barrier\n"
"v_mfma_f32_16x16x32_bf16 v[236:239], v[12:15], a[8:11], v[236:239]\n"
"v_mfma_f32_16x16x32_bf16 v[240:243], a[224:227], v[44:47], 0\n"
"ds_read_b128 v[44:47], v192 offset:30720\n"
"buffer_store_dwordx4 v[64:67], v194, s[24:27], s10 offen sc1\n"
"v_mfma_f32_16x16x32_bf16 v[240:243], a[236:239], v[48:51], v[240:243]\n"
"ds_read_b128 v[48:51], v192 offset:31744\n"
"ds_read_b128 v[64:67], v202 offset:55296\n"
"v_mfma_f32_16x16x32_bf16 v[240:243], a[248:251], a[0:3], v[240:243]\n"
"ds_read_b128 a[0:3], v192 offset:32768\n"
"v_mfma_f32_16x16x32_bf16 v[240:243], v[4:7], a[4:7], v[240:243]\n"
"s_waitcnt lgkmcnt(4)\n"
"ds_read_b128 a[4:7], v192 offset:33792\n"
"v_mfma_f32_16x16x32_bf16 v[240:243], v[16:19], a[8:11], v[240:243]\n"
"ds_read_b128 a[8:11], v192 offset:34816\n"
"buffer_store_dwordx2 v[68:69], v195, s[24:27], s10 offen sc1\n"
"v_mfma_f32_16x16x32_bf16 v[240:243], v[28:31], a[12:15], v[240:243]\n"
"s_waitcnt lgkmcnt(5)\n"
"ds_read_b64 v[68:69], v62 offset:55296\n"
"v_mfma_f32_16x16x32_bf16 v[232:235], v[20:23], a[12:15], v[232:235]\n"
"v_mfma_f32_16x16x32_bf16 v[236:239], v[24:27], a[12:15], v[236:239]\n"
"ds_read_b128 a[12:15], v192 offset:35840\n"
"v_mfma_f32_16x16x32_bf16 v[32:35], a[216:219], v[44:47], 0\n"
"s_waitcnt lgkmcnt(5)\n"
"s_nop 0\n"
"v_cvt_pk_bf16_f32 v240, v240, v241\n"
"v_mfma_f32_16x16x32_bf16 v[32:35], a[228:231], v[48:51], v[32:35]\n"
"s_waitcnt lgkmcnt(4)\n"
"buffer_store_dwordx4 v[64:67], v194, s[24:27], s11 offen sc1\n"
"v_mfma_f32_16x16x32_bf16 v[32:35], a[240:243], a[0:3], v[32:35]\n"
"s_waitcnt lgkmcnt(3)\n"
"v_cvt_pk_bf16_f32 v232, v232, v233\n"
"v_cvt_pk_bf16_f32 v233, v234, v235\n"
"v_mfma_f32_16x16x32_bf16 v[32:35], a[252:255], a[4:7], v[32:35]\n"
"s_waitcnt lgkmcnt(1)\n"
"v_cvt_pk_bf16_f32 v234, v236, v237\n"
"v_cvt_pk_bf16_f32 v235, v238, v239\n"
"v_mfma_f32_16x16x32_bf16 v[32:35], v[8:11], a[8:11], v[32:35]\n"
"ds_write_b128 v196, v[232:235]\n"
"v_mfma_f32_16x16x32_bf16 v[40:43], a[224:227], v[44:47], 0\n"
"s_waitcnt lgkmcnt(1)\n"
"buffer_store_dwordx2 v[68:69], v195, s[24:27], s11 offen sc1\n"
"v_mfma_f32_16x16x32_bf16 v[40:43], a[236:239], v[48:51], v[40:43]\n"
"v_cvt_pk_bf16_f32 v241, v242, v243\n"
"v_mfma_f32_16x16x32_bf16 v[40:43], a[248:251], a[0:3], v[40:43]\n"
"ds_write_b64 v197, v[240:241]\n"
"v_mfma_f32_16x16x32_bf16 v[40:43], v[4:7], a[4:7], v[40:43]\n"
"v_mfma_f32_16x16x32_bf16 v[40:43], v[16:19], a[8:11], v[40:43]\n"
"v_mfma_f32_16x16x32_bf16 v[40:43], v[28:31], a[12:15], v[40:43]\n"
"v_mfma_f32_16x16x32_bf16 v[36:39], a[220:223], v[44:47], 0\n"
"v_mfma_f32_16x16x32_bf16 v[36:39], a[232:235], v[48:51], v[36:39]\n"
"v_mfma_f32_16x16x32_bf16 v[36:39], a[244:247], a[0:3], v[36:39]\n"
"v_mfma_f32_16x16x32_bf16 v[36:39], v[0:3], a[4:7], v[36:39]\n"
"v_mfma_f32_16x16x32_bf16 v[36:39], v[12:15], a[8:11], v[36:39]\n"
"v_mfma_f32_16x16x32_bf16 v[36:39], v[24:27], a[12:15], v[36:39]\n"
"v_mfma_f32_16x16x32_bf16 v[32:35], v[20:23], a[12:15], v[32:35]\n"
"s_nop 0\n"
"v_cvt_pk_bf16_f32 v40, v40, v41\n"
"v_cvt_pk_bf16_f32 v41, v42, v43\n"
"ds_write_b64 v197, v[40:41] offset:36864\n"
"s_nop 3\n"
"v_cvt_pk_bf16_f32 v32, v32, v33\n"
"v_cvt_pk_bf16_f32 v33, v34, v35\n"
"v_cvt_pk_bf16_f32 v34, v36, v37\n"
"v_cvt_pk_bf16_f32 v35, v38, v39\n"
"ds_write_b128 v196, v[32:35] offset:36864\n"
"s_waitcnt vmcnt(6)\n"
"v_max3_u32 v200, v56, v57, v58\n"
"s_waitcnt vmcnt(5)\n"
"v_max3_u32 v200, v200, v59, v60\n"
"v_max_u32 v200, v200, v61\n"
"v_cmp_eq_u32 vcc, -1, v200\n"
"s_nop 4\n"
"s_cbranch_vccnz .Lp13%=\n"
".Lr13%=:\n"
"ds_write_b128 v202, v[56:59] offset:24576\n"
"ds_write_b64 v203, v[60:61] offset:24576\n"
"s_waitcnt lgkmcnt(0)\n"
"s_barrier\n"
"ds_read_b128 a[0:3], v192 offset:24576\n"
"ds_read_b128 a[4:7], v192 offset:25600\n"
"ds_read_b128 a[8:11], v192 offset:26624\n"
"ds_read_b128 a[12:15], v192 offset:27648\n"
"ds_read_b128 v[64:67], v202\n"
"ds_read_b64 v[68:69], v62\n"
"ds_read_b128 v[44:47], v202 offset:36864\n"
"ds_read_b64 v[48:49], v62 offset:36864\n"
"s_waitcnt lgkmcnt(7)\n"
"v_mfma_f32_16x16x32_bf16 v[204:207], a[216:219], a[0:3], 0\n"
"s_waitcnt lgkmcnt(6)\n"
"v_mfma_f32_16x16x32_bf16 v[204:207], a[228:231], a[4:7], v[204:207]\n"
"s_waitcnt lgkmcnt(5)\n"
"v_mfma_f32_16x16x32_bf16 v[204:207], a[240:243], a[8:11], v[204:207]\n"
"s_waitcnt lgkmcnt(4)\n"
"v_mfma_f32_16x16x32_bf16 v[204:207], a[252:255], a[12:15], v[204:207]\n"
"s_waitcnt lgkmcnt(2)\n"
"buffer_store_dwordx4 v[64:67], v194, s[24:27], s20 offen sc1\n"
"v_mfma_f32_16x16x32_bf16 v[212:215], a[224:227], a[0:3], 0\n"
"s_waitcnt lgkmcnt(0)\n"
"ds_read_b128 v[64:67], v192 offset:28672\n"
"v_mfma_f32_16x16x32_bf16 v[212:215], a[236:239], a[4:7], v[212:215]\n"
"v_mfma_f32_16x16x32_bf16 v[212:215], a[248:251], a[8:11], v[212:215]\n"
"v_mfma_f32_16x16x32_bf16 v[212:215], v[4:7], a[12:15], v[212:215]\n"
"buffer_store_dwordx2 v[68:69], v195, s[24:27], s20 offen sc1\n"
"v_mfma_f32_16x16x32_bf16 v[208:211], a[220:223], a[0:3], 0\n"
"ds_read_b128 v[68:71], v192 offset:29696\n"
"v_mfma_f32_16x16x32_bf16 v[208:211], a[232:235], a[4:7], v[208:211]\n"
"v_mfma_f32_16x16x32_bf16 v[208:211], a[244:247], a[8:11], v[208:211]\n"
"v_mfma_f32_16x16x32_bf16 v[208:211], v[0:3], a[12:15], v[208:211]\n"
"s_waitcnt lgkmcnt(1)\n"
"buffer_store_dwordx4 v[44:47], v194, s[24:27], s21 offen sc1\n"
"v_mfma_f32_16x16x32_bf16 v[208:211], v[12:15], v[64:67], v[208:211]\n"
"v_mfma_f32_16x16x32_bf16 v[204:207], v[8:11], v[64:67], v[204:207]\n"
"v_mfma_f32_16x16x32_bf16 v[212:215], v[16:19], v[64:67], v[212:215]\n"
"s_waitcnt lgkmcnt(0)\n"
"v_mfma_f32_16x16x32_bf16 v[212:215], v[28:31], v[68:71], v[212:215]\n"
"buffer_store_dwordx2 v[48:49], v195, s[24:27], s21 offen sc1\n"
"v_mfma_f32_16x16x32_bf16 v[204:207], v[20:23], v[68:71], v[204:207]\n"
"v_mfma_f32_16x16x32_bf16 v[208:211], v[24:27], v[68:71], v[208:211]\n"
"s_nop 4\n"
"v_cvt_pk_bf16_f32 v212, v212, v213\n"
"v_cvt_pk_bf16_f32 v213, v214, v215\n"
"v_cvt_pk_bf16_f32 v204, v204, v205\n"
"v_cvt_pk_bf16_f32 v205, v206, v207\n"
"v_cvt_pk_bf16_f32 v206, v208, v209\n"
"v_cvt_pk_bf16_f32 v207, v210, v211\n"
"ds_write_b128 v196, v[204:207] offset:12288\n"
"ds_write_b64 v197, v[212:213] offset:12288\n"
"s_waitcnt lgkmcnt(0)\n"
"s_barrier\n"
"ds_read_b128 v[44:47], v202 offset:12288\n"
"ds_read_b64 v[48:49], v62 offset:12288\n"
"s_waitcnt lgkmcnt(1)\n"
"buffer_store_dwordx4 v[44:47], v194, s[24:27], s22 offen sc1\n"
"s_waitcnt lgkmcnt(0)\n"
"buffer_store_dwordx2 v[48:49], v195, s[24:27], s22 offen sc1\n"
"s_branch .Lend%=\n"
".Lp1%=:\n"
"s_sub_u32 s42, s42, 1\n"
"s_cbranch_scc1 .Lt1%=\n"
"s_sleep 1\n"
"buffer_load_dword v200, off, s[28:31], s39 sc1\n"
"s_waitcnt vmcnt(0)\n"
"v_add3_u32 v200, v200, s50, -2\n"
"v_cmp_lt_u32 vcc, 64, v200\n"
"s_nop 4\n"
"s_cbranch_vccnz .Lp1%=\n"
"s_branch .Lr1%=\n"
".Lt1%=:\n"
"s_mov_b32 s42, 0\n"
"s_branch .Lr1%=\n"
".Lp4%=:\n"
"s_sub_u32 s42, s42, 1\n"
"s_cbranch_scc1 .Lt4%=\n"
"s_sleep 1\n"
"buffer_load_dword v200, off, s[28:31], s39 sc1\n"
"s_waitcnt vmcnt(0)\n"
"v_add3_u32 v200, v200, s50, -5\n"
"v_cmp_lt_u32 vcc, 64, v200\n"
"s_nop 4\n"
"s_cbranch_vccnz .Lp4%=\n"
"s_branch .Lr4%=\n"
".Lt4%=:\n"
"s_mov_b32 s42, 0\n"
"s_branch .Lr4%=\n"
".Lp7%=:\n"
"s_sub_u32 s42, s42, 1\n"
"s_cbranch_scc1 .Lt7%=\n"
"s_sleep 1\n"
"buffer_load_dword v200, off, s[28:31], s39 sc1\n"
"s_waitcnt vmcnt(0)\n"
"v_add3_u32 v200, v200, s50, -8\n"
"v_cmp_lt_u32 vcc, 64, v200\n"
"s_nop 4\n"
"s_cbranch_vccnz .Lp7%=\n"
"s_branch .Lr7%=\n"
".Lt7%=:\n"
"s_mov_b32 s42, 0\n"
"s_branch .Lr7%=\n"
".Lp9%=:\n"
"s_sub_u32 s42, s42, 1\n"
"s_cbranch_scc1 .Lt9%=\n"
"s_sleep 1\n"
"buffer_load_dword v200, off, s[28:31], s39 sc1\n"
"s_waitcnt vmcnt(0)\n"
"v_add3_u32 v200, v200, s50, -10\n"
"v_cmp_lt_u32 vcc, 64, v200\n"
"s_nop 4\n"
"s_cbranch_vccnz .Lp9%=\n"
"s_branch .Lr9%=\n"
".Lt9%=:\n"
"s_mov_b32 s42, 0\n"
"s_branch .Lr9%=\n"
".Lp10%=:\n"
"s_sub_u32 s42, s42, 1\n"
"s_cbranch_scc1 .Lt10%=\n"
"s_sleep 1\n"
"buffer_load_dword v201, off, s[28:31], s39 sc1\n"
"s_waitcnt vmcnt(0)\n"
"v_add3_u32 v201, v201, s50, -11\n"
"v_cmp_lt_u32 vcc, 64, v201\n"
"s_nop 4\n"
"s_cbranch_vccnz .Lp10%=\n"
"s_branch .Lr10%=\n"
".Lt10%=:\n"
"s_mov_b32 s42, 0\n"
"s_branch .Lr10%=\n"
".Lp11%=:\n"
"s_sub_u32 s42, s42, 1\n"
"s_cbranch_scc1 .Lt11%=\n"
"buffer_load_dwordx4 v[52:55], v192, s[28:31], s15 offen sc1\n"
"buffer_load_dwordx2 v[152:153], v193, s[28:31], s15 offen offset:1024 sc1\n"
"s_waitcnt vmcnt(0)\n"
"v_max3_u32 v200, v52, v53, v54\n"
"v_max3_u32 v200, v200, v55, v152\n"
"v_max_u32 v200, v200, v153\n"
"v_cmp_eq_u32 vcc, -1, v200\n"
"s_nop 4\n"
"s_cbranch_vccnz .Lp11%=\n"
"s_branch .Lr11%=\n"
".Lt11%=:\n"
"s_mov_b32 s42, 0\n"
"s_branch .Lr11%=\n"
".Lp12%=:\n"
"s_sub_u32 s42, s42, 1\n"
"s_cbranch_scc1 .Lt12%=\n"
"buffer_load_dwordx4 v[144:147], v192, s[28:31], s17 offen sc1\n"
"buffer_load_dwordx2 v[154:155], v193, s[28:31], s17 offen offset:1024 sc1\n"
"s_waitcnt vmcnt(0)\n"
"v_max3_u32 v200, v144, v145, v146\n"
"v_max3_u32 v200, v200, v147, v154\n"
"v_max_u32 v200, v200, v155\n"
"v_cmp_eq_u32 vcc, -1, v200\n"
"s_nop 4\n"
"s_cbranch_vccnz .Lp12%=\n"
"s_branch .Lr12%=\n"
".Lt12%=:\n"
"s_mov_b32 s42, 0\n"
"s_branch .Lr12%=\n"
".Lp13%=:\n"
"s_sub_u32 s42, s42, 1\n"
"s_cbranch_scc1 .Lt13%=\n"
"buffer_load_dwordx4 v[56:59], v192, s[28:31], s18 offen sc1\n"
"buffer_load_dwordx2 v[60:61], v193, s[28:31], s18 offen offset:1024 sc1\n"
"s_waitcnt vmcnt(0)\n"
"v_max3_u32 v200, v56, v57, v58\n"
"v_max3_u32 v200, v200, v59, v60\n"
"v_max_u32 v200, v200, v61\n"
"v_cmp_eq_u32 vcc, -1, v200\n"
"s_nop 4\n"
"s_cbranch_vccnz .Lp13%=\n"
"s_branch .Lr13%=\n"
".Lt13%=:\n"
"s_mov_b32 s42, 0\n"
"s_branch .Lr13%=\n"
".Lend%=:\n"
      : "+{v31}"(tid), "+{s2}"(bid), "+{s[0:1]}"(ka)
      : "i"((__attribute__((address_space(3))) char*)smem)
      : "memory", "scc", "vcc", VCLOB, ACLOB, SCLOB);
}

extern "C" void kernel_launch(void* const* d_in, const int* in_sizes, int n_in,
                              void* d_out, int out_size, void* d_ws, size_t ws_size,
                              hipStream_t stream) {
  const uint16_t* x = (const uint16_t*)d_in[0];
  const uint16_t* wqkv = (const uint16_t*)d_in[1];
  const uint16_t* wo = (const uint16_t*)d_in[2];
  const uint16_t* rpb = (const uint16_t*)d_in[3];
  dinat_hs<<<256, 256, 0, stream>>>(x, wqkv, wo, rpb, (uint16_t*)d_out, (char*)d_ws + (2 << 20)
  );
}
```

```cpp
#include <hip/hip_runtime.h>
#include <hip/hip_bf16.h>
#include <stdint.h>

#define VCLOB "v0", "v1", "v2", "v3", "v4", "v5", "v6", "v7", "v8", "v9", "v10", "v11", "v12", "v13", "v14", "v15", "v16", "v17", "v18", "v19", "v20", "v21", "v22", "v23", "v24", "v25", "v26", "v27", "v28", "v29", "v30", "v32", "v33", "v34", "v35", "v36", "v37", "v38", "v39", "v40", "v41", "v42", "v43", "v44", "v45", "v46", "v47", "v48", "v49", "v50", "v51", "v52", "v53", "v54", "v55", "v56", "v57", "v58", "v59", "v60", "v61", "v62", "v63", "v64", "v65", "v66", "v67", "v68", "v69", "v70", "v71", "v72", "v73", "v74", "v75", "v76", "v77", "v78", "v79", "v80", "v81", "v82", "v83", "v84", "v85", "v86", "v87", "v88", "v89", "v90", "v91", "v92", "v93", "v94", "v95", "v96", "v97", "v98", "v99", "v100", "v101", "v102", "v103", "v104", "v105", "v106", "v107", "v108", "v109", "v110", "v111", "v112", "v113", "v114", "v115", "v116", "v117", "v118", "v119", "v120", "v121", "v122", "v123", "v124", "v125", "v126", "v127", "v128", "v129", "v130", "v131", "v132", "v133", "v134", "v135", "v136", "v137", "v138", "v139", "v140", "v141", "v142", "v143", "v144", "v145", "v146", "v147", "v148", "v149", "v150", "v151", "v152", "v153", "v154", "v155", "v156", "v157", "v158", "v159", "v160", "v161", "v162", "v163", "v164", "v165", "v166", "v167", "v168", "v169", "v170", "v171", "v172", "v173", "v174", "v175", "v176", "v177", "v178", "v179", "v180", "v181", "v182", "v183", "v184", "v185", "v186", "v187", "v188", "v189", "v190", "v191", "v192", "v193", "v194", "v195", "v196", "v197", "v198", "v199", "v200", "v201", "v202", "v203", "v204", "v205", "v206", "v207", "v208", "v209", "v210", "v211", "v212", "v213", "v214", "v215", "v216", "v217", "v218", "v219", "v220", "v221", "v222", "v223", "v224", "v225", "v226", "v227", "v228", "v229", "v230", "v231", "v232", "v233", "v234", "v235", "v236", "v237", "v238", "v239", "v240", "v241", "v242", "v243", "v244", "v245", "v246", "v247", "v248", "v249", "v250", "v251", "v252", "v253", "v254", "v255"
#define ACLOB "a0", "a1", "a2", "a3", "a4", "a5", "a6", "a7", "a8", "a9", "a10", "a11", "a12", "a13", "a14", "a15", "a16", "a17", "a18", "a19", "a20", "a21", "a22", "a23", "a24", "a25", "a26", "a27", "a28", "a29", "a30", "a31", "a32", "a33", "a34", "a35", "a36", "a37", "a38", "a39", "a40", "a41", "a42", "a43", "a44", "a45", "a46", "a47", "a48", "a49", "a50", "a51", "a52", "a53", "a54", "a55", "a56", "a57", "a58", "a59", "a60", "a61", "a62", "a63", "a64", "a65", "a66", "a67", "a68", "a69", "a70", "a71", "a72", "a73", "a74", "a75", "a76", "a77", "a78", "a79", "a80", "a81", "a82", "a83", "a84", "a85", "a86", "a87", "a88", "a89", "a90", "a91", "a92", "a93", "a94", "a95", "a96", "a97", "a98", "a99", "a100", "a101", "a102", "a103", "a104", "a105", "a106", "a107", "a108", "a109", "a110", "a111", "a112", "a113", "a114", "a115", "a116", "a117", "a118", "a119", "a120", "a121", "a122", "a123", "a124", "a125", "a126", "a127", "a128", "a129", "a130", "a131", "a132", "a133", "a134", "a135", "a136", "a137", "a138", "a139", "a140", "a141", "a142", "a143", "a144", "a145", "a146", "a147", "a148", "a149", "a150", "a151", "a152", "a153", "a154", "a155", "a156", "a157", "a158", "a159", "a160", "a161", "a162", "a163", "a164", "a165", "a166", "a167", "a168", "a169", "a170", "a171", "a172", "a173", "a174", "a175", "a176", "a177", "a178", "a179", "a180", "a181", "a182", "a183", "a184", "a185", "a186", "a187", "a188", "a189", "a190", "a191", "a192", "a193", "a194", "a195", "a196", "a197", "a198", "a199", "a200", "a201", "a202", "a203", "a204", "a205", "a206", "a207", "a208", "a209", "a210", "a211", "a212", "a213", "a214", "a215", "a216", "a217", "a218", "a219", "a220", "a221", "a222", "a223", "a224", "a225", "a226", "a227", "a228", "a229", "a230", "a231", "a232", "a233", "a234", "a235", "a236", "a237", "a238", "a239", "a240", "a241", "a242", "a243", "a244", "a245", "a246", "a247", "a248", "a249", "a250", "a251", "a252", "a253", "a254", "a255"
#define SCLOB "s3", "s4", "s5", "s6", "s7", "s8", "s9", "s10", "s11", "s12", "s13", "s14", "s15", "s16", "s17", "s18", "s19", "s20", "s21", "s22", "s23", "s24", "s25", "s26", "s27", "s28", "s29", "s30", "s31", "s32", "s33", "s34", "s35", "s36", "s37", "s38", "s39", "s40", "s41", "s42", "s43", "s44", "s45", "s46", "s47", "s48", "s49", "s50", "s51", "s52", "s53", "s54", "s55", "s56", "s57", "s58", "s59", "s60", "s61", "s62", "s63", "s64", "s65", "s66", "s67", "s68", "s69", "s70", "s71", "s72", "s73", "s74", "s75", "s76", "s77", "s78", "s79", "s80", "s81", "s82", "s83", "s84", "s85", "s86", "s87", "s88", "s89", "s90", "s91", "s92", "s93", "s94", "s95", "s96", "s97", "s98", "s99", "s100", "s101"
#define LDS_BYTES 163392

__global__ __launch_bounds__(256, 1) __attribute__((aligned(4096))) void dinat_hs(const uint16_t* __restrict__ x, const uint16_t* __restrict__ wqkv,
                                                   const uint16_t* __restrict__ wo, const uint16_t* __restrict__ rpb,
                                                   uint16_t* __restrict__ out, char* __restrict__ ws
                                                   ) {
  __shared__ __attribute__((aligned(16))) char smem[LDS_BYTES];
  unsigned tid = threadIdx.x, bid = blockIdx.x;
  auto ka = __builtin_amdgcn_kernarg_segment_ptr();
  asm volatile(
"s_load_dwordx2 s[8:9], s[0:1], 0\n"
"s_load_dwordx2 s[12:13], s[0:1], 8\n"
"s_load_dwordx2 s[16:17], s[0:1], 16\n"
"s_load_dwordx2 s[20:21], s[0:1], 24\n"
"s_load_dwordx2 s[24:25], s[0:1], 32\n"
"s_load_dwordx2 s[28:29], s[0:1], 40\n"
"s_getpc_b64 s[4:5]\n"
".Lpc%=:\n"
"s_and_b32 s56, s2, 1\n"
"s_mul_i32 s55, s56, 112\n"
"s_bfe_u32 s53, s2, 0x20005\n"
"s_lshr_b32 s54, s2, 7\n"
"s_sub_u32 s55, s55, 1\n"
"s_mul_i32 s55, s55, s54\n"
"s_mul_i32 s71, s56, -56\n"
"s_mul_i32 s59, s53, 27\n"
"v_lshrrev_b32 v113, 6, v31\n"
"s_add_u32 s55, s55, s71\n"
"s_max_u32 s59, s59, 1\n"
"s_nop 0\n"
"v_readfirstlane_b32 s51, v113\n"
"s_add_u32 s55, s55, 56\n"
"s_sub_u32 s59, s59, 1\n"
"v_and_b32 v112, 63, v31\n"
"s_mul_i32 s58, s55, 172032\n"
"s_mul_i32 s72, s59, 768\n"
"s_lshr_b32 s71, s2, 3\n"
"s_add_u32 s58, s58, s72\n"
"s_and_b32 s73, s2, 4\n"
"s_lshl2_add_u32 s71, s71, s51\n"
"v_min_u32 v121, 2, v112\n"
"s_mul_i32 s73, s73, 21504\n"
"s_and_b32 s74, s2, 2\n"
"s_mul_i32 s72, s71, 384\n"
"v_lshlrev_b32 v121, 7, v121\n"
"s_add_u32 s58, s58, s73\n"
"s_mul_i32 s74, s74, 192\n"
"v_add_u32 v121, s72, v121\n"
"s_bfe_u32 s52, s2, 0x20003\n"
"s_mul_i32 s57, s54, -344064\n"
"s_add_u32 s58, s58, s74\n"
"v_lshlrev_b32 v120, 4, v112\n"
"s_mul_i32 s85, s56, 3\n"
"v_min_u32 v121, .Lend%=-.Lpc%=-4, v121\n"
"s_add_u32 s57, s57, 172032\n"
"s_lshl_b32 s61, s51, 3\n"
"v_lshrrev_b32 v114, 3, v112\n"
"v_xor_b32 v115, v120, v112\n"
"s_sub_u32 s86, s85, 3\n"
"s_mov_b32 m0, 163136\n"
"s_mul_i32 s73, s52, 338\n"
"global_load_lds_dword v121, s[4:5]\n"
"s_and_b32 s74, s51, 1\n"
"s_lshr_b32 s84, s2, 5\n"
"v_and_b32 v115, 0x70, v115\n"
"v_add_u32 v114, s61, v114\n"
"s_mul_i32 s86, s86, s57\n"
"s_and_b32 s73, s73, -4\n"
"s_mul_i32 s74, s74, 88\n"
"v_mul_u32_u24 v123, 0x2aab, v112\n"
"v_add_u32 v124, 64, v112\n"
"s_mov_b32 s22, 1352\n"
"s_mov_b32 s23, 0x20000\n"
"s_add_u32 s84, s84, 0\n"
"v_xor_b32 v116, 64, v115\n"
"v_mul_u32_u24 v117, 0x300, v114\n"
"s_add_u32 s86, s86, s58\n"
"v_max_u32 v118, s85, v114\n"
"s_add_u32 s73, s73, s74\n"
"v_lshlrev_b32 v122, 2, v112\n"
"v_lshrrev_b32 v123, 16, v123\n"
"v_mul_u32_u24 v124, 0x2aab, v124\n"
"s_and_b32 s65, s51, 1\n"
"s_mul_i32 s84, s84, 2\n"
"v_add3_u32 v119, v117, s86, v116\n"
"v_lshlrev_b32 v126, 1, v123\n"
"v_lshrrev_b32 v124, 16, v124\n"
"s_lshr_b32 s66, s51, 1\n"
"s_mul_i32 s70, s52, 96\n"
"s_mul_i32 s85, s65, 384\n"
"s_lshr_b32 s75, 0x9249200, s84\n"
"v_and_b32 v126, 24, v126\n"
"v_lshlrev_b32 v127, 1, v124\n"
"s_add_u32 s85, s85, s70\n"
"s_mul_i32 s86, s66, 36864\n"
"s_movk_i32 s69, 1152\n"
"s_and_b32 s75, s75, 0x180\n"
"v_and_or_b32 v126, v123, 3, v126\n"
"v_and_b32 v127, 24, v127\n"
"v_add_u32 v125, 128, v112\n"
"s_add_u32 s85, s85, s86\n"
"v_mad_u32_u24 v126, v126, s69, v120\n"
"v_mul_u32_u24 v27, 96, v123\n"
"v_and_or_b32 v127, v124, 3, v127\n"
"v_lshrrev_b32 v128, 2, v124\n"
"v_mul_u32_u24 v125, 0x2aab, v125\n"
"s_add_u32 s85, s85, 384\n"
"s_mul_i32 s65, s65, 3072\n"
"s_mul_i32 s71, s66, 6752\n"
"s_mov_b32 s14, 221184\n"
"s_mov_b32 s15, 0x20000\n"
"v_sub_u32 v27, v126, v27\n"
"v_and_or_b32 v127, v128, 4, v127\n"
"v_lshrrev_b32 v125, 16, v125\n"
"s_add_u32 s71, s71, s65\n"
"v_mad_u32_u24 v127, v127, s69, v120\n"
"v_mul_u32_u24 v28, 96, v124\n"
"v_lshlrev_b32 v129, 1, v125\n"
"v_sub_u32 v28, v127, v28\n"
"v_and_b32 v129, 24, v129\n"
"v_add_u32 v28, 1024, v28\n"
"v_and_or_b32 v129, v125, 3, v129\n"
"v_mad_u32_u24 v129, v129, s69, v120\n"
"v_mul_u32_u24 v29, 96, v125\n"
"s_sub_u32 s64, 111, s59\n"
"v_sub_u32 v29, v129, v29\n"
"s_min_u32 s64, s64, 32\n"
"v_add_u32 v29, 6656, v29\n"
"s_mul_i32 s64, s64, 768\n"
"v_add_u32 v118, -3, v118\n"
"s_add_u32 s64, s64, s58\n"
"s_mul_i32 s50, s51, 6752\n"
"v_mul_i32_i24 v118, s57, v118\n"
"s_mov_b32 s10, 19267584\n"
"s_mov_b32 s11, 0x20000\n"
"s_add_u32 s50, s50, 27008\n"
"v_add3_u32 v118, v118, s64, v115\n"
"s_lshr_b32 s76, 0x12492480, s84\n"
"s_and_b32 s76, s76, 0x180\n"
"s_lshr_b32 s77, 0x4924900, s84\n"
"s_and_b32 s77, s77, 0x180\n"
"s_lshl_b32 s72, s56, 3\n"
"s_sub_u32 s72, s72, 2\n"
"s_waitcnt lgkmcnt(0)\n"
"s_add_u32 m0, s74, 162768\n"
"s_mul_i32 s72, s72, s57\n"
"buffer_load_dword v122, s[20:23], s73 offen lds\n"
"s_mul_i32 s73, s75, 576\n"
"s_add_u32 s73, s73, s85\n"
"s_add_u32 m0, s71, 0\n"
"s_mul_i32 s34, s51, 12288\n"
"buffer_load_dwordx4 v27, s[12:15], s73 offen lds\n"
"s_add_u32 s72, s72, s58\n"
"s_add_u32 s34, s34, 81024\n"
"v_add3_u32 v130, v117, s72, v115\n"
"s_add_u32 s74, s72, s57\n"
"v_add3_u32 v131, v117, s74, v116\n"
"s_and_b32 s60, s2, 7\n"
"s_add_u32 m0, s71, 1024\n"
"s_lshr_b32 s84, s2, 5\n"
"s_lshl3_add_u32 s60, s60, s84\n"
"s_mul_i32 s84, s76, 576\n"
"s_add_u32 s84, s84, s85\n"
"s_mul_i32 s86, s77, 576\n"
"buffer_load_dwordx4 v28, s[12:15], s73 offen lds\n"
"s_add_u32 s86, s86, s85\n"
"s_mul_i32 s72, s54, 172032\n"
"s_sub_u32 s36, s58, s72\n"
"s_add_u32 s32, s36, s75\n"
"v_add3_u32 v190, v117, s72, v115\n"
"s_sub_u32 s74, 172032, s72\n"
"v_add3_u32 v191, v117, s74, v116\n"
"v_and_b32 v134, 3, v112\n"
"v_lshrrev_b32 v137, 1, v112\n"
"s_add_u32 m0, s71, 2048\n"
"v_and_or_b32 v137, v137, 4, v134\n"
"v_bfe_u32 v136, v112, 2, 1\n"
"v_lshrrev_b32 v140, 1, v137\n"
"v_min_u32 v96, 0xdc, v31\n"
"s_add_u32 s0, s36, s76\n"
"buffer_load_dwordx4 v29, s[12:15], s73 offen lds\n"
"v_lshrrev_b32 v132, 4, v112\n"
"v_lshl_add_u32 v85, v136, 2, v140\n"
"v_mul_u32_u24 v97, 0xf1, v96\n"
"s_mul_i32 s85, s54, -52\n"
"v_xor_b32 v85, v85, v132\n"
"v_lshrrev_b32 v97, 12, v97\n"
"s_sub_u32 s85, s85, 8\n"
"v_lshlrev_b32 v85, 4, v85\n"
"s_add_u32 m0, s50, 0\n"
"v_lshlrev_b32 v30, 3, v96\n"
"v_mul_i32_i24 v97, s85, v97\n"
"v_lshl_add_u32 v85, v137, 7, v85\n"
"v_mul_u32_u24 v25, 0xc00, v136\n"
"v_lshl_add_u32 v96, v96, 1, v97\n"
"buffer_load_dwordx4 v118, s[8:11], s75 offen lds\n"
"v_add_u32 v25, v25, v85\n"
"v_add_u32 v188, s34, v25\n"
"v_xad_u32 v189, v25, 64, s34\n"
"s_add_u32 s1, s36, s77\n"
"s_mul_i32 s35, s51, 6144\n"
"s_lshl_b32 s33, s57, 1\n"
"s_add_u32 s35, s35, 130176\n"
"s_add_u32 m0, s50, 3072\n"
"v_lshrrev_b32 v84, 2, v112\n"
"v_mul_u32_u24 v84, 96, v84\n"
"v_lshl_add_u32 v23, v134, 3, v84\n"
"v_xor_b32 v69, 64, v25\n"
"v_add_u32 v30, 160896, v30\n"
"buffer_load_dwordx4 v119, s[8:11], s75 offen lds\n"
"v_add_u32 v72, 54016, v23\n"
"v_add_u32 v68, s50, v25\n"
"v_add_u32 v69, s50, v69\n"
"s_mul_i32 s3, s65, 12\n"
"s_add_u32 s70, s70, s3\n"
"s_mul_i32 s81, s77, 576\n"
"s_add_u32 s81, s81, s70\n"
"s_add_u32 m0, s50, 1024\n"
"s_sub_u32 s3, s76, s75\n"
"s_mul_i32 s3, s3, s66\n"
"s_add_u32 s3, s3, s75\n"
"s_mul_i32 s41, s56, -60768\n"
"s_mul_i32 s43, s66, 6752\n"
"buffer_load_dwordx4 v118, s[8:11], s76 offen lds\n"
"s_mul_i32 s3, s3, 576\n"
"s_add_u32 s41, s41, 60768\n"
"s_add_u32 s43, s43, s65\n"
"s_add_u32 s3, s3, s70\n"
"s_add_u32 s43, s43, s41\n"
"s_mov_b32 s42, 0x80000\n"
"v_add_u32 v142, s61, v137\n"
"v_bfe_u32 v86, v142, 1, 1\n"
"v_mul_u32_u24 v87, 43, v142\n"
"s_add_u32 m0, s50, 4096\n"
"s_movk_i32 s68, 6752\n"
"v_and_b32 v133, 48, v112\n"
"v_xor_b32 v86, v86, v132\n"
"v_lshrrev_b32 v87, 9, v87\n"
"s_movk_i32 s67, 192\n"
"buffer_load_dwordx4 v119, s[8:11], s76 offen lds\n"
"v_lshlrev_b32 v86, 4, v86\n"
"v_mul_u32_u24 v87, 80832, v87\n"
"v_mad_u32_u24 v21, v142, s68, v133\n"
"v_mad_u32_u24 v141, v142, s67, v86\n"
"v_sub_u32 v21, v21, v87\n"
"v_and_b32 v88, 1, v112\n"
"s_mul_i32 s48, s56, 13504\n"
"v_add_u32 v21, 6144, v21\n"
"s_add_u32 m0, s50, 2048\n"
"v_add_u32 v187, 74272, v141\n"
"v_add_u32 v198, 60768, v141\n"
"v_mad_u32_u24 v88, v88, s68, v133\n"
"s_mov_b32 s6, 0xf0f0f0f0\n"
"s_mov_b32 s7, 0xf0f0f0f0\n"
"buffer_load_dwordx4 v118, s[8:11], s77 offen lds\n"
"s_mov_b64 exec, s[6:7]\n"
"v_add_u32 v21, s48, v141\n"
"v_mov_b32 v187, v141\n"
"v_add_u32 v198, 60544, v88\n"
"s_mov_b64 exec, -1\n"
"s_lshl_b32 s85, s66, 10\n"
"v_add_u32 v192, 99456, v120\n"
"v_bfe_u32 v138, v112, 4, 1\n"
"v_lshrrev_b32 v139, 3, v112\n"
"s_add_u32 m0, s50, 5120\n"
"s_add_u32 s62, s59, s61\n"
"v_and_b32 v139, 4, v139\n"
"v_sub_u32 v94, v136, v138\n"
"s_sub_u32 s63, 108, s62\n"
"s_mul_i32 s49, s56, 272\n"
"buffer_load_dwordx4 v119, s[8:11], s77 offen lds\n"
"v_mul_i32_i24 v94, 0x88, v94\n"
"v_sub_u32 v95, v137, v139\n"
"s_sub_u32 s62, 3, s62\n"
"v_add_u32 v150, 1, v139\n"
"v_add_u32 v151, 2, v139\n"
"v_add_u32 v152, 3, v139\n"
"v_lshl_add_u32 v94, v95, 3, v94\n"
"v_max_i32 v149, s62, v139\n"
"v_max_i32 v150, s62, v150\n"
"s_add_u32 m0, s34, 6144\n"
"v_max_i32 v151, s62, v151\n"
"v_max_i32 v152, s62, v152\n"
"v_mov_b32 v153, 0x7f\n"
"v_min_i32 v149, s63, v149\n"
"v_min_i32 v150, s63, v150\n"
"buffer_load_dwordx4 v130, s[8:11], s75 offen lds\n"
"v_min_i32 v151, s63, v151\n"
"v_min_i32 v152, s63, v152\n"
"v_lshlrev_b32 v149, v149, v153\n"
"v_lshlrev_b32 v150, v150, v153\n"
"v_lshlrev_b32 v151, v151, v153\n"
"v_lshlrev_b32 v152, v152, v153\n"
"s_sub_u32 s74, 3, s61\n"
"s_add_u32 m0, s34, 9216\n"
"v_lshrrev_b32 v149, v137, v149\n"
"v_lshrrev_b32 v150, v137, v150\n"
"v_lshrrev_b32 v151, v137, v151\n"
"v_lshrrev_b32 v152, v137, v152\n"
"s_max_i32 s74, s74, s62\n"
"buffer_load_dwordx4 v131, s[8:11], s75 offen lds\n"
"v_bfe_i32 v98, v149, 0, 1\n"
"v_bfe_i32 v99, v150, 0, 1\n"
"v_bfe_i32 v102, v149, 8, 1\n"
"v_bfe_i32 v103, v150, 8, 1\n"
"v_sub_u32 v61, v136, v138\n"
"s_mov_b32 s72, 0xffff\n"
"v_bfe_i32 v100, v151, 0, 1\n"
"v_bfe_i32 v101, v152, 0, 1\n"
"s_add_u32 m0, s34, 7168\n"
"v_bfe_i32 v56, v151, 8, 1\n"
"v_bfe_i32 v57, v152, 8, 1\n"
"v_max_i32 v58, s74, v137\n"
"v_ashrrev_i32 v149, 31, v61\n"
"v_add_u32 v150, -1, v61\n"
"buffer_load_dwordx4 v130, s[8:11], s76 offen lds\n"
"v_mov_b32 v160, 0x40de\n"
"v_mov_b32 v161, 0\n"
"v_lshlrev_b32 v245, 4, v134\n"
"v_mov_b64 v[156:157], 0\n"
"s_mov_b32 s6, 0x00f0000f\n"
"s_mov_b32 s7, 0xf0000f00\n"
"v_bfi_b32 v0, s72, v98, v99\n"
"v_bfi_b32 v1, s72, v100, v101\n"
"v_bfi_b32 v2, s72, v102, v103\n"
"v_bfi_b32 v3, s72, v56, v57\n"
"s_mul_i32 s74, s51, 1536\n"
"v_and_b32 v251, 15, v112\n"
"v_not_b32 v149, v149\n"
"v_ashrrev_i32 v150, 31, v150\n"
"v_or_b32 v244, s56, v138\n"
"s_mov_b64 exec, s[6:7]\n"
"v_lshlrev_b64 v[156:157], v245, v[160:161]\n"
"s_mov_b64 exec, -1\n"
"s_lshl2_add_u32 s72, s60, s51\n"
"v_mul_u32_u24 v251, 0x180, v251\n"
"v_and_b32 v252, 7, v112\n"
"v_and_b32 v12, v149, v0\n"
"v_and_b32 v16, v150, v0\n"
"v_and_b32 v13, v149, v1\n"
"v_and_b32 v17, v150, v1\n"
"v_and_b32 v14, v149, v2\n"
"v_and_b32 v18, v150, v2\n"
"v_and_b32 v15, v149, v3\n"
"v_and_b32 v19, v150, v3\n"
"v_sub_u32 v244, v136, v244\n"
"v_mov_b32 v160, 0xffff\n"
"v_mov_b64 v[176:177], 0\n"
"s_mul_i32 s37, s72, 6144\n"
"v_and_b32 v249, 1, v112\n"
"v_add_u32 v251, 99456, v251\n"
"v_lshrrev_b32 v254, 5, v112\n"
"v_ashrrev_i32 v149, 31, v244\n"
"v_add_u32 v150, -1, v244\n"
"s_mov_b64 exec, s[6:7]\n"
"v_lshlrev_b64 v[176:177], v245, v[160:161]\n"
"s_mov_b64 exec, -1\n"
"s_lshl_b32 s72, s51, 6\n"
"v_lshlrev_b32 v255, 3, v134\n"
"v_lshl_add_u32 v84, v23, 2, s72\n"
"v_sub_u32 v84, v84, v255\n"
"v_sub_u32 v84, v84, v255\n"
"v_add_u32 v236, 130176, v84\n"
"v_sub_u32 v85, v236, v255\n"
"s_lshl_b32 s73, s48, 2\n"
"v_mad_u32_u24 v186, v136, s68, v141\n"
"s_add_u32 s73, s73, 6752\n"
"v_add_u32 v22, s73, v186\n"
"v_add_u32 v70, s35, v25\n"
"v_xor_b32 v71, 64, v25\n"
"v_add_u32 v71, s35, v71\n"
"s_sub_u32 s73, 28, s61\n"
"s_min_i32 s73, s73, s63\n"
"v_min_i32 v58, s73, v58\n"
"v_lshlrev_b32 v58, v58, -1\n"
"v_lshrrev_b32 v58, v139, v58\n"
"v_not_b32 v58, v58\n"
"v_and_b32 v59, 5, v58\n"
"v_and_b32 v60, 10, v58\n"
"v_lshlrev_b32 v59, 1, v59\n"
"v_lshl_or_b32 v59, v60, 4, v59\n"
"v_or_b32 v59, 0x44, v59\n"
"s_mov_b64 exec, 0\n"
"s_cmp_eq_u32 s51, 0\n"
"s_cbranch_scc1 .Lic0%=\n"
"s_cmp_eq_u32 s51, 1\n"
"s_cbranch_scc1 .Lic1%=\n"
"s_cmp_eq_u32 s51, 2\n"
"s_cbranch_scc1 .Lic2%=\n"
"s_branch .Lic3%=\n"
".Lib0%=:\n"
"s_mov_b64 exec, -1\n"
"v_lshl_or_b32 v63, v59, 8, v59\n"
"s_mov_b32 s30, 23724032\n"
"s_mov_b32 s31, 0x20000\n"
"v_and_b32 v146, 8, v112\n"
"v_lshl_add_u32 v146, v136, 1, v146\n"
"v_add3_u32 v146, v146, s61, 1\n"
"v_lshrrev_b32 v145, 5, v112\n"
"s_add_u32 m0, s34, 10240\n"
"v_max_u32 v144, 3, v142\n"
"buffer_load_dwordx4 v131, s[8:11], s76 offen lds\n"
"v_mul_u32_u24 v145, s68, v145\n"
"v_bfe_u32 v92, v146, 1, 1\n"
"v_add_u32 v144, -3, v144\n"
"v_add_u32 v145, -768, v145\n"
"v_xor_b32 v92, v92, v134\n"
"v_mov_b32 v143, s48\n"
"v_bfe_u32 v90, v144, 1, 1\n"
"v_lshl_add_u32 v92, v92, 4, v145\n"
"v_mad_u32_u24 v143, v136, s68, v143\n"
"v_xor_b32 v90, v90, v132\n"
"v_cmp_eq_u32 s[46:47], 35, v146\n"
"s_add_u32 m0, s34, 8192\n"
"v_mad_u32_u24 v184, v146, s67, v92\n"
"v_lshl_add_u32 v90, v90, 4, v143\n"
"v_add_u32 v146, 1, v146\n"
"buffer_load_dwordx4 v130, s[8:11], s77 offen lds\n"
"v_mad_u32_u24 v178, v144, s67, v90\n"
"v_bfe_u32 v93, v146, 1, 1\n"
"v_add_u32 v144, 5, v142\n"
"v_xor_b32 v93, v93, v134\n"
"v_min_u32 v144, 32, v144\n"
"v_mul_u32_u24 v147, 13504, v138\n"
"v_lshl_add_u32 v93, v93, 4, v145\n"
"v_bfe_u32 v91, v144, 1, 1\n"
"v_add_u32 v147, s48, v147\n"
"s_add_u32 m0, s34, 11264\n"
"v_mul_i32_i24 v148, -67520, v138\n"
"s_waitcnt vmcnt(11)\n"
"s_barrier\n"
"buffer_load_dwordx4 v131, s[8:11], s77 offen lds\n"
"ds_read_b64_tr_b16 a[0:1], v23\n"
"ds_read_b64_tr_b16 a[2:3], v23 offset:1536\n"
"ds_read_b64_tr_b16 a[4:5], v23 offset:32\n"
"ds_read_b64_tr_b16 a[6:7], v23 offset:1568\n"
"ds_read_b64_tr_b16 a[8:9], v23 offset:64\n"
"v_mad_u32_u24 v185, v146, s67, v93\n"
"ds_read_b64_tr_b16 a[10:11], v23 offset:1600\n"
"s_add_u32 m0, s71, 13504\n"
"ds_read_b64_tr_b16 a[72:73], v23 offset:3072\n"
"ds_read_b64_tr_b16 a[74:75], v23 offset:4608\n"
"ds_read_b64_tr_b16 a[76:77], v23 offset:3104\n"
"v_bfe_u32 v135, v112, 2, 2\n"
"ds_read_b64_tr_b16 a[78:79], v23 offset:4640\n"
"buffer_load_dwordx4 v27, s[12:15], s84 offen lds\n"
"ds_read_b64_tr_b16 a[80:81], v23 offset:3136\n"
"ds_read_b64_tr_b16 a[82:83], v23 offset:4672\n"
"ds_read_b64_tr_b16 a[12:13], v23 offset:6752\n"
"v_xor_b32 v91, v91, v132\n"
"ds_read_b64_tr_b16 a[14:15], v23 offset:8288\n"
"ds_read_b64_tr_b16 a[16:17], v23 offset:6784\n"
"ds_read_b64_tr_b16 a[18:19], v23 offset:8320\n"
"s_add_u32 m0, s71, 14528\n"
"s_waitcnt vmcnt(11)\n"
"ds_read_b128 a[144:147], v68\n"
"v_add_u32 v148, 67520, v148\n"
"v_add_u32 v183, v185, v147\n"
"ds_read_b128 a[148:151], v69\n"
"buffer_load_dwordx4 v28, s[12:15], s84 offen lds\n"
"ds_read_b64_tr_b16 a[20:21], v23 offset:6816\n"
"v_add_u32 v185, v185, v148\n"
"ds_read_b64_tr_b16 a[22:23], v23 offset:8352\n"
"ds_read_b64_tr_b16 a[84:85], v23 offset:9824\n"
"ds_read_b64_tr_b16 a[86:87], v23 offset:11360\n"
"s_mov_b64 exec, s[46:47]\n"
"v_mad_u32_u24 v185, v138, s67, v185\n"
"s_mov_b64 exec, -1\n"
"s_add_u32 m0, s71, 15552\n"
"ds_read_b64_tr_b16 a[88:89], v23 offset:9856\n"
"ds_read_b64_tr_b16 a[90:91], v23 offset:11392\n"
"ds_read_b64_tr_b16 a[92:93], v23 offset:9888\n"
"ds_read_b64_tr_b16 a[94:95], v23 offset:11424\n"
"buffer_load_dwordx4 v29, s[12:15], s84 offen lds\n"
"v_lshl_add_u32 v91, v91, 4, v143\n"
"v_mad_u32_u24 v179, v144, s67, v91\n"
"v_cmp_eq_u32 s[44:45], 32, v144\n"
"v_add_u32 v182, v184, v147\n"
"v_not_b32 v149, v149\n"
"v_add_u32 v184, v184, v148\n"
"v_and_b32 v4, v149, v0\n"
"v_and_b32 v5, v149, v1\n"
"v_and_b32 v6, v149, v2\n"
"v_and_b32 v7, v149, v3\n"
"v_ashrrev_i32 v150, 31, v150\n"
"v_and_b32 v8, v150, v0\n"
"v_and_b32 v9, v150, v1\n"
"v_and_b32 v10, v150, v2\n"
"v_and_b32 v11, v150, v3\n"
"v_lshrrev_b32 v87, 2, v112\n"
"v_sub_u32 v88, 11, v112\n"
"v_and_b32 v88, 0x80000000, v88\n"
"v_and_b32 v89, 1, v114\n"
"v_add_u32 v89, 29, v89\n"
"v_mul_i32_i24 v89, s57, v89\n"
"v_lshlrev_b32 v193, 3, v112\n"
"v_add3_u32 v199, v89, s64, v116\n"
"v_add_u32 v193, 99456, v193\n"
"v_mov_b32 v26, 0xf149f149\n"
"v_mov_b64 v[160:161], v[156:157]\n"
"v_add_u32 v180, 40512, v178\n"
"v_add_u32 v181, 40512, v179\n"
"v_mov_b32 v172, 0x3f803f80\n"
"v_mov_b32 v173, v172\n"
"v_mov_b64 v[164:165], v[156:157]\n"
"v_mov_b64 v[168:169], v[156:157]\n"
"v_mov_b64 v[174:175], v[172:173]\n"
"s_waitcnt vmcnt(11) lgkmcnt(9)\n"
"v_mfma_f32_16x16x32_bf16 v[204:207], a[0:3], a[144:147], 0\n"
"s_waitcnt lgkmcnt(8)\n"
"ds_read_b128 a[152:155], v68 offset:1024\n"
"ds_read_b128 a[156:159], v69 offset:1024\n"
"v_mfma_f32_16x16x32_bf16 v[204:207], a[12:15], a[148:151], v[204:207]\n"
"s_add_u32 m0, s71, 60768\n"
"s_add_u32 s84, s49, 161064\n"
"buffer_load_dwordx4 v27, s[12:15], s86 offen lds\n"
"v_mfma_f32_16x16x32_bf16 v[208:211], a[4:7], a[144:147], 0\n"
"s_lshl_b32 s5, s51, 9\n"
"v_add_u32 v20, s84, v94\n"
"s_mov_b32 s70, 0x3e553b95\n"
"v_mfma_f32_16x16x32_bf16 v[208:211], a[16:19], a[148:151], v[208:211]\n"
"s_lshl_b32 s84, s51, 10\n"
"v_add_u32 v202, s84, v192\n"
"s_mul_i32 s20, s33, 3\n"
"v_mfma_f32_16x16x32_bf16 v[212:215], a[8:11], a[144:147], 0\n"
"s_waitcnt lgkmcnt(8)\n"
"s_add_u32 m0, s71, 61792\n"
"s_mul_i32 s21, s33, 5\n"
"v_mfma_f32_16x16x32_bf16 v[212:215], a[20:23], a[148:151], v[212:215]\n"
"s_mov_b32 s84, 0xc0000007\n"
"s_cmp_eq_u32 s53, 0\n"
"s_cselect_b32 s84, 0xe0000000, s84\n"
"s_cmp_eq_u32 s53, 3\n"
"s_cselect_b32 s84, 7, s84\n"
"v_mfma_f32_16x16x32_bf16 v[216:219], a[72:75], a[144:147], 0\n"
"s_waitcnt vmcnt(10) lgkmcnt(6)\n"
"buffer_load_dwordx4 v28, s[12:15], s86 offen lds\n"
"ds_read_b128 a[160:163], v68 offset:2048\n"
"v_mfma_f32_16x16x32_bf16 v[216:219], a[84:87], a[148:151], v[216:219]\n"
"s_add_u32 s5, s5, 4096\n"
"ds_read_b128 a[164:167], v69 offset:2048\n"
"v_add_u32 v68, s41, v23\n"
"v_mfma_f32_16x16x32_bf16 v[220:223], a[76:79], a[144:147], 0\n"
"s_waitcnt lgkmcnt(6)\n"
"v_add_u32 v69, 154752, v23\n"
"v_add_u32 v62, s5, v193\n"
"v_mfma_f32_16x16x32_bf16 v[220:223], a[88:91], a[148:151], v[220:223]\n"
"s_add_u32 m0, s71, 62816\n"
"s_mul_i32 s71, s54, 312\n"
"s_mul_i32 s22, s33, 6\n"
"v_mfma_f32_16x16x32_bf16 v[224:227], a[80:83], a[144:147], 0\n"
"s_waitcnt lgkmcnt(4)\n"
"buffer_load_dwordx4 v29, s[12:15], s86 offen lds\n"
"v_mfma_f32_16x16x32_bf16 v[224:227], a[92:95], a[148:151], v[224:227]\n"
"s_and_b32 s86, s52, 1\n"
"s_lshl1_add_u32 s71, s86, s71\n"
"s_add_u32 s71, s71, 162760\n"
"v_add_u32 v24, s71, v96\n"
"ds_read_u16 v240, v24 offset:6\n"
"ds_read_u16 v241, v24 offset:4\n"
"ds_read_u16 v242, v24 offset:2\n"
"ds_read_u16 v243, v24\n"
"s_and_b32 s86, s51, 1\n"
"s_lshl3_add_u32 s85, s86, s85\n"
"s_add_u32 s85, s85, 4096\n"
"v_add_u32 v203, s85, v192\n"
"s_lshl_b32 s85, s51, 6\n"
"v_add_u32 v81, s85, v112\n"
"v_add_u32 v154, s85, v112\n"
"v_lshrrev_b32 v81, 1, v81\n"
"v_mul_u32_u24 v155, 0xaab, v154\n"
"v_add_u32 v81, 0x100, v81\n"
"v_lshrrev_b32 v155, 16, v155\n"
"v_mul_u32_u24 v82, 0xaab, v81\n"
"v_mul_i32_i24 v80, -24, v155\n"
"v_lshrrev_b32 v82, 16, v82\n"
"v_add_u32 v154, v154, v80\n"
"v_mul_i32_i24 v83, -24, v82\n"
"v_and_b32 v80, 7, v155\n"
"v_add_u32 v81, v81, v83\n"
"v_xor_b32 v154, v154, v80\n"
"v_and_b32 v83, 7, v82\n"
"v_lshrrev_b32 v80, 1, v155\n"
"v_xor_b32 v81, v81, v83\n"
"v_and_b32 v80, 4, v80\n"
"v_lshrrev_b32 v83, 1, v82\n"
"s_lshl_b32 s86, s52, 3\n"
"s_waitcnt lgkmcnt(2)\n"
"v_lshl_or_b32 v240, v241, 16, v240\n"
"v_and_or_b32 v80, v155, 3, v80\n"
"s_lshl2_add_u32 s71, s60, s52\n"
"s_waitcnt lgkmcnt(0)\n"
"v_lshl_or_b32 v241, v243, 16, v242\n"
"ds_write_b64 v30, v[240:241]\n"
"v_and_b32 v83, 4, v83\n"
"v_add_u32 v80, s86, v80\n"
"v_bfe_u32 v247, v155, 2, 1\n"
"s_mul_i32 s38, s71, 6144\n"
"v_and_or_b32 v83, v82, 3, v83\n"
"v_lshrrev_b32 v246, v80, s84\n"
"v_xor_b32 v247, s54, v247\n"
"v_add_u32 v83, s86, v83\n"
"s_lshl_b32 s71, s51, 2\n"
"v_lshlrev_b32 v246, 31, v246\n"
"v_mul_u32_u24 v247, 172032, v247\n"
"v_mul_u32_u24 v80, 0x300, v80\n"
"s_add_u32 s38, s38, s74\n"
"v_lshrrev_b32 v248, v83, s84\n"
"v_bfe_u32 v250, v82, 2, 1\n"
"v_add_u32 v253, s71, v132\n"
"v_add3_u32 v246, v246, v247, v80\n"
"v_lshlrev_b32 v248, 31, v248\n"
"v_xor_b32 v250, s54, v250\n"
"v_xor_b32 v253, v253, v252\n"
"s_lshl1_add_u32 s74, s51, 16\n"
"v_lshl_add_u32 v194, v154, 4, v246\n"
"v_lshl_add_u32 v248, v249, 3, v248\n"
"v_mul_u32_u24 v250, 172032, v250\n"
"v_mul_u32_u24 v83, 0x300, v83\n"
"v_lshl_add_u32 v196, v253, 4, v251\n"
"v_add_u32 v254, s74, v254\n"
"v_add3_u32 v248, v248, v250, v83\n"
"v_lshl_add_u32 v251, v138, 3, v251\n"
"v_xor_b32 v254, v254, v252\n"
"v_lshl_add_u32 v195, v81, 4, v248\n"
"v_lshl_add_u32 v197, v254, 4, v251\n"
"s_add_u32 m0, s34, 0\n"
"s_add_u32 s38, s38, 31616\n"
"buffer_load_dwordx4 v190, s[8:11], s32 offen lds\n"
"s_add_u32 s39, s38, 22119552\n"
"s_and_b32 s85, s52, 1\n"
"s_lshl2_add_u32 s85, s85, s61\n"
"v_add_u32 v86, s85, v134\n"
"v_mul_u32_u24 v86, 0x300, v86\n"
"s_mov_b64 exec, 0\n"
"s_cmp_eq_u32 s51, 0\n"
"s_cbranch_scc1 .Lic4%=\n"
"s_cmp_eq_u32 s51, 1\n"
"s_cbranch_scc1 .Lic5%=\n"
"s_cmp_eq_u32 s51, 2\n"
"s_cbranch_scc1 .Lic6%=\n"
"s_branch .Lic7%=\n"
".Lib1%=:\n"
"s_mov_b64 exec, -1\n"
"s_add_u32 m0, s34, 3072\n"
"s_waitcnt vmcnt(11)\n"
"buffer_load_dwordx4 v191, s[8:11], s32 offen lds\n"
"s_add_u32 s32, s32, s33\n"
"ds_read_b128 a[168:171], v188 offset:6144\n"
"ds_read_b128 a[172:175], v189 offset:6144\n"
"v_lshl_add_u32 v86, v87, 7, v86\n"
"v_or_b32 v238, v88, v86\n"
"s_mov_b32 s71, 0x3e553b95\n"
"s_lshr_b32 s86, s52, 1\n"
"s_mul_i32 s86, s86, 172032\n"
"s_add_u32 s86, s86, s36\n"
"s_mul_i32 s78, s33, 8\n"
"s_add_u32 s78, s78, s86\n"
"s_add_u32 m0, s34, 1024\n"
"s_mul_i32 s73, s52, 1536\n"
"s_add_u32 s37, s37, s73\n"
"s_lshl_b32 s73, s51, 5\n"
"buffer_load_dwordx4 v190, s[8:11], s0 offen lds\n"
"s_sub_u32 s73, 256, s73\n"
"v_add_u32 v235, s73, v85\n"
"s_mov_b32 s18, 73728\n"
"s_add_u32 s37, s37, 31616\n"
"s_mul_i32 s79, s51, 3072\n"
"s_mul_i32 s80, s66, 12288\n"
"s_mov_b32 s26, 19267584\n"
"s_mov_b32 s27, 0x20000\n"
"s_mov_b32 s19, 0x20000\n"
"s_add_u32 s40, s37, 22119552\n"
"s_sub_u32 s16, s16, 99456\n"
"s_subb_u32 s17, s17, 0\n"
"s_add_u32 m0, s34, 4096\n"
"s_waitcnt vmcnt(11) lgkmcnt(1)\n"
"v_mfma_f32_16x16x32_bf16 v[244:247], a[0:3], a[168:171], 0\n"
"s_waitcnt lgkmcnt(0)\n"
"buffer_load_dwordx4 v191, s[8:11], s0 offen lds\n"
"ds_read_b128 a[176:179], v188 offset:7168\n"
"v_mfma_f32_16x16x32_bf16 v[244:247], a[12:15], a[172:175], v[244:247]\n"
"s_add_u32 s0, s0, s33\n"
"ds_read_b128 a[180:183], v189 offset:7168\n"
"v_mfma_f32_16x16x32_bf16 v[248:251], a[4:7], a[168:171], 0\n"
"s_mov_b32 s18, 173184\n"
"s_add_u32 s80, s80, s79\n"
"v_mfma_f32_16x16x32_bf16 v[248:251], a[16:19], a[172:175], v[248:251]\n"
"s_add_u32 m0, s34, 2048\n"
"v_mfma_f32_16x16x32_bf16 v[252:255], a[8:11], a[168:171], 0\n"
"buffer_load_dwordx4 v190, s[8:11], s1 offen lds\n"
"v_mfma_f32_16x16x32_bf16 v[252:255], a[20:23], a[172:175], v[252:255]\n"
"v_mfma_f32_16x16x32_bf16 v[104:107], a[72:75], a[168:171], 0\n"
"v_mfma_f32_16x16x32_bf16 v[104:107], a[84:87], a[172:175], v[104:107]\n"
"s_waitcnt vmcnt(11)\n"
"ds_read_b128 a[184:187], v188 offset:8192\n"
"ds_read_b128 a[188:191], v189 offset:8192\n"
"v_mfma_f32_16x16x32_bf16 v[108:111], a[76:79], a[168:171], 0\n"
"s_add_u32 m0, s34, 5120\n"
"s_nop 0\n"
"buffer_load_dwordx4 v191, s[8:11], s1 offen lds\n"
"v_mfma_f32_16x16x32_bf16 v[108:111], a[88:91], a[172:175], v[108:111]\n"
"s_add_u32 s1, s1, s33\n"
"v_mfma_f32_16x16x32_bf16 v[228:231], a[80:83], a[168:171], 0\n"
"v_mfma_f32_16x16x32_bf16 v[228:231], a[92:95], a[172:175], v[228:231]\n"
"s_add_u32 m0, s35, 0\n"
"s_nop 0\n"
"buffer_load_dwordx4 v190, s[8:11], s32 offen lds\n"
"s_add_u32 m0, s35, 3072\n"
"s_nop 0\n"
"buffer_load_dwordx4 v191, s[8:11], s32 offen lds\n"
"s_add_u32 s32, s32, s33\n"
"s_add_u32 m0, s35, 1024\n"
"s_waitcnt vmcnt(11)\n"
"s_barrier\n"
"ds_read_b64_tr_b16 a[24:25], v23 offset:13504\n"
"buffer_load_dwordx4 v190, s[8:11], s0 offen lds\n"
"ds_read_b64_tr_b16 a[26:27], v23 offset:15040\n"
"ds_read_b64_tr_b16 a[28:29], v23 offset:13536\n"
"ds_read_b64_tr_b16 a[30:31], v23 offset:15072\n"
"ds_read_b64_tr_b16 a[32:33], v23 offset:13568\n"
"ds_read_b64_tr_b16 a[34:35], v23 offset:15104\n"
"ds_read_b64_tr_b16 a[96:97], v23 offset:16576\n"
"ds_read_b64_tr_b16 a[98:99], v23 offset:18112\n"
"ds_read_b64_tr_b16 a[100:101], v23 offset:16608\n"
"ds_read_b64_tr_b16 a[102:103], v23 offset:18144\n"
"s_add_u32 m0, s35, 4096\n"
"ds_read_b64_tr_b16 a[104:105], v23 offset:16640\n"
"buffer_load_dwordx4 v191, s[8:11], s0 offen lds\n"
"ds_read_b64_tr_b16 a[106:107], v23 offset:18176\n"
"ds_read_b64_tr_b16 a[36:37], v23 offset:20256\n"
"s_add_u32 s0, s0, s33\n"
"ds_read_b64_tr_b16 a[38:39], v23 offset:21792\n"
"ds_read_b64_tr_b16 a[40:41], v23 offset:20288\n"
"ds_read_b64_tr_b16 a[42:43], v23 offset:21824\n"
"ds_read_b64_tr_b16 a[44:45], v23 offset:20320\n"
"s_waitcnt lgkmcnt(15)\n"
"v_mfma_f32_16x16x32_bf16 v[204:207], a[24:27], a[152:155], v[204:207]\n"
"s_waitcnt lgkmcnt(13)\n"
"ds_read_b64_tr_b16 a[46:47], v23 offset:21856\n"
"ds_read_b64_tr_b16 a[108:109], v23 offset:23328\n"
"v_mfma_f32_16x16x32_bf16 v[208:211], a[28:31], a[152:155], v[208:211]\n"
"s_waitcnt lgkmcnt(13)\n"
"ds_read_b64_tr_b16 a[110:111], v23 offset:24864\n"
"ds_read_b64_tr_b16 a[112:113], v23 offset:23360\n"
"v_mfma_f32_16x16x32_bf16 v[212:215], a[32:35], a[152:155], v[212:215]\n"
"s_waitcnt lgkmcnt(13)\n"
"ds_read_b64_tr_b16 a[114:115], v23 offset:24896\n"
"ds_read_b64_tr_b16 a[116:117], v23 offset:23392\n"
"v_mfma_f32_16x16x32_bf16 v[216:219], a[96:99], a[152:155], v[216:219]\n"
"s_waitcnt lgkmcnt(13)\n"
"ds_read_b64_tr_b16 a[118:119], v23 offset:24928\n"
"v_mfma_f32_16x16x32_bf16 v[220:223], a[100:103], a[152:155], v[220:223]\n"
"s_waitcnt vmcnt(10) lgkmcnt(12)\n"
"s_add_u32 m0, s35, 2048\n"
"v_mfma_f32_16x16x32_bf16 v[224:227], a[104:107], a[152:155], v[224:227]\n"
"s_waitcnt lgkmcnt(10)\n"
"buffer_load_dwordx4 v190, s[8:11], s1 offen lds\n"
"v_mfma_f32_16x16x32_bf16 v[204:207], a[36:39], a[156:159], v[204:207]\n"
"s_waitcnt lgkmcnt(8)\n"
"s_barrier\n"
"v_mfma_f32_16x16x32_bf16 v[208:211], a[40:43], a[156:159], v[208:211]\n"
"s_waitcnt lgkmcnt(6)\n"
"ds_read_b64_tr_b16 a[48:49], v23 offset:60768\n"
"ds_read_b64_tr_b16 a[50:51], v23 offset:62304\n"
"v_mfma_f32_16x16x32_bf16 v[212:215], a[44:47], a[156:159], v[212:215]\n"
"s_waitcnt lgkmcnt(6)\n"
"ds_read_b64_tr_b16 a[52:53], v23 offset:60800\n"
"ds_read_b64_tr_b16 a[54:55], v23 offset:62336\n"
"v_mfma_f32_16x16x32_bf16 v[216:219], a[108:111], a[156:159], v[216:219]\n"
"s_waitcnt lgkmcnt(6)\n"
"ds_read_b64_tr_b16 a[56:57], v23 offset:60832\n"
"ds_read_b64_tr_b16 a[58:59], v23 offset:62368\n"
"v_mfma_f32_16x16x32_bf16 v[220:223], a[112:115], a[156:159], v[220:223]\n"
"s_waitcnt lgkmcnt(6)\n"
"ds_read_b64_tr_b16 a[120:121], v72 offset:9824\n"
"ds_read_b64_tr_b16 a[122:123], v72 offset:11360\n"
"v_mfma_f32_16x16x32_bf16 v[224:227], a[116:119], a[156:159], v[224:227]\n"
"s_add_u32 m0, s35, 5120\n"
"ds_read_b64_tr_b16 a[124:125], v72 offset:9856\n"
"buffer_load_dwordx4 v191, s[8:11], s1 offen lds\n"
"v_mfma_f32_16x16x32_bf16 v[244:247], a[24:27], a[176:179], v[244:247]\n"
"s_add_u32 s1, s1, s33\n"
"ds_read_b64_tr_b16 a[126:127], v72 offset:11392\n"
"ds_read_b64_tr_b16 a[128:129], v72 offset:9888\n"
"v_mfma_f32_16x16x32_bf16 v[244:247], a[36:39], a[180:183], v[244:247]\n"
"ds_read_b64_tr_b16 a[130:131], v72 offset:11424\n"
"ds_read_b64_tr_b16 a[60:61], v72 offset:13504\n"
"v_mfma_f32_16x16x32_bf16 v[248:251], a[28:31], a[176:179], v[248:251]\n"
"s_waitcnt lgkmcnt(11)\n"
"ds_read_b64_tr_b16 a[62:63], v72 offset:15040\n"
"ds_read_b64_tr_b16 a[64:65], v72 offset:13536\n"
"v_mfma_f32_16x16x32_bf16 v[204:207], a[48:51], a[160:163], v[204:207]\n"
"s_waitcnt lgkmcnt(11)\n"
"ds_read_b64_tr_b16 a[66:67], v72 offset:15072\n"
"ds_read_b64_tr_b16 a[68:69], v72 offset:13568\n"
"v_mfma_f32_16x16x32_bf16 v[208:211], a[52:55], a[160:163], v[208:211]\n"
"s_waitcnt lgkmcnt(11)\n"
"ds_read_b64_tr_b16 a[70:71], v72 offset:15104\n"
"ds_read_b64_tr_b16 a[132:133], v72 offset:16576\n"
"v_mfma_f32_16x16x32_bf16 v[212:215], a[56:59], a[160:163], v[212:215]\n"
"s_waitcnt lgkmcnt(11)\n"
"ds_read_b64_tr_b16 a[134:135], v72 offset:18112\n"
"ds_read_b64_tr_b16 a[136:137], v72 offset:16608\n"
"v_mfma_f32_16x16x32_bf16 v[216:219], a[120:123], a[160:163], v[216:219]\n"
"s_waitcnt lgkmcnt(11)\n"
"ds_read_b64_tr_b16 a[138:139], v72 offset:18144\n"
"ds_read_b64_tr_b16 a[140:141], v72 offset:16640\n"
"v_mfma_f32_16x16x32_bf16 v[252:255], a[32:35], a[176:179], v[252:255]\n"
"s_waitcnt lgkmcnt(11)\n"
"ds_read_b64_tr_b16 a[142:143], v72 offset:18176\n"
"v_mfma_f32_16x16x32_bf16 v[220:223], a[124:127], a[160:163], v[220:223]\n"
"s_waitcnt lgkmcnt(10)\n"
"v_mfma_f32_16x16x32_bf16 v[224:227], a[128:131], a[160:163], v[224:227]\n"
"s_waitcnt lgkmcnt(8)\n"
"v_mfma_f32_16x16x32_bf16 v[204:207], a[60:63], a[164:167], v[204:207]\n"
"s_waitcnt lgkmcnt(6)\n"
"v_mfma_f32_16x16x32_bf16 v[208:211], a[64:67], a[164:167], v[208:211]\n"
"s_waitcnt lgkmcnt(4)\n"
"v_mfma_f32_16x16x32_bf16 v[212:215], a[68:71], a[164:167], v[212:215]\n"
"s_waitcnt lgkmcnt(2)\n"
"v_mfma_f32_16x16x32_bf16 v[216:219], a[132:135], a[164:167], v[216:219]\n"
"s_waitcnt lgkmcnt(0)\n"
"s_nop 0\n"
"v_cvt_pk_bf16_f32 v204, v204, v205\n"
"v_mfma_f32_16x16x32_bf16 v[220:223], a[136:139], a[164:167], v[220:223]\n"
"s_barrier\n"
"v_mfma_f32_16x16x32_bf16 v[224:227], a[140:143], a[164:167], v[224:227]\n"
"v_cvt_pk_bf16_f32 v205, v206, v207\n"
"v_cvt_pk_bf16_f32 v206, v208, v209\n"
"v_mfma_f32_16x16x32_bf16 v[104:107], a[96:99], a[176:179], v[104:107]\n"
"v_cvt_pk_bf16_f32 v207, v210, v211\n"
"v_cvt_pk_bf16_f32 v212, v212, v213\n"
"v_mfma_f32_16x16x32_bf16 v[104:107], a[108:111], a[180:183], v[104:107]\n"
"ds_write_b128 v21, v[204:207]\n"
"v_mfma_f32_16x16x32_bf16 v[104:107], a[120:123], a[184:187], v[104:107]\n"
"v_cvt_pk_bf16_f32 v213, v214, v215\n"
"v_cvt_pk_bf16_f32 v214, v224, v225\n"
"v_mfma_f32_16x16x32_bf16 v[104:107], a[132:135], a[188:191], v[104:107]\n"
"v_cvt_pk_bf16_f32 v215, v226, v227\n"
"v_cvt_pk_bf16_f32 v216, v216, v217\n"
"v_mfma_f32_16x16x32_bf16 v[108:111], a[100:103], a[176:179], v[108:111]\n"
"ds_write_b128 v21, v[212:215] offset:64\n"
"v_mfma_f32_16x16x32_bf16 v[108:111], a[112:115], a[180:183], v[108:111]\n"
"s_add_u32 m0, s34, 6144\n"
"v_cvt_pk_bf16_f32 v217, v218, v219\n"
"v_cvt_pk_bf16_f32 v218, v220, v221\n"
"v_mfma_f32_16x16x32_bf16 v[108:111], a[124:127], a[184:187], v[108:111]\n"
"buffer_load_dwordx4 v190, s[8:11], s32 offen lds\n"
"v_cvt_pk_bf16_f32 v219, v222, v223\n"
"v_mfma_f32_16x16x32_bf16 v[108:111], a[136:139], a[188:191], v[108:111]\n"
"s_waitcnt vmcnt(11)\n"
"ds_write_b128 v21, v[216:219] offset:128\n"
"v_mfma_f32_16x16x32_bf16 v[228:231], a[104:107], a[176:179], v[228:231]\n"
"s_add_u32 m0, s34, 9216\n"
"v_cvt_pk_bf16_f32 v104, v104, v105\n"
"v_cvt_pk_bf16_f32 v105, v106, v107\n"
"v_mfma_f32_16x16x32_bf16 v[228:231], a[116:119], a[180:183], v[228:231]\n"
"s_nop 0\n"
"v_cvt_pk_bf16_f32 v106, v108, v109\n"
"v_mfma_f32_16x16x32_bf16 v[228:231], a[128:131], a[184:187], v[228:231]\n"
"buffer_load_dwordx4 v191, s[8:11], s32 offen lds\n"
"ds_read_b128 v[32:35], v188\n"
"v_mfma_f32_16x16x32_bf16 v[228:231], a[140:143], a[188:191], v[228:231]\n"
"s_add_u32 s32, s32, s33\n"
"ds_read_b128 v[36:39], v189\n"
"v_cvt_pk_bf16_f32 v107, v110, v111\n"
"v_mfma_f32_16x16x32_bf16 v[248:251], a[40:43], a[180:183], v[248:251]\n"
"ds_write_b128 v22, v[104:107] offset:128\n"
"v_mfma_f32_16x16x32_bf16 v[248:251], a[52:55], a[184:187], v[248:251]\n"
"s_add_u32 m0, s34, 7168\n"
"s_nop 0\n"
"buffer_load_dwordx4 v190, s[8:11], s0 offen lds\n"
"v_mfma_f32_16x16x32_bf16 v[248:251], a[64:67], a[188:191], v[248:251]\n"
"s_waitcnt vmcnt(11)\n"
"ds_read_b128 v[40:43], v188 offset:1024\n"
"v_mfma_f32_16x16x32_bf16 v[252:255], a[44:47], a[180:183], v[252:255]\n"
"ds_read_b128 v[44:47], v189 offset:1024\n"
"ds_read_b64 v[80:81], v20 offset:272\n"
"v_mfma_f32_16x16x32_bf16 v[252:255], a[56:59], a[184:187], v[252:255]\n"
"s_waitcnt lgkmcnt(5)\n"
"ds_read_b64 v[86:87], v20 offset:336\n"
"ds_read_b64 v[88:89], v20 offset:544\n"
"v_mfma_f32_16x16x32_bf16 v[252:255], a[68:71], a[188:191], v[252:255]\n"
"s_add_u32 m0, s34, 10240\n"
"ds_read_b64 v[94:95], v20 offset:608\n"
"buffer_load_dwordx4 v191, s[8:11], s0 offen lds\n"
"v_mfma_f32_16x16x32_bf16 v[244:247], a[48:51], a[184:187], v[244:247]\n"
"s_add_u32 s0, s0, s33\n"
"ds_read_b64 v[96:97], v20 offset:816\n"
"ds_read_b64 v[102:103], v20 offset:880\n"
"v_mfma_f32_16x16x32_bf16 v[244:247], a[60:63], a[188:191], v[244:247]\n"
"v_cvt_pk_bf16_f32 v252, v252, v253\n"
"v_cvt_pk_bf16_f32 v253, v254, v255\n"
"v_mfma_f32_16x16x32_bf16 v[204:207], a[0:3], v[32:35], 0\n"
"s_waitcnt lgkmcnt(9)\n"
"v_cvt_pk_bf16_f32 v254, v228, v229\n"
"v_cvt_pk_bf16_f32 v255, v230, v231\n"
"v_mfma_f32_16x16x32_bf16 v[204:207], a[12:15], v[36:39], v[204:207]\n"
"s_waitcnt lgkmcnt(7)\n"
"ds_write_b128 v22, v[252:255] offset:64\n"
"v_mfma_f32_16x16x32_bf16 v[204:207], a[24:27], v[40:43], v[204:207]\n"
"s_waitcnt vmcnt(10) lgkmcnt(5)\n"
"v_cvt_pk_bf16_f32 v244, v244, v245\n"
"v_cvt_pk_bf16_f32 v245, v246, v247\n"
"v_mfma_f32_16x16x32_bf16 v[204:207], a[36:39], v[44:47], v[204:207]\n"
"s_waitcnt lgkmcnt(3)\n"
"v_cvt_pk_bf16_f32 v246, v248, v249\n"
"v_cvt_pk_bf16_f32 v247, v250, v251\n"
"v_mfma_f32_16x16x32_bf16 v[208:211], a[4:7], v[32:35], 0\n"
"s_waitcnt lgkmcnt(1)\n"
"ds_read_b128 v[48:51], v188 offset:2048\n"
"ds_read_b128 v[52:55], v189 offset:2048\n"
"v_mfma_f32_16x16x32_bf16 v[208:211], a[16:19], v[36:39], v[208:211]\n"
"s_add_u32 m0, s34, 8192\n"
"ds_write_b128 v22, v[244:247]\n"
"v_mfma_f32_16x16x32_bf16 v[208:211], a[28:31], v[40:43], v[208:211]\n"
"buffer_load_dwordx4 v190, s[8:11], s1 offen lds\n"
"v_bfi_b32 v80, v4, v80, v26\n"
"v_mfma_f32_16x16x32_bf16 v[208:211], a[40:43], v[44:47], v[208:211]\n"
"v_bfi_b32 v81, v5, v81, v26\n"
"v_bfi_b32 v86, v6, v86, v26\n"
"v_mfma_f32_16x16x32_bf16 v[212:215], a[8:11], v[32:35], 0\n"
"v_bfi_b32 v87, v7, v87, v26\n"
"v_bfi_b32 v88, v0, v88, v26\n"
"v_mfma_f32_16x16x32_bf16 v[212:215], a[20:23], v[36:39], v[212:215]\n"
"s_add_u32 m0, s34, 11264\n"
"v_bfi_b32 v89, v1, v89, v26\n"
"v_bfi_b32 v94, v2, v94, v26\n"
"v_mfma_f32_16x16x32_bf16 v[212:215], a[32:35], v[40:43], v[212:215]\n"
"s_waitcnt vmcnt(9) lgkmcnt(2)\n"
"buffer_load_dwordx4 v191, s[8:11], s1 offen lds\n"
"ds_read_b128 a[216:219], v70\n"
"v_mfma_f32_16x16x32_bf16 v[212:215], a[44:47], v[44:47], v[212:215]\n"
"s_add_u32 s1, s1, s33\n"
"ds_read_b128 a[220:223], v71\n"
"v_bfi_b32 v95, v3, v95, v26\n"
"v_mfma_f32_16x16x32_bf16 v[212:215], a[56:59], v[48:51], v[212:215]\n"
"s_waitcnt lgkmcnt(3)\n"
"v_bfi_b32 v96, v0, v96, v26\n"
"v_bfi_b32 v97, v1, v97, v26\n"
"v_mfma_f32_16x16x32_bf16 v[212:215], a[68:71], v[52:55], v[212:215]\n"
"s_add_u32 m0, s65, 154752\n"
"v_bfi_b32 v102, v2, v102, v26\n"
"v_bfi_b32 v103, v3, v103, v26\n"
"v_mfma_f32_16x16x32_bf16 v[216:219], a[72:75], v[32:35], 0\n"
"buffer_load_dwordx4 v27, s[12:15], s81 offen lds\n"
"v_mfma_f32_16x16x32_bf16 v[216:219], a[84:87], v[36:39], v[216:219]\n"
"v_mfma_f32_16x16x32_bf16 v[216:219], a[96:99], v[40:43], v[216:219]\n"
"s_nop 0\n"
"v_cvt_pk_bf16_f32 v212, v212, v213\n"
"v_mfma_f32_16x16x32_bf16 v[216:219], a[108:111], v[44:47], v[216:219]\n"
"s_waitcnt lgkmcnt(1)\n"
"v_cvt_pk_bf16_f32 v213, v214, v215\n"
"v_mfma_f32_16x16x32_bf16 v[216:219], a[120:123], v[48:51], v[216:219]\n"
"s_add_u32 m0, s65, 155776\n"
"s_nop 0\n"
"buffer_load_dwordx4 v28, s[12:15], s81 offen lds\n"
"v_mfma_f32_16x16x32_bf16 v[216:219], a[132:135], v[52:55], v[216:219]\n"
"v_mfma_f32_16x16x32_bf16 v[220:223], a[76:79], v[32:35], 0\n"
"v_mfma_f32_16x16x32_bf16 v[220:223], a[88:91], v[36:39], v[220:223]\n"
"s_add_u32 m0, s65, 156800\n"
"v_mfma_f32_16x16x32_bf16 v[220:223], a[100:103], v[40:43], v[220:223]\n"
"buffer_load_dwordx4 v29, s[12:15], s81 offen lds\n"
"v_mfma_f32_16x16x32_bf16 v[220:223], a[112:115], v[44:47], v[220:223]\n"
"v_mfma_f32_16x16x32_bf16 v[220:223], a[124:127], v[48:51], v[220:223]\n"
"s_nop 0\n"
"v_cvt_pk_bf16_f32 v216, v216, v217\n"
"v_mfma_f32_16x16x32_bf16 v[220:223], a[136:139], v[52:55], v[220:223]\n"
"v_cvt_pk_bf16_f32 v217, v218, v219\n"
"v_mfma_f32_16x16x32_bf16 v[224:227], a[80:83], v[32:35], 0\n"
"v_mfma_f32_16x16x32_bf16 v[224:227], a[92:95], v[36:39], v[224:227]\n"
"v_mfma_f32_16x16x32_bf16 v[224:227], a[104:107], v[40:43], v[224:227]\n"
"v_mfma_f32_16x16x32_bf16 v[224:227], a[116:119], v[44:47], v[224:227]\n"
"v_mfma_f32_16x16x32_bf16 v[224:227], a[128:131], v[48:51], v[224:227]\n"
"v_mfma_f32_16x16x32_bf16 v[224:227], a[140:143], v[52:55], v[224:227]\n"
"s_nop 0\n"
"v_cvt_pk_bf16_f32 v218, v220, v221\n"
"v_mfma_f32_16x16x32_bf16 v[204:207], a[48:51], v[48:51], v[204:207]\n"
"v_cvt_pk_bf16_f32 v219, v222, v223\n"
"v_mfma_f32_16x16x32_bf16 v[204:207], a[60:63], v[52:55], v[204:207]\n"
"ds_write_b128 v186, v[216:219] offset:20384\n"
"v_mfma_f32_16x16x32_bf16 v[208:211], a[52:55], v[48:51], v[208:211]\n"
"v_mfma_f32_16x16x32_bf16 v[208:211], a[64:67], v[52:55], v[208:211]\n"
"v_cvt_pk_bf16_f32 v214, v224, v225\n"
"v_cvt_pk_bf16_f32 v215, v226, v227\n"
"v_mfma_f32_16x16x32_bf16 v[244:247], a[0:3], a[216:219], 0\n"
"s_waitcnt lgkmcnt(1)\n"
"ds_write_b128 v186, v[212:215] offset:20320\n"
"v_mfma_f32_16x16x32_bf16 v[244:247], a[12:15], a[220:223], v[244:247]\n"
"v_cvt_pk_bf16_f32 v204, v204, v205\n"
"v_cvt_pk_bf16_f32 v205, v206, v207\n"
"v_mfma_f32_16x16x32_bf16 v[248:251], a[4:7], a[216:219], 0\n"
"v_cvt_pk_bf16_f32 v206, v208, v209\n"
"v_cvt_pk_bf16_f32 v207, v210, v211\n"
"v_mfma_f32_16x16x32_bf16 v[248:251], a[16:19], a[220:223], v[248:251]\n"
"ds_write_b128 v186, v[204:207] offset:20256\n"
"v_mfma_f32_16x16x32_bf16 v[252:255], a[8:11], a[216:219], 0\n"
"v_mfma_f32_16x16x32_bf16 v[252:255], a[20:23], a[220:223], v[252:255]\n"
"s_waitcnt vmcnt(11)\n"
"s_add_u32 m0, s43, 0\n"
"ds_read_b128 a[224:227], v70 offset:1024\n"
"v_mfma_f32_16x16x32_bf16 v[104:107], a[72:75], a[216:219], 0\n"
"buffer_load_dwordx4 v27, s[12:15], s3 offen lds\n"
"ds_read_b128 a[228:231], v71 offset:1024\n"
"v_mfma_f32_16x16x32_bf16 v[104:107], a[84:87], a[220:223], v[104:107]\n"
"v_mfma_f32_16x16x32_bf16 v[108:111], a[76:79], a[216:219], 0\n"
"v_mfma_f32_16x16x32_bf16 v[108:111], a[88:91], a[220:223], v[108:111]\n"
"s_add_u32 m0, s43, 1024\n"
"v_mfma_f32_16x16x32_bf16 v[228:231], a[80:83], a[216:219], 0\n"
"buffer_load_dwordx4 v28, s[12:15], s3 offen lds\n"
"v_mfma_f32_16x16x32_bf16 v[228:231], a[92:95], a[220:223], v[228:231]\n"
"s_waitcnt lgkmcnt(1)\n"
"v_mfma_f32_16x16x32_bf16 v[228:231], a[104:107], a[224:227], v[228:231]\n"
"s_waitcnt vmcnt(11) lgkmcnt(0)\n"
"ds_read_b128 a[232:235], v70 offset:2048\n"
"ds_read_b128 a[236:239], v71 offset:2048\n"
"v_mfma_f32_16x16x32_bf16 v[228:231], a[116:119], a[228:231], v[228:231]\n"
"s_add_u32 m0, s43, 2048\n"
"s_add_u32 s43, s79, 0\n"
"buffer_load_dwordx4 v29, s[12:15], s3 offen lds\n"
"v_mfma_f32_16x16x32_bf16 v[244:247], a[24:27], a[224:227], v[244:247]\n"
"v_mfma_f32_16x16x32_bf16 v[244:247], a[36:39], a[228:231], v[244:247]\n"
"v_mfma_f32_16x16x32_bf16 v[248:251], a[28:31], a[224:227], v[248:251]\n"
"s_add_u32 m0, s34, 0\n"
"v_mfma_f32_16x16x32_bf16 v[248:251], a[40:43], a[228:231], v[248:251]\n"
"buffer_load_dwordx4 v190, s[8:11], s32 offen lds\n"
"v_mfma_f32_16x16x32_bf16 v[252:255], a[32:35], a[224:227], v[252:255]\n"
"v_mfma_f32_16x16x32_bf16 v[252:255], a[44:47], a[228:231], v[252:255]\n"
"s_waitcnt vmcnt(11) lgkmcnt(1)\n"
"v_mfma_f32_16x16x32_bf16 v[252:255], a[56:59], a[232:235], v[252:255]\n"
"s_waitcnt lgkmcnt(0)\n"
"ds_read_b128 a[240:243], v188 offset:6144\n"
"ds_read_b128 a[244:247], v189 offset:6144\n"
"v_mfma_f32_16x16x32_bf16 v[252:255], a[68:71], a[236:239], v[252:255]\n"
"s_add_u32 m0, s34, 3072\n"
"s_nop 0\n"
"buffer_load_dwordx4 v191, s[8:11], s32 offen lds\n"
"v_mfma_f32_16x16x32_bf16 v[104:107], a[96:99], a[224:227], v[104:107]\n"
"s_add_u32 s32, s32, s33\n"
"v_mfma_f32_16x16x32_bf16 v[104:107], a[108:111], a[228:231], v[104:107]\n"
"v_mfma_f32_16x16x32_bf16 v[104:107], a[120:123], a[232:235], v[104:107]\n"
"s_add_u32 m0, s34, 1024\n"
"v_mfma_f32_16x16x32_bf16 v[104:107], a[132:135], a[236:239], v[104:107]\n"
"v_cvt_pk_bf16_f32 v252, v252, v253\n"
"v_cvt_pk_bf16_f32 v253, v254, v255\n"
"v_mfma_f32_16x16x32_bf16 v[108:111], a[100:103], a[224:227], v[108:111]\n"
"buffer_load_dwordx4 v190, s[8:11], s0 offen lds\n"
"v_mfma_f32_16x16x32_bf16 v[108:111], a[112:115], a[228:231], v[108:111]\n"
"s_waitcnt lgkmcnt(1)\n"
"v_mfma_f32_16x16x32_bf16 v[108:111], a[124:127], a[232:235], v[108:111]\n"
"s_nop 0\n"
"v_cvt_pk_bf16_f32 v104, v104, v105\n"
"v_mfma_f32_16x16x32_bf16 v[108:111], a[136:139], a[236:239], v[108:111]\n"
"s_waitcnt vmcnt(11)\n"
"v_cvt_pk_bf16_f32 v105, v106, v107\n"
"s_add_u32 m0, s34, 4096\n"
"v_mfma_f32_16x16x32_bf16 v[244:247], a[48:51], a[232:235], v[244:247]\n"
"buffer_load_dwordx4 v191, s[8:11], s0 offen lds\n"
"ds_read_b128 a[248:251], v188 offset:7168\n"
"v_mfma_f32_16x16x32_bf16 v[244:247], a[60:63], a[236:239], v[244:247]\n"
"s_add_u32 s0, s0, s33\n"
"ds_read_b128 a[252:255], v189 offset:7168\n"
"v_cvt_pk_bf16_f32 v106, v108, v109\n"
"v_mfma_f32_16x16x32_bf16 v[248:251], a[52:55], a[232:235], v[248:251]\n"
"v_cvt_pk_bf16_f32 v107, v110, v111\n"
"v_mfma_f32_16x16x32_bf16 v[248:251], a[64:67], a[236:239], v[248:251]\n"
"s_add_u32 m0, s34, 2048\n"
"ds_write_b128 v186, v[104:107] offset:33888\n"
"v_mfma_f32_16x16x32_bf16 v[228:231], a[128:131], a[232:235], v[228:231]\n"
"buffer_load_dwordx4 v190, s[8:11], s1 offen lds\n"
"v_cvt_pk_bf16_f32 v244, v244, v245\n"
"v_mfma_f32_16x16x32_bf16 v[228:231], a[140:143], a[236:239], v[228:231]\n"
"v_cvt_pk_bf16_f32 v245, v246, v247\n"
"v_mfma_f32_16x16x32_bf16 v[120:123], a[0:3], a[240:243], 0\n"
"s_waitcnt vmcnt(11) lgkmcnt(3)\n"
"v_cvt_pk_bf16_f32 v246, v248, v249\n"
"v_cvt_pk_bf16_f32 v247, v250, v251\n"
"v_mfma_f32_16x16x32_bf16 v[120:123], a[12:15], a[244:247], v[120:123]\n"
"s_waitcnt lgkmcnt(2)\n"
"ds_read_b128 v[56:59], v188 offset:8192\n"
"ds_read_b128 v[64:67], v189 offset:8192\n"
"v_mfma_f32_16x16x32_bf16 v[120:123], a[24:27], a[248:251], v[120:123]\n"
"s_waitcnt lgkmcnt(3)\n"
"ds_write_b128 v186, v[244:247] offset:33760\n"
"v_mfma_f32_16x16x32_bf16 v[120:123], a[36:39], a[252:255], v[120:123]\n"
"s_add_u32 m0, s34, 5120\n"
"v_cvt_pk_bf16_f32 v254, v228, v229\n"
"v_cvt_pk_bf16_f32 v255, v230, v231\n"
"v_mfma_f32_16x16x32_bf16 v[124:127], a[4:7], a[240:243], 0\n"
"buffer_load_dwordx4 v191, s[8:11], s1 offen lds\n"
"ds_write_b128 v186, v[252:255] offset:33824\n"
"v_mfma_f32_16x16x32_bf16 v[124:127], a[16:19], a[244:247], v[124:127]\n"
"s_add_u32 s1, s1, s33\n"
"v_mov_b64 v[252:253], 0\n"
"v_mov_b64 v[254:255], 0\n"
"v_mfma_f32_16x16x32_bf16 v[124:127], a[28:31], a[248:251], v[124:127]\n"
"s_waitcnt lgkmcnt(3)\n"
"s_add_u32 m0, s34, 6144\n"
"ds_read_b64 v[104:105], v20 offset:1088\n"
"v_mfma_f32_16x16x32_bf16 v[124:127], a[40:43], a[252:255], v[124:127]\n"
"buffer_load_dwordx4 v190, s[8:11], s32 offen lds\n"
"ds_read_b64 v[110:111], v20 offset:1152\n"
"v_mfma_f32_16x16x32_bf16 v[124:127], a[52:55], v[56:59], v[124:127]\n"
"s_waitcnt lgkmcnt(4)\n"
"v_subrev_u32 v20, s49, v20\n"
"v_mfma_f32_16x16x32_bf16 v[124:127], a[64:67], v[64:67], v[124:127]\n"
"v_mfma_f32_16x16x32_bf16 v[128:131], a[8:11], a[240:243], 0\n"
"v_mfma_f32_16x16x32_bf16 v[128:131], a[20:23], a[244:247], v[128:131]\n"
"v_mfma_f32_16x16x32_bf16 v[128:131], a[32:35], a[248:251], v[128:131]\n"
"v_mfma_f32_16x16x32_bf16 v[128:131], a[44:47], a[252:255], v[128:131]\n"
"s_waitcnt lgkmcnt(1)\n"
"v_bfi_b32 v104, v8, v104, v26\n"
"v_bfi_b32 v105, v9, v105, v26\n"
"v_mfma_f32_16x16x32_bf16 v[128:131], a[56:59], v[56:59], v[128:131]\n"
"s_waitcnt lgkmcnt(0)\n"
"v_bfi_b32 v110, v10, v110, v26\n"
"v_bfi_b32 v111, v11, v111, v26\n"
"v_mfma_f32_16x16x32_bf16 v[128:131], a[68:71], v[64:67], v[128:131]\n"
"v_mfma_f32_16x16x32_bf16 v[132:135], a[72:75], a[240:243], 0\n"
"v_mfma_f32_16x16x32_bf16 v[132:135], a[84:87], a[244:247], v[132:135]\n"
"v_mfma_f32_16x16x32_bf16 v[132:135], a[96:99], a[248:251], v[132:135]\n"
"v_mfma_f32_16x16x32_bf16 v[132:135], a[108:111], a[252:255], v[132:135]\n"
"v_mfma_f32_16x16x32_bf16 v[132:135], a[120:123], v[56:59], v[132:135]\n"
"v_mfma_f32_16x16x32_bf16 v[132:135], a[132:135], v[64:67], v[132:135]\n"
"v_mfma_f32_16x16x32_bf16 v[136:139], a[76:79], a[240:243], 0\n"
"s_nop 0\n"
"v_cvt_pk_bf16_f32 v128, v128, v129\n"
"v_mfma_f32_16x16x32_bf16 v[136:139], a[88:91], a[244:247], v[136:139]\n"
"v_cvt_pk_bf16_f32 v129, v130, v131\n"
"v_mfma_f32_16x16x32_bf16 v[136:139], a[100:103], a[248:251], v[136:139]\n"
"v_mfma_f32_16x16x32_bf16 v[136:139], a[112:115], a[252:255], v[136:139]\n"
"s_nop 0\n"
"v_cvt_pk_bf16_f32 v132, v132, v133\n"
"v_mfma_f32_16x16x32_bf16 v[136:139], a[124:127], v[56:59], v[136:139]\n"
"v_cvt_pk_bf16_f32 v133, v134, v135\n"
"v_mfma_f32_16x16x32_bf16 v[136:139], a[136:139], v[64:67], v[136:139]\n"
"s_add_u32 m0, s34, 9216\n"
"v_mfma_f32_16x16x32_bf16 v[140:143], a[80:83], a[240:243], 0\n"
"buffer_load_dwordx4 v191, s[8:11], s32 offen lds\n"
"s_add_u32 s32, s32, s33\n"
"v_mfma_f32_16x16x32_bf16 v[140:143], a[92:95], a[244:247], v[140:143]\n"
"v_mfma_f32_16x16x32_bf16 v[140:143], a[104:107], a[248:251], v[140:143]\n"
"v_mfma_f32_16x16x32_bf16 v[140:143], a[116:119], a[252:255], v[140:143]\n"
"s_nop 0\n"
"v_cvt_pk_bf16_f32 v134, v136, v137\n"
"v_mfma_f32_16x16x32_bf16 v[140:143], a[128:131], v[56:59], v[140:143]\n"
"s_add_u32 m0, s34, 7168\n"
"v_cvt_pk_bf16_f32 v135, v138, v139\n"
"v_mfma_f32_16x16x32_bf16 v[140:143], a[140:143], v[64:67], v[140:143]\n"
"buffer_load_dwordx4 v190, s[8:11], s0 offen lds\n"
"ds_write_b128 v186, v[132:135] offset:47392\n"
"v_mfma_f32_16x16x32_bf16 v[120:123], a[48:51], v[56:59], v[120:123]\n"
"v_mfma_f32_16x16x32_bf16 v[120:123], a[60:63], v[64:67], v[120:123]\n"
"s_add_u32 m0, s34, 10240\n"
"s_nop 2\n"
"v_cvt_pk_bf16_f32 v130, v140, v141\n"
"buffer_load_dwordx4 v191, s[8:11], s0 offen lds\n"
"s_add_u32 s0, s0, s33\n"
"s_nop 0\n"
"v_cvt_pk_bf16_f32 v120, v120, v121\n"
"v_cvt_pk_bf16_f32 v121, v122, v123\n"
"v_cvt_pk_bf16_f32 v122, v124, v125\n"
"v_cvt_pk_bf16_f32 v123, v126, v127\n"
"v_cvt_pk_bf16_f32 v131, v142, v143\n"
"ds_write_b128 v186, v[120:123] offset:47264\n"
"s_add_u32 m0, s34, 8192\n"
"s_nop 0\n"
"buffer_load_dwordx4 v190, s[8:11], s1 offen lds\n"
"ds_write_b128 v186, v[128:131] offset:47328\n"
"s_add_u32 m0, s34, 11264\n"
"s_waitcnt vmcnt(11)\n"
"s_barrier\n"
"buffer_load_dwordx4 v191, s[8:11], s1 offen lds\n"
"s_add_u32 s1, s1, s33\n"
"ds_read_b64_tr_b16 a[144:145], v68\n"
"ds_read_b64_tr_b16 a[146:147], v68 offset:1536\n"
"ds_read_b64_tr_b16 a[148:149], v68 offset:32\n"
"ds_read_b64_tr_b16 a[150:151], v68 offset:1568\n"
"ds_read_b64_tr_b16 a[152:153], v68 offset:64\n"
"ds_read_b64_tr_b16 a[154:155], v68 offset:1600\n"
"ds_read_b64_tr_b16 a[156:157], v68 offset:3072\n"
"ds_read_b64_tr_b16 a[158:159], v68 offset:4608\n"
"ds_read_b64_tr_b16 a[160:161], v68 offset:3104\n"
"ds_read_b64_tr_b16 a[162:163], v68 offset:4640\n"
"buffer_load_dword v200, off, s[28:31], s39 sc1\n"
"ds_read_b64_tr_b16 a[164:165], v68 offset:3136\n"
"ds_read_b64_tr_b16 a[166:167], v68 offset:4672\n"
"ds_read_b64_tr_b16 a[168:169], v68 offset:6752\n"
"ds_read_b64_tr_b16 a[170:171], v68 offset:8288\n"
"ds_read_b64_tr_b16 a[172:173], v68 offset:6784\n"
"ds_read_b64_tr_b16 a[174:175], v68 offset:8320\n"
"ds_read_b64_tr_b16 a[176:177], v68 offset:6816\n"
"s_waitcnt lgkmcnt(15)\n"
"v_mfma_f32_16x16x32_bf16 v[204:207], a[144:147], v[32:35], 0\n"
"s_waitcnt lgkmcnt(13)\n"
"ds_read_b64_tr_b16 a[178:179], v68 offset:8352\n"
"ds_read_b64_tr_b16 a[180:181], v68 offset:9824\n"
"v_mfma_f32_16x16x32_bf16 v[208:211], a[148:151], v[32:35], 0\n"
"s_waitcnt lgkmcnt(13)\n"
"ds_read_b64_tr_b16 a[182:183], v68 offset:11360\n"
"ds_read_b64_tr_b16 a[184:185], v68 offset:9856\n"
"v_mfma_f32_16x16x32_bf16 v[212:215], a[152:155], v[32:35], 0\n"
"s_waitcnt lgkmcnt(13)\n"
"ds_read_b64_tr_b16 a[186:187], v68 offset:11392\n"
"ds_read_b64_tr_b16 a[188:189], v68 offset:9888\n"
"v_mfma_f32_16x16x32_bf16 v[204:207], a[156:159], v[36:39], v[204:207]\n"
"s_waitcnt lgkmcnt(13)\n"
"ds_read_b64_tr_b16 a[190:191], v68 offset:11424\n"
"ds_read_b64_tr_b16 a[192:193], v69\n"
"v_mfma_f32_16x16x32_bf16 v[208:211], a[160:163], v[36:39], v[208:211]\n"
"s_waitcnt lgkmcnt(13)\n"
"ds_read_b64_tr_b16 a[194:195], v69 offset:1536\n"
"ds_read_b64_tr_b16 a[196:197], v69 offset:32\n"
"v_mfma_f32_16x16x32_bf16 v[212:215], a[164:167], v[36:39], v[212:215]\n"
"s_waitcnt lgkmcnt(13)\n"
"ds_read_b64_tr_b16 a[198:199], v69 offset:1568\n"
"ds_read_b64_tr_b16 a[200:201], v69 offset:64\n"
"v_mfma_f32_16x16x32_bf16 v[204:207], a[168:171], v[40:43], v[204:207]\n"
"s_waitcnt lgkmcnt(13)\n"
"ds_read_b64_tr_b16 a[202:203], v69 offset:1600\n"
"ds_read_b64_tr_b16 a[204:205], v69 offset:3072\n"
"v_mfma_f32_16x16x32_bf16 v[208:211], a[172:175], v[40:43], v[208:211]\n"
"s_waitcnt lgkmcnt(13)\n"
"ds_read_b64_tr_b16 a[206:207], v69 offset:4608\n"
"ds_read_b64_tr_b16 a[208:209], v69 offset:3104\n"
"v_mfma_f32_16x16x32_bf16 v[212:215], a[176:179], v[40:43], v[212:215]\n"
"s_waitcnt lgkmcnt(13)\n"
"ds_read_b64_tr_b16 a[210:211], v69 offset:4640\n"
"ds_read_b64_tr_b16 a[212:213], v69 offset:3136\n"
"v_mfma_f32_16x16x32_bf16 v[204:207], a[180:183], v[44:47], v[204:207]\n"
"s_waitcnt lgkmcnt(13)\n"
"ds_read_b64_tr_b16 a[214:215], v69 offset:4672\n"
"v_mfma_f32_16x16x32_bf16 v[208:211], a[184:187], v[44:47], v[208:211]\n"
"s_waitcnt lgkmcnt(12)\n"
"v_mfma_f32_16x16x32_bf16 v[212:215], a[188:191], v[44:47], v[212:215]\n"
"s_waitcnt lgkmcnt(10)\n"
"v_mfma_f32_16x16x32_bf16 v[204:207], a[192:195], v[48:51], v[204:207]\n"
"s_waitcnt lgkmcnt(8)\n"
"v_mfma_f32_16x16x32_bf16 v[208:211], a[196:199], v[48:51], v[208:211]\n"
"s_waitcnt lgkmcnt(6)\n"
"v_mfma_f32_16x16x32_bf16 v[212:215], a[200:203], v[48:51], v[212:215]\n"
"s_waitcnt lgkmcnt(4)\n"
"v_mfma_f32_16x16x32_bf16 v[204:207], a[204:207], v[52:55], v[204:207]\n"
"s_waitcnt lgkmcnt(2)\n"
"v_mfma_f32_16x16x32_bf16 v[208:211], a[208:211], v[52:55], v[208:211]\n"
"s_waitcnt lgkmcnt(0)\n"
"s_barrier\n"
"v_mfma_f32_16x16x32_bf16 v[212:215], a[212:215], v[52:55], v[212:215]\n"
"ds_read_b128 v[112:115], v178\n"
"ds_read_b64 v[82:83], v178 offset:64\n"
"s_nop 0\n"
"v_cvt_pk_bf16_f32 v144, v204, v205\n"
"ds_read_b128 v[120:123], v179\n"
"v_cvt_pk_bf16_f32 v145, v206, v207\n"
"v_mfma_f32_16x16x32_bf16 v[204:207], a[144:147], a[216:219], 0\n"
"ds_read_b64 v[84:85], v179 offset:64\n"
"buffer_load_dword v237, v238, s[8:11], s78 offen\n"
"v_mfma_f32_16x16x32_bf16 v[204:207], a[156:159], a[220:223], v[204:207]\n"
"s_add_u32 s78, s78, s33\n"
"ds_read_b128 v[116:119], v178 offset:13504\n"
"v_cvt_pk_bf16_f32 v146, v208, v209\n"
"v_mfma_f32_16x16x32_bf16 v[204:207], a[168:171], a[224:227], v[204:207]\n"
"s_waitcnt vmcnt(12)\n"
"ds_read_b64 v[90:91], v178 offset:13568\n"
"v_cvt_pk_bf16_f32 v147, v210, v211\n"
"v_mfma_f32_16x16x32_bf16 v[204:207], a[180:183], a[228:231], v[204:207]\n"
".Lic0%=:\n"
"s_cbranch_execz .Lib0%=\n"
"ds_read_b128 v[32:35], v188\n"
"v_cvt_pk_bf16_f32 v158, v212, v213\n"
"v_mfma_f32_16x16x32_bf16 v[204:207], a[192:195], a[232:235], v[204:207]\n"
"ds_read_b128 v[36:39], v189\n"
"v_cvt_pk_bf16_f32 v159, v214, v215\n"
"v_mfma_f32_16x16x32_bf16 v[204:207], a[204:207], a[236:239], v[204:207]\n"
"s_waitcnt lgkmcnt(6)\n"
"ds_read_b128 v[124:127], v179 offset:13504\n"
"buffer_load_dword v237, v238, s[8:11], s78 offen\n"
"v_mfma_f32_16x16x32_bf16 v[208:211], a[148:151], a[216:219], 0\n"
"s_add_u32 s78, s78, s33\n"
"ds_read_b64 v[92:93], v179 offset:13568\n"
"v_mfma_f32_16x16x32_bf16 v[112:115], v[112:115], v[144:147], 0\n"
"s_waitcnt lgkmcnt(6)\n"
"ds_read_b128 v[128:131], v178 offset:27008\n"
"v_cvt_pk_bf16_f32 v148, v204, v205\n"
"v_mfma_f32_16x16x32_bf16 v[112:115], v[80:83], v[156:159], v[112:115]\n"
"ds_read_b64 v[98:99], v178 offset:27072\n"
"v_cvt_pk_bf16_f32 v149, v206, v207\n"
"v_mfma_f32_16x16x32_bf16 v[120:123], v[120:123], v[144:147], 0\n"
"s_waitcnt lgkmcnt(6)\n"
"ds_read_b128 v[136:139], v179 offset:27008\n"
"v_subrev_u32 v178, s48, v178\n"
"v_mfma_f32_16x16x32_bf16 v[120:123], v[84:87], v[158:161], v[120:123]\n"
"ds_read_b64 v[100:101], v179 offset:27072\n"
"v_subrev_u32 v179, s48, v179\n"
"v_mfma_f32_16x16x32_bf16 v[116:119], v[116:119], v[144:147], 0\n"
"s_waitcnt vmcnt(11) lgkmcnt(6)\n"
"ds_read_b128 v[40:43], v188 offset:1024\n"
"v_mfma_f32_16x16x32_bf16 v[116:119], v[88:91], v[156:159], v[116:119]\n"
"s_add_u32 m0, s34, 0\n"
"ds_read_b128 v[44:47], v189 offset:1024\n"
"buffer_load_dwordx4 v190, s[8:11], s32 offen lds\n"
"v_mfma_f32_16x16x32_bf16 v[204:207], a[144:147], a[240:243], 0\n"
"s_waitcnt lgkmcnt(6)\n"
"ds_read_b128 v[132:135], v180\n"
"v_max_f32 v112, v112, v120\n"
"v_mfma_f32_16x16x32_bf16 v[204:207], a[156:159], a[244:247], v[204:207]\n"
"ds_read_b64 v[106:107], v180 offset:64\n"
"v_subrev_u32 v180, s48, v180\n"
"v_mfma_f32_16x16x32_bf16 v[124:127], v[124:127], v[144:147], 0\n"
"s_waitcnt lgkmcnt(6)\n"
"ds_read_b128 v[140:143], v181\n"
"v_max_f32 v113, v113, v121\n"
"v_mfma_f32_16x16x32_bf16 v[124:127], v[92:95], v[158:161], v[124:127]\n"
"ds_read_b64 v[108:109], v181 offset:64\n"
"v_subrev_u32 v181, s48, v181\n"
"v_mfma_f32_16x16x32_bf16 v[128:131], v[128:131], v[144:147], 0\n"
"s_waitcnt lgkmcnt(6)\n"
"ds_read_b64_tr_b16 v[224:225], v182 offset:128\n"
"v_max_f32 v114, v114, v122\n"
"v_mfma_f32_16x16x32_bf16 v[128:131], v[96:99], v[156:159], v[128:131]\n"
"ds_read_b64_tr_b16 v[226:227], v183 offset:128\n"
"v_max_f32 v115, v115, v123\n"
"v_mfma_f32_16x16x32_bf16 v[136:139], v[136:139], v[144:147], 0\n"
"ds_read_b64_tr_b16 v[228:229], v182 offset:896\n"
"v_max3_f32 v170, v112, v113, v114\n"
"v_mfma_f32_16x16x32_bf16 v[136:139], v[100:103], v[158:161], v[136:139]\n"
"s_waitcnt lgkmcnt(7)\n"
"ds_read_b64_tr_b16 v[230:231], v183 offset:896\n"
"v_max_f32 v116, v116, v124\n"
"v_mfma_f32_16x16x32_bf16 v[204:207], a[168:171], a[248:251], v[204:207]\n"
"s_waitcnt lgkmcnt(6)\n"
"ds_read_b64_tr_b16 v[244:245], v182 offset:136\n"
"v_max_f32 v117, v117, v125\n"
"v_mfma_f32_16x16x32_bf16 v[204:207], a[180:183], a[252:255], v[204:207]\n"
"s_waitcnt vmcnt(10)\n"
"ds_read_b64_tr_b16 v[246:247], v183 offset:136\n"
"v_max_f32 v118, v118, v126\n"
"v_mfma_f32_16x16x32_bf16 v[132:135], v[132:135], v[144:147], 0\n"
"s_waitcnt lgkmcnt(6)\n"
"ds_read_b128 v[48:51], v188 offset:2048\n"
"v_max_f32 v119, v119, v127\n"
"v_mfma_f32_16x16x32_bf16 v[132:135], v[104:107], v[156:159], v[132:135]\n"
"s_add_u32 m0, s34, 3072\n"
"ds_read_b128 v[52:55], v189 offset:2048\n"
"buffer_load_dwordx4 v191, s[8:11], s32 offen lds\n"
"v_mfma_f32_16x16x32_bf16 v[140:143], v[140:143], v[144:147], 0\n"
"s_add_u32 s32, s32, s33\n"
"ds_read_b64_tr_b16 v[248:249], v182 offset:904\n"
"v_max3_f32 v170, v170, v115, v116\n"
"v_mfma_f32_16x16x32_bf16 v[140:143], v[108:111], v[158:161], v[140:143]\n"
"ds_read_b64_tr_b16 v[250:251], v183 offset:904\n"
"v_max3_f32 v170, v170, v117, v118\n"
"v_mfma_f32_16x16x32_bf16 v[208:211], a[160:163], a[220:223], v[208:211]\n"
"ds_read_b64_tr_b16 v[120:121], v182 offset:72\n"
"v_max_f32 v128, v128, v136\n"
"v_mfma_f32_16x16x32_bf16 v[208:211], a[172:175], a[224:227], v[208:211]\n"
"ds_read_b64_tr_b16 v[122:123], v183 offset:72\n"
"v_max_f32 v129, v129, v137\n"
"v_mfma_f32_16x16x32_bf16 v[208:211], a[184:187], a[228:231], v[208:211]\n"
"ds_read_b64_tr_b16 v[124:125], v182 offset:840\n"
"v_max_f32 v130, v130, v138\n"
"v_mfma_f32_16x16x32_bf16 v[208:211], a[196:199], a[232:235], v[208:211]\n"
"ds_read_b64_tr_b16 v[126:127], v183 offset:840\n"
"v_max_f32 v131, v131, v139\n"
"v_mfma_f32_16x16x32_bf16 v[208:211], a[208:211], a[236:239], v[208:211]\n"
"ds_read_b64_tr_b16 v[216:217], v182 offset:27136\n"
"v_max3_f32 v170, v170, v119, v128\n"
"v_mfma_f32_16x16x32_bf16 v[204:207], a[192:195], v[56:59], v[204:207]\n"
"s_waitcnt lgkmcnt(7)\n"
"ds_read_b64_tr_b16 v[218:219], v183 offset:27136\n"
"v_max3_f32 v170, v170, v129, v130\n"
"v_mfma_f32_16x16x32_bf16 v[204:207], a[204:207], v[64:67], v[204:207]\n"
"s_add_u32 m0, s34, 1024\n"
"v_cvt_pk_bf16_f32 v150, v208, v209\n"
"v_cvt_pk_bf16_f32 v151, v210, v211\n"
"v_mfma_f32_16x16x32_bf16 v[208:211], a[148:151], a[240:243], 0\n"
"buffer_load_dwordx4 v190, s[8:11], s0 offen lds\n"
"ds_read_b64_tr_b16 v[220:221], v182 offset:27904\n"
"v_mfma_f32_16x16x32_bf16 v[208:211], a[160:163], a[244:247], v[208:211]\n"
"ds_read_b64_tr_b16 v[222:223], v183 offset:27904\n"
"v_cvt_pk_bf16_f32 v152, v204, v205\n"
"v_mfma_f32_16x16x32_bf16 v[208:211], a[172:175], a[248:251], v[208:211]\n"
"v_cvt_pk_bf16_f32 v153, v206, v207\n"
"v_max_f32 v132, v132, v140\n"
"v_mfma_f32_16x16x32_bf16 v[208:211], a[184:187], a[252:255], v[208:211]\n"
"ds_read_b64_tr_b16 v[72:73], v182 offset:27144\n"
"ds_read_b64_tr_b16 v[74:75], v183 offset:27144\n"
"v_mfma_f32_16x16x32_bf16 v[208:211], a[196:199], v[56:59], v[208:211]\n"
"ds_read_b64_tr_b16 v[76:77], v182 offset:27912\n"
"v_max_f32 v133, v133, v141\n"
"v_mfma_f32_16x16x32_bf16 v[208:211], a[208:211], v[64:67], v[208:211]\n"
"ds_read_b64_tr_b16 v[78:79], v183 offset:27912\n"
"v_max_f32 v134, v134, v142\n"
"v_mfma_f32_16x16x32_bf16 v[204:207], a[0:3], v[32:35], 0\n"
"ds_read_b64_tr_b16 v[136:137], v182 offset:27080\n"
"v_max_f32 v135, v135, v143\n"
"v_mfma_f32_16x16x32_bf16 v[204:207], a[12:15], v[36:39], v[204:207]\n"
"ds_read_b64_tr_b16 v[138:139], v183 offset:27080\n"
"v_max3_f32 v170, v170, v131, v132\n"
"v_mfma_f32_16x16x32_bf16 v[204:207], a[24:27], v[40:43], v[204:207]\n"
"s_add_u32 m0, s34, 4096\n"
"ds_read_b64_tr_b16 v[140:141], v182 offset:27848\n"
"buffer_load_dwordx4 v191, s[8:11], s0 offen lds\n"
"v_mfma_f32_16x16x32_bf16 v[204:207], a[36:39], v[44:47], v[204:207]\n"
"s_add_u32 s0, s0, s33\n"
"ds_read_b64_tr_b16 v[142:143], v183 offset:27848\n"
"v_cvt_pk_bf16_f32 v154, v208, v209\n"
"v_mfma_f32_16x16x32_bf16 v[204:207], a[48:51], v[48:51], v[204:207]\n"
"v_cvt_pk_bf16_f32 v155, v210, v211\n"
"v_max3_f32 v170, v170, v133, v134\n"
"v_mfma_f32_16x16x32_bf16 v[204:207], a[60:63], v[52:55], v[204:207]\n"
"ds_read_b64 v[80:81], v20 offset:272\n"
"ds_read_b64 v[86:87], v20 offset:336\n"
"v_mfma_f32_16x16x32_bf16 v[212:215], a[152:155], a[216:219], 0\n"
"ds_read_b64 v[88:89], v20 offset:544\n"
"ds_read_b64 v[94:95], v20 offset:608\n"
"v_mfma_f32_16x16x32_bf16 v[212:215], a[164:167], a[220:223], v[212:215]\n"
"ds_read_b64 v[96:97], v20 offset:816\n"
"ds_read_b64 v[102:103], v20 offset:880\n"
"v_mfma_f32_16x16x32_bf16 v[212:215], a[176:179], a[224:227], v[212:215]\n"
"v_cvt_pk_bf16_f32 v204, v204, v205\n"
"v_cvt_pk_bf16_f32 v205, v206, v207\n"
"v_mfma_f32_16x16x32_bf16 v[212:215], a[188:191], a[228:231], v[212:215]\n"
"ds_read_b64 v[104:105], v20 offset:1088\n"
"ds_read_b64 v[110:111], v20 offset:1152\n"
"v_mfma_f32_16x16x32_bf16 v[212:215], a[200:203], a[232:235], v[212:215]\n"
"s_add_u32 m0, s34, 2048\n"
"v_mov_b64 v[98:99], 0\n"
"v_mov_b64 v[100:101], 0\n"
"v_mfma_f32_16x16x32_bf16 v[212:215], a[212:215], a[236:239], v[212:215]\n"
"s_waitcnt lgkmcnt(7)\n"
"buffer_load_dwordx4 v190, s[8:11], s1 offen lds\n"
"v_mov_b64 v[106:107], 0\n"
"v_mfma_f32_16x16x32_bf16 v[208:211], a[4:7], v[32:35], 0\n"
"s_waitcnt lgkmcnt(5)\n"
"v_mov_b64 v[108:109], 0\n"
"v_max_f32 v171, v170, v135\n"
"v_mfma_f32_16x16x32_bf16 v[208:211], a[16:19], v[36:39], v[208:211]\n"
"s_waitcnt lgkmcnt(3)\n"
"v_cvt_pk_bf16_f32 v162, v212, v213\n"
"v_cvt_pk_bf16_f32 v163, v214, v215\n"
"v_mfma_f32_16x16x32_bf16 v[208:211], a[28:31], v[40:43], v[208:211]\n"
"s_waitcnt lgkmcnt(2)\n"
"v_max_f32 v170, v170, v135\n"
"v_subrev_u32 v182, s48, v182\n"
"v_mfma_f32_16x16x32_bf16 v[208:211], a[40:43], v[44:47], v[208:211]\n"
"s_waitcnt lgkmcnt(1)\n"
"v_permlane16_swap_b32 v170, v171\n"
"v_mfma_f32_16x16x32_bf16 v[208:211], a[52:55], v[48:51], v[208:211]\n"
"s_waitcnt lgkmcnt(0)\n"
"v_max_f32 v170, v170, v171\n"
"v_mov_b32 v171, v170\n"
"v_mfma_f32_16x16x32_bf16 v[208:211], a[64:67], v[52:55], v[208:211]\n"
"s_nop 0\n"
"v_permlane32_swap_b32 v170, v171\n"
"v_max_f32 v170, v170, v171\n"
"v_mul_f32 v170, 0xbe553b95, v170\n"
"v_pk_fma_f32 v[112:113], v[112:113], s[70:71], v[170:171] op_sel_hi:[1,0,0]\n"
"v_pk_fma_f32 v[114:115], v[114:115], s[70:71], v[170:171] op_sel_hi:[1,0,0]\n"
"v_pk_fma_f32 v[116:117], v[116:117], s[70:71], v[170:171] op_sel_hi:[1,0,0]\n"
"v_pk_fma_f32 v[118:119], v[118:119], s[70:71], v[170:171] op_sel_hi:[1,0,0]\n"
"v_pk_fma_f32 v[128:129], v[128:129], s[70:71], v[170:171] op_sel_hi:[1,0,0]\n"
"v_pk_fma_f32 v[130:131], v[130:131], s[70:71], v[170:171] op_sel_hi:[1,0,0]\n"
"v_pk_fma_f32 v[132:133], v[132:133], s[70:71], v[170:171] op_sel_hi:[1,0,0]\n"
"v_pk_fma_f32 v[134:135], v[134:135], s[70:71], v[170:171] op_sel_hi:[1,0,0]\n"
"v_mfma_f32_16x16x32_bf16 v[212:215], a[152:155], a[240:243], 0\n"
"s_add_u32 m0, s34, 5120\n"
"v_cvt_pk_bf16_f32 v206, v208, v209\n"
"v_cvt_pk_bf16_f32 v207, v210, v211\n"
"v_mfma_f32_16x16x32_bf16 v[212:215], a[164:167], a[244:247], v[212:215]\n"
"buffer_load_dwordx4 v191, s[8:11], s1 offen lds\n"
"ds_write_b128 v186, v[204:207] offset:60768\n"
"v_mfma_f32_16x16x32_bf16 v[212:215], a[176:179], a[248:251], v[212:215]\n"
"s_add_u32 s1, s1, s33\n"
"v_exp_f32 v112, v112\n"
"v_mfma_f32_16x16x32_bf16 v[212:215], a[188:191], a[252:255], v[212:215]\n"
"s_waitcnt vmcnt(8)\n"
"v_exp_f32 v113, v113\n"
"v_mfma_f32_16x16x32_bf16 v[212:215], a[200:203], v[56:59], v[212:215]\n"
"v_and_b32 v200, 0xffffff00, v200\n"
"v_add_u32 v200, 0x100, v200\n"
"v_mfma_f32_16x16x32_bf16 v[212:215], a[212:215], v[64:67], v[212:215]\n"
"s_mov_b64 exec, 1\n"
"buffer_store_dword v200, off, s[28:31], s39 offset:128 sc1\n"
"s_mov_b64 exec, -1\n"
"v_mfma_f32_16x16x32_bf16 v[208:211], a[80:83], v[32:35], 0\n"
"v_sub_u32 v201, 0, v200\n"
"s_nop 0\n"
"v_readfirstlane_b32 s50, v201\n"
"v_mfma_f32_16x16x32_bf16 v[208:211], a[92:95], v[36:39], v[208:211]\n"
"v_cvt_pk_bf16_f32 v166, v212, v213\n"
"v_cvt_pk_bf16_f32 v167, v214, v215\n"
"v_mfma_f32_16x16x32_bf16 v[208:211], a[104:107], v[40:43], v[208:211]\n"
"v_exp_f32 v114, v114\n"
"v_mfma_f32_16x16x32_bf16 v[208:211], a[116:119], v[44:47], v[208:211]\n"
"v_exp_f32 v115, v115\n"
"v_mfma_f32_16x16x32_bf16 v[208:211], a[128:131], v[48:51], v[208:211]\n"
"v_cvt_pk_bf16_f32 v82, v112, v114\n"
"v_cvt_pk_bf16_f32 v83, v113, v115\n"
"v_mfma_f32_16x16x32_bf16 v[208:211], a[140:143], v[52:55], v[208:211]\n"
"ds_read_b128 v[112:115], v178 offset:13504\n"
"v_exp_f32 v116, v116\n"
"v_mfma_f32_16x16x32_bf16 v[212:215], a[8:11], v[32:35], 0\n"
"v_exp_f32 v117, v117\n"
"v_mfma_f32_16x16x32_bf16 v[212:215], a[20:23], v[36:39], v[212:215]\n"
"v_exp_f32 v118, v118\n"
"v_mfma_f32_16x16x32_bf16 v[212:215], a[32:35], v[40:43], v[212:215]\n"
"v_exp_f32 v119, v119\n"
"v_mfma_f32_16x16x32_bf16 v[212:215], a[44:47], v[44:47], v[212:215]\n"
"v_cvt_pk_bf16_f32 v84, v116, v118\n"
"v_cvt_pk_bf16_f32 v85, v117, v119\n"
"v_mfma_f32_16x16x32_bf16 v[212:215], a[56:59], v[48:51], v[212:215]\n"
"ds_read_b128 v[116:119], v178 offset:27008\n"
"ds_read_b64_tr_b16 a[240:241], v182 offset:13632\n"
"v_mfma_f32_16x16x32_bf16 v[212:215], a[68:71], v[52:55], v[212:215]\n"
"v_exp_f32 v128, v128\n"
"v_mfma_f32_16x16x32_bf16 v[204:207], a[72:75], v[32:35], 0\n"
"v_exp_f32 v129, v129\n"
"v_mfma_f32_16x16x32_bf16 v[204:207], a[84:87], v[36:39], v[204:207]\n"
"v_exp_f32 v130, v130\n"
"v_mfma_f32_16x16x32_bf16 v[204:207], a[96:99], v[40:43], v[204:207]\n"
"v_exp_f32 v131, v131\n"
"v_mfma_f32_16x16x32_bf16 v[204:207], a[108:111], v[44:47], v[204:207]\n"
"v_cvt_pk_bf16_f32 v212, v212, v213\n"
"v_cvt_pk_bf16_f32 v213, v214, v215\n"
"v_mfma_f32_16x16x32_bf16 v[204:207], a[120:123], v[48:51], v[204:207]\n"
"v_cvt_pk_bf16_f32 v214, v208, v209\n"
"v_cvt_pk_bf16_f32 v215, v210, v211\n"
"v_mfma_f32_16x16x32_bf16 v[204:207], a[132:135], v[52:55], v[204:207]\n"
"ds_write_b128 v186, v[212:215] offset:60832\n"
"v_mfma_f32_16x16x32_bf16 v[208:211], a[76:79], v[32:35], 0\n"
"v_cvt_pk_bf16_f32 v90, v128, v130\n"
"v_cvt_pk_bf16_f32 v91, v129, v131\n"
"v_mfma_f32_16x16x32_bf16 v[208:211], a[88:91], v[36:39], v[208:211]\n"
"v_exp_f32 v132, v132\n"
"v_mfma_f32_16x16x32_bf16 v[208:211], a[100:103], v[40:43], v[208:211]\n"
"ds_read_b128 v[128:131], v180\n"
"v_cvt_pk_bf16_f32 v204, v204, v205\n"
"v_mfma_f32_16x16x32_bf16 v[208:211], a[112:115], v[44:47], v[208:211]\n"
"v_cvt_pk_bf16_f32 v205, v206, v207\n"
"v_subrev_u32 v183, s48, v183\n"
"v_mfma_f32_16x16x32_bf16 v[208:211], a[124:127], v[48:51], v[208:211]\n"
"ds_read_b64_tr_b16 a[242:243], v183 offset:13632\n"
"ds_read_b64_tr_b16 a[244:245], v182 offset:14400\n"
"v_mfma_f32_16x16x32_bf16 v[208:211], a[136:139], v[52:55], v[208:211]\n"
"ds_read_b64_tr_b16 a[246:247], v183 offset:14400\n"
"v_exp_f32 v133, v133\n"
"v_mfma_f32_16x16x32_bf16 v[212:215], a[144:147], v[32:35], 0\n"
"v_exp_f32 v134, v134\n"
"v_mfma_f32_16x16x32_bf16 v[212:215], a[156:159], v[36:39], v[212:215]\n"
"v_exp_f32 v135, v135\n"
"v_mfma_f32_16x16x32_bf16 v[212:215], a[168:171], v[40:43], v[212:215]\n"
"v_cvt_pk_bf16_f32 v92, v132, v134\n"
"v_cvt_pk_bf16_f32 v206, v208, v209\n"
"v_mfma_f32_16x16x32_bf16 v[212:215], a[180:183], v[44:47], v[212:215]\n"
"v_cvt_pk_bf16_f32 v207, v210, v211\n"
"v_cvt_pk_bf16_f32 v93, v133, v135\n"
"v_mfma_f32_16x16x32_bf16 v[212:215], a[192:195], v[48:51], v[212:215]\n"
"ds_write_b128 v186, v[204:207] offset:60896\n"
"v_mfma_f32_16x16x32_bf16 v[212:215], a[204:207], v[52:55], v[212:215]\n"
"v_bfi_b32 v80, v4, v80, v26\n"
"v_bfi_b32 v81, v5, v81, v26\n"
"v_smfmac_f32_16x16x64_bf16 v[252:255], v[82:85], v[224:231], v63\n"
"v_bfi_b32 v86, v6, v86, v26\n"
"v_smfmac_f32_16x16x64_bf16 v[252:255], v[90:93], v[216:223], v63\n"
"v_bfi_b32 v87, v7, v87, v26\n"
"v_mfma_f32_16x16x32_bf16 v[208:211], a[148:151], v[32:35], 0\n"
"v_bfi_b32 v88, v0, v88, v26\n"
"v_cvt_pk_bf16_f32 v144, v212, v213\n"
"v_mfma_f32_16x16x32_bf16 v[208:211], a[160:163], v[36:39], v[208:211]\n"
"v_cvt_pk_bf16_f32 v145, v214, v215\n"
"v_bfi_b32 v89, v1, v89, v26\n"
"v_mfma_f32_16x16x32_bf16 v[208:211], a[172:175], v[40:43], v[208:211]\n"
"v_cvt_pk_bf16_f32 v252, v252, v253\n"
"v_cvt_pk_bf16_f32 v253, v254, v255\n"
"v_mfma_f32_16x16x32_bf16 v[208:211], a[184:187], v[44:47], v[208:211]\n"
"s_waitcnt lgkmcnt(0)\n"
"v_bfi_b32 v94, v2, v94, v26\n"
"v_bfi_b32 v95, v3, v95, v26\n"
"v_mfma_f32_16x16x32_bf16 v[208:211], a[196:199], v[48:51], v[208:211]\n"
"s_barrier\n"
"v_mfma_f32_16x16x32_bf16 v[208:211], a[208:211], v[52:55], v[208:211]\n"
"s_add_u32 m0, s79, 130176\n"
"ds_read_b128 v[132:135], v180 offset:13504\n"
"buffer_load_dwordx4 v192, s[16:19], s43 offen lds\n"
"v_smfmac_f32_16x16x64_bf16 v[98:101], v[82:85], v[244:251], v63\n"
"ds_read_b64_tr_b16 v[224:225], v182 offset:40640\n"
"v_smfmac_f32_16x16x64_bf16 v[98:101], v[90:93], v[72:79], v63\n"
"ds_read_b64_tr_b16 v[226:227], v183 offset:40640\n"
"v_smfmac_f32_16x16x64_bf16 v[106:109], v[82:85], v[120:127], v63\n"
"ds_read_b128 v[120:123], v179 offset:13504\n"
"v_smfmac_f32_16x16x64_bf16 v[106:109], v[90:93], v[136:143], v63\n"
"ds_read_b128 v[124:127], v179 offset:27008\n"
"v_mfma_f32_16x16x32_bf16 v[82:85], v[172:175], v[82:85], 0\n"
"ds_read_b128 v[136:139], v181\n"
"ds_read_b128 v[140:143], v181 offset:13504\n"
"v_mfma_f32_16x16x32_bf16 v[90:93], v[172:175], v[90:93], v[82:85]\n"
"buffer_load_dword v237, v238, s[8:11], s78 offen\n"
"v_cvt_pk_bf16_f32 v146, v208, v209\n"
"v_mfma_f32_16x16x32_bf16 v[204:207], a[152:155], v[32:35], 0\n"
"s_add_u32 s78, s78, s33\n"
"ds_read_b128 v[32:35], v188 offset:6144\n"
"ds_read_b64 v[82:83], v178 offset:13568\n"
"v_mfma_f32_16x16x32_bf16 v[204:207], a[164:167], v[36:39], v[204:207]\n"
"v_cvt_pk_bf16_f32 v147, v210, v211\n"
"v_cvt_pk_bf16_f32 v98, v98, v99\n"
"v_mfma_f32_16x16x32_bf16 v[204:207], a[176:179], v[40:43], v[204:207]\n"
"s_add_u32 m0, s79, 130176\n"
"ds_read_b128 v[36:39], v189 offset:6144\n"
"ds_read_b128 v[40:43], v188 offset:7168\n"
"v_mfma_f32_16x16x32_bf16 v[204:207], a[188:191], v[44:47], v[204:207]\n"
"buffer_load_dwordx4 v192, s[16:19], s43 offen offset:1024 lds\n"
"ds_read_b128 v[44:47], v189 offset:7168\n"
"v_mfma_f32_16x16x32_bf16 v[204:207], a[200:203], v[48:51], v[204:207]\n"
"v_rcp_f32 v171, v90\n"
"v_mfma_f32_16x16x32_bf16 v[204:207], a[212:215], v[52:55], v[204:207]\n"
"ds_read_b128 v[48:51], v188 offset:8192\n"
"ds_read_b128 v[52:55], v189 offset:8192\n"
"v_cvt_pk_bf16_f32 v171, v171, v171\n"
"v_and_b32 v92, v171, v176\n"
"v_and_b32 v93, v171, v177\n"
"v_cvt_pk_bf16_f32 v99, v100, v101\n"
"v_cvt_pk_bf16_f32 v106, v106, v107\n"
"v_mfma_f32_16x16x16_bf16 v[252:255], v[252:253], v[92:93], 0\n"
"s_waitcnt lgkmcnt(6)\n"
"ds_read_b64 v[84:85], v179 offset:13568\n"
"ds_read_b64 v[90:91], v178 offset:27072\n"
"v_mfma_f32_16x16x32_bf16 v[212:215], a[0:3], v[32:35], 0\n"
"s_waitcnt lgkmcnt(7)\n"
"buffer_load_dword v237, v238, s[8:11], s78 offen\n"
"v_cvt_pk_bf16_f32 v158, v204, v205\n"
"v_mfma_f32_16x16x32_bf16 v[112:115], v[112:115], v[148:151], 0\n"
"s_waitcnt lgkmcnt(6)\n"
"v_cvt_pk_bf16_f32 v159, v206, v207\n"
"v_cvt_pk_bf16_f32 v107, v108, v109\n"
"v_mfma_f32_16x16x32_bf16 v[112:115], v[80:83], v[160:163], v[112:115]\n"
"s_add_u32 s78, s78, s33\n"
"ds_read_b64_tr_b16 v[228:229], v182 offset:41408\n"
"ds_read_b64_tr_b16 v[230:231], v183 offset:41408\n"
"v_mfma_f32_16x16x32_bf16 v[212:215], a[12:15], v[36:39], v[212:215]\n"
"s_waitcnt lgkmcnt(6)\n"
"ds_read_b64_tr_b16 v[244:245], v182 offset:40648\n"
"v_cvt_pk_bf16_f32 v252, v252, v253\n"
"v_mfma_f32_16x16x32_bf16 v[212:215], a[24:27], v[40:43], v[212:215]\n"
"s_add_u32 m0, s79, 130176\n"
"ds_read_b64_tr_b16 v[246:247], v183 offset:40648\n"
"buffer_load_dwordx4 v192, s[16:19], s43 offen offset:2048 lds\n"
"v_mfma_f32_16x16x32_bf16 v[212:215], a[36:39], v[44:47], v[212:215]\n"
"s_waitcnt lgkmcnt(7)\n"
"ds_read_b64_tr_b16 v[248:249], v182 offset:41416\n"
"v_cvt_pk_bf16_f32 v253, v254, v255\n"
"v_mfma_f32_16x16x32_bf16 v[212:215], a[48:51], v[48:51], v[212:215]\n"
"s_waitcnt lgkmcnt(6)\n"
"ds_read_b64_tr_b16 v[250:251], v183 offset:41416\n"
"v_bfi_b32 v96, v0, v96, v26\n"
"v_mfma_f32_16x16x32_bf16 v[212:215], a[60:63], v[52:55], v[212:215]\n"
"s_waitcnt lgkmcnt(6)\n"
"ds_read_b64_tr_b16 a[248:249], v182 offset:40584\n"
"v_bfi_b32 v97, v1, v97, v26\n"
"v_mfma_f32_16x16x32_bf16 v[120:123], v[120:123], v[148:151], 0\n"
"s_add_u32 s43, s79, 18432\n"
"ds_read_b64_tr_b16 a[250:251], v183 offset:40584\n"
"v_bfi_b32 v102, v2, v102, v26\n"
"v_mfma_f32_16x16x32_bf16 v[120:123], v[84:87], v[162:165], v[120:123]\n"
"ds_read_b64_tr_b16 a[252:253], v182 offset:41352\n"
"v_cvt_pk_bf16_f32 v212, v212, v213\n"
"v_mfma_f32_16x16x32_bf16 v[116:119], v[116:119], v[148:151], 0\n"
"ds_read_b64_tr_b16 a[254:255], v183 offset:41352\n"
"v_cvt_pk_bf16_f32 v213, v214, v215\n"
"v_mfma_f32_16x16x32_bf16 v[116:119], v[88:91], v[160:163], v[116:119]\n"
"v_bfi_b32 v103, v3, v103, v26\n"
"v_bfi_b32 v104, v8, v104, v26\n"
"v_mfma_f32_16x16x16_bf16 v[98:101], v[98:99], v[92:93], 0\n"
"s_add_u32 m0, s34, 6144\n"
"v_bfi_b32 v105, v9, v105, v26\n"
"v_bfi_b32 v110, v10, v110, v26\n"
"v_mfma_f32_16x16x16_bf16 v[106:109], v[106:107], v[92:93], 0\n"
"buffer_load_dwordx4 v190, s[8:11], s32 offen lds\n"
"ds_read_b64 v[92:93], v179 offset:27072\n"
"v_mfma_f32_16x16x32_bf16 v[208:211], a[4:7], v[32:35], 0\n"
"v_bfi_b32 v111, v11, v111, v26\n"
"v_cvt_pk_bf16_f32 v254, v98, v99\n"
"v_mfma_f32_16x16x32_bf16 v[208:211], a[16:19], v[36:39], v[208:211]\n"
"ds_read_b64 v[98:99], v180 offset:64\n"
"v_cvt_pk_bf16_f32 v255, v100, v101\n"
"v_mfma_f32_16x16x32_bf16 v[208:211], a[28:31], v[40:43], v[208:211]\n"
"ds_read_b64 v[100:101], v181 offset:64\n"
"v_cvt_pk_bf16_f32 v106, v106, v107\n"
"v_mfma_f32_16x16x32_bf16 v[208:211], a[40:43], v[44:47], v[208:211]\n"
"buffer_store_dwordx4 v[252:255], v192, s[28:31], s37 offen sc1\n"
"v_mfma_f32_16x16x32_bf16 v[208:211], a[52:55], v[48:51], v[208:211]\n"
"v_cvt_pk_bf16_f32 v107, v108, v109\n"
"v_max_f32 v112, v112, v120\n"
"v_mfma_f32_16x16x32_bf16 v[208:211], a[64:67], v[52:55], v[208:211]\n"
"s_waitcnt lgkmcnt(2)\n"
"ds_read_b64 v[108:109], v181 offset:13568\n"
"v_max_f32 v113, v113, v121\n"
"v_mfma_f32_16x16x32_bf16 v[204:207], a[8:11], v[32:35], 0\n"
"ds_read_b64_tr_b16 v[120:121], v182 offset:13640\n"
"v_max_f32 v114, v114, v122\n"
"v_mfma_f32_16x16x32_bf16 v[204:207], a[20:23], v[36:39], v[204:207]\n"
"s_waitcnt lgkmcnt(3)\n"
"v_cvt_pk_bf16_f32 v214, v208, v209\n"
"v_cvt_pk_bf16_f32 v215, v210, v211\n"
"v_mfma_f32_16x16x32_bf16 v[124:127], v[124:127], v[148:151], 0\n"
"s_waitcnt lgkmcnt(2)\n"
"ds_write_b128 v187, v[212:215]\n"
"v_mfma_f32_16x16x32_bf16 v[124:127], v[92:95], v[162:165], v[124:127]\n"
"s_add_u32 m0, s34, 9216\n"
"v_max_f32 v115, v115, v123\n"
"v_max3_f32 v170, v112, v113, v114\n"
"v_mfma_f32_16x16x32_bf16 v[128:131], v[128:131], v[148:151], 0\n"
"v_mov_b64 v[252:253], 0\n"
"v_mov_b64 v[254:255], 0\n"
"v_mfma_f32_16x16x32_bf16 v[128:131], v[96:99], v[160:163], v[128:131]\n"
"s_waitcnt lgkmcnt(2)\n"
"buffer_load_dwordx4 v191, s[8:11], s32 offen lds\n"
"ds_read_b64_tr_b16 v[122:123], v183 offset:13640\n"
"v_mfma_f32_16x16x32_bf16 v[136:139], v[136:139], v[148:151], 0\n"
"s_add_u32 s32, s32, s33\n"
"v_max_f32 v116, v116, v124\n"
"v_max_f32 v117, v117, v125\n"
"v_mfma_f32_16x16x32_bf16 v[136:139], v[100:103], v[162:165], v[136:139]\n"
"ds_read_b64_tr_b16 v[124:125], v182 offset:14408\n"
"v_max_f32 v118, v118, v126\n"
"v_mfma_f32_16x16x32_bf16 v[140:143], v[140:143], v[148:151], 0\n"
"v_max_f32 v119, v119, v127\n"
"v_max3_f32 v170, v170, v115, v116\n"
"v_mfma_f32_16x16x32_bf16 v[140:143], v[108:111], v[162:165], v[140:143]\n"
"buffer_store_dwordx2 v[106:107], v193, s[28:31], s37 offen offset:1024 sc1\n"
"v_mfma_f32_16x16x32_bf16 v[204:207], a[32:35], v[40:43], v[204:207]\n"
"s_add_u32 s37, s37, 1572864\n"
"ds_read_b64 v[106:107], v180 offset:13568\n"
"ds_read_b64_tr_b16 v[126:127], v183 offset:14408\n"
"v_mfma_f32_16x16x32_bf16 v[204:207], a[44:47], v[44:47], v[204:207]\n"
"v_max3_f32 v170, v170, v117, v118\n"
"v_max_f32 v128, v128, v136\n"
"v_mfma_f32_16x16x32_bf16 v[204:207], a[56:59], v[48:51], v[204:207]\n"
"v_max_f32 v129, v129, v137\n"
"v_max_f32 v130, v130, v138\n"
"v_mfma_f32_16x16x32_bf16 v[204:207], a[68:71], v[52:55], v[204:207]\n"
"ds_read_b64_tr_b16 v[136:137], v182 offset:13576\n"
"v_max_f32 v131, v131, v139\n"
"v_mfma_f32_16x16x32_bf16 v[208:211], a[80:83], v[32:35], 0\n"
"s_add_u32 m0, s34, 7168\n"
"ds_read_b64_tr_b16 v[138:139], v183 offset:13576\n"
"v_max3_f32 v170, v170, v119, v128\n"
"v_mfma_f32_16x16x32_bf16 v[208:211], a[92:95], v[36:39], v[208:211]\n"
"ds_read_b64 v[80:81], v20 offset:272\n"
"buffer_load_dwordx4 v190, s[8:11], s0 offen lds\n"
"v_mfma_f32_16x16x32_bf16 v[208:211], a[104:107], v[40:43], v[208:211]\n"
"v_cvt_pk_bf16_f32 v204, v204, v205\n"
"v_cvt_pk_bf16_f32 v205, v206, v207\n"
"v_mfma_f32_16x16x32_bf16 v[208:211], a[116:119], v[44:47], v[208:211]\n"
"s_waitcnt lgkmcnt(4)\n"
"v_max3_f32 v170, v170, v129, v130\n"
"v_mov_b64 v[98:99], 0\n"
"v_mfma_f32_16x16x32_bf16 v[132:135], v[132:135], v[148:151], 0\n"
"ds_read_b64 v[86:87], v20 offset:336\n"
"ds_read_b64 v[110:111], v20 offset:1152\n"
"v_mfma_f32_16x16x32_bf16 v[132:135], v[104:107], v[160:163], v[132:135]\n"
"ds_read_b64 v[104:105], v20 offset:1088\n"
"v_mov_b64 v[100:101], 0\n"
"v_mfma_f32_16x16x32_bf16 v[208:211], a[128:131], v[48:51], v[208:211]\n"
"v_mov_b64 v[106:107], 0\n"
"v_mov_b64 v[108:109], 0\n"
"v_mfma_f32_16x16x32_bf16 v[208:211], a[140:143], v[52:55], v[208:211]\n"
"s_waitcnt lgkmcnt(3)\n"
"s_nop 0\n"
"v_max_f32 v132, v132, v140\n"
"v_mfma_f32_16x16x32_bf16 v[212:215], a[72:75], v[32:35], 0\n"
"v_max_f32 v133, v133, v141\n"
"v_max_f32 v134, v134, v142\n"
"v_mfma_f32_16x16x32_bf16 v[212:215], a[84:87], v[36:39], v[212:215]\n"
"s_add_u32 m0, s34, 10240\n"
"ds_read_b64_tr_b16 v[140:141], v182 offset:14344\n"
"buffer_load_dwordx4 v191, s[8:11], s0 offen lds\n"
"v_mfma_f32_16x16x32_bf16 v[212:215], a[96:99], v[40:43], v[212:215]\n"
"s_waitcnt lgkmcnt(3)\n"
"v_cvt_pk_bf16_f32 v206, v208, v209\n"
"v_cvt_pk_bf16_f32 v207, v210, v211\n"
"v_mfma_f32_16x16x32_bf16 v[212:215], a[108:111], v[44:47], v[212:215]\n"
"s_waitcnt lgkmcnt(1)\n"
"ds_write_b128 v187, v[204:207] offset:64\n"
"v_mfma_f32_16x16x32_bf16 v[212:215], a[120:123], v[48:51], v[212:215]\n"
"s_add_u32 s0, s0, s33\n"
"v_max_f32 v135, v135, v143\n"
"v_max3_f32 v170, v170, v131, v132\n"
"v_mfma_f32_16x16x32_bf16 v[212:215], a[132:135], v[52:55], v[212:215]\n"
"v_max3_f32 v170, v170, v133, v134\n"
"v_max_f32 v171, v170, v135\n"
"v_mfma_f32_16x16x32_bf16 v[208:211], a[76:79], v[32:35], 0\n"
"ds_read_b64_tr_b16 v[142:143], v183 offset:14344\n"
"v_max_f32 v170, v170, v135\n"
"v_mfma_f32_16x16x32_bf16 v[208:211], a[88:91], v[36:39], v[208:211]\n"
"s_nop 0\n"
"v_permlane16_swap_b32 v170, v171\n"
"v_mfma_f32_16x16x32_bf16 v[208:211], a[100:103], v[40:43], v[208:211]\n"
"s_add_u32 m0, s34, 8192\n"
"v_cvt_pk_bf16_f32 v212, v212, v213\n"
"v_cvt_pk_bf16_f32 v213, v214, v215\n"
"v_mfma_f32_16x16x32_bf16 v[208:211], a[112:115], v[44:47], v[208:211]\n"
"buffer_load_dwordx4 v190, s[8:11], s1 offen lds\n"
"v_max_f32 v170, v170, v171\n"
"v_mfma_f32_16x16x32_bf16 v[208:211], a[124:127], v[48:51], v[208:211]\n"
"v_mov_b32 v171, v170\n"
"v_bfi_b32 v80, v12, v80, v26\n"
"v_mfma_f32_16x16x32_bf16 v[208:211], a[136:139], v[52:55], v[208:211]\n"
"v_permlane32_swap_b32 v170, v171\n"
"v_max_f32 v170, v170, v171\n"
"v_mul_f32 v170, 0xbe553b95, v170\n"
"v_pk_fma_f32 v[112:113], v[112:113], s[70:71], v[170:171] op_sel_hi:[1,0,0]\n"
"v_pk_fma_f32 v[114:115], v[114:115], s[70:71], v[170:171] op_sel_hi:[1,0,0]\n"
"v_pk_fma_f32 v[116:117], v[116:117], s[70:71], v[170:171] op_sel_hi:[1,0,0]\n"
"v_pk_fma_f32 v[118:119], v[118:119], s[70:71], v[170:171] op_sel_hi:[1,0,0]\n"
"v_pk_fma_f32 v[128:129], v[128:129], s[70:71], v[170:171] op_sel_hi:[1,0,0]\n"
"v_pk_fma_f32 v[130:131], v[130:131], s[70:71], v[170:171] op_sel_hi:[1,0,0]\n"
"v_pk_fma_f32 v[132:133], v[132:133], s[70:71], v[170:171] op_sel_hi:[1,0,0]\n"
"v_pk_fma_f32 v[134:135], v[134:135], s[70:71], v[170:171] op_sel_hi:[1,0,0]\n"
"v_mfma_f32_16x16x32_bf16 v[204:207], a[144:147], v[32:35], 0\n"
"v_cvt_pk_bf16_f32 v214, v208, v209\n"
"v_cvt_pk_bf16_f32 v215, v210, v211\n"
"v_mfma_f32_16x16x32_bf16 v[204:207], a[156:159], v[36:39], v[204:207]\n"
"ds_write_b128 v187, v[212:215] offset:128\n"
"v_mfma_f32_16x16x32_bf16 v[204:207], a[168:171], v[40:43], v[204:207]\n"
"s_add_u32 m0, s34, 11264\n"
"v_exp_f32 v112, v112\n"
"v_mfma_f32_16x16x32_bf16 v[204:207], a[180:183], v[44:47], v[204:207]\n"
"buffer_load_dwordx4 v191, s[8:11], s1 offen lds\n"
"v_exp_f32 v113, v113\n"
"v_mfma_f32_16x16x32_bf16 v[204:207], a[192:195], v[48:51], v[204:207]\n"
"s_add_u32 s1, s1, s33\n"
"v_exp_f32 v114, v114\n"
"v_mfma_f32_16x16x32_bf16 v[204:207], a[204:207], v[52:55], v[204:207]\n"
"v_exp_f32 v115, v115\n"
"v_mfma_f32_16x16x32_bf16 v[208:211], a[148:151], v[32:35], 0\n"
"v_cvt_pk_bf16_f32 v82, v112, v114\n"
"v_cvt_pk_bf16_f32 v83, v113, v115\n"
"v_mfma_f32_16x16x32_bf16 v[208:211], a[160:163], v[36:39], v[208:211]\n"
"ds_read_b128 v[112:115], v178 offset:27008\n"
"v_exp_f32 v116, v116\n"
"v_mfma_f32_16x16x32_bf16 v[208:211], a[172:175], v[40:43], v[208:211]\n"
"s_waitcnt vmcnt(8) lgkmcnt(1)\n"
"v_exp_f32 v117, v117\n"
"v_mfma_f32_16x16x32_bf16 v[208:211], a[184:187], v[44:47], v[208:211]\n"
"s_barrier\n"
"v_mfma_f32_16x16x32_bf16 v[208:211], a[196:199], v[48:51], v[208:211]\n"
"s_add_u32 m0, s79, 142464\n"
"buffer_load_dword v201, off, s[28:31], s40 offset:128 sc1\n"
"ds_read_b64_tr_b16 a[216:217], v236\n"
"v_mfma_f32_16x16x32_bf16 v[208:211], a[208:211], v[52:55], v[208:211]\n"
".Lic1%=:\n"
"s_cbranch_execz .Lib0%=\n"
"v_exp_f32 v118, v118\n"
"v_mfma_f32_16x16x32_bf16 v[212:215], a[152:155], v[32:35], 0\n"
"buffer_load_dwordx4 v192, s[16:19], s43 offen lds\n"
"ds_read_b128 v[32:35], v188\n"
"v_mfma_f32_16x16x32_bf16 v[212:215], a[164:167], v[36:39], v[212:215]\n"
"ds_read_b128 v[36:39], v189\n"
"v_cvt_pk_bf16_f32 v84, v116, v118\n"
"v_mfma_f32_16x16x32_bf16 v[212:215], a[176:179], v[40:43], v[212:215]\n"
"ds_read_b128 v[40:43], v188 offset:1024\n"
"v_cvt_pk_bf16_f32 v148, v204, v205\n"
"v_mfma_f32_16x16x32_bf16 v[212:215], a[188:191], v[44:47], v[212:215]\n"
"ds_read_b128 v[44:47], v189 offset:1024\n"
"v_cvt_pk_bf16_f32 v149, v206, v207\n"
"v_mfma_f32_16x16x32_bf16 v[212:215], a[200:203], v[48:51], v[212:215]\n"
"ds_read_b128 v[48:51], v188 offset:2048\n"
"v_exp_f32 v119, v119\n"
"v_mfma_f32_16x16x32_bf16 v[212:215], a[212:215], v[52:55], v[212:215]\n"
"ds_read_b128 v[52:55], v189 offset:2048\n"
"v_cvt_pk_bf16_f32 v85, v117, v119\n"
"v_exp_f32 v128, v128\n"
"s_nop 0\n"
"v_smfmac_f32_16x16x64_bf16 v[252:255], v[82:85], a[240:247], v63\n"
"s_waitcnt lgkmcnt(5)\n"
"ds_read_b128 v[116:119], v180\n"
"v_smfmac_f32_16x16x64_bf16 v[98:101], v[82:85], v[120:127], v63\n"
"s_waitcnt lgkmcnt(5)\n"
"ds_read_b128 v[120:123], v179 offset:27008\n"
"v_smfmac_f32_16x16x64_bf16 v[106:109], v[82:85], v[136:143], v63\n"
"s_add_u32 m0, s79, 142464\n"
"s_nop 0\n"
"buffer_load_dwordx4 v192, s[16:19], s43 offen offset:1024 lds\n"
"v_mfma_f32_16x16x32_bf16 v[82:85], v[172:175], v[82:85], 0\n"
"ds_read_b128 v[124:127], v181\n"
"ds_read_b128 v[136:139], v180 offset:13504\n"
"v_mfma_f32_16x16x32_bf16 v[204:207], a[0:3], v[32:35], 0\n"
"ds_read_b128 v[140:143], v180 offset:27008\n"
"v_cvt_pk_bf16_f32 v150, v208, v209\n"
"v_mfma_f32_16x16x32_bf16 v[204:207], a[12:15], v[36:39], v[204:207]\n"
"s_waitcnt lgkmcnt(8)\n"
"ds_read_b64_tr_b16 a[240:241], v182 offset:27136\n"
"v_cvt_pk_bf16_f32 v151, v210, v211\n"
"v_mfma_f32_16x16x32_bf16 v[204:207], a[24:27], v[40:43], v[204:207]\n"
"s_waitcnt lgkmcnt(8)\n"
"ds_read_b64_tr_b16 a[242:243], v183 offset:27136\n"
"buffer_load_dword v237, v238, s[8:11], s78 offen\n"
"v_mfma_f32_16x16x32_bf16 v[204:207], a[36:39], v[44:47], v[204:207]\n"
"s_waitcnt lgkmcnt(8)\n"
"ds_read_b64_tr_b16 a[244:245], v182 offset:27904\n"
"v_exp_f32 v129, v129\n"
"v_mfma_f32_16x16x32_bf16 v[204:207], a[48:51], v[48:51], v[204:207]\n"
"s_waitcnt lgkmcnt(8)\n"
"ds_read_b64_tr_b16 a[246:247], v183 offset:27904\n"
"v_exp_f32 v130, v130\n"
"v_mfma_f32_16x16x32_bf16 v[204:207], a[60:63], v[52:55], v[204:207]\n"
"s_add_u32 m0, s79, 142464\n"
"v_exp_f32 v131, v131\n"
"v_mfma_f32_16x16x32_bf16 v[208:211], a[4:7], v[32:35], 0\n"
"s_add_u32 s78, s78, s33\n"
"buffer_load_dwordx4 v192, s[16:19], s43 offen offset:2048 lds\n"
"v_cvt_pk_bf16_f32 v90, v128, v130\n"
"v_mfma_f32_16x16x32_bf16 v[208:211], a[16:19], v[36:39], v[208:211]\n"
"s_add_u32 s43, s79, 36864\n"
"v_cvt_pk_bf16_f32 v91, v129, v131\n"
"v_cvt_pk_bf16_f32 v162, v212, v213\n"
"v_mfma_f32_16x16x32_bf16 v[208:211], a[28:31], v[40:43], v[208:211]\n"
"ds_read_b128 v[128:131], v181 offset:13504\n"
"ds_read_b64_tr_b16 a[218:219], v236 offset:6144\n"
"v_mfma_f32_16x16x32_bf16 v[208:211], a[40:43], v[44:47], v[208:211]\n"
"v_exp_f32 v132, v132\n"
"v_mfma_f32_16x16x32_bf16 v[208:211], a[52:55], v[48:51], v[208:211]\n"
"v_exp_f32 v133, v133\n"
"v_mfma_f32_16x16x32_bf16 v[208:211], a[64:67], v[52:55], v[208:211]\n"
"v_exp_f32 v134, v134\n"
"v_exp_f32 v135, v135\n"
"v_cvt_pk_bf16_f32 v92, v132, v134\n"
"v_cvt_pk_bf16_f32 v93, v133, v135\n"
"v_cvt_pk_bf16_f32 v163, v214, v215\n"
"v_mfma_f32_16x16x32_bf16 v[212:215], a[8:11], v[32:35], 0\n"
"s_add_u32 m0, s34, 0\n"
"ds_read_b128 v[132:135], v181 offset:27008\n"
"buffer_load_dwordx4 v190, s[8:11], s32 offen lds\n"
"v_smfmac_f32_16x16x64_bf16 v[252:255], v[90:93], v[224:231], v63\n"
"ds_read_b64_tr_b16 v[216:217], v182 offset:54144\n"
"v_smfmac_f32_16x16x64_bf16 v[98:101], v[90:93], v[244:251], v63\n"
"ds_read_b64_tr_b16 v[218:219], v183 offset:54144\n"
"v_smfmac_f32_16x16x64_bf16 v[106:109], v[90:93], a[248:255], v63\n"
"ds_read_b64_tr_b16 v[220:221], v182 offset:54912\n"
"v_mfma_f32_16x16x32_bf16 v[90:93], v[172:175], v[90:93], v[82:85]\n"
"ds_read_b64_tr_b16 v[222:223], v183 offset:54912\n"
"ds_read_b64_tr_b16 v[72:73], v182 offset:54152\n"
"v_mfma_f32_16x16x32_bf16 v[212:215], a[20:23], v[36:39], v[212:215]\n"
"v_cvt_pk_bf16_f32 v252, v252, v253\n"
"v_cvt_pk_bf16_f32 v253, v254, v255\n"
"v_mfma_f32_16x16x32_bf16 v[212:215], a[32:35], v[40:43], v[212:215]\n"
"ds_read_b64 v[82:83], v178 offset:27072\n"
"ds_read_b64 v[84:85], v179 offset:27072\n"
"v_mfma_f32_16x16x32_bf16 v[212:215], a[44:47], v[44:47], v[212:215]\n"
"v_rcp_f32 v171, v90\n"
"v_mfma_f32_16x16x32_bf16 v[212:215], a[56:59], v[48:51], v[212:215]\n"
"s_add_u32 m0, s34, 3072\n"
"ds_read_b64 v[90:91], v180 offset:64\n"
"buffer_load_dwordx4 v191, s[8:11], s32 offen lds\n"
"v_mfma_f32_16x16x32_bf16 v[212:215], a[68:71], v[52:55], v[212:215]\n"
"s_add_u32 s32, s32, s33\n"
"v_cvt_pk_bf16_f32 v171, v171, v171\n"
"v_and_b32 v92, v171, v176\n"
"v_and_b32 v93, v171, v177\n"
"v_cvt_pk_bf16_f32 v98, v98, v99\n"
"v_cvt_pk_bf16_f32 v99, v100, v101\n"
"v_mfma_f32_16x16x16_bf16 v[252:255], v[252:253], v[92:93], 0\n"
"ds_read_b64_tr_b16 v[74:75], v183 offset:54152\n"
"ds_read_b64_tr_b16 v[76:77], v182 offset:54920\n"
"v_mfma_f32_16x16x16_bf16 v[98:101], v[98:99], v[92:93], 0\n"
"v_cvt_pk_bf16_f32 v106, v106, v107\n"
"v_cvt_pk_bf16_f32 v107, v108, v109\n"
"v_cvt_pk_bf16_f32 v204, v204, v205\n"
"v_cvt_pk_bf16_f32 v205, v206, v207\n"
"v_mfma_f32_16x16x16_bf16 v[106:109], v[106:107], v[92:93], 0\n"
"s_waitcnt lgkmcnt(4)\n"
"ds_read_b64 v[92:93], v181 offset:64\n"
"ds_read_b64_tr_b16 v[78:79], v183 offset:54920\n"
"v_mfma_f32_16x16x32_bf16 v[112:115], v[112:115], v[152:155], 0\n"
"s_waitcnt lgkmcnt(5)\n"
"ds_read_b64_tr_b16 v[8:9], v182 offset:54088\n"
"v_cvt_pk_bf16_f32 v252, v252, v253\n"
"v_mfma_f32_16x16x32_bf16 v[120:123], v[120:123], v[152:155], 0\n"
"s_waitcnt lgkmcnt(5)\n"
"ds_read_b64_tr_b16 v[10:11], v183 offset:54088\n"
"v_cvt_pk_bf16_f32 v253, v254, v255\n"
"v_mfma_f32_16x16x32_bf16 v[116:119], v[116:119], v[152:155], 0\n"
"s_add_u32 m0, s34, 1024\n"
"v_cvt_pk_bf16_f32 v254, v98, v99\n"
"v_cvt_pk_bf16_f32 v255, v100, v101\n"
"v_mfma_f32_16x16x32_bf16 v[116:119], v[88:91], v[164:167], v[116:119]\n"
"buffer_load_dwordx4 v190, s[8:11], s0 offen lds\n"
"ds_read_b64 v[98:99], v180 offset:13568\n"
"ds_read_b64 v[100:101], v181 offset:13568\n"
"v_cvt_pk_bf16_f32 v106, v106, v107\n"
"v_cvt_pk_bf16_f32 v107, v108, v109\n"
"v_bfi_b32 v81, v13, v81, v26\n"
"v_bfi_b32 v86, v14, v86, v26\n"
"v_bfi_b32 v87, v15, v87, v26\n"
"v_mfma_f32_16x16x32_bf16 v[112:115], v[80:83], v[164:167], v[112:115]\n"
"s_waitcnt lgkmcnt(5)\n"
"ds_read_b64 v[108:109], v181 offset:27072\n"
"ds_read_b64_tr_b16 v[12:13], v182 offset:54856\n"
"v_mfma_f32_16x16x32_bf16 v[120:123], v[84:87], v[166:169], v[120:123]\n"
"ds_read_b64_tr_b16 v[14:15], v183 offset:54856\n"
"v_bfi_b32 v104, v16, v104, v26\n"
"v_mfma_f32_16x16x32_bf16 v[124:127], v[124:127], v[152:155], 0\n"
"buffer_store_dwordx4 v[252:255], v192, s[28:31], s37 offen sc1\n"
"v_mfma_f32_16x16x32_bf16 v[124:127], v[92:95], v[166:169], v[124:127]\n"
"v_bfi_b32 v105, v17, v105, v26\n"
"v_bfi_b32 v110, v18, v110, v26\n"
"v_bfi_b32 v111, v19, v111, v26\n"
"v_cvt_pk_bf16_f32 v206, v208, v209\n"
"v_cvt_pk_bf16_f32 v207, v210, v211\n"
"v_mfma_f32_16x16x32_bf16 v[208:211], a[80:83], v[32:35], 0\n"
"s_waitcnt lgkmcnt(3)\n"
"ds_write_b128 v186, v[204:207] offset:6752\n"
"v_mfma_f32_16x16x32_bf16 v[136:139], v[136:139], v[152:155], 0\n"
"s_waitcnt lgkmcnt(3)\n"
"v_cvt_pk_bf16_f32 v212, v212, v213\n"
"v_cvt_pk_bf16_f32 v213, v214, v215\n"
"v_mfma_f32_16x16x32_bf16 v[136:139], v[96:99], v[164:167], v[136:139]\n"
"v_max_f32 v112, v112, v120\n"
"v_max_f32 v113, v113, v121\n"
"v_mfma_f32_16x16x32_bf16 v[128:131], v[128:131], v[152:155], 0\n"
"ds_read_b64_tr_b16 v[120:121], v182 offset:27144\n"
"ds_read_b64_tr_b16 a[220:221], v236 offset:8\n"
"v_mfma_f32_16x16x32_bf16 v[128:131], v[100:103], v[166:169], v[128:131]\n"
"s_add_u32 m0, s34, 4096\n"
"v_max_f32 v114, v114, v122\n"
"v_max_f32 v115, v115, v123\n"
"v_mfma_f32_16x16x32_bf16 v[132:135], v[132:135], v[152:155], 0\n"
"buffer_load_dwordx4 v191, s[8:11], s0 offen lds\n"
"ds_read_b64_tr_b16 v[122:123], v183 offset:27144\n"
"v_mfma_f32_16x16x32_bf16 v[132:135], v[108:111], v[166:169], v[132:135]\n"
"s_add_u32 s0, s0, s33\n"
"ds_read_b64_tr_b16 a[222:223], v236 offset:6152\n"
"v_max3_f32 v170, v112, v113, v114\n"
"v_mfma_f32_16x16x32_bf16 v[208:211], a[92:95], v[36:39], v[208:211]\n"
"v_max_f32 v116, v116, v124\n"
"v_max_f32 v117, v117, v125\n"
"v_mfma_f32_16x16x32_bf16 v[208:211], a[104:107], v[40:43], v[208:211]\n"
"ds_read_b64_tr_b16 v[124:125], v182 offset:27912\n"
"v_max_f32 v118, v118, v126\n"
"v_mfma_f32_16x16x32_bf16 v[208:211], a[116:119], v[44:47], v[208:211]\n"
"buffer_store_dwordx2 v[106:107], v193, s[28:31], s37 offen offset:1024 sc1\n"
"v_mfma_f32_16x16x32_bf16 v[208:211], a[128:131], v[48:51], v[208:211]\n"
"s_add_u32 s37, s37, 1572864\n"
"ds_read_b64 v[106:107], v180 offset:27072\n"
"ds_read_b64_tr_b16 a[224:225], v235\n"
"v_mfma_f32_16x16x32_bf16 v[208:211], a[140:143], v[52:55], v[208:211]\n"
"v_max_f32 v119, v119, v127\n"
"v_max3_f32 v170, v170, v115, v116\n"
"v_mfma_f32_16x16x32_bf16 v[204:207], a[72:75], v[32:35], 0\n"
"ds_read_b64_tr_b16 v[126:127], v183 offset:27912\n"
"v_max3_f32 v170, v170, v117, v118\n"
"v_mfma_f32_16x16x32_bf16 v[204:207], a[84:87], v[36:39], v[204:207]\n"
"ds_read_b64_tr_b16 a[226:227], v235 offset:6144\n"
"v_max_f32 v136, v136, v128\n"
"v_mfma_f32_16x16x32_bf16 v[204:207], a[96:99], v[40:43], v[204:207]\n"
"s_add_u32 m0, s34, 2048\n"
"v_cvt_pk_bf16_f32 v214, v208, v209\n"
"v_cvt_pk_bf16_f32 v215, v210, v211\n"
"v_mfma_f32_16x16x32_bf16 v[204:207], a[108:111], v[44:47], v[204:207]\n"
"ds_write_b128 v186, v[212:215] offset:6816\n"
"v_mfma_f32_16x16x32_bf16 v[204:207], a[120:123], v[48:51], v[204:207]\n"
"s_waitcnt lgkmcnt(4)\n"
"buffer_load_dwordx4 v190, s[8:11], s1 offen lds\n"
"v_max_f32 v137, v137, v129\n"
"v_mfma_f32_16x16x32_bf16 v[140:143], v[140:143], v[152:155], 0\n"
"v_max_f32 v138, v138, v130\n"
"v_max_f32 v139, v139, v131\n"
"v_mfma_f32_16x16x32_bf16 v[140:143], v[104:107], v[164:167], v[140:143]\n"
"ds_read_b64_tr_b16 v[128:129], v182 offset:27080\n"
"ds_read_b64_tr_b16 v[130:131], v183 offset:27080\n"
"v_mfma_f32_16x16x32_bf16 v[204:207], a[132:135], v[52:55], v[204:207]\n"
"v_max3_f32 v170, v170, v119, v136\n"
"v_max3_f32 v170, v170, v137, v138\n"
"v_mfma_f32_16x16x32_bf16 v[208:211], a[76:79], v[32:35], 0\n"
"v_mov_b64 v[252:253], 0\n"
"v_mov_b64 v[254:255], 0\n"
"v_mfma_f32_16x16x32_bf16 v[208:211], a[88:91], v[36:39], v[208:211]\n"
"v_max_f32 v140, v140, v132\n"
"v_max_f32 v141, v141, v133\n"
"v_mfma_f32_16x16x32_bf16 v[208:211], a[100:103], v[40:43], v[208:211]\n"
"ds_read_b64_tr_b16 v[132:133], v182 offset:27848\n"
"v_max_f32 v142, v142, v134\n"
"v_mfma_f32_16x16x32_bf16 v[208:211], a[112:115], v[44:47], v[208:211]\n"
"v_max_f32 v143, v143, v135\n"
"v_max3_f32 v170, v170, v139, v140\n"
"v_mfma_f32_16x16x32_bf16 v[208:211], a[124:127], v[48:51], v[208:211]\n"
"s_add_u32 m0, s34, 5120\n"
"ds_read_b64_tr_b16 v[134:135], v183 offset:27848\n"
"buffer_load_dwordx4 v191, s[8:11], s1 offen lds\n"
"v_mfma_f32_16x16x32_bf16 v[208:211], a[136:139], v[52:55], v[208:211]\n"
"s_add_u32 s1, s1, s33\n"
"v_max3_f32 v170, v170, v141, v142\n"
"v_cvt_pk_bf16_f32 v204, v204, v205\n"
"v_mfma_f32_16x16x32_bf16 v[212:215], a[144:147], v[32:35], 0\n"
"v_cvt_pk_bf16_f32 v205, v206, v207\n"
"v_mov_b64 v[98:99], 0\n"
"v_mfma_f32_16x16x32_bf16 v[212:215], a[156:159], v[36:39], v[212:215]\n"
"v_mov_b64 v[100:101], 0\n"
"v_mov_b64 v[106:107], 0\n"
"v_mfma_f32_16x16x32_bf16 v[212:215], a[168:171], v[40:43], v[212:215]\n"
"v_mov_b64 v[108:109], 0\n"
"v_max_f32 v171, v170, v143\n"
"v_mfma_f32_16x16x32_bf16 v[212:215], a[180:183], v[44:47], v[212:215]\n"
"v_max_f32 v170, v170, v143\n"
"v_cvt_pk_bf16_f32 v206, v208, v209\n"
"v_mfma_f32_16x16x32_bf16 v[212:215], a[192:195], v[48:51], v[212:215]\n"
"v_permlane16_swap_b32 v170, v171\n"
"v_mfma_f32_16x16x32_bf16 v[212:215], a[204:207], v[52:55], v[212:215]\n"
"v_max_f32 v170, v170, v171\n"
"v_mov_b32 v171, v170\n"
"v_cvt_pk_bf16_f32 v207, v210, v211\n"
"v_mfma_f32_16x16x32_bf16 v[208:211], a[148:151], v[32:35], 0\n"
"ds_write_b128 v186, v[204:207] offset:6880\n"
"v_mfma_f32_16x16x32_bf16 v[208:211], a[160:163], v[36:39], v[208:211]\n"
"v_permlane32_swap_b32 v170, v171\n"
"v_max_f32 v170, v170, v171\n"
"v_mul_f32 v170, 0xbe553b95, v170\n"
"v_pk_fma_f32 v[112:113], v[112:113], s[70:71], v[170:171] op_sel_hi:[1,0,0]\n"
"v_pk_fma_f32 v[114:115], v[114:115], s[70:71], v[170:171] op_sel_hi:[1,0,0]\n"
"v_pk_fma_f32 v[116:117], v[116:117], s[70:71], v[170:171] op_sel_hi:[1,0,0]\n"
"v_pk_fma_f32 v[118:119], v[118:119], s[70:71], v[170:171] op_sel_hi:[1,0,0]\n"
"v_pk_fma_f32 v[136:137], v[136:137], s[70:71], v[170:171] op_sel_hi:[1,0,0]\n"
"v_pk_fma_f32 v[138:139], v[138:139], s[70:71], v[170:171] op_sel_hi:[1,0,0]\n"
"v_pk_fma_f32 v[140:141], v[140:141], s[70:71], v[170:171] op_sel_hi:[1,0,0]\n"
"v_pk_fma_f32 v[142:143], v[142:143], s[70:71], v[170:171] op_sel_hi:[1,0,0]\n"
"v_mfma_f32_16x16x32_bf16 v[208:211], a[172:175], v[40:43], v[208:211]\n"
"v_exp_f32 v112, v112\n"
"v_mfma_f32_16x16x32_bf16 v[208:211], a[184:187], v[44:47], v[208:211]\n"
"s_waitcnt vmcnt(12)\n"
"v_exp_f32 v113, v113\n"
"v_mfma_f32_16x16x32_bf16 v[208:211], a[196:199], v[48:51], v[208:211]\n"
"v_readfirstlane_b32 s6, v201\n"
"v_cvt_pk_bf16_f32 v152, v212, v213\n"
"v_mfma_f32_16x16x32_bf16 v[208:211], a[208:211], v[52:55], v[208:211]\n"
"s_waitcnt vmcnt(8) lgkmcnt(0)\n"
"v_exp_f32 v114, v114\n"
"v_mfma_f32_16x16x32_bf16 v[204:207], a[152:155], v[32:35], 0\n"
"s_and_b32 s6, s6, 0xffffff00\n"
"ds_read_b128 v[32:35], v188 offset:6144\n"
"v_cvt_pk_bf16_f32 v82, v112, v114\n"
"v_mfma_f32_16x16x32_bf16 v[204:207], a[164:167], v[36:39], v[204:207]\n"
"s_barrier\n"
"v_mfma_f32_16x16x32_bf16 v[204:207], a[176:179], v[40:43], v[204:207]\n"
"s_add_u32 m0, s79, 130176\n"
"ds_read_b128 v[36:39], v189 offset:6144\n"
"buffer_load_dwordx4 v192, s[16:19], s43 offen lds\n"
"v_mfma_f32_16x16x32_bf16 v[204:207], a[188:191], v[44:47], v[204:207]\n"
"ds_read_b128 v[40:43], v188 offset:7168\n"
"ds_read_b128 v[44:47], v189 offset:7168\n"
"v_mfma_f32_16x16x32_bf16 v[204:207], a[200:203], v[48:51], v[204:207]\n"
"ds_read_b128 v[48:51], v188 offset:8192\n"
"v_cvt_pk_bf16_f32 v153, v214, v215\n"
"v_mfma_f32_16x16x32_bf16 v[204:207], a[212:215], v[52:55], v[204:207]\n"
"ds_read_b128 v[52:55], v189 offset:8192\n"
"v_exp_f32 v115, v115\n"
"v_exp_f32 v116, v116\n"
"v_cvt_pk_bf16_f32 v83, v113, v115\n"
"v_exp_f32 v117, v117\n"
"v_exp_f32 v118, v118\n"
"v_exp_f32 v119, v119\n"
"v_cvt_pk_bf16_f32 v84, v116, v118\n"
"v_cvt_pk_bf16_f32 v85, v117, v119\n"
"v_exp_f32 v136, v136\n"
"s_nop 0\n"
"v_smfmac_f32_16x16x64_bf16 v[252:255], v[82:85], a[240:247], v63\n"
"s_waitcnt lgkmcnt(4)\n"
"ds_read_b128 v[112:115], v180\n"
"v_smfmac_f32_16x16x64_bf16 v[98:101], v[82:85], v[120:127], v63\n"
"s_waitcnt lgkmcnt(3)\n"
"ds_read_b128 v[120:123], v181\n"
"v_smfmac_f32_16x16x64_bf16 v[106:109], v[82:85], v[128:135], v63\n"
"s_add_u32 m0, s79, 130176\n"
"s_nop 0\n"
"buffer_load_dwordx4 v192, s[16:19], s43 offen offset:1024 lds\n"
"v_mfma_f32_16x16x32_bf16 v[82:85], v[172:175], v[82:85], 0\n"
"ds_read_b128 v[116:119], v180 offset:13504\n"
"ds_read_b128 v[124:127], v181 offset:13504\n"
"v_mfma_f32_16x16x32_bf16 v[212:215], a[0:3], v[32:35], 0\n"
"ds_read_b128 v[128:131], v180 offset:27008\n"
"v_cvt_pk_bf16_f32 v154, v208, v209\n"
"v_mfma_f32_16x16x32_bf16 v[212:215], a[12:15], v[36:39], v[212:215]\n"
"ds_read_b128 v[132:135], v178\n"
"v_cvt_pk_bf16_f32 v155, v210, v211\n"
"v_mfma_f32_16x16x32_bf16 v[212:215], a[24:27], v[40:43], v[212:215]\n"
"ds_read_b64_tr_b16 a[228:229], v236 offset:12288\n"
"v_exp_f32 v137, v137\n"
"v_mfma_f32_16x16x32_bf16 v[212:215], a[36:39], v[44:47], v[212:215]\n"
"s_waitcnt lgkmcnt(8)\n"
"v_exp_f32 v138, v138\n"
"v_mfma_f32_16x16x32_bf16 v[212:215], a[48:51], v[48:51], v[212:215]\n"
"s_waitcnt lgkmcnt(7)\n"
"v_exp_f32 v139, v139\n"
"v_mfma_f32_16x16x32_bf16 v[212:215], a[60:63], v[52:55], v[212:215]\n"
"v_cvt_pk_bf16_f32 v90, v136, v138\n"
"v_cvt_pk_bf16_f32 v91, v137, v139\n"
"v_mfma_f32_16x16x32_bf16 v[208:211], a[4:7], v[32:35], 0\n"
"s_add_u32 m0, s79, 130176\n"
"ds_read_b128 v[136:139], v181 offset:27008\n"
"buffer_load_dwordx4 v192, s[16:19], s43 offen offset:2048 lds\n"
"v_mfma_f32_16x16x32_bf16 v[208:211], a[16:19], v[36:39], v[208:211]\n"
"s_add_u32 s43, s79, 55296\n"
"v_exp_f32 v140, v140\n"
"v_mfma_f32_16x16x32_bf16 v[208:211], a[28:31], v[40:43], v[208:211]\n"
"v_exp_f32 v141, v141\n"
"v_mfma_f32_16x16x32_bf16 v[208:211], a[40:43], v[44:47], v[208:211]\n"
"v_exp_f32 v142, v142\n"
"v_mfma_f32_16x16x32_bf16 v[208:211], a[52:55], v[48:51], v[208:211]\n"
"v_exp_f32 v143, v143\n"
"v_mfma_f32_16x16x32_bf16 v[208:211], a[64:67], v[52:55], v[208:211]\n"
"v_cvt_pk_bf16_f32 v92, v140, v142\n"
"v_cvt_pk_bf16_f32 v93, v141, v143\n"
"v_cvt_pk_bf16_f32 v166, v204, v205\n"
"v_cvt_pk_bf16_f32 v167, v206, v207\n"
"v_smfmac_f32_16x16x64_bf16 v[252:255], v[90:93], v[216:223], v63\n"
"ds_read_b64_tr_b16 v[24:25], v184 offset:72\n"
"v_smfmac_f32_16x16x64_bf16 v[98:101], v[90:93], v[72:79], v63\n"
"s_add_u32 m0, s34, 6144\n"
"s_nop 0\n"
"buffer_load_dwordx4 v190, s[8:11], s32 offen lds\n"
"v_smfmac_f32_16x16x64_bf16 v[106:109], v[90:93], v[8:15], v63\n"
"ds_read_b64_tr_b16 v[26:27], v185 offset:72\n"
"v_mfma_f32_16x16x32_bf16 v[90:93], v[172:175], v[90:93], v[82:85]\n"
"ds_read_b64_tr_b16 v[28:29], v184 offset:840\n"
"ds_read_b64_tr_b16 v[30:31], v185 offset:840\n"
"v_mfma_f32_16x16x32_bf16 v[204:207], a[8:11], v[32:35], 0\n"
"v_cvt_pk_bf16_f32 v252, v252, v253\n"
"v_cvt_pk_bf16_f32 v253, v254, v255\n"
"v_mfma_f32_16x16x32_bf16 v[204:207], a[20:23], v[36:39], v[204:207]\n"
"ds_read_b64 v[82:83], v180 offset:64\n"
"ds_read_b64 v[84:85], v181 offset:64\n"
"v_mfma_f32_16x16x32_bf16 v[204:207], a[32:35], v[40:43], v[204:207]\n"
"buffer_load_dword v237, v238, s[8:11], s78 offen\n"
"ds_read_b64_tr_b16 a[230:231], v236 offset:18432\n"
"v_mfma_f32_16x16x32_bf16 v[204:207], a[44:47], v[44:47], v[204:207]\n"
"s_add_u32 s78, s78, s33\n"
"v_rcp_f32 v171, v90\n"
"v_mfma_f32_16x16x32_bf16 v[204:207], a[56:59], v[48:51], v[204:207]\n"
"v_cvt_pk_bf16_f32 v171, v171, v171\n"
"v_and_b32 v92, v171, v176\n"
"v_mfma_f32_16x16x32_bf16 v[204:207], a[68:71], v[52:55], v[204:207]\n"
"s_add_u32 m0, s34, 9216\n"
"ds_read_b64 v[90:91], v180 offset:13568\n"
"v_and_b32 v93, v171, v177\n"
"v_cvt_pk_bf16_f32 v98, v98, v99\n"
"v_cvt_pk_bf16_f32 v99, v100, v101\n"
"v_mfma_f32_16x16x16_bf16 v[252:255], v[252:253], v[92:93], 0\n"
"buffer_load_dwordx4 v191, s[8:11], s32 offen lds\n"
"v_cvt_pk_bf16_f32 v106, v106, v107\n"
"v_mfma_f32_16x16x16_bf16 v[98:101], v[98:99], v[92:93], 0\n"
"s_add_u32 s32, s32, s33\n"
"v_cvt_pk_bf16_f32 v107, v108, v109\n"
"v_cvt_pk_bf16_f32 v212, v212, v213\n"
"v_cvt_pk_bf16_f32 v213, v214, v215\n"
"v_mfma_f32_16x16x16_bf16 v[106:109], v[106:107], v[92:93], 0\n"
"s_waitcnt lgkmcnt(2)\n"
"ds_read_b64 v[92:93], v181 offset:13568\n"
"v_cvt_pk_bf16_f32 v252, v252, v253\n"
"v_mfma_f32_16x16x32_bf16 v[112:115], v[112:115], v[144:147], 0\n"
"v_cvt_pk_bf16_f32 v253, v254, v255\n"
"v_cvt_pk_bf16_f32 v254, v98, v99\n"
"v_mfma_f32_16x16x32_bf16 v[112:115], v[80:83], v[156:159], v[112:115]\n"
"ds_read_b64 v[98:99], v180 offset:27072\n"
"v_cvt_pk_bf16_f32 v255, v100, v101\n"
"v_mfma_f32_16x16x32_bf16 v[120:123], v[120:123], v[144:147], 0\n"
"s_waitcnt lgkmcnt(2)\n"
"buffer_store_dwordx4 v[252:255], v192, s[28:31], s37 offen sc1\n"
"v_mfma_f32_16x16x32_bf16 v[120:123], v[84:87], v[158:161], v[120:123]\n"
"ds_read_b64 v[100:101], v181 offset:27072\n"
"v_cvt_pk_bf16_f32 v106, v106, v107\n"
"v_mfma_f32_16x16x32_bf16 v[116:119], v[116:119], v[144:147], 0\n"
"v_cvt_pk_bf16_f32 v107, v108, v109\n"
"s_mov_b64 exec, s[44:45]\n"
"v_add_u32 v179, 0xc0, v179\n"
"s_mov_b64 exec, -1\n"
"v_mfma_f32_16x16x32_bf16 v[116:119], v[88:91], v[156:159], v[116:119]\n"
"s_waitcnt lgkmcnt(2)\n"
"ds_read_b128 v[140:143], v179\n"
"ds_read_b64 v[108:109], v179 offset:64\n"
"v_mfma_f32_16x16x32_bf16 v[124:127], v[124:127], v[144:147], 0\n"
"ds_read_b64_tr_b16 a[232:233], v236 offset:12296\n"
"v_cvt_pk_bf16_f32 v214, v208, v209\n"
"v_mfma_f32_16x16x32_bf16 v[124:127], v[92:95], v[158:161], v[124:127]\n"
"s_waitcnt lgkmcnt(4)\n"
"v_cvt_pk_bf16_f32 v215, v210, v211\n"
"v_cvt_pk_bf16_f32 v204, v204, v205\n"
"v_mfma_f32_16x16x32_bf16 v[128:131], v[128:131], v[144:147], 0\n"
"ds_write_b128 v186, v[212:215] offset:20256\n"
"v_mfma_f32_16x16x32_bf16 v[128:131], v[96:99], v[156:159], v[128:131]\n"
"s_waitcnt lgkmcnt(4)\n"
"buffer_store_dwordx2 v[106:107], v193, s[28:31], s37 offen offset:1024 sc1\n"
"v_mfma_f32_16x16x32_bf16 v[136:139], v[136:139], v[144:147], 0\n"
"s_add_u32 s37, s37, 1572864\n"
"ds_read_b64 v[106:107], v178 offset:64\n"
"v_cvt_pk_bf16_f32 v205, v206, v207\n"
"v_mfma_f32_16x16x32_bf16 v[136:139], v[100:103], v[158:161], v[136:139]\n"
"v_max_f32 v112, v112, v120\n"
"v_max_f32 v113, v113, v121\n"
"v_mfma_f32_16x16x32_bf16 v[208:211], a[80:83], v[32:35], 0\n"
"s_waitcnt lgkmcnt(3)\n"
"v_max_f32 v114, v114, v122\n"
"v_max_f32 v115, v115, v123\n"
"v_mfma_f32_16x16x32_bf16 v[140:143], v[140:143], v[144:147], 0\n"
"ds_read_b128 v[120:123], v180 offset:13504\n"
"ds_read_b64_tr_b16 a[234:235], v236 offset:18440\n"
"v_mfma_f32_16x16x32_bf16 v[140:143], v[108:111], v[158:161], v[140:143]\n"
"s_add_u32 m0, s34, 7168\n"
"v_max3_f32 v170, v112, v113, v114\n"
"v_max_f32 v116, v116, v124\n"
"v_mfma_f32_16x16x32_bf16 v[208:211], a[92:95], v[36:39], v[208:211]\n"
"v_max_f32 v117, v117, v125\n"
"v_max_f32 v118, v118, v126\n"
"v_mfma_f32_16x16x32_bf16 v[208:211], a[104:107], v[40:43], v[208:211]\n"
"buffer_load_dwordx4 v190, s[8:11], s0 offen lds\n"
"v_max_f32 v119, v119, v127\n"
"v_mfma_f32_16x16x32_bf16 v[208:211], a[116:119], v[44:47], v[208:211]\n"
"s_waitcnt lgkmcnt(2)\n"
"ds_read_b128 v[124:127], v180 offset:27008\n"
"v_max3_f32 v170, v170, v115, v116\n"
"v_mfma_f32_16x16x32_bf16 v[132:135], v[132:135], v[144:147], 0\n"
"ds_read_b64_tr_b16 a[236:237], v235 offset:12288\n"
"v_max3_f32 v170, v170, v117, v118\n"
"v_mfma_f32_16x16x32_bf16 v[132:135], v[104:107], v[156:159], v[132:135]\n"
"ds_read_b64_tr_b16 a[238:239], v235 offset:18432\n"
"v_max_f32 v128, v128, v136\n"
"v_mfma_f32_16x16x32_bf16 v[208:211], a[128:131], v[48:51], v[208:211]\n"
"s_waitcnt vmcnt(11)\n"
"v_max_f32 v129, v129, v137\n"
"v_or_b32 v201, 2, s6\n"
"v_mfma_f32_16x16x32_bf16 v[208:211], a[140:143], v[52:55], v[208:211]\n"
"s_mov_b64 exec, 1\n"
"buffer_store_dword v201, off, s[28:31], s40 sc1\n"
"s_mov_b64 exec, -1\n"
"v_mfma_f32_16x16x32_bf16 v[212:215], a[72:75], v[32:35], 0\n"
"v_max_f32 v130, v130, v138\n"
"v_max_f32 v131, v131, v139\n"
"v_mfma_f32_16x16x32_bf16 v[212:215], a[84:87], v[36:39], v[212:215]\n"
"s_add_u32 m0, s34, 10240\n"
"ds_read_b128 v[136:139], v181 offset:13504\n"
"buffer_load_dwordx4 v191, s[8:11], s0 offen lds\n"
"v_mfma_f32_16x16x32_bf16 v[212:215], a[96:99], v[40:43], v[212:215]\n"
"s_add_u32 s0, s0, s33\n"
"v_cvt_pk_bf16_f32 v206, v208, v209\n"
"v_cvt_pk_bf16_f32 v207, v210, v211\n"
"v_mfma_f32_16x16x32_bf16 v[212:215], a[108:111], v[44:47], v[212:215]\n"
"ds_write_b128 v186, v[204:207] offset:20320\n"
"v_mfma_f32_16x16x32_bf16 v[212:215], a[120:123], v[48:51], v[212:215]\n"
"v_max3_f32 v170, v170, v119, v128\n"
"v_max3_f32 v170, v170, v129, v130\n"
"v_mfma_f32_16x16x32_bf16 v[212:215], a[132:135], v[52:55], v[212:215]\n"
"v_max_f32 v132, v132, v140\n"
"v_max_f32 v133, v133, v141\n"
"v_mfma_f32_16x16x32_bf16 v[208:211], a[76:79], v[32:35], 0\n"
"v_max_f32 v134, v134, v142\n"
"v_max_f32 v135, v135, v143\n"
"v_mfma_f32_16x16x32_bf16 v[208:211], a[88:91], v[36:39], v[208:211]\n"
"ds_read_b128 v[140:143], v181 offset:27008\n"
"v_max3_f32 v170, v170, v131, v132\n"
"v_mfma_f32_16x16x32_bf16 v[208:211], a[100:103], v[40:43], v[208:211]\n"
"s_add_u32 m0, s34, 8192\n"
"v_max3_f32 v170, v170, v133, v134\n"
"v_mov_b64 v[252:253], 0\n"
"v_mfma_f32_16x16x32_bf16 v[208:211], a[112:115], v[44:47], v[208:211]\n"
"buffer_load_dwordx4 v190, s[8:11], s1 offen lds\n"
"v_mov_b64 v[254:255], 0\n"
"v_mfma_f32_16x16x32_bf16 v[208:211], a[124:127], v[48:51], v[208:211]\n"
"v_mov_b64 v[98:99], 0\n"
"v_mov_b64 v[100:101], 0\n"
"v_mfma_f32_16x16x32_bf16 v[208:211], a[136:139], v[52:55], v[208:211]\n"
"v_mov_b64 v[106:107], 0\n"
"v_mov_b64 v[108:109], 0\n"
"v_mfma_f32_16x16x32_bf16 v[204:207], a[144:147], v[32:35], 0\n"
"v_max_f32 v171, v170, v135\n"
"v_max_f32 v170, v170, v135\n"
"v_mfma_f32_16x16x32_bf16 v[204:207], a[156:159], v[36:39], v[204:207]\n"
"s_nop 0\n"
"v_permlane16_swap_b32 v170, v171\n"
"v_mfma_f32_16x16x32_bf16 v[204:207], a[168:171], v[40:43], v[204:207]\n"
"v_max_f32 v170, v170, v171\n"
"v_mov_b32 v171, v170\n"
"v_mfma_f32_16x16x32_bf16 v[204:207], a[180:183], v[44:47], v[204:207]\n"
"s_nop 0\n"
"v_permlane32_swap_b32 v170, v171\n"
"v_max_f32 v170, v170, v171\n"
"v_mul_f32 v170, 0xbe553b95, v170\n"
"v_pk_fma_f32 v[112:113], v[112:113], s[70:71], v[170:171] op_sel_hi:[1,0,0]\n"
"v_pk_fma_f32 v[114:115], v[114:115], s[70:71], v[170:171] op_sel_hi:[1,0,0]\n"
"v_pk_fma_f32 v[116:117], v[116:117], s[70:71], v[170:171] op_sel_hi:[1,0,0]\n"
"v_pk_fma_f32 v[118:119], v[118:119], s[70:71], v[170:171] op_sel_hi:[1,0,0]\n"
"v_pk_fma_f32 v[128:129], v[128:129], s[70:71], v[170:171] op_sel_hi:[1,0,0]\n"
"v_pk_fma_f32 v[130:131], v[130:131], s[70:71], v[170:171] op_sel_hi:[1,0,0]\n"
"v_pk_fma_f32 v[132:133], v[132:133], s[70:71], v[170:171] op_sel_hi:[1,0,0]\n"
"v_pk_fma_f32 v[134:135], v[134:135], s[70:71], v[170:171] op_sel_hi:[1,0,0]\n"
"v_mfma_f32_16x16x32_bf16 v[204:207], a[192:195], v[48:51], v[204:207]\n"
"s_add_u32 m0, s34, 11264\n"
"v_cvt_pk_bf16_f32 v212, v212, v213\n"
"v_cvt_pk_bf16_f32 v213, v214, v215\n"
"v_mfma_f32_16x16x32_bf16 v[204:207], a[204:207], v[52:55], v[204:207]\n"
"buffer_load_dwordx4 v191, s[8:11], s1 offen lds\n"
"s_add_u32 s1, s1, s33\n"
"v_exp_f32 v112, v112\n"
"v_exp_f32 v113, v113\n"
"v_exp_f32 v114, v114\n"
"v_exp_f32 v115, v115\n"
"v_cvt_pk_bf16_f32 v82, v112, v114\n"
"v_cvt_pk_bf16_f32 v83, v113, v115\n"
"v_cvt_pk_bf16_f32 v214, v208, v209\n"
"v_exp_f32 v116, v116\n"
"v_cvt_pk_bf16_f32 v215, v210, v211\n"
"v_mfma_f32_16x16x32_bf16 v[208:211], a[148:151], v[32:35], 0\n"
"ds_write_b128 v186, v[212:215] offset:20384\n"
"v_mfma_f32_16x16x32_bf16 v[208:211], a[160:163], v[36:39], v[208:211]\n"
"v_exp_f32 v117, v117\n"
"v_mfma_f32_16x16x32_bf16 v[208:211], a[172:175], v[40:43], v[208:211]\n"
"v_exp_f32 v118, v118\n"
"v_mfma_f32_16x16x32_bf16 v[208:211], a[184:187], v[44:47], v[208:211]\n"
"ds_read_b128 v[112:115], v178\n"
"v_cvt_pk_bf16_f32 v84, v116, v118\n"
"v_mfma_f32_16x16x32_bf16 v[208:211], a[196:199], v[48:51], v[208:211]\n"
"v_exp_f32 v119, v119\n"
"v_mfma_f32_16x16x32_bf16 v[208:211], a[208:211], v[52:55], v[208:211]\n"
"v_cvt_pk_bf16_f32 v85, v117, v119\n"
"v_cvt_pk_bf16_f32 v144, v204, v205\n"
"v_mfma_f32_16x16x32_bf16 v[212:215], a[152:155], v[32:35], 0\n"
"ds_read_b128 v[32:35], v188\n"
"v_cvt_pk_bf16_f32 v145, v206, v207\n"
"v_mfma_f32_16x16x32_bf16 v[212:215], a[164:167], v[36:39], v[212:215]\n"
"s_waitcnt vmcnt(10) lgkmcnt(2)\n"
"ds_read_b128 v[36:39], v189\n"
"v_cvt_pk_bf16_f32 v146, v208, v209\n"
"v_mfma_f32_16x16x32_bf16 v[212:215], a[176:179], v[40:43], v[212:215]\n"
"s_barrier\n"
"v_mfma_f32_16x16x32_bf16 v[212:215], a[188:191], v[44:47], v[212:215]\n"
"s_add_u32 m0, s79, 142464\n"
"ds_read_b128 v[40:43], v188 offset:1024\n"
"buffer_load_dwordx4 v192, s[16:19], s43 offen lds\n"
"v_mfma_f32_16x16x32_bf16 v[212:215], a[200:203], v[48:51], v[212:215]\n"
".Lic2%=:\n"
"s_cbranch_execz .Lib0%=\n"
"ds_read_b128 v[44:47], v189 offset:1024\n"
"ds_read_b128 v[48:51], v188 offset:2048\n"
"v_mfma_f32_16x16x32_bf16 v[212:215], a[212:215], v[52:55], v[212:215]\n"
"ds_read_b128 v[52:55], v189 offset:2048\n"
"v_cvt_pk_bf16_f32 v147, v210, v211\n"
"v_smfmac_f32_16x16x64_bf16 v[252:255], v[82:85], v[224:231], v63\n"
"ds_read_b64_tr_b16 v[224:225], v184 offset:128\n"
"v_smfmac_f32_16x16x64_bf16 v[98:101], v[82:85], v[244:251], v63\n"
"s_waitcnt lgkmcnt(6)\n"
"ds_read_b64_tr_b16 v[226:227], v185 offset:128\n"
"v_smfmac_f32_16x16x64_bf16 v[106:109], v[82:85], a[248:255], v63\n"
"ds_read_b64_tr_b16 v[228:229], v184 offset:896\n"
"v_mfma_f32_16x16x32_bf16 v[82:85], v[172:175], v[82:85], 0\n"
"ds_read_b64_tr_b16 v[230:231], v185 offset:896\n"
"v_exp_f32 v128, v128\n"
"v_mfma_f32_16x16x32_bf16 v[204:207], a[0:3], v[32:35], 0\n"
"s_waitcnt lgkmcnt(8)\n"
"ds_read_b64_tr_b16 v[244:245], v184 offset:136\n"
"v_exp_f32 v129, v129\n"
"v_mfma_f32_16x16x32_bf16 v[204:207], a[12:15], v[36:39], v[204:207]\n"
"s_waitcnt lgkmcnt(7)\n"
"ds_read_b64_tr_b16 v[246:247], v185 offset:136\n"
"v_exp_f32 v130, v130\n"
"v_mfma_f32_16x16x32_bf16 v[204:207], a[24:27], v[40:43], v[204:207]\n"
"s_waitcnt lgkmcnt(6)\n"
"ds_read_b64_tr_b16 v[248:249], v184 offset:904\n"
"ds_read_b64_tr_b16 v[250:251], v185 offset:904\n"
"v_mfma_f32_16x16x32_bf16 v[204:207], a[36:39], v[44:47], v[204:207]\n"
"s_add_u32 m0, s79, 142464\n"
"v_exp_f32 v131, v131\n"
"v_mfma_f32_16x16x32_bf16 v[204:207], a[48:51], v[48:51], v[204:207]\n"
"buffer_load_dwordx4 v192, s[16:19], s43 offen offset:1024 lds\n"
"ds_read_b128 v[116:119], v178 offset:13504\n"
"v_mfma_f32_16x16x32_bf16 v[204:207], a[60:63], v[52:55], v[204:207]\n"
"ds_read_b64_tr_b16 a[240:241], v236\n"
"v_cvt_pk_bf16_f32 v90, v128, v130\n"
"v_mfma_f32_16x16x32_bf16 v[208:211], a[4:7], v[32:35], 0\n"
"v_cvt_pk_bf16_f32 v91, v129, v131\n"
"v_exp_f32 v132, v132\n"
"v_mfma_f32_16x16x32_bf16 v[208:211], a[16:19], v[36:39], v[208:211]\n"
"ds_read_b128 v[128:131], v179\n"
"v_exp_f32 v133, v133\n"
"v_mfma_f32_16x16x32_bf16 v[208:211], a[28:31], v[40:43], v[208:211]\n"
"v_exp_f32 v134, v134\n"
"v_mfma_f32_16x16x32_bf16 v[208:211], a[40:43], v[44:47], v[208:211]\n"
"v_exp_f32 v135, v135\n"
"v_mfma_f32_16x16x32_bf16 v[208:211], a[52:55], v[48:51], v[208:211]\n"
"v_cvt_pk_bf16_f32 v92, v132, v134\n"
"v_cvt_pk_bf16_f32 v93, v133, v135\n"
"v_mfma_f32_16x16x32_bf16 v[208:211], a[64:67], v[52:55], v[208:211]\n"
"s_waitcnt lgkmcnt(3)\n"
"ds_read_b128 v[132:135], v179 offset:13504\n"
"buffer_load_dword v237, v238, s[8:11], s78 offen\n"
"v_smfmac_f32_16x16x64_bf16 v[252:255], v[90:93], v[224:231], v63\n"
"s_add_u32 s78, s78, s33\n"
"ds_read_b64_tr_b16 a[242:243], v236 offset:6144\n"
"v_smfmac_f32_16x16x64_bf16 v[98:101], v[90:93], v[244:251], v63\n"
"v_cvt_pk_bf16_f32 v158, v212, v213\n"
"v_smfmac_f32_16x16x64_bf16 v[106:109], v[90:93], v[24:31], v63\n"
"s_add_u32 m0, s79, 142464\n"
"v_cvt_pk_bf16_f32 v159, v214, v215\n"
"v_mfma_f32_16x16x32_bf16 v[90:93], v[172:175], v[90:93], v[82:85]\n"
"v_cvt_pk_bf16_f32 v252, v252, v253\n"
"v_cvt_pk_bf16_f32 v253, v254, v255\n"
"v_mfma_f32_16x16x32_bf16 v[212:215], a[8:11], v[32:35], 0\n"
"buffer_load_dwordx4 v192, s[16:19], s43 offen offset:2048 lds\n"
"ds_read_b64 v[82:83], v180 offset:13568\n"
"v_mfma_f32_16x16x32_bf16 v[212:215], a[20:23], v[36:39], v[212:215]\n"
"s_add_u32 s43, s80, 12288\n"
"ds_read_b64 v[84:85], v181 offset:13568\n"
"v_cvt_pk_bf16_f32 v98, v98, v99\n"
"v_mfma_f32_16x16x32_bf16 v[212:215], a[32:35], v[40:43], v[212:215]\n"
"v_rcp_f32 v171, v90\n"
"v_mfma_f32_16x16x32_bf16 v[212:215], a[44:47], v[44:47], v[212:215]\n"
"ds_read_b64 v[90:91], v180 offset:27072\n"
"v_cvt_pk_bf16_f32 v171, v171, v171\n"
"v_mfma_f32_16x16x32_bf16 v[212:215], a[56:59], v[48:51], v[212:215]\n"
"v_and_b32 v92, v171, v176\n"
"v_and_b32 v93, v171, v177\n"
"v_mfma_f32_16x16x32_bf16 v[212:215], a[68:71], v[52:55], v[212:215]\n"
"v_cvt_pk_bf16_f32 v99, v100, v101\n"
"v_cvt_pk_bf16_f32 v106, v106, v107\n"
"v_mfma_f32_16x16x16_bf16 v[252:255], v[252:253], v[92:93], 0\n"
"v_cvt_pk_bf16_f32 v107, v108, v109\n"
"v_cvt_pk_bf16_f32 v204, v204, v205\n"
"v_mfma_f32_16x16x16_bf16 v[98:101], v[98:99], v[92:93], 0\n"
"s_waitcnt lgkmcnt(2)\n"
"v_cvt_pk_bf16_f32 v205, v206, v207\n"
"v_cvt_pk_bf16_f32 v206, v208, v209\n"
"v_mfma_f32_16x16x32_bf16 v[120:123], v[120:123], v[148:151], 0\n"
"s_waitcnt lgkmcnt(1)\n"
"v_cvt_pk_bf16_f32 v252, v252, v253\n"
"v_cvt_pk_bf16_f32 v253, v254, v255\n"
"v_mfma_f32_16x16x32_bf16 v[120:123], v[80:83], v[160:163], v[120:123]\n"
"s_add_u32 m0, s34, 0\n"
"v_cvt_pk_bf16_f32 v254, v98, v99\n"
"v_cvt_pk_bf16_f32 v255, v100, v101\n"
"v_mfma_f32_16x16x32_bf16 v[136:139], v[136:139], v[148:151], 0\n"
"s_waitcnt lgkmcnt(0)\n"
"buffer_load_dwordx4 v190, s[8:11], s32 offen lds\n"
"ds_read_b64 v[98:99], v178 offset:64\n"
"v_mfma_f32_16x16x32_bf16 v[136:139], v[84:87], v[162:165], v[136:139]\n"
"ds_read_b64 v[100:101], v179 offset:64\n"
"ds_read_b64_tr_b16 a[244:245], v236 offset:8\n"
"v_mfma_f32_16x16x32_bf16 v[124:127], v[124:127], v[148:151], 0\n"
"v_cvt_pk_bf16_f32 v207, v210, v211\n"
"v_cvt_pk_bf16_f32 v212, v212, v213\n"
"v_mfma_f32_16x16x32_bf16 v[124:127], v[88:91], v[160:163], v[124:127]\n"
"v_cvt_pk_bf16_f32 v213, v214, v215\n"
"s_mov_b64 exec, s[46:47]\n"
"v_add_u32 v183, 0xc0, v183\n"
"s_mov_b64 exec, -1\n"
"v_mfma_f32_16x16x16_bf16 v[106:109], v[106:107], v[92:93], 0\n"
"buffer_store_dwordx4 v[252:255], v192, s[28:31], s37 offen sc1\n"
"v_mfma_f32_16x16x32_bf16 v[208:211], a[80:83], v[32:35], 0\n"
"ds_read_b64 v[92:93], v181 offset:27072\n"
"ds_write_b128 v186, v[204:207] offset:33760\n"
"v_mfma_f32_16x16x32_bf16 v[208:211], a[92:95], v[36:39], v[208:211]\n"
"s_waitcnt lgkmcnt(3)\n"
"v_max_f32 v120, v120, v136\n"
"v_max_f32 v121, v121, v137\n"
"v_mfma_f32_16x16x32_bf16 v[112:115], v[112:115], v[148:151], 0\n"
"v_cvt_pk_bf16_f32 v106, v106, v107\n"
"v_cvt_pk_bf16_f32 v107, v108, v109\n"
"v_mfma_f32_16x16x32_bf16 v[112:115], v[96:99], v[160:163], v[112:115]\n"
"ds_read_b64 v[108:109], v179 offset:13568\n"
"ds_read_b64_tr_b16 v[136:137], v182 offset:72\n"
"v_mfma_f32_16x16x32_bf16 v[128:131], v[128:131], v[148:151], 0\n"
"v_max_f32 v122, v122, v138\n"
"v_max_f32 v123, v123, v139\n"
"v_mfma_f32_16x16x32_bf16 v[128:131], v[100:103], v[162:165], v[128:131]\n"
"s_add_u32 m0, s34, 3072\n"
"ds_read_b64_tr_b16 v[138:139], v183 offset:72\n"
"v_max3_f32 v170, v120, v121, v122\n"
"v_mfma_f32_16x16x32_bf16 v[208:211], a[104:107], v[40:43], v[208:211]\n"
"s_waitcnt lgkmcnt(4)\n"
"buffer_load_dwordx4 v191, s[8:11], s32 offen lds\n"
"ds_read_b64_tr_b16 a[246:247], v236 offset:6152\n"
"v_mfma_f32_16x16x32_bf16 v[140:143], v[140:143], v[148:151], 0\n"
"s_add_u32 s32, s32, s33\n"
"v_max_f32 v112, v112, v128\n"
"v_max_f32 v113, v113, v129\n"
"v_mfma_f32_16x16x32_bf16 v[140:143], v[92:95], v[162:165], v[140:143]\n"
"ds_read_b64_tr_b16 a[248:249], v235\n"
"v_max_f32 v114, v114, v130\n"
"v_mfma_f32_16x16x32_bf16 v[208:211], a[116:119], v[44:47], v[208:211]\n"
"v_max_f32 v115, v115, v131\n"
"v_mov_b64 v[252:253], 0\n"
"v_mfma_f32_16x16x32_bf16 v[208:211], a[128:131], v[48:51], v[208:211]\n"
"s_waitcnt lgkmcnt(4)\n"
"buffer_store_dwordx2 v[106:107], v193, s[28:31], s37 offen offset:1024 sc1\n"
"v_mfma_f32_16x16x32_bf16 v[132:135], v[132:135], v[148:151], 0\n"
"s_add_u32 s37, s37, 1572864\n"
"ds_read_b64 v[106:107], v178 offset:13568\n"
"ds_read_b128 v[128:131], v180 offset:27008\n"
"v_mfma_f32_16x16x32_bf16 v[132:135], v[108:111], v[162:165], v[132:135]\n"
"v_max_f32 v124, v124, v140\n"
"v_max_f32 v125, v125, v141\n"
"v_mfma_f32_16x16x32_bf16 v[208:211], a[140:143], v[52:55], v[208:211]\n"
"ds_read_b64_tr_b16 v[140:141], v182 offset:840\n"
"v_max_f32 v126, v126, v142\n"
"v_mfma_f32_16x16x32_bf16 v[204:207], a[72:75], v[32:35], 0\n"
"v_max_f32 v127, v127, v143\n"
"v_max3_f32 v170, v170, v123, v124\n"
"v_mfma_f32_16x16x32_bf16 v[204:207], a[84:87], v[36:39], v[204:207]\n"
"s_add_u32 m0, s34, 1024\n"
"ds_read_b64_tr_b16 v[142:143], v183 offset:840\n"
"v_max3_f32 v170, v170, v125, v126\n"
"v_mfma_f32_16x16x32_bf16 v[204:207], a[96:99], v[40:43], v[204:207]\n"
"ds_read_b64_tr_b16 a[250:251], v235 offset:6144\n"
"buffer_load_dwordx4 v190, s[8:11], s0 offen lds\n"
"v_mfma_f32_16x16x32_bf16 v[204:207], a[108:111], v[44:47], v[204:207]\n"
"v_max3_f32 v170, v170, v127, v112\n"
"v_cvt_pk_bf16_f32 v214, v208, v209\n"
"v_mfma_f32_16x16x32_bf16 v[204:207], a[120:123], v[48:51], v[204:207]\n"
"s_waitcnt lgkmcnt(4)\n"
"v_max3_f32 v170, v170, v113, v114\n"
"v_cvt_pk_bf16_f32 v215, v210, v211\n"
"v_mfma_f32_16x16x32_bf16 v[116:119], v[116:119], v[148:151], 0\n"
"ds_write_b128 v186, v[212:215] offset:33824\n"
"v_mfma_f32_16x16x32_bf16 v[116:119], v[104:107], v[160:163], v[116:119]\n"
"buffer_load_dword v200, off, s[28:31], s39 sc1\n"
"v_mov_b64 v[254:255], 0\n"
"v_mfma_f32_16x16x32_bf16 v[204:207], a[132:135], v[52:55], v[204:207]\n"
"v_mov_b64 v[98:99], 0\n"
"v_mov_b64 v[100:101], 0\n"
"v_mfma_f32_16x16x32_bf16 v[208:211], a[76:79], v[32:35], 0\n"
"v_mov_b64 v[106:107], 0\n"
"v_mov_b64 v[108:109], 0\n"
"v_mfma_f32_16x16x32_bf16 v[208:211], a[88:91], v[36:39], v[208:211]\n"
"s_add_u32 m0, s34, 4096\n"
"v_max_f32 v116, v116, v132\n"
"v_max_f32 v117, v117, v133\n"
"v_mfma_f32_16x16x32_bf16 v[208:211], a[100:103], v[40:43], v[208:211]\n"
"buffer_load_dwordx4 v191, s[8:11], s0 offen lds\n"
"v_max_f32 v118, v118, v134\n"
"v_mfma_f32_16x16x32_bf16 v[208:211], a[112:115], v[44:47], v[208:211]\n"
"s_add_u32 s0, s0, s33\n"
"v_max_f32 v119, v119, v135\n"
"v_max3_f32 v170, v170, v115, v116\n"
"v_mfma_f32_16x16x32_bf16 v[208:211], a[124:127], v[48:51], v[208:211]\n"
"ds_read_b128 v[132:135], v178\n"
"v_max3_f32 v170, v170, v117, v118\n"
"v_mfma_f32_16x16x32_bf16 v[208:211], a[136:139], v[52:55], v[208:211]\n"
"v_max_f32 v171, v170, v119\n"
"v_max_f32 v170, v170, v119\n"
"v_mfma_f32_16x16x32_bf16 v[212:215], a[144:147], v[32:35], 0\n"
"s_nop 0\n"
"v_permlane16_swap_b32 v170, v171\n"
"v_mfma_f32_16x16x32_bf16 v[212:215], a[156:159], v[36:39], v[212:215]\n"
"v_max_f32 v170, v170, v171\n"
"v_mov_b32 v171, v170\n"
"v_mfma_f32_16x16x32_bf16 v[212:215], a[168:171], v[40:43], v[212:215]\n"
"s_nop 0\n"
"v_permlane32_swap_b32 v170, v171\n"
"v_max_f32 v170, v170, v171\n"
"v_mul_f32 v170, 0xbe553b95, v170\n"
"v_pk_fma_f32 v[120:121], v[120:121], s[70:71], v[170:171] op_sel_hi:[1,0,0]\n"
"v_pk_fma_f32 v[122:123], v[122:123], s[70:71], v[170:171] op_sel_hi:[1,0,0]\n"
"v_pk_fma_f32 v[124:125], v[124:125], s[70:71], v[170:171] op_sel_hi:[1,0,0]\n"
"v_pk_fma_f32 v[126:127], v[126:127], s[70:71], v[170:171] op_sel_hi:[1,0,0]\n"
"v_pk_fma_f32 v[112:113], v[112:113], s[70:71], v[170:171] op_sel_hi:[1,0,0]\n"
"v_pk_fma_f32 v[114:115], v[114:115], s[70:71], v[170:171] op_sel_hi:[1,0,0]\n"
"v_pk_fma_f32 v[116:117], v[116:117], s[70:71], v[170:171] op_sel_hi:[1,0,0]\n"
"v_pk_fma_f32 v[118:119], v[118:119], s[70:71], v[170:171] op_sel_hi:[1,0,0]\n"
"v_mfma_f32_16x16x32_bf16 v[212:215], a[180:183], v[44:47], v[212:215]\n"
"s_add_u32 m0, s34, 2048\n"
"v_cvt_pk_bf16_f32 v204, v204, v205\n"
"v_cvt_pk_bf16_f32 v205, v206, v207\n"
"v_mfma_f32_16x16x32_bf16 v[212:215], a[192:195], v[48:51], v[212:215]\n"
"buffer_load_dwordx4 v190, s[8:11], s1 offen lds\n"
"v_cvt_pk_bf16_f32 v206, v208, v209\n"
"v_mfma_f32_16x16x32_bf16 v[212:215], a[204:207], v[52:55], v[212:215]\n"
"v_exp_f32 v120, v120\n"
"v_exp_f32 v121, v121\n"
"v_exp_f32 v122, v122\n"
"v_exp_f32 v123, v123\n"
"v_cvt_pk_bf16_f32 v82, v120, v122\n"
"v_cvt_pk_bf16_f32 v83, v121, v123\n"
"v_exp_f32 v124, v124\n"
"v_cvt_pk_bf16_f32 v207, v210, v211\n"
"v_mfma_f32_16x16x32_bf16 v[208:211], a[148:151], v[32:35], 0\n"
"ds_write_b128 v186, v[204:207] offset:33888\n"
"v_mfma_f32_16x16x32_bf16 v[208:211], a[160:163], v[36:39], v[208:211]\n"
"v_exp_f32 v125, v125\n"
"v_mfma_f32_16x16x32_bf16 v[208:211], a[172:175], v[40:43], v[208:211]\n"
"s_add_u32 m0, s34, 5120\n"
"v_exp_f32 v126, v126\n"
"v_mfma_f32_16x16x32_bf16 v[208:211], a[184:187], v[44:47], v[208:211]\n"
"buffer_load_dwordx4 v191, s[8:11], s1 offen lds\n"
"ds_read_b128 v[120:123], v181 offset:27008\n"
"v_mfma_f32_16x16x32_bf16 v[208:211], a[196:199], v[48:51], v[208:211]\n"
"s_add_u32 s1, s1, s33\n"
"v_exp_f32 v127, v127\n"
"v_mfma_f32_16x16x32_bf16 v[208:211], a[208:211], v[52:55], v[208:211]\n"
"v_cvt_pk_bf16_f32 v84, v124, v126\n"
"v_cvt_pk_bf16_f32 v85, v125, v127\n"
"v_mfma_f32_16x16x32_bf16 v[204:207], a[152:155], v[32:35], 0\n"
"s_waitcnt vmcnt(20)\n"
"ds_read_b128 v[32:35], v188 offset:6144\n"
"ds_read_b128 v[124:127], v179\n"
"v_mfma_f32_16x16x32_bf16 v[204:207], a[164:167], v[36:39], v[204:207]\n"
"s_waitcnt vmcnt(9) lgkmcnt(3)\n"
"ds_read_b128 v[36:39], v189 offset:6144\n"
"v_cvt_pk_bf16_f32 v148, v212, v213\n"
"v_mfma_f32_16x16x32_bf16 v[204:207], a[176:179], v[40:43], v[204:207]\n"
"s_barrier\n"
"v_mfma_f32_16x16x32_bf16 v[204:207], a[188:191], v[44:47], v[204:207]\n"
"ds_read_b128 v[40:43], v188 offset:7168\n"
"ds_read_b128 v[44:47], v189 offset:7168\n"
"v_mfma_f32_16x16x32_bf16 v[204:207], a[200:203], v[48:51], v[204:207]\n"
"ds_read_b128 v[48:51], v188 offset:8192\n"
"v_cvt_pk_bf16_f32 v149, v214, v215\n"
"v_mfma_f32_16x16x32_bf16 v[204:207], a[212:215], v[52:55], v[204:207]\n"
"s_add_u32 m0, s79, 130176\n"
"ds_read_b128 v[52:55], v189 offset:8192\n"
"buffer_load_dwordx4 v192, s[16:19], s43 offen lds\n"
"v_smfmac_f32_16x16x64_bf16 v[252:255], v[82:85], v[216:223], v63\n"
"ds_read_b64_tr_b16 v[216:217], v182 offset:128\n"
"v_smfmac_f32_16x16x64_bf16 v[98:101], v[82:85], v[72:79], v63\n"
"s_waitcnt lgkmcnt(7)\n"
"ds_read_b64_tr_b16 v[218:219], v183 offset:128\n"
"v_smfmac_f32_16x16x64_bf16 v[106:109], v[82:85], v[8:15], v63\n"
"ds_read_b64_tr_b16 v[220:221], v182 offset:896\n"
"v_mfma_f32_16x16x32_bf16 v[82:85], v[172:175], v[82:85], 0\n"
"ds_read_b64_tr_b16 v[222:223], v183 offset:896\n"
"v_exp_f32 v112, v112\n"
"v_mfma_f32_16x16x32_bf16 v[212:215], a[0:3], v[32:35], 0\n"
"s_waitcnt lgkmcnt(8)\n"
"ds_read_b64_tr_b16 v[72:73], v182 offset:136\n"
"v_exp_f32 v113, v113\n"
"v_mfma_f32_16x16x32_bf16 v[212:215], a[12:15], v[36:39], v[212:215]\n"
"s_waitcnt lgkmcnt(8)\n"
"ds_read_b64_tr_b16 v[74:75], v183 offset:136\n"
"v_exp_f32 v114, v114\n"
"v_mfma_f32_16x16x32_bf16 v[212:215], a[24:27], v[40:43], v[212:215]\n"
"s_waitcnt lgkmcnt(6)\n"
"ds_read_b64_tr_b16 v[76:77], v182 offset:904\n"
"ds_read_b64_tr_b16 v[78:79], v183 offset:904\n"
"v_mfma_f32_16x16x32_bf16 v[212:215], a[36:39], v[44:47], v[212:215]\n"
"s_add_u32 m0, s79, 130176\n"
"v_exp_f32 v115, v115\n"
"v_mfma_f32_16x16x32_bf16 v[212:215], a[48:51], v[48:51], v[212:215]\n"
"buffer_load_dwordx4 v192, s[16:19], s43 offen offset:1024 lds\n"
"ds_read_b64_tr_b16 a[252:253], v236 offset:12288\n"
"v_mfma_f32_16x16x32_bf16 v[212:215], a[60:63], v[52:55], v[212:215]\n"
"v_exp_f32 v116, v116\n"
"v_exp_f32 v117, v117\n"
"v_exp_f32 v118, v118\n"
"v_exp_f32 v119, v119\n"
"v_cvt_pk_bf16_f32 v90, v112, v114\n"
"v_cvt_pk_bf16_f32 v91, v113, v115\n"
"v_cvt_pk_bf16_f32 v92, v116, v118\n"
"v_cvt_pk_bf16_f32 v93, v117, v119\n"
"v_cvt_pk_bf16_f32 v150, v208, v209\n"
"v_cvt_pk_bf16_f32 v151, v210, v211\n"
"v_smfmac_f32_16x16x64_bf16 v[106:109], v[90:93], v[136:143], v63\n"
"s_waitcnt lgkmcnt(5)\n"
"ds_read_b128 v[112:115], v178 offset:13504\n"
"v_smfmac_f32_16x16x64_bf16 v[252:255], v[90:93], v[216:223], v63\n"
"s_waitcnt lgkmcnt(2)\n"
"ds_read_b128 v[136:139], v179 offset:13504\n"
"v_smfmac_f32_16x16x64_bf16 v[98:101], v[90:93], v[72:79], v63\n"
"ds_read_b128 v[116:119], v178 offset:27008\n"
"v_mfma_f32_16x16x32_bf16 v[90:93], v[172:175], v[90:93], v[82:85]\n"
"s_add_u32 m0, s79, 130176\n"
"ds_read_b128 v[140:143], v179 offset:27008\n"
"buffer_load_dwordx4 v192, s[16:19], s43 offen offset:2048 lds\n"
"v_mfma_f32_16x16x32_bf16 v[208:211], a[4:7], v[32:35], 0\n"
"s_add_u32 s43, s80, 49152\n"
"ds_read_b64 v[82:83], v180 offset:27072\n"
"ds_read_b64 v[84:85], v181 offset:27072\n"
"v_mfma_f32_16x16x32_bf16 v[208:211], a[16:19], v[36:39], v[208:211]\n"
"v_rcp_f32 v171, v90\n"
"v_mfma_f32_16x16x32_bf16 v[208:211], a[28:31], v[40:43], v[208:211]\n"
"ds_read_b64 v[90:91], v178 offset:64\n"
"v_cvt_pk_bf16_f32 v171, v171, v171\n"
"v_mfma_f32_16x16x32_bf16 v[208:211], a[40:43], v[44:47], v[208:211]\n"
"v_cvt_pk_bf16_f32 v252, v252, v253\n"
"v_and_b32 v92, v171, v176\n"
"v_mfma_f32_16x16x32_bf16 v[208:211], a[52:55], v[48:51], v[208:211]\n"
"v_and_b32 v93, v171, v177\n"
"v_cvt_pk_bf16_f32 v253, v254, v255\n"
"v_mfma_f32_16x16x32_bf16 v[208:211], a[64:67], v[52:55], v[208:211]\n"
"v_cvt_pk_bf16_f32 v106, v106, v107\n"
"v_cvt_pk_bf16_f32 v98, v98, v99\n"
"v_mfma_f32_16x16x16_bf16 v[252:255], v[252:253], v[92:93], 0\n"
"v_cvt_pk_bf16_f32 v107, v108, v109\n"
"v_cvt_pk_bf16_f32 v99, v100, v101\n"
"v_cvt_pk_bf16_f32 v162, v204, v205\n"
"v_mfma_f32_16x16x16_bf16 v[106:109], v[106:107], v[92:93], 0\n"
"s_waitcnt lgkmcnt(2)\n"
"buffer_load_dword v237, v238, s[8:11], s78 offen\n"
"ds_read_b64_tr_b16 a[254:255], v236 offset:18432\n"
"v_mfma_f32_16x16x32_bf16 v[128:131], v[128:131], v[152:155], 0\n"
"s_waitcnt lgkmcnt(2)\n"
"v_cvt_pk_bf16_f32 v252, v252, v253\n"
"v_cvt_pk_bf16_f32 v253, v254, v255\n"
"v_mfma_f32_16x16x32_bf16 v[128:131], v[80:83], v[164:167], v[128:131]\n"
"s_waitcnt lgkmcnt(1)\n"
"v_cvt_pk_bf16_f32 v106, v106, v107\n"
"v_cvt_pk_bf16_f32 v107, v108, v109\n"
"v_mfma_f32_16x16x32_bf16 v[120:123], v[120:123], v[152:155], 0\n"
"s_add_u32 m0, s34, 6144\n"
"ds_read_b64 v[108:109], v179 offset:27072\n"
"v_cvt_pk_bf16_f32 v163, v206, v207\n"
"v_mfma_f32_16x16x32_bf16 v[120:123], v[84:87], v[166:169], v[120:123]\n"
"s_add_u32 s78, s78, s33\n"
"buffer_load_dwordx4 v190, s[8:11], s32 offen lds\n"
"v_cvt_pk_bf16_f32 v212, v212, v213\n"
"v_mfma_f32_16x16x16_bf16 v[98:101], v[98:99], v[92:93], 0\n"
"ds_read_b64 v[92:93], v179 offset:64\n"
"v_cvt_pk_bf16_f32 v213, v214, v215\n"
"v_mfma_f32_16x16x32_bf16 v[132:135], v[132:135], v[152:155], 0\n"
"ds_read_b64_tr_b16 v[0:1], v236 offset:12296\n"
"v_cvt_pk_bf16_f32 v214, v208, v209\n"
"v_mfma_f32_16x16x32_bf16 v[132:135], v[88:91], v[164:167], v[132:135]\n"
"v_cvt_pk_bf16_f32 v215, v210, v211\n"
"v_max_f32 v128, v128, v120\n"
"v_mfma_f32_16x16x32_bf16 v[204:207], a[8:11], v[32:35], 0\n"
"buffer_store_dwordx2 v[106:107], v193, s[28:31], s37 offen offset:1024 sc1\n"
"v_mfma_f32_16x16x32_bf16 v[204:207], a[20:23], v[36:39], v[204:207]\n"
"ds_read_b64 v[106:107], v178 offset:27072\n"
"ds_write_b128 v186, v[212:215] offset:47264\n"
"v_mfma_f32_16x16x32_bf16 v[204:207], a[32:35], v[40:43], v[204:207]\n"
"s_waitcnt lgkmcnt(4)\n"
"v_cvt_pk_bf16_f32 v254, v98, v99\n"
"v_cvt_pk_bf16_f32 v255, v100, v101\n"
"v_mfma_f32_16x16x32_bf16 v[204:207], a[44:47], v[44:47], v[204:207]\n"
"s_waitcnt lgkmcnt(3)\n"
"v_max_f32 v129, v129, v121\n"
"v_max_f32 v130, v130, v122\n"
"v_mfma_f32_16x16x32_bf16 v[124:127], v[124:127], v[152:155], 0\n"
"s_add_u32 m0, s34, 9216\n"
"ds_read_b64 v[98:99], v178 offset:13568\n"
"ds_read_b64 v[100:101], v179 offset:13568\n"
"v_mfma_f32_16x16x32_bf16 v[124:127], v[92:95], v[166:169], v[124:127]\n"
"ds_read_b64_tr_b16 v[120:121], v182 offset:13576\n"
"buffer_load_dwordx4 v191, s[8:11], s32 offen lds\n"
"v_mfma_f32_16x16x32_bf16 v[140:143], v[140:143], v[152:155], 0\n"
"s_add_u32 s32, s32, s33\n"
"v_max_f32 v131, v131, v123\n"
"v_max3_f32 v170, v128, v129, v130\n"
"v_mfma_f32_16x16x32_bf16 v[140:143], v[108:111], v[166:169], v[140:143]\n"
"s_waitcnt lgkmcnt(4)\n"
"ds_read_b64_tr_b16 v[122:123], v183 offset:13576\n"
"v_max_f32 v132, v132, v124\n"
"v_mfma_f32_16x16x32_bf16 v[116:119], v[116:119], v[152:155], 0\n"
"ds_read_b64_tr_b16 v[2:3], v236 offset:18440\n"
"v_max_f32 v133, v133, v125\n"
"v_mfma_f32_16x16x32_bf16 v[116:119], v[104:107], v[164:167], v[116:119]\n"
"ds_read_b64_tr_b16 v[124:125], v182 offset:14344\n"
"buffer_store_dwordx4 v[252:255], v192, s[28:31], s37 offen sc1\n"
"v_mfma_f32_16x16x32_bf16 v[204:207], a[56:59], v[48:51], v[204:207]\n"
"s_waitcnt lgkmcnt(5)\n"
"v_max_f32 v134, v134, v126\n"
"v_max_f32 v135, v135, v127\n"
"v_mfma_f32_16x16x32_bf16 v[112:115], v[112:115], v[152:155], 0\n"
"s_waitcnt lgkmcnt(4)\n"
"ds_read_b64_tr_b16 v[126:127], v183 offset:14344\n"
"v_max3_f32 v170, v170, v131, v132\n"
"v_mfma_f32_16x16x32_bf16 v[112:115], v[96:99], v[164:167], v[112:115]\n"
"s_add_u32 s37, s37, 1572864\n"
"v_max3_f32 v170, v170, v133, v134\n"
"v_max_f32 v116, v116, v140\n"
"v_mfma_f32_16x16x32_bf16 v[136:139], v[136:139], v[152:155], 0\n"
"ds_read_b64_tr_b16 v[4:5], v235 offset:12288\n"
"v_max_f32 v117, v117, v141\n"
"v_mfma_f32_16x16x32_bf16 v[136:139], v[100:103], v[166:169], v[136:139]\n"
"ds_read_b64_tr_b16 v[6:7], v235 offset:18432\n"
"v_max_f32 v118, v118, v142\n"
"v_mfma_f32_16x16x32_bf16 v[204:207], a[68:71], v[52:55], v[204:207]\n"
"v_max_f32 v119, v119, v143\n"
"v_mov_b64 v[252:253], 0\n"
"v_mfma_f32_16x16x32_bf16 v[208:211], a[80:83], v[32:35], 0\n"
"s_add_u32 m0, s34, 7168\n"
"ds_read_b128 v[140:143], v178 offset:13504\n"
"v_max_f32 v112, v112, v136\n"
"v_mfma_f32_16x16x32_bf16 v[208:211], a[92:95], v[36:39], v[208:211]\n"
"buffer_load_dwordx4 v190, s[8:11], s0 offen lds\n"
"v_cvt_pk_bf16_f32 v204, v204, v205\n"
"v_mfma_f32_16x16x32_bf16 v[208:211], a[104:107], v[40:43], v[208:211]\n"
"v_cvt_pk_bf16_f32 v205, v206, v207\n"
"v_max_f32 v113, v113, v137\n"
"v_mfma_f32_16x16x32_bf16 v[208:211], a[116:119], v[44:47], v[208:211]\n"
"v_max_f32 v114, v114, v138\n"
"v_max_f32 v115, v115, v139\n"
"v_mfma_f32_16x16x32_bf16 v[208:211], a[128:131], v[48:51], v[208:211]\n"
"ds_read_b128 v[136:139], v178\n"
"v_max3_f32 v170, v170, v135, v112\n"
"v_mfma_f32_16x16x32_bf16 v[208:211], a[140:143], v[52:55], v[208:211]\n"
"v_max3_f32 v170, v170, v113, v114\n"
"v_max3_f32 v170, v170, v115, v116\n"
"v_mfma_f32_16x16x32_bf16 v[212:215], a[72:75], v[32:35], 0\n"
"v_max3_f32 v170, v170, v117, v118\n"
"v_mov_b64 v[254:255], 0\n"
"v_mfma_f32_16x16x32_bf16 v[212:215], a[84:87], v[36:39], v[212:215]\n"
"v_mov_b64 v[98:99], 0\n"
"v_mov_b64 v[100:101], 0\n"
"v_mfma_f32_16x16x32_bf16 v[212:215], a[96:99], v[40:43], v[212:215]\n"
"v_cvt_pk_bf16_f32 v206, v208, v209\n"
"v_cvt_pk_bf16_f32 v207, v210, v211\n"
"v_mfma_f32_16x16x32_bf16 v[212:215], a[108:111], v[44:47], v[212:215]\n"
"s_add_u32 m0, s34, 10240\n"
"ds_write_b128 v186, v[204:207] offset:47328\n"
"v_mfma_f32_16x16x32_bf16 v[212:215], a[120:123], v[48:51], v[212:215]\n"
"buffer_load_dwordx4 v191, s[8:11], s0 offen lds\n"
"v_mov_b64 v[106:107], 0\n"
"v_mfma_f32_16x16x32_bf16 v[212:215], a[132:135], v[52:55], v[212:215]\n"
"s_waitcnt vmcnt(13)\n"
"v_mov_b64 v[108:109], 0\n"
"v_max_f32 v171, v170, v119\n"
"v_mfma_f32_16x16x32_bf16 v[208:211], a[76:79], v[32:35], 0\n"
"s_add_u32 s0, s0, s33\n"
"v_add3_u32 v200, v200, s50, -2\n"
"v_cmp_lt_u32 vcc, 64, v200\n"
"v_mfma_f32_16x16x32_bf16 v[208:211], a[88:91], v[36:39], v[208:211]\n"
"v_max_f32 v170, v170, v119\n"
"v_cvt_pk_bf16_f32 v212, v212, v213\n"
"v_mfma_f32_16x16x32_bf16 v[208:211], a[100:103], v[40:43], v[208:211]\n"
"s_nop 0\n"
"s_cbranch_vccnz .Lp1%=\n"
".Lr1%=:\n"
"v_mfma_f32_16x16x32_bf16 v[208:211], a[112:115], v[44:47], v[208:211]\n"
"buffer_load_dword v201, off, s[28:31], s40 offset:128 sc1\n"
"v_cvt_pk_bf16_f32 v213, v214, v215\n"
"v_mfma_f32_16x16x32_bf16 v[208:211], a[124:127], v[48:51], v[208:211]\n"
"v_permlane16_swap_b32 v170, v171\n"
"v_mfma_f32_16x16x32_bf16 v[208:211], a[136:139], v[52:55], v[208:211]\n"
"buffer_load_dwordx4 v[56:59], v192, s[28:31], s38 offen sc1\n"
"v_max_f32 v170, v170, v171\n"
"v_mfma_f32_16x16x32_bf16 v[204:207], a[144:147], v[32:35], 0\n"
"v_mov_b32 v171, v170\n"
"s_mov_b64 exec, s[44:45]\n"
"v_add_u32 v181, 0xc0, v181\n"
"s_mov_b64 exec, -1\n"
"v_mfma_f32_16x16x32_bf16 v[204:207], a[156:159], v[36:39], v[204:207]\n"
"v_permlane32_swap_b32 v170, v171\n"
"v_max_f32 v170, v170, v171\n"
"v_mul_f32 v170, 0xbe553b95, v170\n"
"v_pk_fma_f32 v[128:129], v[128:129], s[70:71], v[170:171] op_sel_hi:[1,0,0]\n"
"v_pk_fma_f32 v[130:131], v[130:131], s[70:71], v[170:171] op_sel_hi:[1,0,0]\n"
"v_pk_fma_f32 v[132:133], v[132:133], s[70:71], v[170:171] op_sel_hi:[1,0,0]\n"
"v_pk_fma_f32 v[134:135], v[134:135], s[70:71], v[170:171] op_sel_hi:[1,0,0]\n"
"v_pk_fma_f32 v[112:113], v[112:113], s[70:71], v[170:171] op_sel_hi:[1,0,0]\n"
"v_pk_fma_f32 v[114:115], v[114:115], s[70:71], v[170:171] op_sel_hi:[1,0,0]\n"
"v_pk_fma_f32 v[116:117], v[116:117], s[70:71], v[170:171] op_sel_hi:[1,0,0]\n"
"v_pk_fma_f32 v[118:119], v[118:119], s[70:71], v[170:171] op_sel_hi:[1,0,0]\n"
"v_mfma_f32_16x16x32_bf16 v[204:207], a[168:171], v[40:43], v[204:207]\n"
"s_add_u32 m0, s34, 8192\n"
"buffer_load_dwordx2 v[60:61], v193, s[28:31], s38 offen offset:1024 sc1\n"
"v_cvt_pk_bf16_f32 v214, v208, v209\n"
"v_mfma_f32_16x16x32_bf16 v[204:207], a[180:183], v[44:47], v[204:207]\n"
"s_add_u32 s38, s38, 1572864\n"
"v_exp_f32 v128, v128\n"
"v_mfma_f32_16x16x32_bf16 v[204:207], a[192:195], v[48:51], v[204:207]\n"
"v_exp_f32 v129, v129\n"
"v_mfma_f32_16x16x32_bf16 v[204:207], a[204:207], v[52:55], v[204:207]\n"
"v_exp_f32 v130, v130\n"
"v_exp_f32 v131, v131\n"
"buffer_load_dwordx4 v190, s[8:11], s1 offen lds\n"
"v_cvt_pk_bf16_f32 v82, v128, v130\n"
"v_cvt_pk_bf16_f32 v83, v129, v131\n"
"v_exp_f32 v132, v132\n"
"v_cvt_pk_bf16_f32 v215, v210, v211\n"
"v_mfma_f32_16x16x32_bf16 v[208:211], a[148:151], v[32:35], 0\n"
"ds_write_b128 v186, v[212:215] offset:47392\n"
"v_mfma_f32_16x16x32_bf16 v[208:211], a[160:163], v[36:39], v[208:211]\n"
"v_exp_f32 v133, v133\n"
"v_mfma_f32_16x16x32_bf16 v[208:211], a[172:175], v[40:43], v[208:211]\n"
"v_exp_f32 v134, v134\n"
"v_mfma_f32_16x16x32_bf16 v[208:211], a[184:187], v[44:47], v[208:211]\n"
"ds_read_b128 v[128:131], v179\n"
"v_cvt_pk_bf16_f32 v84, v132, v134\n"
"v_mfma_f32_16x16x32_bf16 v[208:211], a[196:199], v[48:51], v[208:211]\n"
"v_exp_f32 v135, v135\n"
"v_mfma_f32_16x16x32_bf16 v[208:211], a[208:211], v[52:55], v[208:211]\n"
"s_add_u32 m0, s34, 11264\n"
"v_cvt_pk_bf16_f32 v85, v133, v135\n"
"v_cvt_pk_bf16_f32 v152, v204, v205\n"
"v_mfma_f32_16x16x32_bf16 v[212:215], a[152:155], v[32:35], 0\n"
"buffer_load_dwordx4 v191, s[8:11], s1 offen lds\n"
"ds_read_b128 v[32:35], v188\n"
"v_mfma_f32_16x16x32_bf16 v[212:215], a[164:167], v[36:39], v[212:215]\n"
"s_add_u32 s1, s1, s33\n"
"ds_read_b128 v[36:39], v189\n"
"ds_read_b128 v[132:135], v179 offset:13504\n"
"v_mfma_f32_16x16x32_bf16 v[212:215], a[176:179], v[40:43], v[212:215]\n"
"s_waitcnt vmcnt(12) lgkmcnt(4)\n"
"v_exp_f32 v112, v112\n"
"v_mfma_f32_16x16x32_bf16 v[212:215], a[188:191], v[44:47], v[212:215]\n"
"s_barrier\n"
"v_mfma_f32_16x16x32_bf16 v[212:215], a[200:203], v[48:51], v[212:215]\n"
"ds_read_b128 v[40:43], v188 offset:1024\n"
"ds_read_b128 v[44:47], v189 offset:1024\n"
"v_mfma_f32_16x16x32_bf16 v[212:215], a[212:215], v[52:55], v[212:215]\n"
"ds_read_b128 v[48:51], v188 offset:2048\n"
"v_cvt_pk_bf16_f32 v153, v206, v207\n"
"v_smfmac_f32_16x16x64_bf16 v[252:255], v[82:85], v[224:231], v63\n"
"ds_read_b128 v[52:55], v189 offset:2048\n"
"v_smfmac_f32_16x16x64_bf16 v[98:101], v[82:85], v[244:251], v63\n"
"s_waitcnt lgkmcnt(6)\n"
"ds_read_b64_tr_b16 v[224:225], v182 offset:13632\n"
"v_smfmac_f32_16x16x64_bf16 v[106:109], v[82:85], v[24:31], v63\n"
"s_waitcnt lgkmcnt(6)\n"
"ds_read_b64_tr_b16 v[226:227], v183 offset:13632\n"
"v_mfma_f32_16x16x32_bf16 v[82:85], v[172:175], v[82:85], 0\n"
"s_add_u32 m0, s79, 142464\n"
"ds_read_b64_tr_b16 v[228:229], v182 offset:14400\n"
"buffer_load_dwordx4 v192, s[16:19], s43 offen lds\n"
"v_mfma_f32_16x16x32_bf16 v[204:207], a[0:3], v[32:35], 0\n"
"ds_read_b64_tr_b16 v[230:231], v183 offset:14400\n"
"v_cvt_pk_bf16_f32 v154, v208, v209\n"
"v_mfma_f32_16x16x32_bf16 v[204:207], a[12:15], v[36:39], v[204:207]\n"
"s_waitcnt lgkmcnt(7)\n"
"ds_read_b64_tr_b16 v[244:245], v182 offset:13640\n"
"v_cvt_pk_bf16_f32 v155, v210, v211\n"
"v_mfma_f32_16x16x32_bf16 v[204:207], a[24:27], v[40:43], v[204:207]\n"
"s_waitcnt lgkmcnt(7)\n"
"ds_read_b64_tr_b16 v[246:247], v183 offset:13640\n"
"v_exp_f32 v113, v113\n"
"v_mfma_f32_16x16x32_bf16 v[204:207], a[36:39], v[44:47], v[204:207]\n"
"s_waitcnt lgkmcnt(7)\n"
"ds_read_b64_tr_b16 v[248:249], v182 offset:14408\n"
"v_exp_f32 v114, v114\n"
"v_mfma_f32_16x16x32_bf16 v[204:207], a[48:51], v[48:51], v[204:207]\n"
"s_waitcnt lgkmcnt(7)\n"
"ds_read_b64_tr_b16 v[250:251], v183 offset:14408\n"
"v_cvt_pk_bf16_f32 v90, v112, v114\n"
"v_mfma_f32_16x16x32_bf16 v[204:207], a[60:63], v[52:55], v[204:207]\n"
".Lic3%=:\n"
"s_cbranch_execz .Lib0%=\n"
"ds_read_b64_tr_b16 v[8:9], v236\n"
"v_exp_f32 v115, v115\n"
"v_mfma_f32_16x16x32_bf16 v[208:211], a[4:7], v[32:35], 0\n"
"s_add_u32 m0, s79, 142464\n"
"v_cvt_pk_bf16_f32 v91, v113, v115\n"
"v_exp_f32 v116, v116\n"
"v_mfma_f32_16x16x32_bf16 v[208:211], a[16:19], v[36:39], v[208:211]\n"
"buffer_load_dwordx4 v192, s[16:19], s43 offen offset:1024 lds\n"
"ds_read_b128 v[112:115], v178 offset:27008\n"
"v_mfma_f32_16x16x32_bf16 v[208:211], a[28:31], v[40:43], v[208:211]\n"
"v_exp_f32 v117, v117\n"
"v_mfma_f32_16x16x32_bf16 v[208:211], a[40:43], v[44:47], v[208:211]\n"
"v_exp_f32 v118, v118\n"
"v_mfma_f32_16x16x32_bf16 v[208:211], a[52:55], v[48:51], v[208:211]\n"
"v_exp_f32 v119, v119\n"
"v_mfma_f32_16x16x32_bf16 v[208:211], a[64:67], v[52:55], v[208:211]\n"
"v_cvt_pk_bf16_f32 v92, v116, v118\n"
"v_cvt_pk_bf16_f32 v93, v117, v119\n"
"v_cvt_pk_bf16_f32 v166, v212, v213\n"
"v_cvt_pk_bf16_f32 v167, v214, v215\n"
"v_smfmac_f32_16x16x64_bf16 v[106:109], v[90:93], v[120:127], v63\n"
"s_waitcnt lgkmcnt(2)\n"
"ds_read_b128 v[120:123], v179 offset:27008\n"
"v_smfmac_f32_16x16x64_bf16 v[252:255], v[90:93], v[224:231], v63\n"
"ds_read_b128 v[116:119], v180\n"
"v_smfmac_f32_16x16x64_bf16 v[98:101], v[90:93], v[244:251], v63\n"
"s_add_u32 m0, s79, 142464\n"
"s_nop 0\n"
"buffer_load_dwordx4 v192, s[16:19], s43 offen offset:2048 lds\n"
"v_mfma_f32_16x16x32_bf16 v[90:93], v[172:175], v[90:93], v[82:85]\n"
"ds_read_b128 v[124:127], v181\n"
"ds_read_b64_tr_b16 v[10:11], v236 offset:6144\n"
"v_mfma_f32_16x16x32_bf16 v[212:215], a[8:11], v[32:35], 0\n"
"v_cvt_pk_bf16_f32 v252, v252, v253\n"
"v_cvt_pk_bf16_f32 v253, v254, v255\n"
"v_mfma_f32_16x16x32_bf16 v[212:215], a[20:23], v[36:39], v[212:215]\n"
"ds_read_b64 v[82:83], v178 offset:64\n"
"v_cvt_pk_bf16_f32 v98, v98, v99\n"
"v_mfma_f32_16x16x32_bf16 v[212:215], a[32:35], v[40:43], v[212:215]\n"
"ds_read_b64 v[84:85], v179 offset:64\n"
"v_cvt_pk_bf16_f32 v99, v100, v101\n"
"v_mfma_f32_16x16x32_bf16 v[212:215], a[44:47], v[44:47], v[212:215]\n"
"v_rcp_f32 v171, v90\n"
"v_mfma_f32_16x16x32_bf16 v[212:215], a[56:59], v[48:51], v[212:215]\n"
"ds_read_b64 v[90:91], v178 offset:13568\n"
"v_cvt_pk_bf16_f32 v171, v171, v171\n"
"v_mfma_f32_16x16x32_bf16 v[212:215], a[68:71], v[52:55], v[212:215]\n"
"v_and_b32 v92, v171, v176\n"
"s_add_u32 m0, s34, 0\n"
"v_and_b32 v93, v171, v177\n"
"v_cvt_pk_bf16_f32 v106, v106, v107\n"
"v_cvt_pk_bf16_f32 v107, v108, v109\n"
"v_mfma_f32_16x16x16_bf16 v[252:255], v[252:253], v[92:93], 0\n"
"buffer_load_dwordx4 v190, s[8:11], s32 offen lds\n"
"v_cvt_pk_bf16_f32 v204, v204, v205\n"
"v_mfma_f32_16x16x16_bf16 v[98:101], v[98:99], v[92:93], 0\n"
"v_cvt_pk_bf16_f32 v205, v206, v207\n"
"v_cvt_pk_bf16_f32 v206, v208, v209\n"
"v_mfma_f32_16x16x16_bf16 v[106:109], v[106:107], v[92:93], 0\n"
"s_waitcnt lgkmcnt(2)\n"
"ds_read_b64 v[92:93], v179 offset:13568\n"
"v_cvt_pk_bf16_f32 v252, v252, v253\n"
"v_mfma_f32_16x16x32_bf16 v[136:139], v[136:139], v[144:147], 0\n"
"s_waitcnt lgkmcnt(2)\n"
"v_cvt_pk_bf16_f32 v253, v254, v255\n"
"v_cvt_pk_bf16_f32 v254, v98, v99\n"
"v_mfma_f32_16x16x32_bf16 v[136:139], v[80:83], v[156:159], v[136:139]\n"
"s_waitcnt lgkmcnt(1)\n"
"ds_read_b64 v[98:99], v178 offset:27072\n"
"v_cvt_pk_bf16_f32 v255, v100, v101\n"
"v_mfma_f32_16x16x32_bf16 v[128:131], v[128:131], v[144:147], 0\n"
"buffer_store_dwordx4 v[252:255], v192, s[28:31], s37 offen sc1\n"
"v_mfma_f32_16x16x32_bf16 v[128:131], v[84:87], v[158:161], v[128:131]\n"
"ds_read_b64 v[100:101], v179 offset:27072\n"
"v_cvt_pk_bf16_f32 v106, v106, v107\n"
"v_mfma_f32_16x16x32_bf16 v[140:143], v[140:143], v[144:147], 0\n"
"v_cvt_pk_bf16_f32 v107, v108, v109\n"
"v_cvt_pk_bf16_f32 v207, v210, v211\n"
"v_mfma_f32_16x16x32_bf16 v[140:143], v[88:91], v[156:159], v[140:143]\n"
"s_waitcnt lgkmcnt(2)\n"
"ds_read_b64 v[108:109], v181 offset:64\n"
"ds_write_b128 v186, v[204:207] offset:60768\n"
"v_mfma_f32_16x16x32_bf16 v[132:135], v[132:135], v[144:147], 0\n"
"s_waitcnt lgkmcnt(3)\n"
"v_cvt_pk_bf16_f32 v212, v212, v213\n"
"v_cvt_pk_bf16_f32 v213, v214, v215\n"
"v_mfma_f32_16x16x32_bf16 v[132:135], v[92:95], v[158:161], v[132:135]\n"
"v_max_f32 v136, v136, v128\n"
"v_max_f32 v137, v137, v129\n"
"v_mfma_f32_16x16x32_bf16 v[112:115], v[112:115], v[144:147], 0\n"
"s_add_u32 m0, s34, 3072\n"
"ds_read_b64_tr_b16 v[128:129], v182 offset:72\n"
"ds_read_b64_tr_b16 v[12:13], v236 offset:8\n"
"v_mfma_f32_16x16x32_bf16 v[112:115], v[96:99], v[156:159], v[112:115]\n"
"s_waitcnt lgkmcnt(4)\n"
"buffer_load_dwordx4 v191, s[8:11], s32 offen lds\n"
"v_max_f32 v138, v138, v130\n"
"v_mfma_f32_16x16x32_bf16 v[120:123], v[120:123], v[144:147], 0\n"
"s_add_u32 s32, s32, s33\n"
"v_max_f32 v139, v139, v131\n"
"v_max3_f32 v170, v136, v137, v138\n"
"v_mfma_f32_16x16x32_bf16 v[120:123], v[100:103], v[158:161], v[120:123]\n"
"s_waitcnt lgkmcnt(3)\n"
"ds_read_b64_tr_b16 v[130:131], v183 offset:72\n"
"ds_read_b64_tr_b16 v[14:15], v236 offset:6152\n"
"v_mfma_f32_16x16x32_bf16 v[124:127], v[124:127], v[144:147], 0\n"
"v_max_f32 v140, v140, v132\n"
"v_max_f32 v141, v141, v133\n"
"v_mfma_f32_16x16x32_bf16 v[124:127], v[108:111], v[158:161], v[124:127]\n"
"buffer_store_dwordx2 v[106:107], v193, s[28:31], s37 offen offset:1024 sc1\n"
"v_mfma_f32_16x16x32_bf16 v[208:211], a[80:83], v[32:35], 0\n"
"s_add_u32 s37, s37, 1572864\n"
"ds_read_b64 v[106:107], v180 offset:64\n"
"ds_read_b64_tr_b16 v[132:133], v182 offset:840\n"
"v_mfma_f32_16x16x32_bf16 v[208:211], a[92:95], v[36:39], v[208:211]\n"
"s_add_u32 s4, s37, 7864320\n"
"v_max_f32 v142, v142, v134\n"
"v_max_f32 v143, v143, v135\n"
"v_mfma_f32_16x16x32_bf16 v[208:211], a[104:107], v[40:43], v[208:211]\n"
"ds_read_b64_tr_b16 v[134:135], v183 offset:840\n"
"v_max3_f32 v170, v170, v139, v140\n"
"v_mfma_f32_16x16x32_bf16 v[208:211], a[116:119], v[44:47], v[208:211]\n"
"v_max3_f32 v170, v170, v141, v142\n"
"v_max_f32 v112, v112, v120\n"
"v_mfma_f32_16x16x32_bf16 v[208:211], a[128:131], v[48:51], v[208:211]\n"
"s_add_u32 m0, s34, 1024\n"
"ds_read_b64_tr_b16 v[16:17], v235\n"
"v_max_f32 v113, v113, v121\n"
"v_mfma_f32_16x16x32_bf16 v[208:211], a[140:143], v[52:55], v[208:211]\n"
"ds_read_b64_tr_b16 v[120:121], v182 offset:27080\n"
"buffer_load_dwordx4 v190, s[8:11], s0 offen lds\n"
"v_mfma_f32_16x16x32_bf16 v[204:207], a[72:75], v[32:35], 0\n"
"ds_read_b64_tr_b16 v[18:19], v235 offset:6144\n"
"v_max_f32 v114, v114, v122\n"
"v_mfma_f32_16x16x32_bf16 v[204:207], a[84:87], v[36:39], v[204:207]\n"
"s_waitcnt lgkmcnt(5)\n"
"v_mov_b64 v[64:65], -1\n"
"v_mov_b64 v[66:67], -1\n"
"v_mfma_f32_16x16x32_bf16 v[116:119], v[116:119], v[144:147], 0\n"
"v_cvt_pk_bf16_f32 v214, v208, v209\n"
"v_cvt_pk_bf16_f32 v215, v210, v211\n"
"v_mfma_f32_16x16x32_bf16 v[116:119], v[104:107], v[156:159], v[116:119]\n"
"ds_write_b128 v186, v[212:215] offset:60832\n"
"v_mfma_f32_16x16x32_bf16 v[204:207], a[96:99], v[40:43], v[204:207]\n"
"buffer_store_dwordx4 v[64:67], v192, s[28:31], s4 offen sc1\n"
"v_mfma_f32_16x16x32_bf16 v[204:207], a[108:111], v[44:47], v[204:207]\n"
"v_max_f32 v115, v115, v123\n"
"v_max3_f32 v170, v170, v143, v112\n"
"v_mfma_f32_16x16x32_bf16 v[204:207], a[120:123], v[48:51], v[204:207]\n"
"ds_read_b64_tr_b16 v[122:123], v183 offset:27080\n"
"v_max3_f32 v170, v170, v113, v114\n"
"v_mfma_f32_16x16x32_bf16 v[204:207], a[132:135], v[52:55], v[204:207]\n"
"v_max_f32 v116, v116, v124\n"
"v_max_f32 v117, v117, v125\n"
"v_mfma_f32_16x16x32_bf16 v[208:211], a[76:79], v[32:35], 0\n"
"ds_read_b64_tr_b16 v[124:125], v182 offset:27848\n"
"v_max_f32 v118, v118, v126\n"
"v_mfma_f32_16x16x32_bf16 v[208:211], a[88:91], v[36:39], v[208:211]\n"
"v_max_f32 v119, v119, v127\n"
"v_max3_f32 v170, v170, v115, v116\n"
"v_mfma_f32_16x16x32_bf16 v[208:211], a[100:103], v[40:43], v[208:211]\n"
"ds_read_b64_tr_b16 v[126:127], v183 offset:27848\n"
"v_max3_f32 v170, v170, v117, v118\n"
"v_mfma_f32_16x16x32_bf16 v[208:211], a[112:115], v[44:47], v[208:211]\n"
"s_add_u32 m0, s34, 4096\n"
"v_mov_b64 v[252:253], 0\n"
"v_mov_b64 v[254:255], 0\n"
"v_mfma_f32_16x16x32_bf16 v[208:211], a[124:127], v[48:51], v[208:211]\n"
"buffer_load_dwordx4 v191, s[8:11], s0 offen lds\n"
"v_mov_b64 v[98:99], 0\n"
"v_mfma_f32_16x16x32_bf16 v[208:211], a[136:139], v[52:55], v[208:211]\n"
"s_waitcnt vmcnt(12)\n"
"v_readfirstlane_b32 s6, v201\n"
"v_mov_b64 v[100:101], 0\n"
"v_mfma_f32_16x16x32_bf16 v[212:215], a[144:147], v[32:35], 0\n"
"s_and_b32 s6, s6, 0xffffff00\n"
"v_or_b32 v201, 5, s6\n"
"v_mov_b64 v[106:107], 0\n"
"v_mfma_f32_16x16x32_bf16 v[212:215], a[156:159], v[36:39], v[212:215]\n"
"s_add_u32 s0, s0, s33\n"
"ds_write_b128 v202, v[56:59] offset:55296\n"
"v_mfma_f32_16x16x32_bf16 v[212:215], a[168:171], v[40:43], v[212:215]\n"
"s_mov_b64 exec, 1\n"
"buffer_store_dword v201, off, s[28:31], s40 sc1\n"
"s_mov_b64 exec, -1\n"
"v_mfma_f32_16x16x32_bf16 v[212:215], a[180:183], v[44:47], v[212:215]\n"
"ds_write_b64 v203, v[60:61] offset:55296\n"
"v_mfma_f32_16x16x32_bf16 v[212:215], a[192:195], v[48:51], v[212:215]\n"
"s_add_u32 m0, s34, 2048\n"
"buffer_load_dwordx4 v[56:59], v192, s[28:31], s38 offen sc1\n"
"v_mov_b64 v[108:109], 0\n"
"v_mfma_f32_16x16x32_bf16 v[212:215], a[204:207], v[52:55], v[212:215]\n"
"v_max_f32 v171, v170, v119\n"
"v_max_f32 v170, v170, v119\n"
"v_cvt_pk_bf16_f32 v204, v204, v205\n"
"v_cvt_pk_bf16_f32 v205, v206, v207\n"
"v_permlane16_swap_b32 v170, v171\n"
"v_max_f32 v170, v170, v171\n"
"v_mov_b32 v171, v170\n"
"v_cvt_pk_bf16_f32 v206, v208, v209\n"
"v_cvt_pk_bf16_f32 v207, v210, v211\n"
"v_mfma_f32_16x16x32_bf16 v[208:211], a[148:151], v[32:35], 0\n"
"buffer_load_dwordx4 v190, s[8:11], s1 offen lds\n"
"ds_write_b128 v186, v[204:207] offset:60896\n"
"v_mfma_f32_16x16x32_bf16 v[208:211], a[160:163], v[36:39], v[208:211]\n"
"v_permlane32_swap_b32 v170, v171\n"
"v_max_f32 v170, v170, v171\n"
"v_mul_f32 v170, 0xbe553b95, v170\n"
"v_pk_fma_f32 v[136:137], v[136:137], s[70:71], v[170:171] op_sel_hi:[1,0,0]\n"
"v_pk_fma_f32 v[138:139], v[138:139], s[70:71], v[170:171] op_sel_hi:[1,0,0]\n"
"v_pk_fma_f32 v[140:141], v[140:141], s[70:71], v[170:171] op_sel_hi:[1,0,0]\n"
"v_pk_fma_f32 v[142:143], v[142:143], s[70:71], v[170:171] op_sel_hi:[1,0,0]\n"
"v_pk_fma_f32 v[112:113], v[112:113], s[70:71], v[170:171] op_sel_hi:[1,0,0]\n"
"v_pk_fma_f32 v[114:115], v[114:115], s[70:71], v[170:171] op_sel_hi:[1,0,0]\n"
"v_pk_fma_f32 v[116:117], v[116:117], s[70:71], v[170:171] op_sel_hi:[1,0,0]\n"
"v_pk_fma_f32 v[118:119], v[118:119], s[70:71], v[170:171] op_sel_hi:[1,0,0]\n"
"v_mfma_f32_16x16x32_bf16 v[208:211], a[172:175], v[40:43], v[208:211]\n"
"buffer_load_dwordx2 v[60:61], v193, s[28:31], s38 offen offset:1024 sc1\n"
"v_exp_f32 v136, v136\n"
"v_mfma_f32_16x16x32_bf16 v[208:211], a[184:187], v[44:47], v[208:211]\n"
"s_add_u32 s38, s38, 1572864\n"
"v_exp_f32 v137, v137\n"
"v_mfma_f32_16x16x32_bf16 v[208:211], a[196:199], v[48:51], v[208:211]\n"
"v_exp_f32 v138, v138\n"
"v_mfma_f32_16x16x32_bf16 v[208:211], a[208:211], v[52:55], v[208:211]\n"
"s_add_u32 m0, s34, 5120\n"
"v_exp_f32 v139, v139\n"
"v_mfma_f32_16x16x32_bf16 v[204:207], a[152:155], v[32:35], 0\n"
"buffer_load_dwordx4 v191, s[8:11], s1 offen lds\n"
"ds_read_b128 v[32:35], v188 offset:6144\n"
"v_mfma_f32_16x16x32_bf16 v[204:207], a[164:167], v[36:39], v[204:207]\n"
"s_add_u32 s1, s1, s33\n"
"ds_read_b128 v[36:39], v189 offset:6144\n"
"v_cvt_pk_bf16_f32 v82, v136, v138\n"
"v_mfma_f32_16x16x32_bf16 v[204:207], a[176:179], v[40:43], v[204:207]\n"
"ds_read_b128 v[40:43], v188 offset:7168\n"
"v_cvt_pk_bf16_f32 v83, v137, v139\n"
"v_mfma_f32_16x16x32_bf16 v[204:207], a[188:191], v[44:47], v[204:207]\n"
"ds_read_b128 v[44:47], v189 offset:7168\n"
"ds_read_b128 v[136:139], v178 offset:13504\n"
"v_mfma_f32_16x16x32_bf16 v[204:207], a[200:203], v[48:51], v[204:207]\n"
"v_exp_f32 v140, v140\n"
"v_mfma_f32_16x16x32_bf16 v[204:207], a[212:215], v[52:55], v[204:207]\n"
"v_exp_f32 v141, v141\n"
"v_exp_f32 v142, v142\n"
"s_waitcnt vmcnt(12) lgkmcnt(5)\n"
"ds_read_b128 v[48:51], v188 offset:8192\n"
"ds_read_b128 v[52:55], v189 offset:8192\n"
"v_exp_f32 v143, v143\n"
"v_cvt_pk_bf16_f32 v84, v140, v142\n"
"v_cvt_pk_bf16_f32 v85, v141, v143\n"
"v_cvt_pk_bf16_f32 v144, v212, v213\n"
"v_cvt_pk_bf16_f32 v145, v214, v215\n"
"v_smfmac_f32_16x16x64_bf16 v[252:255], v[82:85], v[216:223], v63\n"
"s_waitcnt lgkmcnt(5)\n"
"ds_read_b64_tr_b16 v[216:217], v182 offset:27136\n"
"v_smfmac_f32_16x16x64_bf16 v[98:101], v[82:85], v[72:79], v63\n"
"s_waitcnt lgkmcnt(4)\n"
"ds_read_b64_tr_b16 v[218:219], v183 offset:27136\n"
"v_smfmac_f32_16x16x64_bf16 v[106:109], v[82:85], v[128:135], v63\n"
"s_add_u32 m0, s34, 6144\n"
"s_nop 0\n"
"buffer_load_dwordx4 v190, s[8:11], s32 offen lds\n"
"v_mfma_f32_16x16x32_bf16 v[82:85], v[172:175], v[82:85], 0\n"
"s_barrier\n"
"v_mfma_f32_16x16x32_bf16 v[212:215], a[0:3], v[32:35], 0\n"
"ds_read_b64_tr_b16 v[220:221], v182 offset:27904\n"
"ds_read_b64_tr_b16 v[222:223], v183 offset:27904\n"
"v_mfma_f32_16x16x32_bf16 v[212:215], a[12:15], v[36:39], v[212:215]\n"
"ds_read_b128 v[128:131], v179 offset:13504\n"
"v_cvt_pk_bf16_f32 v146, v208, v209\n"
"v_mfma_f32_16x16x32_bf16 v[212:215], a[24:27], v[40:43], v[212:215]\n"
"ds_read_b64_tr_b16 v[72:73], v182 offset:27144\n"
"v_cvt_pk_bf16_f32 v147, v210, v211\n"
"v_mfma_f32_16x16x32_bf16 v[212:215], a[36:39], v[44:47], v[212:215]\n"
"s_waitcnt lgkmcnt(7)\n"
"ds_read_b64_tr_b16 v[74:75], v183 offset:27144\n"
"v_exp_f32 v112, v112\n"
"v_mfma_f32_16x16x32_bf16 v[212:215], a[48:51], v[48:51], v[212:215]\n"
"s_waitcnt lgkmcnt(7)\n"
"ds_read_b64_tr_b16 v[76:77], v182 offset:27912\n"
"v_exp_f32 v113, v113\n"
"v_mfma_f32_16x16x32_bf16 v[212:215], a[60:63], v[52:55], v[212:215]\n"
"s_add_u32 m0, s34, 9216\n"
"ds_read_b64_tr_b16 v[78:79], v183 offset:27912\n"
"buffer_load_dwordx4 v191, s[8:11], s32 offen lds\n"
"v_mfma_f32_16x16x32_bf16 v[208:211], a[4:7], v[32:35], 0\n"
"s_add_u32 s32, s32, s33\n"
"ds_read_b128 v[140:143], v178 offset:27008\n"
"v_exp_f32 v114, v114\n"
"v_mfma_f32_16x16x32_bf16 v[208:211], a[16:19], v[36:39], v[208:211]\n"
"ds_read_b128 v[132:135], v179 offset:27008\n"
"ds_read_b64_tr_b16 v[20:21], v236 offset:12288\n"
"v_mfma_f32_16x16x32_bf16 v[208:211], a[28:31], v[40:43], v[208:211]\n"
"v_exp_f32 v115, v115\n"
"v_mfma_f32_16x16x32_bf16 v[208:211], a[40:43], v[44:47], v[208:211]\n"
"v_cvt_pk_bf16_f32 v90, v112, v114\n"
"v_cvt_pk_bf16_f32 v91, v113, v115\n"
"v_mfma_f32_16x16x32_bf16 v[208:211], a[52:55], v[48:51], v[208:211]\n"
"ds_read_b128 v[112:115], v180\n"
"ds_read_b64_tr_b16 v[22:23], v236 offset:18432\n"
"v_mfma_f32_16x16x32_bf16 v[208:211], a[64:67], v[52:55], v[208:211]\n"
"v_exp_f32 v116, v116\n"
"v_exp_f32 v117, v117\n"
"s_add_u32 m0, s34, 7168\n"
"v_exp_f32 v118, v118\n"
"buffer_load_dwordx4 v190, s[8:11], s0 offen lds\n"
"v_exp_f32 v119, v119\n"
"v_cvt_pk_bf16_f32 v92, v116, v118\n"
"v_cvt_pk_bf16_f32 v93, v117, v119\n"
"v_cvt_pk_bf16_f32 v158, v204, v205\n"
"v_cvt_pk_bf16_f32 v159, v206, v207\n"
"v_smfmac_f32_16x16x64_bf16 v[106:109], v[90:93], v[120:127], v63\n"
"s_waitcnt lgkmcnt(5)\n"
"ds_read_b128 v[120:123], v181\n"
"v_smfmac_f32_16x16x64_bf16 v[252:255], v[90:93], v[216:223], v63\n"
"ds_read_b128 v[116:119], v180 offset:13504\n"
"v_smfmac_f32_16x16x64_bf16 v[98:101], v[90:93], v[72:79], v63\n"
"ds_read_b128 v[124:127], v181 offset:13504\n"
"v_mfma_f32_16x16x32_bf16 v[90:93], v[172:175], v[90:93], v[82:85]\n"
"ds_read_b64_tr_b16 v[24:25], v236 offset:12296\n"
"v_cvt_pk_bf16_f32 v106, v106, v107\n"
"v_mfma_f32_16x16x32_bf16 v[204:207], a[8:11], v[32:35], 0\n"
"v_cvt_pk_bf16_f32 v107, v108, v109\n"
"v_cvt_pk_bf16_f32 v252, v252, v253\n"
"v_mfma_f32_16x16x32_bf16 v[204:207], a[20:23], v[36:39], v[204:207]\n"
"s_add_u32 m0, s34, 10240\n"
"ds_read_b64 v[82:83], v178 offset:13568\n"
"buffer_load_dwordx4 v191, s[8:11], s0 offen lds\n"
"v_mfma_f32_16x16x32_bf16 v[204:207], a[32:35], v[40:43], v[204:207]\n"
"s_add_u32 s0, s0, s33\n"
"ds_read_b64 v[84:85], v179 offset:13568\n"
"ds_read_b64_tr_b16 v[26:27], v236 offset:18440\n"
"v_mfma_f32_16x16x32_bf16 v[204:207], a[44:47], v[44:47], v[204:207]\n"
"v_rcp_f32 v171, v90\n"
"v_mfma_f32_16x16x32_bf16 v[204:207], a[56:59], v[48:51], v[204:207]\n"
"v_cvt_pk_bf16_f32 v171, v171, v171\n"
"v_and_b32 v92, v171, v176\n"
"v_mfma_f32_16x16x32_bf16 v[204:207], a[68:71], v[52:55], v[204:207]\n"
"ds_read_b64 v[90:91], v178 offset:27072\n"
"v_and_b32 v93, v171, v177\n"
"v_cvt_pk_bf16_f32 v253, v254, v255\n"
"v_cvt_pk_bf16_f32 v98, v98, v99\n"
"v_mfma_f32_16x16x16_bf16 v[106:109], v[106:107], v[92:93], 0\n"
"ds_read_b64_tr_b16 v[28:29], v235 offset:12288\n"
"v_cvt_pk_bf16_f32 v99, v100, v101\n"
"v_mfma_f32_16x16x16_bf16 v[252:255], v[252:253], v[92:93], 0\n"
"ds_read_b64_tr_b16 v[30:31], v235 offset:18432\n"
"v_cvt_pk_bf16_f32 v212, v212, v213\n"
"v_mfma_f32_16x16x16_bf16 v[98:101], v[98:99], v[92:93], 0\n"
"s_waitcnt lgkmcnt(5)\n"
"v_cvt_pk_bf16_f32 v213, v214, v215\n"
"v_cvt_pk_bf16_f32 v106, v106, v107\n"
"v_mfma_f32_16x16x32_bf16 v[136:139], v[136:139], v[148:151], 0\n"
"s_waitcnt lgkmcnt(4)\n"
"ds_read_b64 v[92:93], v179 offset:27072\n"
"v_cvt_pk_bf16_f32 v252, v252, v253\n"
"v_mfma_f32_16x16x32_bf16 v[136:139], v[80:83], v[160:163], v[136:139]\n"
"s_add_u32 m0, s34, 8192\n"
"v_cvt_pk_bf16_f32 v253, v254, v255\n"
"v_cvt_pk_bf16_f32 v254, v98, v99\n"
"v_mfma_f32_16x16x32_bf16 v[128:131], v[128:131], v[148:151], 0\n"
"buffer_load_dwordx4 v190, s[8:11], s1 offen lds\n"
"ds_read_b64 v[98:99], v180 offset:64\n"
"v_mfma_f32_16x16x32_bf16 v[128:131], v[84:87], v[162:165], v[128:131]\n"
"s_waitcnt lgkmcnt(4)\n"
"v_cvt_pk_bf16_f32 v255, v100, v101\n"
"v_cvt_pk_bf16_f32 v107, v108, v109\n"
"v_mfma_f32_16x16x32_bf16 v[140:143], v[140:143], v[148:151], 0\n"
"ds_read_b64 v[100:101], v181 offset:64\n"
"ds_read_b64 v[108:109], v181 offset:13568\n"
"v_mfma_f32_16x16x32_bf16 v[140:143], v[88:91], v[160:163], v[140:143]\n"
"s_mov_b64 exec, s[46:47]\n"
"v_add_u32 v185, 0xc0, v185\n"
"s_mov_b64 exec, -1\n"
"v_cvt_pk_bf16_f32 v214, v208, v209\n"
"v_cvt_pk_bf16_f32 v215, v210, v211\n"
"v_mfma_f32_16x16x32_bf16 v[208:211], a[80:83], v[32:35], 0\n"
"buffer_store_dwordx4 v[252:255], v192, s[28:31], s37 offen sc1\n"
"v_mfma_f32_16x16x32_bf16 v[208:211], a[92:95], v[36:39], v[208:211]\n"
"s_waitcnt lgkmcnt(3)\n"
"ds_write_b128 v187, v[212:215]\n"
"v_mfma_f32_16x16x32_bf16 v[132:135], v[132:135], v[148:151], 0\n"
"s_waitcnt lgkmcnt(3)\n"
"v_cvt_pk_bf16_f32 v204, v204, v205\n"
"v_cvt_pk_bf16_f32 v205, v206, v207\n"
"v_mfma_f32_16x16x32_bf16 v[132:135], v[92:95], v[162:165], v[132:135]\n"
"s_waitcnt lgkmcnt(2)\n"
"v_max_f32 v136, v136, v128\n"
"v_max_f32 v137, v137, v129\n"
"v_mfma_f32_16x16x32_bf16 v[112:115], v[112:115], v[148:151], 0\n"
"s_waitcnt lgkmcnt(1)\n"
"ds_read_b64_tr_b16 v[128:129], v182 offset:13576\n"
"v_max_f32 v138, v138, v130\n"
"v_mfma_f32_16x16x32_bf16 v[112:115], v[96:99], v[160:163], v[112:115]\n"
"v_max_f32 v139, v139, v131\n"
"v_max3_f32 v170, v136, v137, v138\n"
"v_mfma_f32_16x16x32_bf16 v[120:123], v[120:123], v[148:151], 0\n"
"s_add_u32 m0, s34, 11264\n"
"ds_read_b64_tr_b16 v[130:131], v183 offset:13576\n"
"v_max_f32 v140, v140, v132\n"
"v_mfma_f32_16x16x32_bf16 v[120:123], v[100:103], v[162:165], v[120:123]\n"
"v_max_f32 v141, v141, v133\n"
"v_max_f32 v142, v142, v134\n"
"v_mfma_f32_16x16x32_bf16 v[124:127], v[124:127], v[148:151], 0\n"
"buffer_load_dwordx4 v191, s[8:11], s1 offen lds\n"
"ds_read_b64_tr_b16 v[132:133], v182 offset:14344\n"
"v_mfma_f32_16x16x32_bf16 v[124:127], v[108:111], v[162:165], v[124:127]\n"
"s_add_u32 s1, s1, s33\n"
"v_max_f32 v143, v143, v135\n"
"v_max3_f32 v170, v170, v139, v140\n"
"v_mfma_f32_16x16x32_bf16 v[208:211], a[104:107], v[40:43], v[208:211]\n"
"ds_read_b64_tr_b16 v[134:135], v183 offset:14344\n"
"v_max3_f32 v170, v170, v141, v142\n"
"v_mfma_f32_16x16x32_bf16 v[208:211], a[116:119], v[44:47], v[208:211]\n"
"v_max_f32 v112, v112, v120\n"
"v_max_f32 v113, v113, v121\n"
"v_mfma_f32_16x16x32_bf16 v[208:211], a[128:131], v[48:51], v[208:211]\n"
"buffer_store_dwordx2 v[106:107], v193, s[28:31], s37 offen offset:1024 sc1\n"
"v_mfma_f32_16x16x32_bf16 v[208:211], a[140:143], v[52:55], v[208:211]\n"
"s_add_u32 s37, s37, 1572864\n"
"ds_read_b64 v[106:107], v180 offset:13568\n"
"ds_read_b64_tr_b16 v[120:121], v182 offset:40584\n"
"v_mfma_f32_16x16x32_bf16 v[212:215], a[72:75], v[32:35], 0\n"
"s_add_u32 s4, s37, 6291456\n"
"v_max_f32 v114, v114, v122\n"
"v_max_f32 v115, v115, v123\n"
"v_mfma_f32_16x16x32_bf16 v[212:215], a[84:87], v[36:39], v[212:215]\n"
"ds_read_b64_tr_b16 v[122:123], v183 offset:40584\n"
"v_cvt_pk_bf16_f32 v206, v208, v209\n"
"v_mfma_f32_16x16x32_bf16 v[212:215], a[96:99], v[40:43], v[212:215]\n"
"v_cvt_pk_bf16_f32 v207, v210, v211\n"
"v_max3_f32 v170, v170, v143, v112\n"
"v_mfma_f32_16x16x32_bf16 v[212:215], a[108:111], v[44:47], v[212:215]\n"
"ds_write_b128 v187, v[204:207] offset:64\n"
"v_mfma_f32_16x16x32_bf16 v[212:215], a[120:123], v[48:51], v[212:215]\n"
"v_max3_f32 v170, v170, v113, v114\n"
"v_mov_b64 v[252:253], 0\n"
"v_mfma_f32_16x16x32_bf16 v[212:215], a[132:135], v[52:55], v[212:215]\n"
"s_waitcnt lgkmcnt(3)\n"
"v_mov_b64 v[254:255], 0\n"
"v_mov_b64 v[68:69], -1\n"
"v_mfma_f32_16x16x32_bf16 v[116:119], v[116:119], v[148:151], 0\n"
"buffer_store_dwordx2 v[68:69], v193, s[28:31], s4 offen offset:1024 sc1\n"
"v_mfma_f32_16x16x32_bf16 v[116:119], v[104:107], v[160:163], v[116:119]\n"
"s_add_u32 s4, s37, 7864320\n"
"ds_read_b128 v[68:71], v192 offset:55296\n"
"v_mov_b64 v[98:99], 0\n"
"v_mfma_f32_16x16x32_bf16 v[208:211], a[76:79], v[32:35], 0\n"
"v_mov_b64 v[100:101], 0\n"
"v_mov_b64 v[106:107], 0\n"
"v_mfma_f32_16x16x32_bf16 v[208:211], a[88:91], v[36:39], v[208:211]\n"
"v_mov_b64 v[108:109], 0\n"
"v_max_f32 v116, v116, v124\n"
"v_mfma_f32_16x16x32_bf16 v[208:211], a[100:103], v[40:43], v[208:211]\n"
"v_max_f32 v117, v117, v125\n"
"v_max_f32 v118, v118, v126\n"
"v_mfma_f32_16x16x32_bf16 v[208:211], a[112:115], v[44:47], v[208:211]\n"
"ds_read_b64_tr_b16 v[124:125], v182 offset:41352\n"
"v_max_f32 v119, v119, v127\n"
"v_mfma_f32_16x16x32_bf16 v[208:211], a[124:127], v[48:51], v[208:211]\n"
"s_waitcnt vmcnt(12)\n"
"ds_read_b64_tr_b16 v[126:127], v183 offset:41352\n"
"buffer_load_dword v200, off, s[28:31], s39 sc1\n"
"v_mfma_f32_16x16x32_bf16 v[208:211], a[136:139], v[52:55], v[208:211]\n"
"ds_write_b128 v202, v[56:59] offset:36864\n"
"v_mfma_f32_16x16x32_bf16 v[204:207], a[144:147], v[32:35], 0\n"
"s_waitcnt lgkmcnt(3)\n"
"v_max3_f32 v170, v170, v115, v116\n"
"v_max3_f32 v170, v170, v117, v118\n"
"v_mfma_f32_16x16x32_bf16 v[232:235], a[216:219], v[68:71], 0\n"
"v_max_f32 v171, v170, v119\n"
"v_max_f32 v170, v170, v119\n"
"v_mfma_f32_16x16x32_bf16 v[236:239], a[220:223], v[68:71], 0\n"
"s_nop 0\n"
"v_permlane16_swap_b32 v170, v171\n"
"v_mfma_f32_16x16x32_bf16 v[240:243], a[224:227], v[68:71], 0\n"
"ds_read_b128 v[68:71], v192 offset:57344\n"
"v_max_f32 v170, v170, v171\n"
"v_mfma_f32_16x16x32_bf16 v[204:207], a[156:159], v[36:39], v[204:207]\n"
"v_cvt_pk_bf16_f32 v212, v212, v213\n"
"v_mov_b32 v171, v170\n"
"v_mfma_f32_16x16x32_bf16 v[204:207], a[168:171], v[40:43], v[204:207]\n"
"v_mov_b64 v[64:65], -1\n"
"v_mov_b64 v[66:67], -1\n"
"v_mfma_f32_16x16x32_bf16 v[204:207], a[180:183], v[44:47], v[204:207]\n"
"buffer_store_dwordx4 v[64:67], v192, s[28:31], s4 offen sc1\n"
"v_mfma_f32_16x16x32_bf16 v[204:207], a[192:195], v[48:51], v[204:207]\n"
"s_nop 0\n"
"ds_read_b128 v[64:67], v192 offset:56320\n"
"v_permlane32_swap_b32 v170, v171\n"
"v_max_f32 v170, v170, v171\n"
"v_mul_f32 v170, 0xbe553b95, v170\n"
"v_pk_fma_f32 v[136:137], v[136:137], s[70:71], v[170:171] op_sel_hi:[1,0,0]\n"
"v_pk_fma_f32 v[138:139], v[138:139], s[70:71], v[170:171] op_sel_hi:[1,0,0]\n"
"v_pk_fma_f32 v[140:141], v[140:141], s[70:71], v[170:171] op_sel_hi:[1,0,0]\n"
"v_pk_fma_f32 v[142:143], v[142:143], s[70:71], v[170:171] op_sel_hi:[1,0,0]\n"
"v_pk_fma_f32 v[112:113], v[112:113], s[70:71], v[170:171] op_sel_hi:[1,0,0]\n"
"v_pk_fma_f32 v[114:115], v[114:115], s[70:71], v[170:171] op_sel_hi:[1,0,0]\n"
"v_pk_fma_f32 v[116:117], v[116:117], s[70:71], v[170:171] op_sel_hi:[1,0,0]\n"
"v_pk_fma_f32 v[118:119], v[118:119], s[70:71], v[170:171] op_sel_hi:[1,0,0]\n"
"v_mfma_f32_16x16x32_bf16 v[204:207], a[204:207], v[52:55], v[204:207]\n"
"s_waitcnt vmcnt(12)\n"
"ds_write_b64 v203, v[60:61] offset:36864\n"
"v_cvt_pk_bf16_f32 v213, v214, v215\n"
"v_exp_f32 v136, v136\n"
"v_exp_f32 v137, v137\n"
"v_exp_f32 v138, v138\n"
"v_exp_f32 v139, v139\n"
"v_cvt_pk_bf16_f32 v214, v208, v209\n"
"v_cvt_pk_bf16_f32 v82, v136, v138\n"
"v_cvt_pk_bf16_f32 v215, v210, v211\n"
"v_mfma_f32_16x16x32_bf16 v[208:211], a[148:151], v[32:35], 0\n"
"s_waitcnt lgkmcnt(1)\n"
"ds_write_b128 v187, v[212:215] offset:128\n"
"v_mfma_f32_16x16x32_bf16 v[208:211], a[160:163], v[36:39], v[208:211]\n"
"v_cvt_pk_bf16_f32 v83, v137, v139\n"
"v_exp_f32 v140, v140\n"
"v_mfma_f32_16x16x32_bf16 v[232:235], a[228:231], v[64:67], v[232:235]\n"
"ds_read_b128 v[136:139], v178 offset:27008\n"
"v_exp_f32 v141, v141\n"
"v_mfma_f32_16x16x32_bf16 v[232:235], a[240:243], v[68:71], v[232:235]\n"
"v_exp_f32 v142, v142\n"
"v_mfma_f32_16x16x32_bf16 v[236:239], a[232:235], v[64:67], v[236:239]\n"
"v_exp_f32 v143, v143\n"
"v_mfma_f32_16x16x32_bf16 v[236:239], a[244:247], v[68:71], v[236:239]\n"
"v_cvt_pk_bf16_f32 v84, v140, v142\n"
"v_cvt_pk_bf16_f32 v85, v141, v143\n"
"v_mfma_f32_16x16x32_bf16 v[240:243], a[236:239], v[64:67], v[240:243]\n"
"ds_read_b128 v[64:67], v192 offset:58368\n"
"ds_read_b128 v[140:143], v180\n"
"v_mfma_f32_16x16x32_bf16 v[240:243], a[248:251], v[68:71], v[240:243]\n"
"ds_read_b128 v[68:71], v192 offset:59392\n"
"v_cvt_pk_bf16_f32 v148, v204, v205\n"
"v_smfmac_f32_16x16x64_bf16 v[252:255], v[82:85], v[224:231], v63\n"
"ds_read_b64_tr_b16 v[224:225], v182 offset:40640\n"
"v_smfmac_f32_16x16x64_bf16 v[98:101], v[82:85], v[244:251], v63\n"
"ds_read_b64_tr_b16 v[226:227], v183 offset:40640\n"
"v_smfmac_f32_16x16x64_bf16 v[106:109], v[82:85], v[128:135], v63\n"
"ds_read_b64_tr_b16 v[228:229], v182 offset:41408\n"
"v_mfma_f32_16x16x32_bf16 v[82:85], v[172:175], v[82:85], 0\n"
"ds_read_b64_tr_b16 v[230:231], v183 offset:41408\n"
"v_cvt_pk_bf16_f32 v149, v206, v207\n"
"v_mfma_f32_16x16x32_bf16 v[208:211], a[172:175], v[40:43], v[208:211]\n"
"ds_read_b64_tr_b16 v[244:245], v182 offset:40648\n"
"v_exp_f32 v112, v112\n"
"v_mfma_f32_16x16x32_bf16 v[208:211], a[184:187], v[44:47], v[208:211]\n"
"s_waitcnt lgkmcnt(7)\n"
"ds_read_b64_tr_b16 v[246:247], v183 offset:40648\n"
"v_exp_f32 v113, v113\n"
"v_mfma_f32_16x16x32_bf16 v[208:211], a[196:199], v[48:51], v[208:211]\n"
"ds_read_b64_tr_b16 v[248:249], v182 offset:41416\n"
"v_exp_f32 v114, v114\n"
"v_mfma_f32_16x16x32_bf16 v[208:211], a[208:211], v[52:55], v[208:211]\n"
"ds_read_b64_tr_b16 v[250:251], v183 offset:41416\n"
"ds_read_b128 v[128:131], v179 offset:27008\n"
"v_mfma_f32_16x16x32_bf16 v[232:235], a[252:255], v[64:67], v[232:235]\n"
"s_waitcnt lgkmcnt(9)\n"
"v_exp_f32 v115, v115\n"
"v_mfma_f32_16x16x32_bf16 v[232:235], v[8:11], v[68:71], v[232:235]\n"
"ds_read_b128 v[132:135], v181\n"
"v_cvt_pk_bf16_f32 v90, v112, v114\n"
"v_mfma_f32_16x16x32_bf16 v[236:239], v[0:3], v[64:67], v[236:239]\n"
"v_cvt_pk_bf16_f32 v91, v113, v115\n"
"v_cvt_pk_bf16_f32 v150, v208, v209\n"
"v_mfma_f32_16x16x32_bf16 v[236:239], v[12:15], v[68:71], v[236:239]\n"
"ds_read_b128 v[112:115], v180 offset:13504\n"
"v_cvt_pk_bf16_f32 v151, v210, v211\n"
"v_mfma_f32_16x16x32_bf16 v[240:243], v[4:7], v[64:67], v[240:243]\n"
"ds_read_b128 v[64:67], v192 offset:60416\n"
"v_exp_f32 v116, v116\n"
"v_mfma_f32_16x16x32_bf16 v[240:243], v[16:19], v[68:71], v[240:243]\n"
"v_exp_f32 v117, v117\n"
"v_mfma_f32_16x16x32_bf16 v[212:215], a[152:155], v[32:35], 0\n"
"ds_read_b128 v[32:35], v188\n"
"v_exp_f32 v118, v118\n"
"v_mfma_f32_16x16x32_bf16 v[212:215], a[164:167], v[36:39], v[212:215]\n"
"ds_read_b128 v[36:39], v189\n"
"v_cvt_pk_bf16_f32 v92, v116, v118\n"
"v_mfma_f32_16x16x32_bf16 v[212:215], a[176:179], v[40:43], v[212:215]\n"
"ds_read_b128 v[40:43], v188 offset:1024\n"
"v_exp_f32 v119, v119\n"
"v_mfma_f32_16x16x32_bf16 v[212:215], a[188:191], v[44:47], v[212:215]\n"
"ds_read_b128 v[44:47], v189 offset:1024\n"
"v_cvt_pk_bf16_f32 v93, v117, v119\n"
"v_mfma_f32_16x16x32_bf16 v[212:215], a[200:203], v[48:51], v[212:215]\n"
"s_waitcnt vmcnt(11) lgkmcnt(4)\n"
"ds_read_b128 v[48:51], v188 offset:2048\n"
"v_mfma_f32_16x16x32_bf16 v[212:215], a[212:215], v[52:55], v[212:215]\n"
".Lic4%=:\n"
"s_cbranch_execz .Lib1%=\n"
"ds_read_b128 v[52:55], v189 offset:2048\n"
"v_smfmac_f32_16x16x64_bf16 v[252:255], v[90:93], v[224:231], v63\n"
"s_waitcnt lgkmcnt(5)\n"
"v_smfmac_f32_16x16x64_bf16 v[98:101], v[90:93], v[244:251], v63\n"
"v_mfma_f32_16x16x32_bf16 v[232:235], v[20:23], v[64:67], v[232:235]\n"
"s_waitcnt lgkmcnt(4)\n"
"s_add_u32 m0, s34, 0\n"
"v_mfma_f32_16x16x32_bf16 v[240:243], v[28:31], v[64:67], v[240:243]\n"
"buffer_load_dwordx4 v190, s[8:11], s32 offen lds\n"
"v_cvt_pk_bf16_f32 v162, v212, v213\n"
"v_mfma_f32_16x16x32_bf16 v[236:239], v[24:27], v[64:67], v[236:239]\n"
"v_cvt_pk_bf16_f32 v252, v252, v253\n"
"v_cvt_pk_bf16_f32 v253, v254, v255\n"
"v_smfmac_f32_16x16x64_bf16 v[106:109], v[90:93], v[120:127], v63\n"
"ds_read_b128 v[120:123], v181 offset:13504\n"
"v_mfma_f32_16x16x32_bf16 v[90:93], v[172:175], v[90:93], v[82:85]\n"
"v_cvt_pk_bf16_f32 v232, v232, v233\n"
"v_cvt_pk_bf16_f32 v233, v234, v235\n"
"v_mfma_f32_16x16x32_bf16 v[204:207], a[0:3], v[32:35], 0\n"
"ds_read_b64 v[82:83], v178 offset:27072\n"
"ds_read_b64 v[84:85], v179 offset:27072\n"
"v_mfma_f32_16x16x32_bf16 v[204:207], a[12:15], v[36:39], v[204:207]\n"
"s_waitcnt lgkmcnt(6)\n"
"v_cvt_pk_bf16_f32 v240, v240, v241\n"
"v_cvt_pk_bf16_f32 v234, v236, v237\n"
"v_mfma_f32_16x16x32_bf16 v[204:207], a[24:27], v[40:43], v[204:207]\n"
"s_waitcnt lgkmcnt(5)\n"
"v_cvt_pk_bf16_f32 v235, v238, v239\n"
"v_cvt_pk_bf16_f32 v241, v242, v243\n"
"v_mfma_f32_16x16x32_bf16 v[204:207], a[36:39], v[44:47], v[204:207]\n"
"s_waitcnt lgkmcnt(4)\n"
"ds_write_b128 v196, v[232:235] offset:30720\n"
"v_mfma_f32_16x16x32_bf16 v[204:207], a[48:51], v[48:51], v[204:207]\n"
"s_waitcnt lgkmcnt(4)\n"
"v_rcp_f32 v171, v90\n"
"v_mfma_f32_16x16x32_bf16 v[204:207], a[60:63], v[52:55], v[204:207]\n"
"s_add_u32 m0, s34, 3072\n"
"v_cvt_pk_bf16_f32 v171, v171, v171\n"
"v_and_b32 v92, v171, v176\n"
"v_mfma_f32_16x16x32_bf16 v[208:211], a[4:7], v[32:35], 0\n"
"s_waitcnt lgkmcnt(2)\n"
"buffer_load_dwordx4 v191, s[8:11], s32 offen lds\n"
"ds_read_b64 v[90:91], v180 offset:64\n"
"v_mfma_f32_16x16x32_bf16 v[136:139], v[136:139], v[152:155], 0\n"
"s_waitcnt lgkmcnt(2)\n"
"ds_write_b64 v197, v[240:241] offset:30720\n"
"v_mfma_f32_16x16x32_bf16 v[136:139], v[80:83], v[164:167], v[136:139]\n"
"s_add_u32 s32, s32, s33\n"
"v_and_b32 v93, v171, v177\n"
"v_cvt_pk_bf16_f32 v163, v214, v215\n"
"v_mfma_f32_16x16x32_bf16 v[128:131], v[128:131], v[152:155], 0\n"
"v_cvt_pk_bf16_f32 v98, v98, v99\n"
"v_cvt_pk_bf16_f32 v99, v100, v101\n"
"v_mfma_f32_16x16x32_bf16 v[128:131], v[84:87], v[166:169], v[128:131]\n"
"v_cvt_pk_bf16_f32 v106, v106, v107\n"
"v_cvt_pk_bf16_f32 v107, v108, v109\n"
"v_mfma_f32_16x16x16_bf16 v[252:255], v[252:253], v[92:93], 0\n"
"v_cvt_pk_bf16_f32 v204, v204, v205\n"
"v_cvt_pk_bf16_f32 v205, v206, v207\n"
"v_mfma_f32_16x16x16_bf16 v[98:101], v[98:99], v[92:93], 0\n"
"s_mov_b64 exec, s[44:45]\n"
"v_add_u32 v179, 0xc0, v179\n"
"s_mov_b64 exec, -1\n"
"v_mfma_f32_16x16x16_bf16 v[106:109], v[106:107], v[92:93], 0\n"
"s_waitcnt lgkmcnt(1)\n"
"ds_read_b64 v[92:93], v181 offset:64\n"
"v_cvt_pk_bf16_f32 v252, v252, v253\n"
"v_mfma_f32_16x16x32_bf16 v[140:143], v[140:143], v[152:155], 0\n"
"s_waitcnt lgkmcnt(1)\n"
"v_cvt_pk_bf16_f32 v253, v254, v255\n"
"v_cvt_pk_bf16_f32 v254, v98, v99\n"
"v_mfma_f32_16x16x32_bf16 v[140:143], v[88:91], v[164:167], v[140:143]\n"
"s_add_u32 m0, s34, 1024\n"
"ds_read_b64 v[98:99], v180 offset:13568\n"
"buffer_load_dwordx4 v190, s[8:11], s0 offen lds\n"
"v_mfma_f32_16x16x32_bf16 v[208:211], a[16:19], v[36:39], v[208:211]\n"
"s_barrier\n"
"v_mfma_f32_16x16x32_bf16 v[208:211], a[28:31], v[40:43], v[208:211]\n"
"ds_read_b128 v[116:119], v180 offset:27008\n"
"ds_read_b128 v[124:127], v181 offset:27008\n"
"v_mfma_f32_16x16x32_bf16 v[208:211], a[40:43], v[44:47], v[208:211]\n"
"v_cvt_pk_bf16_f32 v255, v100, v101\n"
"v_cvt_pk_bf16_f32 v106, v106, v107\n"
"v_mfma_f32_16x16x32_bf16 v[208:211], a[52:55], v[48:51], v[208:211]\n"
"ds_read_b64 v[100:101], v181 offset:13568\n"
"buffer_store_dwordx4 v[252:255], v192, s[28:31], s37 offen sc1\n"
"v_mfma_f32_16x16x32_bf16 v[208:211], a[64:67], v[52:55], v[208:211]\n"
"s_waitcnt lgkmcnt(4)\n"
"v_cvt_pk_bf16_f32 v107, v108, v109\n"
"v_max_f32 v136, v136, v128\n"
"v_mfma_f32_16x16x32_bf16 v[132:135], v[132:135], v[152:155], 0\n"
"s_waitcnt lgkmcnt(3)\n"
"ds_read_b64 v[108:109], v181 offset:27072\n"
"v_max_f32 v137, v137, v129\n"
"v_mfma_f32_16x16x32_bf16 v[132:135], v[92:95], v[166:169], v[132:135]\n"
"s_waitcnt vmcnt(5)\n"
"ds_read_b64_tr_b16 v[128:129], v182 offset:27080\n"
"v_add3_u32 v200, v200, s50, -5\n"
"v_mfma_f32_16x16x32_bf16 v[112:115], v[112:115], v[152:155], 0\n"
"v_cmp_lt_u32 vcc, 64, v200\n"
"v_cvt_pk_bf16_f32 v206, v208, v209\n"
"v_mfma_f32_16x16x32_bf16 v[112:115], v[96:99], v[164:167], v[112:115]\n"
"v_cvt_pk_bf16_f32 v207, v210, v211\n"
"v_max_f32 v138, v138, v130\n"
"v_mfma_f32_16x16x32_bf16 v[212:215], a[8:11], v[32:35], 0\n"
"s_waitcnt lgkmcnt(2)\n"
"ds_write_b128 v186, v[204:207] offset:6752\n"
"v_mfma_f32_16x16x32_bf16 v[120:123], v[120:123], v[152:155], 0\n"
"s_cbranch_vccnz .Lp4%=\n"
".Lr4%=:\n"
"v_mfma_f32_16x16x32_bf16 v[120:123], v[100:103], v[166:169], v[120:123]\n"
"s_waitcnt lgkmcnt(2)\n"
"buffer_load_dwordx4 v[56:59], v192, s[28:31], s38 offen sc1\n"
"v_max_f32 v139, v139, v131\n"
"v_mfma_f32_16x16x32_bf16 v[124:127], v[124:127], v[152:155], 0\n"
"ds_read_b64_tr_b16 v[130:131], v183 offset:27080\n"
"v_max3_f32 v170, v136, v137, v138\n"
"v_mfma_f32_16x16x32_bf16 v[124:127], v[108:111], v[166:169], v[124:127]\n"
"v_max_f32 v140, v140, v132\n"
"v_max_f32 v141, v141, v133\n"
"v_mfma_f32_16x16x32_bf16 v[212:215], a[20:23], v[36:39], v[212:215]\n"
"s_add_u32 m0, s34, 4096\n"
"ds_read_b64_tr_b16 v[132:133], v182 offset:27848\n"
"v_max_f32 v142, v142, v134\n"
"v_mfma_f32_16x16x32_bf16 v[212:215], a[32:35], v[40:43], v[212:215]\n"
"buffer_load_dwordx4 v191, s[8:11], s0 offen lds\n"
"v_max_f32 v143, v143, v135\n"
"v_mfma_f32_16x16x32_bf16 v[212:215], a[44:47], v[44:47], v[212:215]\n"
"s_add_u32 s0, s0, s33\n"
"ds_read_b64_tr_b16 v[134:135], v183 offset:27848\n"
"v_max3_f32 v170, v170, v139, v140\n"
"v_mfma_f32_16x16x32_bf16 v[212:215], a[56:59], v[48:51], v[212:215]\n"
"v_max3_f32 v170, v170, v141, v142\n"
"v_max_f32 v112, v112, v120\n"
"v_mfma_f32_16x16x32_bf16 v[212:215], a[68:71], v[52:55], v[212:215]\n"
"v_max_f32 v113, v113, v121\n"
"v_max_f32 v114, v114, v122\n"
"v_mfma_f32_16x16x32_bf16 v[208:211], a[80:83], v[32:35], 0\n"
"buffer_load_dwordx2 v[60:61], v193, s[28:31], s38 offen offset:1024 sc1\n"
"ds_read_b64_tr_b16 v[120:121], v182 offset:54088\n"
"v_mfma_f32_16x16x32_bf16 v[208:211], a[92:95], v[36:39], v[208:211]\n"
"s_add_u32 s38, s38, 1572864\n"
"v_max_f32 v115, v115, v123\n"
"v_cvt_pk_bf16_f32 v212, v212, v213\n"
"v_mfma_f32_16x16x32_bf16 v[208:211], a[104:107], v[40:43], v[208:211]\n"
"ds_read_b64_tr_b16 v[122:123], v183 offset:54088\n"
"v_cvt_pk_bf16_f32 v213, v214, v215\n"
"v_mfma_f32_16x16x32_bf16 v[208:211], a[116:119], v[44:47], v[208:211]\n"
"v_max3_f32 v170, v170, v143, v112\n"
"v_max3_f32 v170, v170, v113, v114\n"
"v_mfma_f32_16x16x32_bf16 v[208:211], a[128:131], v[48:51], v[208:211]\n"
"s_add_u32 m0, s34, 2048\n"
"buffer_store_dwordx2 v[106:107], v193, s[28:31], s37 offen offset:1024 sc1\n"
"v_mfma_f32_16x16x32_bf16 v[208:211], a[140:143], v[52:55], v[208:211]\n"
"s_add_u32 s37, s37, 1572864\n"
"ds_read_b64 v[106:107], v180 offset:27072\n"
"v_mov_b64 v[252:253], 0\n"
"v_mfma_f32_16x16x32_bf16 v[204:207], a[72:75], v[32:35], 0\n"
"s_add_u32 s4, s37, 6291456\n"
"v_mov_b64 v[254:255], 0\n"
"v_mov_b64 v[98:99], 0\n"
"v_mfma_f32_16x16x32_bf16 v[204:207], a[84:87], v[36:39], v[204:207]\n"
"v_cvt_pk_bf16_f32 v214, v208, v209\n"
"v_cvt_pk_bf16_f32 v215, v210, v211\n"
"v_mfma_f32_16x16x32_bf16 v[204:207], a[96:99], v[40:43], v[204:207]\n"
"ds_write_b128 v186, v[212:215] offset:6816\n"
"v_mfma_f32_16x16x32_bf16 v[204:207], a[108:111], v[44:47], v[204:207]\n"
"v_mov_b64 v[100:101], 0\n"
"v_mov_b64 v[108:109], 0\n"
"v_mfma_f32_16x16x32_bf16 v[204:207], a[120:123], v[48:51], v[204:207]\n"
"buffer_load_dwordx4 v190, s[8:11], s1 offen lds\n"
"v_mfma_f32_16x16x32_bf16 v[204:207], a[132:135], v[52:55], v[204:207]\n"
"s_waitcnt lgkmcnt(1)\n"
"v_mov_b64 v[68:69], -1\n"
"v_mfma_f32_16x16x32_bf16 v[116:119], v[116:119], v[152:155], 0\n"
"v_mfma_f32_16x16x32_bf16 v[116:119], v[104:107], v[164:167], v[116:119]\n"
"v_mov_b64 v[106:107], 0\n"
"v_mfma_f32_16x16x32_bf16 v[208:211], a[76:79], v[32:35], 0\n"
"buffer_store_dwordx2 v[68:69], v193, s[28:31], s4 offen offset:1024 sc1\n"
"v_mfma_f32_16x16x32_bf16 v[208:211], a[88:91], v[36:39], v[208:211]\n"
"s_add_u32 s4, s37, 7864320\n"
"ds_read_b128 v[68:71], v202 offset:30720\n"
"v_cvt_pk_bf16_f32 v204, v204, v205\n"
"v_mfma_f32_16x16x32_bf16 v[208:211], a[100:103], v[40:43], v[208:211]\n"
"v_max_f32 v116, v116, v124\n"
"v_max_f32 v117, v117, v125\n"
"v_mfma_f32_16x16x32_bf16 v[208:211], a[112:115], v[44:47], v[208:211]\n"
"ds_read_b64_tr_b16 v[124:125], v182 offset:54856\n"
"v_max_f32 v118, v118, v126\n"
"v_mfma_f32_16x16x32_bf16 v[208:211], a[124:127], v[48:51], v[208:211]\n"
"v_max_f32 v119, v119, v127\n"
"v_max3_f32 v170, v170, v115, v116\n"
"v_mfma_f32_16x16x32_bf16 v[208:211], a[136:139], v[52:55], v[208:211]\n"
"s_add_u32 m0, s34, 5120\n"
"ds_read_b64_tr_b16 v[126:127], v183 offset:54856\n"
"v_max3_f32 v170, v170, v117, v118\n"
"v_mfma_f32_16x16x32_bf16 v[212:215], a[144:147], v[32:35], 0\n"
"v_max_f32 v171, v170, v119\n"
"v_max_f32 v170, v170, v119\n"
"v_mfma_f32_16x16x32_bf16 v[212:215], a[156:159], v[36:39], v[212:215]\n"
"buffer_load_dwordx4 v191, s[8:11], s1 offen lds\n"
"v_cvt_pk_bf16_f32 v205, v206, v207\n"
"v_mfma_f32_16x16x32_bf16 v[212:215], a[168:171], v[40:43], v[212:215]\n"
"s_waitcnt lgkmcnt(2)\n"
"v_permlane16_swap_b32 v170, v171\n"
"v_mfma_f32_16x16x32_bf16 v[212:215], a[180:183], v[44:47], v[212:215]\n"
"s_add_u32 s1, s1, s33\n"
"v_max_f32 v170, v170, v171\n"
"v_mov_b32 v171, v170\n"
"v_mfma_f32_16x16x32_bf16 v[212:215], a[192:195], v[48:51], v[212:215]\n"
"s_nop 0\n"
"v_permlane32_swap_b32 v170, v171\n"
"v_max_f32 v170, v170, v171\n"
"v_mul_f32 v170, 0xbe553b95, v170\n"
"v_pk_fma_f32 v[136:137], v[136:137], s[70:71], v[170:171] op_sel_hi:[1,0,0]\n"
"v_pk_fma_f32 v[138:139], v[138:139], s[70:71], v[170:171] op_sel_hi:[1,0,0]\n"
"v_pk_fma_f32 v[140:141], v[140:141], s[70:71], v[170:171] op_sel_hi:[1,0,0]\n"
"v_pk_fma_f32 v[142:143], v[142:143], s[70:71], v[170:171] op_sel_hi:[1,0,0]\n"
"v_pk_fma_f32 v[112:113], v[112:113], s[70:71], v[170:171] op_sel_hi:[1,0,0]\n"
"v_pk_fma_f32 v[114:115], v[114:115], s[70:71], v[170:171] op_sel_hi:[1,0,0]\n"
"v_pk_fma_f32 v[116:117], v[116:117], s[70:71], v[170:171] op_sel_hi:[1,0,0]\n"
"v_pk_fma_f32 v[118:119], v[118:119], s[70:71], v[170:171] op_sel_hi:[1,0,0]\n"
"v_mfma_f32_16x16x32_bf16 v[212:215], a[204:207], v[52:55], v[212:215]\n"
"buffer_store_dwordx4 v[68:71], v194, s[24:27], s36 offen sc1\n"
"s_nop 1\n"
"ds_read_b128 v[68:71], v192 offset:36864\n"
"v_mov_b64 v[64:65], -1\n"
"v_mov_b64 v[66:67], -1\n"
"v_exp_f32 v136, v136\n"
"v_exp_f32 v137, v137\n"
"v_exp_f32 v138, v138\n"
"v_exp_f32 v139, v139\n"
"v_cvt_pk_bf16_f32 v82, v136, v138\n"
"v_cvt_pk_bf16_f32 v206, v208, v209\n"
"v_cvt_pk_bf16_f32 v83, v137, v139\n"
"v_exp_f32 v140, v140\n"
"v_cvt_pk_bf16_f32 v207, v210, v211\n"
"v_mfma_f32_16x16x32_bf16 v[208:211], a[148:151], v[32:35], 0\n"
"ds_write_b128 v186, v[204:207] offset:6880\n"
"v_mfma_f32_16x16x32_bf16 v[208:211], a[160:163], v[36:39], v[208:211]\n"
"s_waitcnt lgkmcnt(1)\n"
"v_exp_f32 v141, v141\n"
"v_mfma_f32_16x16x32_bf16 v[208:211], a[172:175], v[40:43], v[208:211]\n"
"buffer_store_dwordx4 v[64:67], v192, s[28:31], s4 offen sc1\n"
"v_mfma_f32_16x16x32_bf16 v[232:235], a[216:219], v[68:71], 0\n"
"s_nop 0\n"
"ds_read_b64 v[64:65], v62 offset:30720\n"
"ds_read_b128 v[136:139], v180\n"
"v_mfma_f32_16x16x32_bf16 v[236:239], a[220:223], v[68:71], 0\n"
"v_exp_f32 v142, v142\n"
"v_mfma_f32_16x16x32_bf16 v[240:243], a[224:227], v[68:71], 0\n"
"ds_read_b128 v[68:71], v192 offset:38912\n"
"v_cvt_pk_bf16_f32 v84, v140, v142\n"
"v_mfma_f32_16x16x32_bf16 v[208:211], a[184:187], v[44:47], v[208:211]\n"
"v_exp_f32 v143, v143\n"
"v_mfma_f32_16x16x32_bf16 v[208:211], a[196:199], v[48:51], v[208:211]\n"
"v_cvt_pk_bf16_f32 v85, v141, v143\n"
"v_cvt_pk_bf16_f32 v152, v212, v213\n"
"v_mfma_f32_16x16x32_bf16 v[208:211], a[208:211], v[52:55], v[208:211]\n"
"ds_read_b128 v[140:143], v180 offset:13504\n"
"v_cvt_pk_bf16_f32 v153, v214, v215\n"
"v_smfmac_f32_16x16x64_bf16 v[252:255], v[82:85], v[216:223], v63\n"
"s_waitcnt lgkmcnt(3)\n"
"ds_read_b64_tr_b16 v[216:217], v182 offset:54144\n"
"v_smfmac_f32_16x16x64_bf16 v[98:101], v[82:85], v[72:79], v63\n"
"buffer_store_dwordx2 v[64:65], v195, s[24:27], s36 offen sc1\n"
"v_smfmac_f32_16x16x64_bf16 v[106:109], v[82:85], v[128:135], v63\n"
"s_add_u32 s36, s36, s33\n"
"ds_read_b128 v[64:67], v192 offset:37888\n"
"v_mfma_f32_16x16x32_bf16 v[82:85], v[172:175], v[82:85], 0\n"
"ds_read_b64_tr_b16 v[218:219], v183 offset:54144\n"
"ds_read_b64_tr_b16 v[220:221], v182 offset:54912\n"
"v_mfma_f32_16x16x32_bf16 v[204:207], a[152:155], v[32:35], 0\n"
"v_exp_f32 v112, v112\n"
"v_mfma_f32_16x16x32_bf16 v[204:207], a[164:167], v[36:39], v[204:207]\n"
"ds_read_b128 v[32:35], v188 offset:6144\n"
"ds_read_b128 v[36:39], v189 offset:6144\n"
"v_mfma_f32_16x16x32_bf16 v[204:207], a[176:179], v[40:43], v[204:207]\n"
"v_exp_f32 v113, v113\n"
"v_mfma_f32_16x16x32_bf16 v[204:207], a[188:191], v[44:47], v[204:207]\n"
"ds_read_b128 v[40:43], v188 offset:7168\n"
"ds_read_b128 v[44:47], v189 offset:7168\n"
"v_mfma_f32_16x16x32_bf16 v[204:207], a[200:203], v[48:51], v[204:207]\n"
"v_exp_f32 v114, v114\n"
"v_mfma_f32_16x16x32_bf16 v[204:207], a[212:215], v[52:55], v[204:207]\n"
"s_waitcnt lgkmcnt(6)\n"
"ds_read_b128 v[48:51], v188 offset:8192\n"
"ds_read_b128 v[52:55], v189 offset:8192\n"
"v_mfma_f32_16x16x32_bf16 v[232:235], a[228:231], v[64:67], v[232:235]\n"
"v_exp_f32 v115, v115\n"
"v_mfma_f32_16x16x32_bf16 v[232:235], a[240:243], v[68:71], v[232:235]\n"
"ds_read_b64_tr_b16 v[222:223], v183 offset:54912\n"
"ds_read_b64_tr_b16 v[72:73], v182 offset:54152\n"
"v_mfma_f32_16x16x32_bf16 v[236:239], a[232:235], v[64:67], v[236:239]\n"
"s_waitcnt lgkmcnt(7)\n"
"v_cvt_pk_bf16_f32 v90, v112, v114\n"
"v_cvt_pk_bf16_f32 v91, v113, v115\n"
"v_mfma_f32_16x16x32_bf16 v[236:239], a[244:247], v[68:71], v[236:239]\n"
"s_waitcnt lgkmcnt(6)\n"
"ds_read_b64_tr_b16 v[74:75], v183 offset:54152\n"
"ds_read_b64_tr_b16 v[76:77], v182 offset:54920\n"
"v_mfma_f32_16x16x32_bf16 v[240:243], a[236:239], v[64:67], v[240:243]\n"
"s_add_u32 m0, s34, 6144\n"
"v_exp_f32 v116, v116\n"
"v_mfma_f32_16x16x32_bf16 v[240:243], a[248:251], v[68:71], v[240:243]\n"
"buffer_load_dwordx4 v190, s[8:11], s32 offen lds\n"
"ds_read_b128 v[64:67], v192 offset:39936\n"
"v_mfma_f32_16x16x32_bf16 v[212:215], a[0:3], v[32:35], 0\n"
"ds_read_b128 v[68:71], v192 offset:40960\n"
"ds_read_b64_tr_b16 v[78:79], v183 offset:54920\n"
"v_mfma_f32_16x16x32_bf16 v[212:215], a[12:15], v[36:39], v[212:215]\n"
"s_waitcnt lgkmcnt(10)\n"
"v_exp_f32 v117, v117\n"
"v_mfma_f32_16x16x32_bf16 v[212:215], a[24:27], v[40:43], v[212:215]\n"
"s_waitcnt lgkmcnt(9)\n"
"ds_read_b128 v[128:131], v181\n"
"ds_read_b128 v[132:135], v181 offset:13504\n"
"v_mfma_f32_16x16x32_bf16 v[212:215], a[36:39], v[44:47], v[212:215]\n"
"s_waitcnt lgkmcnt(10)\n"
"v_exp_f32 v118, v118\n"
"v_mfma_f32_16x16x32_bf16 v[212:215], a[48:51], v[48:51], v[212:215]\n"
"s_waitcnt lgkmcnt(9)\n"
"ds_read_b128 v[112:115], v180 offset:27008\n"
"v_cvt_pk_bf16_f32 v92, v116, v118\n"
"v_mfma_f32_16x16x32_bf16 v[212:215], a[60:63], v[52:55], v[212:215]\n"
"v_exp_f32 v119, v119\n"
"v_cvt_pk_bf16_f32 v154, v208, v209\n"
"v_cvt_pk_bf16_f32 v93, v117, v119\n"
"v_cvt_pk_bf16_f32 v155, v210, v211\n"
"v_mfma_f32_16x16x32_bf16 v[208:211], a[4:7], v[32:35], 0\n"
"s_waitcnt lgkmcnt(5)\n"
"v_cvt_pk_bf16_f32 v166, v204, v205\n"
"v_cvt_pk_bf16_f32 v167, v206, v207\n"
"v_mfma_f32_16x16x32_bf16 v[208:211], a[16:19], v[36:39], v[208:211]\n"
"s_waitcnt lgkmcnt(3)\n"
"v_cvt_pk_bf16_f32 v212, v212, v213\n"
"v_cvt_pk_bf16_f32 v213, v214, v215\n"
"v_mfma_f32_16x16x32_bf16 v[232:235], a[252:255], v[64:67], v[232:235]\n"
"s_add_u32 m0, s34, 9216\n"
"s_nop 0\n"
"buffer_load_dwordx4 v191, s[8:11], s32 offen lds\n"
"v_mfma_f32_16x16x32_bf16 v[232:235], v[8:11], v[68:71], v[232:235]\n"
"s_add_u32 s32, s32, s33\n"
"v_mfma_f32_16x16x32_bf16 v[236:239], v[0:3], v[64:67], v[236:239]\n"
"v_mfma_f32_16x16x32_bf16 v[236:239], v[12:15], v[68:71], v[236:239]\n"
"v_mfma_f32_16x16x32_bf16 v[240:243], v[4:7], v[64:67], v[240:243]\n"
"ds_read_b128 v[64:67], v192 offset:41984\n"
"v_mfma_f32_16x16x32_bf16 v[240:243], v[16:19], v[68:71], v[240:243]\n"
"v_smfmac_f32_16x16x64_bf16 v[252:255], v[90:93], v[216:223], v63\n"
"v_smfmac_f32_16x16x64_bf16 v[98:101], v[90:93], v[72:79], v63\n"
"s_add_u32 m0, s34, 7168\n"
"v_smfmac_f32_16x16x64_bf16 v[106:109], v[90:93], v[120:127], v63\n"
"s_waitcnt vmcnt(11)\n"
"buffer_load_dwordx4 v190, s[8:11], s0 offen lds\n"
"v_mfma_f32_16x16x32_bf16 v[90:93], v[172:175], v[90:93], v[82:85]\n"
"ds_write_b128 v202, v[56:59] offset:43008\n"
"v_mfma_f32_16x16x32_bf16 v[208:211], a[28:31], v[40:43], v[208:211]\n"
"s_waitcnt lgkmcnt(1)\n"
"v_cvt_pk_bf16_f32 v252, v252, v253\n"
"v_cvt_pk_bf16_f32 v253, v254, v255\n"
"v_mfma_f32_16x16x32_bf16 v[208:211], a[40:43], v[44:47], v[208:211]\n"
"v_cvt_pk_bf16_f32 v98, v98, v99\n"
"v_cvt_pk_bf16_f32 v99, v100, v101\n"
"v_mfma_f32_16x16x32_bf16 v[208:211], a[52:55], v[48:51], v[208:211]\n"
"buffer_load_dwordx4 v[56:59], v192, s[28:31], s38 offen sc1\n"
"ds_read_b64 v[82:83], v180 offset:64\n"
"v_mfma_f32_16x16x32_bf16 v[208:211], a[64:67], v[52:55], v[208:211]\n"
"ds_read_b64 v[84:85], v181 offset:64\n"
"ds_read_b128 v[120:123], v181 offset:27008\n"
"v_mfma_f32_16x16x32_bf16 v[232:235], v[20:23], v[64:67], v[232:235]\n"
"v_rcp_f32 v171, v90\n"
"v_mfma_f32_16x16x32_bf16 v[240:243], v[28:31], v[64:67], v[240:243]\n"
"s_add_u32 m0, s34, 10240\n"
"ds_read_b64 v[90:91], v180 offset:13568\n"
"v_cvt_pk_bf16_f32 v171, v171, v171\n"
"v_mfma_f32_16x16x32_bf16 v[236:239], v[24:27], v[64:67], v[236:239]\n"
"buffer_load_dwordx4 v191, s[8:11], s0 offen lds\n"
"v_and_b32 v92, v171, v176\n"
"v_mfma_f32_16x16x32_bf16 v[204:207], a[8:11], v[32:35], 0\n"
"s_add_u32 s0, s0, s33\n"
"v_cvt_pk_bf16_f32 v232, v232, v233\n"
"v_cvt_pk_bf16_f32 v233, v234, v235\n"
"v_mfma_f32_16x16x32_bf16 v[204:207], a[20:23], v[36:39], v[204:207]\n"
"v_cvt_pk_bf16_f32 v240, v240, v241\n"
"v_cvt_pk_bf16_f32 v234, v236, v237\n"
"v_mfma_f32_16x16x32_bf16 v[204:207], a[32:35], v[40:43], v[204:207]\n"
"s_waitcnt lgkmcnt(3)\n"
"v_cvt_pk_bf16_f32 v235, v238, v239\n"
"v_cvt_pk_bf16_f32 v241, v242, v243\n"
"v_mfma_f32_16x16x32_bf16 v[136:139], v[136:139], v[144:147], 0\n"
"s_waitcnt lgkmcnt(2)\n"
"ds_write_b128 v196, v[232:235] offset:55296\n"
"v_mfma_f32_16x16x32_bf16 v[136:139], v[80:83], v[156:159], v[136:139]\n"
"s_waitcnt lgkmcnt(1)\n"
"v_and_b32 v93, v171, v177\n"
"v_cvt_pk_bf16_f32 v106, v106, v107\n"
"v_mfma_f32_16x16x32_bf16 v[128:131], v[128:131], v[144:147], 0\n"
"v_cvt_pk_bf16_f32 v107, v108, v109\n"
"v_cvt_pk_bf16_f32 v214, v208, v209\n"
"v_mfma_f32_16x16x32_bf16 v[128:131], v[84:87], v[158:161], v[128:131]\n"
"s_add_u32 m0, s34, 8192\n"
"ds_write_b64 v197, v[240:241] offset:55296\n"
"v_mfma_f32_16x16x32_bf16 v[140:143], v[140:143], v[144:147], 0\n"
"s_waitcnt vmcnt(11)\n"
"buffer_load_dwordx4 v190, s[8:11], s1 offen lds\n"
"v_or_b32 v201, 8, s6\n"
"v_mfma_f32_16x16x32_bf16 v[140:143], v[88:91], v[156:159], v[140:143]\n"
"ds_write_b64 v203, v[60:61] offset:43008\n"
"v_mfma_f32_16x16x16_bf16 v[252:255], v[252:253], v[92:93], 0\n"
"v_cvt_pk_bf16_f32 v215, v210, v211\n"
"v_max_f32 v136, v136, v128\n"
"v_mfma_f32_16x16x16_bf16 v[98:101], v[98:99], v[92:93], 0\n"
"v_max_f32 v137, v137, v129\n"
"v_max_f32 v138, v138, v130\n"
"v_mfma_f32_16x16x16_bf16 v[106:109], v[106:107], v[92:93], 0\n"
"s_mov_b64 exec, 1\n"
"buffer_store_dword v201, off, s[28:31], s40 sc1\n"
"s_mov_b64 exec, -1\n"
"v_mfma_f32_16x16x32_bf16 v[204:207], a[44:47], v[44:47], v[204:207]\n"
"ds_read_b64 v[92:93], v181 offset:13568\n"
"ds_read_b64_tr_b16 v[128:129], v182 offset:40584\n"
"v_mfma_f32_16x16x32_bf16 v[204:207], a[56:59], v[48:51], v[204:207]\n"
"buffer_load_dwordx2 v[60:61], v193, s[28:31], s38 offen offset:1024 sc1\n"
"v_cvt_pk_bf16_f32 v252, v252, v253\n"
"v_mfma_f32_16x16x32_bf16 v[204:207], a[68:71], v[52:55], v[204:207]\n"
"s_waitcnt lgkmcnt(2)\n"
"v_cvt_pk_bf16_f32 v253, v254, v255\n"
"v_cvt_pk_bf16_f32 v254, v98, v99\n"
"v_mfma_f32_16x16x32_bf16 v[208:211], a[80:83], v[32:35], 0\n"
"s_barrier\n"
"v_mfma_f32_16x16x32_bf16 v[208:211], a[92:95], v[36:39], v[208:211]\n"
"s_add_u32 m0, s34, 11264\n"
"ds_read_b64 v[98:99], v180 offset:27072\n"
"ds_read_b128 v[116:119], v178\n"
"v_mfma_f32_16x16x32_bf16 v[208:211], a[104:107], v[40:43], v[208:211]\n"
"s_add_u32 s38, s38, 1572864\n"
"buffer_load_dwordx4 v191, s[8:11], s1 offen lds\n"
"ds_read_b128 v[124:127], v179\n"
"v_mfma_f32_16x16x32_bf16 v[208:211], a[116:119], v[44:47], v[208:211]\n"
"s_add_u32 s1, s1, s33\n"
"ds_write_b128 v186, v[212:215] offset:20256\n"
"v_mfma_f32_16x16x32_bf16 v[208:211], a[128:131], v[48:51], v[208:211]\n"
"s_waitcnt lgkmcnt(5)\n"
"v_cvt_pk_bf16_f32 v255, v100, v101\n"
"v_cvt_pk_bf16_f32 v106, v106, v107\n"
"v_mfma_f32_16x16x32_bf16 v[132:135], v[132:135], v[144:147], 0\n"
"v_cvt_pk_bf16_f32 v107, v108, v109\n"
"v_cvt_pk_bf16_f32 v204, v204, v205\n"
"v_mfma_f32_16x16x32_bf16 v[132:135], v[92:95], v[158:161], v[132:135]\n"
"buffer_store_dwordx4 v[252:255], v192, s[28:31], s37 offen sc1\n"
"v_mfma_f32_16x16x32_bf16 v[208:211], a[140:143], v[52:55], v[208:211]\n"
"s_waitcnt lgkmcnt(3)\n"
"ds_read_b64 v[100:101], v181 offset:27072\n"
"ds_read_b64 v[108:109], v179 offset:64\n"
"v_mfma_f32_16x16x32_bf16 v[112:115], v[112:115], v[144:147], 0\n"
"ds_read_b128 v[64:67], v202 offset:55296\n"
"v_cvt_pk_bf16_f32 v205, v206, v207\n"
"v_mfma_f32_16x16x32_bf16 v[112:115], v[96:99], v[156:159], v[112:115]\n"
"v_max_f32 v139, v139, v131\n"
"v_max3_f32 v170, v136, v137, v138\n"
"v_mfma_f32_16x16x32_bf16 v[212:215], a[72:75], v[32:35], 0\n"
"ds_read_b64_tr_b16 v[130:131], v183 offset:40584\n"
"v_max_f32 v140, v140, v132\n"
"v_mfma_f32_16x16x32_bf16 v[212:215], a[84:87], v[36:39], v[212:215]\n"
"v_max_f32 v141, v141, v133\n"
"v_max_f32 v142, v142, v134\n"
"v_mfma_f32_16x16x32_bf16 v[212:215], a[96:99], v[40:43], v[212:215]\n"
"ds_read_b64_tr_b16 v[132:133], v182 offset:41352\n"
"v_max_f32 v143, v143, v135\n"
"v_mfma_f32_16x16x32_bf16 v[212:215], a[108:111], v[44:47], v[212:215]\n"
"ds_read_b64_tr_b16 v[134:135], v183 offset:41352\n"
"v_max3_f32 v170, v170, v139, v140\n"
"v_mfma_f32_16x16x32_bf16 v[212:215], a[120:123], v[48:51], v[212:215]\n"
"s_waitcnt lgkmcnt(5)\n"
"buffer_store_dwordx2 v[106:107], v193, s[28:31], s37 offen offset:1024 sc1\n"
"v_mfma_f32_16x16x32_bf16 v[120:123], v[120:123], v[144:147], 0\n"
"s_waitcnt lgkmcnt(4)\n"
"ds_read_b64 v[106:107], v178 offset:64\n"
"v_max3_f32 v170, v170, v141, v142\n"
"v_mfma_f32_16x16x32_bf16 v[120:123], v[100:103], v[158:161], v[120:123]\n"
"s_waitcnt lgkmcnt(4)\n"
"v_cvt_pk_bf16_f32 v206, v208, v209\n"
"v_cvt_pk_bf16_f32 v207, v210, v211\n"
"v_mfma_f32_16x16x32_bf16 v[124:127], v[124:127], v[144:147], 0\n"
"s_add_u32 s37, s37, 1572864\n"
"ds_write_b128 v186, v[204:207] offset:20320\n"
"v_mfma_f32_16x16x32_bf16 v[124:127], v[108:111], v[158:161], v[124:127]\n"
"s_add_u32 s4, s37, 6291456\n"
"v_max_f32 v112, v112, v120\n"
"v_max_f32 v113, v113, v121\n"
"v_mfma_f32_16x16x32_bf16 v[212:215], a[132:135], v[52:55], v[212:215]\n"
"v_max_f32 v114, v114, v122\n"
"v_max_f32 v115, v115, v123\n"
"v_mfma_f32_16x16x32_bf16 v[208:211], a[76:79], v[32:35], 0\n"
"buffer_store_dwordx4 v[64:67], v194, s[24:27], s36 offen sc1\n"
"v_mfma_f32_16x16x32_bf16 v[208:211], a[88:91], v[36:39], v[208:211]\n"
"s_waitcnt lgkmcnt(1)\n"
"ds_read_b128 v[64:67], v192 offset:43008\n"
"ds_read_b64_tr_b16 v[120:121], v184 offset:72\n"
"v_mfma_f32_16x16x32_bf16 v[116:119], v[116:119], v[144:147], 0\n"
"ds_read_b64_tr_b16 v[122:123], v185 offset:72\n"
"v_max3_f32 v170, v170, v143, v112\n"
"v_mfma_f32_16x16x32_bf16 v[116:119], v[104:107], v[156:159], v[116:119]\n"
"v_max3_f32 v170, v170, v113, v114\n"
"v_mov_b64 v[68:69], -1\n"
"v_mfma_f32_16x16x32_bf16 v[208:211], a[100:103], v[40:43], v[208:211]\n"
"v_mov_b64 v[252:253], 0\n"
"v_mov_b64 v[254:255], 0\n"
"v_mfma_f32_16x16x32_bf16 v[208:211], a[112:115], v[44:47], v[208:211]\n"
"v_mov_b64 v[98:99], 0\n"
"v_mov_b64 v[100:101], 0\n"
"v_mfma_f32_16x16x32_bf16 v[208:211], a[124:127], v[48:51], v[208:211]\n"
"v_max_f32 v116, v116, v124\n"
"v_max_f32 v117, v117, v125\n"
"v_mfma_f32_16x16x32_bf16 v[208:211], a[136:139], v[52:55], v[208:211]\n"
"ds_read_b64_tr_b16 v[124:125], v184 offset:840\n"
"v_max_f32 v118, v118, v126\n"
"v_mfma_f32_16x16x32_bf16 v[204:207], a[144:147], v[32:35], 0\n"
"s_waitcnt lgkmcnt(3)\n"
"buffer_store_dwordx2 v[68:69], v193, s[28:31], s4 offen offset:1024 sc1\n"
"v_mfma_f32_16x16x32_bf16 v[204:207], a[156:159], v[36:39], v[204:207]\n"
"ds_read_b64 v[68:69], v62 offset:55296\n"
"v_max_f32 v119, v119, v127\n"
"v_mfma_f32_16x16x32_bf16 v[204:207], a[168:171], v[40:43], v[204:207]\n"
"ds_read_b64_tr_b16 v[126:127], v185 offset:840\n"
"v_max3_f32 v170, v170, v115, v116\n"
"v_mfma_f32_16x16x32_bf16 v[204:207], a[180:183], v[44:47], v[204:207]\n"
"v_max3_f32 v170, v170, v117, v118\n"
"v_mov_b64 v[106:107], 0\n"
"v_mfma_f32_16x16x32_bf16 v[232:235], a[216:219], v[64:67], 0\n"
"v_mov_b64 v[108:109], 0\n"
"v_max_f32 v171, v170, v119\n"
"v_mfma_f32_16x16x32_bf16 v[236:239], a[220:223], v[64:67], 0\n"
"v_max_f32 v170, v170, v119\n"
"v_cvt_pk_bf16_f32 v212, v212, v213\n"
"v_mfma_f32_16x16x32_bf16 v[240:243], a[224:227], v[64:67], 0\n"
"ds_read_b128 v[64:67], v192 offset:45056\n"
"v_cvt_pk_bf16_f32 v213, v214, v215\n"
"v_mfma_f32_16x16x32_bf16 v[204:207], a[192:195], v[48:51], v[204:207]\n"
"s_waitcnt lgkmcnt(2)\n"
"v_permlane16_swap_b32 v170, v171\n"
"v_mfma_f32_16x16x32_bf16 v[204:207], a[204:207], v[52:55], v[204:207]\n"
"buffer_store_dwordx2 v[68:69], v195, s[24:27], s36 offen sc1\n"
"ds_read_b128 v[68:71], v192 offset:44032\n"
"s_add_u32 s36, s36, s33\n"
"v_max_f32 v170, v170, v171\n"
"v_mov_b32 v171, v170\n"
"v_cvt_pk_bf16_f32 v214, v208, v209\n"
"v_cvt_pk_bf16_f32 v215, v210, v211\n"
"v_mfma_f32_16x16x32_bf16 v[208:211], a[148:151], v[32:35], 0\n"
"ds_write_b128 v186, v[212:215] offset:20384\n"
"v_mfma_f32_16x16x32_bf16 v[208:211], a[160:163], v[36:39], v[208:211]\n"
"v_permlane32_swap_b32 v170, v171\n"
"v_max_f32 v170, v170, v171\n"
"v_mul_f32 v170, 0xbe553b95, v170\n"
"v_pk_fma_f32 v[136:137], v[136:137], s[70:71], v[170:171] op_sel_hi:[1,0,0]\n"
"v_pk_fma_f32 v[138:139], v[138:139], s[70:71], v[170:171] op_sel_hi:[1,0,0]\n"
"v_pk_fma_f32 v[140:141], v[140:141], s[70:71], v[170:171] op_sel_hi:[1,0,0]\n"
"v_pk_fma_f32 v[142:143], v[142:143], s[70:71], v[170:171] op_sel_hi:[1,0,0]\n"
"v_pk_fma_f32 v[112:113], v[112:113], s[70:71], v[170:171] op_sel_hi:[1,0,0]\n"
"v_pk_fma_f32 v[114:115], v[114:115], s[70:71], v[170:171] op_sel_hi:[1,0,0]\n"
"v_pk_fma_f32 v[116:117], v[116:117], s[70:71], v[170:171] op_sel_hi:[1,0,0]\n"
"v_pk_fma_f32 v[118:119], v[118:119], s[70:71], v[170:171] op_sel_hi:[1,0,0]\n"
"v_mfma_f32_16x16x32_bf16 v[208:211], a[172:175], v[40:43], v[208:211]\n"
"s_waitcnt lgkmcnt(1)\n"
"v_exp_f32 v136, v136\n"
"v_mfma_f32_16x16x32_bf16 v[208:211], a[184:187], v[44:47], v[208:211]\n"
"v_exp_f32 v137, v137\n"
"v_mfma_f32_16x16x32_bf16 v[208:211], a[196:199], v[48:51], v[208:211]\n"
"v_exp_f32 v138, v138\n"
"v_mfma_f32_16x16x32_bf16 v[208:211], a[208:211], v[52:55], v[208:211]\n"
"v_exp_f32 v139, v139\n"
"v_mfma_f32_16x16x32_bf16 v[232:235], a[228:231], v[68:71], v[232:235]\n"
"v_cvt_pk_bf16_f32 v82, v136, v138\n"
"v_cvt_pk_bf16_f32 v83, v137, v139\n"
"v_mfma_f32_16x16x32_bf16 v[232:235], a[240:243], v[64:67], v[232:235]\n"
"ds_read_b128 v[136:139], v180 offset:13504\n"
"v_exp_f32 v140, v140\n"
"v_mfma_f32_16x16x32_bf16 v[236:239], a[232:235], v[68:71], v[236:239]\n"
"v_exp_f32 v141, v141\n"
"v_mfma_f32_16x16x32_bf16 v[236:239], a[244:247], v[64:67], v[236:239]\n"
"v_exp_f32 v142, v142\n"
"v_mfma_f32_16x16x32_bf16 v[240:243], a[236:239], v[68:71], v[240:243]\n"
"ds_read_b128 v[68:71], v192 offset:46080\n"
"v_cvt_pk_bf16_f32 v84, v140, v142\n"
"v_mfma_f32_16x16x32_bf16 v[240:243], a[248:251], v[64:67], v[240:243]\n"
"ds_read_b128 v[64:67], v192 offset:47104\n"
"v_exp_f32 v143, v143\n"
"v_mfma_f32_16x16x32_bf16 v[212:215], a[152:155], v[32:35], 0\n"
"ds_read_b128 v[32:35], v188\n"
"v_cvt_pk_bf16_f32 v85, v141, v143\n"
"v_mfma_f32_16x16x32_bf16 v[212:215], a[164:167], v[36:39], v[212:215]\n"
".Lic5%=:\n"
"s_cbranch_execz .Lib1%=\n"
"ds_read_b128 v[36:39], v189\n"
"ds_read_b128 v[140:143], v180 offset:27008\n"
"v_mfma_f32_16x16x32_bf16 v[212:215], a[176:179], v[40:43], v[212:215]\n"
"ds_read_b128 v[40:43], v188 offset:1024\n"
"v_cvt_pk_bf16_f32 v144, v204, v205\n"
"v_mfma_f32_16x16x32_bf16 v[212:215], a[188:191], v[44:47], v[212:215]\n"
"ds_read_b128 v[44:47], v189 offset:1024\n"
"v_cvt_pk_bf16_f32 v145, v206, v207\n"
"v_mfma_f32_16x16x32_bf16 v[212:215], a[200:203], v[48:51], v[212:215]\n"
"s_waitcnt vmcnt(17)\n"
"ds_read_b128 v[48:51], v188 offset:2048\n"
"v_cvt_pk_bf16_f32 v146, v208, v209\n"
"v_mfma_f32_16x16x32_bf16 v[212:215], a[212:215], v[52:55], v[212:215]\n"
"s_waitcnt lgkmcnt(7)\n"
"ds_read_b128 v[52:55], v189 offset:2048\n"
"v_exp_f32 v112, v112\n"
"v_smfmac_f32_16x16x64_bf16 v[252:255], v[82:85], v[224:231], v63\n"
"ds_read_b64_tr_b16 v[224:225], v184 offset:128\n"
"v_smfmac_f32_16x16x64_bf16 v[98:101], v[82:85], v[244:251], v63\n"
"s_waitcnt lgkmcnt(7)\n"
"ds_read_b64_tr_b16 v[226:227], v185 offset:128\n"
"v_smfmac_f32_16x16x64_bf16 v[106:109], v[82:85], v[128:135], v63\n"
"s_waitcnt lgkmcnt(7)\n"
"ds_read_b64_tr_b16 v[228:229], v184 offset:896\n"
"v_mfma_f32_16x16x32_bf16 v[82:85], v[172:175], v[82:85], 0\n"
"s_add_u32 m0, s34, 0\n"
"ds_read_b64_tr_b16 v[230:231], v185 offset:896\n"
"buffer_load_dwordx4 v190, s[8:11], s32 offen lds\n"
"v_mfma_f32_16x16x32_bf16 v[232:235], a[252:255], v[68:71], v[232:235]\n"
"s_add_u32 s32, s32, s33\n"
"ds_read_b64_tr_b16 v[244:245], v184 offset:136\n"
"v_cvt_pk_bf16_f32 v147, v210, v211\n"
"v_mfma_f32_16x16x32_bf16 v[232:235], v[8:11], v[64:67], v[232:235]\n"
"ds_read_b64_tr_b16 v[246:247], v185 offset:136\n"
"v_exp_f32 v113, v113\n"
"v_mfma_f32_16x16x32_bf16 v[236:239], v[0:3], v[68:71], v[236:239]\n"
"ds_read_b64_tr_b16 v[248:249], v184 offset:904\n"
"v_exp_f32 v114, v114\n"
"v_mfma_f32_16x16x32_bf16 v[236:239], v[12:15], v[64:67], v[236:239]\n"
"ds_read_b64_tr_b16 v[250:251], v185 offset:904\n"
"ds_read_b128 v[128:131], v181 offset:13504\n"
"v_mfma_f32_16x16x32_bf16 v[240:243], v[4:7], v[68:71], v[240:243]\n"
"v_exp_f32 v115, v115\n"
"v_mfma_f32_16x16x32_bf16 v[240:243], v[16:19], v[64:67], v[240:243]\n"
"s_waitcnt vmcnt(11)\n"
"ds_read_b128 v[68:71], v192 offset:48128\n"
"ds_write_b128 v202, v[56:59] offset:30720\n"
"v_mfma_f32_16x16x32_bf16 v[204:207], a[0:3], v[32:35], 0\n"
"s_waitcnt lgkmcnt(11)\n"
"buffer_load_dwordx4 v[56:59], v192, s[28:31], s38 offen sc1\n"
"v_cvt_pk_bf16_f32 v90, v112, v114\n"
"v_mfma_f32_16x16x32_bf16 v[204:207], a[12:15], v[36:39], v[204:207]\n"
"v_cvt_pk_bf16_f32 v91, v113, v115\n"
"v_exp_f32 v116, v116\n"
"v_mfma_f32_16x16x32_bf16 v[204:207], a[24:27], v[40:43], v[204:207]\n"
"ds_read_b128 v[132:135], v181 offset:27008\n"
"ds_read_b128 v[112:115], v178\n"
"v_mfma_f32_16x16x32_bf16 v[204:207], a[36:39], v[44:47], v[204:207]\n"
"s_add_u32 m0, s34, 3072\n"
"v_exp_f32 v117, v117\n"
"v_mfma_f32_16x16x32_bf16 v[204:207], a[48:51], v[48:51], v[204:207]\n"
"buffer_load_dwordx4 v199, s[8:11], s75 offen lds\n"
"v_exp_f32 v118, v118\n"
"v_mfma_f32_16x16x32_bf16 v[204:207], a[60:63], v[52:55], v[204:207]\n"
"s_waitcnt lgkmcnt(3)\n"
"v_exp_f32 v119, v119\n"
"v_mfma_f32_16x16x32_bf16 v[232:235], v[20:23], v[68:71], v[232:235]\n"
"v_cvt_pk_bf16_f32 v92, v116, v118\n"
"v_cvt_pk_bf16_f32 v93, v117, v119\n"
"v_mfma_f32_16x16x32_bf16 v[240:243], v[28:31], v[68:71], v[240:243]\n"
"v_cvt_pk_bf16_f32 v158, v212, v213\n"
"v_cvt_pk_bf16_f32 v159, v214, v215\n"
"v_smfmac_f32_16x16x64_bf16 v[252:255], v[90:93], v[224:231], v63\n"
"v_cvt_pk_bf16_f32 v204, v204, v205\n"
"v_smfmac_f32_16x16x64_bf16 v[98:101], v[90:93], v[244:251], v63\n"
"v_cvt_pk_bf16_f32 v232, v232, v233\n"
"v_mfma_f32_16x16x32_bf16 v[236:239], v[24:27], v[68:71], v[236:239]\n"
"v_cvt_pk_bf16_f32 v233, v234, v235\n"
"v_cvt_pk_bf16_f32 v240, v240, v241\n"
"v_smfmac_f32_16x16x64_bf16 v[106:109], v[90:93], v[120:127], v63\n"
"s_add_u32 m0, s34, 1024\n"
"s_nop 0\n"
"buffer_load_dwordx4 v190, s[8:11], s0 offen lds\n"
"v_mfma_f32_16x16x32_bf16 v[90:93], v[172:175], v[90:93], v[82:85]\n"
"s_add_u32 s0, s0, s33\n"
"ds_read_b128 v[120:123], v179\n"
"v_cvt_pk_bf16_f32 v234, v236, v237\n"
"v_mfma_f32_16x16x32_bf16 v[208:211], a[4:7], v[32:35], 0\n"
"ds_read_b64 v[82:83], v180 offset:13568\n"
"ds_read_b64 v[84:85], v181 offset:13568\n"
"v_mfma_f32_16x16x32_bf16 v[208:211], a[16:19], v[36:39], v[208:211]\n"
"v_cvt_pk_bf16_f32 v235, v238, v239\n"
"v_cvt_pk_bf16_f32 v241, v242, v243\n"
"v_mfma_f32_16x16x32_bf16 v[208:211], a[28:31], v[40:43], v[208:211]\n"
"s_waitcnt vmcnt(10)\n"
"ds_write_b64 v203, v[60:61] offset:30720\n"
"v_mfma_f32_16x16x32_bf16 v[208:211], a[40:43], v[44:47], v[208:211]\n"
"buffer_load_dword v200, off, s[28:31], s39 sc1\n"
"v_cvt_pk_bf16_f32 v252, v252, v253\n"
"v_mfma_f32_16x16x32_bf16 v[208:211], a[52:55], v[48:51], v[208:211]\n"
"ds_write_b128 v196, v[232:235] offset:49152\n"
"v_mfma_f32_16x16x32_bf16 v[208:211], a[64:67], v[52:55], v[208:211]\n"
"s_add_u32 m0, s34, 4096\n"
"buffer_load_dwordx2 v[60:61], v193, s[28:31], s38 offen offset:1024 sc1\n"
"v_cvt_pk_bf16_f32 v253, v254, v255\n"
"v_mfma_f32_16x16x32_bf16 v[212:215], a[8:11], v[32:35], 0\n"
"s_waitcnt lgkmcnt(3)\n"
"v_rcp_f32 v171, v90\n"
"v_mfma_f32_16x16x32_bf16 v[136:139], v[136:139], v[148:151], 0\n"
"s_waitcnt lgkmcnt(2)\n"
"ds_read_b64 v[90:91], v180 offset:27072\n"
"ds_write_b64 v197, v[240:241] offset:49152\n"
"v_mfma_f32_16x16x32_bf16 v[136:139], v[80:83], v[160:163], v[136:139]\n"
"s_add_u32 s38, s38, 1572864\n"
"v_cvt_pk_bf16_f32 v171, v171, v171\n"
"v_and_b32 v92, v171, v176\n"
"v_mfma_f32_16x16x32_bf16 v[128:131], v[128:131], v[148:151], 0\n"
"buffer_load_dwordx4 v199, s[8:11], s76 offen lds\n"
"v_and_b32 v93, v171, v177\n"
"v_mfma_f32_16x16x32_bf16 v[128:131], v[84:87], v[162:165], v[128:131]\n"
"v_cvt_pk_bf16_f32 v98, v98, v99\n"
"v_cvt_pk_bf16_f32 v99, v100, v101\n"
"v_mfma_f32_16x16x16_bf16 v[252:255], v[252:253], v[92:93], 0\n"
"v_cvt_pk_bf16_f32 v106, v106, v107\n"
"v_cvt_pk_bf16_f32 v107, v108, v109\n"
"v_mfma_f32_16x16x16_bf16 v[98:101], v[98:99], v[92:93], 0\n"
"v_cvt_pk_bf16_f32 v205, v206, v207\n"
"v_cvt_pk_bf16_f32 v206, v208, v209\n"
"v_mfma_f32_16x16x16_bf16 v[106:109], v[106:107], v[92:93], 0\n"
"s_waitcnt lgkmcnt(1)\n"
"ds_read_b64 v[92:93], v181 offset:27072\n"
"v_cvt_pk_bf16_f32 v252, v252, v253\n"
"v_mfma_f32_16x16x32_bf16 v[212:215], a[20:23], v[36:39], v[212:215]\n"
"s_waitcnt lgkmcnt(1)\n"
"v_cvt_pk_bf16_f32 v253, v254, v255\n"
"v_cvt_pk_bf16_f32 v254, v98, v99\n"
"v_mfma_f32_16x16x32_bf16 v[140:143], v[140:143], v[148:151], 0\n"
"s_barrier\n"
"v_mfma_f32_16x16x32_bf16 v[140:143], v[88:91], v[160:163], v[140:143]\n"
"ds_read_b64 v[98:99], v178 offset:64\n"
"ds_read_b128 v[116:119], v178 offset:13504\n"
"v_mfma_f32_16x16x32_bf16 v[212:215], a[32:35], v[40:43], v[212:215]\n"
"s_add_u32 m0, s34, 2048\n"
"ds_read_b128 v[124:127], v179 offset:13504\n"
"buffer_load_dwordx4 v190, s[8:11], s1 offen lds\n"
"v_mfma_f32_16x16x32_bf16 v[212:215], a[44:47], v[44:47], v[212:215]\n"
"s_add_u32 s1, s1, s33\n"
"ds_read_b128 v[64:67], v202 offset:49152\n"
"v_cvt_pk_bf16_f32 v255, v100, v101\n"
"v_mfma_f32_16x16x32_bf16 v[212:215], a[56:59], v[48:51], v[212:215]\n"
"ds_read_b64 v[100:101], v179 offset:64\n"
"v_cvt_pk_bf16_f32 v106, v106, v107\n"
"v_mfma_f32_16x16x32_bf16 v[212:215], a[68:71], v[52:55], v[212:215]\n"
"s_waitcnt lgkmcnt(5)\n"
"ds_read_b64 v[68:69], v62 offset:49152\n"
"v_cvt_pk_bf16_f32 v107, v108, v109\n"
"v_mfma_f32_16x16x32_bf16 v[132:135], v[132:135], v[148:151], 0\n"
"ds_read_b64 v[108:109], v179 offset:13568\n"
"buffer_store_dwordx4 v[252:255], v192, s[28:31], s37 offen sc1\n"
"v_mfma_f32_16x16x32_bf16 v[132:135], v[92:95], v[162:165], v[132:135]\n"
"s_waitcnt lgkmcnt(6)\n"
"v_cvt_pk_bf16_f32 v207, v210, v211\n"
"v_cvt_pk_bf16_f32 v212, v212, v213\n"
"v_mfma_f32_16x16x32_bf16 v[112:115], v[112:115], v[148:151], 0\n"
"ds_write_b128 v186, v[204:207] offset:33760\n"
"v_mfma_f32_16x16x32_bf16 v[112:115], v[96:99], v[160:163], v[112:115]\n"
"s_waitcnt lgkmcnt(4)\n"
"v_cvt_pk_bf16_f32 v213, v214, v215\n"
"v_max_f32 v136, v136, v128\n"
"v_mfma_f32_16x16x32_bf16 v[208:211], a[80:83], v[32:35], 0\n"
"s_waitcnt lgkmcnt(3)\n"
"v_max_f32 v137, v137, v129\n"
"v_max_f32 v138, v138, v130\n"
"v_mfma_f32_16x16x32_bf16 v[120:123], v[120:123], v[148:151], 0\n"
"s_waitcnt lgkmcnt(2)\n"
"ds_read_b64_tr_b16 v[128:129], v182 offset:54088\n"
"v_max_f32 v139, v139, v131\n"
"v_mfma_f32_16x16x32_bf16 v[120:123], v[100:103], v[162:165], v[120:123]\n"
"s_waitcnt lgkmcnt(2)\n"
"ds_read_b64_tr_b16 v[130:131], v183 offset:54088\n"
"v_max3_f32 v170, v136, v137, v138\n"
"v_mfma_f32_16x16x32_bf16 v[124:127], v[124:127], v[148:151], 0\n"
"s_add_u32 m0, s34, 5120\n"
"v_max_f32 v140, v140, v132\n"
"v_max_f32 v141, v141, v133\n"
"v_mfma_f32_16x16x32_bf16 v[124:127], v[108:111], v[162:165], v[124:127]\n"
"buffer_load_dwordx4 v199, s[8:11], s77 offen lds\n"
"ds_read_b64_tr_b16 v[132:133], v182 offset:54856\n"
"v_mfma_f32_16x16x32_bf16 v[208:211], a[92:95], v[36:39], v[208:211]\n"
"s_mul_i32 s8, s33, 0\n"
"v_max_f32 v142, v142, v134\n"
"v_max_f32 v143, v143, v135\n"
"v_mfma_f32_16x16x32_bf16 v[208:211], a[104:107], v[40:43], v[208:211]\n"
"s_mul_i32 s9, s33, 1\n"
"ds_read_b64_tr_b16 v[134:135], v183 offset:54856\n"
"v_max3_f32 v170, v170, v139, v140\n"
"v_mfma_f32_16x16x32_bf16 v[208:211], a[116:119], v[44:47], v[208:211]\n"
"s_mul_i32 s10, s33, 2\n"
"v_max3_f32 v170, v170, v141, v142\n"
"v_max_f32 v112, v112, v120\n"
"v_mfma_f32_16x16x32_bf16 v[208:211], a[128:131], v[48:51], v[208:211]\n"
"s_mul_i32 s11, s33, 4\n"
"buffer_store_dwordx2 v[106:107], v193, s[28:31], s37 offen offset:1024 sc1\n"
"v_mfma_f32_16x16x32_bf16 v[208:211], a[140:143], v[52:55], v[208:211]\n"
"s_add_u32 s37, s37, 1572864\n"
"ds_read_b64 v[106:107], v178 offset:13568\n"
"v_max_f32 v113, v113, v121\n"
"v_mfma_f32_16x16x32_bf16 v[204:207], a[72:75], v[32:35], 0\n"
"ds_read_b64_tr_b16 v[120:121], v182 offset:72\n"
"v_max_f32 v114, v114, v122\n"
"v_mfma_f32_16x16x32_bf16 v[204:207], a[84:87], v[36:39], v[204:207]\n"
"v_max_f32 v115, v115, v123\n"
"v_cvt_pk_bf16_f32 v214, v208, v209\n"
"v_mfma_f32_16x16x32_bf16 v[204:207], a[96:99], v[40:43], v[204:207]\n"
"v_cvt_pk_bf16_f32 v215, v210, v211\n"
"v_max3_f32 v170, v170, v143, v112\n"
"v_mfma_f32_16x16x32_bf16 v[204:207], a[108:111], v[44:47], v[204:207]\n"
"ds_write_b128 v186, v[212:215] offset:33824\n"
"v_mfma_f32_16x16x32_bf16 v[204:207], a[120:123], v[48:51], v[204:207]\n"
"buffer_store_dwordx4 v[64:67], v194, s[24:27], s36 offen sc1\n"
"v_mfma_f32_16x16x32_bf16 v[204:207], a[132:135], v[52:55], v[204:207]\n"
"s_waitcnt lgkmcnt(2)\n"
"ds_read_b128 v[64:67], v192 offset:30720\n"
"v_max3_f32 v170, v170, v113, v114\n"
"v_mfma_f32_16x16x32_bf16 v[116:119], v[116:119], v[148:151], 0\n"
"s_mov_b64 exec, s[46:47]\n"
"v_add_u32 v183, 0xc0, v183\n"
"s_mov_b64 exec, -1\n"
"v_mfma_f32_16x16x32_bf16 v[116:119], v[104:107], v[160:163], v[116:119]\n"
"ds_read_b64_tr_b16 v[122:123], v183 offset:72\n"
"v_cvt_pk_bf16_f32 v204, v204, v205\n"
"v_mfma_f32_16x16x32_bf16 v[208:211], a[76:79], v[32:35], 0\n"
"v_cvt_pk_bf16_f32 v205, v206, v207\n"
"v_mov_b64 v[252:253], 0\n"
"v_mfma_f32_16x16x32_bf16 v[208:211], a[88:91], v[36:39], v[208:211]\n"
"v_mov_b64 v[254:255], 0\n"
"v_mov_b64 v[98:99], 0\n"
"v_mfma_f32_16x16x32_bf16 v[208:211], a[100:103], v[40:43], v[208:211]\n"
"v_max_f32 v116, v116, v124\n"
"v_max_f32 v117, v117, v125\n"
"v_mfma_f32_16x16x32_bf16 v[208:211], a[112:115], v[44:47], v[208:211]\n"
"ds_read_b64_tr_b16 v[124:125], v182 offset:840\n"
"v_max_f32 v118, v118, v126\n"
"v_mfma_f32_16x16x32_bf16 v[208:211], a[124:127], v[48:51], v[208:211]\n"
"s_waitcnt lgkmcnt(2)\n"
"buffer_store_dwordx2 v[68:69], v195, s[24:27], s36 offen sc1\n"
"v_mfma_f32_16x16x32_bf16 v[208:211], a[136:139], v[52:55], v[208:211]\n"
"s_add_u32 s36, s36, s33\n"
"ds_read_b128 v[68:71], v192 offset:31744\n"
"v_max_f32 v119, v119, v127\n"
"v_mfma_f32_16x16x32_bf16 v[232:235], a[216:219], v[64:67], 0\n"
"ds_read_b64_tr_b16 v[126:127], v183 offset:840\n"
"v_max3_f32 v170, v170, v115, v116\n"
"v_mfma_f32_16x16x32_bf16 v[236:239], a[220:223], v[64:67], 0\n"
"v_max3_f32 v170, v170, v117, v118\n"
"v_cvt_pk_bf16_f32 v206, v208, v209\n"
"v_mfma_f32_16x16x32_bf16 v[240:243], a[224:227], v[64:67], 0\n"
"ds_read_b128 v[64:67], v192 offset:32768\n"
"v_cvt_pk_bf16_f32 v207, v210, v211\n"
"v_mfma_f32_16x16x32_bf16 v[208:211], a[148:151], v[32:35], 0\n"
"ds_write_b128 v186, v[204:207] offset:33888\n"
"v_mfma_f32_16x16x32_bf16 v[208:211], a[160:163], v[36:39], v[208:211]\n"
"v_mov_b64 v[100:101], 0\n"
"v_mov_b64 v[106:107], 0\n"
"v_mfma_f32_16x16x32_bf16 v[208:211], a[172:175], v[40:43], v[208:211]\n"
"s_waitcnt lgkmcnt(3)\n"
"v_mov_b64 v[108:109], 0\n"
"v_max_f32 v171, v170, v119\n"
"v_mfma_f32_16x16x32_bf16 v[208:211], a[184:187], v[44:47], v[208:211]\n"
"v_max_f32 v170, v170, v119\n"
"v_mfma_f32_16x16x32_bf16 v[208:211], a[196:199], v[48:51], v[208:211]\n"
"s_nop 0\n"
"v_permlane16_swap_b32 v170, v171\n"
"v_mfma_f32_16x16x32_bf16 v[204:207], a[152:155], v[32:35], 0\n"
"v_max_f32 v170, v170, v171\n"
"v_mov_b32 v171, v170\n"
"v_mfma_f32_16x16x32_bf16 v[204:207], a[164:167], v[36:39], v[204:207]\n"
"s_nop 0\n"
"v_permlane32_swap_b32 v170, v171\n"
"v_max_f32 v170, v170, v171\n"
"v_mul_f32 v170, 0xbe553b95, v170\n"
"v_pk_fma_f32 v[136:137], v[136:137], s[70:71], v[170:171] op_sel_hi:[1,0,0]\n"
"v_pk_fma_f32 v[138:139], v[138:139], s[70:71], v[170:171] op_sel_hi:[1,0,0]\n"
"v_pk_fma_f32 v[140:141], v[140:141], s[70:71], v[170:171] op_sel_hi:[1,0,0]\n"
"v_pk_fma_f32 v[142:143], v[142:143], s[70:71], v[170:171] op_sel_hi:[1,0,0]\n"
"v_pk_fma_f32 v[112:113], v[112:113], s[70:71], v[170:171] op_sel_hi:[1,0,0]\n"
"v_pk_fma_f32 v[114:115], v[114:115], s[70:71], v[170:171] op_sel_hi:[1,0,0]\n"
"v_pk_fma_f32 v[116:117], v[116:117], s[70:71], v[170:171] op_sel_hi:[1,0,0]\n"
"v_pk_fma_f32 v[118:119], v[118:119], s[70:71], v[170:171] op_sel_hi:[1,0,0]\n"
"v_mfma_f32_16x16x32_bf16 v[204:207], a[176:179], v[40:43], v[204:207]\n"
"v_exp_f32 v136, v136\n"
"v_mfma_f32_16x16x32_bf16 v[204:207], a[188:191], v[44:47], v[204:207]\n"
"v_exp_f32 v137, v137\n"
"v_mfma_f32_16x16x32_bf16 v[204:207], a[200:203], v[48:51], v[204:207]\n"
"v_exp_f32 v138, v138\n"
"v_mfma_f32_16x16x32_bf16 v[232:235], a[228:231], v[68:71], v[232:235]\n"
"s_waitcnt lgkmcnt(1)\n"
"v_exp_f32 v139, v139\n"
"v_mfma_f32_16x16x32_bf16 v[232:235], a[240:243], v[64:67], v[232:235]\n"
"v_cvt_pk_bf16_f32 v82, v136, v138\n"
"v_cvt_pk_bf16_f32 v83, v137, v139\n"
"v_mfma_f32_16x16x32_bf16 v[236:239], a[232:235], v[68:71], v[236:239]\n"
"ds_read_b128 v[136:139], v180 offset:27008\n"
"v_exp_f32 v140, v140\n"
"v_mfma_f32_16x16x32_bf16 v[236:239], a[244:247], v[64:67], v[236:239]\n"
"v_exp_f32 v141, v141\n"
"v_mfma_f32_16x16x32_bf16 v[212:215], a[144:147], v[32:35], 0\n"
"ds_read_b128 v[32:35], v188 offset:6144\n"
"v_exp_f32 v142, v142\n"
"v_mfma_f32_16x16x32_bf16 v[212:215], a[156:159], v[36:39], v[212:215]\n"
"ds_read_b128 v[36:39], v189 offset:6144\n"
"v_cvt_pk_bf16_f32 v84, v140, v142\n"
"v_mfma_f32_16x16x32_bf16 v[212:215], a[168:171], v[40:43], v[212:215]\n"
"ds_read_b128 v[40:43], v188 offset:7168\n"
"v_exp_f32 v143, v143\n"
"v_mfma_f32_16x16x32_bf16 v[212:215], a[180:183], v[44:47], v[212:215]\n"
"s_waitcnt vmcnt(11)\n"
"ds_read_b128 v[44:47], v189 offset:7168\n"
"ds_write_b128 v202, v[56:59] offset:55296\n"
"v_mfma_f32_16x16x32_bf16 v[212:215], a[192:195], v[48:51], v[212:215]\n"
"v_cvt_pk_bf16_f32 v85, v141, v143\n"
"v_exp_f32 v112, v112\n"
"v_mfma_f32_16x16x32_bf16 v[212:215], a[204:207], v[52:55], v[212:215]\n"
"ds_read_b128 v[48:51], v188 offset:8192\n"
"ds_read_b128 v[140:143], v178\n"
"v_mfma_f32_16x16x32_bf16 v[240:243], a[236:239], v[68:71], v[240:243]\n"
"ds_read_b128 v[68:71], v192 offset:33792\n"
"v_exp_f32 v113, v113\n"
"v_mfma_f32_16x16x32_bf16 v[240:243], a[248:251], v[64:67], v[240:243]\n"
"ds_read_b128 v[64:67], v192 offset:34816\n"
"v_exp_f32 v114, v114\n"
"v_smfmac_f32_16x16x64_bf16 v[252:255], v[82:85], v[216:223], v63\n"
"ds_read_b64_tr_b16 v[216:217], v182 offset:128\n"
"v_smfmac_f32_16x16x64_bf16 v[98:101], v[82:85], v[72:79], v63\n"
"ds_read_b64_tr_b16 v[218:219], v183 offset:128\n"
"v_smfmac_f32_16x16x64_bf16 v[106:109], v[82:85], v[128:135], v63\n"
"ds_read_b64_tr_b16 v[220:221], v182 offset:896\n"
"v_mfma_f32_16x16x32_bf16 v[82:85], v[172:175], v[82:85], 0\n"
"ds_read_b64_tr_b16 v[222:223], v183 offset:896\n"
"v_cvt_pk_bf16_f32 v148, v212, v213\n"
"v_mfma_f32_16x16x32_bf16 v[208:211], a[208:211], v[52:55], v[208:211]\n"
"ds_read_b64_tr_b16 v[72:73], v182 offset:136\n"
"v_cvt_pk_bf16_f32 v149, v214, v215\n"
"v_mfma_f32_16x16x32_bf16 v[204:207], a[212:215], v[52:55], v[204:207]\n"
"s_waitcnt lgkmcnt(6)\n"
"ds_read_b128 v[52:55], v189 offset:8192\n"
"v_cvt_pk_bf16_f32 v90, v112, v114\n"
"v_mfma_f32_16x16x32_bf16 v[212:215], a[0:3], v[32:35], 0\n"
"ds_read_b64_tr_b16 v[74:75], v183 offset:136\n"
"v_cvt_pk_bf16_f32 v150, v208, v209\n"
"v_mfma_f32_16x16x32_bf16 v[212:215], a[12:15], v[36:39], v[212:215]\n"
"ds_read_b64_tr_b16 v[76:77], v182 offset:904\n"
"v_cvt_pk_bf16_f32 v151, v210, v211\n"
"v_mfma_f32_16x16x32_bf16 v[212:215], a[24:27], v[40:43], v[212:215]\n"
"ds_read_b64_tr_b16 v[78:79], v183 offset:904\n"
"v_exp_f32 v115, v115\n"
"v_mfma_f32_16x16x32_bf16 v[212:215], a[36:39], v[44:47], v[212:215]\n"
"ds_read_b128 v[128:131], v181 offset:27008\n"
"ds_read_b128 v[132:135], v179\n"
"v_mfma_f32_16x16x32_bf16 v[212:215], a[48:51], v[48:51], v[212:215]\n"
"v_cvt_pk_bf16_f32 v91, v113, v115\n"
"v_exp_f32 v116, v116\n"
"v_mfma_f32_16x16x32_bf16 v[232:235], a[252:255], v[68:71], v[232:235]\n"
"s_waitcnt lgkmcnt(11)\n"
"ds_read_b128 v[112:115], v178 offset:13504\n"
"v_exp_f32 v117, v117\n"
"v_mfma_f32_16x16x32_bf16 v[232:235], v[8:11], v[64:67], v[232:235]\n"
"s_waitcnt vmcnt(7) lgkmcnt(6)\n"
"v_exp_f32 v118, v118\n"
"v_mfma_f32_16x16x32_bf16 v[236:239], v[0:3], v[68:71], v[236:239]\n"
"v_add3_u32 v200, v200, s50, -8\n"
"v_cmp_lt_u32 vcc, 64, v200\n"
"v_mfma_f32_16x16x32_bf16 v[236:239], v[12:15], v[64:67], v[236:239]\n"
"ds_write_b64 v203, v[60:61] offset:55296\n"
"v_mfma_f32_16x16x32_bf16 v[240:243], v[4:7], v[68:71], v[240:243]\n"
"s_nop 1\n"
"s_cbranch_vccnz .Lp7%=\n"
".Lr7%=:\n"
"v_mfma_f32_16x16x32_bf16 v[240:243], v[16:19], v[64:67], v[240:243]\n"
"buffer_load_dwordx4 a[198:201], v192, s[28:31], s38 offen sc1\n"
"ds_read_b128 v[68:71], v192 offset:35840\n"
"v_mfma_f32_16x16x32_bf16 v[212:215], a[60:63], v[52:55], v[212:215]\n"
"v_exp_f32 v119, v119\n"
"v_mfma_f32_16x16x32_bf16 v[208:211], a[4:7], v[32:35], 0\n"
"v_cvt_pk_bf16_f32 v92, v116, v118\n"
"v_cvt_pk_bf16_f32 v93, v117, v119\n"
"v_mfma_f32_16x16x32_bf16 v[208:211], a[16:19], v[36:39], v[208:211]\n"
"s_waitcnt lgkmcnt(5)\n"
"v_cvt_pk_bf16_f32 v162, v204, v205\n"
"v_cvt_pk_bf16_f32 v163, v206, v207\n"
"v_smfmac_f32_16x16x64_bf16 v[252:255], v[90:93], v[216:223], v63\n"
"buffer_load_dwordx2 a[202:203], v193, s[28:31], s38 offen offset:1024 sc1\n"
"v_smfmac_f32_16x16x64_bf16 v[98:101], v[90:93], v[72:79], v63\n"
"s_add_u32 s38, s38, 1572864\n"
"v_cvt_pk_bf16_f32 v212, v212, v213\n"
"v_smfmac_f32_16x16x64_bf16 v[106:109], v[90:93], v[120:127], v63\n"
"ds_read_b128 v[120:123], v179 offset:13504\n"
"v_mfma_f32_16x16x32_bf16 v[90:93], v[172:175], v[90:93], v[82:85]\n"
"s_waitcnt lgkmcnt(1)\n"
"v_cvt_pk_bf16_f32 v252, v252, v253\n"
"v_cvt_pk_bf16_f32 v253, v254, v255\n"
"v_mfma_f32_16x16x32_bf16 v[232:235], v[20:23], v[68:71], v[232:235]\n"
"ds_read_b64 v[82:83], v180 offset:27072\n"
"buffer_load_dwordx4 a[204:207], v192, s[28:31], s38 offen sc1\n"
"v_mfma_f32_16x16x32_bf16 v[240:243], v[28:31], v[68:71], v[240:243]\n"
"ds_read_b64 v[84:85], v181 offset:27072\n"
"v_cvt_pk_bf16_f32 v106, v106, v107\n"
"v_mfma_f32_16x16x32_bf16 v[236:239], v[24:27], v[68:71], v[236:239]\n"
"v_rcp_f32 v171, v90\n"
"v_mfma_f32_16x16x32_bf16 v[208:211], a[28:31], v[40:43], v[208:211]\n"
"ds_read_b64 v[90:91], v178 offset:64\n"
"v_cvt_pk_bf16_f32 v171, v171, v171\n"
"v_mfma_f32_16x16x32_bf16 v[208:211], a[40:43], v[44:47], v[208:211]\n"
"v_and_b32 v92, v171, v176\n"
"v_and_b32 v93, v171, v177\n"
"v_mfma_f32_16x16x32_bf16 v[208:211], a[52:55], v[48:51], v[208:211]\n"
"buffer_load_dwordx2 a[208:209], v193, s[28:31], s38 offen offset:1024 sc1\n"
"v_cvt_pk_bf16_f32 v98, v98, v99\n"
"v_mfma_f32_16x16x16_bf16 v[252:255], v[252:253], v[92:93], 0\n"
"s_add_u32 s38, s38, 1572864\n"
"v_cvt_pk_bf16_f32 v107, v108, v109\n"
"v_cvt_pk_bf16_f32 v99, v100, v101\n"
"v_mfma_f32_16x16x32_bf16 v[208:211], a[64:67], v[52:55], v[208:211]\n"
"s_waitcnt lgkmcnt(2)\n"
"v_cvt_pk_bf16_f32 v232, v232, v233\n"
"v_cvt_pk_bf16_f32 v233, v234, v235\n"
"v_mfma_f32_16x16x32_bf16 v[136:139], v[136:139], v[152:155], 0\n"
"s_waitcnt lgkmcnt(1)\n"
"v_cvt_pk_bf16_f32 v240, v240, v241\n"
"v_cvt_pk_bf16_f32 v252, v252, v253\n"
"v_mfma_f32_16x16x32_bf16 v[136:139], v[80:83], v[164:167], v[136:139]\n"
"s_add_u32 s12, s38, 0\n"
"buffer_load_dwordx4 a[144:147], v192, s[28:31], s12 offen sc1\n"
"v_cvt_pk_bf16_f32 v234, v236, v237\n"
"v_mfma_f32_16x16x32_bf16 v[128:131], v[128:131], v[152:155], 0\n"
"s_waitcnt lgkmcnt(0)\n"
"v_cvt_pk_bf16_f32 v235, v238, v239\n"
"v_cvt_pk_bf16_f32 v241, v242, v243\n"
"v_mfma_f32_16x16x32_bf16 v[128:131], v[84:87], v[166:169], v[128:131]\n"
"s_add_u32 s13, s38, 1572864\n"
"ds_write_b128 v196, v[232:235] offset:36864\n"
"v_mfma_f32_16x16x32_bf16 v[140:143], v[140:143], v[152:155], 0\n"
"s_add_u32 s14, s38, 3145728\n"
"v_cvt_pk_bf16_f32 v253, v254, v255\n"
"v_cvt_pk_bf16_f32 v213, v214, v215\n"
"v_mfma_f32_16x16x32_bf16 v[140:143], v[88:91], v[164:167], v[140:143]\n"
"s_add_u32 s16, s38, 4718592\n"
"buffer_load_dwordx2 a[148:149], v193, s[28:31], s12 offen offset:1024 sc1\n"
"ds_write_b64 v197, v[240:241] offset:36864\n"
"v_mfma_f32_16x16x16_bf16 v[106:109], v[106:107], v[92:93], 0\n"
"s_add_u32 s15, s38, 6291456\n"
"v_cvt_pk_bf16_f32 v214, v208, v209\n"
"v_cvt_pk_bf16_f32 v215, v210, v211\n"
"v_mfma_f32_16x16x16_bf16 v[98:101], v[98:99], v[92:93], 0\n"
"s_add_u32 s17, s38, 7864320\n"
"ds_read_b64 v[92:93], v179 offset:64\n"
"v_max_f32 v136, v136, v128\n"
"v_mfma_f32_16x16x32_bf16 v[204:207], a[8:11], v[32:35], 0\n"
"s_add_u32 s18, s38, 9437184\n"
"v_cvt_pk_bf16_f32 v106, v106, v107\n"
"v_cvt_pk_bf16_f32 v107, v108, v109\n"
"v_mfma_f32_16x16x32_bf16 v[204:207], a[20:23], v[36:39], v[204:207]\n"
"buffer_store_dwordx2 v[106:107], v193, s[28:31], s37 offen offset:1024 sc1\n"
"v_mfma_f32_16x16x32_bf16 v[204:207], a[32:35], v[40:43], v[204:207]\n"
"v_cvt_pk_bf16_f32 v254, v98, v99\n"
"v_cvt_pk_bf16_f32 v255, v100, v101\n"
"v_mfma_f32_16x16x32_bf16 v[204:207], a[44:47], v[44:47], v[204:207]\n"
"s_waitcnt lgkmcnt(1)\n"
"ds_read_b64 v[98:99], v178 offset:13568\n"
"ds_read_b64 v[100:101], v179 offset:13568\n"
"v_mfma_f32_16x16x32_bf16 v[204:207], a[56:59], v[48:51], v[204:207]\n"
"s_waitcnt vmcnt(9)\n"
"v_max_f32 v137, v137, v129\n"
"v_max_f32 v138, v138, v130\n"
"v_mfma_f32_16x16x32_bf16 v[204:207], a[68:71], v[52:55], v[204:207]\n"
"s_barrier\n"
"v_mfma_f32_16x16x32_bf16 v[208:211], a[80:83], v[32:35], 0\n"
"s_waitcnt lgkmcnt(2)\n"
"v_or_b32 v201, 10, s6\n"
"v_max_f32 v139, v139, v131\n"
"v_mfma_f32_16x16x32_bf16 v[132:135], v[132:135], v[152:155], 0\n"
"ds_read_b128 v[116:119], v178 offset:27008\n"
"s_mov_b64 exec, 1\n"
"buffer_store_dword v201, off, s[28:31], s40 sc1\n"
"s_mov_b64 exec, -1\n"
"v_mfma_f32_16x16x32_bf16 v[132:135], v[92:95], v[166:169], v[132:135]\n"
"ds_read_b64 v[106:107], v178 offset:27072\n"
"ds_read_b128 v[124:127], v179 offset:27008\n"
"v_mfma_f32_16x16x32_bf16 v[208:211], a[92:95], v[36:39], v[208:211]\n"
"s_waitcnt lgkmcnt(4)\n"
"buffer_store_dwordx4 v[252:255], v192, s[28:31], s37 offen sc1\n"
"v_mfma_f32_16x16x32_bf16 v[112:115], v[112:115], v[152:155], 0\n"
"s_waitcnt lgkmcnt(3)\n"
"ds_read_b64 v[108:109], v179 offset:27072\n"
"ds_write_b128 v186, v[212:215] offset:47264\n"
"v_mfma_f32_16x16x32_bf16 v[112:115], v[96:99], v[164:167], v[112:115]\n"
"s_add_u32 s37, s37, 1572864\n"
"v_max3_f32 v170, v136, v137, v138\n"
"v_max_f32 v140, v140, v132\n"
"v_mfma_f32_16x16x32_bf16 v[120:123], v[120:123], v[152:155], 0\n"
"v_max_f32 v141, v141, v133\n"
"v_max_f32 v142, v142, v134\n"
"v_mfma_f32_16x16x32_bf16 v[120:123], v[100:103], v[166:169], v[120:123]\n"
"s_waitcnt lgkmcnt(3)\n"
"ds_read_b128 v[64:67], v202 offset:36864\n"
"ds_read_b64 v[68:69], v62 offset:36864\n"
"v_mfma_f32_16x16x32_bf16 v[116:119], v[116:119], v[152:155], 0\n"
"ds_read_b64_tr_b16 v[128:129], v184 offset:72\n"
"v_max_f32 v143, v143, v135\n"
"v_mfma_f32_16x16x32_bf16 v[116:119], v[104:107], v[164:167], v[116:119]\n"
"ds_read_b64_tr_b16 v[130:131], v185 offset:72\n"
"v_max3_f32 v170, v170, v139, v140\n"
"v_mfma_f32_16x16x32_bf16 v[208:211], a[104:107], v[40:43], v[208:211]\n"
"s_waitcnt lgkmcnt(5)\n"
"ds_read_b64_tr_b16 v[132:133], v184 offset:840\n"
"v_max3_f32 v170, v170, v141, v142\n"
"v_mfma_f32_16x16x32_bf16 v[124:127], v[124:127], v[152:155], 0\n"
"ds_read_b64_tr_b16 v[134:135], v185 offset:840\n"
"v_max_f32 v112, v112, v120\n"
"v_mfma_f32_16x16x32_bf16 v[124:127], v[108:111], v[166:169], v[124:127]\n"
"ds_read_b64_tr_b16 a[168:169], v182 offset:13576\n"
"v_max_f32 v113, v113, v121\n"
"v_mfma_f32_16x16x32_bf16 v[208:211], a[116:119], v[44:47], v[208:211]\n"
"ds_read_b64_tr_b16 a[170:171], v183 offset:13576\n"
"v_max_f32 v114, v114, v122\n"
"v_mfma_f32_16x16x32_bf16 v[208:211], a[128:131], v[48:51], v[208:211]\n"
"s_waitcnt lgkmcnt(6)\n"
"ds_read_b64_tr_b16 a[172:173], v182 offset:14344\n"
"buffer_store_dwordx4 v[64:67], v194, s[24:27], s36 offen sc1\n"
"v_mfma_f32_16x16x32_bf16 v[208:211], a[140:143], v[52:55], v[208:211]\n"
"s_nop 0\n"
"ds_read_b128 v[64:67], v192 offset:55296\n"
"ds_read_b64_tr_b16 a[174:175], v183 offset:14344\n"
"v_mfma_f32_16x16x32_bf16 v[212:215], a[72:75], v[32:35], 0\n"
"v_max_f32 v115, v115, v123\n"
"v_max3_f32 v170, v170, v143, v112\n"
"v_mfma_f32_16x16x32_bf16 v[212:215], a[84:87], v[36:39], v[212:215]\n"
"ds_read_b128 v[120:123], v178\n"
"v_max3_f32 v170, v170, v113, v114\n"
"v_mfma_f32_16x16x32_bf16 v[212:215], a[96:99], v[40:43], v[212:215]\n"
"v_max_f32 v116, v116, v124\n"
"v_max_f32 v117, v117, v125\n"
"v_mfma_f32_16x16x32_bf16 v[212:215], a[108:111], v[44:47], v[212:215]\n"
"v_cvt_pk_bf16_f32 v204, v204, v205\n"
"v_max_f32 v118, v118, v126\n"
"v_mfma_f32_16x16x32_bf16 v[212:215], a[120:123], v[48:51], v[212:215]\n"
"v_max_f32 v119, v119, v127\n"
"v_cvt_pk_bf16_f32 v205, v206, v207\n"
"v_mfma_f32_16x16x32_bf16 v[212:215], a[132:135], v[52:55], v[212:215]\n"
"ds_read_b128 v[124:127], v178 offset:13504\n"
"v_max3_f32 v170, v170, v115, v116\n"
"v_max3_f32 v170, v170, v117, v118\n"
"v_mov_b64 v[252:253], 0\n"
"v_mov_b64 v[254:255], 0\n"
"v_mov_b64 v[98:99], 0\n"
"v_mov_b64 v[100:101], 0\n"
"v_mov_b64 v[106:107], 0\n"
"v_mov_b64 v[108:109], 0\n"
"v_max_f32 v171, v170, v119\n"
"v_max_f32 v170, v170, v119\n"
"v_cvt_pk_bf16_f32 v206, v208, v209\n"
"v_cvt_pk_bf16_f32 v207, v210, v211\n"
"v_mfma_f32_16x16x32_bf16 v[208:211], a[76:79], v[32:35], 0\n"
"s_waitcnt lgkmcnt(3)\n"
"ds_read_b128 v[32:35], v188\n"
"buffer_store_dwordx2 v[68:69], v195, s[24:27], s36 offen sc1\n"
"v_mfma_f32_16x16x32_bf16 v[232:235], a[216:219], v[64:67], 0\n"
"s_add_u32 s36, s36, s33\n"
"ds_read_b128 v[68:71], v192 offset:56320\n"
"ds_write_b128 v186, v[204:207] offset:47328\n"
"v_mfma_f32_16x16x32_bf16 v[236:239], a[220:223], v[64:67], 0\n"
"v_permlane16_swap_b32 v170, v171\n"
"v_mfma_f32_16x16x32_bf16 v[240:243], a[224:227], v[64:67], 0\n"
"v_max_f32 v170, v170, v171\n"
"v_mov_b32 v171, v170\n"
"v_mfma_f32_16x16x32_bf16 v[208:211], a[88:91], v[36:39], v[208:211]\n"
"ds_read_b128 v[64:67], v192 offset:57344\n"
"ds_read_b128 v[36:39], v189\n"
"v_mfma_f32_16x16x32_bf16 v[208:211], a[100:103], v[40:43], v[208:211]\n"
"s_waitcnt lgkmcnt(4)\n"
"ds_read_b128 v[40:43], v188 offset:1024\n"
"v_permlane32_swap_b32 v170, v171\n"
"v_max_f32 v170, v170, v171\n"
"v_mul_f32 v170, 0xbe553b95, v170\n"
"v_pk_fma_f32 v[136:137], v[136:137], s[70:71], v[170:171] op_sel_hi:[1,0,0]\n"
"v_pk_fma_f32 v[138:139], v[138:139], s[70:71], v[170:171] op_sel_hi:[1,0,0]\n"
"v_pk_fma_f32 v[140:141], v[140:141], s[70:71], v[170:171] op_sel_hi:[1,0,0]\n"
"v_pk_fma_f32 v[142:143], v[142:143], s[70:71], v[170:171] op_sel_hi:[1,0,0]\n"
"v_pk_fma_f32 v[112:113], v[112:113], s[70:71], v[170:171] op_sel_hi:[1,0,0]\n"
"v_pk_fma_f32 v[114:115], v[114:115], s[70:71], v[170:171] op_sel_hi:[1,0,0]\n"
"v_pk_fma_f32 v[116:117], v[116:117], s[70:71], v[170:171] op_sel_hi:[1,0,0]\n"
"v_pk_fma_f32 v[118:119], v[118:119], s[70:71], v[170:171] op_sel_hi:[1,0,0]\n"
".Lic6%=:\n"
"s_cbranch_execz .Lib1%=\n"
"v_mfma_f32_16x16x32_bf16 v[208:211], a[112:115], v[44:47], v[208:211]\n"
"s_waitcnt lgkmcnt(4)\n"
"ds_read_b128 v[44:47], v189 offset:1024\n"
"v_exp_f32 v136, v136\n"
"v_mfma_f32_16x16x32_bf16 v[232:235], a[228:231], v[68:71], v[232:235]\n"
"v_exp_f32 v137, v137\n"
"v_mfma_f32_16x16x32_bf16 v[236:239], a[232:235], v[68:71], v[236:239]\n"
"s_waitcnt lgkmcnt(3)\n"
"v_exp_f32 v138, v138\n"
"v_mfma_f32_16x16x32_bf16 v[236:239], a[244:247], v[64:67], v[236:239]\n"
"v_exp_f32 v139, v139\n"
"v_mfma_f32_16x16x32_bf16 v[240:243], a[236:239], v[68:71], v[240:243]\n"
"ds_read_b128 v[68:71], v192 offset:58368\n"
"v_cvt_pk_bf16_f32 v82, v136, v138\n"
"v_mfma_f32_16x16x32_bf16 v[240:243], a[248:251], v[64:67], v[240:243]\n"
"v_cvt_pk_bf16_f32 v83, v137, v139\n"
"v_exp_f32 v140, v140\n"
"v_mfma_f32_16x16x32_bf16 v[232:235], a[240:243], v[64:67], v[232:235]\n"
"ds_read_b128 v[64:67], v192 offset:59392\n"
"ds_read_b128 v[136:139], v179\n"
"v_mfma_f32_16x16x32_bf16 v[208:211], a[124:127], v[48:51], v[208:211]\n"
"ds_read_b128 v[48:51], v188 offset:2048\n"
"v_exp_f32 v141, v141\n"
"v_mfma_f32_16x16x32_bf16 v[208:211], a[136:139], v[52:55], v[208:211]\n"
"ds_read_b128 v[52:55], v189 offset:2048\n"
"v_exp_f32 v142, v142\n"
"v_mfma_f32_16x16x32_bf16 v[204:207], a[0:3], v[32:35], 0\n"
"s_waitcnt lgkmcnt(7)\n"
"v_exp_f32 v143, v143\n"
"v_mfma_f32_16x16x32_bf16 v[204:207], a[12:15], v[36:39], v[204:207]\n"
"s_waitcnt vmcnt(10) lgkmcnt(4)\n"
"v_cvt_pk_bf16_f32 v84, v140, v142\n"
"v_cvt_pk_bf16_f32 v85, v141, v143\n"
"v_mfma_f32_16x16x32_bf16 v[204:207], a[24:27], v[40:43], v[204:207]\n"
"ds_write_b128 v202, a[198:201] offset:30720\n"
"v_smfmac_f32_16x16x64_bf16 v[252:255], v[82:85], v[224:231], v63\n"
"v_cvt_pk_bf16_f32 v212, v212, v213\n"
"v_smfmac_f32_16x16x64_bf16 v[98:101], v[82:85], v[244:251], v63\n"
"s_waitcnt vmcnt(9)\n"
"ds_read_b64_tr_b16 v[224:225], v182 offset:13632\n"
"v_smfmac_f32_16x16x64_bf16 v[106:109], v[82:85], v[128:135], v63\n"
"ds_write_b64 v203, a[202:203] offset:30720\n"
"v_mfma_f32_16x16x32_bf16 v[82:85], v[172:175], v[82:85], 0\n"
"v_exp_f32 v112, v112\n"
"v_mfma_f32_16x16x32_bf16 v[232:235], a[252:255], v[68:71], v[232:235]\n"
"s_waitcnt lgkmcnt(6)\n"
"ds_read_b64_tr_b16 v[226:227], v183 offset:13632\n"
"ds_read_b64_tr_b16 v[228:229], v182 offset:14400\n"
"v_mfma_f32_16x16x32_bf16 v[232:235], v[8:11], v[64:67], v[232:235]\n"
"ds_read_b64_tr_b16 v[230:231], v183 offset:14400\n"
"v_cvt_pk_bf16_f32 v213, v214, v215\n"
"v_mfma_f32_16x16x32_bf16 v[236:239], v[0:3], v[68:71], v[236:239]\n"
"ds_read_b64_tr_b16 v[244:245], v182 offset:13640\n"
"v_exp_f32 v113, v113\n"
"v_mfma_f32_16x16x32_bf16 v[236:239], v[12:15], v[64:67], v[236:239]\n"
"ds_read_b64_tr_b16 v[246:247], v183 offset:13640\n"
"v_exp_f32 v114, v114\n"
"v_mfma_f32_16x16x32_bf16 v[240:243], v[4:7], v[68:71], v[240:243]\n"
"ds_read_b128 v[68:71], v192 offset:60416\n"
"v_cvt_pk_bf16_f32 v90, v112, v114\n"
"v_mfma_f32_16x16x32_bf16 v[240:243], v[16:19], v[64:67], v[240:243]\n"
"ds_read_b64_tr_b16 v[248:249], v182 offset:14408\n"
"v_exp_f32 v115, v115\n"
"v_mfma_f32_16x16x32_bf16 v[204:207], a[36:39], v[44:47], v[204:207]\n"
"s_waitcnt lgkmcnt(11)\n"
"ds_read_b64_tr_b16 v[250:251], v183 offset:14408\n"
"ds_read_b128 v[140:143], v179 offset:13504\n"
"v_mfma_f32_16x16x32_bf16 v[204:207], a[48:51], v[48:51], v[204:207]\n"
"s_waitcnt lgkmcnt(12)\n"
"v_cvt_pk_bf16_f32 v91, v113, v115\n"
"v_exp_f32 v116, v116\n"
"v_mfma_f32_16x16x32_bf16 v[204:207], a[60:63], v[52:55], v[204:207]\n"
"v_exp_f32 v117, v117\n"
"v_exp_f32 v118, v118\n"
"v_exp_f32 v119, v119\n"
"v_cvt_pk_bf16_f32 v92, v116, v118\n"
"v_cvt_pk_bf16_f32 v93, v117, v119\n"
"v_cvt_pk_bf16_f32 v214, v208, v209\n"
"v_cvt_pk_bf16_f32 v215, v210, v211\n"
"v_smfmac_f32_16x16x64_bf16 v[106:109], v[90:93], a[168:175], v63\n"
"s_waitcnt lgkmcnt(3)\n"
"ds_read_b128 v[128:131], v178 offset:27008\n"
"v_smfmac_f32_16x16x64_bf16 v[252:255], v[90:93], v[224:231], v63\n"
"ds_read_b128 v[112:115], v179 offset:27008\n"
"v_mfma_f32_16x16x32_bf16 v[232:235], v[20:23], v[68:71], v[232:235]\n"
"s_waitcnt lgkmcnt(3)\n"
"ds_write_b128 v186, v[212:215] offset:47392\n"
"v_smfmac_f32_16x16x64_bf16 v[98:101], v[90:93], v[244:251], v63\n"
"v_cvt_pk_bf16_f32 v106, v106, v107\n"
"v_mfma_f32_16x16x32_bf16 v[90:93], v[172:175], v[90:93], v[82:85]\n"
"v_cvt_pk_bf16_f32 v107, v108, v109\n"
"v_cvt_pk_bf16_f32 v252, v252, v253\n"
"v_mfma_f32_16x16x32_bf16 v[240:243], v[28:31], v[68:71], v[240:243]\n"
"ds_read_b64 v[82:83], v178 offset:64\n"
"ds_read_b64 v[84:85], v179 offset:64\n"
"v_mfma_f32_16x16x32_bf16 v[236:239], v[24:27], v[68:71], v[236:239]\n"
"v_cvt_pk_bf16_f32 v232, v232, v233\n"
"v_cvt_pk_bf16_f32 v233, v234, v235\n"
"v_mfma_f32_16x16x32_bf16 v[208:211], a[4:7], v[32:35], 0\n"
"v_rcp_f32 v171, v90\n"
"v_mfma_f32_16x16x32_bf16 v[208:211], a[16:19], v[36:39], v[208:211]\n"
"ds_read_b64 v[90:91], v178 offset:13568\n"
"v_cvt_pk_bf16_f32 v171, v171, v171\n"
"v_mfma_f32_16x16x32_bf16 v[208:211], a[28:31], v[40:43], v[208:211]\n"
"s_waitcnt vmcnt(2)\n"
"v_and_b32 v92, v171, v176\n"
"v_or_b32 v201, 11, s6\n"
"v_mfma_f32_16x16x32_bf16 v[208:211], a[40:43], v[44:47], v[208:211]\n"
"s_mov_b64 exec, 1\n"
"buffer_store_dword v201, off, s[28:31], s40 sc1\n"
"s_mov_b64 exec, -1\n"
"v_mfma_f32_16x16x32_bf16 v[208:211], a[52:55], v[48:51], v[208:211]\n"
"s_waitcnt lgkmcnt(2)\n"
"v_cvt_pk_bf16_f32 v240, v240, v241\n"
"v_cvt_pk_bf16_f32 v234, v236, v237\n"
"v_mfma_f32_16x16x32_bf16 v[208:211], a[64:67], v[52:55], v[208:211]\n"
"s_waitcnt lgkmcnt(1)\n"
"v_cvt_pk_bf16_f32 v235, v238, v239\n"
"v_cvt_pk_bf16_f32 v241, v242, v243\n"
"v_mfma_f32_16x16x32_bf16 v[120:123], v[120:123], v[144:147], 0\n"
"buffer_load_dword v200, off, s[28:31], s39 sc1\n"
"ds_write_b128 v196, v[232:235] offset:43008\n"
"v_mfma_f32_16x16x32_bf16 v[120:123], v[80:83], v[156:159], v[120:123]\n"
"s_waitcnt lgkmcnt(1)\n"
"v_and_b32 v93, v171, v177\n"
"v_cvt_pk_bf16_f32 v253, v254, v255\n"
"v_mfma_f32_16x16x32_bf16 v[136:139], v[136:139], v[144:147], 0\n"
"v_cvt_pk_bf16_f32 v98, v98, v99\n"
"v_cvt_pk_bf16_f32 v99, v100, v101\n"
"v_mfma_f32_16x16x32_bf16 v[136:139], v[84:87], v[158:161], v[136:139]\n"
"ds_write_b64 v197, v[240:241] offset:43008\n"
"v_mfma_f32_16x16x32_bf16 v[124:127], v[124:127], v[144:147], 0\n"
"v_cvt_pk_bf16_f32 v204, v204, v205\n"
"v_cvt_pk_bf16_f32 v205, v206, v207\n"
"v_mfma_f32_16x16x32_bf16 v[124:127], v[88:91], v[156:159], v[124:127]\n"
"s_mov_b64 exec, s[44:45]\n"
"v_add_u32 v181, 0xc0, v181\n"
"s_mov_b64 exec, -1\n"
"v_mfma_f32_16x16x16_bf16 v[252:255], v[252:253], v[92:93], 0\n"
"v_cvt_pk_bf16_f32 v206, v208, v209\n"
"v_cvt_pk_bf16_f32 v207, v210, v211\n"
"v_mfma_f32_16x16x16_bf16 v[98:101], v[98:99], v[92:93], 0\n"
"v_max_f32 v120, v120, v136\n"
"v_max_f32 v121, v121, v137\n"
"v_mfma_f32_16x16x16_bf16 v[106:109], v[106:107], v[92:93], 0\n"
"ds_read_b64 v[92:93], v179 offset:13568\n"
"ds_read_b64_tr_b16 v[136:137], v182 offset:72\n"
"v_mfma_f32_16x16x32_bf16 v[212:215], a[8:11], v[32:35], 0\n"
"s_waitcnt lgkmcnt(2)\n"
"v_cvt_pk_bf16_f32 v252, v252, v253\n"
"v_cvt_pk_bf16_f32 v253, v254, v255\n"
"v_mfma_f32_16x16x32_bf16 v[212:215], a[20:23], v[36:39], v[212:215]\n"
"s_barrier\n"
"v_mfma_f32_16x16x32_bf16 v[212:215], a[32:35], v[40:43], v[212:215]\n"
"ds_write_b128 v202, a[144:147]\n"
"v_mfma_f32_16x16x32_bf16 v[212:215], a[44:47], v[44:47], v[212:215]\n"
"v_cvt_pk_bf16_f32 v254, v98, v99\n"
"v_cvt_pk_bf16_f32 v255, v100, v101\n"
"v_mfma_f32_16x16x32_bf16 v[212:215], a[56:59], v[48:51], v[212:215]\n"
"v_cvt_pk_bf16_f32 v106, v106, v107\n"
"v_cvt_pk_bf16_f32 v107, v108, v109\n"
"v_mfma_f32_16x16x32_bf16 v[212:215], a[68:71], v[52:55], v[212:215]\n"
"s_waitcnt lgkmcnt(2)\n"
"ds_write_b64 v203, a[148:149]\n"
"v_mfma_f32_16x16x32_bf16 v[140:143], v[140:143], v[144:147], 0\n"
"v_min_u32 v252, -2, v252\n"
"v_min_u32 v253, -2, v253\n"
"v_mfma_f32_16x16x32_bf16 v[140:143], v[92:95], v[158:161], v[140:143]\n"
"ds_read_b64 v[98:99], v178 offset:27072\n"
"ds_read_b64 v[100:101], v179 offset:27072\n"
"v_mfma_f32_16x16x32_bf16 v[208:211], a[80:83], v[32:35], 0\n"
"ds_read_b128 v[132:135], v180\n"
"v_min_u32 v254, -2, v254\n"
"v_mfma_f32_16x16x32_bf16 v[208:211], a[92:95], v[36:39], v[208:211]\n"
"ds_read_b128 v[116:119], v181\n"
"v_min_u32 v255, -2, v255\n"
"v_mfma_f32_16x16x32_bf16 v[208:211], a[104:107], v[40:43], v[208:211]\n"
"buffer_store_dwordx4 v[252:255], v192, s[28:31], s37 offen sc1\n"
"v_mfma_f32_16x16x32_bf16 v[208:211], a[116:119], v[44:47], v[208:211]\n"
"ds_read_b64 v[108:109], v181 offset:64\n"
"ds_write_b128 v202, a[204:207] offset:55296\n"
"v_mfma_f32_16x16x32_bf16 v[208:211], a[128:131], v[48:51], v[208:211]\n"
"s_waitcnt lgkmcnt(5)\n"
"v_min_u32 v106, -2, v106\n"
"v_min_u32 v107, -2, v107\n"
"v_mfma_f32_16x16x32_bf16 v[128:131], v[128:131], v[144:147], 0\n"
"s_waitcnt lgkmcnt(4)\n"
"v_max_f32 v122, v122, v138\n"
"v_max_f32 v123, v123, v139\n"
"v_mfma_f32_16x16x32_bf16 v[128:131], v[96:99], v[156:159], v[128:131]\n"
"ds_write_b128 v198, v[204:207]\n"
"v_mfma_f32_16x16x32_bf16 v[112:115], v[112:115], v[144:147], 0\n"
"v_max3_f32 v170, v120, v121, v122\n"
"v_max_f32 v124, v124, v140\n"
"v_mfma_f32_16x16x32_bf16 v[112:115], v[100:103], v[158:161], v[112:115]\n"
"s_waitcnt lgkmcnt(2)\n"
"v_max_f32 v125, v125, v141\n"
"v_max_f32 v126, v126, v142\n"
"v_mfma_f32_16x16x32_bf16 v[116:119], v[116:119], v[144:147], 0\n"
"buffer_store_dwordx2 v[106:107], v193, s[28:31], s37 offen offset:1024 sc1\n"
"v_mfma_f32_16x16x32_bf16 v[116:119], v[108:111], v[158:161], v[116:119]\n"
"s_add_u32 s37, s37, 1572864\n"
"ds_read_b64 v[106:107], v180 offset:64\n"
"ds_read_b128 v[64:67], v202 offset:43008\n"
"v_mfma_f32_16x16x32_bf16 v[208:211], a[140:143], v[52:55], v[208:211]\n"
"ds_read_b64 v[68:69], v62 offset:43008\n"
"v_max_f32 v127, v127, v143\n"
"v_mfma_f32_16x16x32_bf16 v[204:207], a[72:75], v[32:35], 0\n"
"ds_read_b64_tr_b16 v[138:139], v183 offset:72\n"
"v_max3_f32 v170, v170, v123, v124\n"
"v_mfma_f32_16x16x32_bf16 v[204:207], a[84:87], v[36:39], v[204:207]\n"
"ds_read_b64_tr_b16 v[140:141], v182 offset:840\n"
"v_max3_f32 v170, v170, v125, v126\n"
"v_mfma_f32_16x16x32_bf16 v[204:207], a[96:99], v[40:43], v[204:207]\n"
"ds_read_b64_tr_b16 v[142:143], v183 offset:840\n"
"v_max_f32 v128, v128, v112\n"
"v_mfma_f32_16x16x32_bf16 v[204:207], a[108:111], v[44:47], v[204:207]\n"
"ds_write_b64 v203, a[208:209] offset:55296\n"
"v_mfma_f32_16x16x32_bf16 v[204:207], a[120:123], v[48:51], v[204:207]\n"
"v_max_f32 v129, v129, v113\n"
"v_max_f32 v130, v130, v114\n"
"v_mfma_f32_16x16x32_bf16 v[204:207], a[132:135], v[52:55], v[204:207]\n"
"s_waitcnt lgkmcnt(6)\n"
"ds_read_b64_tr_b16 v[112:113], v182 offset:27080\n"
"v_max_f32 v131, v131, v115\n"
"v_mfma_f32_16x16x32_bf16 v[132:135], v[132:135], v[144:147], 0\n"
"s_waitcnt lgkmcnt(5)\n"
"buffer_store_dwordx4 v[64:67], v194, s[24:27], s36 offen sc1\n"
"v_mfma_f32_16x16x32_bf16 v[132:135], v[104:107], v[156:159], v[132:135]\n"
"s_nop 0\n"
"ds_read_b128 v[64:67], v192 offset:30720\n"
"v_max3_f32 v170, v170, v127, v128\n"
"v_max3_f32 v170, v170, v129, v130\n"
"v_cvt_pk_bf16_f32 v212, v212, v213\n"
"v_cvt_pk_bf16_f32 v213, v214, v215\n"
"v_mov_b64 v[252:253], 0\n"
"v_mov_b64 v[254:255], 0\n"
"v_mov_b64 v[98:99], 0\n"
"v_max_f32 v132, v132, v116\n"
"v_max_f32 v133, v133, v117\n"
"v_max_f32 v134, v134, v118\n"
"v_max_f32 v135, v135, v119\n"
"v_max3_f32 v170, v170, v131, v132\n"
"v_max3_f32 v170, v170, v133, v134\n"
"v_mov_b64 v[100:101], 0\n"
"v_mov_b64 v[106:107], 0\n"
"ds_read_b64_tr_b16 v[114:115], v183 offset:27080\n"
"ds_read_b64_tr_b16 v[116:117], v182 offset:27848\n"
"v_mov_b64 v[108:109], 0\n"
"v_max_f32 v171, v170, v135\n"
"v_max_f32 v170, v170, v135\n"
"v_cvt_pk_bf16_f32 v214, v208, v209\n"
"v_cvt_pk_bf16_f32 v215, v210, v211\n"
"v_mfma_f32_16x16x32_bf16 v[208:211], a[76:79], v[32:35], 0\n"
"s_waitcnt lgkmcnt(2)\n"
"ds_read_b64_tr_b16 v[118:119], v183 offset:27848\n"
"buffer_store_dwordx2 v[68:69], v195, s[24:27], s36 offen sc1\n"
"v_mfma_f32_16x16x32_bf16 v[232:235], a[216:219], v[64:67], 0\n"
"s_add_u32 s36, s36, s33\n"
"ds_read_b128 v[68:71], v192 offset:31744\n"
"ds_write_b128 v198, v[212:215] offset:64\n"
"v_mfma_f32_16x16x32_bf16 v[236:239], a[220:223], v[64:67], 0\n"
"v_permlane16_swap_b32 v170, v171\n"
"v_mfma_f32_16x16x32_bf16 v[240:243], a[224:227], v[64:67], 0\n"
"v_max_f32 v170, v170, v171\n"
"v_mov_b32 v171, v170\n"
"v_mfma_f32_16x16x32_bf16 v[208:211], a[88:91], v[36:39], v[208:211]\n"
"ds_read_b128 v[64:67], v192 offset:32768\n"
"v_permlane32_swap_b32 v170, v171\n"
"v_max_f32 v170, v170, v171\n"
"v_mul_f32 v170, 0xbe553b95, v170\n"
"v_pk_fma_f32 v[120:121], v[120:121], s[70:71], v[170:171] op_sel_hi:[1,0,0]\n"
"v_pk_fma_f32 v[122:123], v[122:123], s[70:71], v[170:171] op_sel_hi:[1,0,0]\n"
"v_pk_fma_f32 v[124:125], v[124:125], s[70:71], v[170:171] op_sel_hi:[1,0,0]\n"
"v_pk_fma_f32 v[126:127], v[126:127], s[70:71], v[170:171] op_sel_hi:[1,0,0]\n"
"v_pk_fma_f32 v[128:129], v[128:129], s[70:71], v[170:171] op_sel_hi:[1,0,0]\n"
"v_pk_fma_f32 v[130:131], v[130:131], s[70:71], v[170:171] op_sel_hi:[1,0,0]\n"
"v_pk_fma_f32 v[132:133], v[132:133], s[70:71], v[170:171] op_sel_hi:[1,0,0]\n"
"v_pk_fma_f32 v[134:135], v[134:135], s[70:71], v[170:171] op_sel_hi:[1,0,0]\n"
"v_mfma_f32_16x16x32_bf16 v[208:211], a[100:103], v[40:43], v[208:211]\n"
"s_waitcnt lgkmcnt(2)\n"
"v_exp_f32 v120, v120\n"
"v_mfma_f32_16x16x32_bf16 v[208:211], a[112:115], v[44:47], v[208:211]\n"
"v_exp_f32 v121, v121\n"
"v_mfma_f32_16x16x32_bf16 v[232:235], a[228:231], v[68:71], v[232:235]\n"
"v_exp_f32 v122, v122\n"
"v_mfma_f32_16x16x32_bf16 v[236:239], a[232:235], v[68:71], v[236:239]\n"
"s_waitcnt lgkmcnt(0)\n"
"v_exp_f32 v123, v123\n"
"v_mfma_f32_16x16x32_bf16 v[236:239], a[244:247], v[64:67], v[236:239]\n"
"v_cvt_pk_bf16_f32 v82, v120, v122\n"
"v_cvt_pk_bf16_f32 v83, v121, v123\n"
"v_mfma_f32_16x16x32_bf16 v[240:243], a[236:239], v[68:71], v[240:243]\n"
"ds_read_b128 v[120:123], v178 offset:13504\n"
"ds_read_b128 v[68:71], v192 offset:33792\n"
"v_mfma_f32_16x16x32_bf16 v[240:243], a[248:251], v[64:67], v[240:243]\n"
"v_exp_f32 v124, v124\n"
"v_mfma_f32_16x16x32_bf16 v[232:235], a[240:243], v[64:67], v[232:235]\n"
"ds_read_b128 v[64:67], v192 offset:34816\n"
"v_exp_f32 v125, v125\n"
"v_mfma_f32_16x16x32_bf16 v[208:211], a[124:127], v[48:51], v[208:211]\n"
"v_exp_f32 v126, v126\n"
"v_mfma_f32_16x16x32_bf16 v[208:211], a[136:139], v[52:55], v[208:211]\n"
"v_exp_f32 v127, v127\n"
"v_cvt_pk_bf16_f32 v84, v124, v126\n"
"v_cvt_pk_bf16_f32 v85, v125, v127\n"
"v_cvt_pk_bf16_f32 v204, v204, v205\n"
"v_cvt_pk_bf16_f32 v205, v206, v207\n"
"v_smfmac_f32_16x16x64_bf16 v[252:255], v[82:85], v[216:223], v63\n"
"ds_read_b128 v[124:127], v178 offset:27008\n"
"v_smfmac_f32_16x16x64_bf16 v[98:101], v[82:85], v[72:79], v63\n"
"ds_read_b64_tr_b16 v[216:217], v182 offset:27136\n"
"v_smfmac_f32_16x16x64_bf16 v[106:109], v[82:85], v[136:143], v63\n"
"s_waitcnt vmcnt(4) lgkmcnt(3)\n"
"v_add3_u32 v200, v200, s50, -10\n"
"v_mfma_f32_16x16x32_bf16 v[82:85], v[172:175], v[82:85], 0\n"
"v_cmp_lt_u32 vcc, 64, v200\n"
"v_exp_f32 v128, v128\n"
"v_mfma_f32_16x16x32_bf16 v[232:235], a[252:255], v[68:71], v[232:235]\n"
"s_waitcnt lgkmcnt(2)\n"
"ds_read_b128 v[136:139], v179 offset:13504\n"
"ds_read_b128 v[140:143], v179 offset:27008\n"
"v_mfma_f32_16x16x32_bf16 v[232:235], v[8:11], v[64:67], v[232:235]\n"
"s_cbranch_vccnz .Lp9%=\n"
".Lr9%=:\n"
"v_mfma_f32_16x16x32_bf16 v[236:239], v[0:3], v[68:71], v[236:239]\n"
"buffer_load_dwordx4 a[150:153], v192, s[28:31], s13 offen sc1\n"
"ds_read_b64_tr_b16 v[218:219], v183 offset:27136\n"
"v_mfma_f32_16x16x32_bf16 v[236:239], v[12:15], v[64:67], v[236:239]\n"
"ds_read_b64_tr_b16 v[220:221], v182 offset:27904\n"
"ds_read_b64_tr_b16 v[222:223], v183 offset:27904\n"
"v_mfma_f32_16x16x32_bf16 v[240:243], v[4:7], v[68:71], v[240:243]\n"
"v_exp_f32 v129, v129\n"
"v_mfma_f32_16x16x32_bf16 v[240:243], v[16:19], v[64:67], v[240:243]\n"
"ds_read_b128 v[68:71], v192 offset:35840\n"
"ds_read_b64_tr_b16 v[72:73], v182 offset:27144\n"
"v_exp_f32 v130, v130\n"
"buffer_load_dword v201, off, s[28:31], s39 sc1\n"
"ds_read_b64_tr_b16 v[74:75], v183 offset:27144\n"
"v_exp_f32 v131, v131\n"
"ds_read_b64_tr_b16 v[76:77], v182 offset:27912\n"
"buffer_load_dwordx2 a[154:155], v193, s[28:31], s13 offen offset:1024 sc1\n"
"ds_read_b64_tr_b16 v[78:79], v183 offset:27912\n"
"v_cvt_pk_bf16_f32 v90, v128, v130\n"
"v_cvt_pk_bf16_f32 v91, v129, v131\n"
"ds_read_b128 v[128:131], v180\n"
"v_exp_f32 v132, v132\n"
"v_exp_f32 v133, v133\n"
"v_exp_f32 v134, v134\n"
"v_exp_f32 v135, v135\n"
"v_cvt_pk_bf16_f32 v92, v132, v134\n"
"v_cvt_pk_bf16_f32 v93, v133, v135\n"
"v_cvt_pk_bf16_f32 v206, v208, v209\n"
"v_cvt_pk_bf16_f32 v207, v210, v211\n"
"v_smfmac_f32_16x16x64_bf16 v[106:109], v[90:93], v[112:119], v63\n"
"s_waitcnt lgkmcnt(5)\n"
"buffer_load_dwordx4 a[156:159], v192, s[28:31], s14 offen sc1\n"
"v_smfmac_f32_16x16x64_bf16 v[252:255], v[90:93], v[216:223], v63\n"
"ds_read_b128 v[112:115], v181\n"
"v_mfma_f32_16x16x32_bf16 v[232:235], v[20:23], v[68:71], v[232:235]\n"
"ds_write_b128 v198, v[204:207] offset:128\n"
"v_mfma_f32_16x16x32_bf16 v[236:239], v[24:27], v[68:71], v[236:239]\n"
"s_waitcnt lgkmcnt(3)\n"
"v_cvt_pk_bf16_f32 v106, v106, v107\n"
"v_cvt_pk_bf16_f32 v107, v108, v109\n"
"v_smfmac_f32_16x16x64_bf16 v[98:101], v[90:93], v[72:79], v63\n"
"buffer_load_dwordx2 a[160:161], v193, s[28:31], s14 offen offset:1024 sc1\n"
"v_mfma_f32_16x16x32_bf16 v[90:93], v[172:175], v[90:93], v[82:85]\n"
"v_cvt_pk_bf16_f32 v252, v252, v253\n"
"v_cvt_pk_bf16_f32 v253, v254, v255\n"
"v_mfma_f32_16x16x32_bf16 v[240:243], v[28:31], v[68:71], v[240:243]\n"
"ds_read_b64 v[82:83], v178 offset:13568\n"
"ds_read_b64 v[84:85], v179 offset:13568\n"
"s_nop 0\n"
"v_cvt_pk_bf16_f32 v98, v98, v99\n"
"s_nop 0\n"
"v_rcp_f32 v171, v90\n"
"ds_read_b64 v[90:91], v178 offset:27072\n"
"v_cvt_pk_bf16_f32 v171, v171, v171\n"
"v_and_b32 v92, v171, v176\n"
"v_and_b32 v93, v171, v177\n"
"v_cvt_pk_bf16_f32 v99, v100, v101\n"
"v_cvt_pk_bf16_f32 v232, v232, v233\n"
"v_mfma_f32_16x16x16_bf16 v[252:255], v[252:253], v[92:93], 0\n"
"s_waitcnt lgkmcnt(3)\n"
"v_cvt_pk_bf16_f32 v233, v234, v235\n"
"v_cvt_pk_bf16_f32 v240, v240, v241\n"
"v_mfma_f32_16x16x16_bf16 v[98:101], v[98:99], v[92:93], 0\n"
"s_barrier\n"
"v_mfma_f32_16x16x16_bf16 v[106:109], v[106:107], v[92:93], 0\n"
"ds_read_b64 v[92:93], v179 offset:27072\n"
"ds_read_b128 v[132:135], v180 offset:13504\n"
"v_cvt_pk_bf16_f32 v252, v252, v253\n"
"v_cvt_pk_bf16_f32 v253, v254, v255\n"
"v_min_u32 v252, -2, v252\n"
"v_min_u32 v253, -2, v253\n"
"v_cvt_pk_bf16_f32 v254, v98, v99\n"
"v_cvt_pk_bf16_f32 v255, v100, v101\n"
"v_cvt_pk_bf16_f32 v106, v106, v107\n"
"v_cvt_pk_bf16_f32 v107, v108, v109\n"
"v_min_u32 v254, -2, v254\n"
"v_min_u32 v255, -2, v255\n"
"v_min_u32 v106, -2, v106\n"
"v_min_u32 v107, -2, v107\n"
"v_cvt_pk_bf16_f32 v234, v236, v237\n"
"v_cvt_pk_bf16_f32 v235, v238, v239\n"
"v_cvt_pk_bf16_f32 v241, v242, v243\n"
"s_waitcnt lgkmcnt(2)\n"
"v_mfma_f32_16x16x32_bf16 v[120:123], v[120:123], v[148:151], 0\n"
"buffer_store_dwordx4 v[252:255], v192, s[28:31], s37 offen sc1\n"
"v_mfma_f32_16x16x32_bf16 v[120:123], v[80:83], v[160:163], v[120:123]\n"
"s_waitcnt lgkmcnt(1)\n"
"ds_read_b64 v[98:99], v180 offset:64\n"
"ds_read_b64 v[100:101], v181 offset:64\n"
"v_mfma_f32_16x16x32_bf16 v[136:139], v[136:139], v[148:151], 0\n"
"ds_read_b128 v[116:119], v181 offset:13504\n"
"v_mov_b64 v[252:253], 0\n"
"v_mfma_f32_16x16x32_bf16 v[136:139], v[84:87], v[162:165], v[136:139]\n"
"ds_read_b64 v[108:109], v181 offset:13568\n"
"v_mov_b64 v[254:255], 0\n"
"v_mfma_f32_16x16x32_bf16 v[124:127], v[124:127], v[148:151], 0\n"
"ds_read_b128 v[64:67], v192 offset:55296\n"
"v_mfma_f32_16x16x32_bf16 v[124:127], v[88:91], v[160:163], v[124:127]\n"
"ds_read_b128 v[68:71], v192 offset:56320\n"
"v_mfma_f32_16x16x32_bf16 v[140:143], v[140:143], v[148:151], 0\n"
"ds_write_b128 v196, v[232:235] offset:49152\n"
"v_mfma_f32_16x16x32_bf16 v[140:143], v[92:95], v[162:165], v[140:143]\n"
"s_waitcnt lgkmcnt(6)\n"
"v_max_f32 v120, v120, v136\n"
"v_max_f32 v121, v121, v137\n"
"v_mfma_f32_16x16x32_bf16 v[128:131], v[128:131], v[148:151], 0\n"
"s_waitcnt lgkmcnt(5)\n"
"buffer_store_dwordx2 v[106:107], v193, s[28:31], s37 offen offset:1024 sc1\n"
"v_mfma_f32_16x16x32_bf16 v[128:131], v[96:99], v[160:163], v[128:131]\n"
"s_add_u32 s37, s37, 1572864\n"
"ds_read_b64 v[106:107], v180 offset:13568\n"
"ds_read_b64_tr_b16 v[136:137], v182 offset:40584\n"
"v_mfma_f32_16x16x32_bf16 v[112:115], v[112:115], v[148:151], 0\n"
"s_waitcnt lgkmcnt(5)\n"
"v_max_f32 v122, v122, v138\n"
"v_max_f32 v123, v123, v139\n"
"v_mfma_f32_16x16x32_bf16 v[112:115], v[100:103], v[162:165], v[112:115]\n"
"s_waitcnt lgkmcnt(4)\n"
"ds_read_b64_tr_b16 v[138:139], v183 offset:40584\n"
"v_max3_f32 v170, v120, v121, v122\n"
"v_mfma_f32_16x16x32_bf16 v[116:119], v[116:119], v[148:151], 0\n"
"v_max_f32 v124, v124, v140\n"
"v_max_f32 v125, v125, v141\n"
"v_mfma_f32_16x16x32_bf16 v[116:119], v[108:111], v[162:165], v[116:119]\n"
"ds_read_b64_tr_b16 v[140:141], v182 offset:41352\n"
"v_max_f32 v126, v126, v142\n"
"v_mfma_f32_16x16x32_bf16 v[232:235], a[216:219], v[64:67], 0\n"
"s_waitcnt lgkmcnt(5)\n"
"v_max_f32 v127, v127, v143\n"
"v_max3_f32 v170, v170, v123, v124\n"
"v_mfma_f32_16x16x32_bf16 v[232:235], a[228:231], v[68:71], v[232:235]\n"
"buffer_load_dwordx4 v[52:55], v192, s[28:31], s15 offen sc1\n"
"ds_read_b64_tr_b16 v[142:143], v183 offset:41352\n"
"v_mfma_f32_16x16x32_bf16 v[236:239], a[220:223], v[64:67], 0\n"
"s_waitcnt lgkmcnt(4)\n"
"v_max3_f32 v170, v170, v125, v126\n"
"v_max_f32 v128, v128, v112\n"
"v_mfma_f32_16x16x32_bf16 v[132:135], v[132:135], v[148:151], 0\n"
"v_max_f32 v129, v129, v113\n"
"v_max_f32 v130, v130, v114\n"
"v_mfma_f32_16x16x32_bf16 v[132:135], v[104:107], v[160:163], v[132:135]\n"
"v_max_f32 v131, v131, v115\n"
"v_max3_f32 v170, v170, v127, v128\n"
"v_mfma_f32_16x16x32_bf16 v[236:239], a[232:235], v[68:71], v[236:239]\n"
"buffer_load_dwordx2 v[152:153], v193, s[28:31], s15 offen offset:1024 sc1\n"
"v_max3_f32 v170, v170, v129, v130\n"
"v_mov_b64 v[98:99], 0\n"
"v_mov_b64 v[100:101], 0\n"
"s_nop 0\n"
"v_max_f32 v132, v132, v116\n"
"v_max_f32 v133, v133, v117\n"
"v_max_f32 v134, v134, v118\n"
"v_max_f32 v135, v135, v119\n"
"v_max3_f32 v170, v170, v131, v132\n"
"v_max3_f32 v170, v170, v133, v134\n"
"v_mov_b64 v[106:107], 0\n"
"v_mov_b64 v[108:109], 0\n"
"v_max_f32 v171, v170, v135\n"
"v_max_f32 v170, v170, v135\n"
"ds_write_b64 v197, v[240:241] offset:49152\n"
"v_mfma_f32_16x16x32_bf16 v[240:243], a[224:227], v[64:67], 0\n"
"v_permlane16_swap_b32 v170, v171\n"
"v_mfma_f32_16x16x32_bf16 v[240:243], a[236:239], v[68:71], v[240:243]\n"
"ds_read_b128 v[64:67], v192 offset:57344\n"
"ds_read_b128 v[68:71], v192 offset:58368\n"
"v_max_f32 v170, v170, v171\n"
"v_mov_b32 v171, v170\n"
"s_nop 1\n"
"v_permlane32_swap_b32 v170, v171\n"
"v_max_f32 v170, v170, v171\n"
"v_mul_f32 v170, 0xbe553b95, v170\n"
"v_pk_fma_f32 v[120:121], v[120:121], s[70:71], v[170:171] op_sel_hi:[1,0,0]\n"
"v_pk_fma_f32 v[122:123], v[122:123], s[70:71], v[170:171] op_sel_hi:[1,0,0]\n"
"v_pk_fma_f32 v[124:125], v[124:125], s[70:71], v[170:171] op_sel_hi:[1,0,0]\n"
"v_pk_fma_f32 v[126:127], v[126:127], s[70:71], v[170:171] op_sel_hi:[1,0,0]\n"
"v_pk_fma_f32 v[128:129], v[128:129], s[70:71], v[170:171] op_sel_hi:[1,0,0]\n"
"v_pk_fma_f32 v[130:131], v[130:131], s[70:71], v[170:171] op_sel_hi:[1,0,0]\n"
"v_pk_fma_f32 v[132:133], v[132:133], s[70:71], v[170:171] op_sel_hi:[1,0,0]\n"
"v_pk_fma_f32 v[134:135], v[134:135], s[70:71], v[170:171] op_sel_hi:[1,0,0]\n"
"v_exp_f32 v120, v120\n"
"v_exp_f32 v121, v121\n"
"v_exp_f32 v122, v122\n"
"v_exp_f32 v123, v123\n"
"v_cvt_pk_bf16_f32 v82, v120, v122\n"
"v_cvt_pk_bf16_f32 v83, v121, v123\n"
"v_exp_f32 v124, v124\n"
"v_exp_f32 v125, v125\n"
"v_exp_f32 v126, v126\n"
"v_exp_f32 v127, v127\n"
"v_cvt_pk_bf16_f32 v84, v124, v126\n"
"v_cvt_pk_bf16_f32 v85, v125, v127\n"
"v_exp_f32 v128, v128\n"
"s_nop 0\n"
"v_smfmac_f32_16x16x64_bf16 v[252:255], v[82:85], v[224:231], v63\n"
"s_waitcnt lgkmcnt(1)\n"
"ds_read_b64_tr_b16 v[224:225], v182 offset:40640\n"
"v_smfmac_f32_16x16x64_bf16 v[98:101], v[82:85], v[244:251], v63\n"
"ds_read_b64_tr_b16 v[226:227], v183 offset:40640\n"
"v_smfmac_f32_16x16x64_bf16 v[106:109], v[82:85], a[168:175], v63\n"
"ds_read_b64_tr_b16 v[228:229], v182 offset:41408\n"
"v_mfma_f32_16x16x32_bf16 v[82:85], v[172:175], v[82:85], 0\n"
"ds_read_b64_tr_b16 v[230:231], v183 offset:41408\n"
"ds_read_b64_tr_b16 v[244:245], v182 offset:40648\n"
"v_mfma_f32_16x16x32_bf16 v[232:235], a[240:243], v[64:67], v[232:235]\n"
"s_waitcnt lgkmcnt(5)\n"
"v_exp_f32 v129, v129\n"
"v_mfma_f32_16x16x32_bf16 v[232:235], a[252:255], v[68:71], v[232:235]\n"
"ds_read_b64_tr_b16 v[246:247], v183 offset:40648\n"
"ds_read_b64_tr_b16 v[248:249], v182 offset:41416\n"
"v_mfma_f32_16x16x32_bf16 v[236:239], a[244:247], v[64:67], v[236:239]\n"
"v_exp_f32 v130, v130\n"
"v_mfma_f32_16x16x32_bf16 v[236:239], v[0:3], v[68:71], v[236:239]\n"
"ds_read_b64_tr_b16 v[250:251], v183 offset:41416\n"
"v_cvt_pk_bf16_f32 v90, v128, v130\n"
"v_mfma_f32_16x16x32_bf16 v[240:243], a[248:251], v[64:67], v[240:243]\n"
"ds_read_b128 v[64:67], v192 offset:59392\n"
"v_exp_f32 v131, v131\n"
"v_mfma_f32_16x16x32_bf16 v[240:243], v[4:7], v[68:71], v[240:243]\n"
"ds_read_b128 v[68:71], v192 offset:60416\n"
"v_cvt_pk_bf16_f32 v91, v129, v131\n"
"v_exp_f32 v132, v132\n"
"v_exp_f32 v133, v133\n"
"v_exp_f32 v134, v134\n"
"v_exp_f32 v135, v135\n"
"v_cvt_pk_bf16_f32 v92, v132, v134\n"
"v_cvt_pk_bf16_f32 v93, v133, v135\n"
"s_nop 1\n"
"v_smfmac_f32_16x16x64_bf16 v[106:109], v[90:93], v[136:143], v63\n"
"s_waitcnt vmcnt(7) lgkmcnt(6)\n"
"v_smfmac_f32_16x16x64_bf16 v[252:255], v[90:93], v[224:231], v63\n"
"s_waitcnt lgkmcnt(2)\n"
"v_add3_u32 v201, v201, s50, -11\n"
"v_smfmac_f32_16x16x64_bf16 v[98:101], v[90:93], v[244:251], v63\n"
"s_waitcnt lgkmcnt(1)\n"
"v_cmp_lt_u32 vcc, 64, v201\n"
"v_mfma_f32_16x16x32_bf16 v[90:93], v[172:175], v[90:93], v[82:85]\n"
"s_waitcnt lgkmcnt(0)\n"
"ds_write_b128 v202, a[150:153] offset:24576\n"
"v_mfma_f32_16x16x32_bf16 v[232:235], v[8:11], v[64:67], v[232:235]\n"
"s_nop 0\n"
"s_cbranch_vccnz .Lp10%=\n"
".Lr10%=:\n"
"v_mfma_f32_16x16x32_bf16 v[232:235], v[20:23], v[68:71], v[232:235]\n"
"s_barrier\n"
"v_mfma_f32_16x16x32_bf16 v[240:243], v[16:19], v[64:67], v[240:243]\n"
"s_waitcnt vmcnt(4)\n"
"buffer_load_dwordx4 a[162:165], v192, s[28:31], s16 offen sc1\n"
"ds_write_b64 v203, a[154:155] offset:24576\n"
"v_mfma_f32_16x16x32_bf16 v[240:243], v[28:31], v[68:71], v[240:243]\n"
"v_rcp_f32 v171, v90\n"
"v_mfma_f32_16x16x32_bf16 v[236:239], v[12:15], v[64:67], v[236:239]\n"
"ds_write_b128 v202, a[156:159] offset:30720\n"
"v_mfma_f32_16x16x32_bf16 v[236:239], v[24:27], v[68:71], v[236:239]\n"
"buffer_load_dwordx2 a[166:167], v193, s[28:31], s16 offen offset:1024 sc1\n"
"v_cvt_pk_bf16_f32 v171, v171, v171\n"
"v_cvt_pk_bf16_f32 v252, v252, v253\n"
"v_and_b32 v92, v171, v176\n"
"v_and_b32 v93, v171, v177\n"
"ds_write_b64 v203, a[160:161] offset:30720\n"
"v_cvt_pk_bf16_f32 v253, v254, v255\n"
"v_cvt_pk_bf16_f32 v98, v98, v99\n"
"v_cvt_pk_bf16_f32 v99, v100, v101\n"
"v_mfma_f32_16x16x16_bf16 v[252:255], v[252:253], v[92:93], 0\n"
"ds_read_b128 v[44:47], v192\n"
"ds_read_b128 v[48:51], v192 offset:1024\n"
"v_mfma_f32_16x16x16_bf16 v[98:101], v[98:99], v[92:93], 0\n"
"ds_read_b128 a[0:3], v192 offset:2048\n"
"v_cvt_pk_bf16_f32 v106, v106, v107\n"
"v_cvt_pk_bf16_f32 v107, v108, v109\n"
"v_cvt_pk_bf16_f32 v232, v232, v233\n"
"v_cvt_pk_bf16_f32 v233, v234, v235\n"
"v_mfma_f32_16x16x16_bf16 v[106:109], v[106:107], v[92:93], 0\n"
"ds_read_b128 a[4:7], v192 offset:3072\n"
"ds_read_b128 a[8:11], v192 offset:4096\n"
"v_cvt_pk_bf16_f32 v252, v252, v253\n"
"v_cvt_pk_bf16_f32 v253, v254, v255\n"
"v_cvt_pk_bf16_f32 v254, v98, v99\n"
"ds_read_b128 a[12:15], v192 offset:5120\n"
"v_cvt_pk_bf16_f32 v255, v100, v101\n"
"v_min_u32 v252, -2, v252\n"
"v_cvt_pk_bf16_f32 v106, v106, v107\n"
"v_min_u32 v253, -2, v253\n"
"v_min_u32 v254, -2, v254\n"
"v_min_u32 v255, -2, v255\n"
"v_cvt_pk_bf16_f32 v107, v108, v109\n"
"v_min_u32 v106, -2, v106\n"
".Lic7%=:\n"
"s_cbranch_execz .Lib1%=\n"
"v_min_u32 v107, -2, v107\n"
"v_cvt_pk_bf16_f32 v240, v240, v241\n"
"v_cvt_pk_bf16_f32 v234, v236, v237\n"
"v_cvt_pk_bf16_f32 v235, v238, v239\n"
"v_cvt_pk_bf16_f32 v241, v242, v243\n"
"s_waitcnt lgkmcnt(5)\n"
"v_mfma_f32_16x16x32_bf16 v[204:207], a[216:219], v[44:47], 0\n"
"s_waitcnt lgkmcnt(4)\n"
"buffer_store_dwordx4 v[252:255], v192, s[28:31], s37 offen sc1\n"
"v_mfma_f32_16x16x32_bf16 v[204:207], a[228:231], v[48:51], v[204:207]\n"
"s_waitcnt lgkmcnt(3)\n"
"ds_write_b128 v196, v[232:235] offset:36864\n"
"v_mfma_f32_16x16x32_bf16 v[204:207], a[240:243], a[0:3], v[204:207]\n"
"s_waitcnt lgkmcnt(3)\n"
"v_mfma_f32_16x16x32_bf16 v[204:207], a[252:255], a[4:7], v[204:207]\n"
"s_waitcnt lgkmcnt(2)\n"
"buffer_store_dwordx2 v[106:107], v193, s[28:31], s37 offen offset:1024 sc1\n"
"v_mfma_f32_16x16x32_bf16 v[204:207], v[8:11], a[8:11], v[204:207]\n"
"s_waitcnt lgkmcnt(1)\n"
"ds_write_b64 v197, v[240:241] offset:36864\n"
"v_mfma_f32_16x16x32_bf16 v[204:207], v[20:23], a[12:15], v[204:207]\n"
"s_add_u32 s37, s37, 1572864\n"
"v_mfma_f32_16x16x32_bf16 v[208:211], a[220:223], v[44:47], 0\n"
"ds_read_b128 v[64:67], v202 offset:49152\n"
"ds_read_b64 v[68:69], v62 offset:49152\n"
"v_mfma_f32_16x16x32_bf16 v[208:211], a[232:235], v[48:51], v[208:211]\n"
"v_mfma_f32_16x16x32_bf16 v[208:211], a[244:247], a[0:3], v[208:211]\n"
"v_mfma_f32_16x16x32_bf16 v[208:211], v[0:3], a[4:7], v[208:211]\n"
"s_nop 0\n"
"v_cvt_pk_bf16_f32 v204, v204, v205\n"
"v_mfma_f32_16x16x32_bf16 v[208:211], v[12:15], a[8:11], v[208:211]\n"
"v_cvt_pk_bf16_f32 v205, v206, v207\n"
"v_mfma_f32_16x16x32_bf16 v[208:211], v[24:27], a[12:15], v[208:211]\n"
"v_mfma_f32_16x16x32_bf16 v[212:215], a[224:227], v[44:47], 0\n"
"v_mfma_f32_16x16x32_bf16 v[212:215], a[236:239], v[48:51], v[212:215]\n"
"s_waitcnt lgkmcnt(0)\n"
"buffer_store_dwordx4 v[64:67], v194, s[24:27], s36 offen sc1\n"
"v_mfma_f32_16x16x32_bf16 v[212:215], a[248:251], a[0:3], v[212:215]\n"
"s_barrier\n"
"v_mfma_f32_16x16x32_bf16 v[212:215], v[4:7], a[4:7], v[212:215]\n"
"s_waitcnt vmcnt(6)\n"
"ds_read_b128 a[24:27], v192 offset:24576\n"
"ds_read_b128 a[28:31], v192 offset:25600\n"
"v_mfma_f32_16x16x32_bf16 v[212:215], v[16:19], a[8:11], v[212:215]\n"
"ds_read_b128 a[32:35], v192 offset:26624\n"
"v_cvt_pk_bf16_f32 v206, v208, v209\n"
"v_mfma_f32_16x16x32_bf16 v[212:215], v[28:31], a[12:15], v[212:215]\n"
"buffer_load_dwordx4 v[144:147], v192, s[28:31], s17 offen sc1\n"
"ds_read_b128 a[36:39], v192 offset:27648\n"
"v_cvt_pk_bf16_f32 v207, v210, v211\n"
"v_max3_u32 v200, v52, v53, v54\n"
"ds_read_b128 a[40:43], v192 offset:28672\n"
"s_waitcnt vmcnt(6)\n"
"ds_read_b128 a[44:47], v192 offset:29696\n"
"s_nop 0\n"
"v_cvt_pk_bf16_f32 v212, v212, v213\n"
"v_cvt_pk_bf16_f32 v213, v214, v215\n"
"v_max3_u32 v200, v200, v55, v152\n"
"v_max_u32 v200, v200, v153\n"
"v_cmp_eq_u32 vcc, -1, v200\n"
"ds_read_b128 a[48:51], v192 offset:30720\n"
"buffer_load_dwordx2 v[154:155], v193, s[28:31], s17 offen offset:1024 sc1\n"
"ds_read_b128 a[52:55], v192 offset:31744\n"
"s_nop 1\n"
"s_cbranch_vccnz .Lp11%=\n"
".Lr11%=:\n"
"ds_write_b128 v202, v[52:55] offset:18432\n"
"s_waitcnt lgkmcnt(8)\n"
"v_mfma_f32_16x16x32_bf16 v[232:235], a[216:219], a[24:27], 0\n"
"s_waitcnt lgkmcnt(7)\n"
"v_mfma_f32_16x16x32_bf16 v[232:235], a[228:231], a[28:31], v[232:235]\n"
"s_waitcnt lgkmcnt(6)\n"
"ds_write_b64 v203, v[152:153] offset:18432\n"
"v_mfma_f32_16x16x32_bf16 v[232:235], a[240:243], a[32:35], v[232:235]\n"
"s_waitcnt lgkmcnt(6)\n"
"buffer_store_dwordx2 v[68:69], v195, s[24:27], s36 offen sc1\n"
"v_mfma_f32_16x16x32_bf16 v[232:235], a[252:255], a[36:39], v[232:235]\n"
"s_waitcnt lgkmcnt(3)\n"
"ds_read_b128 v[64:67], v192 offset:32768\n"
"ds_read_b128 v[68:71], v192 offset:33792\n"
"v_mfma_f32_16x16x32_bf16 v[232:235], v[8:11], a[40:43], v[232:235]\n"
"s_add_u32 s36, s36, s33\n"
"ds_read_b128 v[44:47], v192 offset:34816\n"
"v_mfma_f32_16x16x32_bf16 v[232:235], v[20:23], a[44:47], v[232:235]\n"
"ds_read_b128 v[48:51], v192 offset:35840\n"
"v_mfma_f32_16x16x32_bf16 v[236:239], a[220:223], a[24:27], 0\n"
"ds_write_b128 v196, v[204:207] offset:12288\n"
"v_mfma_f32_16x16x32_bf16 v[236:239], a[232:235], a[28:31], v[236:239]\n"
"v_mfma_f32_16x16x32_bf16 v[236:239], a[244:247], a[32:35], v[236:239]\n"
"ds_write_b64 v197, v[212:213] offset:12288\n"
"v_mfma_f32_16x16x32_bf16 v[236:239], v[0:3], a[36:39], v[236:239]\n"
"s_nop 0\n"
"v_cvt_pk_bf16_f32 v232, v232, v233\n"
"v_mfma_f32_16x16x32_bf16 v[236:239], v[12:15], a[40:43], v[236:239]\n"
"s_waitcnt vmcnt(7)\n"
"ds_write_b128 v202, a[162:165] offset:49152\n"
"v_mfma_f32_16x16x32_bf16 v[236:239], v[24:27], a[44:47], v[236:239]\n"
"v_cvt_pk_bf16_f32 v233, v234, v235\n"
"v_mfma_f32_16x16x32_bf16 v[240:243], a[224:227], a[24:27], 0\n"
"ds_read_b128 a[16:19], v202 offset:36864\n"
"v_mfma_f32_16x16x32_bf16 v[240:243], a[236:239], a[28:31], v[240:243]\n"
"s_waitcnt vmcnt(6)\n"
"ds_read_b64 a[20:21], v62 offset:36864\n"
"v_mfma_f32_16x16x32_bf16 v[240:243], a[248:251], a[32:35], v[240:243]\n"
"ds_write_b64 v203, a[166:167] offset:49152\n"
"v_mfma_f32_16x16x32_bf16 v[240:243], v[4:7], a[36:39], v[240:243]\n"
"v_cvt_pk_bf16_f32 v234, v236, v237\n"
"v_cvt_pk_bf16_f32 v235, v238, v239\n"
"v_mfma_f32_16x16x32_bf16 v[240:243], v[16:19], a[40:43], v[240:243]\n"
"ds_write_b128 v196, v[232:235] offset:43008\n"
"v_mfma_f32_16x16x32_bf16 v[240:243], v[28:31], a[44:47], v[240:243]\n"
"v_mfma_f32_16x16x32_bf16 v[32:35], a[216:219], a[48:51], 0\n"
"s_waitcnt lgkmcnt(3)\n"
"v_mfma_f32_16x16x32_bf16 v[32:35], a[228:231], a[52:55], v[32:35]\n"
"s_waitcnt lgkmcnt(2)\n"
"buffer_store_dwordx4 a[16:19], v194, s[24:27], s36 offen sc1\n"
"v_mfma_f32_16x16x32_bf16 v[32:35], a[240:243], v[64:67], v[32:35]\n"
"s_barrier\n"
"v_mfma_f32_16x16x32_bf16 v[32:35], a[252:255], v[68:71], v[32:35]\n"
"ds_read_b128 a[8:11], v192 offset:18432\n"
"ds_read_b128 a[12:15], v192 offset:19456\n"
"v_mfma_f32_16x16x32_bf16 v[32:35], v[8:11], v[44:47], v[32:35]\n"
"ds_read_b128 a[16:19], v192 offset:20480\n"
"v_cvt_pk_bf16_f32 v240, v240, v241\n"
"v_mfma_f32_16x16x32_bf16 v[32:35], v[20:23], v[48:51], v[32:35]\n"
"ds_read_b128 a[24:27], v192 offset:22528\n"
"buffer_store_dwordx2 a[20:21], v195, s[24:27], s36 offen sc1\n"
"v_mfma_f32_16x16x32_bf16 v[36:39], a[220:223], a[48:51], 0\n"
"s_add_u32 s36, s36, s33\n"
"ds_read_b128 a[20:23], v192 offset:21504\n"
"v_cvt_pk_bf16_f32 v241, v242, v243\n"
"v_mfma_f32_16x16x32_bf16 v[36:39], a[232:235], a[52:55], v[36:39]\n"
"s_add_u32 s8, s8, s36\n"
"ds_read_b128 a[28:31], v192 offset:23552\n"
"v_cvt_pk_bf16_f32 v32, v32, v33\n"
"v_mfma_f32_16x16x32_bf16 v[36:39], a[244:247], v[64:67], v[36:39]\n"
"s_add_u32 s9, s9, s36\n"
"ds_write_b64 v197, v[240:241] offset:43008\n"
"v_mfma_f32_16x16x32_bf16 v[36:39], v[0:3], v[68:71], v[36:39]\n"
"s_waitcnt lgkmcnt(6)\n"
"v_cvt_pk_bf16_f32 v33, v34, v35\n"
"s_add_u32 s10, s10, s36\n"
"v_mfma_f32_16x16x32_bf16 v[36:39], v[12:15], v[44:47], v[36:39]\n"
"s_add_u32 s11, s11, s36\n"
"ds_read_b128 a[0:3], v202 offset:12288\n"
"ds_read_b64 a[4:5], v62 offset:12288\n"
"v_mfma_f32_16x16x32_bf16 v[36:39], v[24:27], v[48:51], v[36:39]\n"
"s_add_u32 s20, s20, s36\n"
"s_add_u32 s21, s21, s36\n"
"s_add_u32 s22, s22, s36\n"
"v_mfma_f32_16x16x32_bf16 v[40:43], a[224:227], a[48:51], 0\n"
"v_mfma_f32_16x16x32_bf16 v[40:43], a[236:239], a[52:55], v[40:43]\n"
"v_mfma_f32_16x16x32_bf16 v[40:43], a[248:251], v[64:67], v[40:43]\n"
"v_mfma_f32_16x16x32_bf16 v[40:43], v[4:7], v[68:71], v[40:43]\n"
"s_nop 0\n"
"v_cvt_pk_bf16_f32 v34, v36, v37\n"
"v_mfma_f32_16x16x32_bf16 v[40:43], v[16:19], v[44:47], v[40:43]\n"
"v_cvt_pk_bf16_f32 v35, v38, v39\n"
"v_mfma_f32_16x16x32_bf16 v[40:43], v[28:31], v[48:51], v[40:43]\n"
"s_waitcnt lgkmcnt(0)\n"
"ds_write_b128 v196, v[32:35] offset:6144\n"
"v_mfma_f32_16x16x32_bf16 v[204:207], a[216:219], a[8:11], 0\n"
"buffer_store_dwordx4 a[0:3], v194, s[24:27], s8 offen sc1\n"
"v_mfma_f32_16x16x32_bf16 v[204:207], a[228:231], a[12:15], v[204:207]\n"
"v_mfma_f32_16x16x32_bf16 v[204:207], a[240:243], a[16:19], v[204:207]\n"
"v_mfma_f32_16x16x32_bf16 v[204:207], a[252:255], a[20:23], v[204:207]\n"
"s_nop 0\n"
"v_cvt_pk_bf16_f32 v40, v40, v41\n"
"v_mfma_f32_16x16x32_bf16 v[204:207], v[8:11], a[24:27], v[204:207]\n"
"buffer_store_dwordx2 a[4:5], v195, s[24:27], s8 offen sc1\n"
"v_mfma_f32_16x16x32_bf16 v[204:207], v[20:23], a[28:31], v[204:207]\n"
"v_cvt_pk_bf16_f32 v41, v42, v43\n"
"v_mfma_f32_16x16x32_bf16 v[208:211], a[220:223], a[8:11], 0\n"
"ds_write_b64 v197, v[40:41] offset:6144\n"
"v_mfma_f32_16x16x32_bf16 v[208:211], a[232:235], a[12:15], v[208:211]\n"
"v_mfma_f32_16x16x32_bf16 v[208:211], a[244:247], a[16:19], v[208:211]\n"
"s_waitcnt vmcnt(6)\n"
"v_mfma_f32_16x16x32_bf16 v[208:211], v[0:3], a[20:23], v[208:211]\n"
"v_max3_u32 v200, v144, v145, v146\n"
"v_cvt_pk_bf16_f32 v204, v204, v205\n"
"v_mfma_f32_16x16x32_bf16 v[208:211], v[12:15], a[24:27], v[208:211]\n"
"s_waitcnt vmcnt(5)\n"
"v_cvt_pk_bf16_f32 v205, v206, v207\n"
"v_max3_u32 v200, v200, v147, v154\n"
"v_mfma_f32_16x16x32_bf16 v[208:211], v[24:27], a[28:31], v[208:211]\n"
"v_max_u32 v200, v200, v155\n"
"v_cmp_eq_u32 vcc, -1, v200\n"
"v_mfma_f32_16x16x32_bf16 v[212:215], a[224:227], a[8:11], 0\n"
"v_mfma_f32_16x16x32_bf16 v[212:215], a[236:239], a[12:15], v[212:215]\n"
"s_waitcnt lgkmcnt(0)\n"
"s_barrier\n"
"v_mfma_f32_16x16x32_bf16 v[212:215], a[248:251], a[16:19], v[212:215]\n"
"s_cbranch_vccnz .Lp12%=\n"
".Lr12%=:\n"
"v_mfma_f32_16x16x32_bf16 v[212:215], v[4:7], a[20:23], v[212:215]\n"
"ds_write_b128 v202, v[144:147] offset:30720\n"
"v_mfma_f32_16x16x32_bf16 v[212:215], v[16:19], a[24:27], v[212:215]\n"
"v_cvt_pk_bf16_f32 v206, v208, v209\n"
"v_cvt_pk_bf16_f32 v207, v210, v211\n"
"v_mfma_f32_16x16x32_bf16 v[212:215], v[28:31], a[28:31], v[212:215]\n"
"ds_write_b64 v203, v[154:155] offset:30720\n"
"ds_read_b128 a[48:51], v192 offset:49152\n"
"ds_read_b128 a[52:55], v192 offset:50176\n"
"s_nop 4\n"
"v_cvt_pk_bf16_f32 v212, v212, v213\n"
"buffer_load_dwordx4 v[56:59], v192, s[28:31], s18 offen sc1\n"
"ds_read_b128 v[64:67], v192 offset:51200\n"
"ds_read_b128 v[68:71], v192 offset:52224\n"
"v_cvt_pk_bf16_f32 v213, v214, v215\n"
"ds_read_b128 v[44:47], v192 offset:53248\n"
"ds_read_b128 v[48:51], v192 offset:54272\n"
"ds_read_b128 a[32:35], v202 offset:43008\n"
"buffer_load_dwordx2 v[60:61], v193, s[28:31], s18 offen offset:1024 sc1\n"
"ds_write_b128 v196, v[204:207] offset:55296\n"
"s_waitcnt lgkmcnt(7)\n"
"v_mfma_f32_16x16x32_bf16 v[232:235], a[216:219], a[48:51], 0\n"
"s_waitcnt lgkmcnt(6)\n"
"v_mfma_f32_16x16x32_bf16 v[232:235], a[228:231], a[52:55], v[232:235]\n"
"s_waitcnt lgkmcnt(5)\n"
"ds_read_b64 a[36:37], v62 offset:43008\n"
"ds_write_b64 v197, v[212:213] offset:55296\n"
"v_mfma_f32_16x16x32_bf16 v[232:235], a[240:243], v[64:67], v[232:235]\n"
"s_waitcnt lgkmcnt(6)\n"
"v_mfma_f32_16x16x32_bf16 v[232:235], a[252:255], v[68:71], v[232:235]\n"
"s_waitcnt lgkmcnt(5)\n"
"ds_read_b128 a[40:43], v202 offset:6144\n"
"ds_read_b64 a[44:45], v62 offset:6144\n"
"v_mfma_f32_16x16x32_bf16 v[232:235], v[8:11], v[44:47], v[232:235]\n"
"s_waitcnt lgkmcnt(5)\n"
"v_mfma_f32_16x16x32_bf16 v[232:235], v[20:23], v[48:51], v[232:235]\n"
"buffer_store_dwordx4 a[32:35], v194, s[24:27], s9 offen sc1\n"
"v_mfma_f32_16x16x32_bf16 v[236:239], a[220:223], a[48:51], 0\n"
"s_waitcnt lgkmcnt(3)\n"
"v_mfma_f32_16x16x32_bf16 v[236:239], a[232:235], a[52:55], v[236:239]\n"
"s_waitcnt lgkmcnt(2)\n"
"v_mfma_f32_16x16x32_bf16 v[236:239], a[244:247], v[64:67], v[236:239]\n"
"s_barrier\n"
"v_mfma_f32_16x16x32_bf16 v[236:239], v[0:3], v[68:71], v[236:239]\n"
"s_waitcnt lgkmcnt(1)\n"
"ds_read_b128 a[8:11], v192 offset:30720\n"
"ds_read_b128 a[12:15], v192 offset:31744\n"
"v_mfma_f32_16x16x32_bf16 v[236:239], v[12:15], v[44:47], v[236:239]\n"
"s_waitcnt lgkmcnt(2)\n"
"ds_read_b128 a[16:19], v192 offset:32768\n"
"buffer_store_dwordx2 a[36:37], v195, s[24:27], s9 offen sc1\n"
"v_mfma_f32_16x16x32_bf16 v[236:239], v[24:27], v[48:51], v[236:239]\n"
"ds_read_b128 a[20:23], v192 offset:33792\n"
"v_cvt_pk_bf16_f32 v232, v232, v233\n"
"v_mfma_f32_16x16x32_bf16 v[240:243], a[224:227], a[48:51], 0\n"
"ds_read_b128 a[24:27], v192 offset:34816\n"
"v_cvt_pk_bf16_f32 v233, v234, v235\n"
"v_mfma_f32_16x16x32_bf16 v[240:243], a[236:239], a[52:55], v[240:243]\n"
"ds_read_b128 a[28:31], v192 offset:35840\n"
"v_mfma_f32_16x16x32_bf16 v[240:243], a[248:251], v[64:67], v[240:243]\n"
"ds_read_b128 a[0:3], v202 offset:55296\n"
"buffer_store_dwordx4 a[40:43], v194, s[24:27], s10 offen sc1\n"
"v_mfma_f32_16x16x32_bf16 v[240:243], v[4:7], v[68:71], v[240:243]\n"
"s_waitcnt lgkmcnt(6)\n"
"ds_read_b64 a[4:5], v62 offset:55296\n"
"v_cvt_pk_bf16_f32 v234, v236, v237\n"
"v_mfma_f32_16x16x32_bf16 v[240:243], v[16:19], v[44:47], v[240:243]\n"
"v_cvt_pk_bf16_f32 v235, v238, v239\n"
"v_mfma_f32_16x16x32_bf16 v[240:243], v[28:31], v[48:51], v[240:243]\n"
"ds_write_b128 v196, v[232:235]\n"
"v_mfma_f32_16x16x32_bf16 v[32:35], a[216:219], a[8:11], 0\n"
"s_waitcnt lgkmcnt(7)\n"
"buffer_store_dwordx2 a[44:45], v195, s[24:27], s10 offen sc1\n"
"v_mfma_f32_16x16x32_bf16 v[32:35], a[228:231], a[12:15], v[32:35]\n"
"s_waitcnt lgkmcnt(2)\n"
"v_mfma_f32_16x16x32_bf16 v[32:35], a[240:243], a[16:19], v[32:35]\n"
"s_waitcnt lgkmcnt(1)\n"
"v_cvt_pk_bf16_f32 v240, v240, v241\n"
"v_cvt_pk_bf16_f32 v241, v242, v243\n"
"v_mfma_f32_16x16x32_bf16 v[32:35], a[252:255], a[20:23], v[32:35]\n"
"ds_write_b64 v197, v[240:241]\n"
"v_mfma_f32_16x16x32_bf16 v[32:35], v[8:11], a[24:27], v[32:35]\n"
"buffer_store_dwordx4 a[0:3], v194, s[24:27], s11 offen sc1\n"
"v_mfma_f32_16x16x32_bf16 v[32:35], v[20:23], a[28:31], v[32:35]\n"
"v_mfma_f32_16x16x32_bf16 v[40:43], a[224:227], a[8:11], 0\n"
"v_mfma_f32_16x16x32_bf16 v[40:43], a[236:239], a[12:15], v[40:43]\n"
"v_mfma_f32_16x16x32_bf16 v[40:43], a[248:251], a[16:19], v[40:43]\n"
"buffer_store_dwordx2 a[4:5], v195, s[24:27], s11 offen sc1\n"
"v_mfma_f32_16x16x32_bf16 v[40:43], v[4:7], a[20:23], v[40:43]\n"
"v_mfma_f32_16x16x32_bf16 v[40:43], v[16:19], a[24:27], v[40:43]\n"
"v_mfma_f32_16x16x32_bf16 v[40:43], v[28:31], a[28:31], v[40:43]\n"
"s_nop 0\n"
"v_cvt_pk_bf16_f32 v32, v32, v33\n"
"v_mfma_f32_16x16x32_bf16 v[36:39], a[220:223], a[8:11], 0\n"
"v_cvt_pk_bf16_f32 v33, v34, v35\n"
"v_mfma_f32_16x16x32_bf16 v[36:39], a[232:235], a[12:15], v[36:39]\n"
"v_mfma_f32_16x16x32_bf16 v[36:39], a[244:247], a[16:19], v[36:39]\n"
"v_mfma_f32_16x16x32_bf16 v[36:39], v[0:3], a[20:23], v[36:39]\n"
"s_nop 0\n"
"v_cvt_pk_bf16_f32 v40, v40, v41\n"
"v_mfma_f32_16x16x32_bf16 v[36:39], v[12:15], a[24:27], v[36:39]\n"
"v_cvt_pk_bf16_f32 v41, v42, v43\n"
"v_mfma_f32_16x16x32_bf16 v[36:39], v[24:27], a[28:31], v[36:39]\n"
"ds_write_b64 v197, v[40:41] offset:36864\n"
"s_nop 6\n"
"v_cvt_pk_bf16_f32 v34, v36, v37\n"
"v_cvt_pk_bf16_f32 v35, v38, v39\n"
"ds_write_b128 v196, v[32:35] offset:36864\n"
"s_waitcnt vmcnt(7)\n"
"v_max3_u32 v200, v56, v57, v58\n"
"s_waitcnt vmcnt(6)\n"
"v_max3_u32 v200, v200, v59, v60\n"
"v_max_u32 v200, v200, v61\n"
"v_cmp_eq_u32 vcc, -1, v200\n"
"s_nop 4\n"
"s_cbranch_vccnz .Lp13%=\n"
".Lr13%=:\n"
"ds_write_b128 v202, v[56:59] offset:24576\n"
"ds_write_b64 v203, v[60:61] offset:24576\n"
"s_waitcnt lgkmcnt(0)\n"
"s_barrier\n"
"ds_read_b128 a[48:51], v192 offset:24576\n"
"ds_read_b128 a[52:55], v192 offset:25600\n"
"ds_read_b128 v[64:67], v192 offset:26624\n"
"ds_read_b128 v[68:71], v192 offset:27648\n"
"ds_read_b128 v[44:47], v192 offset:28672\n"
"ds_read_b128 v[48:51], v192 offset:29696\n"
"ds_read_b128 a[32:35], v202\n"
"ds_read_b64 a[36:37], v62\n"
"ds_read_b128 a[40:43], v202 offset:36864\n"
"s_waitcnt lgkmcnt(8)\n"
"v_mfma_f32_16x16x32_bf16 v[204:207], a[216:219], a[48:51], 0\n"
"s_waitcnt lgkmcnt(7)\n"
"ds_read_b64 a[44:45], v62 offset:36864\n"
"v_mfma_f32_16x16x32_bf16 v[204:207], a[228:231], a[52:55], v[204:207]\n"
"s_waitcnt lgkmcnt(7)\n"
"v_mfma_f32_16x16x32_bf16 v[204:207], a[240:243], v[64:67], v[204:207]\n"
"s_waitcnt lgkmcnt(6)\n"
"v_mfma_f32_16x16x32_bf16 v[204:207], a[252:255], v[68:71], v[204:207]\n"
"s_waitcnt lgkmcnt(5)\n"
"v_mfma_f32_16x16x32_bf16 v[204:207], v[8:11], v[44:47], v[204:207]\n"
"s_waitcnt lgkmcnt(4)\n"
"v_mfma_f32_16x16x32_bf16 v[204:207], v[20:23], v[48:51], v[204:207]\n"
"s_waitcnt lgkmcnt(2)\n"
"buffer_store_dwordx4 a[32:35], v194, s[24:27], s20 offen sc1\n"
"v_mfma_f32_16x16x32_bf16 v[212:215], a[224:227], a[48:51], 0\n"
"s_waitcnt lgkmcnt(0)\n"
"v_mfma_f32_16x16x32_bf16 v[212:215], a[236:239], a[52:55], v[212:215]\n"
"v_mfma_f32_16x16x32_bf16 v[212:215], a[248:251], v[64:67], v[212:215]\n"
"v_mfma_f32_16x16x32_bf16 v[212:215], v[4:7], v[68:71], v[212:215]\n"
"buffer_store_dwordx2 a[36:37], v195, s[24:27], s20 offen sc1\n"
"v_mfma_f32_16x16x32_bf16 v[212:215], v[16:19], v[44:47], v[212:215]\n"
"v_cvt_pk_bf16_f32 v204, v204, v205\n"
"v_cvt_pk_bf16_f32 v205, v206, v207\n"
"v_mfma_f32_16x16x32_bf16 v[212:215], v[28:31], v[48:51], v[212:215]\n"
"v_mfma_f32_16x16x32_bf16 v[208:211], a[220:223], a[48:51], 0\n"
"v_mfma_f32_16x16x32_bf16 v[208:211], a[232:235], a[52:55], v[208:211]\n"
"buffer_store_dwordx4 a[40:43], v194, s[24:27], s21 offen sc1\n"
"v_mfma_f32_16x16x32_bf16 v[208:211], a[244:247], v[64:67], v[208:211]\n"
"v_mfma_f32_16x16x32_bf16 v[208:211], v[0:3], v[68:71], v[208:211]\n"
"v_mfma_f32_16x16x32_bf16 v[208:211], v[12:15], v[44:47], v[208:211]\n"
"v_mfma_f32_16x16x32_bf16 v[208:211], v[24:27], v[48:51], v[208:211]\n"
"buffer_store_dwordx2 a[44:45], v195, s[24:27], s21 offen sc1\n"
"v_cvt_pk_bf16_f32 v212, v212, v213\n"
"v_cvt_pk_bf16_f32 v213, v214, v215\n"
"ds_write_b64 v197, v[212:213] offset:12288\n"
"s_nop 3\n"
"v_cvt_pk_bf16_f32 v206, v208, v209\n"
"v_cvt_pk_bf16_f32 v207, v210, v211\n"
"ds_write_b128 v196, v[204:207] offset:12288\n"
"s_waitcnt lgkmcnt(0)\n"
"s_barrier\n"
"ds_read_b128 a[0:3], v202 offset:12288\n"
"ds_read_b64 a[4:5], v62 offset:12288\n"
"s_waitcnt lgkmcnt(1)\n"
"buffer_store_dwordx4 a[0:3], v194, s[24:27], s22 offen sc1\n"
"s_waitcnt lgkmcnt(0)\n"
"buffer_store_dwordx2 a[4:5], v195, s[24:27], s22 offen sc1\n"
"s_branch .Lend%=\n"
".Lp1%=:\n"
"s_sub_u32 s42, s42, 1\n"
"s_cbranch_scc1 .Lt1%=\n"
"s_sleep 1\n"
"buffer_load_dword v200, off, s[28:31], s39 sc1\n"
"s_waitcnt vmcnt(0)\n"
"v_add3_u32 v200, v200, s50, -2\n"
"v_cmp_lt_u32 vcc, 64, v200\n"
"s_nop 4\n"
"s_cbranch_vccnz .Lp1%=\n"
"s_branch .Lr1%=\n"
".Lt1%=:\n"
"s_mov_b32 s42, 0\n"
"s_branch .Lr1%=\n"
".Lp4%=:\n"
"s_sub_u32 s42, s42, 1\n"
"s_cbranch_scc1 .Lt4%=\n"
"s_sleep 1\n"
"buffer_load_dword v200, off, s[28:31], s39 sc1\n"
"s_waitcnt vmcnt(0)\n"
"v_add3_u32 v200, v200, s50, -5\n"
"v_cmp_lt_u32 vcc, 64, v200\n"
"s_nop 4\n"
"s_cbranch_vccnz .Lp4%=\n"
"s_branch .Lr4%=\n"
".Lt4%=:\n"
"s_mov_b32 s42, 0\n"
"s_branch .Lr4%=\n"
".Lp7%=:\n"
"s_sub_u32 s42, s42, 1\n"
"s_cbranch_scc1 .Lt7%=\n"
"s_sleep 1\n"
"buffer_load_dword v200, off, s[28:31], s39 sc1\n"
"s_waitcnt vmcnt(0)\n"
"v_add3_u32 v200, v200, s50, -8\n"
"v_cmp_lt_u32 vcc, 64, v200\n"
"s_nop 4\n"
"s_cbranch_vccnz .Lp7%=\n"
"s_branch .Lr7%=\n"
".Lt7%=:\n"
"s_mov_b32 s42, 0\n"
"s_branch .Lr7%=\n"
".Lp9%=:\n"
"s_sub_u32 s42, s42, 1\n"
"s_cbranch_scc1 .Lt9%=\n"
"s_sleep 1\n"
"buffer_load_dword v200, off, s[28:31], s39 sc1\n"
"s_waitcnt vmcnt(0)\n"
"v_add3_u32 v200, v200, s50, -10\n"
"v_cmp_lt_u32 vcc, 64, v200\n"
"s_nop 4\n"
"s_cbranch_vccnz .Lp9%=\n"
"s_branch .Lr9%=\n"
".Lt9%=:\n"
"s_mov_b32 s42, 0\n"
"s_branch .Lr9%=\n"
".Lp10%=:\n"
"s_sub_u32 s42, s42, 1\n"
"s_cbranch_scc1 .Lt10%=\n"
"s_sleep 1\n"
"buffer_load_dword v201, off, s[28:31], s39 sc1\n"
"s_waitcnt vmcnt(0)\n"
"v_add3_u32 v201, v201, s50, -11\n"
"v_cmp_lt_u32 vcc, 64, v201\n"
"s_nop 4\n"
"s_cbranch_vccnz .Lp10%=\n"
"s_branch .Lr10%=\n"
".Lt10%=:\n"
"s_mov_b32 s42, 0\n"
"s_branch .Lr10%=\n"
".Lp11%=:\n"
"s_sub_u32 s42, s42, 1\n"
"s_cbranch_scc1 .Lt11%=\n"
"buffer_load_dwordx4 v[52:55], v192, s[28:31], s15 offen sc1\n"
"buffer_load_dwordx2 v[152:153], v193, s[28:31], s15 offen offset:1024 sc1\n"
"s_waitcnt vmcnt(0)\n"
"v_max3_u32 v200, v52, v53, v54\n"
"v_max3_u32 v200, v200, v55, v152\n"
"v_max_u32 v200, v200, v153\n"
"v_cmp_eq_u32 vcc, -1, v200\n"
"s_nop 4\n"
"s_cbranch_vccnz .Lp11%=\n"
"s_branch .Lr11%=\n"
".Lt11%=:\n"
"s_mov_b32 s42, 0\n"
"s_branch .Lr11%=\n"
".Lp12%=:\n"
"s_sub_u32 s42, s42, 1\n"
"s_cbranch_scc1 .Lt12%=\n"
"buffer_load_dwordx4 v[144:147], v192, s[28:31], s17 offen sc1\n"
"buffer_load_dwordx2 v[154:155], v193, s[28:31], s17 offen offset:1024 sc1\n"
"s_waitcnt vmcnt(0)\n"
"v_max3_u32 v200, v144, v145, v146\n"
"v_max3_u32 v200, v200, v147, v154\n"
"v_max_u32 v200, v200, v155\n"
"v_cmp_eq_u32 vcc, -1, v200\n"
"s_nop 4\n"
"s_cbranch_vccnz .Lp12%=\n"
"s_branch .Lr12%=\n"
".Lt12%=:\n"
"s_mov_b32 s42, 0\n"
"s_branch .Lr12%=\n"
".Lp13%=:\n"
"s_sub_u32 s42, s42, 1\n"
"s_cbranch_scc1 .Lt13%=\n"
"buffer_load_dwordx4 v[56:59], v192, s[28:31], s18 offen sc1\n"
"buffer_load_dwordx2 v[60:61], v193, s[28:31], s18 offen offset:1024 sc1\n"
"s_waitcnt vmcnt(0)\n"
"v_max3_u32 v200, v56, v57, v58\n"
"v_max3_u32 v200, v200, v59, v60\n"
"v_max_u32 v200, v200, v61\n"
"v_cmp_eq_u32 vcc, -1, v200\n"
"s_nop 4\n"
"s_cbranch_vccnz .Lp13%=\n"
"s_branch .Lr13%=\n"
".Lt13%=:\n"
"s_mov_b32 s42, 0\n"
"s_branch .Lr13%=\n"
".Lend%=:\n"
      : "+{v31}"(tid), "+{s2}"(bid), "+{s[0:1]}"(ka)
      : "i"((__attribute__((address_space(3))) char*)smem)
      : "memory", "scc", "vcc", VCLOB, ACLOB, SCLOB);
}

extern "C" void kernel_launch(void* const* d_in, const int* in_sizes, int n_in,
                              void* d_out, int out_size, void* d_ws, size_t ws_size,
                              hipStream_t stream) {
  const uint16_t* x = (const uint16_t*)d_in[0];
  const uint16_t* wqkv = (const uint16_t*)d_in[1];
  const uint16_t* wo = (const uint16_t*)d_in[2];
  const uint16_t* rpb = (const uint16_t*)d_in[3];
  dinat_hs<<<256, 256, 0, stream>>>(x, wqkv, wo, rpb, (uint16_t*)d_out, (char*)d_ws + (2 << 20)
  );
}
```
